```python
import torch

def _softmax(x, axis=-1):
    return torch.softmax(x, dim=axis)

def _gelu(x):
    return torch.nn.functional.gelu(x, approximate='tanh')

def _silu(x):
    return torch.nn.functional.silu(x)

def _sigmoid(x):
    return torch.sigmoid(x)

def _layernorm(x, axis=-1, eps=1e-05):
    m = x.mean(dim=axis, keepdim=True)
    v = x.var(dim=axis, keepdim=True, unbiased=False)
    return (x - m) / torch.sqrt(v + eps)

def _scatter_add(tgt, idx, src, axis=0):
    return tgt.clone().index_add_(axis, idx, src)

def _segment_sum(src, seg, n):
    out = torch.zeros((n,) + src.shape[1:], dtype=src.dtype)
    return out.index_add_(0, seg, src)

def _pad(x, pw):
    flat = tuple((v for pair in reversed(pw) for v in pair))
    return torch.nn.functional.pad(x, flat)
kernel_name = 'standalone__tree_speculative_verify_tree_attention_over_candidate_token_tree__sampling'

def setup_inputs():

    _w = lambda a: (lambda t: t.to(torch.bfloat16) if t.is_floating_point() else t)(torch.as_tensor(a))
    B, Hq, Hkv, T, C, D = (16, 32, 8, 32, 4096, 128)
    tree_mask = torch.zeros((B, T, T), dtype=float)
    for b in range(B):
        parent = torch.zeros(T, dtype=torch.int64)
        for i in range(1, T):
            parent[i] = int(torch.randint(0, i, (1,)).item())
        for i in range(T):
            j = i
            tree_mask[b, i, j] = 1.0
            while j > 0:
                j = parent[j]
                tree_mask[b, i, j] = 1.0
    return {'q': _w((lambda *s: torch.randn(*s, dtype=torch.bfloat16) * 0.8)(B, Hq, T, D)), 'k_cache': _w((lambda *s: torch.randn(*s, dtype=torch.bfloat16) * 0.8)(B, Hkv, C, D)), 'v_cache': _w((lambda *s: torch.randn(*s, dtype=torch.bfloat16) * 0.05)(B, Hkv, C, D)), 'k_tree': _w((lambda *s: torch.randn(*s, dtype=torch.bfloat16) * 0.8)(B, Hkv, T, D)), 'v_tree': _w((lambda *s: torch.randn(*s, dtype=torch.bfloat16) * 0.05)(B, Hkv, T, D)), 'tree_mask': _w(tree_mask)}

def reference(q, k_cache, v_cache, k_tree, v_tree, tree_mask):
    B, Hq, T, D = q.shape
    Hkv, C = (k_cache.shape[1], k_cache.shape[2])
    rep = Hq // Hkv
    kc = (lambda x, n, axis: torch.repeat_interleave(x, n, dim=axis))(k_cache, rep, axis=1)
    vc = (lambda x, n, axis: torch.repeat_interleave(x, n, dim=axis))(v_cache, rep, axis=1)
    kt = (lambda x, n, axis: torch.repeat_interleave(x, n, dim=axis))(k_tree, rep, axis=1)
    vt = (lambda x, n, axis: torch.repeat_interleave(x, n, dim=axis))(v_tree, rep, axis=1)
    k = torch.concatenate([kc, kt], axis=2)
    v = torch.concatenate([vc, vt], axis=2)
    scale = D ** (-0.5)
    scores = torch.einsum('bhqd,bhkd->bhqk', q, k) * scale
    ctx_mask = torch.ones((B, 1, T, C), dtype=scores.dtype)
    tm = tree_mask[:, None, :, :].clone()
    full_mask = torch.concatenate([ctx_mask, tm], axis=-1)
    scores = scores + (1.0 - full_mask) * -1000000000.0
    p = _softmax(scores, axis=-1)
    return torch.einsum('bhqk,bhkd->bhqd', p, v)
```

```cpp
#include <hip/hip_runtime.h>
#include <hip/hip_bf16.h>
#include <stdint.h>

#pragma clang diagnostic ignored "-Wint-to-pointer-cast"
#pragma clang diagnostic ignored "-Winline-asm"

namespace {

constexpr int B_ = 16, HQ_ = 32, HKV_ = 8, T_ = 32, C_ = 4096, D_ = 128;
constexpr int TILE = 64;
constexpr int KIMG = TILE * D_ * 2;
constexpr int STAGE_BYTES = 2 * KIMG;
constexpr int TREE_OFF = 4 * STAGE_BYTES;
constexpr int Q_OFF = 3 * STAGE_BYTES;
constexpr int LDS_BYTES = TREE_OFF + 8192 + 1024;
constexpr unsigned C2 = 325, WIN = 36;
constexpr int CODE_BYTES = 21504;

constexpr size_t WS_EPOCH = 0;
constexpr size_t WS_REF = WS_EPOCH + 128 * 4 * 128;
constexpr size_t WS_X = WS_REF + 16384;
constexpr size_t XW_BYTES = 4096 + 512;
constexpr size_t WS_END = WS_X + (size_t)128 * 2 * 4 * XW_BYTES;

typedef float    f32x32 __attribute__((ext_vector_type(32)));
typedef float    f32x4  __attribute__((ext_vector_type(4)));
typedef unsigned long long u64x8 __attribute__((ext_vector_type(8)));
typedef unsigned u32x8  __attribute__((ext_vector_type(8)));
typedef unsigned u32x4  __attribute__((ext_vector_type(4)));
typedef unsigned u32x2  __attribute__((ext_vector_type(2)));
typedef __attribute__((address_space(3))) f32x4 lds_f32x4;
typedef __attribute__((address_space(1))) unsigned long long gu64;

#define DI __device__ __forceinline__

DI void dma16(unsigned lds_off, unsigned voff, __amdgpu_buffer_rsrc_t rsrc, unsigned soff) {
  asm volatile("s_mov_b32 m0, %0\n\ts_nop 0\n\tbuffer_load_dwordx4 %1, %2, %3 offen nt lds"
               :: "s"(lds_off), "v"(voff), "s"(rsrc), "s"(soff) : "memory");
}

__global__ __launch_bounds__(256, 1)
void tree_attn_main(const __hip_bfloat16*, const __hip_bfloat16*, const __hip_bfloat16*, const __hip_bfloat16*,
                    const __hip_bfloat16*, const __hip_bfloat16*, __hip_bfloat16*, char*) {
  u64x8 ka;
  asm volatile("s_load_dwordx16 s[4:19], %1, 0x0" : "={s[4:19]}"(ka) : "s"(__builtin_amdgcn_kernarg_segment_ptr()) : "memory");
  __shared__ __attribute__((aligned(1024))) char smem[LDS_BYTES];
  unsigned long long pc;
  asm volatile("s_getpc_b64 %0" : "=s"(pc));
  const int tid = threadIdx.x, lane = tid & 63;
  const int wid = __builtin_amdgcn_readfirstlane(tid >> 6);
  const int rh = wid & 1, kh = wid >> 1;
  const int xg = blockIdx.x & 7, jj = blockIdx.x >> 3;
  const int bk = xg * 16 + (jj >> 1), split = jj & 1;
  const int c16 = lane & 15, g = lane >> 4;
  const unsigned lds0 = (unsigned)(uintptr_t)smem;
  if (wid >= 2) {
    __amdgpu_buffer_rsrc_t srdC = __builtin_amdgcn_make_buffer_rsrc((void*)(pc & ~63ull), 0, CODE_BYTES, 0x00020000);
    asm volatile("s_mov_b32 m0, %0\n\ts_nop 0\n\tbuffer_load_dwordx4 %1, %2, %3 offen lds"
                 :: "s"(lds0 + TREE_OFF + 8192), "v"(lane * 16), "s"(srdC), "s"((unsigned)(wid == 3 ? min(jj, CODE_BYTES / 1024 - 1) : jj & 3) * 1024) : "memory");
  }

  const unsigned voffK = (unsigned)((8 * wid + (lane >> 3)) * 256 + (((lane & 7) ^ (4 * (wid & 1) + g)) << 4));
  const unsigned voffV = (unsigned)((8 * wid + (lane >> 3)) * 256 + (((lane & 7) ^ (g << 1)) << 4));
  const unsigned wbase = lds0 + (unsigned)wid * 2048;

  *(lds_f32x4*)(lds0 + KIMG + (1 - split) * 8192 + tid * 16) = f32x4{0.f, 0.f, 0.f, 0.f};
  *(lds_f32x4*)(lds0 + KIMG + (1 - split) * 8192 + 4096 + tid * 16) = f32x4{0.f, 0.f, 0.f, 0.f};
  u32x8 ad;
#pragma unroll
  for (int j = 0; j < 2; ++j)
    ad[j] = lds0 + (unsigned)(2048 * (c16 >> 3) + 128 * (c16 & 7) + (((4 * j + g) ^ (c16 >> 1)) << 4) + kh * 8192);
  {
    const int qq = c16 >> 2, p = lane & 3;
#pragma unroll
    for (int j = 0; j < 4; ++j)
      ad[2 + j] = lds0 + (unsigned)(2048 * (g >> 1) + 128 * (4 * (g & 1) + qq) + kh * 8192 + KIMG +
                                    (((2 * (j ^ (2 * kh)) + (p >> 1)) ^ ((2 * (g & 1) + (qq >> 1)) << 1)) << 4) + 8 * (p & 1));
  }
  ad[6] = voffK; ad[7] = voffV;
  u32x2 ra = {(unsigned)((lane >> 2) * 512 + (lane & 3) * 16), (unsigned)((blockIdx.x & 15) * 512 + (lane & 7) * 64)};
  unsigned mko = (unsigned)(((bk >> 3) * T_ + c16) * (T_ * 2) + g * 8);
  size_t qo = (size_t)bk * 128 * D_ * 2;
  f32x32 z1, z2, z3;
  asm volatile(
      "v_mov_b64 v[32:33], 0\n"
      "v_mov_b64 v[34:35], 0\n"
      "v_mov_b64 v[36:37], 0\n"
      "v_mov_b64 v[38:39], 0\n"
      "v_mov_b64 v[40:41], 0\n"
      "v_mov_b64 v[42:43], 0\n"
      "v_mov_b64 v[44:45], 0\n"
      "v_mov_b64 v[46:47], 0\n"
      "v_mov_b64 v[48:49], 0\n"
      "v_mov_b64 v[50:51], 0\n"
      "v_mov_b64 v[52:53], 0\n"
      "v_mov_b64 v[54:55], 0\n"
      "v_mov_b64 v[56:57], 0\n"
      "v_mov_b64 v[58:59], 0\n"
      "v_mov_b64 v[60:61], 0\n"
      "v_mov_b64 v[62:63], 0\n"
      "v_mov_b64 v[64:65], 0\n"
      "v_mov_b64 v[66:67], 0\n"
      "v_mov_b64 v[68:69], 0\n"
      "v_mov_b64 v[70:71], 0\n"
      "v_mov_b64 v[72:73], 0\n"
      "v_mov_b64 v[74:75], 0\n"
      "v_mov_b64 v[76:77], 0\n"
      "v_mov_b64 v[78:79], 0\n"
      "v_mov_b64 v[80:81], 0\n"
      "v_mov_b64 v[82:83], 0\n"
      "v_mov_b64 v[84:85], 0\n"
      "v_mov_b64 v[86:87], 0\n"
      "v_mov_b64 v[88:89], 0\n"
      "v_mov_b64 v[90:91], 0\n"
      "v_mov_b64 v[92:93], 0\n"
      "v_mov_b64 v[94:95], 0\n"
      "v_mov_b64 v[96:97], 0\n"
      "v_mov_b64 v[98:99], 0\n"
      "v_mov_b64 v[100:101], 0\n"
      "v_mov_b64 v[102:103], 0\n"
      "v_mov_b64 v[104:105], 0\n"
      "v_mov_b64 v[106:107], 0\n"
      "v_mov_b64 v[108:109], 0\n"
      "v_mov_b64 v[110:111], 0\n"
      "v_mov_b64 v[112:113], 0\n"
      "v_mov_b64 v[114:115], 0\n"
      "v_mov_b64 v[116:117], 0\n"
      "v_mov_b64 v[118:119], 0\n"
      "v_mov_b64 v[120:121], 0\n"
      "v_mov_b64 v[122:123], 0\n"
      "v_mov_b64 v[124:125], 0\n"
      "v_mov_b64 v[126:127], 0\n"
      : "={v[32:63]}"(z1), "={v[64:95]}"(z2), "={v[96:127]}"(z3));
  asm volatile("" : "+{v[242:249]}"(ad), "+{v[250:251]}"(ra), "+v"(mko), "+s"(qo) :: "memory");
  asm volatile("s_waitcnt lgkmcnt(0)" : "+{s[4:19]}"(ka) :: "memory");
  const char *qg = (const char*)ka[0], *kc = (const char*)ka[1], *vc = (const char*)ka[2], *kt = (const char*)ka[3],
             *vt = (const char*)ka[4], *tmask = (const char*)ka[5];
  char *out = (char*)ka[6], *ws = (char*)ka[7];
  __amdgpu_buffer_rsrc_t srdQ = __builtin_amdgcn_make_buffer_rsrc((void*)((const char*)qg + qo), 0, 128 * D_ * 2, 0x00020000);
#pragma unroll
  for (int i = 0; i < 8; ++i) dma16(wbase + Q_OFF + (i >> 1) * 8192 + (i & 1) * 1024, ad[6], srdQ, (i >> 1) * 8192 + (i & 1) * 128);
  u32x8 mk;
  asm volatile("s_cmp_lg_u32 %3, 0\n\ts_cbranch_scc1 1f\n\t"
               "global_load_dwordx2 v[208:209], %1, %2\n\tglobal_load_dwordx2 v[210:211], %1, %2 offset:32\n\t"
               "global_load_dwordx2 v[212:213], %1, %2 offset:1024\n\tglobal_load_dwordx2 v[214:215], %1, %2 offset:1056\n1:"
               : "=&{v[208:215]}"(mk) : "v"(mko), "s"(tmask), "s"(wid ^ (3 * split)) : "memory", "scc");
  unsigned tch;
  asm volatile("s_load_dword s92, %1, 0x0\n\ts_load_dword s92, %2, 0x0"
               : "={s92}"(tch) : "s"(kc + qo * 32), "s"(vc + qo * 32) : "memory");
  const size_t kvoff = ((size_t)bk * C_ + (size_t)split * 2 * TILE) * D_ * 2;
  const int kvbytes = (C_ - split * 2 * TILE) * D_ * 2;
  const unsigned so0 = (bk & 1) * KIMG, inc = KIMG - 2 * so0;
  __amdgpu_buffer_rsrc_t srdK = __builtin_amdgcn_make_buffer_rsrc((void*)((const char*)kc + kvoff), 0, kvbytes, 0x00020000);
  __amdgpu_buffer_rsrc_t srdV = __builtin_amdgcn_make_buffer_rsrc((void*)((const char*)vc + kvoff), 0, kvbytes, 0x00020000);
  __amdgpu_buffer_rsrc_t srdKt = __builtin_amdgcn_make_buffer_rsrc((void*)((const char*)kt + (size_t)bk * T_ * D_ * 2), 0, T_ * D_ * 2, 0x00020000);
  dma16(wbase + TREE_OFF, ad[6], srdKt, 0);
  dma16(wbase + TREE_OFF + 1024, ad[6], srdKt, 128);
  char* line = ws + WS_EPOCH + (size_t)(bk * 4 + wid) * 128;
  u32x2 epoch;
  asm volatile("global_load_dwordx2 v[216:217], %1, off sc1" : "=&{v[216:217]}"(epoch) : "v"(line) : "memory");
  __amdgpu_buffer_rsrc_t srdVt = __builtin_amdgcn_make_buffer_rsrc((void*)((const char*)vt + (size_t)bk * T_ * D_ * 2), 0, T_ * D_ * 2, 0x00020000);
  __amdgpu_buffer_rsrc_t srdR = __builtin_amdgcn_make_buffer_rsrc((void*)(ws + WS_REF), 0, 16384, 0x00020000);
  __amdgpu_buffer_rsrc_t srdX = __builtin_amdgcn_make_buffer_rsrc((void*)(ws + WS_X + (size_t)(bk * 8 + wid) * XW_BYTES), 0, (int)(5 * XW_BYTES), 0x00020000);
  const unsigned v0 = KIMG + 1024 * split, v1 = KIMG + 1024 * (1 - split) - 128;
  const unsigned n1 = 5 + (unsigned)((jj >> 2) + xg * 5) % 9u;
  unsigned e0, e1;
  asm volatile(
      "s_mov_b32 s60, %[so0]\n"
      "s_add_u32 s61, %[so0], 8192\n"
      "s_mov_b32 s79, %[inc]\n"
      "s_mov_b32 s62, %[wb]\n"
      "s_mov_b32 s70, 300\n"
      "s_mov_b32 s72, %[other]\n"
      "s_memrealtime s[82:83]\n"
      "s_mov_b32 s80, 0x443212a\n"
      "s_mov_b32 s81, 0x0\n"
      "s_mov_b32 s86, 108\n"
      "s_mov_b32 s87, 0\n"
      "s_mov_b32 s93, 29\n"
      "s_mov_b32 s89, 0\n"
      "s_lshl_b32 s91, %[wb], 18\n"
      "s_sub_u32 s91, 0x10000000, s91\n"
      "v_mov_b64 v[0:1], 0\n"
      "v_mov_b64 v[2:3], 0\n"
      "v_mov_b64 v[4:5], 0\n"
      "v_mov_b64 v[6:7], 0\n"
      "v_mov_b64 v[8:9], 0\n"
      "v_mov_b64 v[10:11], 0\n"
      "v_mov_b64 v[12:13], 0\n"
      "v_mov_b64 v[14:15], 0\n"
      "v_mov_b64 v[16:17], 0\n"
      "v_mov_b64 v[18:19], 0\n"
      "v_mov_b64 v[20:21], 0\n"
      "v_mov_b64 v[22:23], 0\n"
      "v_mov_b64 v[24:25], 0\n"
      "v_mov_b64 v[26:27], 0\n"
      "v_mov_b64 v[28:29], 0\n"
      "v_mov_b64 v[30:31], 0\n"
      "v_mov_b64 v[192:193], 0\n"
      "v_mov_b64 v[194:195], 0\n"
      "v_mov_b64 v[196:197], 0\n"
      "v_mov_b64 v[198:199], 0\n"
      "v_mov_b64 v[200:201], 0\n"
      "v_mov_b64 v[202:203], 0\n"
      "v_mov_b64 v[204:205], 0\n"
      "v_mov_b64 v[206:207], 0\n"
      "v_mov_b64 v[228:229], 0\n"
      "v_mov_b64 v[230:231], 0\n"
      "v_mov_b64 v[232:233], 0\n"
      "v_mov_b64 v[234:235], 0\n"
      "v_accvgpr_write_b32 a112, 0\n"
      "v_accvgpr_write_b32 a113, 0\n"
      "v_accvgpr_write_b32 a114, 0\n"
      "v_accvgpr_write_b32 a115, 0\n"
      "v_accvgpr_write_b32 a116, 0\n"
      "v_accvgpr_write_b32 a117, 0\n"
      "v_accvgpr_write_b32 a118, 0\n"
      "v_accvgpr_write_b32 a119, 0\n"
      "v_accvgpr_write_b32 a120, 0\n"
      "v_accvgpr_write_b32 a121, 0\n"
      "v_accvgpr_write_b32 a122, 0\n"
      "v_accvgpr_write_b32 a123, 0\n"
      "v_accvgpr_write_b32 a124, 0\n"
      "v_accvgpr_write_b32 a125, 0\n"
      "v_accvgpr_write_b32 a126, 0\n"
      "v_accvgpr_write_b32 a127, 0\n"
      "v_mov_b32 v224, 0x7f800000\n"
      "v_mov_b32 v225, 0x7f800000\n"
      "v_mov_b32 v226, 0x7f800000\n"
      "v_mov_b32 v227, 0x7f800000\n"
      "v_add_u32 v240, %[qoff], v242\n"
      "v_add_u32 v236, %[toff], v242\n"
      "v_add_u32 v241, %[qoff], v243\n"
      "v_add_u32 v237, %[toff], v243\n"
      "s_add_u32 m0, s62, 0\n"
      "s_nop 0\n"
      "buffer_load_dwordx4 v248, %[srdk], s60 offen nt lds\n"
      "s_add_u32 m0, s62, 896\n"
      "s_nop 0\n"
      "buffer_load_dwordx4 v248, %[srdk], s60 offen offset:128 nt lds\n"
      "s_add_u32 m0, s62, 8192\n"
      "s_nop 0\n"
      "buffer_load_dwordx4 v248, %[srdk], s61 offen nt lds\n"
      "s_add_u32 m0, s62, 9088\n"
      "s_nop 0\n"
      "buffer_load_dwordx4 v248, %[srdk], s61 offen offset:128 nt lds\n"
      "s_add_u32 m0, %[tv], %[v0]\n"
      "s_nop 0\n"
      "buffer_load_dwordx4 v249, %[srdt], 0 offen nt lds\n"
      "s_add_u32 m0, %[tv], %[v1]\n"
      "s_nop 0\n"
      "buffer_load_dwordx4 v249, %[srdt], 0 offen offset:128 nt lds\n"
      "s_mov_b32 s77, s60\n"
      "s_mov_b32 s78, s61\n"
      "s_add_u32 s60, s60, s79\n"
      "s_add_u32 s61, s61, s79\n"
      "s_sub_u32 s79, 0x10000, s79\n"
      "s_add_u32 s62, s62, 0x8000\n"
      "s_and_b32 s62, s62, 0x1ffff\n"
      "s_add_u32 m0, s62, 0\n"
      "s_nop 0\n"
      "buffer_load_dwordx4 v248, %[srdk], s60 offen nt lds\n"
      "s_add_u32 m0, s62, 896\n"
      "s_nop 0\n"
      "buffer_load_dwordx4 v248, %[srdk], s60 offen offset:128 nt lds\n"
      "s_add_u32 m0, s62, 8192\n"
      "s_nop 0\n"
      "buffer_load_dwordx4 v248, %[srdk], s61 offen nt lds\n"
      "s_add_u32 m0, s62, 9088\n"
      "s_nop 0\n"
      "buffer_load_dwordx4 v248, %[srdk], s61 offen offset:128 nt lds\n"
      "s_waitcnt vmcnt(11)\n"
      "s_barrier\n"
      "s_memrealtime s[68:69]\n"
      "ds_read_b128 a[64:67], v236 offset:0\n"
      "ds_read_b128 a[68:71], v236 offset:4096\n"
      "ds_read_b128 a[72:75], v237 offset:0\n"
      "ds_read_b128 a[76:79], v237 offset:4096\n"
      "ds_read_b128 a[80:83], v236 offset:1024\n"
      "ds_read_b128 a[84:87], v236 offset:5120\n"
      "ds_read_b128 a[88:91], v237 offset:1024\n"
      "ds_read_b128 a[92:95], v237 offset:5120\n"
      "ds_read_b128 a[0:3], v240 offset:0\n"
      "ds_read_b128 a[4:7], v240 offset:4096\n"
      "s_waitcnt lgkmcnt(7)\n"
      "ds_read_b128 a[8:11], v240 offset:8192\n"
      "ds_read_b128 a[12:15], v240 offset:12288\n"
      "ds_read_b128 a[16:19], v241 offset:0\n"
      "ds_read_b128 a[20:23], v241 offset:4096\n"
      "ds_read_b128 a[24:27], v241 offset:8192\n"
      "ds_read_b128 a[28:31], v241 offset:12288\n"
      "ds_read_b128 a[32:35], v240 offset:1024\n"
      "s_waitcnt lgkmcnt(7)\n"
      "ds_read_b128 a[36:39], v240 offset:5120\n"
      "ds_read_b128 a[40:43], v240 offset:9216\n"
      "ds_read_b128 a[44:47], v240 offset:13312\n"
      "ds_read_b128 a[48:51], v241 offset:1024\n"
      "ds_read_b128 a[52:55], v241 offset:5120\n"
      "ds_read_b128 a[56:59], v241 offset:9216\n"
      "ds_read_b128 a[60:63], v241 offset:13312\n"
      "s_add_u32 m0, s62, %[v0]\n"
      "s_nop 0\n"
      "buffer_load_dwordx4 v249, %[srdv], s77 offen nt lds\n"
      "s_add_u32 m0, s62, %[v1]\n"
      "s_nop 0\n"
      "buffer_load_dwordx4 v249, %[srdv], s77 offen offset:128 nt lds\n"
      "s_add_u32 m0, s62, %[v2]\n"
      "s_nop 0\n"
      "buffer_load_dwordx4 v249, %[srdv], s78 offen nt lds\n"
      "s_add_u32 m0, s62, %[v3]\n"
      "s_nop 0\n"
      "buffer_load_dwordx4 v249, %[srdv], s78 offen offset:128 nt lds\n"
      "s_mov_b32 s77, s60\n"
      "s_mov_b32 s78, s61\n"
      "s_add_u32 s60, s60, s79\n"
      "s_add_u32 s61, s61, s79\n"
      "s_sub_u32 s79, 0x10000, s79\n"
      "s_add_u32 s62, s62, 0x8000\n"
      "s_and_b32 s62, s62, 0x1ffff\n"
      "s_waitcnt lgkmcnt(0)\n"
      "s_mov_b32 s83, 0\n"
      "s_sub_u32 s67, s68, s82\n"
      "s_min_u32 s67, s67, 140\n"
      "s_add_u32 s66, s82, s67\n"
      "s_add_u32 s66, s66, 108\n"
      "s_mov_b32 s88, s66\n"
      "s_add_u32 s90, s88, s91\n"
      "s_sub_u32 s71, s68, 400\n"
      "s_cmp_lt_u32 %[lead], 512\n"
      "s_cbranch_scc0 NOLEAD_%=\n"
      "v_mov_b32 v236, s68\n"
      "v_mov_b32 v237, s68\n"
      "v_mov_b32 v238, s68\n"
      "v_mov_b32 v239, s68\n"
      "buffer_store_dwordx4 v[236:239], v250, %[srdr], %[lead] offen sc1\n"
      "s_nop 1\n"
      "NOLEAD_%=:\n"
      "s_cmp_lg_u32 s72, 0\n"
      "s_cbranch_scc1 NOTREE_%=\n"
      "v_mfma_f32_16x16x32_bf16 v[160:163], a[64:67], a[0:3], 0\n"
      "v_lshlrev_b32 v128, 16, v208\n"
      "v_and_b32 v129, 0xffff0000, v208\n"
      "v_mfma_f32_16x16x32_bf16 v[168:171], a[64:67], a[4:7], 0\n"
      "v_lshlrev_b32 v130, 16, v209\n"
      "v_and_b32 v131, 0xffff0000, v209\n"
      "v_mfma_f32_16x16x32_bf16 v[176:179], a[64:67], a[8:11], 0\n"
      "v_lshlrev_b32 v132, 16, v210\n"
      "v_and_b32 v133, 0xffff0000, v210\n"
      "v_mfma_f32_16x16x32_bf16 v[184:187], a[64:67], a[12:15], 0\n"
      "v_lshlrev_b32 v134, 16, v211\n"
      "v_and_b32 v135, 0xffff0000, v211\n"
      "v_mfma_f32_16x16x32_bf16 v[164:167], a[68:71], a[0:3], 0\n"
      "v_lshlrev_b32 v136, 16, v212\n"
      "v_and_b32 v137, 0xffff0000, v212\n"
      "v_mfma_f32_16x16x32_bf16 v[172:175], a[68:71], a[4:7], 0\n"
      "v_lshlrev_b32 v138, 16, v213\n"
      "v_and_b32 v139, 0xffff0000, v213\n"
      "v_mfma_f32_16x16x32_bf16 v[180:183], a[68:71], a[8:11], 0\n"
      "v_lshlrev_b32 v140, 16, v214\n"
      "v_and_b32 v141, 0xffff0000, v214\n"
      "v_mfma_f32_16x16x32_bf16 v[188:191], a[68:71], a[12:15], 0\n"
      "v_lshlrev_b32 v142, 16, v215\n"
      "v_and_b32 v143, 0xffff0000, v215\n"
      "v_mfma_f32_16x16x32_bf16 v[160:163], a[72:75], a[16:19], v[160:163]\n"
      "v_sub_f32 v128, 1.0, v128\n"
      "v_sub_f32 v129, 1.0, v129\n"
      "v_mfma_f32_16x16x32_bf16 v[168:171], a[72:75], a[20:23], v[168:171]\n"
      "v_sub_f32 v130, 1.0, v130\n"
      "v_sub_f32 v131, 1.0, v131\n"
      "v_mfma_f32_16x16x32_bf16 v[176:179], a[72:75], a[24:27], v[176:179]\n"
      "v_sub_f32 v132, 1.0, v132\n"
      "v_sub_f32 v133, 1.0, v133\n"
      "v_mfma_f32_16x16x32_bf16 v[184:187], a[72:75], a[28:31], v[184:187]\n"
      "v_sub_f32 v134, 1.0, v134\n"
      "v_sub_f32 v135, 1.0, v135\n"
      "v_mfma_f32_16x16x32_bf16 v[164:167], a[76:79], a[16:19], v[164:167]\n"
      "v_sub_f32 v136, 1.0, v136\n"
      "v_sub_f32 v137, 1.0, v137\n"
      "v_mfma_f32_16x16x32_bf16 v[172:175], a[76:79], a[20:23], v[172:175]\n"
      "v_sub_f32 v138, 1.0, v138\n"
      "v_sub_f32 v139, 1.0, v139\n"
      "v_mfma_f32_16x16x32_bf16 v[180:183], a[76:79], a[24:27], v[180:183]\n"
      "v_sub_f32 v140, 1.0, v140\n"
      "v_sub_f32 v141, 1.0, v141\n"
      "v_mfma_f32_16x16x32_bf16 v[188:191], a[76:79], a[28:31], v[188:191]\n"
      "v_sub_f32 v142, 1.0, v142\n"
      "v_sub_f32 v143, 1.0, v143\n"
      "v_mfma_f32_16x16x32_bf16 v[160:163], a[80:83], a[32:35], v[160:163]\n"
      "v_mfma_f32_16x16x32_bf16 v[168:171], a[80:83], a[36:39], v[168:171]\n"
      "v_mfma_f32_16x16x32_bf16 v[176:179], a[80:83], a[40:43], v[176:179]\n"
      "v_mfma_f32_16x16x32_bf16 v[184:187], a[80:83], a[44:47], v[184:187]\n"
      "v_mfma_f32_16x16x32_bf16 v[164:167], a[84:87], a[32:35], v[164:167]\n"
      "v_mfma_f32_16x16x32_bf16 v[172:175], a[84:87], a[36:39], v[172:175]\n"
      "v_mfma_f32_16x16x32_bf16 v[180:183], a[84:87], a[40:43], v[180:183]\n"
      "v_mfma_f32_16x16x32_bf16 v[188:191], a[84:87], a[44:47], v[188:191]\n"
      "v_mfma_f32_16x16x32_bf16 v[160:163], a[88:91], a[48:51], v[160:163]\n"
      "v_mfma_f32_16x16x32_bf16 v[168:171], a[88:91], a[52:55], v[168:171]\n"
      "v_mfma_f32_16x16x32_bf16 v[176:179], a[88:91], a[56:59], v[176:179]\n"
      "v_mfma_f32_16x16x32_bf16 v[184:187], a[88:91], a[60:63], v[184:187]\n"
      "v_mfma_f32_16x16x32_bf16 v[164:167], a[92:95], a[48:51], v[164:167]\n"
      "v_mfma_f32_16x16x32_bf16 v[172:175], a[92:95], a[52:55], v[172:175]\n"
      "v_mfma_f32_16x16x32_bf16 v[180:183], a[92:95], a[56:59], v[180:183]\n"
      "v_mfma_f32_16x16x32_bf16 v[188:191], a[92:95], a[60:63], v[188:191]\n"
      "s_nop 3\n"
      "v_fmamk_f32 v160, v128, 0xd0289660, v160\n"
      "v_fmamk_f32 v161, v129, 0xd0289660, v161\n"
      "v_fmamk_f32 v162, v130, 0xd0289660, v162\n"
      "v_fmamk_f32 v163, v131, 0xd0289660, v163\n"
      "v_fmamk_f32 v168, v136, 0xd0289660, v168\n"
      "v_fmamk_f32 v169, v137, 0xd0289660, v169\n"
      "v_fmamk_f32 v170, v138, 0xd0289660, v170\n"
      "v_fmamk_f32 v171, v139, 0xd0289660, v171\n"
      "v_fmamk_f32 v176, v128, 0xd0289660, v176\n"
      "v_fmamk_f32 v177, v129, 0xd0289660, v177\n"
      "v_fmamk_f32 v178, v130, 0xd0289660, v178\n"
      "v_fmamk_f32 v179, v131, 0xd0289660, v179\n"
      "v_fmamk_f32 v184, v136, 0xd0289660, v184\n"
      "v_fmamk_f32 v185, v137, 0xd0289660, v185\n"
      "v_fmamk_f32 v186, v138, 0xd0289660, v186\n"
      "v_fmamk_f32 v187, v139, 0xd0289660, v187\n"
      "v_fmamk_f32 v164, v132, 0xd0289660, v164\n"
      "v_fmamk_f32 v165, v133, 0xd0289660, v165\n"
      "v_fmamk_f32 v166, v134, 0xd0289660, v166\n"
      "v_fmamk_f32 v167, v135, 0xd0289660, v167\n"
      "v_fmamk_f32 v172, v140, 0xd0289660, v172\n"
      "v_fmamk_f32 v173, v141, 0xd0289660, v173\n"
      "v_fmamk_f32 v174, v142, 0xd0289660, v174\n"
      "v_fmamk_f32 v175, v143, 0xd0289660, v175\n"
      "v_fmamk_f32 v180, v132, 0xd0289660, v180\n"
      "v_fmamk_f32 v181, v133, 0xd0289660, v181\n"
      "v_fmamk_f32 v182, v134, 0xd0289660, v182\n"
      "v_fmamk_f32 v183, v135, 0xd0289660, v183\n"
      "v_fmamk_f32 v188, v140, 0xd0289660, v188\n"
      "v_fmamk_f32 v189, v141, 0xd0289660, v189\n"
      "v_fmamk_f32 v190, v142, 0xd0289660, v190\n"
      "v_fmamk_f32 v191, v143, 0xd0289660, v191\n"
      "v_max3_f32 v236, v160, v161, v162\n"
      "v_max3_f32 v237, v168, v169, v170\n"
      "v_max3_f32 v238, v176, v177, v178\n"
      "v_max3_f32 v239, v184, v185, v186\n"
      "v_max3_f32 v236, v236, v163, v164\n"
      "v_max3_f32 v237, v237, v171, v172\n"
      "v_max3_f32 v238, v238, v179, v180\n"
      "v_max3_f32 v239, v239, v187, v188\n"
      "v_max3_f32 v236, v236, v165, v166\n"
      "v_max3_f32 v237, v237, v173, v174\n"
      "v_max3_f32 v238, v238, v181, v182\n"
      "v_max3_f32 v239, v239, v189, v190\n"
      "v_max_f32 v236, v236, v167\n"
      "v_max_f32 v237, v237, v175\n"
      "v_max_f32 v238, v238, v183\n"
      "v_max_f32 v239, v239, v191\n"
      "v_mov_b32 v208, v236\n"
      "v_mov_b32 v209, v237\n"
      "v_mov_b32 v210, v238\n"
      "v_mov_b32 v211, v239\n"
      "v_permlane16_swap_b32 v236, v208\n"
      "v_permlane16_swap_b32 v237, v209\n"
      "v_permlane16_swap_b32 v238, v210\n"
      "v_permlane16_swap_b32 v239, v211\n"
      "v_max_f32 v236, v236, v208\n"
      "v_max_f32 v237, v237, v209\n"
      "v_max_f32 v238, v238, v210\n"
      "v_max_f32 v239, v239, v211\n"
      "v_mov_b32 v208, v236\n"
      "v_mov_b32 v209, v237\n"
      "v_mov_b32 v210, v238\n"
      "v_mov_b32 v211, v239\n"
      "v_permlane32_swap_b32 v236, v208\n"
      "v_permlane32_swap_b32 v237, v209\n"
      "v_permlane32_swap_b32 v238, v210\n"
      "v_permlane32_swap_b32 v239, v211\n"
      "v_max_f32 v236, v236, v208\n"
      "v_max_f32 v237, v237, v209\n"
      "v_max_f32 v238, v238, v210\n"
      "v_max_f32 v239, v239, v211\n"
      "v_mul_f32 v224, 0xbe0293ee, v236\n"
      "v_mul_f32 v225, 0xbe0293ee, v237\n"
      "v_mul_f32 v226, 0xbe0293ee, v238\n"
      "v_mul_f32 v227, 0xbe0293ee, v239\n"
      "v_fmamk_f32 v240, v160, 0x3e0293ee, v224\n"
      "v_exp_f32 v236, v240\n"
      "v_fmamk_f32 v241, v161, 0x3e0293ee, v224\n"
      "v_add_f32 v228, v228, v232\n"
      "v_exp_f32 v237, v241\n"
      "v_fmamk_f32 v240, v162, 0x3e0293ee, v224\n"
      "v_add_f32 v232, v236, v237\n"
      "v_cvt_pk_bf16_f32 v208, v236, v237\n"
      "v_exp_f32 v238, v240\n"
      "v_fmamk_f32 v241, v163, 0x3e0293ee, v224\n"
      "v_add_f32 v232, v232, v238\n"
      "v_exp_f32 v239, v241\n"
      "v_fmamk_f32 v240, v168, 0x3e0293ee, v225\n"
      "v_add_f32 v232, v232, v239\n"
      "v_cvt_pk_bf16_f32 v209, v238, v239\n"
      "v_exp_f32 v236, v240\n"
      "v_fmamk_f32 v241, v169, 0x3e0293ee, v225\n"
      "v_add_f32 v229, v229, v233\n"
      "v_exp_f32 v237, v241\n"
      "v_fmamk_f32 v240, v170, 0x3e0293ee, v225\n"
      "v_add_f32 v233, v236, v237\n"
      "v_cvt_pk_bf16_f32 v212, v236, v237\n"
      "v_exp_f32 v238, v240\n"
      "v_fmamk_f32 v241, v171, 0x3e0293ee, v225\n"
      "v_add_f32 v233, v233, v238\n"
      "v_exp_f32 v239, v241\n"
      "v_fmamk_f32 v240, v176, 0x3e0293ee, v226\n"
      "v_add_f32 v233, v233, v239\n"
      "v_cvt_pk_bf16_f32 v213, v238, v239\n"
      "NOTREE_%=:\n"
      "s_waitcnt vmcnt(14)\n"
      "v_readfirstlane_b32 %[e0], v216\n"
      "v_readfirstlane_b32 %[e1], v217\n"
      "s_memrealtime s[68:69]\n"
      "s_sub_u32 s63, %[n1], 1\n"
      "LOOP1_%=:\n"
      "s_waitcnt vmcnt(8) lgkmcnt(0)\n"
      "s_sub_u32 s67, s68, s90\n"
      "s_cmp_lt_i32 s67, 0\n"
      "s_cbranch_scc1 GW0_%=\n"
      "GX0_%=:\n"
      "s_barrier\n"
      "v_mfma_f32_16x16x32_bf16 v[64:67], a[112:115], v[192:195], v[64:67]\n"
      "s_memrealtime s[68:69]\n"
      "ds_read_b128 a[64:67], v242 offset:0\n"
      "v_exp_f32 v236, v240\n"
      "v_mfma_f32_16x16x32_bf16 v[68:71], a[112:115], v[196:199], v[68:71]\n"
      "s_add_u32 m0, s62, 0\n"
      "ds_read_b128 a[68:71], v242 offset:4096\n"
      "v_fmamk_f32 v241, v177, 0x3e0293ee, v226\n"
      "v_add_f32 v230, v230, v234\n"
      "v_mfma_f32_16x16x32_bf16 v[72:75], a[112:115], v[200:203], v[72:75]\n"
      "ds_read_b128 a[72:75], v243 offset:0\n"
      "buffer_load_dwordx4 v248, %[srdk], s60 offen nt lds\n"
      "v_exp_f32 v237, v241\n"
      "v_mfma_f32_16x16x32_bf16 v[76:79], a[112:115], v[204:207], v[76:79]\n"
      "s_add_u32 m0, s62, 896\n"
      "ds_read_b128 a[76:79], v243 offset:4096\n"
      "v_fmamk_f32 v240, v178, 0x3e0293ee, v226\n"
      "v_add_f32 v234, v236, v237\n"
      "v_cvt_pk_bf16_f32 v216, v236, v237\n"
      "v_mfma_f32_16x16x32_bf16 v[80:83], a[116:119], v[192:195], v[80:83]\n"
      "ds_read_b128 a[80:83], v242 offset:1024\n"
      "buffer_load_dwordx4 v248, %[srdk], s60 offen offset:128 nt lds\n"
      "v_exp_f32 v238, v240\n"
      "v_mfma_f32_16x16x32_bf16 v[84:87], a[116:119], v[196:199], v[84:87]\n"
      "s_add_u32 m0, s62, 8192\n"
      "ds_read_b128 a[84:87], v242 offset:5120\n"
      "v_fmamk_f32 v241, v179, 0x3e0293ee, v226\n"
      "v_add_f32 v234, v234, v238\n"
      "v_mfma_f32_16x16x32_bf16 v[88:91], a[116:119], v[200:203], v[88:91]\n"
      "ds_read_b128 a[88:91], v243 offset:1024\n"
      "buffer_load_dwordx4 v248, %[srdk], s61 offen nt lds\n"
      "v_exp_f32 v239, v241\n"
      "v_mfma_f32_16x16x32_bf16 v[92:95], a[116:119], v[204:207], v[92:95]\n"
      "s_add_u32 m0, s62, 9088\n"
      "ds_read_b128 a[92:95], v243 offset:5120\n"
      "v_fmamk_f32 v240, v184, 0x3e0293ee, v227\n"
      "v_add_f32 v234, v234, v239\n"
      "v_cvt_pk_bf16_f32 v217, v238, v239\n"
      "v_mfma_f32_16x16x32_bf16 v[96:99], a[120:123], v[192:195], v[96:99]\n"
      "buffer_load_dwordx4 v248, %[srdk], s61 offen offset:128 nt lds\n"
      "v_exp_f32 v236, v240\n"
      "v_mfma_f32_16x16x32_bf16 v[100:103], a[120:123], v[196:199], v[100:103]\n"
      "v_fmamk_f32 v241, v185, 0x3e0293ee, v227\n"
      "v_add_f32 v231, v231, v235\n"
      "v_mfma_f32_16x16x32_bf16 v[104:107], a[120:123], v[200:203], v[104:107]\n"
      "v_exp_f32 v237, v241\n"
      "v_mfma_f32_16x16x32_bf16 v[108:111], a[120:123], v[204:207], v[108:111]\n"
      "v_fmamk_f32 v240, v186, 0x3e0293ee, v227\n"
      "v_add_f32 v235, v236, v237\n"
      "v_cvt_pk_bf16_f32 v220, v236, v237\n"
      "v_mfma_f32_16x16x32_bf16 v[112:115], a[124:127], v[192:195], v[112:115]\n"
      "v_exp_f32 v238, v240\n"
      "v_mfma_f32_16x16x32_bf16 v[116:119], a[124:127], v[196:199], v[116:119]\n"
      "v_fmamk_f32 v241, v187, 0x3e0293ee, v227\n"
      "v_add_f32 v235, v235, v238\n"
      "v_mfma_f32_16x16x32_bf16 v[120:123], a[124:127], v[200:203], v[120:123]\n"
      "v_exp_f32 v239, v241\n"
      "v_mfma_f32_16x16x32_bf16 v[124:127], a[124:127], v[204:207], v[124:127]\n"
      "s_waitcnt lgkmcnt(0)\n"
      "v_fmamk_f32 v240, v164, 0x3e0293ee, v224\n"
      "v_add_f32 v235, v235, v239\n"
      "v_cvt_pk_bf16_f32 v221, v238, v239\n"
      "v_mfma_f32_16x16x32_bf16 v[128:131], a[64:67], a[0:3], 0\n"
      "s_sub_u32 s67, s68, s66\n"
      "v_exp_f32 v236, v240\n"
      "v_mfma_f32_16x16x32_bf16 v[136:139], a[64:67], a[4:7], 0\n"
      "s_cmp_gt_i32 s67, s70\n"
      "ds_read_b64_tr_b16 a[128:129], v244 offset:0\n"
      "v_fmamk_f32 v241, v165, 0x3e0293ee, v224\n"
      "v_add_f32 v232, v232, v236\n"
      "v_mfma_f32_16x16x32_bf16 v[144:147], a[64:67], a[8:11], 0\n"
      "s_cselect_b32 s69, s86, 0\n"
      "ds_read_b64_tr_b16 a[130:131], v244 offset:4096\n"
      "v_exp_f32 v237, v241\n"
      "v_mfma_f32_16x16x32_bf16 v[152:155], a[64:67], a[12:15], 0\n"
      "s_add_u32 s66, s66, s69\n"
      "ds_read_b64_tr_b16 a[132:133], v245 offset:0\n"
      "v_fmamk_f32 v240, v166, 0x3e0293ee, v224\n"
      "v_add_f32 v232, v232, v237\n"
      "v_cvt_pk_bf16_f32 v210, v236, v237\n"
      "v_mfma_f32_16x16x32_bf16 v[132:135], a[68:71], a[0:3], 0\n"
      "s_add_u32 s67, s67, 50\n"
      "ds_read_b64_tr_b16 a[134:135], v245 offset:4096\n"
      "v_exp_f32 v238, v240\n"
      "v_mfma_f32_16x16x32_bf16 v[140:143], a[68:71], a[4:7], 0\n"
      "s_min_i32 s70, s70, s67\n"
      "ds_read_b64_tr_b16 a[136:137], v246 offset:0\n"
      "v_fmamk_f32 v241, v167, 0x3e0293ee, v224\n"
      "v_add_f32 v232, v232, v238\n"
      "v_mfma_f32_16x16x32_bf16 v[148:151], a[68:71], a[8:11], 0\n"
      "s_max_i32 s70, s70, 85\n"
      "ds_read_b64_tr_b16 a[138:139], v246 offset:4096\n"
      "v_exp_f32 v239, v241\n"
      "v_mfma_f32_16x16x32_bf16 v[156:159], a[68:71], a[12:15], 0\n"
      "s_add_u32 s66, s66, s86\n"
      "ds_read_b64_tr_b16 a[140:141], v247 offset:0\n"
      "v_fmamk_f32 v240, v172, 0x3e0293ee, v225\n"
      "v_add_f32 v232, v232, v239\n"
      "v_cvt_pk_bf16_f32 v211, v238, v239\n"
      "v_mfma_f32_16x16x32_bf16 v[128:131], a[72:75], a[16:19], v[128:131]\n"
      "s_and_b32 s76, s80, 31\n"
      "ds_read_b64_tr_b16 a[142:143], v247 offset:4096\n"
      "v_exp_f32 v236, v240\n"
      "v_mfma_f32_16x16x32_bf16 v[136:139], a[72:75], a[20:23], v[136:139]\n"
      "s_lshr_b64 s[80:81], s[80:81], 5\n"
      "ds_read_b64_tr_b16 a[144:145], v244 offset:1024\n"
      "v_fmamk_f32 v241, v173, 0x3e0293ee, v225\n"
      "v_add_f32 v233, v233, v236\n"
      "v_mfma_f32_16x16x32_bf16 v[144:147], a[72:75], a[24:27], v[144:147]\n"
      "s_add_u32 s66, s66, s76\n"
      "ds_read_b64_tr_b16 a[146:147], v244 offset:5120\n"
      "v_exp_f32 v237, v241\n"
      "v_mfma_f32_16x16x32_bf16 v[152:155], a[72:75], a[28:31], v[152:155]\n"
      "s_sub_u32 s67, s68, s82\n"
      "ds_read_b64_tr_b16 a[148:149], v245 offset:1024\n"
      "v_fmamk_f32 v240, v174, 0x3e0293ee, v225\n"
      "v_add_f32 v233, v233, v237\n"
      "v_cvt_pk_bf16_f32 v214, v236, v237\n"
      "v_mfma_f32_16x16x32_bf16 v[132:135], a[76:79], a[16:19], v[132:135]\n"
      "s_min_i32 s76, s67, s83\n"
      "ds_read_b64_tr_b16 a[150:151], v245 offset:5120\n"
      "v_exp_f32 v238, v240\n"
      "v_mfma_f32_16x16x32_bf16 v[140:143], a[76:79], a[20:23], v[140:143]\n"
      "s_mov_b32 s83, s67\n"
      "ds_read_b64_tr_b16 a[152:153], v246 offset:1024\n"
      "v_fmamk_f32 v241, v175, 0x3e0293ee, v225\n"
      "v_add_f32 v233, v233, v238\n"
      "v_mfma_f32_16x16x32_bf16 v[148:151], a[76:79], a[24:27], v[148:151]\n"
      "s_sub_u32 s76, s76, 26\n"
      "ds_read_b64_tr_b16 a[154:155], v246 offset:5120\n"
      "v_exp_f32 v239, v241\n"
      "v_mfma_f32_16x16x32_bf16 v[156:159], a[76:79], a[28:31], v[156:159]\n"
      "s_min_i32 s76, s76, 148\n"
      "ds_read_b64_tr_b16 a[156:157], v247 offset:1024\n"
      "v_fmamk_f32 v240, v180, 0x3e0293ee, v226\n"
      "v_add_f32 v233, v233, v239\n"
      "v_cvt_pk_bf16_f32 v215, v238, v239\n"
      "v_mfma_f32_16x16x32_bf16 v[128:131], a[80:83], a[32:35], v[128:131]\n"
      "s_max_i32 s76, s76, 94\n"
      "ds_read_b64_tr_b16 a[158:159], v247 offset:5120\n"
      "v_exp_f32 v236, v240\n"
      "v_mfma_f32_16x16x32_bf16 v[136:139], a[80:83], a[36:39], v[136:139]\n"
      "s_mov_b32 s82, s68\n"
      "v_fmamk_f32 v241, v181, 0x3e0293ee, v226\n"
      "v_add_f32 v234, v234, v236\n"
      "v_mfma_f32_16x16x32_bf16 v[144:147], a[80:83], a[40:43], v[144:147]\n"
      "s_add_u32 s65, s82, s76\n"
      "v_exp_f32 v237, v241\n"
      "v_mfma_f32_16x16x32_bf16 v[152:155], a[80:83], a[44:47], v[152:155]\n"
      "s_sub_u32 s93, s93, 1\n"
      "v_fmamk_f32 v240, v182, 0x3e0293ee, v226\n"
      "v_add_f32 v234, v234, v237\n"
      "v_cvt_pk_bf16_f32 v218, v236, v237\n"
      "v_mfma_f32_16x16x32_bf16 v[132:135], a[84:87], a[32:35], v[132:135]\n"
      "s_cmp_lt_i32 s93, 10\n"
      "v_exp_f32 v238, v240\n"
      "v_mfma_f32_16x16x32_bf16 v[140:143], a[84:87], a[36:39], v[140:143]\n"
      "s_cselect_b32 s69, 3, 0\n"
      "v_fmamk_f32 v241, v183, 0x3e0293ee, v226\n"
      "v_add_f32 v234, v234, v238\n"
      "v_mfma_f32_16x16x32_bf16 v[148:151], a[84:87], a[40:43], v[148:151]\n"
      "s_sub_u32 s66, s66, s69\n"
      "v_exp_f32 v239, v241\n"
      "v_mfma_f32_16x16x32_bf16 v[156:159], a[84:87], a[44:47], v[156:159]\n"
      "s_sub_u32 s67, s65, s66\n"
      "v_fmamk_f32 v240, v188, 0x3e0293ee, v227\n"
      "v_add_f32 v234, v234, v239\n"
      "v_cvt_pk_bf16_f32 v219, v238, v239\n"
      "v_mfma_f32_16x16x32_bf16 v[128:131], a[88:91], a[48:51], v[128:131]\n"
      "s_cmp_lt_i32 s67, 0\n"
      "v_exp_f32 v236, v240\n"
      "v_mfma_f32_16x16x32_bf16 v[136:139], a[88:91], a[52:55], v[136:139]\n"
      "s_cselect_b32 s88, s66, s65\n"
      "v_fmamk_f32 v241, v189, 0x3e0293ee, v227\n"
      "v_add_f32 v235, v235, v236\n"
      "v_mfma_f32_16x16x32_bf16 v[144:147], a[88:91], a[56:59], v[144:147]\n"
      "s_add_u32 s90, s88, s91\n"
      "v_exp_f32 v237, v241\n"
      "v_mfma_f32_16x16x32_bf16 v[152:155], a[88:91], a[60:63], v[152:155]\n"
      "v_fmamk_f32 v240, v190, 0x3e0293ee, v227\n"
      "v_add_f32 v235, v235, v237\n"
      "v_cvt_pk_bf16_f32 v222, v236, v237\n"
      "v_mfma_f32_16x16x32_bf16 v[132:135], a[92:95], a[48:51], v[132:135]\n"
      "s_mov_b32 s64, 0\n"
      "v_exp_f32 v238, v240\n"
      "v_mfma_f32_16x16x32_bf16 v[140:143], a[92:95], a[52:55], v[140:143]\n"
      "v_fmamk_f32 v241, v191, 0x3e0293ee, v227\n"
      "v_add_f32 v235, v235, v238\n"
      "v_mfma_f32_16x16x32_bf16 v[148:151], a[92:95], a[56:59], v[148:151]\n"
      "v_exp_f32 v239, v241\n"
      "v_mfma_f32_16x16x32_bf16 v[156:159], a[92:95], a[60:63], v[156:159]\n"
      "s_waitcnt lgkmcnt(8)\n"
      "v_add_f32 v235, v235, v239\n"
      "v_cvt_pk_bf16_f32 v223, v238, v239\n"
      "v_max3_f32 v240, v232, v233, v234\n"
      "v_add_f32 v240, v240, v235\n"
      "v_cmp_nge_f32 vcc, 0x44800000, v240\n"
      "s_cbranch_vccnz FB1_%=\n"
      "RET1_0_%=:\n"
      "v_mfma_f32_16x16x32_bf16 v[0:3], a[128:131], v[208:211], v[0:3]\n"
      "s_add_u32 m0, s62, %[v0]\n"
      "v_fmamk_f32 v240, v128, 0x3e0293ee, v224\n"
      "v_exp_f32 v236, v240\n"
      "v_mfma_f32_16x16x32_bf16 v[4:7], a[128:131], v[212:215], v[4:7]\n"
      "buffer_load_dwordx4 v249, %[srdv], s77 offen nt lds\n"
      "v_fmamk_f32 v241, v129, 0x3e0293ee, v224\n"
      "v_add_f32 v228, v228, v232\n"
      "v_mfma_f32_16x16x32_bf16 v[8:11], a[128:131], v[216:219], v[8:11]\n"
      "s_add_u32 m0, s62, %[v1]\n"
      "v_exp_f32 v237, v241\n"
      "v_mfma_f32_16x16x32_bf16 v[12:15], a[128:131], v[220:223], v[12:15]\n"
      "buffer_load_dwordx4 v249, %[srdv], s77 offen offset:128 nt lds\n"
      "v_fmamk_f32 v240, v130, 0x3e0293ee, v224\n"
      "v_add_f32 v232, v236, v237\n"
      "v_cvt_pk_bf16_f32 v192, v236, v237\n"
      "v_mfma_f32_16x16x32_bf16 v[16:19], a[132:135], v[208:211], v[16:19]\n"
      "s_add_u32 m0, s62, %[v2]\n"
      "v_exp_f32 v238, v240\n"
      "v_mfma_f32_16x16x32_bf16 v[20:23], a[132:135], v[212:215], v[20:23]\n"
      "buffer_load_dwordx4 v249, %[srdv], s78 offen nt lds\n"
      "v_fmamk_f32 v241, v131, 0x3e0293ee, v224\n"
      "v_add_f32 v232, v232, v238\n"
      "v_mfma_f32_16x16x32_bf16 v[24:27], a[132:135], v[216:219], v[24:27]\n"
      "s_add_u32 m0, s62, %[v3]\n"
      "v_exp_f32 v239, v241\n"
      "v_mfma_f32_16x16x32_bf16 v[28:31], a[132:135], v[220:223], v[28:31]\n"
      "s_mov_b32 s77, s60\n"
      "buffer_load_dwordx4 v249, %[srdv], s78 offen offset:128 nt lds\n"
      "v_fmamk_f32 v240, v136, 0x3e0293ee, v225\n"
      "v_add_f32 v232, v232, v239\n"
      "v_cvt_pk_bf16_f32 v193, v238, v239\n"
      "v_mfma_f32_16x16x32_bf16 v[32:35], a[136:139], v[208:211], v[32:35]\n"
      "s_mov_b32 s78, s61\n"
      "v_exp_f32 v236, v240\n"
      "v_mfma_f32_16x16x32_bf16 v[36:39], a[136:139], v[212:215], v[36:39]\n"
      "s_add_u32 s60, s60, s79\n"
      "v_fmamk_f32 v241, v137, 0x3e0293ee, v225\n"
      "v_add_f32 v229, v229, v233\n"
      "v_mfma_f32_16x16x32_bf16 v[40:43], a[136:139], v[216:219], v[40:43]\n"
      "s_add_u32 s61, s61, s79\n"
      "v_exp_f32 v237, v241\n"
      "v_mfma_f32_16x16x32_bf16 v[44:47], a[136:139], v[220:223], v[44:47]\n"
      "s_sub_u32 s79, 0x10000, s79\n"
      "v_fmamk_f32 v240, v138, 0x3e0293ee, v225\n"
      "v_add_f32 v233, v236, v237\n"
      "v_cvt_pk_bf16_f32 v196, v236, v237\n"
      "v_mfma_f32_16x16x32_bf16 v[48:51], a[140:143], v[208:211], v[48:51]\n"
      "s_memrealtime s[68:69]\n"
      "v_exp_f32 v238, v240\n"
      "v_mfma_f32_16x16x32_bf16 v[52:55], a[140:143], v[212:215], v[52:55]\n"
      "s_add_u32 s62, s62, 0x8000\n"
      "v_fmamk_f32 v241, v139, 0x3e0293ee, v225\n"
      "v_add_f32 v233, v233, v238\n"
      "v_mfma_f32_16x16x32_bf16 v[56:59], a[140:143], v[216:219], v[56:59]\n"
      "s_and_b32 s62, s62, 0x1ffff\n"
      "v_exp_f32 v239, v241\n"
      "v_mfma_f32_16x16x32_bf16 v[60:63], a[140:143], v[220:223], v[60:63]\n"
      "v_fmamk_f32 v240, v144, 0x3e0293ee, v226\n"
      "v_add_f32 v233, v233, v239\n"
      "v_cvt_pk_bf16_f32 v197, v238, v239\n"
      "s_waitcnt vmcnt(8) lgkmcnt(0)\n"
      "s_sub_u32 s67, s68, s90\n"
      "s_cmp_lt_i32 s67, 0\n"
      "s_cbranch_scc1 GW1_%=\n"
      "GX1_%=:\n"
      "s_barrier\n"
      "v_mfma_f32_16x16x32_bf16 v[64:67], a[144:147], v[208:211], v[64:67]\n"
      "s_memrealtime s[68:69]\n"
      "ds_read_b128 a[64:67], v242 offset:32768\n"
      "v_exp_f32 v236, v240\n"
      "v_mfma_f32_16x16x32_bf16 v[68:71], a[144:147], v[212:215], v[68:71]\n"
      "s_add_u32 m0, s62, 0\n"
      "ds_read_b128 a[68:71], v242 offset:36864\n"
      "v_fmamk_f32 v241, v145, 0x3e0293ee, v226\n"
      "v_add_f32 v230, v230, v234\n"
      "v_mfma_f32_16x16x32_bf16 v[72:75], a[144:147], v[216:219], v[72:75]\n"
      "ds_read_b128 a[72:75], v243 offset:32768\n"
      "buffer_load_dwordx4 v248, %[srdk], s60 offen nt lds\n"
      "v_exp_f32 v237, v241\n"
      "v_mfma_f32_16x16x32_bf16 v[76:79], a[144:147], v[220:223], v[76:79]\n"
      "s_add_u32 m0, s62, 896\n"
      "ds_read_b128 a[76:79], v243 offset:36864\n"
      "v_fmamk_f32 v240, v146, 0x3e0293ee, v226\n"
      "v_add_f32 v234, v236, v237\n"
      "v_cvt_pk_bf16_f32 v200, v236, v237\n"
      "v_mfma_f32_16x16x32_bf16 v[80:83], a[148:151], v[208:211], v[80:83]\n"
      "ds_read_b128 a[80:83], v242 offset:33792\n"
      "buffer_load_dwordx4 v248, %[srdk], s60 offen offset:128 nt lds\n"
      "v_exp_f32 v238, v240\n"
      "v_mfma_f32_16x16x32_bf16 v[84:87], a[148:151], v[212:215], v[84:87]\n"
      "s_add_u32 m0, s62, 8192\n"
      "ds_read_b128 a[84:87], v242 offset:37888\n"
      "v_fmamk_f32 v241, v147, 0x3e0293ee, v226\n"
      "v_add_f32 v234, v234, v238\n"
      "v_mfma_f32_16x16x32_bf16 v[88:91], a[148:151], v[216:219], v[88:91]\n"
      "ds_read_b128 a[88:91], v243 offset:33792\n"
      "buffer_load_dwordx4 v248, %[srdk], s61 offen nt lds\n"
      "v_exp_f32 v239, v241\n"
      "v_mfma_f32_16x16x32_bf16 v[92:95], a[148:151], v[220:223], v[92:95]\n"
      "s_add_u32 m0, s62, 9088\n"
      "ds_read_b128 a[92:95], v243 offset:37888\n"
      "v_fmamk_f32 v240, v152, 0x3e0293ee, v227\n"
      "v_add_f32 v234, v234, v239\n"
      "v_cvt_pk_bf16_f32 v201, v238, v239\n"
      "v_mfma_f32_16x16x32_bf16 v[96:99], a[152:155], v[208:211], v[96:99]\n"
      "buffer_load_dwordx4 v248, %[srdk], s61 offen offset:128 nt lds\n"
      "v_exp_f32 v236, v240\n"
      "v_mfma_f32_16x16x32_bf16 v[100:103], a[152:155], v[212:215], v[100:103]\n"
      "v_fmamk_f32 v241, v153, 0x3e0293ee, v227\n"
      "v_add_f32 v231, v231, v235\n"
      "v_mfma_f32_16x16x32_bf16 v[104:107], a[152:155], v[216:219], v[104:107]\n"
      "v_exp_f32 v237, v241\n"
      "v_mfma_f32_16x16x32_bf16 v[108:111], a[152:155], v[220:223], v[108:111]\n"
      "v_fmamk_f32 v240, v154, 0x3e0293ee, v227\n"
      "v_add_f32 v235, v236, v237\n"
      "v_cvt_pk_bf16_f32 v204, v236, v237\n"
      "v_mfma_f32_16x16x32_bf16 v[112:115], a[156:159], v[208:211], v[112:115]\n"
      "v_exp_f32 v238, v240\n"
      "v_mfma_f32_16x16x32_bf16 v[116:119], a[156:159], v[212:215], v[116:119]\n"
      "v_fmamk_f32 v241, v155, 0x3e0293ee, v227\n"
      "v_add_f32 v235, v235, v238\n"
      "v_mfma_f32_16x16x32_bf16 v[120:123], a[156:159], v[216:219], v[120:123]\n"
      "v_exp_f32 v239, v241\n"
      "v_mfma_f32_16x16x32_bf16 v[124:127], a[156:159], v[220:223], v[124:127]\n"
      "s_waitcnt lgkmcnt(0)\n"
      "v_fmamk_f32 v240, v132, 0x3e0293ee, v224\n"
      "v_add_f32 v235, v235, v239\n"
      "v_cvt_pk_bf16_f32 v205, v238, v239\n"
      "v_mfma_f32_16x16x32_bf16 v[160:163], a[64:67], a[0:3], 0\n"
      "s_sub_u32 s67, s68, s66\n"
      "v_exp_f32 v236, v240\n"
      "v_mfma_f32_16x16x32_bf16 v[168:171], a[64:67], a[4:7], 0\n"
      "s_cmp_gt_i32 s67, s70\n"
      "ds_read_b64_tr_b16 a[96:97], v244 offset:32768\n"
      "v_fmamk_f32 v241, v133, 0x3e0293ee, v224\n"
      "v_add_f32 v232, v232, v236\n"
      "v_mfma_f32_16x16x32_bf16 v[176:179], a[64:67], a[8:11], 0\n"
      "s_cselect_b32 s69, s86, 0\n"
      "ds_read_b64_tr_b16 a[98:99], v244 offset:36864\n"
      "v_exp_f32 v237, v241\n"
      "v_mfma_f32_16x16x32_bf16 v[184:187], a[64:67], a[12:15], 0\n"
      "s_add_u32 s66, s66, s69\n"
      "ds_read_b64_tr_b16 a[100:101], v245 offset:32768\n"
      "v_fmamk_f32 v240, v134, 0x3e0293ee, v224\n"
      "v_add_f32 v232, v232, v237\n"
      "v_cvt_pk_bf16_f32 v194, v236, v237\n"
      "v_mfma_f32_16x16x32_bf16 v[164:167], a[68:71], a[0:3], 0\n"
      "s_add_u32 s67, s67, 50\n"
      "ds_read_b64_tr_b16 a[102:103], v245 offset:36864\n"
      "v_exp_f32 v238, v240\n"
      "v_mfma_f32_16x16x32_bf16 v[172:175], a[68:71], a[4:7], 0\n"
      "s_min_i32 s70, s70, s67\n"
      "ds_read_b64_tr_b16 a[104:105], v246 offset:32768\n"
      "v_fmamk_f32 v241, v135, 0x3e0293ee, v224\n"
      "v_add_f32 v232, v232, v238\n"
      "v_mfma_f32_16x16x32_bf16 v[180:183], a[68:71], a[8:11], 0\n"
      "s_max_i32 s70, s70, 85\n"
      "ds_read_b64_tr_b16 a[106:107], v246 offset:36864\n"
      "v_exp_f32 v239, v241\n"
      "v_mfma_f32_16x16x32_bf16 v[188:191], a[68:71], a[12:15], 0\n"
      "s_add_u32 s66, s66, s86\n"
      "ds_read_b64_tr_b16 a[108:109], v247 offset:32768\n"
      "v_fmamk_f32 v240, v140, 0x3e0293ee, v225\n"
      "v_add_f32 v232, v232, v239\n"
      "v_cvt_pk_bf16_f32 v195, v238, v239\n"
      "v_mfma_f32_16x16x32_bf16 v[160:163], a[72:75], a[16:19], v[160:163]\n"
      "s_and_b32 s76, s80, 31\n"
      "ds_read_b64_tr_b16 a[110:111], v247 offset:36864\n"
      "v_exp_f32 v236, v240\n"
      "v_mfma_f32_16x16x32_bf16 v[168:171], a[72:75], a[20:23], v[168:171]\n"
      "s_lshr_b64 s[80:81], s[80:81], 5\n"
      "ds_read_b64_tr_b16 a[112:113], v244 offset:33792\n"
      "v_fmamk_f32 v241, v141, 0x3e0293ee, v225\n"
      "v_add_f32 v233, v233, v236\n"
      "v_mfma_f32_16x16x32_bf16 v[176:179], a[72:75], a[24:27], v[176:179]\n"
      "s_add_u32 s66, s66, s76\n"
      "ds_read_b64_tr_b16 a[114:115], v244 offset:37888\n"
      "v_exp_f32 v237, v241\n"
      "v_mfma_f32_16x16x32_bf16 v[184:187], a[72:75], a[28:31], v[184:187]\n"
      "s_sub_u32 s67, s68, s82\n"
      "ds_read_b64_tr_b16 a[116:117], v245 offset:33792\n"
      "v_fmamk_f32 v240, v142, 0x3e0293ee, v225\n"
      "v_add_f32 v233, v233, v237\n"
      "v_cvt_pk_bf16_f32 v198, v236, v237\n"
      "v_mfma_f32_16x16x32_bf16 v[164:167], a[76:79], a[16:19], v[164:167]\n"
      "s_min_i32 s76, s67, s83\n"
      "ds_read_b64_tr_b16 a[118:119], v245 offset:37888\n"
      "v_exp_f32 v238, v240\n"
      "v_mfma_f32_16x16x32_bf16 v[172:175], a[76:79], a[20:23], v[172:175]\n"
      "s_mov_b32 s83, s67\n"
      "ds_read_b64_tr_b16 a[120:121], v246 offset:33792\n"
      "v_fmamk_f32 v241, v143, 0x3e0293ee, v225\n"
      "v_add_f32 v233, v233, v238\n"
      "v_mfma_f32_16x16x32_bf16 v[180:183], a[76:79], a[24:27], v[180:183]\n"
      "s_sub_u32 s76, s76, 26\n"
      "ds_read_b64_tr_b16 a[122:123], v246 offset:37888\n"
      "v_exp_f32 v239, v241\n"
      "v_mfma_f32_16x16x32_bf16 v[188:191], a[76:79], a[28:31], v[188:191]\n"
      "s_min_i32 s76, s76, 148\n"
      "ds_read_b64_tr_b16 a[124:125], v247 offset:33792\n"
      "v_fmamk_f32 v240, v148, 0x3e0293ee, v226\n"
      "v_add_f32 v233, v233, v239\n"
      "v_cvt_pk_bf16_f32 v199, v238, v239\n"
      "v_mfma_f32_16x16x32_bf16 v[160:163], a[80:83], a[32:35], v[160:163]\n"
      "s_max_i32 s76, s76, 94\n"
      "ds_read_b64_tr_b16 a[126:127], v247 offset:37888\n"
      "v_exp_f32 v236, v240\n"
      "v_mfma_f32_16x16x32_bf16 v[168:171], a[80:83], a[36:39], v[168:171]\n"
      "s_mov_b32 s82, s68\n"
      "v_fmamk_f32 v241, v149, 0x3e0293ee, v226\n"
      "v_add_f32 v234, v234, v236\n"
      "v_mfma_f32_16x16x32_bf16 v[176:179], a[80:83], a[40:43], v[176:179]\n"
      "s_add_u32 s65, s82, s76\n"
      "v_exp_f32 v237, v241\n"
      "v_mfma_f32_16x16x32_bf16 v[184:187], a[80:83], a[44:47], v[184:187]\n"
      "s_sub_u32 s93, s93, 1\n"
      "v_fmamk_f32 v240, v150, 0x3e0293ee, v226\n"
      "v_add_f32 v234, v234, v237\n"
      "v_cvt_pk_bf16_f32 v202, v236, v237\n"
      "v_mfma_f32_16x16x32_bf16 v[164:167], a[84:87], a[32:35], v[164:167]\n"
      "s_cmp_lt_i32 s93, 10\n"
      "v_exp_f32 v238, v240\n"
      "v_mfma_f32_16x16x32_bf16 v[172:175], a[84:87], a[36:39], v[172:175]\n"
      "s_cselect_b32 s69, 3, 0\n"
      "v_fmamk_f32 v241, v151, 0x3e0293ee, v226\n"
      "v_add_f32 v234, v234, v238\n"
      "v_mfma_f32_16x16x32_bf16 v[180:183], a[84:87], a[40:43], v[180:183]\n"
      "s_sub_u32 s66, s66, s69\n"
      "v_exp_f32 v239, v241\n"
      "v_mfma_f32_16x16x32_bf16 v[188:191], a[84:87], a[44:47], v[188:191]\n"
      "s_sub_u32 s67, s65, s66\n"
      "v_fmamk_f32 v240, v156, 0x3e0293ee, v227\n"
      "v_add_f32 v234, v234, v239\n"
      "v_cvt_pk_bf16_f32 v203, v238, v239\n"
      "v_mfma_f32_16x16x32_bf16 v[160:163], a[88:91], a[48:51], v[160:163]\n"
      "s_cmp_lt_i32 s67, 0\n"
      "v_exp_f32 v236, v240\n"
      "v_mfma_f32_16x16x32_bf16 v[168:171], a[88:91], a[52:55], v[168:171]\n"
      "s_cselect_b32 s88, s66, s65\n"
      "v_fmamk_f32 v241, v157, 0x3e0293ee, v227\n"
      "v_add_f32 v235, v235, v236\n"
      "v_mfma_f32_16x16x32_bf16 v[176:179], a[88:91], a[56:59], v[176:179]\n"
      "s_add_u32 s90, s88, s91\n"
      "v_exp_f32 v237, v241\n"
      "v_mfma_f32_16x16x32_bf16 v[184:187], a[88:91], a[60:63], v[184:187]\n"
      "v_fmamk_f32 v240, v158, 0x3e0293ee, v227\n"
      "v_add_f32 v235, v235, v237\n"
      "v_cvt_pk_bf16_f32 v206, v236, v237\n"
      "v_mfma_f32_16x16x32_bf16 v[164:167], a[92:95], a[48:51], v[164:167]\n"
      "s_mov_b32 s64, 0\n"
      "v_exp_f32 v238, v240\n"
      "v_mfma_f32_16x16x32_bf16 v[172:175], a[92:95], a[52:55], v[172:175]\n"
      "v_fmamk_f32 v241, v159, 0x3e0293ee, v227\n"
      "v_add_f32 v235, v235, v238\n"
      "v_mfma_f32_16x16x32_bf16 v[180:183], a[92:95], a[56:59], v[180:183]\n"
      "v_exp_f32 v239, v241\n"
      "v_mfma_f32_16x16x32_bf16 v[188:191], a[92:95], a[60:63], v[188:191]\n"
      "s_waitcnt lgkmcnt(8)\n"
      "v_add_f32 v235, v235, v239\n"
      "v_cvt_pk_bf16_f32 v207, v238, v239\n"
      "v_max3_f32 v240, v232, v233, v234\n"
      "v_add_f32 v240, v240, v235\n"
      "v_cmp_nge_f32 vcc, 0x44800000, v240\n"
      "s_cbranch_vccnz FB0_%=\n"
      "RET0_0_%=:\n"
      "v_mfma_f32_16x16x32_bf16 v[0:3], a[96:99], v[192:195], v[0:3]\n"
      "s_add_u32 m0, s62, %[v0]\n"
      "v_fmamk_f32 v240, v160, 0x3e0293ee, v224\n"
      "v_exp_f32 v236, v240\n"
      "v_mfma_f32_16x16x32_bf16 v[4:7], a[96:99], v[196:199], v[4:7]\n"
      "buffer_load_dwordx4 v249, %[srdv], s77 offen nt lds\n"
      "v_fmamk_f32 v241, v161, 0x3e0293ee, v224\n"
      "v_add_f32 v228, v228, v232\n"
      "v_mfma_f32_16x16x32_bf16 v[8:11], a[96:99], v[200:203], v[8:11]\n"
      "s_add_u32 m0, s62, %[v1]\n"
      "v_exp_f32 v237, v241\n"
      "v_xor_b32 v242, 0x10000, v242\n"
      "v_mfma_f32_16x16x32_bf16 v[12:15], a[96:99], v[204:207], v[12:15]\n"
      "buffer_load_dwordx4 v249, %[srdv], s77 offen offset:128 nt lds\n"
      "v_fmamk_f32 v240, v162, 0x3e0293ee, v224\n"
      "v_add_f32 v232, v236, v237\n"
      "v_cvt_pk_bf16_f32 v208, v236, v237\n"
      "v_mfma_f32_16x16x32_bf16 v[16:19], a[100:103], v[192:195], v[16:19]\n"
      "s_add_u32 m0, s62, %[v2]\n"
      "v_exp_f32 v238, v240\n"
      "v_xor_b32 v243, 0x10000, v243\n"
      "v_mfma_f32_16x16x32_bf16 v[20:23], a[100:103], v[196:199], v[20:23]\n"
      "buffer_load_dwordx4 v249, %[srdv], s78 offen nt lds\n"
      "v_fmamk_f32 v241, v163, 0x3e0293ee, v224\n"
      "v_add_f32 v232, v232, v238\n"
      "v_mfma_f32_16x16x32_bf16 v[24:27], a[100:103], v[200:203], v[24:27]\n"
      "s_add_u32 m0, s62, %[v3]\n"
      "v_exp_f32 v239, v241\n"
      "v_xor_b32 v244, 0x10000, v244\n"
      "v_mfma_f32_16x16x32_bf16 v[28:31], a[100:103], v[204:207], v[28:31]\n"
      "s_mov_b32 s77, s60\n"
      "buffer_load_dwordx4 v249, %[srdv], s78 offen offset:128 nt lds\n"
      "v_fmamk_f32 v240, v168, 0x3e0293ee, v225\n"
      "v_add_f32 v232, v232, v239\n"
      "v_cvt_pk_bf16_f32 v209, v238, v239\n"
      "v_mfma_f32_16x16x32_bf16 v[32:35], a[104:107], v[192:195], v[32:35]\n"
      "s_mov_b32 s78, s61\n"
      "v_exp_f32 v236, v240\n"
      "v_xor_b32 v245, 0x10000, v245\n"
      "v_mfma_f32_16x16x32_bf16 v[36:39], a[104:107], v[196:199], v[36:39]\n"
      "s_add_u32 s60, s60, s79\n"
      "v_fmamk_f32 v241, v169, 0x3e0293ee, v225\n"
      "v_add_f32 v229, v229, v233\n"
      "v_mfma_f32_16x16x32_bf16 v[40:43], a[104:107], v[200:203], v[40:43]\n"
      "s_add_u32 s61, s61, s79\n"
      "v_exp_f32 v237, v241\n"
      "v_xor_b32 v246, 0x10000, v246\n"
      "v_mfma_f32_16x16x32_bf16 v[44:47], a[104:107], v[204:207], v[44:47]\n"
      "s_sub_u32 s79, 0x10000, s79\n"
      "v_fmamk_f32 v240, v170, 0x3e0293ee, v225\n"
      "v_add_f32 v233, v236, v237\n"
      "v_cvt_pk_bf16_f32 v212, v236, v237\n"
      "v_mfma_f32_16x16x32_bf16 v[48:51], a[108:111], v[192:195], v[48:51]\n"
      "s_memrealtime s[68:69]\n"
      "v_exp_f32 v238, v240\n"
      "v_xor_b32 v247, 0x10000, v247\n"
      "v_mfma_f32_16x16x32_bf16 v[52:55], a[108:111], v[196:199], v[52:55]\n"
      "s_add_u32 s62, s62, 0x8000\n"
      "v_fmamk_f32 v241, v171, 0x3e0293ee, v225\n"
      "v_add_f32 v233, v233, v238\n"
      "v_mfma_f32_16x16x32_bf16 v[56:59], a[108:111], v[200:203], v[56:59]\n"
      "s_and_b32 s62, s62, 0x1ffff\n"
      "v_exp_f32 v239, v241\n"
      "v_mfma_f32_16x16x32_bf16 v[60:63], a[108:111], v[204:207], v[60:63]\n"
      "s_sub_u32 s63, s63, 1\n"
      "v_fmamk_f32 v240, v176, 0x3e0293ee, v226\n"
      "v_add_f32 v233, v233, v239\n"
      "v_cvt_pk_bf16_f32 v213, v238, v239\n"
      "s_cbranch_scc0 LOOP1_%=\n"
      "s_waitcnt vmcnt(8) lgkmcnt(0)\n"
      "s_sub_u32 s67, s68, s90\n"
      "s_cmp_lt_i32 s67, 0\n"
      "s_cbranch_scc1 GW2_%=\n"
      "GX2_%=:\n"
      "s_barrier\n"
      "v_mfma_f32_16x16x32_bf16 v[64:67], a[112:115], v[192:195], v[64:67]\n"
      "s_memrealtime s[68:69]\n"
      "ds_read_b128 a[64:67], v242 offset:0\n"
      "v_exp_f32 v236, v240\n"
      "v_mfma_f32_16x16x32_bf16 v[68:71], a[112:115], v[196:199], v[68:71]\n"
      "s_add_u32 m0, s62, 0\n"
      "ds_read_b128 a[68:71], v242 offset:4096\n"
      "v_fmamk_f32 v241, v177, 0x3e0293ee, v226\n"
      "v_add_f32 v230, v230, v234\n"
      "v_mfma_f32_16x16x32_bf16 v[72:75], a[112:115], v[200:203], v[72:75]\n"
      "ds_read_b128 a[72:75], v243 offset:0\n"
      "buffer_load_dwordx4 v248, %[srdk], s60 offen nt lds\n"
      "v_exp_f32 v237, v241\n"
      "v_mfma_f32_16x16x32_bf16 v[76:79], a[112:115], v[204:207], v[76:79]\n"
      "s_add_u32 m0, s62, 896\n"
      "ds_read_b128 a[76:79], v243 offset:4096\n"
      "v_fmamk_f32 v240, v178, 0x3e0293ee, v226\n"
      "v_add_f32 v234, v236, v237\n"
      "v_cvt_pk_bf16_f32 v216, v236, v237\n"
      "v_mfma_f32_16x16x32_bf16 v[80:83], a[116:119], v[192:195], v[80:83]\n"
      "ds_read_b128 a[80:83], v242 offset:1024\n"
      "buffer_load_dwordx4 v248, %[srdk], s60 offen offset:128 nt lds\n"
      "v_exp_f32 v238, v240\n"
      "v_mfma_f32_16x16x32_bf16 v[84:87], a[116:119], v[196:199], v[84:87]\n"
      "s_add_u32 m0, s62, 8192\n"
      "ds_read_b128 a[84:87], v242 offset:5120\n"
      "v_fmamk_f32 v241, v179, 0x3e0293ee, v226\n"
      "v_add_f32 v234, v234, v238\n"
      "v_mfma_f32_16x16x32_bf16 v[88:91], a[116:119], v[200:203], v[88:91]\n"
      "ds_read_b128 a[88:91], v243 offset:1024\n"
      "buffer_load_dwordx4 v248, %[srdk], s61 offen nt lds\n"
      "v_exp_f32 v239, v241\n"
      "v_mfma_f32_16x16x32_bf16 v[92:95], a[116:119], v[204:207], v[92:95]\n"
      "s_add_u32 m0, s62, 9088\n"
      "ds_read_b128 a[92:95], v243 offset:5120\n"
      "v_fmamk_f32 v240, v184, 0x3e0293ee, v227\n"
      "v_add_f32 v234, v234, v239\n"
      "v_cvt_pk_bf16_f32 v217, v238, v239\n"
      "v_mfma_f32_16x16x32_bf16 v[96:99], a[120:123], v[192:195], v[96:99]\n"
      "buffer_load_dwordx4 v248, %[srdk], s61 offen offset:128 nt lds\n"
      "v_exp_f32 v236, v240\n"
      "v_mfma_f32_16x16x32_bf16 v[100:103], a[120:123], v[196:199], v[100:103]\n"
      "v_fmamk_f32 v241, v185, 0x3e0293ee, v227\n"
      "v_add_f32 v231, v231, v235\n"
      "v_mfma_f32_16x16x32_bf16 v[104:107], a[120:123], v[200:203], v[104:107]\n"
      "v_exp_f32 v237, v241\n"
      "v_mfma_f32_16x16x32_bf16 v[108:111], a[120:123], v[204:207], v[108:111]\n"
      "v_fmamk_f32 v240, v186, 0x3e0293ee, v227\n"
      "v_add_f32 v235, v236, v237\n"
      "v_cvt_pk_bf16_f32 v220, v236, v237\n"
      "v_mfma_f32_16x16x32_bf16 v[112:115], a[124:127], v[192:195], v[112:115]\n"
      "v_exp_f32 v238, v240\n"
      "v_mfma_f32_16x16x32_bf16 v[116:119], a[124:127], v[196:199], v[116:119]\n"
      "v_fmamk_f32 v241, v187, 0x3e0293ee, v227\n"
      "v_add_f32 v235, v235, v238\n"
      "v_mfma_f32_16x16x32_bf16 v[120:123], a[124:127], v[200:203], v[120:123]\n"
      "v_exp_f32 v239, v241\n"
      "v_mfma_f32_16x16x32_bf16 v[124:127], a[124:127], v[204:207], v[124:127]\n"
      "s_waitcnt lgkmcnt(0)\n"
      "v_fmamk_f32 v240, v164, 0x3e0293ee, v224\n"
      "v_add_f32 v235, v235, v239\n"
      "v_cvt_pk_bf16_f32 v221, v238, v239\n"
      "v_mfma_f32_16x16x32_bf16 v[128:131], a[64:67], a[0:3], 0\n"
      "s_sub_u32 s67, s68, s66\n"
      "v_exp_f32 v236, v240\n"
      "v_mfma_f32_16x16x32_bf16 v[136:139], a[64:67], a[4:7], 0\n"
      "s_cmp_gt_i32 s67, s70\n"
      "ds_read_b64_tr_b16 a[128:129], v244 offset:0\n"
      "v_fmamk_f32 v241, v165, 0x3e0293ee, v224\n"
      "v_add_f32 v232, v232, v236\n"
      "v_mfma_f32_16x16x32_bf16 v[144:147], a[64:67], a[8:11], 0\n"
      "s_cselect_b32 s69, s86, 0\n"
      "ds_read_b64_tr_b16 a[130:131], v244 offset:4096\n"
      "v_exp_f32 v237, v241\n"
      "v_mfma_f32_16x16x32_bf16 v[152:155], a[64:67], a[12:15], 0\n"
      "s_add_u32 s66, s66, s69\n"
      "ds_read_b64_tr_b16 a[132:133], v245 offset:0\n"
      "v_fmamk_f32 v240, v166, 0x3e0293ee, v224\n"
      "v_add_f32 v232, v232, v237\n"
      "v_cvt_pk_bf16_f32 v210, v236, v237\n"
      "v_mfma_f32_16x16x32_bf16 v[132:135], a[68:71], a[0:3], 0\n"
      "s_add_u32 s67, s67, 50\n"
      "ds_read_b64_tr_b16 a[134:135], v245 offset:4096\n"
      "v_exp_f32 v238, v240\n"
      "v_mfma_f32_16x16x32_bf16 v[140:143], a[68:71], a[4:7], 0\n"
      "s_min_i32 s70, s70, s67\n"
      "ds_read_b64_tr_b16 a[136:137], v246 offset:0\n"
      "v_fmamk_f32 v241, v167, 0x3e0293ee, v224\n"
      "v_add_f32 v232, v232, v238\n"
      "v_mfma_f32_16x16x32_bf16 v[148:151], a[68:71], a[8:11], 0\n"
      "s_max_i32 s70, s70, 85\n"
      "ds_read_b64_tr_b16 a[138:139], v246 offset:4096\n"
      "v_exp_f32 v239, v241\n"
      "v_mfma_f32_16x16x32_bf16 v[156:159], a[68:71], a[12:15], 0\n"
      "s_add_u32 s66, s66, s86\n"
      "ds_read_b64_tr_b16 a[140:141], v247 offset:0\n"
      "v_fmamk_f32 v240, v172, 0x3e0293ee, v225\n"
      "v_add_f32 v232, v232, v239\n"
      "v_cvt_pk_bf16_f32 v211, v238, v239\n"
      "v_mfma_f32_16x16x32_bf16 v[128:131], a[72:75], a[16:19], v[128:131]\n"
      "s_and_b32 s76, s80, 31\n"
      "ds_read_b64_tr_b16 a[142:143], v247 offset:4096\n"
      "v_exp_f32 v236, v240\n"
      "v_mfma_f32_16x16x32_bf16 v[136:139], a[72:75], a[20:23], v[136:139]\n"
      "s_lshr_b64 s[80:81], s[80:81], 5\n"
      "ds_read_b64_tr_b16 a[144:145], v244 offset:1024\n"
      "v_fmamk_f32 v241, v173, 0x3e0293ee, v225\n"
      "v_add_f32 v233, v233, v236\n"
      "v_mfma_f32_16x16x32_bf16 v[144:147], a[72:75], a[24:27], v[144:147]\n"
      "s_add_u32 s66, s66, s76\n"
      "ds_read_b64_tr_b16 a[146:147], v244 offset:5120\n"
      "v_exp_f32 v237, v241\n"
      "v_mfma_f32_16x16x32_bf16 v[152:155], a[72:75], a[28:31], v[152:155]\n"
      "s_sub_u32 s67, s68, s82\n"
      "ds_read_b64_tr_b16 a[148:149], v245 offset:1024\n"
      "v_fmamk_f32 v240, v174, 0x3e0293ee, v225\n"
      "v_add_f32 v233, v233, v237\n"
      "v_cvt_pk_bf16_f32 v214, v236, v237\n"
      "v_mfma_f32_16x16x32_bf16 v[132:135], a[76:79], a[16:19], v[132:135]\n"
      "s_min_i32 s76, s67, s83\n"
      "ds_read_b64_tr_b16 a[150:151], v245 offset:5120\n"
      "v_exp_f32 v238, v240\n"
      "v_mfma_f32_16x16x32_bf16 v[140:143], a[76:79], a[20:23], v[140:143]\n"
      "s_mov_b32 s83, s67\n"
      "ds_read_b64_tr_b16 a[152:153], v246 offset:1024\n"
      "v_fmamk_f32 v241, v175, 0x3e0293ee, v225\n"
      "v_add_f32 v233, v233, v238\n"
      "v_mfma_f32_16x16x32_bf16 v[148:151], a[76:79], a[24:27], v[148:151]\n"
      "s_sub_u32 s76, s76, 26\n"
      "ds_read_b64_tr_b16 a[154:155], v246 offset:5120\n"
      "v_exp_f32 v239, v241\n"
      "v_mfma_f32_16x16x32_bf16 v[156:159], a[76:79], a[28:31], v[156:159]\n"
      "s_min_i32 s76, s76, 148\n"
      "ds_read_b64_tr_b16 a[156:157], v247 offset:1024\n"
      "v_fmamk_f32 v240, v180, 0x3e0293ee, v226\n"
      "v_add_f32 v233, v233, v239\n"
      "v_cvt_pk_bf16_f32 v215, v238, v239\n"
      "v_mfma_f32_16x16x32_bf16 v[128:131], a[80:83], a[32:35], v[128:131]\n"
      "s_max_i32 s76, s76, 94\n"
      "ds_read_b64_tr_b16 a[158:159], v247 offset:5120\n"
      "v_exp_f32 v236, v240\n"
      "v_mfma_f32_16x16x32_bf16 v[136:139], a[80:83], a[36:39], v[136:139]\n"
      "s_mov_b32 s82, s68\n"
      "v_fmamk_f32 v241, v181, 0x3e0293ee, v226\n"
      "v_add_f32 v234, v234, v236\n"
      "v_mfma_f32_16x16x32_bf16 v[144:147], a[80:83], a[40:43], v[144:147]\n"
      "s_lshr_b32 s69, s86, 1\n"
      "v_exp_f32 v237, v241\n"
      "v_mfma_f32_16x16x32_bf16 v[152:155], a[80:83], a[44:47], v[152:155]\n"
      "s_add_u32 s66, s66, s69\n"
      "v_fmamk_f32 v240, v182, 0x3e0293ee, v226\n"
      "v_add_f32 v234, v234, v237\n"
      "v_cvt_pk_bf16_f32 v218, v236, v237\n"
      "v_mfma_f32_16x16x32_bf16 v[132:135], a[84:87], a[32:35], v[132:135]\n"
      "s_add_u32 s82, s82, s69\n"
      "v_exp_f32 v238, v240\n"
      "v_mfma_f32_16x16x32_bf16 v[140:143], a[84:87], a[36:39], v[140:143]\n"
      "s_add_u32 s65, s82, s76\n"
      "v_fmamk_f32 v241, v183, 0x3e0293ee, v226\n"
      "v_add_f32 v234, v234, v238\n"
      "v_mfma_f32_16x16x32_bf16 v[148:151], a[84:87], a[40:43], v[148:151]\n"
      "s_sub_u32 s93, s93, 1\n"
      "v_exp_f32 v239, v241\n"
      "v_mfma_f32_16x16x32_bf16 v[156:159], a[84:87], a[44:47], v[156:159]\n"
      "s_cmp_lt_i32 s93, 10\n"
      "v_fmamk_f32 v240, v188, 0x3e0293ee, v227\n"
      "v_add_f32 v234, v234, v239\n"
      "v_cvt_pk_bf16_f32 v219, v238, v239\n"
      "v_mfma_f32_16x16x32_bf16 v[128:131], a[88:91], a[48:51], v[128:131]\n"
      "s_cselect_b32 s69, 3, 0\n"
      "v_exp_f32 v236, v240\n"
      "v_mfma_f32_16x16x32_bf16 v[136:139], a[88:91], a[52:55], v[136:139]\n"
      "s_sub_u32 s66, s66, s69\n"
      "v_fmamk_f32 v241, v189, 0x3e0293ee, v227\n"
      "v_add_f32 v235, v235, v236\n"
      "v_mfma_f32_16x16x32_bf16 v[144:147], a[88:91], a[56:59], v[144:147]\n"
      "s_sub_u32 s67, s65, s66\n"
      "v_exp_f32 v237, v241\n"
      "v_mfma_f32_16x16x32_bf16 v[152:155], a[88:91], a[60:63], v[152:155]\n"
      "s_cmp_lt_i32 s67, 0\n"
      "s_cselect_b32 s88, s66, s65\n"
      "v_fmamk_f32 v240, v190, 0x3e0293ee, v227\n"
      "v_add_f32 v235, v235, v237\n"
      "v_cvt_pk_bf16_f32 v222, v236, v237\n"
      "v_mfma_f32_16x16x32_bf16 v[132:135], a[92:95], a[48:51], v[132:135]\n"
      "s_mov_b32 s64, 1\n"
      "v_exp_f32 v238, v240\n"
      "v_mfma_f32_16x16x32_bf16 v[140:143], a[92:95], a[52:55], v[140:143]\n"
      "s_add_u32 s90, s88, s91\n"
      "v_fmamk_f32 v241, v191, 0x3e0293ee, v227\n"
      "v_add_f32 v235, v235, v238\n"
      "v_mfma_f32_16x16x32_bf16 v[148:151], a[92:95], a[56:59], v[148:151]\n"
      "v_exp_f32 v239, v241\n"
      "v_mfma_f32_16x16x32_bf16 v[156:159], a[92:95], a[60:63], v[156:159]\n"
      "s_waitcnt lgkmcnt(8)\n"
      "v_add_f32 v235, v235, v239\n"
      "v_cvt_pk_bf16_f32 v223, v238, v239\n"
      "v_max3_f32 v240, v232, v233, v234\n"
      "v_add_f32 v240, v240, v235\n"
      "v_cmp_nge_f32 vcc, 0x44800000, v240\n"
      "s_cbranch_vccnz FB1_%=\n"
      "RET1_1_%=:\n"
      "v_mfma_f32_16x16x32_bf16 v[0:3], a[128:131], v[208:211], v[0:3]\n"
      "s_add_u32 m0, s62, %[v0]\n"
      "v_fmamk_f32 v240, v128, 0x3e0293ee, v224\n"
      "v_exp_f32 v236, v240\n"
      "v_mfma_f32_16x16x32_bf16 v[4:7], a[128:131], v[212:215], v[4:7]\n"
      "buffer_load_dwordx4 v249, %[srdv], s77 offen nt lds\n"
      "v_fmamk_f32 v241, v129, 0x3e0293ee, v224\n"
      "v_add_f32 v228, v228, v232\n"
      "v_mfma_f32_16x16x32_bf16 v[8:11], a[128:131], v[216:219], v[8:11]\n"
      "s_add_u32 m0, s62, %[v1]\n"
      "v_exp_f32 v237, v241\n"
      "v_mfma_f32_16x16x32_bf16 v[12:15], a[128:131], v[220:223], v[12:15]\n"
      "buffer_load_dwordx4 v249, %[srdv], s77 offen offset:128 nt lds\n"
      "v_fmamk_f32 v240, v130, 0x3e0293ee, v224\n"
      "v_add_f32 v232, v236, v237\n"
      "v_cvt_pk_bf16_f32 v192, v236, v237\n"
      "v_mfma_f32_16x16x32_bf16 v[16:19], a[132:135], v[208:211], v[16:19]\n"
      "s_add_u32 m0, s62, %[v2]\n"
      "v_exp_f32 v238, v240\n"
      "v_mfma_f32_16x16x32_bf16 v[20:23], a[132:135], v[212:215], v[20:23]\n"
      "buffer_load_dwordx4 v249, %[srdv], s78 offen nt lds\n"
      "v_fmamk_f32 v241, v131, 0x3e0293ee, v224\n"
      "v_add_f32 v232, v232, v238\n"
      "v_mfma_f32_16x16x32_bf16 v[24:27], a[132:135], v[216:219], v[24:27]\n"
      "s_add_u32 m0, s62, %[v3]\n"
      "v_exp_f32 v239, v241\n"
      "v_mfma_f32_16x16x32_bf16 v[28:31], a[132:135], v[220:223], v[28:31]\n"
      "s_mov_b32 s77, s60\n"
      "buffer_load_dwordx4 v249, %[srdv], s78 offen offset:128 nt lds\n"
      "v_fmamk_f32 v240, v136, 0x3e0293ee, v225\n"
      "v_add_f32 v232, v232, v239\n"
      "v_cvt_pk_bf16_f32 v193, v238, v239\n"
      "v_mfma_f32_16x16x32_bf16 v[32:35], a[136:139], v[208:211], v[32:35]\n"
      "s_mov_b32 s78, s61\n"
      "v_exp_f32 v236, v240\n"
      "v_mfma_f32_16x16x32_bf16 v[36:39], a[136:139], v[212:215], v[36:39]\n"
      "s_add_u32 s60, s60, s79\n"
      "v_fmamk_f32 v241, v137, 0x3e0293ee, v225\n"
      "v_add_f32 v229, v229, v233\n"
      "v_mfma_f32_16x16x32_bf16 v[40:43], a[136:139], v[216:219], v[40:43]\n"
      "s_add_u32 s61, s61, s79\n"
      "v_exp_f32 v237, v241\n"
      "v_mfma_f32_16x16x32_bf16 v[44:47], a[136:139], v[220:223], v[44:47]\n"
      "s_sub_u32 s79, 0x10000, s79\n"
      "v_fmamk_f32 v240, v138, 0x3e0293ee, v225\n"
      "v_add_f32 v233, v236, v237\n"
      "v_cvt_pk_bf16_f32 v196, v236, v237\n"
      "v_mfma_f32_16x16x32_bf16 v[48:51], a[140:143], v[208:211], v[48:51]\n"
      "s_memrealtime s[68:69]\n"
      "v_exp_f32 v238, v240\n"
      "v_mfma_f32_16x16x32_bf16 v[52:55], a[140:143], v[212:215], v[52:55]\n"
      "s_add_u32 s62, s62, 0x8000\n"
      "v_fmamk_f32 v241, v139, 0x3e0293ee, v225\n"
      "v_add_f32 v233, v233, v238\n"
      "v_mfma_f32_16x16x32_bf16 v[56:59], a[140:143], v[216:219], v[56:59]\n"
      "s_and_b32 s62, s62, 0x1ffff\n"
      "v_exp_f32 v239, v241\n"
      "v_mfma_f32_16x16x32_bf16 v[60:63], a[140:143], v[220:223], v[60:63]\n"
      "v_fmamk_f32 v240, v144, 0x3e0293ee, v226\n"
      "v_add_f32 v233, v233, v239\n"
      "v_cvt_pk_bf16_f32 v197, v238, v239\n"
      "s_waitcnt vmcnt(8) lgkmcnt(0)\n"
      "s_barrier\n"
      "v_mfma_f32_16x16x32_bf16 v[64:67], a[144:147], v[208:211], v[64:67]\n"
      "ds_read_b128 a[64:67], v242 offset:32768\n"
      "v_exp_f32 v236, v240\n"
      "v_mfma_f32_16x16x32_bf16 v[68:71], a[144:147], v[212:215], v[68:71]\n"
      "ds_read_b128 a[68:71], v242 offset:36864\n"
      "v_fmamk_f32 v241, v145, 0x3e0293ee, v226\n"
      "v_add_f32 v230, v230, v234\n"
      "v_mfma_f32_16x16x32_bf16 v[72:75], a[144:147], v[216:219], v[72:75]\n"
      "ds_read_b128 a[72:75], v243 offset:32768\n"
      "v_exp_f32 v237, v241\n"
      "v_mfma_f32_16x16x32_bf16 v[76:79], a[144:147], v[220:223], v[76:79]\n"
      "ds_read_b128 a[76:79], v243 offset:36864\n"
      "v_fmamk_f32 v240, v146, 0x3e0293ee, v226\n"
      "v_add_f32 v234, v236, v237\n"
      "v_cvt_pk_bf16_f32 v200, v236, v237\n"
      "v_mfma_f32_16x16x32_bf16 v[80:83], a[148:151], v[208:211], v[80:83]\n"
      "ds_read_b128 a[80:83], v242 offset:33792\n"
      "v_exp_f32 v238, v240\n"
      "v_mfma_f32_16x16x32_bf16 v[84:87], a[148:151], v[212:215], v[84:87]\n"
      "ds_read_b128 a[84:87], v242 offset:37888\n"
      "v_fmamk_f32 v241, v147, 0x3e0293ee, v226\n"
      "v_add_f32 v234, v234, v238\n"
      "v_mfma_f32_16x16x32_bf16 v[88:91], a[148:151], v[216:219], v[88:91]\n"
      "ds_read_b128 a[88:91], v243 offset:33792\n"
      "v_exp_f32 v239, v241\n"
      "v_mfma_f32_16x16x32_bf16 v[92:95], a[148:151], v[220:223], v[92:95]\n"
      "ds_read_b128 a[92:95], v243 offset:37888\n"
      "v_fmamk_f32 v240, v152, 0x3e0293ee, v227\n"
      "v_add_f32 v234, v234, v239\n"
      "v_cvt_pk_bf16_f32 v201, v238, v239\n"
      "v_mfma_f32_16x16x32_bf16 v[96:99], a[152:155], v[208:211], v[96:99]\n"
      "v_exp_f32 v236, v240\n"
      "v_mfma_f32_16x16x32_bf16 v[100:103], a[152:155], v[212:215], v[100:103]\n"
      "v_fmamk_f32 v241, v153, 0x3e0293ee, v227\n"
      "v_add_f32 v231, v231, v235\n"
      "v_mfma_f32_16x16x32_bf16 v[104:107], a[152:155], v[216:219], v[104:107]\n"
      "v_exp_f32 v237, v241\n"
      "v_mfma_f32_16x16x32_bf16 v[108:111], a[152:155], v[220:223], v[108:111]\n"
      "v_fmamk_f32 v240, v154, 0x3e0293ee, v227\n"
      "v_add_f32 v235, v236, v237\n"
      "v_cvt_pk_bf16_f32 v204, v236, v237\n"
      "v_mfma_f32_16x16x32_bf16 v[112:115], a[156:159], v[208:211], v[112:115]\n"
      "v_exp_f32 v238, v240\n"
      "v_mfma_f32_16x16x32_bf16 v[116:119], a[156:159], v[212:215], v[116:119]\n"
      "v_fmamk_f32 v241, v155, 0x3e0293ee, v227\n"
      "v_add_f32 v235, v235, v238\n"
      "v_mfma_f32_16x16x32_bf16 v[120:123], a[156:159], v[216:219], v[120:123]\n"
      "v_exp_f32 v239, v241\n"
      "v_mfma_f32_16x16x32_bf16 v[124:127], a[156:159], v[220:223], v[124:127]\n"
      "s_waitcnt lgkmcnt(0)\n"
      "v_fmamk_f32 v240, v132, 0x3e0293ee, v224\n"
      "v_add_f32 v235, v235, v239\n"
      "v_cvt_pk_bf16_f32 v205, v238, v239\n"
      "v_mfma_f32_16x16x32_bf16 v[160:163], a[64:67], a[0:3], 0\n"
      "v_exp_f32 v236, v240\n"
      "v_mfma_f32_16x16x32_bf16 v[168:171], a[64:67], a[4:7], 0\n"
      "ds_read_b64_tr_b16 a[96:97], v244 offset:32768\n"
      "v_fmamk_f32 v241, v133, 0x3e0293ee, v224\n"
      "v_add_f32 v232, v232, v236\n"
      "v_mfma_f32_16x16x32_bf16 v[176:179], a[64:67], a[8:11], 0\n"
      "ds_read_b64_tr_b16 a[98:99], v244 offset:36864\n"
      "v_exp_f32 v237, v241\n"
      "v_mfma_f32_16x16x32_bf16 v[184:187], a[64:67], a[12:15], 0\n"
      "ds_read_b64_tr_b16 a[100:101], v245 offset:32768\n"
      "v_fmamk_f32 v240, v134, 0x3e0293ee, v224\n"
      "v_add_f32 v232, v232, v237\n"
      "v_cvt_pk_bf16_f32 v194, v236, v237\n"
      "v_mfma_f32_16x16x32_bf16 v[164:167], a[68:71], a[0:3], 0\n"
      "ds_read_b64_tr_b16 a[102:103], v245 offset:36864\n"
      "v_exp_f32 v238, v240\n"
      "v_mfma_f32_16x16x32_bf16 v[172:175], a[68:71], a[4:7], 0\n"
      "ds_read_b64_tr_b16 a[104:105], v246 offset:32768\n"
      "v_fmamk_f32 v241, v135, 0x3e0293ee, v224\n"
      "v_add_f32 v232, v232, v238\n"
      "v_mfma_f32_16x16x32_bf16 v[180:183], a[68:71], a[8:11], 0\n"
      "ds_read_b64_tr_b16 a[106:107], v246 offset:36864\n"
      "v_exp_f32 v239, v241\n"
      "v_mfma_f32_16x16x32_bf16 v[188:191], a[68:71], a[12:15], 0\n"
      "ds_read_b64_tr_b16 a[108:109], v247 offset:32768\n"
      "v_fmamk_f32 v240, v140, 0x3e0293ee, v225\n"
      "v_add_f32 v232, v232, v239\n"
      "v_cvt_pk_bf16_f32 v195, v238, v239\n"
      "v_mfma_f32_16x16x32_bf16 v[160:163], a[72:75], a[16:19], v[160:163]\n"
      "ds_read_b64_tr_b16 a[110:111], v247 offset:36864\n"
      "v_exp_f32 v236, v240\n"
      "v_mfma_f32_16x16x32_bf16 v[168:171], a[72:75], a[20:23], v[168:171]\n"
      "ds_read_b64_tr_b16 a[112:113], v244 offset:33792\n"
      "v_fmamk_f32 v241, v141, 0x3e0293ee, v225\n"
      "v_add_f32 v233, v233, v236\n"
      "v_mfma_f32_16x16x32_bf16 v[176:179], a[72:75], a[24:27], v[176:179]\n"
      "ds_read_b64_tr_b16 a[114:115], v244 offset:37888\n"
      "v_exp_f32 v237, v241\n"
      "v_mfma_f32_16x16x32_bf16 v[184:187], a[72:75], a[28:31], v[184:187]\n"
      "ds_read_b64_tr_b16 a[116:117], v245 offset:33792\n"
      "v_fmamk_f32 v240, v142, 0x3e0293ee, v225\n"
      "v_add_f32 v233, v233, v237\n"
      "v_cvt_pk_bf16_f32 v198, v236, v237\n"
      "v_mfma_f32_16x16x32_bf16 v[164:167], a[76:79], a[16:19], v[164:167]\n"
      "ds_read_b64_tr_b16 a[118:119], v245 offset:37888\n"
      "v_exp_f32 v238, v240\n"
      "v_mfma_f32_16x16x32_bf16 v[172:175], a[76:79], a[20:23], v[172:175]\n"
      "ds_read_b64_tr_b16 a[120:121], v246 offset:33792\n"
      "v_fmamk_f32 v241, v143, 0x3e0293ee, v225\n"
      "v_add_f32 v233, v233, v238\n"
      "v_mfma_f32_16x16x32_bf16 v[180:183], a[76:79], a[24:27], v[180:183]\n"
      "ds_read_b64_tr_b16 a[122:123], v246 offset:37888\n"
      "v_exp_f32 v239, v241\n"
      "v_mfma_f32_16x16x32_bf16 v[188:191], a[76:79], a[28:31], v[188:191]\n"
      "ds_read_b64_tr_b16 a[124:125], v247 offset:33792\n"
      "v_fmamk_f32 v240, v148, 0x3e0293ee, v226\n"
      "v_add_f32 v233, v233, v239\n"
      "v_cvt_pk_bf16_f32 v199, v238, v239\n"
      "v_mfma_f32_16x16x32_bf16 v[160:163], a[80:83], a[32:35], v[160:163]\n"
      "ds_read_b64_tr_b16 a[126:127], v247 offset:37888\n"
      "v_exp_f32 v236, v240\n"
      "v_mfma_f32_16x16x32_bf16 v[168:171], a[80:83], a[36:39], v[168:171]\n"
      "s_add_u32 m0, s62, 0\n"
      "v_fmamk_f32 v241, v149, 0x3e0293ee, v226\n"
      "v_add_f32 v234, v234, v236\n"
      "v_mfma_f32_16x16x32_bf16 v[176:179], a[80:83], a[40:43], v[176:179]\n"
      "buffer_load_dwordx4 v248, %[srdk], s60 offen nt lds\n"
      "v_exp_f32 v237, v241\n"
      "v_mfma_f32_16x16x32_bf16 v[184:187], a[80:83], a[44:47], v[184:187]\n"
      "s_add_u32 m0, s62, 896\n"
      "v_fmamk_f32 v240, v150, 0x3e0293ee, v226\n"
      "v_add_f32 v234, v234, v237\n"
      "v_cvt_pk_bf16_f32 v202, v236, v237\n"
      "v_mfma_f32_16x16x32_bf16 v[164:167], a[84:87], a[32:35], v[164:167]\n"
      "buffer_load_dwordx4 v248, %[srdk], s60 offen offset:128 nt lds\n"
      "v_exp_f32 v238, v240\n"
      "v_mfma_f32_16x16x32_bf16 v[172:175], a[84:87], a[36:39], v[172:175]\n"
      "s_add_u32 m0, s62, 8192\n"
      "v_fmamk_f32 v241, v151, 0x3e0293ee, v226\n"
      "v_add_f32 v234, v234, v238\n"
      "v_mfma_f32_16x16x32_bf16 v[180:183], a[84:87], a[40:43], v[180:183]\n"
      "buffer_load_dwordx4 v248, %[srdk], s61 offen nt lds\n"
      "v_exp_f32 v239, v241\n"
      "v_mfma_f32_16x16x32_bf16 v[188:191], a[84:87], a[44:47], v[188:191]\n"
      "s_add_u32 m0, s62, 9088\n"
      "v_fmamk_f32 v240, v156, 0x3e0293ee, v227\n"
      "v_add_f32 v234, v234, v239\n"
      "v_cvt_pk_bf16_f32 v203, v238, v239\n"
      "v_mfma_f32_16x16x32_bf16 v[160:163], a[88:91], a[48:51], v[160:163]\n"
      "buffer_load_dwordx4 v248, %[srdk], s61 offen offset:128 nt lds\n"
      "v_exp_f32 v236, v240\n"
      "v_mfma_f32_16x16x32_bf16 v[168:171], a[88:91], a[52:55], v[168:171]\n"
      "v_fmamk_f32 v241, v157, 0x3e0293ee, v227\n"
      "v_add_f32 v235, v235, v236\n"
      "v_mfma_f32_16x16x32_bf16 v[176:179], a[88:91], a[56:59], v[176:179]\n"
      "v_exp_f32 v237, v241\n"
      "v_mfma_f32_16x16x32_bf16 v[184:187], a[88:91], a[60:63], v[184:187]\n"
      "v_fmamk_f32 v240, v158, 0x3e0293ee, v227\n"
      "v_add_f32 v235, v235, v237\n"
      "v_cvt_pk_bf16_f32 v206, v236, v237\n"
      "v_mfma_f32_16x16x32_bf16 v[164:167], a[92:95], a[48:51], v[164:167]\n"
      "s_mov_b32 s64, 1\n"
      "v_exp_f32 v238, v240\n"
      "v_mfma_f32_16x16x32_bf16 v[172:175], a[92:95], a[52:55], v[172:175]\n"
      "v_fmamk_f32 v241, v159, 0x3e0293ee, v227\n"
      "v_add_f32 v235, v235, v238\n"
      "v_mfma_f32_16x16x32_bf16 v[180:183], a[92:95], a[56:59], v[180:183]\n"
      "v_exp_f32 v239, v241\n"
      "v_mfma_f32_16x16x32_bf16 v[188:191], a[92:95], a[60:63], v[188:191]\n"
      "s_waitcnt lgkmcnt(8)\n"
      "v_add_f32 v235, v235, v239\n"
      "v_cvt_pk_bf16_f32 v207, v238, v239\n"
      "v_max3_f32 v240, v232, v233, v234\n"
      "v_add_f32 v240, v240, v235\n"
      "v_cmp_nge_f32 vcc, 0x44800000, v240\n"
      "s_cbranch_vccnz FB0_%=\n"
      "RET0_1_%=:\n"
      "v_mfma_f32_16x16x32_bf16 v[0:3], a[96:99], v[192:195], v[0:3]\n"
      "v_fmamk_f32 v240, v160, 0x3e0293ee, v224\n"
      "v_exp_f32 v236, v240\n"
      "v_mfma_f32_16x16x32_bf16 v[4:7], a[96:99], v[196:199], v[4:7]\n"
      "v_fmamk_f32 v241, v161, 0x3e0293ee, v224\n"
      "v_add_f32 v228, v228, v232\n"
      "v_mfma_f32_16x16x32_bf16 v[8:11], a[96:99], v[200:203], v[8:11]\n"
      "v_exp_f32 v237, v241\n"
      "v_xor_b32 v242, 0x10000, v242\n"
      "v_mfma_f32_16x16x32_bf16 v[12:15], a[96:99], v[204:207], v[12:15]\n"
      "v_fmamk_f32 v240, v162, 0x3e0293ee, v224\n"
      "v_add_f32 v232, v236, v237\n"
      "v_cvt_pk_bf16_f32 v208, v236, v237\n"
      "v_mfma_f32_16x16x32_bf16 v[16:19], a[100:103], v[192:195], v[16:19]\n"
      "v_exp_f32 v238, v240\n"
      "v_xor_b32 v243, 0x10000, v243\n"
      "v_mfma_f32_16x16x32_bf16 v[20:23], a[100:103], v[196:199], v[20:23]\n"
      "v_fmamk_f32 v241, v163, 0x3e0293ee, v224\n"
      "v_add_f32 v232, v232, v238\n"
      "v_mfma_f32_16x16x32_bf16 v[24:27], a[100:103], v[200:203], v[24:27]\n"
      "v_exp_f32 v239, v241\n"
      "v_xor_b32 v244, 0x10000, v244\n"
      "v_mfma_f32_16x16x32_bf16 v[28:31], a[100:103], v[204:207], v[28:31]\n"
      "v_fmamk_f32 v240, v168, 0x3e0293ee, v225\n"
      "v_add_f32 v232, v232, v239\n"
      "v_cvt_pk_bf16_f32 v209, v238, v239\n"
      "v_mfma_f32_16x16x32_bf16 v[32:35], a[104:107], v[192:195], v[32:35]\n"
      "v_exp_f32 v236, v240\n"
      "v_xor_b32 v245, 0x10000, v245\n"
      "v_mfma_f32_16x16x32_bf16 v[36:39], a[104:107], v[196:199], v[36:39]\n"
      "v_fmamk_f32 v241, v169, 0x3e0293ee, v225\n"
      "v_add_f32 v229, v229, v233\n"
      "v_mfma_f32_16x16x32_bf16 v[40:43], a[104:107], v[200:203], v[40:43]\n"
      "v_exp_f32 v237, v241\n"
      "v_xor_b32 v246, 0x10000, v246\n"
      "v_mfma_f32_16x16x32_bf16 v[44:47], a[104:107], v[204:207], v[44:47]\n"
      "v_fmamk_f32 v240, v170, 0x3e0293ee, v225\n"
      "v_add_f32 v233, v236, v237\n"
      "v_cvt_pk_bf16_f32 v212, v236, v237\n"
      "v_mfma_f32_16x16x32_bf16 v[48:51], a[108:111], v[192:195], v[48:51]\n"
      "s_memrealtime s[68:69]\n"
      "v_exp_f32 v238, v240\n"
      "v_xor_b32 v247, 0x10000, v247\n"
      "v_mfma_f32_16x16x32_bf16 v[52:55], a[108:111], v[196:199], v[52:55]\n"
      "v_fmamk_f32 v241, v171, 0x3e0293ee, v225\n"
      "v_add_f32 v233, v233, v238\n"
      "v_mfma_f32_16x16x32_bf16 v[56:59], a[108:111], v[200:203], v[56:59]\n"
      "v_exp_f32 v239, v241\n"
      "v_mfma_f32_16x16x32_bf16 v[60:63], a[108:111], v[204:207], v[60:63]\n"
      "v_fmamk_f32 v240, v176, 0x3e0293ee, v226\n"
      "v_add_f32 v233, v233, v239\n"
      "v_cvt_pk_bf16_f32 v213, v238, v239\n"
      "s_sub_u32 s63, %[n2], 1\n"
      "LOOP2_%=:\n"
      "s_waitcnt vmcnt(8) lgkmcnt(0)\n"
      "s_sub_u32 s67, s68, s90\n"
      "s_cmp_lt_i32 s67, 0\n"
      "s_cbranch_scc1 GW3_%=\n"
      "GX3_%=:\n"
      "s_barrier\n"
      "v_mfma_f32_16x16x32_bf16 v[64:67], a[112:115], v[192:195], v[64:67]\n"
      "s_memrealtime s[68:69]\n"
      "ds_read_b128 a[64:67], v242 offset:0\n"
      "v_exp_f32 v236, v240\n"
      "v_mfma_f32_16x16x32_bf16 v[68:71], a[112:115], v[196:199], v[68:71]\n"
      "s_add_u32 m0, s62, %[v0]\n"
      "ds_read_b128 a[68:71], v242 offset:4096\n"
      "v_fmamk_f32 v241, v177, 0x3e0293ee, v226\n"
      "v_add_f32 v230, v230, v234\n"
      "v_mfma_f32_16x16x32_bf16 v[72:75], a[112:115], v[200:203], v[72:75]\n"
      "ds_read_b128 a[72:75], v243 offset:0\n"
      "buffer_load_dwordx4 v249, %[srdv], s77 offen nt lds\n"
      "v_exp_f32 v237, v241\n"
      "v_mfma_f32_16x16x32_bf16 v[76:79], a[112:115], v[204:207], v[76:79]\n"
      "s_add_u32 m0, s62, %[v1]\n"
      "ds_read_b128 a[76:79], v243 offset:4096\n"
      "v_fmamk_f32 v240, v178, 0x3e0293ee, v226\n"
      "v_add_f32 v234, v236, v237\n"
      "v_cvt_pk_bf16_f32 v216, v236, v237\n"
      "v_mfma_f32_16x16x32_bf16 v[80:83], a[116:119], v[192:195], v[80:83]\n"
      "ds_read_b128 a[80:83], v242 offset:1024\n"
      "buffer_load_dwordx4 v249, %[srdv], s77 offen offset:128 nt lds\n"
      "v_exp_f32 v238, v240\n"
      "v_mfma_f32_16x16x32_bf16 v[84:87], a[116:119], v[196:199], v[84:87]\n"
      "s_add_u32 m0, s62, %[v2]\n"
      "ds_read_b128 a[84:87], v242 offset:5120\n"
      "v_fmamk_f32 v241, v179, 0x3e0293ee, v226\n"
      "v_add_f32 v234, v234, v238\n"
      "v_mfma_f32_16x16x32_bf16 v[88:91], a[116:119], v[200:203], v[88:91]\n"
      "ds_read_b128 a[88:91], v243 offset:1024\n"
      "buffer_load_dwordx4 v249, %[srdv], s78 offen nt lds\n"
      "v_exp_f32 v239, v241\n"
      "v_mfma_f32_16x16x32_bf16 v[92:95], a[116:119], v[204:207], v[92:95]\n"
      "s_add_u32 m0, s62, %[v3]\n"
      "ds_read_b128 a[92:95], v243 offset:5120\n"
      "v_fmamk_f32 v240, v184, 0x3e0293ee, v227\n"
      "v_add_f32 v234, v234, v239\n"
      "v_cvt_pk_bf16_f32 v217, v238, v239\n"
      "v_mfma_f32_16x16x32_bf16 v[96:99], a[120:123], v[192:195], v[96:99]\n"
      "s_mov_b32 s77, s60\n"
      "buffer_load_dwordx4 v249, %[srdv], s78 offen offset:128 nt lds\n"
      "v_exp_f32 v236, v240\n"
      "v_mfma_f32_16x16x32_bf16 v[100:103], a[120:123], v[196:199], v[100:103]\n"
      "s_mov_b32 s78, s61\n"
      "v_fmamk_f32 v241, v185, 0x3e0293ee, v227\n"
      "v_add_f32 v231, v231, v235\n"
      "v_mfma_f32_16x16x32_bf16 v[104:107], a[120:123], v[200:203], v[104:107]\n"
      "s_add_u32 s60, s60, s79\n"
      "v_exp_f32 v237, v241\n"
      "v_mfma_f32_16x16x32_bf16 v[108:111], a[120:123], v[204:207], v[108:111]\n"
      "s_add_u32 s61, s61, s79\n"
      "v_fmamk_f32 v240, v186, 0x3e0293ee, v227\n"
      "v_add_f32 v235, v236, v237\n"
      "v_cvt_pk_bf16_f32 v220, v236, v237\n"
      "v_mfma_f32_16x16x32_bf16 v[112:115], a[124:127], v[192:195], v[112:115]\n"
      "s_sub_u32 s79, 0x10000, s79\n"
      "v_exp_f32 v238, v240\n"
      "v_mfma_f32_16x16x32_bf16 v[116:119], a[124:127], v[196:199], v[116:119]\n"
      "s_add_u32 s62, s62, 0x8000\n"
      "v_fmamk_f32 v241, v187, 0x3e0293ee, v227\n"
      "v_add_f32 v235, v235, v238\n"
      "v_mfma_f32_16x16x32_bf16 v[120:123], a[124:127], v[200:203], v[120:123]\n"
      "s_and_b32 s62, s62, 0x1ffff\n"
      "v_exp_f32 v239, v241\n"
      "v_mfma_f32_16x16x32_bf16 v[124:127], a[124:127], v[204:207], v[124:127]\n"
      "s_waitcnt lgkmcnt(0)\n"
      "v_fmamk_f32 v240, v164, 0x3e0293ee, v224\n"
      "v_add_f32 v235, v235, v239\n"
      "v_cvt_pk_bf16_f32 v221, v238, v239\n"
      "v_mfma_f32_16x16x32_bf16 v[128:131], a[64:67], a[0:3], 0\n"
      "s_sub_u32 s67, s68, s66\n"
      "v_exp_f32 v236, v240\n"
      "v_mfma_f32_16x16x32_bf16 v[136:139], a[64:67], a[4:7], 0\n"
      "s_cmp_gt_i32 s67, s70\n"
      "v_fmamk_f32 v241, v165, 0x3e0293ee, v224\n"
      "v_add_f32 v232, v232, v236\n"
      "v_mfma_f32_16x16x32_bf16 v[144:147], a[64:67], a[8:11], 0\n"
      "s_cselect_b32 s69, s86, 0\n"
      "v_exp_f32 v237, v241\n"
      "v_mfma_f32_16x16x32_bf16 v[152:155], a[64:67], a[12:15], 0\n"
      "s_add_u32 s66, s66, s69\n"
      "v_fmamk_f32 v240, v166, 0x3e0293ee, v224\n"
      "v_add_f32 v232, v232, v237\n"
      "v_cvt_pk_bf16_f32 v210, v236, v237\n"
      "v_mfma_f32_16x16x32_bf16 v[132:135], a[68:71], a[0:3], 0\n"
      "s_add_u32 s67, s67, 50\n"
      "v_exp_f32 v238, v240\n"
      "v_mfma_f32_16x16x32_bf16 v[140:143], a[68:71], a[4:7], 0\n"
      "s_min_i32 s70, s70, s67\n"
      "v_fmamk_f32 v241, v167, 0x3e0293ee, v224\n"
      "v_add_f32 v232, v232, v238\n"
      "v_mfma_f32_16x16x32_bf16 v[148:151], a[68:71], a[8:11], 0\n"
      "s_max_i32 s70, s70, 85\n"
      "v_exp_f32 v239, v241\n"
      "v_mfma_f32_16x16x32_bf16 v[156:159], a[68:71], a[12:15], 0\n"
      "s_add_u32 s66, s66, s86\n"
      "v_fmamk_f32 v240, v172, 0x3e0293ee, v225\n"
      "v_add_f32 v232, v232, v239\n"
      "v_cvt_pk_bf16_f32 v211, v238, v239\n"
      "v_mfma_f32_16x16x32_bf16 v[128:131], a[72:75], a[16:19], v[128:131]\n"
      "s_and_b32 s76, s80, 31\n"
      "v_exp_f32 v236, v240\n"
      "v_mfma_f32_16x16x32_bf16 v[136:139], a[72:75], a[20:23], v[136:139]\n"
      "s_lshr_b64 s[80:81], s[80:81], 5\n"
      "v_fmamk_f32 v241, v173, 0x3e0293ee, v225\n"
      "v_add_f32 v233, v233, v236\n"
      "v_mfma_f32_16x16x32_bf16 v[144:147], a[72:75], a[24:27], v[144:147]\n"
      "s_add_u32 s66, s66, s76\n"
      "v_exp_f32 v237, v241\n"
      "v_mfma_f32_16x16x32_bf16 v[152:155], a[72:75], a[28:31], v[152:155]\n"
      "s_sub_u32 s67, s68, s82\n"
      "v_fmamk_f32 v240, v174, 0x3e0293ee, v225\n"
      "v_add_f32 v233, v233, v237\n"
      "v_cvt_pk_bf16_f32 v214, v236, v237\n"
      "v_mfma_f32_16x16x32_bf16 v[132:135], a[76:79], a[16:19], v[132:135]\n"
      "s_min_i32 s76, s67, s83\n"
      "v_exp_f32 v238, v240\n"
      "v_mfma_f32_16x16x32_bf16 v[140:143], a[76:79], a[20:23], v[140:143]\n"
      "s_mov_b32 s83, s67\n"
      "v_fmamk_f32 v241, v175, 0x3e0293ee, v225\n"
      "v_add_f32 v233, v233, v238\n"
      "s_waitcnt vmcnt(8)\n"
      "s_barrier\n"
      "v_mfma_f32_16x16x32_bf16 v[148:151], a[76:79], a[24:27], v[148:151]\n"
      "s_sub_u32 s76, s76, 26\n"
      "ds_read_b64_tr_b16 a[128:129], v244 offset:0\n"
      "v_exp_f32 v239, v241\n"
      "v_mfma_f32_16x16x32_bf16 v[156:159], a[76:79], a[28:31], v[156:159]\n"
      "s_min_i32 s76, s76, 148\n"
      "ds_read_b64_tr_b16 a[130:131], v244 offset:4096\n"
      "v_fmamk_f32 v240, v180, 0x3e0293ee, v226\n"
      "v_add_f32 v233, v233, v239\n"
      "v_cvt_pk_bf16_f32 v215, v238, v239\n"
      "v_mfma_f32_16x16x32_bf16 v[128:131], a[80:83], a[32:35], v[128:131]\n"
      "s_max_i32 s76, s76, 94\n"
      "ds_read_b64_tr_b16 a[132:133], v245 offset:0\n"
      "v_exp_f32 v236, v240\n"
      "v_mfma_f32_16x16x32_bf16 v[136:139], a[80:83], a[36:39], v[136:139]\n"
      "s_mov_b32 s82, s68\n"
      "ds_read_b64_tr_b16 a[134:135], v245 offset:4096\n"
      "v_fmamk_f32 v241, v181, 0x3e0293ee, v226\n"
      "v_add_f32 v234, v234, v236\n"
      "v_mfma_f32_16x16x32_bf16 v[144:147], a[80:83], a[40:43], v[144:147]\n"
      "s_add_u32 s65, s82, s76\n"
      "ds_read_b64_tr_b16 a[136:137], v246 offset:0\n"
      "v_exp_f32 v237, v241\n"
      "v_mfma_f32_16x16x32_bf16 v[152:155], a[80:83], a[44:47], v[152:155]\n"
      "s_sub_u32 s93, s93, 1\n"
      "ds_read_b64_tr_b16 a[138:139], v246 offset:4096\n"
      "v_fmamk_f32 v240, v182, 0x3e0293ee, v226\n"
      "v_add_f32 v234, v234, v237\n"
      "v_cvt_pk_bf16_f32 v218, v236, v237\n"
      "v_mfma_f32_16x16x32_bf16 v[132:135], a[84:87], a[32:35], v[132:135]\n"
      "s_cmp_lt_i32 s93, 10\n"
      "ds_read_b64_tr_b16 a[140:141], v247 offset:0\n"
      "v_exp_f32 v238, v240\n"
      "v_mfma_f32_16x16x32_bf16 v[140:143], a[84:87], a[36:39], v[140:143]\n"
      "s_cselect_b32 s69, 3, 0\n"
      "ds_read_b64_tr_b16 a[142:143], v247 offset:4096\n"
      "v_fmamk_f32 v241, v183, 0x3e0293ee, v226\n"
      "v_add_f32 v234, v234, v238\n"
      "v_mfma_f32_16x16x32_bf16 v[148:151], a[84:87], a[40:43], v[148:151]\n"
      "s_sub_u32 s66, s66, s69\n"
      "ds_read_b64_tr_b16 a[144:145], v244 offset:1024\n"
      "v_exp_f32 v239, v241\n"
      "v_mfma_f32_16x16x32_bf16 v[156:159], a[84:87], a[44:47], v[156:159]\n"
      "s_sub_u32 s67, s65, s66\n"
      "ds_read_b64_tr_b16 a[146:147], v244 offset:5120\n"
      "v_fmamk_f32 v240, v188, 0x3e0293ee, v227\n"
      "v_add_f32 v234, v234, v239\n"
      "v_cvt_pk_bf16_f32 v219, v238, v239\n"
      "v_mfma_f32_16x16x32_bf16 v[128:131], a[88:91], a[48:51], v[128:131]\n"
      "s_cmp_lt_i32 s67, 0\n"
      "ds_read_b64_tr_b16 a[148:149], v245 offset:1024\n"
      "v_exp_f32 v236, v240\n"
      "v_mfma_f32_16x16x32_bf16 v[136:139], a[88:91], a[52:55], v[136:139]\n"
      "s_cselect_b32 s88, s66, s65\n"
      "ds_read_b64_tr_b16 a[150:151], v245 offset:5120\n"
      "v_fmamk_f32 v241, v189, 0x3e0293ee, v227\n"
      "v_add_f32 v235, v235, v236\n"
      "v_mfma_f32_16x16x32_bf16 v[144:147], a[88:91], a[56:59], v[144:147]\n"
      "s_add_u32 s90, s88, s91\n"
      "ds_read_b64_tr_b16 a[152:153], v246 offset:1024\n"
      "v_exp_f32 v237, v241\n"
      "v_mfma_f32_16x16x32_bf16 v[152:155], a[88:91], a[60:63], v[152:155]\n"
      "ds_read_b64_tr_b16 a[154:155], v246 offset:5120\n"
      "v_fmamk_f32 v240, v190, 0x3e0293ee, v227\n"
      "v_add_f32 v235, v235, v237\n"
      "v_cvt_pk_bf16_f32 v222, v236, v237\n"
      "v_mfma_f32_16x16x32_bf16 v[132:135], a[92:95], a[48:51], v[132:135]\n"
      "s_mov_b32 s64, 2\n"
      "ds_read_b64_tr_b16 a[156:157], v247 offset:1024\n"
      "v_exp_f32 v238, v240\n"
      "v_mfma_f32_16x16x32_bf16 v[140:143], a[92:95], a[52:55], v[140:143]\n"
      "ds_read_b64_tr_b16 a[158:159], v247 offset:5120\n"
      "v_fmamk_f32 v241, v191, 0x3e0293ee, v227\n"
      "v_add_f32 v235, v235, v238\n"
      "v_mfma_f32_16x16x32_bf16 v[148:151], a[92:95], a[56:59], v[148:151]\n"
      "v_exp_f32 v239, v241\n"
      "v_mfma_f32_16x16x32_bf16 v[156:159], a[92:95], a[60:63], v[156:159]\n"
      "s_waitcnt lgkmcnt(8)\n"
      "v_add_f32 v235, v235, v239\n"
      "v_cvt_pk_bf16_f32 v223, v238, v239\n"
      "v_max3_f32 v240, v232, v233, v234\n"
      "v_add_f32 v240, v240, v235\n"
      "v_cmp_nge_f32 vcc, 0x44800000, v240\n"
      "s_cbranch_vccnz FB1_%=\n"
      "RET1_2_%=:\n"
      "v_mfma_f32_16x16x32_bf16 v[0:3], a[128:131], v[208:211], v[0:3]\n"
      "s_add_u32 m0, s62, 0\n"
      "v_fmamk_f32 v240, v128, 0x3e0293ee, v224\n"
      "v_exp_f32 v236, v240\n"
      "v_mfma_f32_16x16x32_bf16 v[4:7], a[128:131], v[212:215], v[4:7]\n"
      "buffer_load_dwordx4 v248, %[srdk], s60 offen nt lds\n"
      "v_fmamk_f32 v241, v129, 0x3e0293ee, v224\n"
      "v_add_f32 v228, v228, v232\n"
      "v_mfma_f32_16x16x32_bf16 v[8:11], a[128:131], v[216:219], v[8:11]\n"
      "s_add_u32 m0, s62, 896\n"
      "v_exp_f32 v237, v241\n"
      "v_mfma_f32_16x16x32_bf16 v[12:15], a[128:131], v[220:223], v[12:15]\n"
      "buffer_load_dwordx4 v248, %[srdk], s60 offen offset:128 nt lds\n"
      "v_fmamk_f32 v240, v130, 0x3e0293ee, v224\n"
      "v_add_f32 v232, v236, v237\n"
      "v_cvt_pk_bf16_f32 v192, v236, v237\n"
      "v_mfma_f32_16x16x32_bf16 v[16:19], a[132:135], v[208:211], v[16:19]\n"
      "s_add_u32 m0, s62, 8192\n"
      "v_exp_f32 v238, v240\n"
      "v_mfma_f32_16x16x32_bf16 v[20:23], a[132:135], v[212:215], v[20:23]\n"
      "buffer_load_dwordx4 v248, %[srdk], s61 offen nt lds\n"
      "v_fmamk_f32 v241, v131, 0x3e0293ee, v224\n"
      "v_add_f32 v232, v232, v238\n"
      "v_mfma_f32_16x16x32_bf16 v[24:27], a[132:135], v[216:219], v[24:27]\n"
      "s_add_u32 m0, s62, 9088\n"
      "v_exp_f32 v239, v241\n"
      "v_mfma_f32_16x16x32_bf16 v[28:31], a[132:135], v[220:223], v[28:31]\n"
      "buffer_load_dwordx4 v248, %[srdk], s61 offen offset:128 nt lds\n"
      "v_fmamk_f32 v240, v136, 0x3e0293ee, v225\n"
      "v_add_f32 v232, v232, v239\n"
      "v_cvt_pk_bf16_f32 v193, v238, v239\n"
      "v_mfma_f32_16x16x32_bf16 v[32:35], a[136:139], v[208:211], v[32:35]\n"
      "v_exp_f32 v236, v240\n"
      "v_mfma_f32_16x16x32_bf16 v[36:39], a[136:139], v[212:215], v[36:39]\n"
      "v_fmamk_f32 v241, v137, 0x3e0293ee, v225\n"
      "v_add_f32 v229, v229, v233\n"
      "v_mfma_f32_16x16x32_bf16 v[40:43], a[136:139], v[216:219], v[40:43]\n"
      "v_exp_f32 v237, v241\n"
      "v_mfma_f32_16x16x32_bf16 v[44:47], a[136:139], v[220:223], v[44:47]\n"
      "v_fmamk_f32 v240, v138, 0x3e0293ee, v225\n"
      "v_add_f32 v233, v236, v237\n"
      "v_cvt_pk_bf16_f32 v196, v236, v237\n"
      "v_mfma_f32_16x16x32_bf16 v[48:51], a[140:143], v[208:211], v[48:51]\n"
      "s_memrealtime s[68:69]\n"
      "v_exp_f32 v238, v240\n"
      "v_mfma_f32_16x16x32_bf16 v[52:55], a[140:143], v[212:215], v[52:55]\n"
      "v_fmamk_f32 v241, v139, 0x3e0293ee, v225\n"
      "v_add_f32 v233, v233, v238\n"
      "v_mfma_f32_16x16x32_bf16 v[56:59], a[140:143], v[216:219], v[56:59]\n"
      "v_exp_f32 v239, v241\n"
      "v_mfma_f32_16x16x32_bf16 v[60:63], a[140:143], v[220:223], v[60:63]\n"
      "v_fmamk_f32 v240, v144, 0x3e0293ee, v226\n"
      "v_add_f32 v233, v233, v239\n"
      "v_cvt_pk_bf16_f32 v197, v238, v239\n"
      "s_waitcnt vmcnt(8) lgkmcnt(0)\n"
      "s_sub_u32 s67, s68, s90\n"
      "s_cmp_lt_i32 s67, 0\n"
      "s_cbranch_scc1 GW4_%=\n"
      "GX4_%=:\n"
      "s_barrier\n"
      "v_mfma_f32_16x16x32_bf16 v[64:67], a[144:147], v[208:211], v[64:67]\n"
      "s_memrealtime s[68:69]\n"
      "ds_read_b128 a[64:67], v242 offset:32768\n"
      "v_exp_f32 v236, v240\n"
      "v_mfma_f32_16x16x32_bf16 v[68:71], a[144:147], v[212:215], v[68:71]\n"
      "s_add_u32 m0, s62, %[v0]\n"
      "ds_read_b128 a[68:71], v242 offset:36864\n"
      "v_fmamk_f32 v241, v145, 0x3e0293ee, v226\n"
      "v_add_f32 v230, v230, v234\n"
      "v_mfma_f32_16x16x32_bf16 v[72:75], a[144:147], v[216:219], v[72:75]\n"
      "ds_read_b128 a[72:75], v243 offset:32768\n"
      "buffer_load_dwordx4 v249, %[srdv], s77 offen nt lds\n"
      "v_exp_f32 v237, v241\n"
      "v_mfma_f32_16x16x32_bf16 v[76:79], a[144:147], v[220:223], v[76:79]\n"
      "s_add_u32 m0, s62, %[v1]\n"
      "ds_read_b128 a[76:79], v243 offset:36864\n"
      "v_fmamk_f32 v240, v146, 0x3e0293ee, v226\n"
      "v_add_f32 v234, v236, v237\n"
      "v_cvt_pk_bf16_f32 v200, v236, v237\n"
      "v_mfma_f32_16x16x32_bf16 v[80:83], a[148:151], v[208:211], v[80:83]\n"
      "ds_read_b128 a[80:83], v242 offset:33792\n"
      "buffer_load_dwordx4 v249, %[srdv], s77 offen offset:128 nt lds\n"
      "v_exp_f32 v238, v240\n"
      "v_mfma_f32_16x16x32_bf16 v[84:87], a[148:151], v[212:215], v[84:87]\n"
      "s_add_u32 m0, s62, %[v2]\n"
      "ds_read_b128 a[84:87], v242 offset:37888\n"
      "v_fmamk_f32 v241, v147, 0x3e0293ee, v226\n"
      "v_add_f32 v234, v234, v238\n"
      "v_mfma_f32_16x16x32_bf16 v[88:91], a[148:151], v[216:219], v[88:91]\n"
      "ds_read_b128 a[88:91], v243 offset:33792\n"
      "buffer_load_dwordx4 v249, %[srdv], s78 offen nt lds\n"
      "v_exp_f32 v239, v241\n"
      "v_mfma_f32_16x16x32_bf16 v[92:95], a[148:151], v[220:223], v[92:95]\n"
      "s_add_u32 m0, s62, %[v3]\n"
      "ds_read_b128 a[92:95], v243 offset:37888\n"
      "v_fmamk_f32 v240, v152, 0x3e0293ee, v227\n"
      "v_add_f32 v234, v234, v239\n"
      "v_cvt_pk_bf16_f32 v201, v238, v239\n"
      "v_mfma_f32_16x16x32_bf16 v[96:99], a[152:155], v[208:211], v[96:99]\n"
      "s_mov_b32 s77, s60\n"
      "buffer_load_dwordx4 v249, %[srdv], s78 offen offset:128 nt lds\n"
      "v_exp_f32 v236, v240\n"
      "v_mfma_f32_16x16x32_bf16 v[100:103], a[152:155], v[212:215], v[100:103]\n"
      "s_mov_b32 s78, s61\n"
      "v_fmamk_f32 v241, v153, 0x3e0293ee, v227\n"
      "v_add_f32 v231, v231, v235\n"
      "v_mfma_f32_16x16x32_bf16 v[104:107], a[152:155], v[216:219], v[104:107]\n"
      "s_add_u32 s60, s60, s79\n"
      "v_exp_f32 v237, v241\n"
      "v_mfma_f32_16x16x32_bf16 v[108:111], a[152:155], v[220:223], v[108:111]\n"
      "s_add_u32 s61, s61, s79\n"
      "v_fmamk_f32 v240, v154, 0x3e0293ee, v227\n"
      "v_add_f32 v235, v236, v237\n"
      "v_cvt_pk_bf16_f32 v204, v236, v237\n"
      "v_mfma_f32_16x16x32_bf16 v[112:115], a[156:159], v[208:211], v[112:115]\n"
      "s_sub_u32 s79, 0x10000, s79\n"
      "v_exp_f32 v238, v240\n"
      "v_mfma_f32_16x16x32_bf16 v[116:119], a[156:159], v[212:215], v[116:119]\n"
      "s_add_u32 s62, s62, 0x8000\n"
      "v_fmamk_f32 v241, v155, 0x3e0293ee, v227\n"
      "v_add_f32 v235, v235, v238\n"
      "v_mfma_f32_16x16x32_bf16 v[120:123], a[156:159], v[216:219], v[120:123]\n"
      "s_and_b32 s62, s62, 0x1ffff\n"
      "v_exp_f32 v239, v241\n"
      "v_mfma_f32_16x16x32_bf16 v[124:127], a[156:159], v[220:223], v[124:127]\n"
      "s_waitcnt lgkmcnt(0)\n"
      "v_fmamk_f32 v240, v132, 0x3e0293ee, v224\n"
      "v_add_f32 v235, v235, v239\n"
      "v_cvt_pk_bf16_f32 v205, v238, v239\n"
      "v_mfma_f32_16x16x32_bf16 v[160:163], a[64:67], a[0:3], 0\n"
      "s_sub_u32 s67, s68, s66\n"
      "v_exp_f32 v236, v240\n"
      "v_mfma_f32_16x16x32_bf16 v[168:171], a[64:67], a[4:7], 0\n"
      "s_cmp_gt_i32 s67, s70\n"
      "v_fmamk_f32 v241, v133, 0x3e0293ee, v224\n"
      "v_add_f32 v232, v232, v236\n"
      "v_mfma_f32_16x16x32_bf16 v[176:179], a[64:67], a[8:11], 0\n"
      "s_cselect_b32 s69, s86, 0\n"
      "v_exp_f32 v237, v241\n"
      "v_mfma_f32_16x16x32_bf16 v[184:187], a[64:67], a[12:15], 0\n"
      "s_add_u32 s66, s66, s69\n"
      "v_fmamk_f32 v240, v134, 0x3e0293ee, v224\n"
      "v_add_f32 v232, v232, v237\n"
      "v_cvt_pk_bf16_f32 v194, v236, v237\n"
      "v_mfma_f32_16x16x32_bf16 v[164:167], a[68:71], a[0:3], 0\n"
      "s_add_u32 s67, s67, 50\n"
      "v_exp_f32 v238, v240\n"
      "v_mfma_f32_16x16x32_bf16 v[172:175], a[68:71], a[4:7], 0\n"
      "s_min_i32 s70, s70, s67\n"
      "v_fmamk_f32 v241, v135, 0x3e0293ee, v224\n"
      "v_add_f32 v232, v232, v238\n"
      "v_mfma_f32_16x16x32_bf16 v[180:183], a[68:71], a[8:11], 0\n"
      "s_max_i32 s70, s70, 85\n"
      "v_exp_f32 v239, v241\n"
      "v_mfma_f32_16x16x32_bf16 v[188:191], a[68:71], a[12:15], 0\n"
      "s_add_u32 s66, s66, s86\n"
      "v_fmamk_f32 v240, v140, 0x3e0293ee, v225\n"
      "v_add_f32 v232, v232, v239\n"
      "v_cvt_pk_bf16_f32 v195, v238, v239\n"
      "v_mfma_f32_16x16x32_bf16 v[160:163], a[72:75], a[16:19], v[160:163]\n"
      "s_and_b32 s76, s80, 31\n"
      "v_exp_f32 v236, v240\n"
      "v_mfma_f32_16x16x32_bf16 v[168:171], a[72:75], a[20:23], v[168:171]\n"
      "s_lshr_b64 s[80:81], s[80:81], 5\n"
      "v_fmamk_f32 v241, v141, 0x3e0293ee, v225\n"
      "v_add_f32 v233, v233, v236\n"
      "v_mfma_f32_16x16x32_bf16 v[176:179], a[72:75], a[24:27], v[176:179]\n"
      "s_add_u32 s66, s66, s76\n"
      "v_exp_f32 v237, v241\n"
      "v_mfma_f32_16x16x32_bf16 v[184:187], a[72:75], a[28:31], v[184:187]\n"
      "s_sub_u32 s67, s68, s82\n"
      "v_fmamk_f32 v240, v142, 0x3e0293ee, v225\n"
      "v_add_f32 v233, v233, v237\n"
      "v_cvt_pk_bf16_f32 v198, v236, v237\n"
      "v_mfma_f32_16x16x32_bf16 v[164:167], a[76:79], a[16:19], v[164:167]\n"
      "s_min_i32 s76, s67, s83\n"
      "v_exp_f32 v238, v240\n"
      "v_mfma_f32_16x16x32_bf16 v[172:175], a[76:79], a[20:23], v[172:175]\n"
      "s_mov_b32 s83, s67\n"
      "v_fmamk_f32 v241, v143, 0x3e0293ee, v225\n"
      "v_add_f32 v233, v233, v238\n"
      "s_waitcnt vmcnt(8)\n"
      "s_barrier\n"
      "v_mfma_f32_16x16x32_bf16 v[180:183], a[76:79], a[24:27], v[180:183]\n"
      "s_sub_u32 s76, s76, 26\n"
      "ds_read_b64_tr_b16 a[96:97], v244 offset:32768\n"
      "v_exp_f32 v239, v241\n"
      "v_mfma_f32_16x16x32_bf16 v[188:191], a[76:79], a[28:31], v[188:191]\n"
      "s_min_i32 s76, s76, 148\n"
      "ds_read_b64_tr_b16 a[98:99], v244 offset:36864\n"
      "v_fmamk_f32 v240, v148, 0x3e0293ee, v226\n"
      "v_add_f32 v233, v233, v239\n"
      "v_cvt_pk_bf16_f32 v199, v238, v239\n"
      "v_mfma_f32_16x16x32_bf16 v[160:163], a[80:83], a[32:35], v[160:163]\n"
      "s_max_i32 s76, s76, 94\n"
      "ds_read_b64_tr_b16 a[100:101], v245 offset:32768\n"
      "v_exp_f32 v236, v240\n"
      "v_mfma_f32_16x16x32_bf16 v[168:171], a[80:83], a[36:39], v[168:171]\n"
      "s_mov_b32 s82, s68\n"
      "ds_read_b64_tr_b16 a[102:103], v245 offset:36864\n"
      "v_fmamk_f32 v241, v149, 0x3e0293ee, v226\n"
      "v_add_f32 v234, v234, v236\n"
      "v_mfma_f32_16x16x32_bf16 v[176:179], a[80:83], a[40:43], v[176:179]\n"
      "s_add_u32 s65, s82, s76\n"
      "ds_read_b64_tr_b16 a[104:105], v246 offset:32768\n"
      "v_exp_f32 v237, v241\n"
      "v_mfma_f32_16x16x32_bf16 v[184:187], a[80:83], a[44:47], v[184:187]\n"
      "s_sub_u32 s93, s93, 1\n"
      "ds_read_b64_tr_b16 a[106:107], v246 offset:36864\n"
      "v_fmamk_f32 v240, v150, 0x3e0293ee, v226\n"
      "v_add_f32 v234, v234, v237\n"
      "v_cvt_pk_bf16_f32 v202, v236, v237\n"
      "v_mfma_f32_16x16x32_bf16 v[164:167], a[84:87], a[32:35], v[164:167]\n"
      "s_cmp_lt_i32 s93, 10\n"
      "ds_read_b64_tr_b16 a[108:109], v247 offset:32768\n"
      "v_exp_f32 v238, v240\n"
      "v_mfma_f32_16x16x32_bf16 v[172:175], a[84:87], a[36:39], v[172:175]\n"
      "s_cselect_b32 s69, 3, 0\n"
      "ds_read_b64_tr_b16 a[110:111], v247 offset:36864\n"
      "v_fmamk_f32 v241, v151, 0x3e0293ee, v226\n"
      "v_add_f32 v234, v234, v238\n"
      "v_mfma_f32_16x16x32_bf16 v[180:183], a[84:87], a[40:43], v[180:183]\n"
      "s_sub_u32 s66, s66, s69\n"
      "ds_read_b64_tr_b16 a[112:113], v244 offset:33792\n"
      "v_exp_f32 v239, v241\n"
      "v_mfma_f32_16x16x32_bf16 v[188:191], a[84:87], a[44:47], v[188:191]\n"
      "s_sub_u32 s67, s65, s66\n"
      "ds_read_b64_tr_b16 a[114:115], v244 offset:37888\n"
      "v_fmamk_f32 v240, v156, 0x3e0293ee, v227\n"
      "v_add_f32 v234, v234, v239\n"
      "v_cvt_pk_bf16_f32 v203, v238, v239\n"
      "v_mfma_f32_16x16x32_bf16 v[160:163], a[88:91], a[48:51], v[160:163]\n"
      "s_cmp_lt_i32 s67, 0\n"
      "ds_read_b64_tr_b16 a[116:117], v245 offset:33792\n"
      "v_exp_f32 v236, v240\n"
      "v_mfma_f32_16x16x32_bf16 v[168:171], a[88:91], a[52:55], v[168:171]\n"
      "s_cselect_b32 s88, s66, s65\n"
      "ds_read_b64_tr_b16 a[118:119], v245 offset:37888\n"
      "v_fmamk_f32 v241, v157, 0x3e0293ee, v227\n"
      "v_add_f32 v235, v235, v236\n"
      "v_mfma_f32_16x16x32_bf16 v[176:179], a[88:91], a[56:59], v[176:179]\n"
      "s_add_u32 s90, s88, s91\n"
      "ds_read_b64_tr_b16 a[120:121], v246 offset:33792\n"
      "v_exp_f32 v237, v241\n"
      "v_mfma_f32_16x16x32_bf16 v[184:187], a[88:91], a[60:63], v[184:187]\n"
      "ds_read_b64_tr_b16 a[122:123], v246 offset:37888\n"
      "v_fmamk_f32 v240, v158, 0x3e0293ee, v227\n"
      "v_add_f32 v235, v235, v237\n"
      "v_cvt_pk_bf16_f32 v206, v236, v237\n"
      "v_mfma_f32_16x16x32_bf16 v[164:167], a[92:95], a[48:51], v[164:167]\n"
      "s_mov_b32 s64, 2\n"
      "ds_read_b64_tr_b16 a[124:125], v247 offset:33792\n"
      "v_exp_f32 v238, v240\n"
      "v_mfma_f32_16x16x32_bf16 v[172:175], a[92:95], a[52:55], v[172:175]\n"
      "ds_read_b64_tr_b16 a[126:127], v247 offset:37888\n"
      "v_fmamk_f32 v241, v159, 0x3e0293ee, v227\n"
      "v_add_f32 v235, v235, v238\n"
      "v_mfma_f32_16x16x32_bf16 v[180:183], a[92:95], a[56:59], v[180:183]\n"
      "v_exp_f32 v239, v241\n"
      "v_mfma_f32_16x16x32_bf16 v[188:191], a[92:95], a[60:63], v[188:191]\n"
      "s_waitcnt lgkmcnt(8)\n"
      "v_add_f32 v235, v235, v239\n"
      "v_cvt_pk_bf16_f32 v207, v238, v239\n"
      "v_max3_f32 v240, v232, v233, v234\n"
      "v_add_f32 v240, v240, v235\n"
      "v_cmp_nge_f32 vcc, 0x44800000, v240\n"
      "s_cbranch_vccnz FB0_%=\n"
      "RET0_2_%=:\n"
      "v_mfma_f32_16x16x32_bf16 v[0:3], a[96:99], v[192:195], v[0:3]\n"
      "s_add_u32 m0, s62, 0\n"
      "v_fmamk_f32 v240, v160, 0x3e0293ee, v224\n"
      "v_exp_f32 v236, v240\n"
      "v_mfma_f32_16x16x32_bf16 v[4:7], a[96:99], v[196:199], v[4:7]\n"
      "buffer_load_dwordx4 v248, %[srdk], s60 offen nt lds\n"
      "v_fmamk_f32 v241, v161, 0x3e0293ee, v224\n"
      "v_add_f32 v228, v228, v232\n"
      "v_mfma_f32_16x16x32_bf16 v[8:11], a[96:99], v[200:203], v[8:11]\n"
      "s_add_u32 m0, s62, 896\n"
      "v_exp_f32 v237, v241\n"
      "v_xor_b32 v242, 0x10000, v242\n"
      "v_mfma_f32_16x16x32_bf16 v[12:15], a[96:99], v[204:207], v[12:15]\n"
      "buffer_load_dwordx4 v248, %[srdk], s60 offen offset:128 nt lds\n"
      "v_fmamk_f32 v240, v162, 0x3e0293ee, v224\n"
      "v_add_f32 v232, v236, v237\n"
      "v_cvt_pk_bf16_f32 v208, v236, v237\n"
      "v_mfma_f32_16x16x32_bf16 v[16:19], a[100:103], v[192:195], v[16:19]\n"
      "s_add_u32 m0, s62, 8192\n"
      "v_exp_f32 v238, v240\n"
      "v_xor_b32 v243, 0x10000, v243\n"
      "v_mfma_f32_16x16x32_bf16 v[20:23], a[100:103], v[196:199], v[20:23]\n"
      "buffer_load_dwordx4 v248, %[srdk], s61 offen nt lds\n"
      "v_fmamk_f32 v241, v163, 0x3e0293ee, v224\n"
      "v_add_f32 v232, v232, v238\n"
      "v_mfma_f32_16x16x32_bf16 v[24:27], a[100:103], v[200:203], v[24:27]\n"
      "s_add_u32 m0, s62, 9088\n"
      "v_exp_f32 v239, v241\n"
      "v_xor_b32 v244, 0x10000, v244\n"
      "v_mfma_f32_16x16x32_bf16 v[28:31], a[100:103], v[204:207], v[28:31]\n"
      "buffer_load_dwordx4 v248, %[srdk], s61 offen offset:128 nt lds\n"
      "v_fmamk_f32 v240, v168, 0x3e0293ee, v225\n"
      "v_add_f32 v232, v232, v239\n"
      "v_cvt_pk_bf16_f32 v209, v238, v239\n"
      "v_mfma_f32_16x16x32_bf16 v[32:35], a[104:107], v[192:195], v[32:35]\n"
      "v_exp_f32 v236, v240\n"
      "v_xor_b32 v245, 0x10000, v245\n"
      "v_mfma_f32_16x16x32_bf16 v[36:39], a[104:107], v[196:199], v[36:39]\n"
      "v_fmamk_f32 v241, v169, 0x3e0293ee, v225\n"
      "v_add_f32 v229, v229, v233\n"
      "v_mfma_f32_16x16x32_bf16 v[40:43], a[104:107], v[200:203], v[40:43]\n"
      "v_exp_f32 v237, v241\n"
      "v_xor_b32 v246, 0x10000, v246\n"
      "v_mfma_f32_16x16x32_bf16 v[44:47], a[104:107], v[204:207], v[44:47]\n"
      "v_fmamk_f32 v240, v170, 0x3e0293ee, v225\n"
      "v_add_f32 v233, v236, v237\n"
      "v_cvt_pk_bf16_f32 v212, v236, v237\n"
      "v_mfma_f32_16x16x32_bf16 v[48:51], a[108:111], v[192:195], v[48:51]\n"
      "s_memrealtime s[68:69]\n"
      "v_exp_f32 v238, v240\n"
      "v_xor_b32 v247, 0x10000, v247\n"
      "v_mfma_f32_16x16x32_bf16 v[52:55], a[108:111], v[196:199], v[52:55]\n"
      "v_fmamk_f32 v241, v171, 0x3e0293ee, v225\n"
      "v_add_f32 v233, v233, v238\n"
      "v_mfma_f32_16x16x32_bf16 v[56:59], a[108:111], v[200:203], v[56:59]\n"
      "v_exp_f32 v239, v241\n"
      "v_mfma_f32_16x16x32_bf16 v[60:63], a[108:111], v[204:207], v[60:63]\n"
      "s_sub_u32 s63, s63, 1\n"
      "v_fmamk_f32 v240, v176, 0x3e0293ee, v226\n"
      "v_add_f32 v233, v233, v239\n"
      "v_cvt_pk_bf16_f32 v213, v238, v239\n"
      "s_cbranch_scc0 LOOP2_%=\n"
      "s_waitcnt vmcnt(8) lgkmcnt(0)\n"
      "s_sub_u32 s67, s68, s90\n"
      "s_cmp_lt_i32 s67, 0\n"
      "s_cbranch_scc1 GW5_%=\n"
      "GX5_%=:\n"
      "s_barrier\n"
      "v_mfma_f32_16x16x32_bf16 v[64:67], a[112:115], v[192:195], v[64:67]\n"
      "ds_read_b128 a[64:67], v242 offset:0\n"
      "v_exp_f32 v236, v240\n"
      "v_mfma_f32_16x16x32_bf16 v[68:71], a[112:115], v[196:199], v[68:71]\n"
      "s_add_u32 m0, s62, %[v0]\n"
      "ds_read_b128 a[68:71], v242 offset:4096\n"
      "v_fmamk_f32 v241, v177, 0x3e0293ee, v226\n"
      "v_add_f32 v230, v230, v234\n"
      "v_mfma_f32_16x16x32_bf16 v[72:75], a[112:115], v[200:203], v[72:75]\n"
      "ds_read_b128 a[72:75], v243 offset:0\n"
      "buffer_load_dwordx4 v249, %[srdv], s77 offen nt lds\n"
      "v_exp_f32 v237, v241\n"
      "v_mfma_f32_16x16x32_bf16 v[76:79], a[112:115], v[204:207], v[76:79]\n"
      "s_add_u32 m0, s62, %[v1]\n"
      "ds_read_b128 a[76:79], v243 offset:4096\n"
      "v_fmamk_f32 v240, v178, 0x3e0293ee, v226\n"
      "v_add_f32 v234, v236, v237\n"
      "v_cvt_pk_bf16_f32 v216, v236, v237\n"
      "v_mfma_f32_16x16x32_bf16 v[80:83], a[116:119], v[192:195], v[80:83]\n"
      "ds_read_b128 a[80:83], v242 offset:1024\n"
      "buffer_load_dwordx4 v249, %[srdv], s77 offen offset:128 nt lds\n"
      "v_exp_f32 v238, v240\n"
      "v_mfma_f32_16x16x32_bf16 v[84:87], a[116:119], v[196:199], v[84:87]\n"
      "s_add_u32 m0, s62, %[v2]\n"
      "ds_read_b128 a[84:87], v242 offset:5120\n"
      "v_fmamk_f32 v241, v179, 0x3e0293ee, v226\n"
      "v_add_f32 v234, v234, v238\n"
      "v_mfma_f32_16x16x32_bf16 v[88:91], a[116:119], v[200:203], v[88:91]\n"
      "ds_read_b128 a[88:91], v243 offset:1024\n"
      "buffer_load_dwordx4 v249, %[srdv], s78 offen nt lds\n"
      "v_exp_f32 v239, v241\n"
      "v_mfma_f32_16x16x32_bf16 v[92:95], a[116:119], v[204:207], v[92:95]\n"
      "s_add_u32 m0, s62, %[v3]\n"
      "ds_read_b128 a[92:95], v243 offset:5120\n"
      "v_fmamk_f32 v240, v184, 0x3e0293ee, v227\n"
      "v_add_f32 v234, v234, v239\n"
      "v_cvt_pk_bf16_f32 v217, v238, v239\n"
      "v_mfma_f32_16x16x32_bf16 v[96:99], a[120:123], v[192:195], v[96:99]\n"
      "s_mov_b32 s77, s60\n"
      "buffer_load_dwordx4 v249, %[srdv], s78 offen offset:128 nt lds\n"
      "v_exp_f32 v236, v240\n"
      "v_mfma_f32_16x16x32_bf16 v[100:103], a[120:123], v[196:199], v[100:103]\n"
      "s_mov_b32 s78, s61\n"
      "v_fmamk_f32 v241, v185, 0x3e0293ee, v227\n"
      "v_add_f32 v231, v231, v235\n"
      "v_mfma_f32_16x16x32_bf16 v[104:107], a[120:123], v[200:203], v[104:107]\n"
      "s_add_u32 s60, s60, s79\n"
      "v_exp_f32 v237, v241\n"
      "v_mfma_f32_16x16x32_bf16 v[108:111], a[120:123], v[204:207], v[108:111]\n"
      "s_add_u32 s61, s61, s79\n"
      "v_fmamk_f32 v240, v186, 0x3e0293ee, v227\n"
      "v_add_f32 v235, v236, v237\n"
      "v_cvt_pk_bf16_f32 v220, v236, v237\n"
      "v_mfma_f32_16x16x32_bf16 v[112:115], a[124:127], v[192:195], v[112:115]\n"
      "s_sub_u32 s79, 0x10000, s79\n"
      "v_exp_f32 v238, v240\n"
      "v_mfma_f32_16x16x32_bf16 v[116:119], a[124:127], v[196:199], v[116:119]\n"
      "s_add_u32 s62, s62, 0x8000\n"
      "v_fmamk_f32 v241, v187, 0x3e0293ee, v227\n"
      "v_add_f32 v235, v235, v238\n"
      "v_mfma_f32_16x16x32_bf16 v[120:123], a[124:127], v[200:203], v[120:123]\n"
      "s_and_b32 s62, s62, 0x1ffff\n"
      "v_exp_f32 v239, v241\n"
      "v_mfma_f32_16x16x32_bf16 v[124:127], a[124:127], v[204:207], v[124:127]\n"
      "s_waitcnt lgkmcnt(0)\n"
      "v_fmamk_f32 v240, v164, 0x3e0293ee, v224\n"
      "v_add_f32 v235, v235, v239\n"
      "v_cvt_pk_bf16_f32 v221, v238, v239\n"
      "v_mfma_f32_16x16x32_bf16 v[128:131], a[64:67], a[0:3], 0\n"
      "v_exp_f32 v236, v240\n"
      "v_mfma_f32_16x16x32_bf16 v[136:139], a[64:67], a[4:7], 0\n"
      "v_fmamk_f32 v241, v165, 0x3e0293ee, v224\n"
      "v_add_f32 v232, v232, v236\n"
      "v_mfma_f32_16x16x32_bf16 v[144:147], a[64:67], a[8:11], 0\n"
      "v_exp_f32 v237, v241\n"
      "v_mfma_f32_16x16x32_bf16 v[152:155], a[64:67], a[12:15], 0\n"
      "v_fmamk_f32 v240, v166, 0x3e0293ee, v224\n"
      "v_add_f32 v232, v232, v237\n"
      "v_cvt_pk_bf16_f32 v210, v236, v237\n"
      "v_mfma_f32_16x16x32_bf16 v[132:135], a[68:71], a[0:3], 0\n"
      "v_exp_f32 v238, v240\n"
      "v_mfma_f32_16x16x32_bf16 v[140:143], a[68:71], a[4:7], 0\n"
      "v_fmamk_f32 v241, v167, 0x3e0293ee, v224\n"
      "v_add_f32 v232, v232, v238\n"
      "v_mfma_f32_16x16x32_bf16 v[148:151], a[68:71], a[8:11], 0\n"
      "v_exp_f32 v239, v241\n"
      "v_mfma_f32_16x16x32_bf16 v[156:159], a[68:71], a[12:15], 0\n"
      "v_fmamk_f32 v240, v172, 0x3e0293ee, v225\n"
      "v_add_f32 v232, v232, v239\n"
      "v_cvt_pk_bf16_f32 v211, v238, v239\n"
      "v_mfma_f32_16x16x32_bf16 v[128:131], a[72:75], a[16:19], v[128:131]\n"
      "v_exp_f32 v236, v240\n"
      "v_mfma_f32_16x16x32_bf16 v[136:139], a[72:75], a[20:23], v[136:139]\n"
      "v_fmamk_f32 v241, v173, 0x3e0293ee, v225\n"
      "v_add_f32 v233, v233, v236\n"
      "v_mfma_f32_16x16x32_bf16 v[144:147], a[72:75], a[24:27], v[144:147]\n"
      "v_exp_f32 v237, v241\n"
      "v_mfma_f32_16x16x32_bf16 v[152:155], a[72:75], a[28:31], v[152:155]\n"
      "v_fmamk_f32 v240, v174, 0x3e0293ee, v225\n"
      "v_add_f32 v233, v233, v237\n"
      "v_cvt_pk_bf16_f32 v214, v236, v237\n"
      "v_mfma_f32_16x16x32_bf16 v[132:135], a[76:79], a[16:19], v[132:135]\n"
      "v_exp_f32 v238, v240\n"
      "v_mfma_f32_16x16x32_bf16 v[140:143], a[76:79], a[20:23], v[140:143]\n"
      "v_fmamk_f32 v241, v175, 0x3e0293ee, v225\n"
      "v_add_f32 v233, v233, v238\n"
      "s_waitcnt vmcnt(8)\n"
      "s_barrier\n"
      "v_mfma_f32_16x16x32_bf16 v[148:151], a[76:79], a[24:27], v[148:151]\n"
      "ds_read_b64_tr_b16 a[128:129], v244 offset:0\n"
      "v_exp_f32 v239, v241\n"
      "v_mfma_f32_16x16x32_bf16 v[156:159], a[76:79], a[28:31], v[156:159]\n"
      "ds_read_b64_tr_b16 a[130:131], v244 offset:4096\n"
      "v_fmamk_f32 v240, v180, 0x3e0293ee, v226\n"
      "v_add_f32 v233, v233, v239\n"
      "v_cvt_pk_bf16_f32 v215, v238, v239\n"
      "v_mfma_f32_16x16x32_bf16 v[128:131], a[80:83], a[32:35], v[128:131]\n"
      "ds_read_b64_tr_b16 a[132:133], v245 offset:0\n"
      "v_exp_f32 v236, v240\n"
      "v_mfma_f32_16x16x32_bf16 v[136:139], a[80:83], a[36:39], v[136:139]\n"
      "ds_read_b64_tr_b16 a[134:135], v245 offset:4096\n"
      "v_fmamk_f32 v241, v181, 0x3e0293ee, v226\n"
      "v_add_f32 v234, v234, v236\n"
      "v_mfma_f32_16x16x32_bf16 v[144:147], a[80:83], a[40:43], v[144:147]\n"
      "ds_read_b64_tr_b16 a[136:137], v246 offset:0\n"
      "v_exp_f32 v237, v241\n"
      "v_mfma_f32_16x16x32_bf16 v[152:155], a[80:83], a[44:47], v[152:155]\n"
      "ds_read_b64_tr_b16 a[138:139], v246 offset:4096\n"
      "v_fmamk_f32 v240, v182, 0x3e0293ee, v226\n"
      "v_add_f32 v234, v234, v237\n"
      "v_cvt_pk_bf16_f32 v218, v236, v237\n"
      "v_mfma_f32_16x16x32_bf16 v[132:135], a[84:87], a[32:35], v[132:135]\n"
      "ds_read_b64_tr_b16 a[140:141], v247 offset:0\n"
      "v_exp_f32 v238, v240\n"
      "v_mfma_f32_16x16x32_bf16 v[140:143], a[84:87], a[36:39], v[140:143]\n"
      "ds_read_b64_tr_b16 a[142:143], v247 offset:4096\n"
      "v_fmamk_f32 v241, v183, 0x3e0293ee, v226\n"
      "v_add_f32 v234, v234, v238\n"
      "v_mfma_f32_16x16x32_bf16 v[148:151], a[84:87], a[40:43], v[148:151]\n"
      "ds_read_b64_tr_b16 a[144:145], v244 offset:1024\n"
      "v_exp_f32 v239, v241\n"
      "v_mfma_f32_16x16x32_bf16 v[156:159], a[84:87], a[44:47], v[156:159]\n"
      "ds_read_b64_tr_b16 a[146:147], v244 offset:5120\n"
      "v_fmamk_f32 v240, v188, 0x3e0293ee, v227\n"
      "v_add_f32 v234, v234, v239\n"
      "v_cvt_pk_bf16_f32 v219, v238, v239\n"
      "v_mfma_f32_16x16x32_bf16 v[128:131], a[88:91], a[48:51], v[128:131]\n"
      "ds_read_b64_tr_b16 a[148:149], v245 offset:1024\n"
      "v_exp_f32 v236, v240\n"
      "v_mfma_f32_16x16x32_bf16 v[136:139], a[88:91], a[52:55], v[136:139]\n"
      "ds_read_b64_tr_b16 a[150:151], v245 offset:5120\n"
      "v_fmamk_f32 v241, v189, 0x3e0293ee, v227\n"
      "v_add_f32 v235, v235, v236\n"
      "v_mfma_f32_16x16x32_bf16 v[144:147], a[88:91], a[56:59], v[144:147]\n"
      "ds_read_b64_tr_b16 a[152:153], v246 offset:1024\n"
      "v_exp_f32 v237, v241\n"
      "v_mfma_f32_16x16x32_bf16 v[152:155], a[88:91], a[60:63], v[152:155]\n"
      "ds_read_b64_tr_b16 a[154:155], v246 offset:5120\n"
      "v_fmamk_f32 v240, v190, 0x3e0293ee, v227\n"
      "v_add_f32 v235, v235, v237\n"
      "v_cvt_pk_bf16_f32 v222, v236, v237\n"
      "v_mfma_f32_16x16x32_bf16 v[132:135], a[92:95], a[48:51], v[132:135]\n"
      "s_mov_b32 s64, 3\n"
      "ds_read_b64_tr_b16 a[156:157], v247 offset:1024\n"
      "v_exp_f32 v238, v240\n"
      "v_mfma_f32_16x16x32_bf16 v[140:143], a[92:95], a[52:55], v[140:143]\n"
      "ds_read_b64_tr_b16 a[158:159], v247 offset:5120\n"
      "v_fmamk_f32 v241, v191, 0x3e0293ee, v227\n"
      "v_add_f32 v235, v235, v238\n"
      "v_mfma_f32_16x16x32_bf16 v[148:151], a[92:95], a[56:59], v[148:151]\n"
      "v_exp_f32 v239, v241\n"
      "v_mfma_f32_16x16x32_bf16 v[156:159], a[92:95], a[60:63], v[156:159]\n"
      "s_waitcnt lgkmcnt(8)\n"
      "v_add_f32 v235, v235, v239\n"
      "v_cvt_pk_bf16_f32 v223, v238, v239\n"
      "v_max3_f32 v240, v232, v233, v234\n"
      "v_add_f32 v240, v240, v235\n"
      "v_cmp_nge_f32 vcc, 0x44800000, v240\n"
      "s_cbranch_vccnz FB1_%=\n"
      "RET1_3_%=:\n"
      "v_mfma_f32_16x16x32_bf16 v[0:3], a[128:131], v[208:211], v[0:3]\n"
      "s_add_u32 m0, s62, %[v0]\n"
      "v_fmamk_f32 v240, v128, 0x3e0293ee, v224\n"
      "v_exp_f32 v236, v240\n"
      "v_mfma_f32_16x16x32_bf16 v[4:7], a[128:131], v[212:215], v[4:7]\n"
      "buffer_load_dwordx4 v249, %[srdv], s77 offen nt lds\n"
      "v_fmamk_f32 v241, v129, 0x3e0293ee, v224\n"
      "v_add_f32 v228, v228, v232\n"
      "v_mfma_f32_16x16x32_bf16 v[8:11], a[128:131], v[216:219], v[8:11]\n"
      "s_add_u32 m0, s62, %[v1]\n"
      "v_exp_f32 v237, v241\n"
      "v_mfma_f32_16x16x32_bf16 v[12:15], a[128:131], v[220:223], v[12:15]\n"
      "buffer_load_dwordx4 v249, %[srdv], s77 offen offset:128 nt lds\n"
      "v_fmamk_f32 v240, v130, 0x3e0293ee, v224\n"
      "v_add_f32 v232, v236, v237\n"
      "v_cvt_pk_bf16_f32 v192, v236, v237\n"
      "v_mfma_f32_16x16x32_bf16 v[16:19], a[132:135], v[208:211], v[16:19]\n"
      "s_add_u32 m0, s62, %[v2]\n"
      "v_exp_f32 v238, v240\n"
      "v_mfma_f32_16x16x32_bf16 v[20:23], a[132:135], v[212:215], v[20:23]\n"
      "buffer_load_dwordx4 v249, %[srdv], s78 offen nt lds\n"
      "v_fmamk_f32 v241, v131, 0x3e0293ee, v224\n"
      "v_add_f32 v232, v232, v238\n"
      "v_mfma_f32_16x16x32_bf16 v[24:27], a[132:135], v[216:219], v[24:27]\n"
      "s_add_u32 m0, s62, %[v3]\n"
      "v_exp_f32 v239, v241\n"
      "v_mfma_f32_16x16x32_bf16 v[28:31], a[132:135], v[220:223], v[28:31]\n"
      "buffer_load_dwordx4 v249, %[srdv], s78 offen offset:128 nt lds\n"
      "v_fmamk_f32 v240, v136, 0x3e0293ee, v225\n"
      "v_add_f32 v232, v232, v239\n"
      "v_cvt_pk_bf16_f32 v193, v238, v239\n"
      "v_mfma_f32_16x16x32_bf16 v[32:35], a[136:139], v[208:211], v[32:35]\n"
      "v_exp_f32 v236, v240\n"
      "v_mfma_f32_16x16x32_bf16 v[36:39], a[136:139], v[212:215], v[36:39]\n"
      "v_fmamk_f32 v241, v137, 0x3e0293ee, v225\n"
      "v_add_f32 v229, v229, v233\n"
      "v_mfma_f32_16x16x32_bf16 v[40:43], a[136:139], v[216:219], v[40:43]\n"
      "v_exp_f32 v237, v241\n"
      "v_mfma_f32_16x16x32_bf16 v[44:47], a[136:139], v[220:223], v[44:47]\n"
      "v_fmamk_f32 v240, v138, 0x3e0293ee, v225\n"
      "v_add_f32 v233, v236, v237\n"
      "v_cvt_pk_bf16_f32 v196, v236, v237\n"
      "v_mfma_f32_16x16x32_bf16 v[48:51], a[140:143], v[208:211], v[48:51]\n"
      "s_memrealtime s[68:69]\n"
      "v_exp_f32 v238, v240\n"
      "v_mfma_f32_16x16x32_bf16 v[52:55], a[140:143], v[212:215], v[52:55]\n"
      "v_fmamk_f32 v241, v139, 0x3e0293ee, v225\n"
      "v_add_f32 v233, v233, v238\n"
      "v_mfma_f32_16x16x32_bf16 v[56:59], a[140:143], v[216:219], v[56:59]\n"
      "v_exp_f32 v239, v241\n"
      "v_mfma_f32_16x16x32_bf16 v[60:63], a[140:143], v[220:223], v[60:63]\n"
      "v_fmamk_f32 v240, v144, 0x3e0293ee, v226\n"
      "v_add_f32 v233, v233, v239\n"
      "v_cvt_pk_bf16_f32 v197, v238, v239\n"
      "s_waitcnt vmcnt(8) lgkmcnt(0)\n"
      "s_barrier\n"
      "v_mfma_f32_16x16x32_bf16 v[64:67], a[144:147], v[208:211], v[64:67]\n"
      "ds_read_b128 a[64:67], v242 offset:32768\n"
      "v_exp_f32 v236, v240\n"
      "v_mfma_f32_16x16x32_bf16 v[68:71], a[144:147], v[212:215], v[68:71]\n"
      "ds_read_b128 a[68:71], v242 offset:36864\n"
      "v_fmamk_f32 v241, v145, 0x3e0293ee, v226\n"
      "v_add_f32 v230, v230, v234\n"
      "v_mfma_f32_16x16x32_bf16 v[72:75], a[144:147], v[216:219], v[72:75]\n"
      "ds_read_b128 a[72:75], v243 offset:32768\n"
      "v_exp_f32 v237, v241\n"
      "v_mfma_f32_16x16x32_bf16 v[76:79], a[144:147], v[220:223], v[76:79]\n"
      "ds_read_b128 a[76:79], v243 offset:36864\n"
      "v_fmamk_f32 v240, v146, 0x3e0293ee, v226\n"
      "v_add_f32 v234, v236, v237\n"
      "v_cvt_pk_bf16_f32 v200, v236, v237\n"
      "v_mfma_f32_16x16x32_bf16 v[80:83], a[148:151], v[208:211], v[80:83]\n"
      "ds_read_b128 a[80:83], v242 offset:33792\n"
      "v_exp_f32 v238, v240\n"
      "v_mfma_f32_16x16x32_bf16 v[84:87], a[148:151], v[212:215], v[84:87]\n"
      "ds_read_b128 a[84:87], v242 offset:37888\n"
      "v_fmamk_f32 v241, v147, 0x3e0293ee, v226\n"
      "v_add_f32 v234, v234, v238\n"
      "v_mfma_f32_16x16x32_bf16 v[88:91], a[148:151], v[216:219], v[88:91]\n"
      "ds_read_b128 a[88:91], v243 offset:33792\n"
      "v_exp_f32 v239, v241\n"
      "v_mfma_f32_16x16x32_bf16 v[92:95], a[148:151], v[220:223], v[92:95]\n"
      "ds_read_b128 a[92:95], v243 offset:37888\n"
      "v_fmamk_f32 v240, v152, 0x3e0293ee, v227\n"
      "v_add_f32 v234, v234, v239\n"
      "v_cvt_pk_bf16_f32 v201, v238, v239\n"
      "v_mfma_f32_16x16x32_bf16 v[96:99], a[152:155], v[208:211], v[96:99]\n"
      "v_exp_f32 v236, v240\n"
      "v_mfma_f32_16x16x32_bf16 v[100:103], a[152:155], v[212:215], v[100:103]\n"
      "v_fmamk_f32 v241, v153, 0x3e0293ee, v227\n"
      "v_add_f32 v231, v231, v235\n"
      "v_mfma_f32_16x16x32_bf16 v[104:107], a[152:155], v[216:219], v[104:107]\n"
      "v_exp_f32 v237, v241\n"
      "v_mfma_f32_16x16x32_bf16 v[108:111], a[152:155], v[220:223], v[108:111]\n"
      "v_fmamk_f32 v240, v154, 0x3e0293ee, v227\n"
      "v_add_f32 v235, v236, v237\n"
      "v_cvt_pk_bf16_f32 v204, v236, v237\n"
      "v_mfma_f32_16x16x32_bf16 v[112:115], a[156:159], v[208:211], v[112:115]\n"
      "v_exp_f32 v238, v240\n"
      "v_mfma_f32_16x16x32_bf16 v[116:119], a[156:159], v[212:215], v[116:119]\n"
      "v_fmamk_f32 v241, v155, 0x3e0293ee, v227\n"
      "v_add_f32 v235, v235, v238\n"
      "v_mfma_f32_16x16x32_bf16 v[120:123], a[156:159], v[216:219], v[120:123]\n"
      "v_exp_f32 v239, v241\n"
      "v_mfma_f32_16x16x32_bf16 v[124:127], a[156:159], v[220:223], v[124:127]\n"
      "s_waitcnt lgkmcnt(0)\n"
      "v_fmamk_f32 v240, v132, 0x3e0293ee, v224\n"
      "v_add_f32 v235, v235, v239\n"
      "v_cvt_pk_bf16_f32 v205, v238, v239\n"
      "v_mfma_f32_16x16x32_bf16 v[160:163], a[64:67], a[0:3], 0\n"
      "v_exp_f32 v236, v240\n"
      "v_mfma_f32_16x16x32_bf16 v[168:171], a[64:67], a[4:7], 0\n"
      "v_fmamk_f32 v241, v133, 0x3e0293ee, v224\n"
      "v_add_f32 v232, v232, v236\n"
      "v_mfma_f32_16x16x32_bf16 v[176:179], a[64:67], a[8:11], 0\n"
      "v_exp_f32 v237, v241\n"
      "v_mfma_f32_16x16x32_bf16 v[184:187], a[64:67], a[12:15], 0\n"
      "v_fmamk_f32 v240, v134, 0x3e0293ee, v224\n"
      "v_add_f32 v232, v232, v237\n"
      "v_cvt_pk_bf16_f32 v194, v236, v237\n"
      "v_mfma_f32_16x16x32_bf16 v[164:167], a[68:71], a[0:3], 0\n"
      "v_exp_f32 v238, v240\n"
      "v_mbcnt_lo_u32_b32 v248, -1, 0\n"
      "v_mfma_f32_16x16x32_bf16 v[172:175], a[68:71], a[4:7], 0\n"
      "v_fmamk_f32 v241, v135, 0x3e0293ee, v224\n"
      "v_add_f32 v232, v232, v238\n"
      "v_mfma_f32_16x16x32_bf16 v[180:183], a[68:71], a[8:11], 0\n"
      "v_exp_f32 v239, v241\n"
      "v_mbcnt_hi_u32_b32 v248, -1, v248\n"
      "v_mfma_f32_16x16x32_bf16 v[188:191], a[68:71], a[12:15], 0\n"
      "v_fmamk_f32 v240, v140, 0x3e0293ee, v225\n"
      "v_add_f32 v232, v232, v239\n"
      "v_cvt_pk_bf16_f32 v195, v238, v239\n"
      "v_mfma_f32_16x16x32_bf16 v[160:163], a[72:75], a[16:19], v[160:163]\n"
      "v_exp_f32 v236, v240\n"
      "v_lshlrev_b32 v248, 4, v248\n"
      "v_mfma_f32_16x16x32_bf16 v[168:171], a[72:75], a[20:23], v[168:171]\n"
      "v_fmamk_f32 v241, v141, 0x3e0293ee, v225\n"
      "v_add_f32 v233, v233, v236\n"
      "v_mfma_f32_16x16x32_bf16 v[176:179], a[72:75], a[24:27], v[176:179]\n"
      "v_exp_f32 v237, v241\n"
      "v_add_u32 v242, %[xw], v248\n"
      "v_mfma_f32_16x16x32_bf16 v[184:187], a[72:75], a[28:31], v[184:187]\n"
      "v_fmamk_f32 v240, v142, 0x3e0293ee, v225\n"
      "v_add_f32 v233, v233, v237\n"
      "v_cvt_pk_bf16_f32 v198, v236, v237\n"
      "v_mfma_f32_16x16x32_bf16 v[164:167], a[76:79], a[16:19], v[164:167]\n"
      "v_exp_f32 v238, v240\n"
      "v_xor_b32 v243, 0x4000, v242\n"
      "v_mfma_f32_16x16x32_bf16 v[172:175], a[76:79], a[20:23], v[172:175]\n"
      "v_fmamk_f32 v241, v143, 0x3e0293ee, v225\n"
      "v_add_f32 v233, v233, v238\n"
      "v_mfma_f32_16x16x32_bf16 v[180:183], a[76:79], a[24:27], v[180:183]\n"
      "v_exp_f32 v239, v241\n"
      "v_mfma_f32_16x16x32_bf16 v[188:191], a[76:79], a[28:31], v[188:191]\n"
      "v_fmamk_f32 v240, v148, 0x3e0293ee, v226\n"
      "v_add_f32 v233, v233, v239\n"
      "v_cvt_pk_bf16_f32 v199, v238, v239\n"
      "v_mfma_f32_16x16x32_bf16 v[160:163], a[80:83], a[32:35], v[160:163]\n"
      "v_exp_f32 v236, v240\n"
      "v_mfma_f32_16x16x32_bf16 v[168:171], a[80:83], a[36:39], v[168:171]\n"
      "v_fmamk_f32 v241, v149, 0x3e0293ee, v226\n"
      "v_add_f32 v234, v234, v236\n"
      "v_mfma_f32_16x16x32_bf16 v[176:179], a[80:83], a[40:43], v[176:179]\n"
      "v_exp_f32 v237, v241\n"
      "v_mfma_f32_16x16x32_bf16 v[184:187], a[80:83], a[44:47], v[184:187]\n"
      "v_fmamk_f32 v240, v150, 0x3e0293ee, v226\n"
      "v_add_f32 v234, v234, v237\n"
      "v_cvt_pk_bf16_f32 v202, v236, v237\n"
      "s_waitcnt vmcnt(4)\n"
      "s_barrier\n"
      "v_mfma_f32_16x16x32_bf16 v[164:167], a[84:87], a[32:35], v[164:167]\n"
      "ds_read_b64_tr_b16 a[120:121], v246 offset:33792\n"
      "v_exp_f32 v238, v240\n"
      "v_mfma_f32_16x16x32_bf16 v[172:175], a[84:87], a[36:39], v[172:175]\n"
      "ds_read_b64_tr_b16 a[122:123], v246 offset:37888\n"
      "v_fmamk_f32 v241, v151, 0x3e0293ee, v226\n"
      "v_add_f32 v234, v234, v238\n"
      "v_mfma_f32_16x16x32_bf16 v[180:183], a[84:87], a[40:43], v[180:183]\n"
      "ds_read_b64_tr_b16 a[124:125], v247 offset:33792\n"
      "v_exp_f32 v239, v241\n"
      "v_mfma_f32_16x16x32_bf16 v[188:191], a[84:87], a[44:47], v[188:191]\n"
      "ds_read_b64_tr_b16 a[126:127], v247 offset:37888\n"
      "v_fmamk_f32 v240, v156, 0x3e0293ee, v227\n"
      "v_add_f32 v234, v234, v239\n"
      "v_cvt_pk_bf16_f32 v203, v238, v239\n"
      "v_mfma_f32_16x16x32_bf16 v[160:163], a[88:91], a[48:51], v[160:163]\n"
      "ds_read_b64_tr_b16 a[96:97], v244 offset:32768\n"
      "v_exp_f32 v236, v240\n"
      "v_mfma_f32_16x16x32_bf16 v[168:171], a[88:91], a[52:55], v[168:171]\n"
      "ds_read_b64_tr_b16 a[98:99], v244 offset:36864\n"
      "v_fmamk_f32 v241, v157, 0x3e0293ee, v227\n"
      "v_add_f32 v235, v235, v236\n"
      "v_mfma_f32_16x16x32_bf16 v[176:179], a[88:91], a[56:59], v[176:179]\n"
      "ds_read_b64_tr_b16 a[100:101], v245 offset:32768\n"
      "v_exp_f32 v237, v241\n"
      "v_mfma_f32_16x16x32_bf16 v[184:187], a[88:91], a[60:63], v[184:187]\n"
      "ds_read_b64_tr_b16 a[102:103], v245 offset:36864\n"
      "v_fmamk_f32 v240, v158, 0x3e0293ee, v227\n"
      "v_add_f32 v235, v235, v237\n"
      "v_cvt_pk_bf16_f32 v206, v236, v237\n"
      "v_mfma_f32_16x16x32_bf16 v[164:167], a[92:95], a[48:51], v[164:167]\n"
      "s_mov_b32 s64, 3\n"
      "ds_read_b64_tr_b16 a[104:105], v246 offset:32768\n"
      "v_exp_f32 v238, v240\n"
      "v_mfma_f32_16x16x32_bf16 v[172:175], a[92:95], a[52:55], v[172:175]\n"
      "ds_read_b64_tr_b16 a[106:107], v246 offset:36864\n"
      "v_fmamk_f32 v241, v159, 0x3e0293ee, v227\n"
      "v_add_f32 v235, v235, v238\n"
      "v_mfma_f32_16x16x32_bf16 v[180:183], a[92:95], a[56:59], v[180:183]\n"
      "v_exp_f32 v239, v241\n"
      "v_mfma_f32_16x16x32_bf16 v[188:191], a[92:95], a[60:63], v[188:191]\n"
      "v_add_f32 v235, v235, v239\n"
      "v_cvt_pk_bf16_f32 v207, v238, v239\n"
      "v_max3_f32 v240, v232, v233, v234\n"
      "v_add_f32 v240, v240, v235\n"
      "v_cmp_nge_f32 vcc, 0x44800000, v240\n"
      "s_cbranch_vccnz FB0_%=\n"
      "RET0_3_%=:\n"
      "s_waitcnt lgkmcnt(2)\n"
      "v_mfma_f32_16x16x32_bf16 v[96:99], a[120:123], v[192:195], v[96:99]\n"
      "ds_read_b64_tr_b16 a[108:109], v247 offset:32768\n"
      "v_fmamk_f32 v240, v160, 0x3e0293ee, v224\n"
      "v_exp_f32 v236, v240\n"
      "v_fmamk_f32 v241, v161, 0x3e0293ee, v224\n"
      "v_mfma_f32_16x16x32_bf16 v[100:103], a[120:123], v[196:199], v[100:103]\n"
      "ds_read_b64_tr_b16 a[110:111], v247 offset:36864\n"
      "v_exp_f32 v237, v241\n"
      "v_fmamk_f32 v240, v162, 0x3e0293ee, v224\n"
      "v_add_f32 v228, v228, v232\n"
      "v_mfma_f32_16x16x32_bf16 v[104:107], a[120:123], v[200:203], v[104:107]\n"
      "ds_read_b64_tr_b16 a[112:113], v244 offset:33792\n"
      "v_exp_f32 v238, v240\n"
      "v_fmamk_f32 v241, v163, 0x3e0293ee, v224\n"
      "v_add_f32 v232, v236, v237\n"
      "v_cvt_pk_bf16_f32 v208, v236, v237\n"
      "v_mfma_f32_16x16x32_bf16 v[108:111], a[120:123], v[204:207], v[108:111]\n"
      "ds_read_b64_tr_b16 a[114:115], v244 offset:37888\n"
      "v_exp_f32 v239, v241\n"
      "v_fmamk_f32 v240, v168, 0x3e0293ee, v225\n"
      "v_add_f32 v232, v232, v238\n"
      "v_mfma_f32_16x16x32_bf16 v[112:115], a[124:127], v[192:195], v[112:115]\n"
      "ds_read_b64_tr_b16 a[116:117], v245 offset:33792\n"
      "v_exp_f32 v236, v240\n"
      "v_fmamk_f32 v241, v169, 0x3e0293ee, v225\n"
      "v_add_f32 v232, v232, v239\n"
      "v_cvt_pk_bf16_f32 v209, v238, v239\n"
      "v_mfma_f32_16x16x32_bf16 v[116:119], a[124:127], v[196:199], v[116:119]\n"
      "ds_read_b64_tr_b16 a[118:119], v245 offset:37888\n"
      "v_exp_f32 v237, v241\n"
      "v_fmamk_f32 v240, v170, 0x3e0293ee, v225\n"
      "v_add_f32 v229, v229, v233\n"
      "v_mfma_f32_16x16x32_bf16 v[120:123], a[124:127], v[200:203], v[120:123]\n"
      "v_exp_f32 v238, v240\n"
      "v_fmamk_f32 v241, v171, 0x3e0293ee, v225\n"
      "v_add_f32 v233, v236, v237\n"
      "v_cvt_pk_bf16_f32 v212, v236, v237\n"
      "v_mfma_f32_16x16x32_bf16 v[124:127], a[124:127], v[204:207], v[124:127]\n"
      "v_exp_f32 v239, v241\n"
      "v_fmamk_f32 v240, v176, 0x3e0293ee, v226\n"
      "v_add_f32 v233, v233, v238\n"
      "v_mfma_f32_16x16x32_bf16 v[0:3], a[96:99], v[192:195], v[0:3]\n"
      "v_exp_f32 v236, v240\n"
      "v_fmamk_f32 v241, v177, 0x3e0293ee, v226\n"
      "v_add_f32 v233, v233, v239\n"
      "v_cvt_pk_bf16_f32 v213, v238, v239\n"
      "v_mfma_f32_16x16x32_bf16 v[4:7], a[96:99], v[196:199], v[4:7]\n"
      "ds_write_b128 v242, v[96:99] offset:32768\n"
      "v_exp_f32 v237, v241\n"
      "v_fmamk_f32 v240, v178, 0x3e0293ee, v226\n"
      "v_add_f32 v230, v230, v234\n"
      "v_mfma_f32_16x16x32_bf16 v[8:11], a[96:99], v[200:203], v[8:11]\n"
      "v_exp_f32 v238, v240\n"
      "v_fmamk_f32 v241, v179, 0x3e0293ee, v226\n"
      "v_add_f32 v234, v236, v237\n"
      "v_cvt_pk_bf16_f32 v216, v236, v237\n"
      "v_xor_b32 v244, 0x10000, v244\n"
      "v_mfma_f32_16x16x32_bf16 v[12:15], a[96:99], v[204:207], v[12:15]\n"
      "ds_write_b128 v242, v[100:103] offset:33792\n"
      "v_exp_f32 v239, v241\n"
      "v_fmamk_f32 v240, v184, 0x3e0293ee, v227\n"
      "v_add_f32 v234, v234, v238\n"
      "v_mfma_f32_16x16x32_bf16 v[16:19], a[100:103], v[192:195], v[16:19]\n"
      "v_exp_f32 v236, v240\n"
      "v_fmamk_f32 v241, v185, 0x3e0293ee, v227\n"
      "v_add_f32 v234, v234, v239\n"
      "v_cvt_pk_bf16_f32 v217, v238, v239\n"
      "v_xor_b32 v245, 0x10000, v245\n"
      "v_mfma_f32_16x16x32_bf16 v[20:23], a[100:103], v[196:199], v[20:23]\n"
      "ds_write_b128 v242, v[104:107] offset:34816\n"
      "v_exp_f32 v237, v241\n"
      "v_fmamk_f32 v240, v186, 0x3e0293ee, v227\n"
      "v_add_f32 v231, v231, v235\n"
      "v_mfma_f32_16x16x32_bf16 v[24:27], a[100:103], v[200:203], v[24:27]\n"
      "v_exp_f32 v238, v240\n"
      "v_fmamk_f32 v241, v187, 0x3e0293ee, v227\n"
      "v_add_f32 v235, v236, v237\n"
      "v_cvt_pk_bf16_f32 v220, v236, v237\n"
      "v_xor_b32 v246, 0x10000, v246\n"
      "v_mfma_f32_16x16x32_bf16 v[28:31], a[100:103], v[204:207], v[28:31]\n"
      "ds_write_b128 v242, v[108:111] offset:35840\n"
      "v_exp_f32 v239, v241\n"
      "v_fmamk_f32 v240, v164, 0x3e0293ee, v224\n"
      "v_add_f32 v235, v235, v238\n"
      "s_waitcnt lgkmcnt(4)\n"
      "v_mfma_f32_16x16x32_bf16 v[32:35], a[104:107], v[192:195], v[32:35]\n"
      "v_exp_f32 v236, v240\n"
      "v_fmamk_f32 v241, v165, 0x3e0293ee, v224\n"
      "v_add_f32 v235, v235, v239\n"
      "v_cvt_pk_bf16_f32 v221, v238, v239\n"
      "v_xor_b32 v247, 0x10000, v247\n"
      "v_mfma_f32_16x16x32_bf16 v[36:39], a[104:107], v[196:199], v[36:39]\n"
      "ds_write_b128 v242, v[112:115] offset:36864\n"
      "v_exp_f32 v237, v241\n"
      "v_fmamk_f32 v240, v166, 0x3e0293ee, v224\n"
      "v_add_f32 v232, v232, v236\n"
      "v_mfma_f32_16x16x32_bf16 v[40:43], a[104:107], v[200:203], v[40:43]\n"
      "v_exp_f32 v238, v240\n"
      "v_fmamk_f32 v241, v167, 0x3e0293ee, v224\n"
      "v_add_f32 v232, v232, v237\n"
      "v_cvt_pk_bf16_f32 v210, v236, v237\n"
      "v_mfma_f32_16x16x32_bf16 v[44:47], a[104:107], v[204:207], v[44:47]\n"
      "ds_write_b128 v242, v[116:119] offset:37888\n"
      "v_exp_f32 v239, v241\n"
      "v_fmamk_f32 v240, v172, 0x3e0293ee, v225\n"
      "v_add_f32 v232, v232, v238\n"
      "v_mfma_f32_16x16x32_bf16 v[48:51], a[108:111], v[192:195], v[48:51]\n"
      "v_exp_f32 v236, v240\n"
      "v_fmamk_f32 v241, v173, 0x3e0293ee, v225\n"
      "v_add_f32 v232, v232, v239\n"
      "v_cvt_pk_bf16_f32 v211, v238, v239\n"
      "v_xor_b32 v250, 0x2000, v244\n"
      "v_mfma_f32_16x16x32_bf16 v[52:55], a[108:111], v[196:199], v[52:55]\n"
      "ds_write_b128 v242, v[120:123] offset:38912\n"
      "ds_write_b128 v242, v[208:211] offset:2048\n"
      "v_exp_f32 v237, v241\n"
      "v_fmamk_f32 v240, v174, 0x3e0293ee, v225\n"
      "v_add_f32 v233, v233, v236\n"
      "v_mfma_f32_16x16x32_bf16 v[56:59], a[108:111], v[200:203], v[56:59]\n"
      "v_exp_f32 v238, v240\n"
      "v_fmamk_f32 v241, v175, 0x3e0293ee, v225\n"
      "v_add_f32 v233, v233, v237\n"
      "v_cvt_pk_bf16_f32 v214, v236, v237\n"
      "v_xor_b32 v251, 0x2000, v245\n"
      "v_mfma_f32_16x16x32_bf16 v[60:63], a[108:111], v[204:207], v[60:63]\n"
      "ds_write_b128 v242, v[124:127] offset:39936\n"
      "v_exp_f32 v239, v241\n"
      "v_fmamk_f32 v240, v180, 0x3e0293ee, v226\n"
      "v_add_f32 v233, v233, v238\n"
      "v_mfma_f32_16x16x32_bf16 v[64:67], a[112:115], v[192:195], v[64:67]\n"
      "v_exp_f32 v236, v240\n"
      "v_fmamk_f32 v241, v181, 0x3e0293ee, v226\n"
      "v_add_f32 v233, v233, v239\n"
      "v_cvt_pk_bf16_f32 v215, v238, v239\n"
      "v_mfma_f32_16x16x32_bf16 v[68:71], a[112:115], v[196:199], v[68:71]\n"
      "ds_write_b128 v242, v[212:215] offset:3072\n"
      "v_exp_f32 v237, v241\n"
      "v_fmamk_f32 v240, v182, 0x3e0293ee, v226\n"
      "v_add_f32 v234, v234, v236\n"
      "v_mfma_f32_16x16x32_bf16 v[72:75], a[112:115], v[200:203], v[72:75]\n"
      "v_exp_f32 v238, v240\n"
      "v_fmamk_f32 v241, v183, 0x3e0293ee, v226\n"
      "v_add_f32 v234, v234, v237\n"
      "v_cvt_pk_bf16_f32 v218, v236, v237\n"
      "v_mfma_f32_16x16x32_bf16 v[76:79], a[112:115], v[204:207], v[76:79]\n"
      "v_exp_f32 v239, v241\n"
      "v_fmamk_f32 v240, v188, 0x3e0293ee, v227\n"
      "v_add_f32 v234, v234, v238\n"
      "v_mfma_f32_16x16x32_bf16 v[80:83], a[116:119], v[192:195], v[80:83]\n"
      "s_mov_b32 s64, 4\n"
      "v_exp_f32 v236, v240\n"
      "v_fmamk_f32 v241, v189, 0x3e0293ee, v227\n"
      "v_add_f32 v234, v234, v239\n"
      "v_cvt_pk_bf16_f32 v219, v238, v239\n"
      "v_mfma_f32_16x16x32_bf16 v[84:87], a[116:119], v[196:199], v[84:87]\n"
      "ds_write_b128 v242, v[216:219] offset:4096\n"
      "v_exp_f32 v237, v241\n"
      "v_fmamk_f32 v240, v190, 0x3e0293ee, v227\n"
      "v_add_f32 v235, v235, v236\n"
      "v_mfma_f32_16x16x32_bf16 v[88:91], a[116:119], v[200:203], v[88:91]\n"
      "v_exp_f32 v238, v240\n"
      "v_fmamk_f32 v241, v191, 0x3e0293ee, v227\n"
      "v_add_f32 v235, v235, v237\n"
      "v_cvt_pk_bf16_f32 v222, v236, v237\n"
      "v_mfma_f32_16x16x32_bf16 v[92:95], a[116:119], v[204:207], v[92:95]\n"
      "v_exp_f32 v239, v241\n"
      "v_add_f32 v235, v235, v238\n"
      "v_add_f32 v235, v235, v239\n"
      "v_cvt_pk_bf16_f32 v223, v238, v239\n"
      "v_max3_f32 v240, v232, v233, v234\n"
      "v_add_f32 v240, v240, v235\n"
      "v_cmp_nge_f32 vcc, 0x44800000, v240\n"
      "s_cbranch_vccnz FB1_%=\n"
      "RET1_4_%=:\n"
      "ds_write_b128 v242, v[220:223] offset:5120\n"
      "ds_write_b128 v242, v[224:227]\n"
      "v_add_f32 v228, v228, v232\n"
      "v_add_f32 v229, v229, v233\n"
      "v_add_f32 v230, v230, v234\n"
      "v_add_f32 v231, v231, v235\n"
      "ds_write_b128 v242, v[228:231] offset:1024\n"
      "s_waitcnt vmcnt(0) lgkmcnt(0)\n"
      "s_barrier\n"
      "ds_read_b64_tr_b16 a[144:145], v244 offset:1024\n"
      "ds_read_b64_tr_b16 a[146:147], v244 offset:5120\n"
      "ds_read_b64_tr_b16 a[148:149], v245 offset:1024\n"
      "ds_read_b64_tr_b16 a[150:151], v245 offset:5120\n"
      "ds_read_b128 v[128:131], v243\n"
      "ds_read_b128 v[132:135], v243 offset:1024\n"
      "ds_read_b128 v[192:195], v243 offset:2048\n"
      "ds_read_b128 v[196:199], v243 offset:3072\n"
      "ds_read_b128 v[200:203], v243 offset:4096\n"
      "ds_read_b128 v[204:207], v243 offset:5120\n"
      "s_waitcnt lgkmcnt(8)\n"
      "v_mfma_f32_16x16x32_bf16 v[64:67], a[144:147], v[208:211], v[64:67]\n"
      "ds_read_b64_tr_b16 a[152:153], v250 offset:1024\n"
      "ds_read_b64_tr_b16 a[154:155], v250 offset:5120\n"
      "v_mfma_f32_16x16x32_bf16 v[68:71], a[144:147], v[212:215], v[68:71]\n"
      "ds_read_b128 v[96:99], v243 offset:32768\n"
      "ds_read_b128 v[100:103], v243 offset:33792\n"
      "v_mfma_f32_16x16x32_bf16 v[72:75], a[144:147], v[216:219], v[72:75]\n"
      "ds_read_b128 v[104:107], v243 offset:34816\n"
      "ds_read_b128 v[108:111], v243 offset:35840\n"
      "v_mfma_f32_16x16x32_bf16 v[76:79], a[144:147], v[220:223], v[76:79]\n"
      "ds_read_b64_tr_b16 a[156:157], v251 offset:1024\n"
      "ds_read_b64_tr_b16 a[158:159], v251 offset:5120\n"
      "s_waitcnt lgkmcnt(12)\n"
      "v_mfma_f32_16x16x32_bf16 v[80:83], a[148:151], v[208:211], v[80:83]\n"
      "ds_read_b128 v[112:115], v243 offset:36864\n"
      "ds_read_b128 v[116:119], v243 offset:37888\n"
      "v_sub_f32 v232, v128, v224\n"
      "v_sub_f32 v233, v129, v225\n"
      "v_mfma_f32_16x16x32_bf16 v[84:87], a[148:151], v[212:215], v[84:87]\n"
      "ds_read_b128 v[120:123], v243 offset:38912\n"
      "ds_read_b128 v[124:127], v243 offset:39936\n"
      "v_sub_f32 v234, v130, v226\n"
      "v_sub_f32 v235, v131, v227\n"
      "v_mfma_f32_16x16x32_bf16 v[88:91], a[148:151], v[216:219], v[88:91]\n"
      "v_min_f32 v236, 0, -v232\n"
      "v_min_f32 v237, 0, -v233\n"
      "v_mfma_f32_16x16x32_bf16 v[92:95], a[148:151], v[220:223], v[92:95]\n"
      "v_min_f32 v238, 0, -v234\n"
      "v_min_f32 v239, 0, -v235\n"
      "s_waitcnt lgkmcnt(6)\n"
      "v_mfma_f32_16x16x32_bf16 v[96:99], a[152:155], v[192:195], v[96:99]\n"
      "ds_read_b64_tr_b16 a[136:137], v246 offset:0\n"
      "v_min_f32 v232, 0, v232\n"
      "v_min_f32 v233, 0, v233\n"
      "v_mfma_f32_16x16x32_bf16 v[100:103], a[152:155], v[196:199], v[100:103]\n"
      "ds_read_b64_tr_b16 a[138:139], v246 offset:4096\n"
      "v_min_f32 v234, 0, v234\n"
      "v_min_f32 v235, 0, v235\n"
      "v_mfma_f32_16x16x32_bf16 v[104:107], a[152:155], v[200:203], v[104:107]\n"
      "ds_read_b64_tr_b16 a[140:141], v247 offset:0\n"
      "v_exp_f32 v232, v232\n"
      "v_exp_f32 v233, v233\n"
      "v_mfma_f32_16x16x32_bf16 v[108:111], a[152:155], v[204:207], v[108:111]\n"
      "ds_read_b64_tr_b16 a[142:143], v247 offset:4096\n"
      "v_exp_f32 v234, v234\n"
      "v_exp_f32 v235, v235\n"
      "s_waitcnt lgkmcnt(4)\n"
      "v_mfma_f32_16x16x32_bf16 v[112:115], a[156:159], v[192:195], v[112:115]\n"
      "ds_read_b64_tr_b16 a[128:129], v244 offset:0\n"
      "v_exp_f32 v236, v236\n"
      "v_exp_f32 v237, v237\n"
      "v_mfma_f32_16x16x32_bf16 v[116:119], a[156:159], v[196:199], v[116:119]\n"
      "ds_read_b64_tr_b16 a[130:131], v244 offset:4096\n"
      "v_exp_f32 v238, v238\n"
      "v_exp_f32 v239, v239\n"
      "v_mfma_f32_16x16x32_bf16 v[120:123], a[156:159], v[200:203], v[120:123]\n"
      "ds_read_b64_tr_b16 a[132:133], v245 offset:0\n"
      "v_min_f32 v224, v224, v128\n"
      "v_min_f32 v225, v225, v129\n"
      "v_mfma_f32_16x16x32_bf16 v[124:127], a[156:159], v[204:207], v[124:127]\n"
      "ds_read_b64_tr_b16 a[134:135], v245 offset:4096\n"
      "v_min_f32 v226, v226, v130\n"
      "v_min_f32 v227, v227, v131\n"
      "v_pk_mul_f32 v[64:65], v[64:65], v[232:233] op_sel:[0,0] op_sel_hi:[1,0]\n"
      "v_pk_mul_f32 v[66:67], v[66:67], v[232:233] op_sel:[0,0] op_sel_hi:[1,0]\n"
      "v_pk_mul_f32 v[68:69], v[68:69], v[232:233] op_sel:[0,1] op_sel_hi:[1,1]\n"
      "v_pk_mul_f32 v[70:71], v[70:71], v[232:233] op_sel:[0,1] op_sel_hi:[1,1]\n"
      "v_pk_mul_f32 v[72:73], v[72:73], v[234:235] op_sel:[0,0] op_sel_hi:[1,0]\n"
      "v_pk_mul_f32 v[74:75], v[74:75], v[234:235] op_sel:[0,0] op_sel_hi:[1,0]\n"
      "v_pk_mul_f32 v[76:77], v[76:77], v[234:235] op_sel:[0,1] op_sel_hi:[1,1]\n"
      "v_pk_mul_f32 v[78:79], v[78:79], v[234:235] op_sel:[0,1] op_sel_hi:[1,1]\n"
      "v_pk_mul_f32 v[80:81], v[80:81], v[232:233] op_sel:[0,0] op_sel_hi:[1,0]\n"
      "v_pk_mul_f32 v[82:83], v[82:83], v[232:233] op_sel:[0,0] op_sel_hi:[1,0]\n"
      "v_pk_mul_f32 v[84:85], v[84:85], v[232:233] op_sel:[0,1] op_sel_hi:[1,1]\n"
      "v_pk_mul_f32 v[86:87], v[86:87], v[232:233] op_sel:[0,1] op_sel_hi:[1,1]\n"
      "v_pk_mul_f32 v[88:89], v[88:89], v[234:235] op_sel:[0,0] op_sel_hi:[1,0]\n"
      "v_pk_mul_f32 v[90:91], v[90:91], v[234:235] op_sel:[0,0] op_sel_hi:[1,0]\n"
      "v_pk_mul_f32 v[92:93], v[92:93], v[234:235] op_sel:[0,1] op_sel_hi:[1,1]\n"
      "v_pk_mul_f32 v[94:95], v[94:95], v[234:235] op_sel:[0,1] op_sel_hi:[1,1]\n"
      "v_pk_fma_f32 v[64:65], v[96:97], v[236:237], v[64:65] op_sel:[0,0,0] op_sel_hi:[1,0,1]\n"
      "v_pk_fma_f32 v[66:67], v[98:99], v[236:237], v[66:67] op_sel:[0,0,0] op_sel_hi:[1,0,1]\n"
      "v_pk_fma_f32 v[68:69], v[100:101], v[236:237], v[68:69] op_sel:[0,1,0] op_sel_hi:[1,1,1]\n"
      "v_pk_fma_f32 v[70:71], v[102:103], v[236:237], v[70:71] op_sel:[0,1,0] op_sel_hi:[1,1,1]\n"
      "v_cvt_pk_bf16_f32 v160, v64, v65\n"
      "v_cvt_pk_bf16_f32 v161, v66, v67\n"
      "v_cvt_pk_bf16_f32 v162, v68, v69\n"
      "v_cvt_pk_bf16_f32 v163, v70, v71\n"
      "buffer_store_dwordx4 v[160:163], v248, %[srdx], %[xo] offen offset:0 sc1\n"
      "v_pk_fma_f32 v[72:73], v[104:105], v[238:239], v[72:73] op_sel:[0,0,0] op_sel_hi:[1,0,1]\n"
      "v_pk_fma_f32 v[74:75], v[106:107], v[238:239], v[74:75] op_sel:[0,0,0] op_sel_hi:[1,0,1]\n"
      "v_pk_fma_f32 v[76:77], v[108:109], v[238:239], v[76:77] op_sel:[0,1,0] op_sel_hi:[1,1,1]\n"
      "v_pk_fma_f32 v[78:79], v[110:111], v[238:239], v[78:79] op_sel:[0,1,0] op_sel_hi:[1,1,1]\n"
      "v_cvt_pk_bf16_f32 v164, v72, v73\n"
      "v_cvt_pk_bf16_f32 v165, v74, v75\n"
      "v_cvt_pk_bf16_f32 v166, v76, v77\n"
      "v_cvt_pk_bf16_f32 v167, v78, v79\n"
      "buffer_store_dwordx4 v[164:167], v248, %[srdx], %[xo] offen offset:1024 sc1\n"
      "v_pk_fma_f32 v[80:81], v[112:113], v[236:237], v[80:81] op_sel:[0,0,0] op_sel_hi:[1,0,1]\n"
      "v_pk_fma_f32 v[82:83], v[114:115], v[236:237], v[82:83] op_sel:[0,0,0] op_sel_hi:[1,0,1]\n"
      "v_pk_fma_f32 v[84:85], v[116:117], v[236:237], v[84:85] op_sel:[0,1,0] op_sel_hi:[1,1,1]\n"
      "v_pk_fma_f32 v[86:87], v[118:119], v[236:237], v[86:87] op_sel:[0,1,0] op_sel_hi:[1,1,1]\n"
      "v_cvt_pk_bf16_f32 v168, v80, v81\n"
      "v_cvt_pk_bf16_f32 v169, v82, v83\n"
      "v_cvt_pk_bf16_f32 v170, v84, v85\n"
      "v_cvt_pk_bf16_f32 v171, v86, v87\n"
      "buffer_store_dwordx4 v[168:171], v248, %[srdx], %[xo] offen offset:2048 sc1\n"
      "v_pk_fma_f32 v[88:89], v[120:121], v[238:239], v[88:89] op_sel:[0,0,0] op_sel_hi:[1,0,1]\n"
      "v_pk_fma_f32 v[90:91], v[122:123], v[238:239], v[90:91] op_sel:[0,0,0] op_sel_hi:[1,0,1]\n"
      "v_pk_fma_f32 v[92:93], v[124:125], v[238:239], v[92:93] op_sel:[0,1,0] op_sel_hi:[1,1,1]\n"
      "v_pk_fma_f32 v[94:95], v[126:127], v[238:239], v[94:95] op_sel:[0,1,0] op_sel_hi:[1,1,1]\n"
      "v_cvt_pk_bf16_f32 v172, v88, v89\n"
      "v_cvt_pk_bf16_f32 v173, v90, v91\n"
      "v_cvt_pk_bf16_f32 v174, v92, v93\n"
      "v_cvt_pk_bf16_f32 v175, v94, v95\n"
      "buffer_store_dwordx4 v[172:175], v248, %[srdx], %[xo] offen offset:3072 sc1\n"
      "v_mul_f32 v228, v228, v232\n"
      "v_mul_f32 v229, v229, v233\n"
      "v_mul_f32 v230, v230, v234\n"
      "v_mul_f32 v231, v231, v235\n"
      "v_fmac_f32 v228, v132, v236\n"
      "v_fmac_f32 v229, v133, v237\n"
      "v_fmac_f32 v230, v134, v238\n"
      "v_fmac_f32 v231, v135, v239\n"
      "v_mov_b32 v128, v228\n"
      "v_mov_b32 v129, v229\n"
      "v_mov_b32 v130, v230\n"
      "v_mov_b32 v131, v231\n"
      "v_permlane16_swap_b32 v228, v128\n"
      "v_permlane16_swap_b32 v229, v129\n"
      "v_permlane16_swap_b32 v230, v130\n"
      "v_permlane16_swap_b32 v231, v131\n"
      "v_add_f32 v228, v228, v128\n"
      "v_add_f32 v229, v229, v129\n"
      "v_add_f32 v230, v230, v130\n"
      "v_add_f32 v231, v231, v131\n"
      "v_mov_b32 v128, v228\n"
      "v_mov_b32 v129, v229\n"
      "v_mov_b32 v130, v230\n"
      "v_mov_b32 v131, v231\n"
      "v_permlane32_swap_b32 v228, v128\n"
      "v_permlane32_swap_b32 v229, v129\n"
      "v_permlane32_swap_b32 v230, v130\n"
      "v_permlane32_swap_b32 v231, v131\n"
      "v_add_f32 v228, v228, v128\n"
      "v_add_f32 v229, v229, v129\n"
      "v_add_f32 v230, v230, v130\n"
      "v_add_f32 v231, v231, v131\n"
      "s_add_u32 s67, %[xo], 0x1000\n"
      "s_mov_b64 exec, 0xffff\n"
      "buffer_store_dwordx4 v[224:227], v248, %[srdx], s67 offen sc1\n"
      "buffer_store_dwordx4 v[228:231], v248, %[srdx], s67 offen offset:256 sc1\n"
      "s_mov_b64 exec, -1\n"
      "s_waitcnt lgkmcnt(0)\n"
      "v_mfma_f32_16x16x32_bf16 v[32:35], a[136:139], v[208:211], v[32:35]\n"
      "s_add_u32 s74, %[e0], 0x7f4a7c15\n"
      "s_addc_u32 s75, %[e1], 0x9e3779b9\n"
      "v_mov_b32 v128, s74\n"
      "v_mfma_f32_16x16x32_bf16 v[36:39], a[136:139], v[212:215], v[36:39]\n"
      "v_mov_b32 v129, s75\n"
      "v_mov_b32 v130, %[fo]\n"
      "v_sub_u32 v131, 24, v130\n"
      "v_mfma_f32_16x16x32_bf16 v[40:43], a[136:139], v[216:219], v[40:43]\n"
      "v_mov_b32 v138, 0\n"
      "v_add_u32 v242, 0x10000, v242\n"
      "v_add_u32 v243, 0x10000, v243\n"
      "v_mfma_f32_16x16x32_bf16 v[44:47], a[136:139], v[220:223], v[44:47]\n"
      "v_and_b32 v137, 0xf0, v248\n"
      "v_lshrrev_b32 v136, 3, v248\n"
      "v_and_b32 v136, 32, v136\n"
      "v_mfma_f32_16x16x32_bf16 v[48:51], a[140:143], v[208:211], v[48:51]\n"
      "v_lshl_or_b32 v136, v137, 4, v136\n"
      "v_lshrrev_b32 v240, 5, v248\n"
      "v_and_or_b32 v136, v240, 16, v136\n"
      "v_mfma_f32_16x16x32_bf16 v[52:55], a[140:143], v[212:215], v[52:55]\n"
      "s_sub_u32 s76, 18432, %[xo]\n"
      "s_add_u32 s69, s76, 0x1000\n"
      "s_mov_b32 s63, 0x100000\n"
      "v_mfma_f32_16x16x32_bf16 v[56:59], a[140:143], v[216:219], v[56:59]\n"
      "v_mfma_f32_16x16x32_bf16 v[60:63], a[140:143], v[220:223], v[60:63]\n"
      "v_mfma_f32_16x16x32_bf16 v[0:3], a[128:131], v[208:211], v[0:3]\n"
      "ds_write_b128 v242, v[32:35] offset:0\n"
      "v_mfma_f32_16x16x32_bf16 v[4:7], a[128:131], v[212:215], v[4:7]\n"
      "ds_write_b128 v242, v[36:39] offset:1024\n"
      "v_mfma_f32_16x16x32_bf16 v[8:11], a[128:131], v[216:219], v[8:11]\n"
      "ds_write_b128 v242, v[40:43] offset:2048\n"
      "v_mfma_f32_16x16x32_bf16 v[12:15], a[128:131], v[220:223], v[12:15]\n"
      "ds_write_b128 v242, v[44:47] offset:3072\n"
      "v_mfma_f32_16x16x32_bf16 v[16:19], a[132:135], v[208:211], v[16:19]\n"
      "ds_write_b128 v242, v[48:51] offset:4096\n"
      "v_mfma_f32_16x16x32_bf16 v[20:23], a[132:135], v[212:215], v[20:23]\n"
      "ds_write_b128 v242, v[52:55] offset:5120\n"
      "v_mfma_f32_16x16x32_bf16 v[24:27], a[132:135], v[216:219], v[24:27]\n"
      "ds_write_b128 v242, v[56:59] offset:6144\n"
      "v_mfma_f32_16x16x32_bf16 v[28:31], a[132:135], v[220:223], v[28:31]\n"
      "ds_write_b128 v242, v[60:63] offset:7168\n"
      "s_waitcnt vmcnt(0)\n"
      "s_mov_b64 exec, 1\n"
      "global_store_dwordx2 v130, v[128:129], %[line] sc1\n"
      "s_mov_b64 exec, -1\n"
      "s_waitcnt lgkmcnt(0)\n"
      "s_barrier\n"
      "ds_read_b128 v[32:35], v243 offset:0\n"
      "ds_read_b128 v[36:39], v243 offset:1024\n"
      "ds_read_b128 v[40:43], v243 offset:2048\n"
      "ds_read_b128 v[44:47], v243 offset:3072\n"
      "ds_read_b128 v[48:51], v243 offset:4096\n"
      "ds_read_b128 v[52:55], v243 offset:5120\n"
      "ds_read_b128 v[56:59], v243 offset:6144\n"
      "ds_read_b128 v[60:63], v243 offset:7168\n"
      "v_pk_mul_f32 v[0:1], v[0:1], v[232:233] op_sel:[0,0] op_sel_hi:[1,0]\n"
      "v_pk_mul_f32 v[2:3], v[2:3], v[232:233] op_sel:[0,0] op_sel_hi:[1,0]\n"
      "v_pk_mul_f32 v[4:5], v[4:5], v[232:233] op_sel:[0,1] op_sel_hi:[1,1]\n"
      "v_pk_mul_f32 v[6:7], v[6:7], v[232:233] op_sel:[0,1] op_sel_hi:[1,1]\n"
      "v_pk_mul_f32 v[8:9], v[8:9], v[234:235] op_sel:[0,0] op_sel_hi:[1,0]\n"
      "v_pk_mul_f32 v[10:11], v[10:11], v[234:235] op_sel:[0,0] op_sel_hi:[1,0]\n"
      "v_pk_mul_f32 v[12:13], v[12:13], v[234:235] op_sel:[0,1] op_sel_hi:[1,1]\n"
      "v_pk_mul_f32 v[14:15], v[14:15], v[234:235] op_sel:[0,1] op_sel_hi:[1,1]\n"
      "v_pk_mul_f32 v[16:17], v[16:17], v[232:233] op_sel:[0,0] op_sel_hi:[1,0]\n"
      "v_pk_mul_f32 v[18:19], v[18:19], v[232:233] op_sel:[0,0] op_sel_hi:[1,0]\n"
      "v_pk_mul_f32 v[20:21], v[20:21], v[232:233] op_sel:[0,1] op_sel_hi:[1,1]\n"
      "v_pk_mul_f32 v[22:23], v[22:23], v[232:233] op_sel:[0,1] op_sel_hi:[1,1]\n"
      "v_pk_mul_f32 v[24:25], v[24:25], v[234:235] op_sel:[0,0] op_sel_hi:[1,0]\n"
      "v_pk_mul_f32 v[26:27], v[26:27], v[234:235] op_sel:[0,0] op_sel_hi:[1,0]\n"
      "v_pk_mul_f32 v[28:29], v[28:29], v[234:235] op_sel:[0,1] op_sel_hi:[1,1]\n"
      "v_pk_mul_f32 v[30:31], v[30:31], v[234:235] op_sel:[0,1] op_sel_hi:[1,1]\n"
      "global_load_dwordx2 v[132:133], v131, %[line] sc1\n"
      "s_waitcnt lgkmcnt(6)\n"
      "v_pk_fma_f32 v[0:1], v[32:33], v[236:237], v[0:1] op_sel:[0,0,0] op_sel_hi:[1,0,1]\n"
      "v_pk_fma_f32 v[2:3], v[34:35], v[236:237], v[2:3] op_sel:[0,0,0] op_sel_hi:[1,0,1]\n"
      "v_pk_fma_f32 v[4:5], v[36:37], v[236:237], v[4:5] op_sel:[0,1,0] op_sel_hi:[1,1,1]\n"
      "v_pk_fma_f32 v[6:7], v[38:39], v[236:237], v[6:7] op_sel:[0,1,0] op_sel_hi:[1,1,1]\n"
      "s_waitcnt lgkmcnt(4)\n"
      "v_pk_fma_f32 v[8:9], v[40:41], v[238:239], v[8:9] op_sel:[0,0,0] op_sel_hi:[1,0,1]\n"
      "v_pk_fma_f32 v[10:11], v[42:43], v[238:239], v[10:11] op_sel:[0,0,0] op_sel_hi:[1,0,1]\n"
      "v_pk_fma_f32 v[12:13], v[44:45], v[238:239], v[12:13] op_sel:[0,1,0] op_sel_hi:[1,1,1]\n"
      "v_pk_fma_f32 v[14:15], v[46:47], v[238:239], v[14:15] op_sel:[0,1,0] op_sel_hi:[1,1,1]\n"
      "s_waitcnt lgkmcnt(2)\n"
      "v_pk_fma_f32 v[16:17], v[48:49], v[236:237], v[16:17] op_sel:[0,0,0] op_sel_hi:[1,0,1]\n"
      "v_pk_fma_f32 v[18:19], v[50:51], v[236:237], v[18:19] op_sel:[0,0,0] op_sel_hi:[1,0,1]\n"
      "v_pk_fma_f32 v[20:21], v[52:53], v[236:237], v[20:21] op_sel:[0,1,0] op_sel_hi:[1,1,1]\n"
      "v_pk_fma_f32 v[22:23], v[54:55], v[236:237], v[22:23] op_sel:[0,1,0] op_sel_hi:[1,1,1]\n"
      "s_waitcnt lgkmcnt(0)\n"
      "v_pk_fma_f32 v[24:25], v[56:57], v[238:239], v[24:25] op_sel:[0,0,0] op_sel_hi:[1,0,1]\n"
      "v_pk_fma_f32 v[26:27], v[58:59], v[238:239], v[26:27] op_sel:[0,0,0] op_sel_hi:[1,0,1]\n"
      "v_pk_fma_f32 v[28:29], v[60:61], v[238:239], v[28:29] op_sel:[0,1,0] op_sel_hi:[1,1,1]\n"
      "v_pk_fma_f32 v[30:31], v[62:63], v[238:239], v[30:31] op_sel:[0,1,0] op_sel_hi:[1,1,1]\n"
      "POLL_%=:\n"
      "global_load_dwordx2 v[134:135], v131, %[line] sc1\n"
      "s_waitcnt vmcnt(1)\n"
      "v_cmp_ne_u64 vcc, v[132:133], v[128:129]\n"
      "s_cbranch_vccz GOT_%=\n"
      "global_load_dwordx2 v[132:133], v131, %[line] sc1\n"
      "s_waitcnt vmcnt(1)\n"
      "v_cmp_ne_u64 vcc, v[134:135], v[128:129]\n"
      "s_cbranch_vccz GOT_%=\n"
      "s_sub_u32 s63, s63, 1\n"
      "s_cbranch_scc0 POLL_%=\n"
      "GOT_%=:\n"
      "buffer_load_dwordx4 v[192:195], v137, %[srdx], s69 offen sc1\n"
      "buffer_load_dwordx4 v[196:199], v137, %[srdx], s69 offen offset:256 sc1\n"
      "buffer_load_dwordx4 v[96:99], v248, %[srdx], s76 offen offset:0 sc1\n"
      "buffer_load_dwordx4 v[104:107], v248, %[srdx], s76 offen offset:2048 sc1\n"
      "buffer_load_dwordx4 v[100:103], v248, %[srdx], s76 offen offset:1024 sc1\n"
      "buffer_load_dwordx4 v[108:111], v248, %[srdx], s76 offen offset:3072 sc1\n"
      "s_waitcnt vmcnt(4)\n"
      "v_sub_f32 v200, v192, v224\n"
      "v_sub_f32 v201, v193, v225\n"
      "v_sub_f32 v202, v194, v226\n"
      "v_sub_f32 v203, v195, v227\n"
      "v_min_f32 v204, 0, -v200\n"
      "v_min_f32 v205, 0, -v201\n"
      "v_min_f32 v206, 0, -v202\n"
      "v_min_f32 v207, 0, -v203\n"
      "v_min_f32 v200, 0, v200\n"
      "v_min_f32 v201, 0, v201\n"
      "v_min_f32 v202, 0, v202\n"
      "v_min_f32 v203, 0, v203\n"
      "v_exp_f32 v200, v200\n"
      "v_exp_f32 v201, v201\n"
      "v_exp_f32 v202, v202\n"
      "v_exp_f32 v203, v203\n"
      "v_exp_f32 v204, v204\n"
      "v_exp_f32 v205, v205\n"
      "v_exp_f32 v206, v206\n"
      "v_exp_f32 v207, v207\n"
      "v_mul_f32 v228, v228, v200\n"
      "v_mul_f32 v229, v229, v201\n"
      "v_mul_f32 v230, v230, v202\n"
      "v_mul_f32 v231, v231, v203\n"
      "v_fmac_f32 v228, v196, v204\n"
      "v_fmac_f32 v229, v197, v205\n"
      "v_fmac_f32 v230, v198, v206\n"
      "v_fmac_f32 v231, v199, v207\n"
      "v_rcp_f32 v228, v228\n"
      "v_rcp_f32 v229, v229\n"
      "v_rcp_f32 v230, v230\n"
      "v_rcp_f32 v231, v231\n"
      "s_waitcnt vmcnt(2)\n"
      "v_lshlrev_b32 v64, 16, v96\n"
      "v_and_b32 v65, 0xffff0000, v96\n"
      "v_lshlrev_b32 v66, 16, v97\n"
      "v_and_b32 v67, 0xffff0000, v97\n"
      "v_lshlrev_b32 v68, 16, v98\n"
      "v_and_b32 v69, 0xffff0000, v98\n"
      "v_lshlrev_b32 v70, 16, v99\n"
      "v_and_b32 v71, 0xffff0000, v99\n"
      "v_lshlrev_b32 v80, 16, v104\n"
      "v_and_b32 v81, 0xffff0000, v104\n"
      "v_lshlrev_b32 v82, 16, v105\n"
      "v_and_b32 v83, 0xffff0000, v105\n"
      "v_lshlrev_b32 v84, 16, v106\n"
      "v_and_b32 v85, 0xffff0000, v106\n"
      "v_lshlrev_b32 v86, 16, v107\n"
      "v_and_b32 v87, 0xffff0000, v107\n"
      "v_pk_mul_f32 v[200:201], v[200:201], v[228:229]\n"
      "v_pk_mul_f32 v[204:205], v[204:205], v[228:229]\n"
      "v_pk_mul_f32 v[202:203], v[202:203], v[230:231]\n"
      "v_pk_mul_f32 v[206:207], v[206:207], v[230:231]\n"
      "v_pk_mul_f32 v[0:1], v[0:1], v[200:201] op_sel:[0,0] op_sel_hi:[1,0]\n"
      "v_pk_mul_f32 v[2:3], v[2:3], v[200:201] op_sel:[0,0] op_sel_hi:[1,0]\n"
      "v_pk_mul_f32 v[16:17], v[16:17], v[200:201] op_sel:[0,0] op_sel_hi:[1,0]\n"
      "v_pk_mul_f32 v[18:19], v[18:19], v[200:201] op_sel:[0,0] op_sel_hi:[1,0]\n"
      "v_pk_fma_f32 v[0:1], v[64:65], v[204:205], v[0:1] op_sel:[0,0,0] op_sel_hi:[1,0,1]\n"
      "v_pk_fma_f32 v[2:3], v[66:67], v[204:205], v[2:3] op_sel:[0,0,0] op_sel_hi:[1,0,1]\n"
      "v_pk_fma_f32 v[16:17], v[80:81], v[204:205], v[16:17] op_sel:[0,0,0] op_sel_hi:[1,0,1]\n"
      "v_pk_fma_f32 v[18:19], v[82:83], v[204:205], v[18:19] op_sel:[0,0,0] op_sel_hi:[1,0,1]\n"
      "v_cvt_pk_bf16_f32 v176, v0, v1\n"
      "v_cvt_pk_bf16_f32 v177, v2, v3\n"
      "v_cvt_pk_bf16_f32 v178, v16, v17\n"
      "v_cvt_pk_bf16_f32 v179, v18, v19\n"
      "v_add_u32 v240, 0, v136\n"
      "v_permlane16_swap_b32 v176, v178\n"
      "v_permlane16_swap_b32 v177, v179\n"
      "global_store_dwordx4 v240, v[176:179], %[outp] sc1\n"
      "v_pk_mul_f32 v[4:5], v[4:5], v[200:201] op_sel:[0,1] op_sel_hi:[1,1]\n"
      "v_pk_mul_f32 v[6:7], v[6:7], v[200:201] op_sel:[0,1] op_sel_hi:[1,1]\n"
      "v_pk_mul_f32 v[20:21], v[20:21], v[200:201] op_sel:[0,1] op_sel_hi:[1,1]\n"
      "v_pk_mul_f32 v[22:23], v[22:23], v[200:201] op_sel:[0,1] op_sel_hi:[1,1]\n"
      "v_pk_fma_f32 v[4:5], v[68:69], v[204:205], v[4:5] op_sel:[0,1,0] op_sel_hi:[1,1,1]\n"
      "v_pk_fma_f32 v[6:7], v[70:71], v[204:205], v[6:7] op_sel:[0,1,0] op_sel_hi:[1,1,1]\n"
      "v_pk_fma_f32 v[20:21], v[84:85], v[204:205], v[20:21] op_sel:[0,1,0] op_sel_hi:[1,1,1]\n"
      "v_pk_fma_f32 v[22:23], v[86:87], v[204:205], v[22:23] op_sel:[0,1,0] op_sel_hi:[1,1,1]\n"
      "v_cvt_pk_bf16_f32 v180, v4, v5\n"
      "v_cvt_pk_bf16_f32 v181, v6, v7\n"
      "v_cvt_pk_bf16_f32 v182, v20, v21\n"
      "v_cvt_pk_bf16_f32 v183, v22, v23\n"
      "v_add_u32 v241, 4096, v136\n"
      "v_permlane16_swap_b32 v180, v182\n"
      "v_permlane16_swap_b32 v181, v183\n"
      "global_store_dwordx4 v241, v[180:183], %[outp] sc1\n"
      "s_waitcnt vmcnt(2)\n"
      "v_lshlrev_b32 v72, 16, v100\n"
      "v_and_b32 v73, 0xffff0000, v100\n"
      "v_lshlrev_b32 v74, 16, v101\n"
      "v_and_b32 v75, 0xffff0000, v101\n"
      "v_lshlrev_b32 v76, 16, v102\n"
      "v_and_b32 v77, 0xffff0000, v102\n"
      "v_lshlrev_b32 v78, 16, v103\n"
      "v_and_b32 v79, 0xffff0000, v103\n"
      "v_lshlrev_b32 v88, 16, v108\n"
      "v_and_b32 v89, 0xffff0000, v108\n"
      "v_lshlrev_b32 v90, 16, v109\n"
      "v_and_b32 v91, 0xffff0000, v109\n"
      "v_lshlrev_b32 v92, 16, v110\n"
      "v_and_b32 v93, 0xffff0000, v110\n"
      "v_lshlrev_b32 v94, 16, v111\n"
      "v_and_b32 v95, 0xffff0000, v111\n"
      "v_pk_mul_f32 v[8:9], v[8:9], v[202:203] op_sel:[0,0] op_sel_hi:[1,0]\n"
      "v_pk_mul_f32 v[10:11], v[10:11], v[202:203] op_sel:[0,0] op_sel_hi:[1,0]\n"
      "v_pk_mul_f32 v[24:25], v[24:25], v[202:203] op_sel:[0,0] op_sel_hi:[1,0]\n"
      "v_pk_mul_f32 v[26:27], v[26:27], v[202:203] op_sel:[0,0] op_sel_hi:[1,0]\n"
      "v_pk_fma_f32 v[8:9], v[72:73], v[206:207], v[8:9] op_sel:[0,0,0] op_sel_hi:[1,0,1]\n"
      "v_pk_fma_f32 v[10:11], v[74:75], v[206:207], v[10:11] op_sel:[0,0,0] op_sel_hi:[1,0,1]\n"
      "v_pk_fma_f32 v[24:25], v[88:89], v[206:207], v[24:25] op_sel:[0,0,0] op_sel_hi:[1,0,1]\n"
      "v_pk_fma_f32 v[26:27], v[90:91], v[206:207], v[26:27] op_sel:[0,0,0] op_sel_hi:[1,0,1]\n"
      "v_cvt_pk_bf16_f32 v184, v8, v9\n"
      "v_cvt_pk_bf16_f32 v185, v10, v11\n"
      "v_cvt_pk_bf16_f32 v186, v24, v25\n"
      "v_cvt_pk_bf16_f32 v187, v26, v27\n"
      "v_add_u32 v240, 8192, v136\n"
      "v_permlane16_swap_b32 v184, v186\n"
      "v_permlane16_swap_b32 v185, v187\n"
      "global_store_dwordx4 v240, v[184:187], %[outp] sc1\n"
      "v_pk_mul_f32 v[12:13], v[12:13], v[202:203] op_sel:[0,1] op_sel_hi:[1,1]\n"
      "v_pk_mul_f32 v[14:15], v[14:15], v[202:203] op_sel:[0,1] op_sel_hi:[1,1]\n"
      "v_pk_mul_f32 v[28:29], v[28:29], v[202:203] op_sel:[0,1] op_sel_hi:[1,1]\n"
      "v_pk_mul_f32 v[30:31], v[30:31], v[202:203] op_sel:[0,1] op_sel_hi:[1,1]\n"
      "v_pk_fma_f32 v[12:13], v[76:77], v[206:207], v[12:13] op_sel:[0,1,0] op_sel_hi:[1,1,1]\n"
      "v_pk_fma_f32 v[14:15], v[78:79], v[206:207], v[14:15] op_sel:[0,1,0] op_sel_hi:[1,1,1]\n"
      "v_pk_fma_f32 v[28:29], v[92:93], v[206:207], v[28:29] op_sel:[0,1,0] op_sel_hi:[1,1,1]\n"
      "v_pk_fma_f32 v[30:31], v[94:95], v[206:207], v[30:31] op_sel:[0,1,0] op_sel_hi:[1,1,1]\n"
      "v_cvt_pk_bf16_f32 v188, v12, v13\n"
      "v_cvt_pk_bf16_f32 v189, v14, v15\n"
      "v_cvt_pk_bf16_f32 v190, v28, v29\n"
      "v_cvt_pk_bf16_f32 v191, v30, v31\n"
      "v_add_u32 v241, 12288, v136\n"
      "v_permlane16_swap_b32 v188, v190\n"
      "v_permlane16_swap_b32 v189, v191\n"
      "global_store_dwordx4 v241, v[188:191], %[outp] sc1\n"
      "s_mov_b64 exec, 1\n"
      "global_store_dwordx2 v138, v[128:129], %[line] sc1\n"
      "s_mov_b64 exec, -1\n"
      ".subsection 1\n"
      "GW0_%=:\n"
      "s_call_b64 s[84:85], WAIT_%=\n"
      "s_branch GX0_%=\n"
      "GW1_%=:\n"
      "s_call_b64 s[84:85], WAIT_%=\n"
      "s_branch GX1_%=\n"
      "GW2_%=:\n"
      "s_call_b64 s[84:85], WAIT_%=\n"
      "s_branch GX2_%=\n"
      "GW3_%=:\n"
      "s_call_b64 s[84:85], WAIT_%=\n"
      "s_branch GX3_%=\n"
      "GW4_%=:\n"
      "s_call_b64 s[84:85], WAIT_%=\n"
      "s_branch GX4_%=\n"
      "GW5_%=:\n"
      "s_call_b64 s[84:85], WAIT_%=\n"
      "s_branch GX5_%=\n"
      "WAIT_%=:\n"
      "s_cmp_eq_u32 s91, 0\n"
      "s_cbranch_scc1 SPIN_%=\n"
      "s_cmp_le_u32 s87, 12\n"
      "s_cselect_b32 s70, 300, s70\n"
      "s_cmp_gt_u32 s87, 2\n"
      "s_cbranch_scc1 H3_%=\n"
      "s_cmp_lg_u32 s87, 1\n"
      "s_cbranch_scc1 H2_%=\n"
      "buffer_load_dword v250, v251, %[srdr], 0 offen sc1\n"
      "s_branch HR_%=\n"
      "H2_%=:\n"
      "s_cmp_lg_u32 s87, 2\n"
      "s_cbranch_scc1 HR_%=\n"
      "s_waitcnt vmcnt(8)\n"
      "v_subrev_u32 v250, s71, v250\n"
      "v_min_u32 v250, 0x258, v250\n"
      "s_mov_b32 s66, 0\n"
      "v_readlane_b32 s67, v250, 0\n"
      "s_nop 0\n"
      "s_add_u32 s66, s66, s67\n"
      "v_readlane_b32 s67, v250, 1\n"
      "s_nop 0\n"
      "s_add_u32 s66, s66, s67\n"
      "v_readlane_b32 s67, v250, 2\n"
      "s_nop 0\n"
      "s_add_u32 s66, s66, s67\n"
      "v_readlane_b32 s67, v250, 3\n"
      "s_nop 0\n"
      "s_add_u32 s66, s66, s67\n"
      "v_readlane_b32 s67, v250, 4\n"
      "s_nop 0\n"
      "s_add_u32 s66, s66, s67\n"
      "v_readlane_b32 s67, v250, 5\n"
      "s_nop 0\n"
      "s_add_u32 s66, s66, s67\n"
      "v_readlane_b32 s67, v250, 6\n"
      "s_nop 0\n"
      "s_add_u32 s66, s66, s67\n"
      "v_readlane_b32 s67, v250, 7\n"
      "s_nop 0\n"
      "s_add_u32 s66, s66, s67\n"
      "s_lshr_b32 s66, s66, 3\n"
      "s_add_u32 s66, s66, s71\n"
      "s_add_u32 s66, s66, %[cph]\n"
      "s_sub_u32 s67, s65, s66\n"
      "s_cmp_lt_i32 s67, 0\n"
      "s_cselect_b32 s88, s66, s65\n"
      "s_branch H5_%=\n"
      "H3_%=:\n"
      "s_bitcmp1_b32 0x8880, s87\n"
      "s_cbranch_scc0 H4_%=\n"
      "v_mbcnt_lo_u32_b32 v251, -1, 0\n"
      "v_mbcnt_hi_u32_b32 v251, -1, v251\n"
      "v_and_b32 v251, 31, v251\n"
      "v_lshlrev_b32 v251, 6, v251\n"
      "v_add_u32 v251, %[vrd], v251\n"
      "buffer_load_dword v250, v251, %[srdr], 0 offen sc1\n"
      "s_branch HR_%=\n"
      "H4_%=:\n"
      "s_bitcmp1_b32 0x11100, s87\n"
      "s_cbranch_scc0 H5_%=\n"
      "s_waitcnt vmcnt(8)\n"
      "v_sub_u32 v250, s68, v250\n"
      "v_cmp_gt_u32 vcc, 1600, v250\n"
      "s_nop 3\n"
      "s_bcnt1_i32_b32 s67, vcc_lo\n"
      "s_cmp_lt_u32 s67, 13\n"
      "s_cbranch_scc1 H5_%=\n"
      "s_mov_b32 s87, 30\n"
      "s_add_u32 s66, s66, 70\n"
      "s_mov_b32 s86, 113\n"
      "s_mov_b64 s[80:81], 0x2a9f4\n"
      "s_sub_u32 s67, s65, s66\n"
      "s_cmp_lt_i32 s67, 0\n"
      "s_cselect_b32 s88, s66, s65\n"
      "s_branch HR_%=\n"
      "H5_%=:\n"
      "s_cmp_lt_u32 %[rep], 2048\n"
      "s_cbranch_scc0 HR_%=\n"
      "s_bitcmp1_b32 0x3330, s87\n"
      "s_cbranch_scc0 HR_%=\n"
      "s_memrealtime s[68:69]\n"
      "s_waitcnt lgkmcnt(0)\n"
      "s_sub_u32 s76, s68, s82\n"
      "s_add_u32 s76, s76, 12\n"
      "s_max_i32 s76, s76, 102\n"
      "s_waitcnt vmcnt(4)\n"
      "s_memrealtime s[68:69]\n"
      "s_waitcnt lgkmcnt(0)\n"
      "s_sub_u32 s67, s68, s82\n"
      "s_sub_u32 s67, s67, s76\n"
      "s_max_i32 s67, s67, 0\n"
      "s_min_i32 s67, s67, 12\n"
      "s_add_u32 s89, s89, s67\n"
      "s_bitcmp1_b32 0x2220, s87\n"
      "s_cbranch_scc0 HR_%=\n"
      "s_cmp_lt_u32 s89, 14\n"
      "s_mov_b32 s89, 0\n"
      "s_cbranch_scc1 HR_%=\n"
      "v_mbcnt_lo_u32_b32 v251, -1, 0\n"
      "v_mbcnt_hi_u32_b32 v251, -1, v251\n"
      "v_lshlrev_b32 v250, 7, v251\n"
      "v_and_b32 v250, 0x1800, v250\n"
      "v_and_b32 v251, 15, v251\n"
      "v_lshl_or_b32 v251, v251, 2, v250\n"
      "v_mov_b32 v250, s68\n"
      "s_add_u32 s67, %[rep], 8192\n"
      "buffer_store_dword v250, v251, %[srdr], s67 offen sc1\n"
      "HR_%=:\n"
      "s_add_u32 s87, s87, 1\n"
      "s_cmp_gt_u32 s87, 16\n"
      "s_cselect_b32 s91, 0, s91\n"
      "SPIN_%=:\n"
      "s_sub_u32 s67, s68, s88\n"
      "s_cmp_ge_i32 s67, 0\n"
      "s_cbranch_scc1 GO_%=\n"
      "s_sleep 1\n"
      "s_memrealtime s[68:69]\n"
      "s_waitcnt lgkmcnt(0)\n"
      "s_branch SPIN_%=\n"
      "GO_%=:\n"
      "s_setpc_b64 s[84:85]\n"
      "FB1_%=:\n"
      "s_cmp_eq_u32 s72, 0\n"
      "s_cbranch_scc1 FULL1_%=\n"
      "s_mov_b32 s72, 0\n"
      "s_nop 7\n"
      "v_max3_f32 v236, v128, v129, v130\n"
      "v_max3_f32 v237, v136, v137, v138\n"
      "v_max3_f32 v238, v144, v145, v146\n"
      "v_max3_f32 v239, v152, v153, v154\n"
      "v_max3_f32 v236, v236, v131, v132\n"
      "v_max3_f32 v237, v237, v139, v140\n"
      "v_max3_f32 v238, v238, v147, v148\n"
      "v_max3_f32 v239, v239, v155, v156\n"
      "v_max3_f32 v236, v236, v133, v134\n"
      "v_max3_f32 v237, v237, v141, v142\n"
      "v_max3_f32 v238, v238, v149, v150\n"
      "v_max3_f32 v239, v239, v157, v158\n"
      "v_max_f32 v236, v236, v135\n"
      "v_max_f32 v237, v237, v143\n"
      "v_max_f32 v238, v238, v151\n"
      "v_max_f32 v239, v239, v159\n"
      "v_mov_b32 v192, v236\n"
      "v_mov_b32 v193, v237\n"
      "v_mov_b32 v194, v238\n"
      "v_mov_b32 v195, v239\n"
      "v_permlane16_swap_b32 v236, v192\n"
      "v_permlane16_swap_b32 v237, v193\n"
      "v_permlane16_swap_b32 v238, v194\n"
      "v_permlane16_swap_b32 v239, v195\n"
      "v_max_f32 v236, v236, v192\n"
      "v_max_f32 v237, v237, v193\n"
      "v_max_f32 v238, v238, v194\n"
      "v_max_f32 v239, v239, v195\n"
      "v_mov_b32 v192, v236\n"
      "v_mov_b32 v193, v237\n"
      "v_mov_b32 v194, v238\n"
      "v_mov_b32 v195, v239\n"
      "v_permlane32_swap_b32 v236, v192\n"
      "v_permlane32_swap_b32 v237, v193\n"
      "v_permlane32_swap_b32 v238, v194\n"
      "v_permlane32_swap_b32 v239, v195\n"
      "v_max_f32 v236, v236, v192\n"
      "v_max_f32 v237, v237, v193\n"
      "v_max_f32 v238, v238, v194\n"
      "v_max_f32 v239, v239, v195\n"
      "v_mul_f32 v224, 0xbe0293ee, v236\n"
      "v_mul_f32 v225, 0xbe0293ee, v237\n"
      "v_mul_f32 v226, 0xbe0293ee, v238\n"
      "v_mul_f32 v227, 0xbe0293ee, v239\n"
      "v_mov_b64 v[208:209], 0\n"
      "v_mov_b64 v[210:211], 0\n"
      "v_mov_b64 v[212:213], 0\n"
      "v_mov_b64 v[214:215], 0\n"
      "v_mov_b64 v[216:217], 0\n"
      "v_mov_b64 v[218:219], 0\n"
      "v_mov_b64 v[220:221], 0\n"
      "v_mov_b64 v[222:223], 0\n"
      "v_mov_b64 v[232:233], 0\n"
      "v_mov_b64 v[234:235], 0\n"
      "s_nop 1\n"
      "s_branch RET1_0_%=\n"
      "FB0_%=:\n"
      "s_nop 7\n"
      "s_nop 7\n"
      "v_max3_f32 v236, v128, v129, v130\n"
      "v_max3_f32 v237, v136, v137, v138\n"
      "v_max3_f32 v238, v144, v145, v146\n"
      "v_max3_f32 v239, v152, v153, v154\n"
      "v_max3_f32 v236, v236, v131, v132\n"
      "v_max3_f32 v237, v237, v139, v140\n"
      "v_max3_f32 v238, v238, v147, v148\n"
      "v_max3_f32 v239, v239, v155, v156\n"
      "v_max3_f32 v236, v236, v133, v134\n"
      "v_max3_f32 v237, v237, v141, v142\n"
      "v_max3_f32 v238, v238, v149, v150\n"
      "v_max3_f32 v239, v239, v157, v158\n"
      "v_max_f32 v236, v236, v135\n"
      "v_max_f32 v237, v237, v143\n"
      "v_max_f32 v238, v238, v151\n"
      "v_max_f32 v239, v239, v159\n"
      "v_mov_b32 v192, v236\n"
      "v_mov_b32 v193, v237\n"
      "v_mov_b32 v194, v238\n"
      "v_mov_b32 v195, v239\n"
      "v_permlane16_swap_b32 v236, v192\n"
      "v_permlane16_swap_b32 v237, v193\n"
      "v_permlane16_swap_b32 v238, v194\n"
      "v_permlane16_swap_b32 v239, v195\n"
      "v_max_f32 v236, v236, v192\n"
      "v_max_f32 v237, v237, v193\n"
      "v_max_f32 v238, v238, v194\n"
      "v_max_f32 v239, v239, v195\n"
      "v_mov_b32 v192, v236\n"
      "v_mov_b32 v193, v237\n"
      "v_mov_b32 v194, v238\n"
      "v_mov_b32 v195, v239\n"
      "v_permlane32_swap_b32 v236, v192\n"
      "v_permlane32_swap_b32 v237, v193\n"
      "v_permlane32_swap_b32 v238, v194\n"
      "v_permlane32_swap_b32 v239, v195\n"
      "v_max_f32 v236, v236, v192\n"
      "v_max_f32 v237, v237, v193\n"
      "v_max_f32 v238, v238, v194\n"
      "v_max_f32 v239, v239, v195\n"
      "v_mul_f32 v236, 0xbe0293ee, v236\n"
      "v_mul_f32 v237, 0xbe0293ee, v237\n"
      "v_mul_f32 v238, 0xbe0293ee, v238\n"
      "v_mul_f32 v239, 0xbe0293ee, v239\n"
      "v_min_f32 v236, v236, v224\n"
      "v_min_f32 v237, v237, v225\n"
      "v_min_f32 v238, v238, v226\n"
      "v_min_f32 v239, v239, v227\n"
      "v_sub_f32 v232, v236, v224\n"
      "v_sub_f32 v233, v237, v225\n"
      "v_sub_f32 v234, v238, v226\n"
      "v_sub_f32 v235, v239, v227\n"
      "v_exp_f32 v232, v232\n"
      "v_exp_f32 v233, v233\n"
      "v_exp_f32 v234, v234\n"
      "v_exp_f32 v235, v235\n"
      "v_mov_b32 v224, v236\n"
      "v_mov_b32 v225, v237\n"
      "v_mov_b32 v226, v238\n"
      "v_mov_b32 v227, v239\n"
      "v_mul_f32 v0, v0, v232\n"
      "v_mul_f32 v1, v1, v232\n"
      "v_mul_f32 v2, v2, v232\n"
      "v_mul_f32 v3, v3, v232\n"
      "v_mul_f32 v4, v4, v233\n"
      "v_mul_f32 v5, v5, v233\n"
      "v_mul_f32 v6, v6, v233\n"
      "v_mul_f32 v7, v7, v233\n"
      "v_mul_f32 v8, v8, v234\n"
      "v_mul_f32 v9, v9, v234\n"
      "v_mul_f32 v10, v10, v234\n"
      "v_mul_f32 v11, v11, v234\n"
      "v_mul_f32 v12, v12, v235\n"
      "v_mul_f32 v13, v13, v235\n"
      "v_mul_f32 v14, v14, v235\n"
      "v_mul_f32 v15, v15, v235\n"
      "v_mul_f32 v16, v16, v232\n"
      "v_mul_f32 v17, v17, v232\n"
      "v_mul_f32 v18, v18, v232\n"
      "v_mul_f32 v19, v19, v232\n"
      "v_mul_f32 v20, v20, v233\n"
      "v_mul_f32 v21, v21, v233\n"
      "v_mul_f32 v22, v22, v233\n"
      "v_mul_f32 v23, v23, v233\n"
      "v_mul_f32 v24, v24, v234\n"
      "v_mul_f32 v25, v25, v234\n"
      "v_mul_f32 v26, v26, v234\n"
      "v_mul_f32 v27, v27, v234\n"
      "v_mul_f32 v28, v28, v235\n"
      "v_mul_f32 v29, v29, v235\n"
      "v_mul_f32 v30, v30, v235\n"
      "v_mul_f32 v31, v31, v235\n"
      "v_mul_f32 v32, v32, v232\n"
      "v_mul_f32 v33, v33, v232\n"
      "v_mul_f32 v34, v34, v232\n"
      "v_mul_f32 v35, v35, v232\n"
      "v_mul_f32 v36, v36, v233\n"
      "v_mul_f32 v37, v37, v233\n"
      "v_mul_f32 v38, v38, v233\n"
      "v_mul_f32 v39, v39, v233\n"
      "v_mul_f32 v40, v40, v234\n"
      "v_mul_f32 v41, v41, v234\n"
      "v_mul_f32 v42, v42, v234\n"
      "v_mul_f32 v43, v43, v234\n"
      "v_mul_f32 v44, v44, v235\n"
      "v_mul_f32 v45, v45, v235\n"
      "v_mul_f32 v46, v46, v235\n"
      "v_mul_f32 v47, v47, v235\n"
      "v_mul_f32 v48, v48, v232\n"
      "v_mul_f32 v49, v49, v232\n"
      "v_mul_f32 v50, v50, v232\n"
      "v_mul_f32 v51, v51, v232\n"
      "v_mul_f32 v52, v52, v233\n"
      "v_mul_f32 v53, v53, v233\n"
      "v_mul_f32 v54, v54, v233\n"
      "v_mul_f32 v55, v55, v233\n"
      "v_mul_f32 v56, v56, v234\n"
      "v_mul_f32 v57, v57, v234\n"
      "v_mul_f32 v58, v58, v234\n"
      "v_mul_f32 v59, v59, v234\n"
      "v_mul_f32 v60, v60, v235\n"
      "v_mul_f32 v61, v61, v235\n"
      "v_mul_f32 v62, v62, v235\n"
      "v_mul_f32 v63, v63, v235\n"
      "v_mul_f32 v64, v64, v232\n"
      "v_mul_f32 v65, v65, v232\n"
      "v_mul_f32 v66, v66, v232\n"
      "v_mul_f32 v67, v67, v232\n"
      "v_mul_f32 v68, v68, v233\n"
      "v_mul_f32 v69, v69, v233\n"
      "v_mul_f32 v70, v70, v233\n"
      "v_mul_f32 v71, v71, v233\n"
      "v_mul_f32 v72, v72, v234\n"
      "v_mul_f32 v73, v73, v234\n"
      "v_mul_f32 v74, v74, v234\n"
      "v_mul_f32 v75, v75, v234\n"
      "v_mul_f32 v76, v76, v235\n"
      "v_mul_f32 v77, v77, v235\n"
      "v_mul_f32 v78, v78, v235\n"
      "v_mul_f32 v79, v79, v235\n"
      "v_mul_f32 v80, v80, v232\n"
      "v_mul_f32 v81, v81, v232\n"
      "v_mul_f32 v82, v82, v232\n"
      "v_mul_f32 v83, v83, v232\n"
      "v_mul_f32 v84, v84, v233\n"
      "v_mul_f32 v85, v85, v233\n"
      "v_mul_f32 v86, v86, v233\n"
      "v_mul_f32 v87, v87, v233\n"
      "v_mul_f32 v88, v88, v234\n"
      "v_mul_f32 v89, v89, v234\n"
      "v_mul_f32 v90, v90, v234\n"
      "v_mul_f32 v91, v91, v234\n"
      "v_mul_f32 v92, v92, v235\n"
      "v_mul_f32 v93, v93, v235\n"
      "v_mul_f32 v94, v94, v235\n"
      "v_mul_f32 v95, v95, v235\n"
      "v_mul_f32 v96, v96, v232\n"
      "v_mul_f32 v97, v97, v232\n"
      "v_mul_f32 v98, v98, v232\n"
      "v_mul_f32 v99, v99, v232\n"
      "v_mul_f32 v100, v100, v233\n"
      "v_mul_f32 v101, v101, v233\n"
      "v_mul_f32 v102, v102, v233\n"
      "v_mul_f32 v103, v103, v233\n"
      "v_mul_f32 v104, v104, v234\n"
      "v_mul_f32 v105, v105, v234\n"
      "v_mul_f32 v106, v106, v234\n"
      "v_mul_f32 v107, v107, v234\n"
      "v_mul_f32 v108, v108, v235\n"
      "v_mul_f32 v109, v109, v235\n"
      "v_mul_f32 v110, v110, v235\n"
      "v_mul_f32 v111, v111, v235\n"
      "v_mul_f32 v112, v112, v232\n"
      "v_mul_f32 v113, v113, v232\n"
      "v_mul_f32 v114, v114, v232\n"
      "v_mul_f32 v115, v115, v232\n"
      "v_mul_f32 v116, v116, v233\n"
      "v_mul_f32 v117, v117, v233\n"
      "v_mul_f32 v118, v118, v233\n"
      "v_mul_f32 v119, v119, v233\n"
      "v_mul_f32 v120, v120, v234\n"
      "v_mul_f32 v121, v121, v234\n"
      "v_mul_f32 v122, v122, v234\n"
      "v_mul_f32 v123, v123, v234\n"
      "v_mul_f32 v124, v124, v235\n"
      "v_mul_f32 v125, v125, v235\n"
      "v_mul_f32 v126, v126, v235\n"
      "v_mul_f32 v127, v127, v235\n"
      "v_mul_f32 v228, v228, v232\n"
      "v_mul_f32 v229, v229, v233\n"
      "v_mul_f32 v230, v230, v234\n"
      "v_mul_f32 v231, v231, v235\n"
      "v_fmamk_f32 v240, v128, 0x3e0293ee, v224\n"
      "v_exp_f32 v236, v240\n"
      "v_fmamk_f32 v241, v129, 0x3e0293ee, v224\n"
      "v_exp_f32 v237, v241\n"
      "v_fmamk_f32 v240, v130, 0x3e0293ee, v224\n"
      "v_exp_f32 v238, v240\n"
      "v_fmamk_f32 v241, v131, 0x3e0293ee, v224\n"
      "v_add_f32 v232, v236, v237\n"
      "v_cvt_pk_bf16_f32 v192, v236, v237\n"
      "v_exp_f32 v239, v241\n"
      "v_fmamk_f32 v240, v136, 0x3e0293ee, v225\n"
      "v_add_f32 v232, v232, v238\n"
      "v_exp_f32 v236, v240\n"
      "v_fmamk_f32 v241, v137, 0x3e0293ee, v225\n"
      "v_add_f32 v232, v232, v239\n"
      "v_cvt_pk_bf16_f32 v193, v238, v239\n"
      "v_exp_f32 v237, v241\n"
      "v_fmamk_f32 v240, v138, 0x3e0293ee, v225\n"
      "v_exp_f32 v238, v240\n"
      "v_fmamk_f32 v241, v139, 0x3e0293ee, v225\n"
      "v_add_f32 v233, v236, v237\n"
      "v_cvt_pk_bf16_f32 v196, v236, v237\n"
      "v_exp_f32 v239, v241\n"
      "v_fmamk_f32 v240, v144, 0x3e0293ee, v226\n"
      "v_add_f32 v233, v233, v238\n"
      "v_exp_f32 v236, v240\n"
      "v_fmamk_f32 v241, v145, 0x3e0293ee, v226\n"
      "v_add_f32 v233, v233, v239\n"
      "v_cvt_pk_bf16_f32 v197, v238, v239\n"
      "v_exp_f32 v237, v241\n"
      "v_fmamk_f32 v240, v146, 0x3e0293ee, v226\n"
      "v_exp_f32 v238, v240\n"
      "v_fmamk_f32 v241, v147, 0x3e0293ee, v226\n"
      "v_add_f32 v234, v236, v237\n"
      "v_cvt_pk_bf16_f32 v200, v236, v237\n"
      "v_exp_f32 v239, v241\n"
      "v_fmamk_f32 v240, v152, 0x3e0293ee, v227\n"
      "v_add_f32 v234, v234, v238\n"
      "v_exp_f32 v236, v240\n"
      "v_fmamk_f32 v241, v153, 0x3e0293ee, v227\n"
      "v_add_f32 v234, v234, v239\n"
      "v_cvt_pk_bf16_f32 v201, v238, v239\n"
      "v_exp_f32 v237, v241\n"
      "v_fmamk_f32 v240, v154, 0x3e0293ee, v227\n"
      "v_exp_f32 v238, v240\n"
      "v_fmamk_f32 v241, v155, 0x3e0293ee, v227\n"
      "v_add_f32 v235, v236, v237\n"
      "v_cvt_pk_bf16_f32 v204, v236, v237\n"
      "v_exp_f32 v239, v241\n"
      "v_fmamk_f32 v240, v132, 0x3e0293ee, v224\n"
      "v_add_f32 v235, v235, v238\n"
      "v_exp_f32 v236, v240\n"
      "v_fmamk_f32 v241, v133, 0x3e0293ee, v224\n"
      "v_add_f32 v235, v235, v239\n"
      "v_cvt_pk_bf16_f32 v205, v238, v239\n"
      "v_exp_f32 v237, v241\n"
      "v_fmamk_f32 v240, v134, 0x3e0293ee, v224\n"
      "v_add_f32 v232, v232, v236\n"
      "v_exp_f32 v238, v240\n"
      "v_fmamk_f32 v241, v135, 0x3e0293ee, v224\n"
      "v_add_f32 v232, v232, v237\n"
      "v_cvt_pk_bf16_f32 v194, v236, v237\n"
      "v_exp_f32 v239, v241\n"
      "v_fmamk_f32 v240, v140, 0x3e0293ee, v225\n"
      "v_add_f32 v232, v232, v238\n"
      "v_exp_f32 v236, v240\n"
      "v_fmamk_f32 v241, v141, 0x3e0293ee, v225\n"
      "v_add_f32 v232, v232, v239\n"
      "v_cvt_pk_bf16_f32 v195, v238, v239\n"
      "v_exp_f32 v237, v241\n"
      "v_fmamk_f32 v240, v142, 0x3e0293ee, v225\n"
      "v_add_f32 v233, v233, v236\n"
      "v_exp_f32 v238, v240\n"
      "v_fmamk_f32 v241, v143, 0x3e0293ee, v225\n"
      "v_add_f32 v233, v233, v237\n"
      "v_cvt_pk_bf16_f32 v198, v236, v237\n"
      "v_exp_f32 v239, v241\n"
      "v_fmamk_f32 v240, v148, 0x3e0293ee, v226\n"
      "v_add_f32 v233, v233, v238\n"
      "v_exp_f32 v236, v240\n"
      "v_fmamk_f32 v241, v149, 0x3e0293ee, v226\n"
      "v_add_f32 v233, v233, v239\n"
      "v_cvt_pk_bf16_f32 v199, v238, v239\n"
      "v_exp_f32 v237, v241\n"
      "v_fmamk_f32 v240, v150, 0x3e0293ee, v226\n"
      "v_add_f32 v234, v234, v236\n"
      "v_exp_f32 v238, v240\n"
      "v_fmamk_f32 v241, v151, 0x3e0293ee, v226\n"
      "v_add_f32 v234, v234, v237\n"
      "v_cvt_pk_bf16_f32 v202, v236, v237\n"
      "v_exp_f32 v239, v241\n"
      "v_fmamk_f32 v240, v156, 0x3e0293ee, v227\n"
      "v_add_f32 v234, v234, v238\n"
      "v_exp_f32 v236, v240\n"
      "v_fmamk_f32 v241, v157, 0x3e0293ee, v227\n"
      "v_add_f32 v234, v234, v239\n"
      "v_cvt_pk_bf16_f32 v203, v238, v239\n"
      "v_exp_f32 v237, v241\n"
      "v_fmamk_f32 v240, v158, 0x3e0293ee, v227\n"
      "v_add_f32 v235, v235, v236\n"
      "v_exp_f32 v238, v240\n"
      "v_fmamk_f32 v241, v159, 0x3e0293ee, v227\n"
      "v_add_f32 v235, v235, v237\n"
      "v_cvt_pk_bf16_f32 v206, v236, v237\n"
      "v_exp_f32 v239, v241\n"
      "v_add_f32 v235, v235, v238\n"
      "v_add_f32 v235, v235, v239\n"
      "v_cvt_pk_bf16_f32 v207, v238, v239\n"
      "s_nop 1\n"
      "s_cmp_eq_u32 s64, 0\n"
      "s_cbranch_scc1 RET0_0_%=\n"
      "s_cmp_eq_u32 s64, 1\n"
      "s_cbranch_scc1 RET0_1_%=\n"
      "s_cmp_eq_u32 s64, 2\n"
      "s_cbranch_scc1 RET0_2_%=\n"
      "s_branch RET0_3_%=\n"
      "FULL1_%=:\n"
      "s_nop 7\n"
      "s_nop 7\n"
      "v_max3_f32 v236, v160, v161, v162\n"
      "v_max3_f32 v237, v168, v169, v170\n"
      "v_max3_f32 v238, v176, v177, v178\n"
      "v_max3_f32 v239, v184, v185, v186\n"
      "v_max3_f32 v236, v236, v163, v164\n"
      "v_max3_f32 v237, v237, v171, v172\n"
      "v_max3_f32 v238, v238, v179, v180\n"
      "v_max3_f32 v239, v239, v187, v188\n"
      "v_max3_f32 v236, v236, v165, v166\n"
      "v_max3_f32 v237, v237, v173, v174\n"
      "v_max3_f32 v238, v238, v181, v182\n"
      "v_max3_f32 v239, v239, v189, v190\n"
      "v_max_f32 v236, v236, v167\n"
      "v_max_f32 v237, v237, v175\n"
      "v_max_f32 v238, v238, v183\n"
      "v_max_f32 v239, v239, v191\n"
      "v_mov_b32 v208, v236\n"
      "v_mov_b32 v209, v237\n"
      "v_mov_b32 v210, v238\n"
      "v_mov_b32 v211, v239\n"
      "v_permlane16_swap_b32 v236, v208\n"
      "v_permlane16_swap_b32 v237, v209\n"
      "v_permlane16_swap_b32 v238, v210\n"
      "v_permlane16_swap_b32 v239, v211\n"
      "v_max_f32 v236, v236, v208\n"
      "v_max_f32 v237, v237, v209\n"
      "v_max_f32 v238, v238, v210\n"
      "v_max_f32 v239, v239, v211\n"
      "v_mov_b32 v208, v236\n"
      "v_mov_b32 v209, v237\n"
      "v_mov_b32 v210, v238\n"
      "v_mov_b32 v211, v239\n"
      "v_permlane32_swap_b32 v236, v208\n"
      "v_permlane32_swap_b32 v237, v209\n"
      "v_permlane32_swap_b32 v238, v210\n"
      "v_permlane32_swap_b32 v239, v211\n"
      "v_max_f32 v236, v236, v208\n"
      "v_max_f32 v237, v237, v209\n"
      "v_max_f32 v238, v238, v210\n"
      "v_max_f32 v239, v239, v211\n"
      "v_mul_f32 v236, 0xbe0293ee, v236\n"
      "v_mul_f32 v237, 0xbe0293ee, v237\n"
      "v_mul_f32 v238, 0xbe0293ee, v238\n"
      "v_mul_f32 v239, 0xbe0293ee, v239\n"
      "v_min_f32 v236, v236, v224\n"
      "v_min_f32 v237, v237, v225\n"
      "v_min_f32 v238, v238, v226\n"
      "v_min_f32 v239, v239, v227\n"
      "v_sub_f32 v232, v236, v224\n"
      "v_sub_f32 v233, v237, v225\n"
      "v_sub_f32 v234, v238, v226\n"
      "v_sub_f32 v235, v239, v227\n"
      "v_exp_f32 v232, v232\n"
      "v_exp_f32 v233, v233\n"
      "v_exp_f32 v234, v234\n"
      "v_exp_f32 v235, v235\n"
      "v_mov_b32 v224, v236\n"
      "v_mov_b32 v225, v237\n"
      "v_mov_b32 v226, v238\n"
      "v_mov_b32 v227, v239\n"
      "v_mul_f32 v0, v0, v232\n"
      "v_mul_f32 v1, v1, v232\n"
      "v_mul_f32 v2, v2, v232\n"
      "v_mul_f32 v3, v3, v232\n"
      "v_mul_f32 v4, v4, v233\n"
      "v_mul_f32 v5, v5, v233\n"
      "v_mul_f32 v6, v6, v233\n"
      "v_mul_f32 v7, v7, v233\n"
      "v_mul_f32 v8, v8, v234\n"
      "v_mul_f32 v9, v9, v234\n"
      "v_mul_f32 v10, v10, v234\n"
      "v_mul_f32 v11, v11, v234\n"
      "v_mul_f32 v12, v12, v235\n"
      "v_mul_f32 v13, v13, v235\n"
      "v_mul_f32 v14, v14, v235\n"
      "v_mul_f32 v15, v15, v235\n"
      "v_mul_f32 v16, v16, v232\n"
      "v_mul_f32 v17, v17, v232\n"
      "v_mul_f32 v18, v18, v232\n"
      "v_mul_f32 v19, v19, v232\n"
      "v_mul_f32 v20, v20, v233\n"
      "v_mul_f32 v21, v21, v233\n"
      "v_mul_f32 v22, v22, v233\n"
      "v_mul_f32 v23, v23, v233\n"
      "v_mul_f32 v24, v24, v234\n"
      "v_mul_f32 v25, v25, v234\n"
      "v_mul_f32 v26, v26, v234\n"
      "v_mul_f32 v27, v27, v234\n"
      "v_mul_f32 v28, v28, v235\n"
      "v_mul_f32 v29, v29, v235\n"
      "v_mul_f32 v30, v30, v235\n"
      "v_mul_f32 v31, v31, v235\n"
      "v_mul_f32 v32, v32, v232\n"
      "v_mul_f32 v33, v33, v232\n"
      "v_mul_f32 v34, v34, v232\n"
      "v_mul_f32 v35, v35, v232\n"
      "v_mul_f32 v36, v36, v233\n"
      "v_mul_f32 v37, v37, v233\n"
      "v_mul_f32 v38, v38, v233\n"
      "v_mul_f32 v39, v39, v233\n"
      "v_mul_f32 v40, v40, v234\n"
      "v_mul_f32 v41, v41, v234\n"
      "v_mul_f32 v42, v42, v234\n"
      "v_mul_f32 v43, v43, v234\n"
      "v_mul_f32 v44, v44, v235\n"
      "v_mul_f32 v45, v45, v235\n"
      "v_mul_f32 v46, v46, v235\n"
      "v_mul_f32 v47, v47, v235\n"
      "v_mul_f32 v48, v48, v232\n"
      "v_mul_f32 v49, v49, v232\n"
      "v_mul_f32 v50, v50, v232\n"
      "v_mul_f32 v51, v51, v232\n"
      "v_mul_f32 v52, v52, v233\n"
      "v_mul_f32 v53, v53, v233\n"
      "v_mul_f32 v54, v54, v233\n"
      "v_mul_f32 v55, v55, v233\n"
      "v_mul_f32 v56, v56, v234\n"
      "v_mul_f32 v57, v57, v234\n"
      "v_mul_f32 v58, v58, v234\n"
      "v_mul_f32 v59, v59, v234\n"
      "v_mul_f32 v60, v60, v235\n"
      "v_mul_f32 v61, v61, v235\n"
      "v_mul_f32 v62, v62, v235\n"
      "v_mul_f32 v63, v63, v235\n"
      "v_mul_f32 v64, v64, v232\n"
      "v_mul_f32 v65, v65, v232\n"
      "v_mul_f32 v66, v66, v232\n"
      "v_mul_f32 v67, v67, v232\n"
      "v_mul_f32 v68, v68, v233\n"
      "v_mul_f32 v69, v69, v233\n"
      "v_mul_f32 v70, v70, v233\n"
      "v_mul_f32 v71, v71, v233\n"
      "v_mul_f32 v72, v72, v234\n"
      "v_mul_f32 v73, v73, v234\n"
      "v_mul_f32 v74, v74, v234\n"
      "v_mul_f32 v75, v75, v234\n"
      "v_mul_f32 v76, v76, v235\n"
      "v_mul_f32 v77, v77, v235\n"
      "v_mul_f32 v78, v78, v235\n"
      "v_mul_f32 v79, v79, v235\n"
      "v_mul_f32 v80, v80, v232\n"
      "v_mul_f32 v81, v81, v232\n"
      "v_mul_f32 v82, v82, v232\n"
      "v_mul_f32 v83, v83, v232\n"
      "v_mul_f32 v84, v84, v233\n"
      "v_mul_f32 v85, v85, v233\n"
      "v_mul_f32 v86, v86, v233\n"
      "v_mul_f32 v87, v87, v233\n"
      "v_mul_f32 v88, v88, v234\n"
      "v_mul_f32 v89, v89, v234\n"
      "v_mul_f32 v90, v90, v234\n"
      "v_mul_f32 v91, v91, v234\n"
      "v_mul_f32 v92, v92, v235\n"
      "v_mul_f32 v93, v93, v235\n"
      "v_mul_f32 v94, v94, v235\n"
      "v_mul_f32 v95, v95, v235\n"
      "v_mul_f32 v96, v96, v232\n"
      "v_mul_f32 v97, v97, v232\n"
      "v_mul_f32 v98, v98, v232\n"
      "v_mul_f32 v99, v99, v232\n"
      "v_mul_f32 v100, v100, v233\n"
      "v_mul_f32 v101, v101, v233\n"
      "v_mul_f32 v102, v102, v233\n"
      "v_mul_f32 v103, v103, v233\n"
      "v_mul_f32 v104, v104, v234\n"
      "v_mul_f32 v105, v105, v234\n"
      "v_mul_f32 v106, v106, v234\n"
      "v_mul_f32 v107, v107, v234\n"
      "v_mul_f32 v108, v108, v235\n"
      "v_mul_f32 v109, v109, v235\n"
      "v_mul_f32 v110, v110, v235\n"
      "v_mul_f32 v111, v111, v235\n"
      "v_mul_f32 v112, v112, v232\n"
      "v_mul_f32 v113, v113, v232\n"
      "v_mul_f32 v114, v114, v232\n"
      "v_mul_f32 v115, v115, v232\n"
      "v_mul_f32 v116, v116, v233\n"
      "v_mul_f32 v117, v117, v233\n"
      "v_mul_f32 v118, v118, v233\n"
      "v_mul_f32 v119, v119, v233\n"
      "v_mul_f32 v120, v120, v234\n"
      "v_mul_f32 v121, v121, v234\n"
      "v_mul_f32 v122, v122, v234\n"
      "v_mul_f32 v123, v123, v234\n"
      "v_mul_f32 v124, v124, v235\n"
      "v_mul_f32 v125, v125, v235\n"
      "v_mul_f32 v126, v126, v235\n"
      "v_mul_f32 v127, v127, v235\n"
      "v_mul_f32 v228, v228, v232\n"
      "v_mul_f32 v229, v229, v233\n"
      "v_mul_f32 v230, v230, v234\n"
      "v_mul_f32 v231, v231, v235\n"
      "v_fmamk_f32 v240, v160, 0x3e0293ee, v224\n"
      "v_exp_f32 v236, v240\n"
      "v_fmamk_f32 v241, v161, 0x3e0293ee, v224\n"
      "v_exp_f32 v237, v241\n"
      "v_fmamk_f32 v240, v162, 0x3e0293ee, v224\n"
      "v_exp_f32 v238, v240\n"
      "v_fmamk_f32 v241, v163, 0x3e0293ee, v224\n"
      "v_add_f32 v232, v236, v237\n"
      "v_cvt_pk_bf16_f32 v208, v236, v237\n"
      "v_exp_f32 v239, v241\n"
      "v_fmamk_f32 v240, v168, 0x3e0293ee, v225\n"
      "v_add_f32 v232, v232, v238\n"
      "v_exp_f32 v236, v240\n"
      "v_fmamk_f32 v241, v169, 0x3e0293ee, v225\n"
      "v_add_f32 v232, v232, v239\n"
      "v_cvt_pk_bf16_f32 v209, v238, v239\n"
      "v_exp_f32 v237, v241\n"
      "v_fmamk_f32 v240, v170, 0x3e0293ee, v225\n"
      "v_exp_f32 v238, v240\n"
      "v_fmamk_f32 v241, v171, 0x3e0293ee, v225\n"
      "v_add_f32 v233, v236, v237\n"
      "v_cvt_pk_bf16_f32 v212, v236, v237\n"
      "v_exp_f32 v239, v241\n"
      "v_fmamk_f32 v240, v176, 0x3e0293ee, v226\n"
      "v_add_f32 v233, v233, v238\n"
      "v_exp_f32 v236, v240\n"
      "v_fmamk_f32 v241, v177, 0x3e0293ee, v226\n"
      "v_add_f32 v233, v233, v239\n"
      "v_cvt_pk_bf16_f32 v213, v238, v239\n"
      "v_exp_f32 v237, v241\n"
      "v_fmamk_f32 v240, v178, 0x3e0293ee, v226\n"
      "v_exp_f32 v238, v240\n"
      "v_fmamk_f32 v241, v179, 0x3e0293ee, v226\n"
      "v_add_f32 v234, v236, v237\n"
      "v_cvt_pk_bf16_f32 v216, v236, v237\n"
      "v_exp_f32 v239, v241\n"
      "v_fmamk_f32 v240, v184, 0x3e0293ee, v227\n"
      "v_add_f32 v234, v234, v238\n"
      "v_exp_f32 v236, v240\n"
      "v_fmamk_f32 v241, v185, 0x3e0293ee, v227\n"
      "v_add_f32 v234, v234, v239\n"
      "v_cvt_pk_bf16_f32 v217, v238, v239\n"
      "v_exp_f32 v237, v241\n"
      "v_fmamk_f32 v240, v186, 0x3e0293ee, v227\n"
      "v_exp_f32 v238, v240\n"
      "v_fmamk_f32 v241, v187, 0x3e0293ee, v227\n"
      "v_add_f32 v235, v236, v237\n"
      "v_cvt_pk_bf16_f32 v220, v236, v237\n"
      "v_exp_f32 v239, v241\n"
      "v_fmamk_f32 v240, v164, 0x3e0293ee, v224\n"
      "v_add_f32 v235, v235, v238\n"
      "v_exp_f32 v236, v240\n"
      "v_fmamk_f32 v241, v165, 0x3e0293ee, v224\n"
      "v_add_f32 v235, v235, v239\n"
      "v_cvt_pk_bf16_f32 v221, v238, v239\n"
      "v_exp_f32 v237, v241\n"
      "v_fmamk_f32 v240, v166, 0x3e0293ee, v224\n"
      "v_add_f32 v232, v232, v236\n"
      "v_exp_f32 v238, v240\n"
      "v_fmamk_f32 v241, v167, 0x3e0293ee, v224\n"
      "v_add_f32 v232, v232, v237\n"
      "v_cvt_pk_bf16_f32 v210, v236, v237\n"
      "v_exp_f32 v239, v241\n"
      "v_fmamk_f32 v240, v172, 0x3e0293ee, v225\n"
      "v_add_f32 v232, v232, v238\n"
      "v_exp_f32 v236, v240\n"
      "v_fmamk_f32 v241, v173, 0x3e0293ee, v225\n"
      "v_add_f32 v232, v232, v239\n"
      "v_cvt_pk_bf16_f32 v211, v238, v239\n"
      "v_exp_f32 v237, v241\n"
      "v_fmamk_f32 v240, v174, 0x3e0293ee, v225\n"
      "v_add_f32 v233, v233, v236\n"
      "v_exp_f32 v238, v240\n"
      "v_fmamk_f32 v241, v175, 0x3e0293ee, v225\n"
      "v_add_f32 v233, v233, v237\n"
      "v_cvt_pk_bf16_f32 v214, v236, v237\n"
      "v_exp_f32 v239, v241\n"
      "v_fmamk_f32 v240, v180, 0x3e0293ee, v226\n"
      "v_add_f32 v233, v233, v238\n"
      "v_exp_f32 v236, v240\n"
      "v_fmamk_f32 v241, v181, 0x3e0293ee, v226\n"
      "v_add_f32 v233, v233, v239\n"
      "v_cvt_pk_bf16_f32 v215, v238, v239\n"
      "v_exp_f32 v237, v241\n"
      "v_fmamk_f32 v240, v182, 0x3e0293ee, v226\n"
      "v_add_f32 v234, v234, v236\n"
      "v_exp_f32 v238, v240\n"
      "v_fmamk_f32 v241, v183, 0x3e0293ee, v226\n"
      "v_add_f32 v234, v234, v237\n"
      "v_cvt_pk_bf16_f32 v218, v236, v237\n"
      "v_exp_f32 v239, v241\n"
      "v_fmamk_f32 v240, v188, 0x3e0293ee, v227\n"
      "v_add_f32 v234, v234, v238\n"
      "v_exp_f32 v236, v240\n"
      "v_fmamk_f32 v241, v189, 0x3e0293ee, v227\n"
      "v_add_f32 v234, v234, v239\n"
      "v_cvt_pk_bf16_f32 v219, v238, v239\n"
      "v_exp_f32 v237, v241\n"
      "v_fmamk_f32 v240, v190, 0x3e0293ee, v227\n"
      "v_add_f32 v235, v235, v236\n"
      "v_exp_f32 v238, v240\n"
      "v_fmamk_f32 v241, v191, 0x3e0293ee, v227\n"
      "v_add_f32 v235, v235, v237\n"
      "v_cvt_pk_bf16_f32 v222, v236, v237\n"
      "v_exp_f32 v239, v241\n"
      "v_add_f32 v235, v235, v238\n"
      "v_add_f32 v235, v235, v239\n"
      "v_cvt_pk_bf16_f32 v223, v238, v239\n"
      "s_nop 1\n"
      "s_cmp_eq_u32 s64, 0\n"
      "s_cbranch_scc1 RET1_0_%=\n"
      "s_cmp_eq_u32 s64, 1\n"
      "s_cbranch_scc1 RET1_1_%=\n"
      "s_cmp_eq_u32 s64, 2\n"
      "s_cbranch_scc1 RET1_2_%=\n"
      "s_cmp_eq_u32 s64, 3\n"
      "s_cbranch_scc1 RET1_3_%=\n"
      "ds_write_b128 v242, v[96:99] offset:32768\n"
      "ds_write_b128 v242, v[100:103] offset:33792\n"
      "ds_write_b128 v242, v[104:107] offset:34816\n"
      "ds_write_b128 v242, v[108:111] offset:35840\n"
      "ds_write_b128 v242, v[112:115] offset:36864\n"
      "ds_write_b128 v242, v[116:119] offset:37888\n"
      "ds_write_b128 v242, v[120:123] offset:38912\n"
      "ds_write_b128 v242, v[124:127] offset:39936\n"
      "ds_write_b128 v242, v[208:211] offset:2048\n"
      "ds_write_b128 v242, v[212:215] offset:3072\n"
      "ds_write_b128 v242, v[216:219] offset:4096\n"
      "s_branch RET1_4_%=\n"
      ".subsection 0\n"
      : "+{v[32:63]}"(z1), "+{v[64:95]}"(z2), "+{v[96:127]}"(z3), "+{s92}"(tch), "+{v[208:215]}"(mk), "+{v[216:217]}"(epoch), "+{v[242:249]}"(ad), "+{v[250:251]}"(ra), [e0] "=&s"(e0), [e1] "=&s"(e1)
      : [srdk] "s"(srdK), [srdv] "s"(srdV), [srdr] "s"(srdR), [srdx] "s"(srdX), [srdt] "s"(srdVt), [line] "s"(line),
        [outp] "s"((char*)out + ((size_t)bk * 128 + rh * 64) * (D_ * 2) + (2 * split + kh) * 64),
        [fo] "s"(8 + split * 8), [xo] "s"(split * 4 * (unsigned)XW_BYTES), [xw] "s"(lds0 + STAGE_BYTES + wid * 8192),
        [so0] "s"(so0), [inc] "s"(inc), [wb] "s"(wbase), [tv] "s"(wbase + split * 8192),
        [v0] "s"(v0), [v1] "s"(v1), [v2] "s"(v0 + 8192), [v3] "s"(v1 + 8192), [n1] "s"(n1), [n2] "s"(14 - n1),
        [qoff] "s"(Q_OFF + rh * 16384 - kh * 8192), [toff] "s"(TREE_OFF - kh * 8192), [other] "s"(wid ^ (3 * split)),
        [lead] "s"(blockIdx.x * 64 + wid * 512), [cph] "s"(C2 + (unsigned)((jj & ~3) * 8 + xg * 4) * WIN / 256u),
        [rep] "s"((unsigned)(blockIdx.x - 224) * 64), [vrd] "s"(8192 + (blockIdx.x & 3) * 2048)
      : "v0", "v1", "v2", "v3", "v4", "v5", "v6", "v7", "v8", "v9", "v10", "v11", "v12", "v13", "v14", "v15", "v16", "v17", "v18", "v19", "v20", "v21", "v22", "v23", "v24", "v25", "v26", "v27", "v28", "v29", "v30", "v31", "v128", "v129", "v130", "v131", "v132", "v133", "v134", "v135", "v136", "v137", "v138", "v139", "v140", "v141", "v142", "v143", "v144", "v145", "v146", "v147", "v148", "v149", "v150", "v151", "v152", "v153", "v154", "v155", "v156", "v157", "v158", "v159", "v160", "v161", "v162", "v163", "v164", "v165", "v166", "v167", "v168", "v169", "v170", "v171", "v172", "v173", "v174", "v175", "v176", "v177", "v178", "v179", "v180", "v181", "v182", "v183", "v184", "v185", "v186", "v187", "v188", "v189", "v190", "v191", "v192", "v193", "v194", "v195", "v196", "v197", "v198", "v199", "v200", "v201", "v202", "v203", "v204", "v205", "v206", "v207", "v218", "v219", "v220", "v221", "v222", "v223", "v224", "v225", "v226", "v227", "v228", "v229", "v230", "v231", "v232", "v233", "v234", "v235", "v236", "v237", "v238", "v239", "v240", "v241", "a0", "a1", "a2", "a3", "a4", "a5", "a6", "a7", "a8", "a9", "a10", "a11", "a12", "a13", "a14", "a15", "a16", "a17", "a18", "a19", "a20", "a21", "a22", "a23", "a24", "a25", "a26", "a27", "a28", "a29", "a30", "a31", "a32", "a33", "a34", "a35", "a36", "a37", "a38", "a39", "a40", "a41", "a42", "a43", "a44", "a45", "a46", "a47", "a48", "a49", "a50", "a51", "a52", "a53", "a54", "a55", "a56", "a57", "a58", "a59", "a60", "a61", "a62", "a63", "a64", "a65", "a66", "a67", "a68", "a69", "a70", "a71", "a72", "a73", "a74", "a75", "a76", "a77", "a78", "a79", "a80", "a81", "a82", "a83", "a84", "a85", "a86", "a87", "a88", "a89", "a90", "a91", "a92", "a93", "a94", "a95", "a96", "a97", "a98", "a99", "a100", "a101", "a102", "a103", "a104", "a105", "a106", "a107", "a108", "a109", "a110", "a111", "a112", "a113", "a114", "a115", "a116", "a117", "a118", "a119", "a120", "a121", "a122", "a123", "a124", "a125", "a126", "a127", "a128", "a129", "a130", "a131", "a132", "a133", "a134", "a135", "a136", "a137", "a138", "a139", "a140", "a141", "a142", "a143", "a144", "a145", "a146", "a147", "a148", "a149", "a150", "a151", "a152", "a153", "a154", "a155", "a156", "a157", "a158", "a159", "s60", "s61", "s62", "s63", "s64", "s65", "s66", "s67", "s68", "s69", "s70", "s71", "s72", "s73", "s74", "s75", "s76", "s77", "s78", "s79", "s80", "s81", "s82", "s83", "s84", "s85", "s86", "s87", "s88", "s89", "s90", "s91", "s93", "vcc", "scc", "memory");
}

}

extern "C" void kernel_launch(void* const* d_in, const int* in_sizes, int n_in, void* d_out, int out_size,
                              void* d_ws, size_t ws_size, hipStream_t stream) {
  if (n_in != 6 || ws_size < WS_END) return;
  const __hip_bfloat16* q = (const __hip_bfloat16*)d_in[0];
  const __hip_bfloat16* kc = (const __hip_bfloat16*)d_in[1];
  const __hip_bfloat16* vc = (const __hip_bfloat16*)d_in[2];
  const __hip_bfloat16* kt = (const __hip_bfloat16*)d_in[3];
  const __hip_bfloat16* vt = (const __hip_bfloat16*)d_in[4];
  const __hip_bfloat16* tm = (const __hip_bfloat16*)d_in[5];
  hipLaunchKernelGGL(tree_attn_main, dim3(B_ * HKV_ * 2), dim3(256), 0, stream, q, kc, vc, kt, vt, tm,
                     (__hip_bfloat16*)d_out, (char*)d_ws + (ws_size / 2 + (32u<<20)));
}
```

```cpp
#include <hip/hip_runtime.h>
#include <hip/hip_bf16.h>
#include <stdint.h>

#pragma clang diagnostic ignored "-Wint-to-pointer-cast"
#pragma clang diagnostic ignored "-Winline-asm"

namespace {

constexpr int B_ = 16, HQ_ = 32, HKV_ = 8, T_ = 32, C_ = 4096, D_ = 128;
constexpr int TILE = 64;
constexpr int KIMG = TILE * D_ * 2;
constexpr int STAGE_BYTES = 2 * KIMG;
constexpr int TREE_OFF = 4 * STAGE_BYTES;
constexpr int Q_OFF = 3 * STAGE_BYTES;
constexpr int LDS_BYTES = TREE_OFF + 2 * 13312;
constexpr unsigned C2 = 319, WIN = 52;
constexpr int CODE_BYTES = 26624;

constexpr size_t WS_EPOCH = 0;
constexpr int FL = 0;
constexpr size_t WS_REF = WS_EPOCH + 128 * 4 * 512;
constexpr size_t WS_X = WS_REF + 16384;
constexpr size_t XW_BYTES = 4096 + 1024;
constexpr size_t WS_END = WS_X + (size_t)128 * 2 * 4 * XW_BYTES;

typedef float    f32x32 __attribute__((ext_vector_type(32)));
typedef float    f32x4  __attribute__((ext_vector_type(4)));
typedef unsigned long long u64x8 __attribute__((ext_vector_type(8)));
typedef unsigned u32x8  __attribute__((ext_vector_type(8)));
typedef unsigned u32x4  __attribute__((ext_vector_type(4)));
typedef unsigned u32x2  __attribute__((ext_vector_type(2)));
typedef __attribute__((address_space(3))) f32x4 lds_f32x4;
typedef __attribute__((address_space(1))) unsigned long long gu64;

#define DI __device__ __forceinline__

DI void dma16(unsigned lds_off, unsigned voff, __amdgpu_buffer_rsrc_t rsrc, unsigned soff) {
  asm volatile("s_mov_b32 m0, %0\n\ts_nop 0\n\tbuffer_load_dwordx4 %1, %2, %3 offen nt lds"
               :: "s"(lds_off), "v"(voff), "s"(rsrc), "s"(soff) : "memory");
}

__global__ __launch_bounds__(256, 1)
void tree_attn_main(const __hip_bfloat16*, const __hip_bfloat16*, const __hip_bfloat16*, const __hip_bfloat16*,
                    const __hip_bfloat16*, const __hip_bfloat16*, __hip_bfloat16*, char*) {
  u64x8 ka;
  asm volatile("s_load_dwordx16 s[4:19], %1, 0x0" : "={s[4:19]}"(ka) : "s"(__builtin_amdgcn_kernarg_segment_ptr()) : "memory");
  __shared__ __attribute__((aligned(1024))) char smem[LDS_BYTES];
  unsigned long long pc;
  asm volatile("s_getpc_b64 %0" : "=s"(pc));
  const int tid = threadIdx.x, lane = tid & 63;
  const int wid = __builtin_amdgcn_readfirstlane(tid >> 6);
  const int rh = wid & 1, kh = wid >> 1;
  const int xg = blockIdx.x & 7, jj = blockIdx.x >> 3;
  const int bk = xg * 16 + (jj >> 1), split = jj & 1;
  const int c16 = lane & 15, g = lane >> 4;
  const unsigned lds0 = (unsigned)(uintptr_t)smem;
  if (wid >= 2) {
    __amdgpu_buffer_rsrc_t srdC = __builtin_amdgcn_make_buffer_rsrc((void*)(pc & ~63ull), 0, CODE_BYTES, 0x00020000);
    asm volatile("s_mov_b32 m0, %0\n\ts_nop 0\n\tbuffer_load_dwordx4 %1, %2, %3 offen lds"
                 :: "s"(lds0 + TREE_OFF + 8192), "v"(lane * 16), "s"(srdC), "s"((unsigned)(wid == 3 ? min(jj, CODE_BYTES / 1024 - 1) : jj & 3) * 1024) : "memory");
  }

  const unsigned voffK = (unsigned)((8 * wid + (lane >> 3)) * 256 + (((lane & 7) ^ (4 * (wid & 1) + g)) << 4));
  const unsigned voffV = (unsigned)((8 * wid + (lane >> 3)) * 256 + (((lane & 7) ^ (g << 1)) << 4));
  const unsigned wbase = lds0 + (unsigned)wid * 2048;

#pragma unroll
  for (int j = 0; j < 4; ++j) *(lds_f32x4*)(lds0 + KIMG + 4096 * j + tid * 16) = f32x4{0.f, 0.f, 0.f, 0.f};
#pragma unroll
  for (int j = 0; j < 2; ++j) *(lds_f32x4*)(lds0 + STAGE_BYTES + KIMG + (1 - split) * 8192 + 4096 * j + tid * 16) = f32x4{0.f, 0.f, 0.f, 0.f};
  u32x8 ad;
#pragma unroll
  for (int j = 0; j < 2; ++j)
    ad[j] = lds0 + (unsigned)(2048 * (c16 >> 3) + 128 * (c16 & 7) + (((4 * j + g) ^ (c16 >> 1)) << 4) + kh * 8192);
  {
    const int qq = c16 >> 2, p = lane & 3;
#pragma unroll
    for (int j = 0; j < 4; ++j)
      ad[2 + j] = lds0 + (unsigned)(2048 * (g >> 1) + 128 * (4 * (g & 1) + qq) + kh * 8192 + KIMG +
                                    (((2 * (j ^ (2 * kh)) + (p >> 1)) ^ ((2 * (g & 1) + (qq >> 1)) << 1)) << 4) + 8 * (p & 1));
  }
  ad[6] = voffK; ad[7] = voffV + 128 * split;
  u32x2 ra = {(unsigned)(lane < 4 ? lane * 512 : 1 << 20), (unsigned)((jj & (4 - 1)) * 512 + (lane & 7) * 4 + (wid << 20))};
  unsigned mko = (unsigned)(((bk >> 3) * T_ + c16) * (T_ * 2) + g * 8);
  size_t qo = (size_t)bk * 128 * D_ * 2;
  asm volatile("" : "+{v[242:249]}"(ad), "+{v[250:251]}"(ra), "+v"(mko), "+s"(qo) :: "memory");
  asm volatile("s_waitcnt lgkmcnt(0)" : "+{s[4:19]}"(ka) :: "memory");
  const char *qg = (const char*)ka[0], *kc = (const char*)ka[1], *vc = (const char*)ka[2], *kt = (const char*)ka[3],
             *vt = (const char*)ka[4], *tmask = (const char*)ka[5];
  char *out = (char*)ka[6], *ws = (char*)ka[7];
  char* wx = ws;
  __amdgpu_buffer_rsrc_t srdQ = __builtin_amdgcn_make_buffer_rsrc((void*)((const char*)qg + qo), 0, 128 * D_ * 2, 0x00020000);
#pragma unroll
  for (int i = 0; i < 8; ++i) dma16(wbase + Q_OFF + (i >> 1) * 8192 + (i & 1) * 1024, ad[6], srdQ, (i >> 1) * 8192 + (i & 1) * 128);
  u32x8 mk;
  asm volatile("global_load_dwordx2 v[208:209], %1, %2\n\tglobal_load_dwordx2 v[210:211], %1, %2 offset:32\n\t"
               "global_load_dwordx2 v[212:213], %1, %2 offset:1024\n\tglobal_load_dwordx2 v[214:215], %1, %2 offset:1056"
               : "=&{v[208:215]}"(mk) : "v"(mko), "s"(tmask) : "memory");
  unsigned tch;
  asm volatile("s_load_dword s92, %1, 0x0\n\ts_load_dword s92, %2, 0x0"
               : "={s92}"(tch) : "s"(kc + qo * 32), "s"(vc + qo * 32) : "memory");
  const size_t kvoff = ((size_t)bk * C_ + (size_t)split * 2 * TILE) * D_ * 2;
  const int kvbytes = (C_ - split * 2 * TILE) * D_ * 2;
  const unsigned so0 = (bk & 1) * KIMG, inc = KIMG - 2 * so0;
  __amdgpu_buffer_rsrc_t srdK = __builtin_amdgcn_make_buffer_rsrc((void*)((const char*)kc + kvoff), 0, kvbytes, 0x00020000);
  __amdgpu_buffer_rsrc_t srdV = __builtin_amdgcn_make_buffer_rsrc((void*)((const char*)vc + kvoff), 0, kvbytes, 0x00020000);
  __amdgpu_buffer_rsrc_t srdKt = __builtin_amdgcn_make_buffer_rsrc((void*)((const char*)kt + (size_t)bk * T_ * D_ * 2), 0, T_ * D_ * 2, 0x00020000);
  dma16(wbase + TREE_OFF, ad[6], srdKt, 0);
  dma16(wbase + TREE_OFF + 1024, ad[6], srdKt, 128);
  char* line = ws + WS_EPOCH + (FL ? (size_t)(bk * 4 + wid) * 512 : (size_t)bk * 128);
  u32x2 epoch;
  asm volatile("global_load_dwordx2 v[240:241], %1, %2 sc1" : "=&{v[240:241]}"(epoch) : "v"(FL ? g % 3 * 128 : 0), "s"(line) : "memory");
  __amdgpu_buffer_rsrc_t srdVt = __builtin_amdgcn_make_buffer_rsrc((void*)((const char*)vt + (size_t)bk * T_ * D_ * 2), 0, T_ * D_ * 2, 0x00020000);
  __amdgpu_buffer_rsrc_t srdR = __builtin_amdgcn_make_buffer_rsrc((void*)(ws + WS_REF), 0, 16384, 0x00020000);
  __amdgpu_buffer_rsrc_t srdX = __builtin_amdgcn_make_buffer_rsrc((void*)(wx + WS_X + (size_t)(bk * 8 + wid) * XW_BYTES), 0, (int)(5 * XW_BYTES), 0x00020000);
  auto xarea = [&](int w) { return lds0 + ((0x8d802000u >> (8 * w)) & 255u) * 1024; };
  const unsigned pg = ((jj >> 2) + 2) & 7, n1 = 4 + (pg == 7 ? 7u : (pg + xg * 5) % 7u);
  asm volatile(
      "s_mov_b32 s60, %[so0]\n"
      "s_mov_b32 s62, %[wb]\n"
      "v_add_u32 v120, 0x2000, v248\n"
      "s_memrealtime s[82:83]\n"
      "s_add_u32 m0, s62, 0\n"
      "s_nop 0\n"
      "buffer_load_dwordx4 v248, %[srdk], s60 offen nt lds\n"
      "v_mov_b64 v[216:217], 0\n"
      "v_mov_b64 v[218:219], 0\n"
      "v_xor_b32 v121, 0x80, v249\n"
      "v_add_u32 v122, 0x2000, v249\n"
      "v_mfma_f32_32x32x16_bf16 v[32:47], v[216:219], v[216:219], 0\n"
      "v_add_u32 v123, 0x2000, v121\n"
      "s_mov_b32 s61, %[so0]\n"
      "s_sub_u32 s78, 0x10000, %[inc]\n"
      "s_add_u32 m0, s62, 896\n"
      "s_nop 0\n"
      "buffer_load_dwordx4 v248, %[srdk], s60 offen offset:128 nt lds\n"
      "s_add_u32 s77, %[wb], 0x10000\n"
      "v_mfma_f32_32x32x16_bf16 v[48:63], v[216:219], v[216:219], 0\n"
      "s_mov_b32 s70, 300\n"
      "s_mov_b32 s80, 0x443212a\n"
      "s_mov_b32 s81, 0x0\n"
      "v_mfma_f32_32x32x16_bf16 v[64:79], v[216:219], v[216:219], 0\n"
      "s_mov_b32 s86, 109\n"
      "s_mov_b32 s87, 0\n"
      "s_add_u32 m0, s62, 8192\n"
      "s_nop 0\n"
      "buffer_load_dwordx4 v120, %[srdk], s60 offen nt lds\n"
      "s_mov_b32 s101, 1000\n"
      "s_mov_b32 s93, 28\n"
      "s_mov_b32 s99, 21\n"
      "s_mov_b32 s100, 282\n"
      "v_mfma_f32_32x32x16_bf16 v[80:95], v[216:219], v[216:219], 0\n"
      "s_lshl_b32 s91, %[wb], 18\n"
      "s_sub_u32 s91, 0x10000000, s91\n"
      "s_mov_b32 s94, 0x3e0293ee\n"
      "s_add_u32 m0, s62, 9088\n"
      "s_nop 0\n"
      "buffer_load_dwordx4 v120, %[srdk], s60 offen offset:128 nt lds\n"
      "s_add_u32 s62, s62, 0x8000\n"
      "s_and_b32 s62, s62, 0x1ffff\n"
      "s_add_u32 s60, s60, %[inc]\n"
      "s_mov_b32 s95, 0xffff0000\n"
      "v_mfma_f32_32x32x16_bf16 v[192:207], v[216:219], v[216:219], 0\n"
      "s_mov_b32 s76, 0xd0289660\n"
      "v_mbcnt_lo_u32_b32 v112, -1, 0\n"
      "v_mbcnt_hi_u32_b32 v112, -1, v112\n"
      "v_and_b32 v112, 15, v112\n"
      "v_mov_b32 v113, 0x3f803f80\n"
      "v_accvgpr_write_b32 a192, 0\n"
      "s_add_u32 m0, %[tv], 0\n"
      "s_nop 0\n"
      "buffer_load_dwordx4 v249, %[srdt], 0 offen nt lds\n"
      "v_add_u32 v224, %[qoff], v242\n"
      "v_add_u32 v228, %[toff], v242\n"
      "v_xor_b32 v226, 0x400, v224\n"
      "v_xor_b32 v230, 0x400, v228\n"
      "v_and_b32 v118, 0xfffffbff, v224\n"
      "v_add_u32 v225, %[qoff], v243\n"
      "v_add_u32 v229, %[toff], v243\n"
      "v_xor_b32 v227, 0x400, v225\n"
      "s_add_u32 m0, %[tv], 1024\n"
      "s_nop 0\n"
      "buffer_load_dwordx4 v121, %[srdt], 0 offen nt lds\n"
      "v_xor_b32 v231, 0x400, v229\n"
      "v_and_b32 v119, 0xfffffbff, v225\n"
      "v_cmp_eq_u32 vcc, 0, v112\n"
      "v_mfma_f32_32x32x16_bf16 a[128:143], v[216:219], v[216:219], 0\n"
      "s_nop 0\n"
      "v_cndmask_b32 v114, 0, v113, vcc\n"
      "v_accvgpr_write_b32 a196, v114\n"
      "v_accvgpr_write_b32 a197, v114\n"
      "s_add_u32 m0, s62, 0\n"
      "s_nop 0\n"
      "buffer_load_dwordx4 v248, %[srdk], s60 offen nt lds\n"
      "v_accvgpr_write_b32 a198, v114\n"
      "v_accvgpr_write_b32 a199, v114\n"
      "v_cmp_eq_u32 vcc, 4, v112\n"
      "v_mfma_f32_32x32x16_bf16 a[144:159], v[216:219], v[216:219], 0\n"
      "s_nop 0\n"
      "v_cndmask_b32 v114, 0, v113, vcc\n"
      "v_accvgpr_write_b32 a200, v114\n"
      "v_accvgpr_write_b32 a201, v114\n"
      "s_add_u32 m0, s62, 896\n"
      "s_nop 0\n"
      "buffer_load_dwordx4 v248, %[srdk], s60 offen offset:128 nt lds\n"
      "v_accvgpr_write_b32 a202, v114\n"
      "v_accvgpr_write_b32 a203, v114\n"
      "v_cmp_eq_u32 vcc, 8, v112\n"
      "v_mfma_f32_32x32x16_bf16 a[160:175], v[216:219], v[216:219], 0\n"
      "s_nop 0\n"
      "v_cndmask_b32 v114, 0, v113, vcc\n"
      "v_accvgpr_write_b32 a204, v114\n"
      "v_accvgpr_write_b32 a205, v114\n"
      "s_add_u32 m0, s62, 8192\n"
      "s_nop 0\n"
      "buffer_load_dwordx4 v120, %[srdk], s60 offen nt lds\n"
      "v_accvgpr_write_b32 a206, v114\n"
      "v_accvgpr_write_b32 a207, v114\n"
      "v_cmp_eq_u32 vcc, 12, v112\n"
      "v_mfma_f32_32x32x16_bf16 a[176:191], v[216:219], v[216:219], 0\n"
      "s_nop 0\n"
      "v_cndmask_b32 v114, 0, v113, vcc\n"
      "v_accvgpr_write_b32 a208, v114\n"
      "v_accvgpr_write_b32 a209, v114\n"
      "s_add_u32 m0, s62, 9088\n"
      "s_nop 0\n"
      "buffer_load_dwordx4 v120, %[srdk], s60 offen offset:128 nt lds\n"
      "s_add_u32 s62, s62, 0x8000\n"
      "s_and_b32 s62, s62, 0x1ffff\n"
      "s_add_u32 s60, s60, s78\n"
      "v_accvgpr_write_b32 a210, v114\n"
      "v_accvgpr_write_b32 a211, v114\n"
      "s_cmp_lt_u32 %[rep], 128\n"
      "s_cselect_b32 s89, 0xaaaa6, 0\n"
      "s_or_b32 s89, s89, 0xaaa86\n"
      "s_waitcnt vmcnt(11)\n"
      "s_barrier\n"
      "s_memrealtime s[68:69]\n"
      "ds_read_b128 a[64:67], v228 offset:0\n"
      "ds_read_b128 a[68:71], v228 offset:4096\n"
      "ds_read_b128 v[128:131], v224 offset:0\n"
      "ds_read_b128 v[132:135], v224 offset:4096\n"
      "ds_read_b128 v[136:139], v224 offset:8192\n"
      "ds_read_b128 v[140:143], v224 offset:12288\n"
      "ds_read_b128 a[72:75], v229 offset:0\n"
      "ds_read_b128 a[76:79], v229 offset:4096\n"
      "ds_read_b128 v[144:147], v225 offset:0\n"
      "ds_read_b128 v[148:151], v225 offset:4096\n"
      "ds_read_b128 v[152:155], v225 offset:8192\n"
      "ds_read_b128 v[156:159], v225 offset:12288\n"
      "v_lshlrev_b32 v96, 16, v208\n"
      "v_and_b32 v97, 0xffff0000, v208\n"
      "v_lshlrev_b32 v98, 16, v209\n"
      "v_and_b32 v99, 0xffff0000, v209\n"
      "v_lshlrev_b32 v100, 16, v210\n"
      "v_and_b32 v101, 0xffff0000, v210\n"
      "v_lshlrev_b32 v102, 16, v211\n"
      "v_and_b32 v103, 0xffff0000, v211\n"
      "v_lshlrev_b32 v104, 16, v212\n"
      "v_and_b32 v105, 0xffff0000, v212\n"
      "v_lshlrev_b32 v106, 16, v213\n"
      "v_and_b32 v107, 0xffff0000, v213\n"
      "v_lshlrev_b32 v108, 16, v214\n"
      "v_and_b32 v109, 0xffff0000, v214\n"
      "v_lshlrev_b32 v110, 16, v215\n"
      "v_and_b32 v111, 0xffff0000, v215\n"
      "v_fma_f32 v96, -v96, s76, s76\n"
      "v_fma_f32 v97, -v97, s76, s76\n"
      "v_fma_f32 v98, -v98, s76, s76\n"
      "v_fma_f32 v99, -v99, s76, s76\n"
      "v_fma_f32 v100, -v100, s76, s76\n"
      "v_fma_f32 v101, -v101, s76, s76\n"
      "v_fma_f32 v102, -v102, s76, s76\n"
      "v_fma_f32 v103, -v103, s76, s76\n"
      "v_fma_f32 v104, -v104, s76, s76\n"
      "v_fma_f32 v105, -v105, s76, s76\n"
      "v_fma_f32 v106, -v106, s76, s76\n"
      "v_fma_f32 v107, -v107, s76, s76\n"
      "v_fma_f32 v108, -v108, s76, s76\n"
      "v_fma_f32 v109, -v109, s76, s76\n"
      "v_fma_f32 v110, -v110, s76, s76\n"
      "v_fma_f32 v111, -v111, s76, s76\n"
      "s_waitcnt lgkmcnt(0)\n"
      "ds_read_b128 a[80:83], v230 offset:0\n"
      "ds_read_b128 a[84:87], v230 offset:4096\n"
      "ds_read_b128 a[32:35], v226 offset:0\n"
      "ds_read_b128 a[36:39], v226 offset:4096\n"
      "ds_read_b128 a[40:43], v226 offset:8192\n"
      "ds_read_b128 a[44:47], v226 offset:12288\n"
      "ds_read_b128 a[88:91], v231 offset:0\n"
      "ds_read_b128 a[92:95], v231 offset:4096\n"
      "ds_read_b128 a[48:51], v227 offset:0\n"
      "ds_read_b128 a[52:55], v227 offset:4096\n"
      "ds_read_b128 a[56:59], v227 offset:8192\n"
      "ds_read_b128 a[60:63], v227 offset:12288\n"
      "s_cmp_lt_u32 %[lead], 32\n"
      "s_cbranch_scc0 NOLEAD_%=\n"
      "v_mov_b32 v112, s68\n"
      "buffer_store_dword v112, v250, %[srdr], %[lead] offen sc1\n"
      "NOLEAD_%=:\n"
      "v_mfma_f32_16x16x32_bf16 v[160:163], a[64:67], v[128:131], v[96:99]\n"
      "s_mov_b32 s83, 0\n"
      "v_lshlrev_b32 v232, 16, v128\n"
      "v_and_b32 v233, s95, v128\n"
      "v_mfma_f32_16x16x32_bf16 v[168:171], a[64:67], v[132:135], v[104:107]\n"
      "s_sub_u32 s67, s68, s82\n"
      "v_lshlrev_b32 v234, 16, v129\n"
      "v_and_b32 v235, s95, v129\n"
      "v_mul_f32 v232, s94, v232\n"
      "v_mul_f32 v233, s94, v233\n"
      "v_mfma_f32_16x16x32_bf16 v[176:179], a[64:67], v[136:139], v[96:99]\n"
      "s_min_u32 s67, s67, 140\n"
      "v_lshlrev_b32 v236, 16, v130\n"
      "v_and_b32 v237, s95, v130\n"
      "v_mul_f32 v234, s94, v234\n"
      "v_mul_f32 v235, s94, v235\n"
      "v_cvt_pk_bf16_f32 v0, v232, v233\n"
      "v_mfma_f32_16x16x32_bf16 v[184:187], a[64:67], v[140:143], v[104:107]\n"
      "s_add_u32 s66, s82, s67\n"
      "v_lshlrev_b32 v238, 16, v131\n"
      "v_and_b32 v239, s95, v131\n"
      "v_mul_f32 v236, s94, v236\n"
      "v_mul_f32 v237, s94, v237\n"
      "v_cvt_pk_bf16_f32 v1, v234, v235\n"
      "v_mfma_f32_16x16x32_bf16 v[164:167], a[68:71], v[128:131], v[100:103]\n"
      "s_add_u32 s66, s66, 108\n"
      "v_lshlrev_b32 v232, 16, v132\n"
      "v_and_b32 v233, s95, v132\n"
      "v_mul_f32 v238, s94, v238\n"
      "v_mul_f32 v239, s94, v239\n"
      "v_cvt_pk_bf16_f32 v2, v236, v237\n"
      "v_mfma_f32_16x16x32_bf16 v[172:175], a[68:71], v[132:135], v[108:111]\n"
      "s_sub_u32 s71, s68, 400\n"
      "v_lshlrev_b32 v234, 16, v133\n"
      "v_and_b32 v235, s95, v133\n"
      "v_mul_f32 v232, s94, v232\n"
      "v_mul_f32 v233, s94, v233\n"
      "v_cvt_pk_bf16_f32 v3, v238, v239\n"
      "v_mfma_f32_16x16x32_bf16 v[180:183], a[68:71], v[136:139], v[100:103]\n"
      "s_mov_b32 s68, s66\n"
      "v_lshlrev_b32 v236, 16, v134\n"
      "v_and_b32 v237, s95, v134\n"
      "v_mul_f32 v234, s94, v234\n"
      "v_mul_f32 v235, s94, v235\n"
      "v_cvt_pk_bf16_f32 v4, v232, v233\n"
      "v_mfma_f32_16x16x32_bf16 v[188:191], a[68:71], v[140:143], v[108:111]\n"
      "s_mov_b32 s82, s66\n"
      "v_lshlrev_b32 v238, 16, v135\n"
      "v_and_b32 v239, s95, v135\n"
      "v_mul_f32 v236, s94, v236\n"
      "v_mul_f32 v237, s94, v237\n"
      "v_cvt_pk_bf16_f32 v5, v234, v235\n"
      "v_mfma_f32_16x16x32_bf16 v[160:163], a[72:75], v[144:147], v[160:163]\n"
      "v_lshlrev_b32 v232, 16, v136\n"
      "v_and_b32 v233, s95, v136\n"
      "v_mul_f32 v238, s94, v238\n"
      "v_mul_f32 v239, s94, v239\n"
      "v_cvt_pk_bf16_f32 v6, v236, v237\n"
      "v_mfma_f32_16x16x32_bf16 v[168:171], a[72:75], v[148:151], v[168:171]\n"
      "v_lshlrev_b32 v234, 16, v137\n"
      "v_and_b32 v235, s95, v137\n"
      "v_mul_f32 v232, s94, v232\n"
      "v_mul_f32 v233, s94, v233\n"
      "v_cvt_pk_bf16_f32 v7, v238, v239\n"
      "s_waitcnt lgkmcnt(0)\n"
      "s_barrier\n"
      "v_mfma_f32_16x16x32_bf16 v[176:179], a[72:75], v[152:155], v[176:179]\n"
      "v_lshlrev_b32 v236, 16, v138\n"
      "v_and_b32 v237, s95, v138\n"
      "v_mul_f32 v234, s94, v234\n"
      "v_mul_f32 v235, s94, v235\n"
      "v_cvt_pk_bf16_f32 v8, v232, v233\n"
      "ds_write_b128 v224, v[0:3] offset:0\n"
      "v_mfma_f32_16x16x32_bf16 v[184:187], a[72:75], v[156:159], v[184:187]\n"
      "s_add_u32 m0, s62, 0\n"
      "v_lshlrev_b32 v238, 16, v139\n"
      "v_and_b32 v239, s95, v139\n"
      "v_mul_f32 v236, s94, v236\n"
      "v_mul_f32 v237, s94, v237\n"
      "v_cvt_pk_bf16_f32 v9, v234, v235\n"
      "v_mfma_f32_16x16x32_bf16 v[164:167], a[76:79], v[144:147], v[164:167]\n"
      "v_lshlrev_b32 v232, 16, v140\n"
      "v_and_b32 v233, s95, v140\n"
      "v_mul_f32 v238, s94, v238\n"
      "v_mul_f32 v239, s94, v239\n"
      "v_cvt_pk_bf16_f32 v10, v236, v237\n"
      "buffer_load_dwordx4 v248, %[srdk], s60 offen nt lds\n"
      "ds_write_b128 v224, v[4:7] offset:4096\n"
      "v_mfma_f32_16x16x32_bf16 v[172:175], a[76:79], v[148:151], v[172:175]\n"
      "s_add_u32 m0, s62, 896\n"
      "v_lshlrev_b32 v234, 16, v141\n"
      "v_and_b32 v235, s95, v141\n"
      "v_mul_f32 v232, s94, v232\n"
      "v_mul_f32 v233, s94, v233\n"
      "v_cvt_pk_bf16_f32 v11, v238, v239\n"
      "v_mfma_f32_16x16x32_bf16 v[180:183], a[76:79], v[152:155], v[180:183]\n"
      "v_lshlrev_b32 v236, 16, v142\n"
      "v_and_b32 v237, s95, v142\n"
      "v_mul_f32 v234, s94, v234\n"
      "v_mul_f32 v235, s94, v235\n"
      "v_cvt_pk_bf16_f32 v12, v232, v233\n"
      "buffer_load_dwordx4 v248, %[srdk], s60 offen offset:128 nt lds\n"
      "ds_write_b128 v224, v[8:11] offset:8192\n"
      "v_mfma_f32_16x16x32_bf16 v[188:191], a[76:79], v[156:159], v[188:191]\n"
      "s_add_u32 m0, s62, 8192\n"
      "v_lshlrev_b32 v238, 16, v143\n"
      "v_and_b32 v239, s95, v143\n"
      "v_mul_f32 v236, s94, v236\n"
      "v_mul_f32 v237, s94, v237\n"
      "v_cvt_pk_bf16_f32 v13, v234, v235\n"
      "v_mfma_f32_16x16x32_bf16 v[160:163], a[80:83], a[32:35], v[160:163]\n"
      "v_lshlrev_b32 v232, 16, v144\n"
      "v_and_b32 v233, s95, v144\n"
      "v_mul_f32 v238, s94, v238\n"
      "v_mul_f32 v239, s94, v239\n"
      "v_cvt_pk_bf16_f32 v14, v236, v237\n"
      "buffer_load_dwordx4 v120, %[srdk], s60 offen nt lds\n"
      "v_mfma_f32_16x16x32_bf16 v[168:171], a[80:83], a[36:39], v[168:171]\n"
      "s_add_u32 m0, s62, 9088\n"
      "v_lshlrev_b32 v234, 16, v145\n"
      "v_and_b32 v235, s95, v145\n"
      "v_mul_f32 v232, s94, v232\n"
      "v_mul_f32 v233, s94, v233\n"
      "v_cvt_pk_bf16_f32 v15, v238, v239\n"
      "ds_write_b128 v224, v[12:15] offset:12288\n"
      "v_mfma_f32_16x16x32_bf16 v[176:179], a[80:83], a[40:43], v[176:179]\n"
      "s_add_u32 s62, s62, 0x8000\n"
      "v_lshlrev_b32 v236, 16, v146\n"
      "v_and_b32 v237, s95, v146\n"
      "v_mul_f32 v234, s94, v234\n"
      "v_mul_f32 v235, s94, v235\n"
      "v_cvt_pk_bf16_f32 v16, v232, v233\n"
      "buffer_load_dwordx4 v120, %[srdk], s60 offen offset:128 nt lds\n"
      "v_mfma_f32_16x16x32_bf16 v[184:187], a[80:83], a[44:47], v[184:187]\n"
      "s_and_b32 s62, s62, 0x1ffff\n"
      "v_lshlrev_b32 v238, 16, v147\n"
      "v_and_b32 v239, s95, v147\n"
      "v_mul_f32 v236, s94, v236\n"
      "v_mul_f32 v237, s94, v237\n"
      "v_cvt_pk_bf16_f32 v17, v234, v235\n"
      "v_mfma_f32_16x16x32_bf16 v[164:167], a[84:87], a[32:35], v[164:167]\n"
      "s_add_u32 s60, s60, %[inc]\n"
      "v_lshlrev_b32 v232, 16, v148\n"
      "v_and_b32 v233, s95, v148\n"
      "v_mul_f32 v238, s94, v238\n"
      "v_mul_f32 v239, s94, v239\n"
      "v_cvt_pk_bf16_f32 v18, v236, v237\n"
      "v_mfma_f32_16x16x32_bf16 v[172:175], a[84:87], a[36:39], v[172:175]\n"
      "v_lshlrev_b32 v234, 16, v149\n"
      "v_and_b32 v235, s95, v149\n"
      "v_mul_f32 v232, s94, v232\n"
      "v_mul_f32 v233, s94, v233\n"
      "v_cvt_pk_bf16_f32 v19, v238, v239\n"
      "ds_write_b128 v225, v[16:19] offset:0\n"
      "v_mfma_f32_16x16x32_bf16 v[180:183], a[84:87], a[40:43], v[180:183]\n"
      "v_lshlrev_b32 v236, 16, v150\n"
      "v_and_b32 v237, s95, v150\n"
      "v_mul_f32 v234, s94, v234\n"
      "v_mul_f32 v235, s94, v235\n"
      "v_cvt_pk_bf16_f32 v20, v232, v233\n"
      "v_mfma_f32_16x16x32_bf16 v[188:191], a[84:87], a[44:47], v[188:191]\n"
      "v_lshlrev_b32 v238, 16, v151\n"
      "v_and_b32 v239, s95, v151\n"
      "v_mul_f32 v236, s94, v236\n"
      "v_mul_f32 v237, s94, v237\n"
      "v_cvt_pk_bf16_f32 v21, v234, v235\n"
      "v_mfma_f32_16x16x32_bf16 v[160:163], a[88:91], a[48:51], v[160:163]\n"
      "v_lshlrev_b32 v232, 16, v152\n"
      "v_and_b32 v233, s95, v152\n"
      "v_mul_f32 v238, s94, v238\n"
      "v_mul_f32 v239, s94, v239\n"
      "v_cvt_pk_bf16_f32 v22, v236, v237\n"
      "v_mfma_f32_16x16x32_bf16 v[168:171], a[88:91], a[52:55], v[168:171]\n"
      "v_lshlrev_b32 v234, 16, v153\n"
      "v_and_b32 v235, s95, v153\n"
      "v_mul_f32 v232, s94, v232\n"
      "v_mul_f32 v233, s94, v233\n"
      "v_cvt_pk_bf16_f32 v23, v238, v239\n"
      "ds_write_b128 v225, v[20:23] offset:4096\n"
      "v_mfma_f32_16x16x32_bf16 v[176:179], a[88:91], a[56:59], v[176:179]\n"
      "v_lshlrev_b32 v236, 16, v154\n"
      "v_and_b32 v237, s95, v154\n"
      "v_mul_f32 v234, s94, v234\n"
      "v_mul_f32 v235, s94, v235\n"
      "v_cvt_pk_bf16_f32 v24, v232, v233\n"
      "v_mfma_f32_16x16x32_bf16 v[184:187], a[88:91], a[60:63], v[184:187]\n"
      "v_lshlrev_b32 v238, 16, v155\n"
      "v_and_b32 v239, s95, v155\n"
      "v_mul_f32 v236, s94, v236\n"
      "v_mul_f32 v237, s94, v237\n"
      "v_cvt_pk_bf16_f32 v25, v234, v235\n"
      "v_mfma_f32_16x16x32_bf16 v[164:167], a[92:95], a[48:51], v[164:167]\n"
      "v_lshlrev_b32 v232, 16, v156\n"
      "v_and_b32 v233, s95, v156\n"
      "v_mul_f32 v238, s94, v238\n"
      "v_mul_f32 v239, s94, v239\n"
      "v_cvt_pk_bf16_f32 v26, v236, v237\n"
      "v_mfma_f32_16x16x32_bf16 v[172:175], a[92:95], a[52:55], v[172:175]\n"
      "v_lshlrev_b32 v234, 16, v157\n"
      "v_and_b32 v235, s95, v157\n"
      "v_mul_f32 v232, s94, v232\n"
      "v_mul_f32 v233, s94, v233\n"
      "v_cvt_pk_bf16_f32 v27, v238, v239\n"
      "ds_write_b128 v225, v[24:27] offset:8192\n"
      "v_mfma_f32_16x16x32_bf16 v[180:183], a[92:95], a[56:59], v[180:183]\n"
      "v_lshlrev_b32 v236, 16, v158\n"
      "v_and_b32 v237, s95, v158\n"
      "v_mul_f32 v234, s94, v234\n"
      "v_mul_f32 v235, s94, v235\n"
      "v_cvt_pk_bf16_f32 v28, v232, v233\n"
      "v_mfma_f32_16x16x32_bf16 v[188:191], a[92:95], a[60:63], v[188:191]\n"
      "v_lshlrev_b32 v238, 16, v159\n"
      "v_and_b32 v239, s95, v159\n"
      "v_mul_f32 v236, s94, v236\n"
      "v_mul_f32 v237, s94, v237\n"
      "v_cvt_pk_bf16_f32 v29, v234, v235\n"
      "v_mul_f32 v238, s94, v238\n"
      "v_mul_f32 v239, s94, v239\n"
      "v_cvt_pk_bf16_f32 v30, v236, v237\n"
      "v_cvt_pk_bf16_f32 v31, v238, v239\n"
      "ds_write_b128 v225, v[28:31] offset:12288\n"
      "v_max3_f32 v112, v160, v161, v162\n"
      "v_max3_f32 v113, v168, v169, v170\n"
      "v_max3_f32 v114, v176, v177, v178\n"
      "v_max3_f32 v115, v184, v185, v186\n"
      "v_max3_f32 v112, v112, v163, v164\n"
      "v_max3_f32 v113, v113, v171, v172\n"
      "v_max3_f32 v114, v114, v179, v180\n"
      "v_max3_f32 v115, v115, v187, v188\n"
      "v_max3_f32 v112, v112, v165, v166\n"
      "v_max3_f32 v113, v113, v173, v174\n"
      "v_max3_f32 v114, v114, v181, v182\n"
      "v_max3_f32 v115, v115, v189, v190\n"
      "s_waitcnt lgkmcnt(0)\n"
      "s_barrier\n"
      "ds_read_b128 a[0:3], v118 offset:0\n"
      "ds_read_b128 a[4:7], v118 offset:4096\n"
      "ds_read_b128 a[8:11], v118 offset:8192\n"
      "ds_read_b128 a[12:15], v118 offset:12288\n"
      "ds_read_b128 a[16:19], v119 offset:0\n"
      "ds_read_b128 a[20:23], v119 offset:4096\n"
      "ds_read_b128 a[24:27], v119 offset:8192\n"
      "ds_read_b128 a[28:31], v119 offset:12288\n"
      "ds_read_b128 a[32:35], v118 offset:1024\n"
      "ds_read_b128 a[36:39], v118 offset:5120\n"
      "ds_read_b128 a[40:43], v118 offset:9216\n"
      "ds_read_b128 a[44:47], v118 offset:13312\n"
      "v_max_f32 v112, v112, v167\n"
      "v_max_f32 v113, v113, v175\n"
      "v_max_f32 v114, v114, v183\n"
      "v_max_f32 v115, v115, v191\n"
      "v_mov_b32 v208, v112\n"
      "v_mov_b32 v209, v113\n"
      "v_mov_b32 v210, v114\n"
      "v_mov_b32 v211, v115\n"
      "v_permlane16_swap_b32 v112, v208\n"
      "v_permlane16_swap_b32 v113, v209\n"
      "v_permlane16_swap_b32 v114, v210\n"
      "v_permlane16_swap_b32 v115, v211\n"
      "s_waitcnt lgkmcnt(8)\n"
      "ds_read_b128 a[48:51], v119 offset:1024\n"
      "ds_read_b128 a[52:55], v119 offset:5120\n"
      "ds_read_b128 a[56:59], v119 offset:9216\n"
      "ds_read_b128 a[60:63], v119 offset:13312\n"
      "v_max_f32 v112, v112, v208\n"
      "v_max_f32 v113, v113, v209\n"
      "v_max_f32 v114, v114, v210\n"
      "v_max_f32 v115, v115, v211\n"
      "v_mov_b32 v208, v112\n"
      "v_mov_b32 v209, v113\n"
      "v_mov_b32 v210, v114\n"
      "v_mov_b32 v211, v115\n"
      "v_permlane32_swap_b32 v112, v208\n"
      "v_permlane32_swap_b32 v113, v209\n"
      "v_permlane32_swap_b32 v114, v210\n"
      "v_permlane32_swap_b32 v115, v211\n"
      "v_max_f32 v112, v112, v208\n"
      "v_max_f32 v113, v113, v209\n"
      "v_max_f32 v114, v114, v210\n"
      "v_max_f32 v115, v115, v211\n"
      "v_mov_b32 v116, 0xc1800000\n"
      "v_fmamk_f32 v112, v112, 0xbe0293ee, v116\n"
      "v_fmamk_f32 v113, v113, 0xbe0293ee, v116\n"
      "v_fmamk_f32 v114, v114, 0xbe0293ee, v116\n"
      "v_fmamk_f32 v115, v115, 0xbe0293ee, v116\n"
      "v_floor_f32 v112, v112\n"
      "v_floor_f32 v113, v113\n"
      "v_floor_f32 v114, v114\n"
      "v_floor_f32 v115, v115\n"
      "s_waitcnt vmcnt(10) lgkmcnt(0)\n"
      "s_barrier\n"
      "ds_read_b128 a[64:67], v242 offset:0\n"
      "ds_read_b128 a[68:71], v242 offset:4096\n"
      "ds_read_b128 a[72:75], v243 offset:0\n"
      "ds_read_b128 a[76:79], v243 offset:4096\n"
      "ds_read_b128 a[80:83], v242 offset:1024\n"
      "ds_read_b128 a[84:87], v242 offset:5120\n"
      "ds_read_b128 a[88:91], v243 offset:1024\n"
      "ds_read_b128 a[92:95], v243 offset:5120\n"
      "v_min_f32 v224, 0x7f000000, v112\n"
      "v_min_f32 v228, 0x7f000000, v113\n"
      "v_min_f32 v232, 0x7f000000, v114\n"
      "v_min_f32 v236, 0x7f000000, v115\n"
      "v_add_f32 v112, %[neg], v224\n"
      "v_add_f32 v113, %[neg], v228\n"
      "v_add_f32 v114, %[neg], v232\n"
      "v_add_f32 v115, %[neg], v236\n"
      "v_mov_b32 v225, v224\n"
      "v_mov_b32 v229, v228\n"
      "v_mov_b32 v233, v232\n"
      "v_mov_b32 v237, v236\n"
      "v_mov_b64 v[226:227], v[224:225]\n"
      "v_mov_b64 v[230:231], v[228:229]\n"
      "v_mov_b64 v[234:235], v[232:233]\n"
      "v_mov_b64 v[238:239], v[236:237]\n"
      "s_mov_b32 s95, s94\n"
      "v_readfirstlane_b32 s72, v240\n"
      "v_readfirstlane_b32 s79, v241\n"
      "s_add_u32 m0, s77, 16384\n"
      "v_pk_fma_f32 v[160:161], v[160:161], s[94:95], v[112:113] op_sel:[0,0,0] op_sel_hi:[1,1,0]\n"
      "buffer_load_dwordx4 v249, %[srdv], s61 offen nt lds\n"
      "v_pk_fma_f32 v[162:163], v[162:163], s[94:95], v[112:113] op_sel:[0,0,0] op_sel_hi:[1,1,0]\n"
      "v_pk_fma_f32 v[164:165], v[164:165], s[94:95], v[112:113] op_sel:[0,0,0] op_sel_hi:[1,1,0]\n"
      "v_pk_fma_f32 v[166:167], v[166:167], s[94:95], v[112:113] op_sel:[0,0,0] op_sel_hi:[1,1,0]\n"
      "s_add_u32 m0, s77, 17408\n"
      "v_pk_fma_f32 v[168:169], v[168:169], s[94:95], v[112:113] op_sel:[0,0,1] op_sel_hi:[1,1,1]\n"
      "buffer_load_dwordx4 v121, %[srdv], s61 offen nt lds\n"
      "v_pk_fma_f32 v[170:171], v[170:171], s[94:95], v[112:113] op_sel:[0,0,1] op_sel_hi:[1,1,1]\n"
      "v_pk_fma_f32 v[172:173], v[172:173], s[94:95], v[112:113] op_sel:[0,0,1] op_sel_hi:[1,1,1]\n"
      "v_pk_fma_f32 v[174:175], v[174:175], s[94:95], v[112:113] op_sel:[0,0,1] op_sel_hi:[1,1,1]\n"
      "s_add_u32 m0, s77, 24576\n"
      "v_pk_fma_f32 v[176:177], v[176:177], s[94:95], v[114:115] op_sel:[0,0,0] op_sel_hi:[1,1,0]\n"
      "buffer_load_dwordx4 v122, %[srdv], s61 offen nt lds\n"
      "v_pk_fma_f32 v[178:179], v[178:179], s[94:95], v[114:115] op_sel:[0,0,0] op_sel_hi:[1,1,0]\n"
      "v_pk_fma_f32 v[180:181], v[180:181], s[94:95], v[114:115] op_sel:[0,0,0] op_sel_hi:[1,1,0]\n"
      "v_pk_fma_f32 v[182:183], v[182:183], s[94:95], v[114:115] op_sel:[0,0,0] op_sel_hi:[1,1,0]\n"
      "s_add_u32 m0, s77, 25600\n"
      "v_pk_fma_f32 v[184:185], v[184:185], s[94:95], v[114:115] op_sel:[0,0,1] op_sel_hi:[1,1,1]\n"
      "buffer_load_dwordx4 v123, %[srdv], s61 offen nt lds\n"
      "v_pk_fma_f32 v[186:187], v[186:187], s[94:95], v[114:115] op_sel:[0,0,1] op_sel_hi:[1,1,1]\n"
      "v_pk_fma_f32 v[188:189], v[188:189], s[94:95], v[114:115] op_sel:[0,0,1] op_sel_hi:[1,1,1]\n"
      "v_pk_fma_f32 v[190:191], v[190:191], s[94:95], v[114:115] op_sel:[0,0,1] op_sel_hi:[1,1,1]\n"
      "s_add_u32 s77, s77, 0x8000\n"
      "s_and_b32 s77, s77, 0x1ffff\n"
      "s_add_u32 s61, s61, %[inc]\n"
      "v_exp_f32 v112, v160\n"
      "v_exp_f32 v113, v161\n"
      "v_exp_f32 v114, v162\n"
      "v_cvt_pk_bf16_f32 v208, v112, v113\n"
      "v_exp_f32 v115, v163\n"
      "v_exp_f32 v112, v164\n"
      "v_cvt_pk_bf16_f32 v209, v114, v115\n"
      "v_exp_f32 v113, v165\n"
      "v_or_b32 v116, v208, v209\n"
      "v_exp_f32 v114, v166\n"
      "v_cvt_pk_bf16_f32 v210, v112, v113\n"
      "v_exp_f32 v115, v167\n"
      "v_exp_f32 v112, v168\n"
      "v_cvt_pk_bf16_f32 v211, v114, v115\n"
      "v_exp_f32 v113, v169\n"
      "v_or3_b32 v116, v116, v210, v211\n"
      "v_exp_f32 v114, v170\n"
      "v_cvt_pk_bf16_f32 v212, v112, v113\n"
      "v_exp_f32 v115, v171\n"
      "v_exp_f32 v112, v172\n"
      "v_cvt_pk_bf16_f32 v213, v114, v115\n"
      "v_exp_f32 v113, v173\n"
      "v_or3_b32 v116, v116, v212, v213\n"
      "s_waitcnt lgkmcnt(0)\n"
      "v_cvt_pk_bf16_f32 v214, v112, v113\n"
      "s_sub_u32 s63, %[n1], 1\n"
      "s_branch ENTRY_%=\n"
      "LOOP1_%=:\n"
      "s_waitcnt vmcnt(12) lgkmcnt(0)\n"
      "s_sub_u32 s67, s68, s90\n"
      "s_cmp_lt_i32 s67, 0\n"
      "s_cbranch_scc1 GW0_%=\n"
      "GX0_%=:\n"
      "s_barrier\n"
      "v_mfma_f32_16x16x32_bf16 v[40:43], a[96:99], v[216:219], v[40:43]\n"
      "s_memrealtime s[68:69]\n"
      "buffer_load_dword v250, v251, %[srdr], 0 offen sc1\n"
      "ds_read_b128 a[64:67], v242 offset:0\n"
      "v_exp_f32 v114, v166\n"
      "v_mfma_f32_16x16x32_bf16 v[44:47], a[96:99], v[220:223], v[44:47]\n"
      "s_add_u32 m0, s77, 16384\n"
      "ds_read_b128 a[68:71], v242 offset:4096\n"
      "ds_read_b128 a[72:75], v243 offset:0\n"
      "v_mfma_f32_16x16x32_bf16 v[60:63], a[100:103], v[220:223], v[60:63]\n"
      "buffer_load_dwordx4 v249, %[srdv], s61 offen nt lds\n"
      "v_exp_f32 v115, v167\n"
      "v_mfma_f32_16x16x32_bf16 v[56:59], a[100:103], v[216:219], v[56:59]\n"
      "s_add_u32 m0, s77, 17408\n"
      "ds_read_b128 a[76:79], v243 offset:4096\n"
      "v_cvt_pk_bf16_f32 v211, v114, v115\n"
      "v_mfma_f32_16x16x32_bf16 a[136:139], a[104:107], v[216:219], a[136:139]\n"
      "buffer_load_dwordx4 v121, %[srdv], s61 offen nt lds\n"
      "v_exp_f32 v112, v168\n"
      "v_mfma_f32_16x16x32_bf16 a[140:143], a[104:107], v[220:223], a[140:143]\n"
      "s_add_u32 m0, s77, 24576\n"
      "ds_read_b128 a[80:83], v242 offset:1024\n"
      "v_or3_b32 v116, v116, v210, v211\n"
      "v_mfma_f32_16x16x32_bf16 a[156:159], a[108:111], v[220:223], a[156:159]\n"
      "buffer_load_dwordx4 v122, %[srdv], s61 offen nt lds\n"
      "v_exp_f32 v113, v169\n"
      "v_mfma_f32_16x16x32_bf16 a[152:155], a[108:111], v[216:219], a[152:155]\n"
      "s_add_u32 m0, s77, 25600\n"
      "ds_read_b128 a[84:87], v242 offset:5120\n"
      "v_cvt_pk_bf16_f32 v212, v112, v113\n"
      "v_mfma_f32_16x16x32_bf16 v[72:75], a[112:115], v[216:219], v[72:75]\n"
      "s_add_u32 s77, s77, 0x8000\n"
      "buffer_load_dwordx4 v123, %[srdv], s61 offen nt lds\n"
      "v_exp_f32 v114, v170\n"
      "v_mfma_f32_16x16x32_bf16 v[76:79], a[112:115], v[220:223], v[76:79]\n"
      "s_and_b32 s77, s77, 0x1ffff\n"
      "ds_read_b128 a[88:91], v243 offset:1024\n"
      "ds_read_b128 a[92:95], v243 offset:5120\n"
      "v_mfma_f32_16x16x32_bf16 v[92:95], a[116:119], v[220:223], v[92:95]\n"
      "s_add_u32 s61, s61, %[inc]\n"
      "v_exp_f32 v115, v171\n"
      "v_mfma_f32_16x16x32_bf16 v[88:91], a[116:119], v[216:219], v[88:91]\n"
      "v_cvt_pk_bf16_f32 v213, v114, v115\n"
      "v_mfma_f32_16x16x32_bf16 a[168:171], a[120:123], v[216:219], a[168:171]\n"
      "v_exp_f32 v112, v172\n"
      "v_mfma_f32_16x16x32_bf16 a[172:175], a[120:123], v[220:223], a[172:175]\n"
      "v_or3_b32 v116, v116, v212, v213\n"
      "v_mfma_f32_16x16x32_bf16 a[188:191], a[124:127], v[220:223], a[188:191]\n"
      "v_exp_f32 v113, v173\n"
      "v_mfma_f32_16x16x32_bf16 a[184:187], a[124:127], v[216:219], a[184:187]\n"
      "s_waitcnt lgkmcnt(0)\n"
      "v_cvt_pk_bf16_f32 v214, v112, v113\n"
      "ENTRY_%=:\n"
      "v_mfma_f32_16x16x32_bf16 v[128:131], a[64:67], a[0:3], v[224:227]\n"
      "s_sub_u32 s67, s68, s66\n"
      "v_exp_f32 v114, v174\n"
      "v_mfma_f32_16x16x32_bf16 v[128:131], a[72:75], a[16:19], v[128:131]\n"
      "s_cmp_gt_i32 s67, s70\n"
      "ds_read_b64_tr_b16 a[96:97], v244 offset:0\n"
      "ds_read_b64_tr_b16 a[98:99], v244 offset:4096\n"
      "v_mfma_f32_16x16x32_bf16 v[128:131], a[80:83], a[32:35], v[128:131]\n"
      "s_cselect_b32 s69, s86, 0\n"
      "v_exp_f32 v115, v175\n"
      "v_mfma_f32_16x16x32_bf16 v[128:131], a[88:91], a[48:51], v[128:131]\n"
      "s_add_u32 s66, s66, s69\n"
      "ds_read_b64_tr_b16 a[100:101], v245 offset:0\n"
      "v_cvt_pk_bf16_f32 v215, v114, v115\n"
      "v_mfma_f32_16x16x32_bf16 v[132:135], a[92:95], a[48:51], v[224:227]\n"
      "s_add_u32 s67, s67, s101\n"
      "v_exp_f32 v112, v176\n"
      "v_mfma_f32_16x16x32_bf16 v[132:135], a[84:87], a[32:35], v[132:135]\n"
      "s_min_i32 s70, s70, s67\n"
      "ds_read_b64_tr_b16 a[102:103], v245 offset:4096\n"
      "v_or3_b32 v116, v116, v214, v215\n"
      "v_mfma_f32_16x16x32_bf16 v[132:135], a[76:79], a[16:19], v[132:135]\n"
      "s_max_i32 s70, s70, 85\n"
      "v_exp_f32 v113, v177\n"
      "v_mfma_f32_16x16x32_bf16 v[132:135], a[68:71], a[0:3], v[132:135]\n"
      "s_add_u32 s66, s66, s86\n"
      "ds_read_b64_tr_b16 a[104:105], v246 offset:0\n"
      "v_cvt_pk_bf16_f32 v216, v112, v113\n"
      "v_mfma_f32_16x16x32_bf16 v[140:143], a[68:71], a[4:7], v[228:231]\n"
      "s_and_b32 s76, s80, 31\n"
      "v_exp_f32 v114, v178\n"
      "v_mfma_f32_16x16x32_bf16 v[140:143], a[76:79], a[20:23], v[140:143]\n"
      "s_lshr_b64 s[80:81], s[80:81], 5\n"
      "ds_read_b64_tr_b16 a[106:107], v246 offset:4096\n"
      "ds_read_b64_tr_b16 a[108:109], v247 offset:0\n"
      "v_mfma_f32_16x16x32_bf16 v[140:143], a[84:87], a[36:39], v[140:143]\n"
      "s_add_u32 s66, s66, s76\n"
      "v_exp_f32 v115, v179\n"
      "v_mfma_f32_16x16x32_bf16 v[140:143], a[92:95], a[52:55], v[140:143]\n"
      "s_sub_u32 s67, s68, s82\n"
      "ds_read_b64_tr_b16 a[110:111], v247 offset:4096\n"
      "v_cvt_pk_bf16_f32 v217, v114, v115\n"
      "v_mfma_f32_16x16x32_bf16 v[136:139], a[88:91], a[52:55], v[228:231]\n"
      "s_min_i32 s76, s67, s83\n"
      "v_exp_f32 v112, v180\n"
      "v_mfma_f32_16x16x32_bf16 v[136:139], a[80:83], a[36:39], v[136:139]\n"
      "s_mov_b32 s83, s67\n"
      "ds_read_b64_tr_b16 a[112:113], v244 offset:1024\n"
      "v_or3_b32 v116, v116, v216, v217\n"
      "v_mfma_f32_16x16x32_bf16 v[136:139], a[72:75], a[20:23], v[136:139]\n"
      "s_sub_u32 s76, s76, 36\n"
      "v_exp_f32 v113, v181\n"
      "v_mfma_f32_16x16x32_bf16 v[136:139], a[64:67], a[4:7], v[136:139]\n"
      "s_min_i32 s76, s76, 148\n"
      "ds_read_b64_tr_b16 a[114:115], v244 offset:5120\n"
      "v_cvt_pk_bf16_f32 v218, v112, v113\n"
      "v_mfma_f32_16x16x32_bf16 v[144:147], a[64:67], a[8:11], v[232:235]\n"
      "s_max_i32 s76, s76, 70\n"
      "v_exp_f32 v114, v182\n"
      "v_mfma_f32_16x16x32_bf16 v[144:147], a[72:75], a[24:27], v[144:147]\n"
      "s_mov_b32 s82, s68\n"
      "ds_read_b64_tr_b16 a[116:117], v245 offset:1024\n"
      "ds_read_b64_tr_b16 a[118:119], v245 offset:5120\n"
      "v_mfma_f32_16x16x32_bf16 v[144:147], a[80:83], a[40:43], v[144:147]\n"
      "s_add_u32 s65, s82, s76\n"
      "v_exp_f32 v115, v183\n"
      "v_mfma_f32_16x16x32_bf16 v[144:147], a[88:91], a[56:59], v[144:147]\n"
      "s_sub_u32 s93, s93, 1\n"
      "ds_read_b64_tr_b16 a[120:121], v246 offset:1024\n"
      "v_cvt_pk_bf16_f32 v219, v114, v115\n"
      "v_mfma_f32_16x16x32_bf16 v[148:151], a[92:95], a[56:59], v[232:235]\n"
      "s_cmp_le_i32 s93, 14\n"
      "v_exp_f32 v112, v184\n"
      "v_mfma_f32_16x16x32_bf16 v[148:151], a[84:87], a[40:43], v[148:151]\n"
      "s_cselect_b32 s86, 107, s86\n"
      "ds_read_b64_tr_b16 a[122:123], v246 offset:5120\n"
      "v_or3_b32 v116, v116, v218, v219\n"
      "v_mfma_f32_16x16x32_bf16 v[148:151], a[76:79], a[24:27], v[148:151]\n"
      "s_sub_u32 s67, s65, s66\n"
      "v_exp_f32 v113, v185\n"
      "v_mfma_f32_16x16x32_bf16 v[148:151], a[68:71], a[8:11], v[148:151]\n"
      "s_cmp_lt_i32 s67, 0\n"
      "ds_read_b64_tr_b16 a[124:125], v247 offset:1024\n"
      "v_cvt_pk_bf16_f32 v220, v112, v113\n"
      "v_mfma_f32_16x16x32_bf16 v[156:159], a[68:71], a[12:15], v[236:239]\n"
      "s_cselect_b32 s88, s66, s65\n"
      "v_exp_f32 v114, v186\n"
      "v_mfma_f32_16x16x32_bf16 v[156:159], a[76:79], a[28:31], v[156:159]\n"
      "s_add_u32 s90, s88, s91\n"
      "ds_read_b64_tr_b16 a[126:127], v247 offset:5120\n"
      "v_mfma_f32_16x16x32_bf16 v[156:159], a[84:87], a[44:47], v[156:159]\n"
      "v_exp_f32 v115, v187\n"
      "v_mfma_f32_16x16x32_bf16 v[156:159], a[92:95], a[60:63], v[156:159]\n"
      "v_cvt_pk_bf16_f32 v221, v114, v115\n"
      "v_mfma_f32_16x16x32_bf16 v[152:155], a[88:91], a[60:63], v[236:239]\n"
      "v_exp_f32 v112, v188\n"
      "v_mfma_f32_16x16x32_bf16 v[152:155], a[80:83], a[44:47], v[152:155]\n"
      "v_or3_b32 v116, v116, v220, v221\n"
      "v_mfma_f32_16x16x32_bf16 v[152:155], a[72:75], a[28:31], v[152:155]\n"
      "v_exp_f32 v113, v189\n"
      "v_mfma_f32_16x16x32_bf16 v[152:155], a[64:67], a[12:15], v[152:155]\n"
      "s_waitcnt lgkmcnt(10)\n"
      "v_cvt_pk_bf16_f32 v222, v112, v113\n"
      "v_mfma_f32_16x16x32_bf16 v[32:35], a[96:99], v[192:195], v[32:35]\n"
      "v_exp_f32 v114, v190\n"
      "v_mfma_f32_16x16x32_bf16 v[48:51], a[100:103], v[192:195], v[48:51]\n"
      "v_mfma_f32_16x16x32_bf16 a[128:131], a[104:107], v[192:195], a[128:131]\n"
      "s_waitcnt lgkmcnt(6)\n"
      "v_exp_f32 v115, v191\n"
      "v_mfma_f32_16x16x32_bf16 a[144:147], a[108:111], v[192:195], a[144:147]\n"
      "v_cvt_pk_bf16_f32 v223, v114, v115\n"
      "v_mfma_f32_16x16x32_bf16 v[64:67], a[112:115], v[192:195], v[64:67]\n"
      "s_waitcnt lgkmcnt(2)\n"
      "v_mfma_f32_16x16x32_bf16 v[80:83], a[116:119], v[192:195], v[80:83]\n"
      "v_or3_b32 v116, v116, v222, v223\n"
      "v_mfma_f32_16x16x32_bf16 a[160:163], a[120:123], v[192:195], a[160:163]\n"
      "s_mov_b32 s64, 0\n"
      "s_waitcnt lgkmcnt(0)\n"
      "v_and_b32 v116, 0x40004000, v116\n"
      "v_mfma_f32_16x16x32_bf16 a[176:179], a[124:127], v[192:195], a[176:179]\n"
      "v_cmp_ne_u32 vcc, 0, v116\n"
      "s_cbranch_vccnz FB1_%=\n"
      "RET1_0_%=:\n"
      "v_mfma_f32_16x16x32_bf16 a[180:183], a[124:127], v[196:199], a[180:183]\n"
      "v_exp_f32 v112, v128\n"
      "s_barrier\n"
      "v_mfma_f32_16x16x32_bf16 a[164:167], a[120:123], v[196:199], a[164:167]\n"
      "v_mfma_f32_16x16x32_bf16 a[192:195], a[196:199], v[208:211], a[192:195]\n"
      "s_add_u32 m0, s62, 0\n"
      "v_exp_f32 v113, v129\n"
      "v_mfma_f32_16x16x32_bf16 v[84:87], a[116:119], v[196:199], v[84:87]\n"
      "buffer_load_dwordx4 v248, %[srdk], s60 offen nt lds\n"
      "v_cvt_pk_bf16_f32 v192, v112, v113\n"
      "v_mfma_f32_16x16x32_bf16 v[68:71], a[112:115], v[196:199], v[68:71]\n"
      "s_add_u32 m0, s62, 896\n"
      "v_exp_f32 v114, v130\n"
      "v_mfma_f32_16x16x32_bf16 a[192:195], a[200:203], v[212:215], a[192:195]\n"
      "buffer_load_dwordx4 v248, %[srdk], s60 offen offset:128 nt lds\n"
      "v_mfma_f32_16x16x32_bf16 a[148:151], a[108:111], v[196:199], a[148:151]\n"
      "s_add_u32 m0, s62, 8192\n"
      "v_exp_f32 v115, v131\n"
      "v_mfma_f32_16x16x32_bf16 a[132:135], a[104:107], v[196:199], a[132:135]\n"
      "s_memrealtime s[68:69]\n"
      "buffer_load_dwordx4 v120, %[srdk], s60 offen nt lds\n"
      "v_cvt_pk_bf16_f32 v193, v114, v115\n"
      "v_mfma_f32_16x16x32_bf16 a[192:195], a[204:207], v[216:219], a[192:195]\n"
      "s_add_u32 m0, s62, 9088\n"
      "v_exp_f32 v112, v132\n"
      "v_mfma_f32_16x16x32_bf16 v[52:55], a[100:103], v[196:199], v[52:55]\n"
      "s_add_u32 s62, s62, 0x8000\n"
      "buffer_load_dwordx4 v120, %[srdk], s60 offen offset:128 nt lds\n"
      "v_or_b32 v116, v192, v193\n"
      "v_mfma_f32_16x16x32_bf16 v[36:39], a[96:99], v[196:199], v[36:39]\n"
      "s_and_b32 s62, s62, 0x1ffff\n"
      "v_exp_f32 v113, v133\n"
      "v_mfma_f32_16x16x32_bf16 a[192:195], a[208:211], v[220:223], a[192:195]\n"
      "s_add_u32 s60, s60, s78\n"
      "v_cvt_pk_bf16_f32 v194, v112, v113\n"
      "s_waitcnt vmcnt(12) lgkmcnt(0)\n"
      "s_sub_u32 s67, s68, s90\n"
      "s_cmp_lt_i32 s67, 0\n"
      "s_cbranch_scc1 GW1_%=\n"
      "GX1_%=:\n"
      "s_barrier\n"
      "v_mfma_f32_16x16x32_bf16 v[40:43], a[96:99], v[200:203], v[40:43]\n"
      "s_memrealtime s[68:69]\n"
      "ds_read_b128 a[64:67], v242 offset:32768\n"
      "v_exp_f32 v114, v134\n"
      "v_mfma_f32_16x16x32_bf16 v[44:47], a[96:99], v[204:207], v[44:47]\n"
      "s_add_u32 m0, s77, 16384\n"
      "ds_read_b128 a[68:71], v242 offset:36864\n"
      "ds_read_b128 a[72:75], v243 offset:32768\n"
      "v_mfma_f32_16x16x32_bf16 v[60:63], a[100:103], v[204:207], v[60:63]\n"
      "buffer_load_dwordx4 v249, %[srdv], s61 offen nt lds\n"
      "v_exp_f32 v115, v135\n"
      "v_mfma_f32_16x16x32_bf16 v[56:59], a[100:103], v[200:203], v[56:59]\n"
      "s_add_u32 m0, s77, 17408\n"
      "ds_read_b128 a[76:79], v243 offset:36864\n"
      "v_cvt_pk_bf16_f32 v195, v114, v115\n"
      "v_mfma_f32_16x16x32_bf16 a[136:139], a[104:107], v[200:203], a[136:139]\n"
      "buffer_load_dwordx4 v121, %[srdv], s61 offen nt lds\n"
      "v_exp_f32 v112, v136\n"
      "v_mfma_f32_16x16x32_bf16 a[140:143], a[104:107], v[204:207], a[140:143]\n"
      "s_add_u32 m0, s77, 24576\n"
      "ds_read_b128 a[80:83], v242 offset:33792\n"
      "v_or3_b32 v116, v116, v194, v195\n"
      "v_mfma_f32_16x16x32_bf16 a[156:159], a[108:111], v[204:207], a[156:159]\n"
      "buffer_load_dwordx4 v122, %[srdv], s61 offen nt lds\n"
      "v_exp_f32 v113, v137\n"
      "v_mfma_f32_16x16x32_bf16 a[152:155], a[108:111], v[200:203], a[152:155]\n"
      "s_add_u32 m0, s77, 25600\n"
      "ds_read_b128 a[84:87], v242 offset:37888\n"
      "v_cvt_pk_bf16_f32 v196, v112, v113\n"
      "v_mfma_f32_16x16x32_bf16 v[72:75], a[112:115], v[200:203], v[72:75]\n"
      "s_add_u32 s77, s77, 0x8000\n"
      "buffer_load_dwordx4 v123, %[srdv], s61 offen nt lds\n"
      "v_exp_f32 v114, v138\n"
      "v_mfma_f32_16x16x32_bf16 v[76:79], a[112:115], v[204:207], v[76:79]\n"
      "s_and_b32 s77, s77, 0x1ffff\n"
      "ds_read_b128 a[88:91], v243 offset:33792\n"
      "ds_read_b128 a[92:95], v243 offset:37888\n"
      "v_mfma_f32_16x16x32_bf16 v[92:95], a[116:119], v[204:207], v[92:95]\n"
      "s_add_u32 s61, s61, s78\n"
      "v_exp_f32 v115, v139\n"
      "v_mfma_f32_16x16x32_bf16 v[88:91], a[116:119], v[200:203], v[88:91]\n"
      "v_cvt_pk_bf16_f32 v197, v114, v115\n"
      "v_mfma_f32_16x16x32_bf16 a[168:171], a[120:123], v[200:203], a[168:171]\n"
      "v_exp_f32 v112, v140\n"
      "v_mfma_f32_16x16x32_bf16 a[172:175], a[120:123], v[204:207], a[172:175]\n"
      "v_or3_b32 v116, v116, v196, v197\n"
      "v_mfma_f32_16x16x32_bf16 a[188:191], a[124:127], v[204:207], a[188:191]\n"
      "v_exp_f32 v113, v141\n"
      "v_mfma_f32_16x16x32_bf16 a[184:187], a[124:127], v[200:203], a[184:187]\n"
      "s_waitcnt lgkmcnt(0)\n"
      "v_cvt_pk_bf16_f32 v198, v112, v113\n"
      "v_mfma_f32_16x16x32_bf16 v[160:163], a[64:67], a[0:3], v[224:227]\n"
      "s_sub_u32 s67, s68, s66\n"
      "v_exp_f32 v114, v142\n"
      "v_mfma_f32_16x16x32_bf16 v[160:163], a[72:75], a[16:19], v[160:163]\n"
      "s_cmp_gt_i32 s67, s70\n"
      "ds_read_b64_tr_b16 a[96:97], v244 offset:32768\n"
      "ds_read_b64_tr_b16 a[98:99], v244 offset:36864\n"
      "v_mfma_f32_16x16x32_bf16 v[160:163], a[80:83], a[32:35], v[160:163]\n"
      "s_cselect_b32 s69, s86, 0\n"
      "v_exp_f32 v115, v143\n"
      "v_mfma_f32_16x16x32_bf16 v[160:163], a[88:91], a[48:51], v[160:163]\n"
      "s_add_u32 s66, s66, s69\n"
      "ds_read_b64_tr_b16 a[100:101], v245 offset:32768\n"
      "v_cvt_pk_bf16_f32 v199, v114, v115\n"
      "v_mfma_f32_16x16x32_bf16 v[164:167], a[92:95], a[48:51], v[224:227]\n"
      "s_add_u32 s67, s67, s101\n"
      "v_exp_f32 v112, v144\n"
      "v_mfma_f32_16x16x32_bf16 v[164:167], a[84:87], a[32:35], v[164:167]\n"
      "s_min_i32 s70, s70, s67\n"
      "ds_read_b64_tr_b16 a[102:103], v245 offset:36864\n"
      "v_or3_b32 v116, v116, v198, v199\n"
      "v_mfma_f32_16x16x32_bf16 v[164:167], a[76:79], a[16:19], v[164:167]\n"
      "s_max_i32 s70, s70, 85\n"
      "v_exp_f32 v113, v145\n"
      "v_mfma_f32_16x16x32_bf16 v[164:167], a[68:71], a[0:3], v[164:167]\n"
      "s_add_u32 s66, s66, s86\n"
      "ds_read_b64_tr_b16 a[104:105], v246 offset:32768\n"
      "v_cvt_pk_bf16_f32 v200, v112, v113\n"
      "v_mfma_f32_16x16x32_bf16 v[172:175], a[68:71], a[4:7], v[228:231]\n"
      "s_and_b32 s76, s80, 31\n"
      "v_exp_f32 v114, v146\n"
      "v_mfma_f32_16x16x32_bf16 v[172:175], a[76:79], a[20:23], v[172:175]\n"
      "s_lshr_b64 s[80:81], s[80:81], 5\n"
      "ds_read_b64_tr_b16 a[106:107], v246 offset:36864\n"
      "ds_read_b64_tr_b16 a[108:109], v247 offset:32768\n"
      "v_mfma_f32_16x16x32_bf16 v[172:175], a[84:87], a[36:39], v[172:175]\n"
      "s_add_u32 s66, s66, s76\n"
      "v_exp_f32 v115, v147\n"
      "v_mfma_f32_16x16x32_bf16 v[172:175], a[92:95], a[52:55], v[172:175]\n"
      "s_sub_u32 s67, s68, s82\n"
      "ds_read_b64_tr_b16 a[110:111], v247 offset:36864\n"
      "v_cvt_pk_bf16_f32 v201, v114, v115\n"
      "v_mfma_f32_16x16x32_bf16 v[168:171], a[88:91], a[52:55], v[228:231]\n"
      "s_min_i32 s76, s67, s83\n"
      "v_exp_f32 v112, v148\n"
      "v_mfma_f32_16x16x32_bf16 v[168:171], a[80:83], a[36:39], v[168:171]\n"
      "s_mov_b32 s83, s67\n"
      "ds_read_b64_tr_b16 a[112:113], v244 offset:33792\n"
      "v_or3_b32 v116, v116, v200, v201\n"
      "v_mfma_f32_16x16x32_bf16 v[168:171], a[72:75], a[20:23], v[168:171]\n"
      "s_sub_u32 s76, s76, 36\n"
      "v_exp_f32 v113, v149\n"
      "v_mfma_f32_16x16x32_bf16 v[168:171], a[64:67], a[4:7], v[168:171]\n"
      "s_min_i32 s76, s76, 148\n"
      "ds_read_b64_tr_b16 a[114:115], v244 offset:37888\n"
      "v_cvt_pk_bf16_f32 v202, v112, v113\n"
      "v_mfma_f32_16x16x32_bf16 v[176:179], a[64:67], a[8:11], v[232:235]\n"
      "s_max_i32 s76, s76, 70\n"
      "v_exp_f32 v114, v150\n"
      "v_mfma_f32_16x16x32_bf16 v[176:179], a[72:75], a[24:27], v[176:179]\n"
      "s_mov_b32 s82, s68\n"
      "ds_read_b64_tr_b16 a[116:117], v245 offset:33792\n"
      "ds_read_b64_tr_b16 a[118:119], v245 offset:37888\n"
      "v_mfma_f32_16x16x32_bf16 v[176:179], a[80:83], a[40:43], v[176:179]\n"
      "s_add_u32 s65, s82, s76\n"
      "v_exp_f32 v115, v151\n"
      "v_mfma_f32_16x16x32_bf16 v[176:179], a[88:91], a[56:59], v[176:179]\n"
      "s_sub_u32 s93, s93, 1\n"
      "ds_read_b64_tr_b16 a[120:121], v246 offset:33792\n"
      "v_cvt_pk_bf16_f32 v203, v114, v115\n"
      "v_mfma_f32_16x16x32_bf16 v[180:183], a[92:95], a[56:59], v[232:235]\n"
      "s_cmp_le_i32 s93, 14\n"
      "v_exp_f32 v112, v152\n"
      "v_mfma_f32_16x16x32_bf16 v[180:183], a[84:87], a[40:43], v[180:183]\n"
      "s_cselect_b32 s86, 107, s86\n"
      "ds_read_b64_tr_b16 a[122:123], v246 offset:37888\n"
      "v_or3_b32 v116, v116, v202, v203\n"
      "v_mfma_f32_16x16x32_bf16 v[180:183], a[76:79], a[24:27], v[180:183]\n"
      "s_sub_u32 s67, s65, s66\n"
      "v_exp_f32 v113, v153\n"
      "v_mfma_f32_16x16x32_bf16 v[180:183], a[68:71], a[8:11], v[180:183]\n"
      "s_cmp_lt_i32 s67, 0\n"
      "ds_read_b64_tr_b16 a[124:125], v247 offset:33792\n"
      "v_cvt_pk_bf16_f32 v204, v112, v113\n"
      "v_mfma_f32_16x16x32_bf16 v[188:191], a[68:71], a[12:15], v[236:239]\n"
      "s_cselect_b32 s88, s66, s65\n"
      "v_exp_f32 v114, v154\n"
      "v_mfma_f32_16x16x32_bf16 v[188:191], a[76:79], a[28:31], v[188:191]\n"
      "s_add_u32 s90, s88, s91\n"
      "ds_read_b64_tr_b16 a[126:127], v247 offset:37888\n"
      "v_mfma_f32_16x16x32_bf16 v[188:191], a[84:87], a[44:47], v[188:191]\n"
      "v_exp_f32 v115, v155\n"
      "v_mfma_f32_16x16x32_bf16 v[188:191], a[92:95], a[60:63], v[188:191]\n"
      "v_cvt_pk_bf16_f32 v205, v114, v115\n"
      "v_mfma_f32_16x16x32_bf16 v[184:187], a[88:91], a[60:63], v[236:239]\n"
      "v_exp_f32 v112, v156\n"
      "v_mfma_f32_16x16x32_bf16 v[184:187], a[80:83], a[44:47], v[184:187]\n"
      "v_or3_b32 v116, v116, v204, v205\n"
      "v_mfma_f32_16x16x32_bf16 v[184:187], a[72:75], a[28:31], v[184:187]\n"
      "v_exp_f32 v113, v157\n"
      "v_mfma_f32_16x16x32_bf16 v[184:187], a[64:67], a[12:15], v[184:187]\n"
      "s_waitcnt lgkmcnt(10)\n"
      "v_cvt_pk_bf16_f32 v206, v112, v113\n"
      "v_mfma_f32_16x16x32_bf16 v[32:35], a[96:99], v[208:211], v[32:35]\n"
      "v_exp_f32 v114, v158\n"
      "v_mfma_f32_16x16x32_bf16 v[48:51], a[100:103], v[208:211], v[48:51]\n"
      "v_mfma_f32_16x16x32_bf16 a[128:131], a[104:107], v[208:211], a[128:131]\n"
      "s_waitcnt lgkmcnt(6)\n"
      "v_exp_f32 v115, v159\n"
      "v_mfma_f32_16x16x32_bf16 a[144:147], a[108:111], v[208:211], a[144:147]\n"
      "v_cvt_pk_bf16_f32 v207, v114, v115\n"
      "v_mfma_f32_16x16x32_bf16 v[64:67], a[112:115], v[208:211], v[64:67]\n"
      "s_waitcnt lgkmcnt(2)\n"
      "v_mfma_f32_16x16x32_bf16 v[80:83], a[116:119], v[208:211], v[80:83]\n"
      "v_or3_b32 v116, v116, v206, v207\n"
      "v_mfma_f32_16x16x32_bf16 a[160:163], a[120:123], v[208:211], a[160:163]\n"
      "s_mov_b32 s64, 0\n"
      "s_waitcnt lgkmcnt(0)\n"
      "v_and_b32 v116, 0x40004000, v116\n"
      "v_mfma_f32_16x16x32_bf16 a[176:179], a[124:127], v[208:211], a[176:179]\n"
      "v_cmp_ne_u32 vcc, 0, v116\n"
      "s_cbranch_vccnz FB0_%=\n"
      "RET0_0_%=:\n"
      "v_mfma_f32_16x16x32_bf16 a[180:183], a[124:127], v[212:215], a[180:183]\n"
      "v_exp_f32 v112, v160\n"
      "s_barrier\n"
      "v_mfma_f32_16x16x32_bf16 a[164:167], a[120:123], v[212:215], a[164:167]\n"
      "v_xor_b32 v242, 0x10000, v242\n"
      "v_mfma_f32_16x16x32_bf16 a[192:195], a[196:199], v[192:195], a[192:195]\n"
      "s_add_u32 m0, s62, 0\n"
      "v_exp_f32 v113, v161\n"
      "v_mfma_f32_16x16x32_bf16 v[84:87], a[116:119], v[212:215], v[84:87]\n"
      "buffer_load_dwordx4 v248, %[srdk], s60 offen nt lds\n"
      "v_cvt_pk_bf16_f32 v208, v112, v113\n"
      "v_xor_b32 v243, 0x10000, v243\n"
      "v_mfma_f32_16x16x32_bf16 v[68:71], a[112:115], v[212:215], v[68:71]\n"
      "s_add_u32 m0, s62, 896\n"
      "v_exp_f32 v114, v162\n"
      "v_mfma_f32_16x16x32_bf16 a[192:195], a[200:203], v[196:199], a[192:195]\n"
      "buffer_load_dwordx4 v248, %[srdk], s60 offen offset:128 nt lds\n"
      "v_xor_b32 v244, 0x10000, v244\n"
      "v_mfma_f32_16x16x32_bf16 a[148:151], a[108:111], v[212:215], a[148:151]\n"
      "s_add_u32 m0, s62, 8192\n"
      "v_exp_f32 v115, v163\n"
      "v_mfma_f32_16x16x32_bf16 a[132:135], a[104:107], v[212:215], a[132:135]\n"
      "s_memrealtime s[68:69]\n"
      "buffer_load_dwordx4 v120, %[srdk], s60 offen nt lds\n"
      "v_cvt_pk_bf16_f32 v209, v114, v115\n"
      "v_xor_b32 v245, 0x10000, v245\n"
      "v_mfma_f32_16x16x32_bf16 a[192:195], a[204:207], v[200:203], a[192:195]\n"
      "s_add_u32 m0, s62, 9088\n"
      "v_exp_f32 v112, v164\n"
      "v_mfma_f32_16x16x32_bf16 v[52:55], a[100:103], v[212:215], v[52:55]\n"
      "s_add_u32 s62, s62, 0x8000\n"
      "buffer_load_dwordx4 v120, %[srdk], s60 offen offset:128 nt lds\n"
      "v_or_b32 v116, v208, v209\n"
      "v_xor_b32 v246, 0x10000, v246\n"
      "v_mfma_f32_16x16x32_bf16 v[36:39], a[96:99], v[212:215], v[36:39]\n"
      "s_and_b32 s62, s62, 0x1ffff\n"
      "s_add_u32 s60, s60, %[inc]\n"
      "v_exp_f32 v113, v165\n"
      "v_mfma_f32_16x16x32_bf16 a[192:195], a[208:211], v[204:207], a[192:195]\n"
      "s_sub_u32 s63, s63, 1\n"
      "v_cvt_pk_bf16_f32 v210, v112, v113\n"
      "v_xor_b32 v247, 0x10000, v247\n"
      "s_cbranch_scc0 LOOP1_%=\n"
      "s_waitcnt vmcnt(12) lgkmcnt(0)\n"
      "s_sub_u32 s67, s68, s90\n"
      "s_cmp_lt_i32 s67, 0\n"
      "s_cbranch_scc1 GW2_%=\n"
      "GX2_%=:\n"
      "s_barrier\n"
      "v_mfma_f32_16x16x32_bf16 v[40:43], a[96:99], v[216:219], v[40:43]\n"
      "s_memrealtime s[68:69]\n"
      "buffer_load_dword v250, v251, %[srdr], 0 offen sc1\n"
      "ds_read_b128 a[64:67], v242 offset:0\n"
      "v_exp_f32 v114, v166\n"
      "v_mfma_f32_16x16x32_bf16 v[44:47], a[96:99], v[220:223], v[44:47]\n"
      "s_add_u32 m0, s77, 16384\n"
      "ds_read_b128 a[68:71], v242 offset:4096\n"
      "ds_read_b128 a[72:75], v243 offset:0\n"
      "v_mfma_f32_16x16x32_bf16 v[60:63], a[100:103], v[220:223], v[60:63]\n"
      "buffer_load_dwordx4 v249, %[srdv], s61 offen nt lds\n"
      "v_exp_f32 v115, v167\n"
      "v_mfma_f32_16x16x32_bf16 v[56:59], a[100:103], v[216:219], v[56:59]\n"
      "s_add_u32 m0, s77, 17408\n"
      "ds_read_b128 a[76:79], v243 offset:4096\n"
      "v_cvt_pk_bf16_f32 v211, v114, v115\n"
      "v_mfma_f32_16x16x32_bf16 a[136:139], a[104:107], v[216:219], a[136:139]\n"
      "buffer_load_dwordx4 v121, %[srdv], s61 offen nt lds\n"
      "v_exp_f32 v112, v168\n"
      "v_mfma_f32_16x16x32_bf16 a[140:143], a[104:107], v[220:223], a[140:143]\n"
      "s_add_u32 m0, s77, 24576\n"
      "ds_read_b128 a[80:83], v242 offset:1024\n"
      "v_or3_b32 v116, v116, v210, v211\n"
      "v_mfma_f32_16x16x32_bf16 a[156:159], a[108:111], v[220:223], a[156:159]\n"
      "buffer_load_dwordx4 v122, %[srdv], s61 offen nt lds\n"
      "v_exp_f32 v113, v169\n"
      "v_mfma_f32_16x16x32_bf16 a[152:155], a[108:111], v[216:219], a[152:155]\n"
      "s_add_u32 m0, s77, 25600\n"
      "ds_read_b128 a[84:87], v242 offset:5120\n"
      "v_cvt_pk_bf16_f32 v212, v112, v113\n"
      "v_mfma_f32_16x16x32_bf16 v[72:75], a[112:115], v[216:219], v[72:75]\n"
      "s_add_u32 s77, s77, 0x8000\n"
      "buffer_load_dwordx4 v123, %[srdv], s61 offen nt lds\n"
      "v_exp_f32 v114, v170\n"
      "v_mfma_f32_16x16x32_bf16 v[76:79], a[112:115], v[220:223], v[76:79]\n"
      "s_and_b32 s77, s77, 0x1ffff\n"
      "ds_read_b128 a[88:91], v243 offset:1024\n"
      "ds_read_b128 a[92:95], v243 offset:5120\n"
      "v_mfma_f32_16x16x32_bf16 v[92:95], a[116:119], v[220:223], v[92:95]\n"
      "s_add_u32 s61, s61, %[inc]\n"
      "v_exp_f32 v115, v171\n"
      "v_mfma_f32_16x16x32_bf16 v[88:91], a[116:119], v[216:219], v[88:91]\n"
      "v_cvt_pk_bf16_f32 v213, v114, v115\n"
      "v_mfma_f32_16x16x32_bf16 a[168:171], a[120:123], v[216:219], a[168:171]\n"
      "v_exp_f32 v112, v172\n"
      "v_mfma_f32_16x16x32_bf16 a[172:175], a[120:123], v[220:223], a[172:175]\n"
      "v_or3_b32 v116, v116, v212, v213\n"
      "v_mfma_f32_16x16x32_bf16 a[188:191], a[124:127], v[220:223], a[188:191]\n"
      "v_exp_f32 v113, v173\n"
      "v_mfma_f32_16x16x32_bf16 a[184:187], a[124:127], v[216:219], a[184:187]\n"
      "s_waitcnt lgkmcnt(0)\n"
      "v_cvt_pk_bf16_f32 v214, v112, v113\n"
      "v_mfma_f32_16x16x32_bf16 v[128:131], a[64:67], a[0:3], v[224:227]\n"
      "s_sub_u32 s67, s68, s66\n"
      "v_exp_f32 v114, v174\n"
      "v_mfma_f32_16x16x32_bf16 v[128:131], a[72:75], a[16:19], v[128:131]\n"
      "s_cmp_gt_i32 s67, s70\n"
      "ds_read_b64_tr_b16 a[96:97], v244 offset:0\n"
      "ds_read_b64_tr_b16 a[98:99], v244 offset:4096\n"
      "v_mfma_f32_16x16x32_bf16 v[128:131], a[80:83], a[32:35], v[128:131]\n"
      "s_cselect_b32 s69, s86, 0\n"
      "v_exp_f32 v115, v175\n"
      "v_mfma_f32_16x16x32_bf16 v[128:131], a[88:91], a[48:51], v[128:131]\n"
      "s_add_u32 s66, s66, s69\n"
      "ds_read_b64_tr_b16 a[100:101], v245 offset:0\n"
      "v_cvt_pk_bf16_f32 v215, v114, v115\n"
      "v_mfma_f32_16x16x32_bf16 v[132:135], a[92:95], a[48:51], v[224:227]\n"
      "s_add_u32 s67, s67, s101\n"
      "v_exp_f32 v112, v176\n"
      "v_mfma_f32_16x16x32_bf16 v[132:135], a[84:87], a[32:35], v[132:135]\n"
      "s_min_i32 s70, s70, s67\n"
      "ds_read_b64_tr_b16 a[102:103], v245 offset:4096\n"
      "v_or3_b32 v116, v116, v214, v215\n"
      "v_mfma_f32_16x16x32_bf16 v[132:135], a[76:79], a[16:19], v[132:135]\n"
      "s_max_i32 s70, s70, 85\n"
      "v_exp_f32 v113, v177\n"
      "v_mfma_f32_16x16x32_bf16 v[132:135], a[68:71], a[0:3], v[132:135]\n"
      "s_add_u32 s66, s66, s86\n"
      "ds_read_b64_tr_b16 a[104:105], v246 offset:0\n"
      "v_cvt_pk_bf16_f32 v216, v112, v113\n"
      "v_mfma_f32_16x16x32_bf16 v[140:143], a[68:71], a[4:7], v[228:231]\n"
      "s_and_b32 s76, s80, 31\n"
      "v_exp_f32 v114, v178\n"
      "v_mfma_f32_16x16x32_bf16 v[140:143], a[76:79], a[20:23], v[140:143]\n"
      "s_lshr_b64 s[80:81], s[80:81], 5\n"
      "ds_read_b64_tr_b16 a[106:107], v246 offset:4096\n"
      "ds_read_b64_tr_b16 a[108:109], v247 offset:0\n"
      "v_mfma_f32_16x16x32_bf16 v[140:143], a[84:87], a[36:39], v[140:143]\n"
      "s_add_u32 s66, s66, s76\n"
      "v_exp_f32 v115, v179\n"
      "v_mfma_f32_16x16x32_bf16 v[140:143], a[92:95], a[52:55], v[140:143]\n"
      "s_sub_u32 s67, s68, s82\n"
      "ds_read_b64_tr_b16 a[110:111], v247 offset:4096\n"
      "v_cvt_pk_bf16_f32 v217, v114, v115\n"
      "v_mfma_f32_16x16x32_bf16 v[136:139], a[88:91], a[52:55], v[228:231]\n"
      "s_min_i32 s76, s67, s83\n"
      "v_exp_f32 v112, v180\n"
      "v_mfma_f32_16x16x32_bf16 v[136:139], a[80:83], a[36:39], v[136:139]\n"
      "s_mov_b32 s83, s67\n"
      "ds_read_b64_tr_b16 a[112:113], v244 offset:1024\n"
      "v_or3_b32 v116, v116, v216, v217\n"
      "v_mfma_f32_16x16x32_bf16 v[136:139], a[72:75], a[20:23], v[136:139]\n"
      "s_sub_u32 s76, s76, 36\n"
      "v_exp_f32 v113, v181\n"
      "v_mfma_f32_16x16x32_bf16 v[136:139], a[64:67], a[4:7], v[136:139]\n"
      "s_min_i32 s76, s76, 148\n"
      "ds_read_b64_tr_b16 a[114:115], v244 offset:5120\n"
      "v_cvt_pk_bf16_f32 v218, v112, v113\n"
      "v_mfma_f32_16x16x32_bf16 v[144:147], a[64:67], a[8:11], v[232:235]\n"
      "s_max_i32 s76, s76, 70\n"
      "v_exp_f32 v114, v182\n"
      "v_mfma_f32_16x16x32_bf16 v[144:147], a[72:75], a[24:27], v[144:147]\n"
      "s_mov_b32 s82, s68\n"
      "ds_read_b64_tr_b16 a[116:117], v245 offset:1024\n"
      "ds_read_b64_tr_b16 a[118:119], v245 offset:5120\n"
      "v_mfma_f32_16x16x32_bf16 v[144:147], a[80:83], a[40:43], v[144:147]\n"
      "s_lshr_b32 s69, s86, 1\n"
      "v_exp_f32 v115, v183\n"
      "v_mfma_f32_16x16x32_bf16 v[144:147], a[88:91], a[56:59], v[144:147]\n"
      "s_add_u32 s66, s66, s69\n"
      "ds_read_b64_tr_b16 a[120:121], v246 offset:1024\n"
      "v_cvt_pk_bf16_f32 v219, v114, v115\n"
      "v_mfma_f32_16x16x32_bf16 v[148:151], a[92:95], a[56:59], v[232:235]\n"
      "s_add_u32 s82, s82, s69\n"
      "v_exp_f32 v112, v184\n"
      "v_mfma_f32_16x16x32_bf16 v[148:151], a[84:87], a[40:43], v[148:151]\n"
      "s_add_u32 s65, s82, s76\n"
      "ds_read_b64_tr_b16 a[122:123], v246 offset:5120\n"
      "v_or3_b32 v116, v116, v218, v219\n"
      "v_mfma_f32_16x16x32_bf16 v[148:151], a[76:79], a[24:27], v[148:151]\n"
      "s_sub_u32 s93, s93, 1\n"
      "v_exp_f32 v113, v185\n"
      "v_mfma_f32_16x16x32_bf16 v[148:151], a[68:71], a[8:11], v[148:151]\n"
      "s_cmp_le_i32 s93, 14\n"
      "ds_read_b64_tr_b16 a[124:125], v247 offset:1024\n"
      "v_cvt_pk_bf16_f32 v220, v112, v113\n"
      "v_mfma_f32_16x16x32_bf16 v[156:159], a[68:71], a[12:15], v[236:239]\n"
      "s_cselect_b32 s86, 107, s86\n"
      "v_exp_f32 v114, v186\n"
      "v_mfma_f32_16x16x32_bf16 v[156:159], a[76:79], a[28:31], v[156:159]\n"
      "s_sub_u32 s67, s65, s66\n"
      "ds_read_b64_tr_b16 a[126:127], v247 offset:5120\n"
      "v_mfma_f32_16x16x32_bf16 v[156:159], a[84:87], a[44:47], v[156:159]\n"
      "s_cmp_lt_i32 s67, 0\n"
      "v_exp_f32 v115, v187\n"
      "v_mfma_f32_16x16x32_bf16 v[156:159], a[92:95], a[60:63], v[156:159]\n"
      "s_cselect_b32 s88, s66, s65\n"
      "v_cvt_pk_bf16_f32 v221, v114, v115\n"
      "v_mfma_f32_16x16x32_bf16 v[152:155], a[88:91], a[60:63], v[236:239]\n"
      "s_add_u32 s90, s88, s91\n"
      "v_exp_f32 v112, v188\n"
      "v_mfma_f32_16x16x32_bf16 v[152:155], a[80:83], a[44:47], v[152:155]\n"
      "v_or3_b32 v116, v116, v220, v221\n"
      "v_mfma_f32_16x16x32_bf16 v[152:155], a[72:75], a[28:31], v[152:155]\n"
      "v_exp_f32 v113, v189\n"
      "v_mfma_f32_16x16x32_bf16 v[152:155], a[64:67], a[12:15], v[152:155]\n"
      "s_waitcnt lgkmcnt(10)\n"
      "v_cvt_pk_bf16_f32 v222, v112, v113\n"
      "v_mfma_f32_16x16x32_bf16 v[32:35], a[96:99], v[192:195], v[32:35]\n"
      "v_exp_f32 v114, v190\n"
      "v_mfma_f32_16x16x32_bf16 v[48:51], a[100:103], v[192:195], v[48:51]\n"
      "v_mfma_f32_16x16x32_bf16 a[128:131], a[104:107], v[192:195], a[128:131]\n"
      "s_waitcnt lgkmcnt(6)\n"
      "v_exp_f32 v115, v191\n"
      "v_mfma_f32_16x16x32_bf16 a[144:147], a[108:111], v[192:195], a[144:147]\n"
      "v_cvt_pk_bf16_f32 v223, v114, v115\n"
      "v_mfma_f32_16x16x32_bf16 v[64:67], a[112:115], v[192:195], v[64:67]\n"
      "s_waitcnt lgkmcnt(2)\n"
      "v_mfma_f32_16x16x32_bf16 v[80:83], a[116:119], v[192:195], v[80:83]\n"
      "v_or3_b32 v116, v116, v222, v223\n"
      "v_mfma_f32_16x16x32_bf16 a[160:163], a[120:123], v[192:195], a[160:163]\n"
      "s_mov_b32 s64, 1\n"
      "s_waitcnt lgkmcnt(0)\n"
      "v_and_b32 v116, 0x40004000, v116\n"
      "v_mfma_f32_16x16x32_bf16 a[176:179], a[124:127], v[192:195], a[176:179]\n"
      "v_cmp_ne_u32 vcc, 0, v116\n"
      "s_cbranch_vccnz FB1_%=\n"
      "RET1_1_%=:\n"
      "v_mfma_f32_16x16x32_bf16 a[180:183], a[124:127], v[196:199], a[180:183]\n"
      "v_exp_f32 v112, v128\n"
      "s_barrier\n"
      "v_mfma_f32_16x16x32_bf16 a[164:167], a[120:123], v[196:199], a[164:167]\n"
      "v_mfma_f32_16x16x32_bf16 a[192:195], a[196:199], v[208:211], a[192:195]\n"
      "s_add_u32 m0, s62, 0\n"
      "v_exp_f32 v113, v129\n"
      "v_mfma_f32_16x16x32_bf16 v[84:87], a[116:119], v[196:199], v[84:87]\n"
      "buffer_load_dwordx4 v248, %[srdk], s60 offen nt lds\n"
      "v_cvt_pk_bf16_f32 v192, v112, v113\n"
      "v_mfma_f32_16x16x32_bf16 v[68:71], a[112:115], v[196:199], v[68:71]\n"
      "s_add_u32 m0, s62, 896\n"
      "v_exp_f32 v114, v130\n"
      "v_mfma_f32_16x16x32_bf16 a[192:195], a[200:203], v[212:215], a[192:195]\n"
      "buffer_load_dwordx4 v248, %[srdk], s60 offen offset:128 nt lds\n"
      "v_mfma_f32_16x16x32_bf16 a[148:151], a[108:111], v[196:199], a[148:151]\n"
      "s_add_u32 m0, s62, 8192\n"
      "v_exp_f32 v115, v131\n"
      "v_mfma_f32_16x16x32_bf16 a[132:135], a[104:107], v[196:199], a[132:135]\n"
      "s_memrealtime s[68:69]\n"
      "buffer_load_dwordx4 v120, %[srdk], s60 offen nt lds\n"
      "v_cvt_pk_bf16_f32 v193, v114, v115\n"
      "v_mfma_f32_16x16x32_bf16 a[192:195], a[204:207], v[216:219], a[192:195]\n"
      "s_add_u32 m0, s62, 9088\n"
      "v_exp_f32 v112, v132\n"
      "v_mfma_f32_16x16x32_bf16 v[52:55], a[100:103], v[196:199], v[52:55]\n"
      "s_add_u32 s62, s62, 0x8000\n"
      "buffer_load_dwordx4 v120, %[srdk], s60 offen offset:128 nt lds\n"
      "v_or_b32 v116, v192, v193\n"
      "v_mfma_f32_16x16x32_bf16 v[36:39], a[96:99], v[196:199], v[36:39]\n"
      "s_and_b32 s62, s62, 0x1ffff\n"
      "v_exp_f32 v113, v133\n"
      "v_mfma_f32_16x16x32_bf16 a[192:195], a[208:211], v[220:223], a[192:195]\n"
      "s_add_u32 s60, s60, s78\n"
      "v_cvt_pk_bf16_f32 v194, v112, v113\n"
      "s_waitcnt vmcnt(12) lgkmcnt(0)\n"
      "s_barrier\n"
      "v_mfma_f32_16x16x32_bf16 v[40:43], a[96:99], v[200:203], v[40:43]\n"
      "ds_read_b128 a[64:67], v242 offset:32768\n"
      "v_exp_f32 v114, v134\n"
      "v_mfma_f32_16x16x32_bf16 v[44:47], a[96:99], v[204:207], v[44:47]\n"
      "ds_read_b128 a[68:71], v242 offset:36864\n"
      "ds_read_b128 a[72:75], v243 offset:32768\n"
      "v_mfma_f32_16x16x32_bf16 v[60:63], a[100:103], v[204:207], v[60:63]\n"
      "v_exp_f32 v115, v135\n"
      "v_mfma_f32_16x16x32_bf16 v[56:59], a[100:103], v[200:203], v[56:59]\n"
      "ds_read_b128 a[76:79], v243 offset:36864\n"
      "v_cvt_pk_bf16_f32 v195, v114, v115\n"
      "v_mfma_f32_16x16x32_bf16 a[136:139], a[104:107], v[200:203], a[136:139]\n"
      "v_exp_f32 v112, v136\n"
      "v_mfma_f32_16x16x32_bf16 a[140:143], a[104:107], v[204:207], a[140:143]\n"
      "ds_read_b128 a[80:83], v242 offset:33792\n"
      "v_or3_b32 v116, v116, v194, v195\n"
      "v_mfma_f32_16x16x32_bf16 a[156:159], a[108:111], v[204:207], a[156:159]\n"
      "v_exp_f32 v113, v137\n"
      "v_mfma_f32_16x16x32_bf16 a[152:155], a[108:111], v[200:203], a[152:155]\n"
      "ds_read_b128 a[84:87], v242 offset:37888\n"
      "v_cvt_pk_bf16_f32 v196, v112, v113\n"
      "v_mfma_f32_16x16x32_bf16 v[72:75], a[112:115], v[200:203], v[72:75]\n"
      "v_exp_f32 v114, v138\n"
      "v_mfma_f32_16x16x32_bf16 v[76:79], a[112:115], v[204:207], v[76:79]\n"
      "ds_read_b128 a[88:91], v243 offset:33792\n"
      "ds_read_b128 a[92:95], v243 offset:37888\n"
      "v_mfma_f32_16x16x32_bf16 v[92:95], a[116:119], v[204:207], v[92:95]\n"
      "v_exp_f32 v115, v139\n"
      "v_mfma_f32_16x16x32_bf16 v[88:91], a[116:119], v[200:203], v[88:91]\n"
      "v_cvt_pk_bf16_f32 v197, v114, v115\n"
      "v_mfma_f32_16x16x32_bf16 a[168:171], a[120:123], v[200:203], a[168:171]\n"
      "v_exp_f32 v112, v140\n"
      "v_mfma_f32_16x16x32_bf16 a[172:175], a[120:123], v[204:207], a[172:175]\n"
      "v_or3_b32 v116, v116, v196, v197\n"
      "v_mfma_f32_16x16x32_bf16 a[188:191], a[124:127], v[204:207], a[188:191]\n"
      "v_exp_f32 v113, v141\n"
      "v_mfma_f32_16x16x32_bf16 a[184:187], a[124:127], v[200:203], a[184:187]\n"
      "s_waitcnt lgkmcnt(0)\n"
      "v_cvt_pk_bf16_f32 v198, v112, v113\n"
      "v_mfma_f32_16x16x32_bf16 v[160:163], a[64:67], a[0:3], v[224:227]\n"
      "v_exp_f32 v114, v142\n"
      "v_mfma_f32_16x16x32_bf16 v[160:163], a[72:75], a[16:19], v[160:163]\n"
      "ds_read_b64_tr_b16 a[96:97], v244 offset:32768\n"
      "ds_read_b64_tr_b16 a[98:99], v244 offset:36864\n"
      "v_mfma_f32_16x16x32_bf16 v[160:163], a[80:83], a[32:35], v[160:163]\n"
      "v_exp_f32 v115, v143\n"
      "v_mfma_f32_16x16x32_bf16 v[160:163], a[88:91], a[48:51], v[160:163]\n"
      "ds_read_b64_tr_b16 a[100:101], v245 offset:32768\n"
      "v_cvt_pk_bf16_f32 v199, v114, v115\n"
      "v_mfma_f32_16x16x32_bf16 v[164:167], a[92:95], a[48:51], v[224:227]\n"
      "v_exp_f32 v112, v144\n"
      "v_mfma_f32_16x16x32_bf16 v[164:167], a[84:87], a[32:35], v[164:167]\n"
      "ds_read_b64_tr_b16 a[102:103], v245 offset:36864\n"
      "v_or3_b32 v116, v116, v198, v199\n"
      "v_mfma_f32_16x16x32_bf16 v[164:167], a[76:79], a[16:19], v[164:167]\n"
      "v_exp_f32 v113, v145\n"
      "v_mfma_f32_16x16x32_bf16 v[164:167], a[68:71], a[0:3], v[164:167]\n"
      "ds_read_b64_tr_b16 a[104:105], v246 offset:32768\n"
      "v_cvt_pk_bf16_f32 v200, v112, v113\n"
      "v_mfma_f32_16x16x32_bf16 v[172:175], a[68:71], a[4:7], v[228:231]\n"
      "v_exp_f32 v114, v146\n"
      "v_mfma_f32_16x16x32_bf16 v[172:175], a[76:79], a[20:23], v[172:175]\n"
      "ds_read_b64_tr_b16 a[106:107], v246 offset:36864\n"
      "ds_read_b64_tr_b16 a[108:109], v247 offset:32768\n"
      "v_mfma_f32_16x16x32_bf16 v[172:175], a[84:87], a[36:39], v[172:175]\n"
      "v_exp_f32 v115, v147\n"
      "v_mfma_f32_16x16x32_bf16 v[172:175], a[92:95], a[52:55], v[172:175]\n"
      "ds_read_b64_tr_b16 a[110:111], v247 offset:36864\n"
      "v_cvt_pk_bf16_f32 v201, v114, v115\n"
      "v_mfma_f32_16x16x32_bf16 v[168:171], a[88:91], a[52:55], v[228:231]\n"
      "v_exp_f32 v112, v148\n"
      "v_mfma_f32_16x16x32_bf16 v[168:171], a[80:83], a[36:39], v[168:171]\n"
      "ds_read_b64_tr_b16 a[112:113], v244 offset:33792\n"
      "v_or3_b32 v116, v116, v200, v201\n"
      "v_mfma_f32_16x16x32_bf16 v[168:171], a[72:75], a[20:23], v[168:171]\n"
      "v_exp_f32 v113, v149\n"
      "v_mfma_f32_16x16x32_bf16 v[168:171], a[64:67], a[4:7], v[168:171]\n"
      "ds_read_b64_tr_b16 a[114:115], v244 offset:37888\n"
      "v_cvt_pk_bf16_f32 v202, v112, v113\n"
      "v_mfma_f32_16x16x32_bf16 v[176:179], a[64:67], a[8:11], v[232:235]\n"
      "v_exp_f32 v114, v150\n"
      "v_mfma_f32_16x16x32_bf16 v[176:179], a[72:75], a[24:27], v[176:179]\n"
      "ds_read_b64_tr_b16 a[116:117], v245 offset:33792\n"
      "ds_read_b64_tr_b16 a[118:119], v245 offset:37888\n"
      "v_mfma_f32_16x16x32_bf16 v[176:179], a[80:83], a[40:43], v[176:179]\n"
      "v_exp_f32 v115, v151\n"
      "v_mfma_f32_16x16x32_bf16 v[176:179], a[88:91], a[56:59], v[176:179]\n"
      "ds_read_b64_tr_b16 a[120:121], v246 offset:33792\n"
      "v_cvt_pk_bf16_f32 v203, v114, v115\n"
      "v_mfma_f32_16x16x32_bf16 v[180:183], a[92:95], a[56:59], v[232:235]\n"
      "v_exp_f32 v112, v152\n"
      "v_mfma_f32_16x16x32_bf16 v[180:183], a[84:87], a[40:43], v[180:183]\n"
      "ds_read_b64_tr_b16 a[122:123], v246 offset:37888\n"
      "v_or3_b32 v116, v116, v202, v203\n"
      "v_mfma_f32_16x16x32_bf16 v[180:183], a[76:79], a[24:27], v[180:183]\n"
      "v_exp_f32 v113, v153\n"
      "v_mfma_f32_16x16x32_bf16 v[180:183], a[68:71], a[8:11], v[180:183]\n"
      "ds_read_b64_tr_b16 a[124:125], v247 offset:33792\n"
      "v_cvt_pk_bf16_f32 v204, v112, v113\n"
      "v_mfma_f32_16x16x32_bf16 v[188:191], a[68:71], a[12:15], v[236:239]\n"
      "v_exp_f32 v114, v154\n"
      "v_mfma_f32_16x16x32_bf16 v[188:191], a[76:79], a[28:31], v[188:191]\n"
      "ds_read_b64_tr_b16 a[126:127], v247 offset:37888\n"
      "v_mfma_f32_16x16x32_bf16 v[188:191], a[84:87], a[44:47], v[188:191]\n"
      "v_exp_f32 v115, v155\n"
      "v_mfma_f32_16x16x32_bf16 v[188:191], a[92:95], a[60:63], v[188:191]\n"
      "v_cvt_pk_bf16_f32 v205, v114, v115\n"
      "v_mfma_f32_16x16x32_bf16 v[184:187], a[88:91], a[60:63], v[236:239]\n"
      "v_exp_f32 v112, v156\n"
      "v_mfma_f32_16x16x32_bf16 v[184:187], a[80:83], a[44:47], v[184:187]\n"
      "v_or3_b32 v116, v116, v204, v205\n"
      "v_mfma_f32_16x16x32_bf16 v[184:187], a[72:75], a[28:31], v[184:187]\n"
      "v_exp_f32 v113, v157\n"
      "v_mfma_f32_16x16x32_bf16 v[184:187], a[64:67], a[12:15], v[184:187]\n"
      "s_waitcnt lgkmcnt(10)\n"
      "v_cvt_pk_bf16_f32 v206, v112, v113\n"
      "v_mfma_f32_16x16x32_bf16 v[32:35], a[96:99], v[208:211], v[32:35]\n"
      "v_exp_f32 v114, v158\n"
      "v_mfma_f32_16x16x32_bf16 v[48:51], a[100:103], v[208:211], v[48:51]\n"
      "v_mfma_f32_16x16x32_bf16 a[128:131], a[104:107], v[208:211], a[128:131]\n"
      "s_waitcnt lgkmcnt(6)\n"
      "v_exp_f32 v115, v159\n"
      "v_mfma_f32_16x16x32_bf16 a[144:147], a[108:111], v[208:211], a[144:147]\n"
      "v_cvt_pk_bf16_f32 v207, v114, v115\n"
      "v_mfma_f32_16x16x32_bf16 v[64:67], a[112:115], v[208:211], v[64:67]\n"
      "s_waitcnt lgkmcnt(2)\n"
      "v_mfma_f32_16x16x32_bf16 v[80:83], a[116:119], v[208:211], v[80:83]\n"
      "v_or3_b32 v116, v116, v206, v207\n"
      "v_mfma_f32_16x16x32_bf16 a[160:163], a[120:123], v[208:211], a[160:163]\n"
      "s_mov_b32 s64, 1\n"
      "s_waitcnt lgkmcnt(0)\n"
      "v_and_b32 v116, 0x40004000, v116\n"
      "v_mfma_f32_16x16x32_bf16 a[176:179], a[124:127], v[208:211], a[176:179]\n"
      "v_cmp_ne_u32 vcc, 0, v116\n"
      "s_cbranch_vccnz FB0_%=\n"
      "RET0_1_%=:\n"
      "v_mfma_f32_16x16x32_bf16 a[180:183], a[124:127], v[212:215], a[180:183]\n"
      "v_exp_f32 v112, v160\n"
      "v_mfma_f32_16x16x32_bf16 a[164:167], a[120:123], v[212:215], a[164:167]\n"
      "v_xor_b32 v242, 0x10000, v242\n"
      "v_mfma_f32_16x16x32_bf16 a[192:195], a[196:199], v[192:195], a[192:195]\n"
      "s_add_u32 m0, s77, 16384\n"
      "v_exp_f32 v113, v161\n"
      "v_mfma_f32_16x16x32_bf16 v[84:87], a[116:119], v[212:215], v[84:87]\n"
      "buffer_load_dwordx4 v249, %[srdv], s61 offen nt lds\n"
      "v_cvt_pk_bf16_f32 v208, v112, v113\n"
      "v_xor_b32 v243, 0x10000, v243\n"
      "v_mfma_f32_16x16x32_bf16 v[68:71], a[112:115], v[212:215], v[68:71]\n"
      "s_add_u32 m0, s77, 17408\n"
      "v_exp_f32 v114, v162\n"
      "v_mfma_f32_16x16x32_bf16 a[192:195], a[200:203], v[196:199], a[192:195]\n"
      "buffer_load_dwordx4 v121, %[srdv], s61 offen nt lds\n"
      "v_xor_b32 v244, 0x10000, v244\n"
      "v_mfma_f32_16x16x32_bf16 a[148:151], a[108:111], v[212:215], a[148:151]\n"
      "s_add_u32 m0, s77, 24576\n"
      "v_exp_f32 v115, v163\n"
      "v_mfma_f32_16x16x32_bf16 a[132:135], a[104:107], v[212:215], a[132:135]\n"
      "s_memrealtime s[68:69]\n"
      "buffer_load_dwordx4 v122, %[srdv], s61 offen nt lds\n"
      "v_cvt_pk_bf16_f32 v209, v114, v115\n"
      "v_xor_b32 v245, 0x10000, v245\n"
      "v_mfma_f32_16x16x32_bf16 a[192:195], a[204:207], v[200:203], a[192:195]\n"
      "s_add_u32 m0, s77, 25600\n"
      "v_exp_f32 v112, v164\n"
      "v_mfma_f32_16x16x32_bf16 v[52:55], a[100:103], v[212:215], v[52:55]\n"
      "s_add_u32 s77, s77, 0x8000\n"
      "buffer_load_dwordx4 v123, %[srdv], s61 offen nt lds\n"
      "v_or_b32 v116, v208, v209\n"
      "v_xor_b32 v246, 0x10000, v246\n"
      "v_mfma_f32_16x16x32_bf16 v[36:39], a[96:99], v[212:215], v[36:39]\n"
      "s_and_b32 s77, s77, 0x1ffff\n"
      "v_exp_f32 v113, v165\n"
      "v_mfma_f32_16x16x32_bf16 a[192:195], a[208:211], v[204:207], a[192:195]\n"
      "s_add_u32 s61, s61, s78\n"
      "v_cvt_pk_bf16_f32 v210, v112, v113\n"
      "v_xor_b32 v247, 0x10000, v247\n"
      "s_waitcnt vmcnt(8) lgkmcnt(0)\n"
      "s_sub_u32 s67, s68, s90\n"
      "s_cmp_lt_i32 s67, 0\n"
      "s_cbranch_scc1 GW3_%=\n"
      "GX3_%=:\n"
      "s_barrier\n"
      "v_mfma_f32_16x16x32_bf16 v[40:43], a[96:99], v[216:219], v[40:43]\n"
      "s_memrealtime s[68:69]\n"
      "buffer_load_dword v250, v251, %[srdr], 0 offen sc1\n"
      "ds_read_b128 a[64:67], v242 offset:0\n"
      "v_exp_f32 v114, v166\n"
      "v_mfma_f32_16x16x32_bf16 v[44:47], a[96:99], v[220:223], v[44:47]\n"
      "s_add_u32 m0, s62, 0\n"
      "ds_read_b128 a[68:71], v242 offset:4096\n"
      "ds_read_b128 a[72:75], v243 offset:0\n"
      "v_mfma_f32_16x16x32_bf16 v[60:63], a[100:103], v[220:223], v[60:63]\n"
      "buffer_load_dwordx4 v248, %[srdk], s60 offen nt lds\n"
      "v_exp_f32 v115, v167\n"
      "v_mfma_f32_16x16x32_bf16 v[56:59], a[100:103], v[216:219], v[56:59]\n"
      "s_add_u32 m0, s62, 896\n"
      "ds_read_b128 a[76:79], v243 offset:4096\n"
      "v_cvt_pk_bf16_f32 v211, v114, v115\n"
      "v_mfma_f32_16x16x32_bf16 a[136:139], a[104:107], v[216:219], a[136:139]\n"
      "buffer_load_dwordx4 v248, %[srdk], s60 offen offset:128 nt lds\n"
      "v_exp_f32 v112, v168\n"
      "v_mfma_f32_16x16x32_bf16 a[140:143], a[104:107], v[220:223], a[140:143]\n"
      "s_add_u32 m0, s62, 8192\n"
      "ds_read_b128 a[80:83], v242 offset:1024\n"
      "v_or3_b32 v116, v116, v210, v211\n"
      "v_mfma_f32_16x16x32_bf16 a[156:159], a[108:111], v[220:223], a[156:159]\n"
      "buffer_load_dwordx4 v120, %[srdk], s60 offen nt lds\n"
      "v_exp_f32 v113, v169\n"
      "v_mfma_f32_16x16x32_bf16 a[152:155], a[108:111], v[216:219], a[152:155]\n"
      "s_add_u32 m0, s62, 9088\n"
      "ds_read_b128 a[84:87], v242 offset:5120\n"
      "v_cvt_pk_bf16_f32 v212, v112, v113\n"
      "v_mfma_f32_16x16x32_bf16 v[72:75], a[112:115], v[216:219], v[72:75]\n"
      "s_add_u32 s62, s62, 0x8000\n"
      "buffer_load_dwordx4 v120, %[srdk], s60 offen offset:128 nt lds\n"
      "v_exp_f32 v114, v170\n"
      "v_mfma_f32_16x16x32_bf16 v[76:79], a[112:115], v[220:223], v[76:79]\n"
      "s_and_b32 s62, s62, 0x1ffff\n"
      "ds_read_b128 a[88:91], v243 offset:1024\n"
      "ds_read_b128 a[92:95], v243 offset:5120\n"
      "v_mfma_f32_16x16x32_bf16 v[92:95], a[116:119], v[220:223], v[92:95]\n"
      "s_add_u32 s60, s60, %[inc]\n"
      "v_exp_f32 v115, v171\n"
      "v_mfma_f32_16x16x32_bf16 v[88:91], a[116:119], v[216:219], v[88:91]\n"
      "v_cvt_pk_bf16_f32 v213, v114, v115\n"
      "v_mfma_f32_16x16x32_bf16 a[168:171], a[120:123], v[216:219], a[168:171]\n"
      "v_exp_f32 v112, v172\n"
      "v_mfma_f32_16x16x32_bf16 a[172:175], a[120:123], v[220:223], a[172:175]\n"
      "v_or3_b32 v116, v116, v212, v213\n"
      "v_mfma_f32_16x16x32_bf16 a[188:191], a[124:127], v[220:223], a[188:191]\n"
      "v_exp_f32 v113, v173\n"
      "v_mfma_f32_16x16x32_bf16 a[184:187], a[124:127], v[216:219], a[184:187]\n"
      "s_waitcnt lgkmcnt(0)\n"
      "v_cvt_pk_bf16_f32 v214, v112, v113\n"
      "v_mfma_f32_16x16x32_bf16 v[128:131], a[64:67], a[0:3], v[224:227]\n"
      "s_sub_u32 s67, s68, s66\n"
      "v_exp_f32 v114, v174\n"
      "v_mfma_f32_16x16x32_bf16 v[128:131], a[72:75], a[16:19], v[128:131]\n"
      "s_cmp_gt_i32 s67, s70\n"
      "ds_read_b64_tr_b16 a[96:97], v244 offset:0\n"
      "ds_read_b64_tr_b16 a[98:99], v244 offset:4096\n"
      "v_mfma_f32_16x16x32_bf16 v[128:131], a[80:83], a[32:35], v[128:131]\n"
      "s_cselect_b32 s69, s86, 0\n"
      "v_exp_f32 v115, v175\n"
      "v_mfma_f32_16x16x32_bf16 v[128:131], a[88:91], a[48:51], v[128:131]\n"
      "s_add_u32 s66, s66, s69\n"
      "ds_read_b64_tr_b16 a[100:101], v245 offset:0\n"
      "v_cvt_pk_bf16_f32 v215, v114, v115\n"
      "v_mfma_f32_16x16x32_bf16 v[132:135], a[92:95], a[48:51], v[224:227]\n"
      "s_add_u32 s67, s67, s101\n"
      "v_exp_f32 v112, v176\n"
      "v_mfma_f32_16x16x32_bf16 v[132:135], a[84:87], a[32:35], v[132:135]\n"
      "s_min_i32 s70, s70, s67\n"
      "ds_read_b64_tr_b16 a[102:103], v245 offset:4096\n"
      "v_or3_b32 v116, v116, v214, v215\n"
      "v_mfma_f32_16x16x32_bf16 v[132:135], a[76:79], a[16:19], v[132:135]\n"
      "s_max_i32 s70, s70, 85\n"
      "v_exp_f32 v113, v177\n"
      "v_mfma_f32_16x16x32_bf16 v[132:135], a[68:71], a[0:3], v[132:135]\n"
      "s_add_u32 s66, s66, s86\n"
      "ds_read_b64_tr_b16 a[104:105], v246 offset:0\n"
      "v_cvt_pk_bf16_f32 v216, v112, v113\n"
      "v_mfma_f32_16x16x32_bf16 v[140:143], a[68:71], a[4:7], v[228:231]\n"
      "s_and_b32 s76, s80, 31\n"
      "v_exp_f32 v114, v178\n"
      "v_mfma_f32_16x16x32_bf16 v[140:143], a[76:79], a[20:23], v[140:143]\n"
      "s_lshr_b64 s[80:81], s[80:81], 5\n"
      "ds_read_b64_tr_b16 a[106:107], v246 offset:4096\n"
      "ds_read_b64_tr_b16 a[108:109], v247 offset:0\n"
      "v_mfma_f32_16x16x32_bf16 v[140:143], a[84:87], a[36:39], v[140:143]\n"
      "s_add_u32 s66, s66, s76\n"
      "v_exp_f32 v115, v179\n"
      "v_mfma_f32_16x16x32_bf16 v[140:143], a[92:95], a[52:55], v[140:143]\n"
      "s_sub_u32 s67, s68, s82\n"
      "ds_read_b64_tr_b16 a[110:111], v247 offset:4096\n"
      "v_cvt_pk_bf16_f32 v217, v114, v115\n"
      "v_mfma_f32_16x16x32_bf16 v[136:139], a[88:91], a[52:55], v[228:231]\n"
      "s_min_i32 s76, s67, s83\n"
      "v_exp_f32 v112, v180\n"
      "v_mfma_f32_16x16x32_bf16 v[136:139], a[80:83], a[36:39], v[136:139]\n"
      "s_mov_b32 s83, s67\n"
      "ds_read_b64_tr_b16 a[112:113], v244 offset:1024\n"
      "v_or3_b32 v116, v116, v216, v217\n"
      "v_mfma_f32_16x16x32_bf16 v[136:139], a[72:75], a[20:23], v[136:139]\n"
      "s_sub_u32 s76, s76, 36\n"
      "v_exp_f32 v113, v181\n"
      "v_mfma_f32_16x16x32_bf16 v[136:139], a[64:67], a[4:7], v[136:139]\n"
      "s_min_i32 s76, s76, 148\n"
      "ds_read_b64_tr_b16 a[114:115], v244 offset:5120\n"
      "v_cvt_pk_bf16_f32 v218, v112, v113\n"
      "v_mfma_f32_16x16x32_bf16 v[144:147], a[64:67], a[8:11], v[232:235]\n"
      "s_max_i32 s76, s76, 70\n"
      "v_exp_f32 v114, v182\n"
      "v_mfma_f32_16x16x32_bf16 v[144:147], a[72:75], a[24:27], v[144:147]\n"
      "s_mov_b32 s82, s68\n"
      "ds_read_b64_tr_b16 a[116:117], v245 offset:1024\n"
      "ds_read_b64_tr_b16 a[118:119], v245 offset:5120\n"
      "v_mfma_f32_16x16x32_bf16 v[144:147], a[80:83], a[40:43], v[144:147]\n"
      "s_lshr_b32 s69, s86, 1\n"
      "v_exp_f32 v115, v183\n"
      "v_mfma_f32_16x16x32_bf16 v[144:147], a[88:91], a[56:59], v[144:147]\n"
      "s_add_u32 s66, s66, s69\n"
      "ds_read_b64_tr_b16 a[120:121], v246 offset:1024\n"
      "v_cvt_pk_bf16_f32 v219, v114, v115\n"
      "v_mfma_f32_16x16x32_bf16 v[148:151], a[92:95], a[56:59], v[232:235]\n"
      "s_add_u32 s82, s82, s69\n"
      "v_exp_f32 v112, v184\n"
      "v_mfma_f32_16x16x32_bf16 v[148:151], a[84:87], a[40:43], v[148:151]\n"
      "s_add_u32 s65, s82, s76\n"
      "ds_read_b64_tr_b16 a[122:123], v246 offset:5120\n"
      "v_or3_b32 v116, v116, v218, v219\n"
      "v_mfma_f32_16x16x32_bf16 v[148:151], a[76:79], a[24:27], v[148:151]\n"
      "s_sub_u32 s93, s93, 1\n"
      "v_exp_f32 v113, v185\n"
      "v_mfma_f32_16x16x32_bf16 v[148:151], a[68:71], a[8:11], v[148:151]\n"
      "s_cmp_le_i32 s93, 14\n"
      "ds_read_b64_tr_b16 a[124:125], v247 offset:1024\n"
      "v_cvt_pk_bf16_f32 v220, v112, v113\n"
      "v_mfma_f32_16x16x32_bf16 v[156:159], a[68:71], a[12:15], v[236:239]\n"
      "s_cselect_b32 s86, 107, s86\n"
      "v_exp_f32 v114, v186\n"
      "v_mfma_f32_16x16x32_bf16 v[156:159], a[76:79], a[28:31], v[156:159]\n"
      "s_sub_u32 s67, s65, s66\n"
      "ds_read_b64_tr_b16 a[126:127], v247 offset:5120\n"
      "v_mfma_f32_16x16x32_bf16 v[156:159], a[84:87], a[44:47], v[156:159]\n"
      "s_cmp_lt_i32 s67, 0\n"
      "v_exp_f32 v115, v187\n"
      "v_mfma_f32_16x16x32_bf16 v[156:159], a[92:95], a[60:63], v[156:159]\n"
      "s_cselect_b32 s88, s66, s65\n"
      "v_cvt_pk_bf16_f32 v221, v114, v115\n"
      "v_mfma_f32_16x16x32_bf16 v[152:155], a[88:91], a[60:63], v[236:239]\n"
      "s_add_u32 s90, s88, s91\n"
      "v_exp_f32 v112, v188\n"
      "v_mfma_f32_16x16x32_bf16 v[152:155], a[80:83], a[44:47], v[152:155]\n"
      "v_or3_b32 v116, v116, v220, v221\n"
      "v_mfma_f32_16x16x32_bf16 v[152:155], a[72:75], a[28:31], v[152:155]\n"
      "v_exp_f32 v113, v189\n"
      "v_mfma_f32_16x16x32_bf16 v[152:155], a[64:67], a[12:15], v[152:155]\n"
      "s_waitcnt lgkmcnt(10)\n"
      "v_cvt_pk_bf16_f32 v222, v112, v113\n"
      "v_mfma_f32_16x16x32_bf16 v[32:35], a[96:99], v[192:195], v[32:35]\n"
      "v_exp_f32 v114, v190\n"
      "v_mfma_f32_16x16x32_bf16 v[48:51], a[100:103], v[192:195], v[48:51]\n"
      "v_mfma_f32_16x16x32_bf16 a[128:131], a[104:107], v[192:195], a[128:131]\n"
      "s_waitcnt lgkmcnt(6)\n"
      "v_exp_f32 v115, v191\n"
      "v_mfma_f32_16x16x32_bf16 a[144:147], a[108:111], v[192:195], a[144:147]\n"
      "v_cvt_pk_bf16_f32 v223, v114, v115\n"
      "v_mfma_f32_16x16x32_bf16 v[64:67], a[112:115], v[192:195], v[64:67]\n"
      "s_waitcnt lgkmcnt(2)\n"
      "v_mfma_f32_16x16x32_bf16 v[80:83], a[116:119], v[192:195], v[80:83]\n"
      "v_or3_b32 v116, v116, v222, v223\n"
      "v_mfma_f32_16x16x32_bf16 a[160:163], a[120:123], v[192:195], a[160:163]\n"
      "s_mov_b32 s64, 2\n"
      "s_waitcnt lgkmcnt(0)\n"
      "v_and_b32 v116, 0x40004000, v116\n"
      "v_mfma_f32_16x16x32_bf16 a[176:179], a[124:127], v[192:195], a[176:179]\n"
      "v_cmp_ne_u32 vcc, 0, v116\n"
      "s_cbranch_vccnz FB1_%=\n"
      "RET1_2_%=:\n"
      "v_mfma_f32_16x16x32_bf16 a[180:183], a[124:127], v[196:199], a[180:183]\n"
      "v_exp_f32 v112, v128\n"
      "v_mfma_f32_16x16x32_bf16 a[164:167], a[120:123], v[196:199], a[164:167]\n"
      "v_mfma_f32_16x16x32_bf16 a[192:195], a[196:199], v[208:211], a[192:195]\n"
      "s_add_u32 m0, s77, 16384\n"
      "v_exp_f32 v113, v129\n"
      "v_mfma_f32_16x16x32_bf16 v[84:87], a[116:119], v[196:199], v[84:87]\n"
      "buffer_load_dwordx4 v249, %[srdv], s61 offen nt lds\n"
      "v_cvt_pk_bf16_f32 v192, v112, v113\n"
      "v_mfma_f32_16x16x32_bf16 v[68:71], a[112:115], v[196:199], v[68:71]\n"
      "s_add_u32 m0, s77, 17408\n"
      "v_exp_f32 v114, v130\n"
      "v_mfma_f32_16x16x32_bf16 a[192:195], a[200:203], v[212:215], a[192:195]\n"
      "buffer_load_dwordx4 v121, %[srdv], s61 offen nt lds\n"
      "v_mfma_f32_16x16x32_bf16 a[148:151], a[108:111], v[196:199], a[148:151]\n"
      "s_add_u32 m0, s77, 24576\n"
      "v_exp_f32 v115, v131\n"
      "v_mfma_f32_16x16x32_bf16 a[132:135], a[104:107], v[196:199], a[132:135]\n"
      "s_memrealtime s[68:69]\n"
      "buffer_load_dwordx4 v122, %[srdv], s61 offen nt lds\n"
      "v_cvt_pk_bf16_f32 v193, v114, v115\n"
      "v_mfma_f32_16x16x32_bf16 a[192:195], a[204:207], v[216:219], a[192:195]\n"
      "s_add_u32 m0, s77, 25600\n"
      "v_exp_f32 v112, v132\n"
      "v_mfma_f32_16x16x32_bf16 v[52:55], a[100:103], v[196:199], v[52:55]\n"
      "s_add_u32 s77, s77, 0x8000\n"
      "buffer_load_dwordx4 v123, %[srdv], s61 offen nt lds\n"
      "v_or_b32 v116, v192, v193\n"
      "v_mfma_f32_16x16x32_bf16 v[36:39], a[96:99], v[196:199], v[36:39]\n"
      "s_and_b32 s77, s77, 0x1ffff\n"
      "v_exp_f32 v113, v133\n"
      "v_mfma_f32_16x16x32_bf16 a[192:195], a[208:211], v[220:223], a[192:195]\n"
      "s_add_u32 s61, s61, %[inc]\n"
      "v_cvt_pk_bf16_f32 v194, v112, v113\n"
      "s_waitcnt vmcnt(9) lgkmcnt(0)\n"
      "s_barrier\n"
      "v_mfma_f32_16x16x32_bf16 v[40:43], a[96:99], v[200:203], v[40:43]\n"
      "ds_read_b128 a[64:67], v242 offset:32768\n"
      "v_exp_f32 v114, v134\n"
      "v_mfma_f32_16x16x32_bf16 v[44:47], a[96:99], v[204:207], v[44:47]\n"
      "ds_read_b128 a[68:71], v242 offset:36864\n"
      "ds_read_b128 a[72:75], v243 offset:32768\n"
      "v_mfma_f32_16x16x32_bf16 v[60:63], a[100:103], v[204:207], v[60:63]\n"
      "v_exp_f32 v115, v135\n"
      "v_mfma_f32_16x16x32_bf16 v[56:59], a[100:103], v[200:203], v[56:59]\n"
      "ds_read_b128 a[76:79], v243 offset:36864\n"
      "v_cvt_pk_bf16_f32 v195, v114, v115\n"
      "v_mfma_f32_16x16x32_bf16 a[136:139], a[104:107], v[200:203], a[136:139]\n"
      "v_exp_f32 v112, v136\n"
      "v_mfma_f32_16x16x32_bf16 a[140:143], a[104:107], v[204:207], a[140:143]\n"
      "ds_read_b128 a[80:83], v242 offset:33792\n"
      "v_or3_b32 v116, v116, v194, v195\n"
      "v_mfma_f32_16x16x32_bf16 a[156:159], a[108:111], v[204:207], a[156:159]\n"
      "v_exp_f32 v113, v137\n"
      "v_mfma_f32_16x16x32_bf16 a[152:155], a[108:111], v[200:203], a[152:155]\n"
      "ds_read_b128 a[84:87], v242 offset:37888\n"
      "v_cvt_pk_bf16_f32 v196, v112, v113\n"
      "v_mfma_f32_16x16x32_bf16 v[72:75], a[112:115], v[200:203], v[72:75]\n"
      "v_exp_f32 v114, v138\n"
      "v_mfma_f32_16x16x32_bf16 v[76:79], a[112:115], v[204:207], v[76:79]\n"
      "ds_read_b128 a[88:91], v243 offset:33792\n"
      "ds_read_b128 a[92:95], v243 offset:37888\n"
      "v_mfma_f32_16x16x32_bf16 v[92:95], a[116:119], v[204:207], v[92:95]\n"
      "v_exp_f32 v115, v139\n"
      "v_mfma_f32_16x16x32_bf16 v[88:91], a[116:119], v[200:203], v[88:91]\n"
      "v_cvt_pk_bf16_f32 v197, v114, v115\n"
      "v_mfma_f32_16x16x32_bf16 a[168:171], a[120:123], v[200:203], a[168:171]\n"
      "v_exp_f32 v112, v140\n"
      "v_mfma_f32_16x16x32_bf16 a[172:175], a[120:123], v[204:207], a[172:175]\n"
      "v_or3_b32 v116, v116, v196, v197\n"
      "v_mfma_f32_16x16x32_bf16 a[188:191], a[124:127], v[204:207], a[188:191]\n"
      "v_exp_f32 v113, v141\n"
      "v_mfma_f32_16x16x32_bf16 a[184:187], a[124:127], v[200:203], a[184:187]\n"
      "s_waitcnt lgkmcnt(0)\n"
      "v_cvt_pk_bf16_f32 v198, v112, v113\n"
      "v_mfma_f32_16x16x32_bf16 v[160:163], a[64:67], a[0:3], v[224:227]\n"
      "v_exp_f32 v114, v142\n"
      "v_mfma_f32_16x16x32_bf16 v[160:163], a[72:75], a[16:19], v[160:163]\n"
      "ds_read_b64_tr_b16 a[96:97], v244 offset:32768\n"
      "ds_read_b64_tr_b16 a[98:99], v244 offset:36864\n"
      "v_mfma_f32_16x16x32_bf16 v[160:163], a[80:83], a[32:35], v[160:163]\n"
      "v_exp_f32 v115, v143\n"
      "v_mfma_f32_16x16x32_bf16 v[160:163], a[88:91], a[48:51], v[160:163]\n"
      "ds_read_b64_tr_b16 a[100:101], v245 offset:32768\n"
      "v_cvt_pk_bf16_f32 v199, v114, v115\n"
      "v_mfma_f32_16x16x32_bf16 v[164:167], a[92:95], a[48:51], v[224:227]\n"
      "v_exp_f32 v112, v144\n"
      "v_mfma_f32_16x16x32_bf16 v[164:167], a[84:87], a[32:35], v[164:167]\n"
      "ds_read_b64_tr_b16 a[102:103], v245 offset:36864\n"
      "v_or3_b32 v116, v116, v198, v199\n"
      "v_mfma_f32_16x16x32_bf16 v[164:167], a[76:79], a[16:19], v[164:167]\n"
      "v_exp_f32 v113, v145\n"
      "v_mfma_f32_16x16x32_bf16 v[164:167], a[68:71], a[0:3], v[164:167]\n"
      "ds_read_b64_tr_b16 a[104:105], v246 offset:32768\n"
      "v_cvt_pk_bf16_f32 v200, v112, v113\n"
      "v_mfma_f32_16x16x32_bf16 v[172:175], a[68:71], a[4:7], v[228:231]\n"
      "v_exp_f32 v114, v146\n"
      "v_mfma_f32_16x16x32_bf16 v[172:175], a[76:79], a[20:23], v[172:175]\n"
      "ds_read_b64_tr_b16 a[106:107], v246 offset:36864\n"
      "ds_read_b64_tr_b16 a[108:109], v247 offset:32768\n"
      "v_mfma_f32_16x16x32_bf16 v[172:175], a[84:87], a[36:39], v[172:175]\n"
      "v_exp_f32 v115, v147\n"
      "v_mfma_f32_16x16x32_bf16 v[172:175], a[92:95], a[52:55], v[172:175]\n"
      "ds_read_b64_tr_b16 a[110:111], v247 offset:36864\n"
      "v_cvt_pk_bf16_f32 v201, v114, v115\n"
      "v_mfma_f32_16x16x32_bf16 v[168:171], a[88:91], a[52:55], v[228:231]\n"
      "v_exp_f32 v112, v148\n"
      "v_mfma_f32_16x16x32_bf16 v[168:171], a[80:83], a[36:39], v[168:171]\n"
      "ds_read_b64_tr_b16 a[112:113], v244 offset:33792\n"
      "v_or3_b32 v116, v116, v200, v201\n"
      "v_mfma_f32_16x16x32_bf16 v[168:171], a[72:75], a[20:23], v[168:171]\n"
      "v_exp_f32 v113, v149\n"
      "v_mfma_f32_16x16x32_bf16 v[168:171], a[64:67], a[4:7], v[168:171]\n"
      "ds_read_b64_tr_b16 a[114:115], v244 offset:37888\n"
      "v_cvt_pk_bf16_f32 v202, v112, v113\n"
      "v_mfma_f32_16x16x32_bf16 v[176:179], a[64:67], a[8:11], v[232:235]\n"
      "v_exp_f32 v114, v150\n"
      "v_mfma_f32_16x16x32_bf16 v[176:179], a[72:75], a[24:27], v[176:179]\n"
      "ds_read_b64_tr_b16 a[116:117], v245 offset:33792\n"
      "ds_read_b64_tr_b16 a[118:119], v245 offset:37888\n"
      "v_mfma_f32_16x16x32_bf16 v[176:179], a[80:83], a[40:43], v[176:179]\n"
      "v_exp_f32 v115, v151\n"
      "v_mfma_f32_16x16x32_bf16 v[176:179], a[88:91], a[56:59], v[176:179]\n"
      "ds_read_b64_tr_b16 a[120:121], v246 offset:33792\n"
      "v_cvt_pk_bf16_f32 v203, v114, v115\n"
      "v_mfma_f32_16x16x32_bf16 v[180:183], a[92:95], a[56:59], v[232:235]\n"
      "v_exp_f32 v112, v152\n"
      "v_mfma_f32_16x16x32_bf16 v[180:183], a[84:87], a[40:43], v[180:183]\n"
      "ds_read_b64_tr_b16 a[122:123], v246 offset:37888\n"
      "v_or3_b32 v116, v116, v202, v203\n"
      "v_mfma_f32_16x16x32_bf16 v[180:183], a[76:79], a[24:27], v[180:183]\n"
      "v_exp_f32 v113, v153\n"
      "v_mfma_f32_16x16x32_bf16 v[180:183], a[68:71], a[8:11], v[180:183]\n"
      "ds_read_b64_tr_b16 a[124:125], v247 offset:33792\n"
      "v_cvt_pk_bf16_f32 v204, v112, v113\n"
      "v_mfma_f32_16x16x32_bf16 v[188:191], a[68:71], a[12:15], v[236:239]\n"
      "v_exp_f32 v114, v154\n"
      "v_mfma_f32_16x16x32_bf16 v[188:191], a[76:79], a[28:31], v[188:191]\n"
      "ds_read_b64_tr_b16 a[126:127], v247 offset:37888\n"
      "v_mfma_f32_16x16x32_bf16 v[188:191], a[84:87], a[44:47], v[188:191]\n"
      "v_exp_f32 v115, v155\n"
      "v_mfma_f32_16x16x32_bf16 v[188:191], a[92:95], a[60:63], v[188:191]\n"
      "v_cvt_pk_bf16_f32 v205, v114, v115\n"
      "v_mfma_f32_16x16x32_bf16 v[184:187], a[88:91], a[60:63], v[236:239]\n"
      "v_exp_f32 v112, v156\n"
      "v_mfma_f32_16x16x32_bf16 v[184:187], a[80:83], a[44:47], v[184:187]\n"
      "v_or3_b32 v116, v116, v204, v205\n"
      "v_mfma_f32_16x16x32_bf16 v[184:187], a[72:75], a[28:31], v[184:187]\n"
      "v_exp_f32 v113, v157\n"
      "v_mfma_f32_16x16x32_bf16 v[184:187], a[64:67], a[12:15], v[184:187]\n"
      "s_waitcnt lgkmcnt(10)\n"
      "v_cvt_pk_bf16_f32 v206, v112, v113\n"
      "v_mfma_f32_16x16x32_bf16 v[32:35], a[96:99], v[208:211], v[32:35]\n"
      "v_exp_f32 v114, v158\n"
      "v_mfma_f32_16x16x32_bf16 v[48:51], a[100:103], v[208:211], v[48:51]\n"
      "v_mfma_f32_16x16x32_bf16 a[128:131], a[104:107], v[208:211], a[128:131]\n"
      "s_waitcnt lgkmcnt(6)\n"
      "v_exp_f32 v115, v159\n"
      "v_mfma_f32_16x16x32_bf16 a[144:147], a[108:111], v[208:211], a[144:147]\n"
      "v_cvt_pk_bf16_f32 v207, v114, v115\n"
      "v_mfma_f32_16x16x32_bf16 v[64:67], a[112:115], v[208:211], v[64:67]\n"
      "s_waitcnt lgkmcnt(2)\n"
      "v_mfma_f32_16x16x32_bf16 v[80:83], a[116:119], v[208:211], v[80:83]\n"
      "v_or3_b32 v116, v116, v206, v207\n"
      "v_mfma_f32_16x16x32_bf16 a[160:163], a[120:123], v[208:211], a[160:163]\n"
      "s_mov_b32 s64, 2\n"
      "s_waitcnt lgkmcnt(0)\n"
      "v_and_b32 v116, 0x40004000, v116\n"
      "v_mfma_f32_16x16x32_bf16 a[176:179], a[124:127], v[208:211], a[176:179]\n"
      "v_cmp_ne_u32 vcc, 0, v116\n"
      "s_cbranch_vccnz FB0_%=\n"
      "RET0_2_%=:\n"
      "v_mfma_f32_16x16x32_bf16 a[180:183], a[124:127], v[212:215], a[180:183]\n"
      "v_exp_f32 v112, v160\n"
      "v_mfma_f32_16x16x32_bf16 a[164:167], a[120:123], v[212:215], a[164:167]\n"
      "v_xor_b32 v242, 0x10000, v242\n"
      "v_mfma_f32_16x16x32_bf16 a[192:195], a[196:199], v[192:195], a[192:195]\n"
      "s_add_u32 m0, s62, 0\n"
      "v_exp_f32 v113, v161\n"
      "v_mfma_f32_16x16x32_bf16 v[84:87], a[116:119], v[212:215], v[84:87]\n"
      "buffer_load_dwordx4 v248, %[srdk], s60 offen nt lds\n"
      "v_cvt_pk_bf16_f32 v208, v112, v113\n"
      "v_xor_b32 v243, 0x10000, v243\n"
      "v_mfma_f32_16x16x32_bf16 v[68:71], a[112:115], v[212:215], v[68:71]\n"
      "s_add_u32 m0, s62, 896\n"
      "v_exp_f32 v114, v162\n"
      "v_mfma_f32_16x16x32_bf16 a[192:195], a[200:203], v[196:199], a[192:195]\n"
      "buffer_load_dwordx4 v248, %[srdk], s60 offen offset:128 nt lds\n"
      "v_xor_b32 v244, 0x10000, v244\n"
      "v_mfma_f32_16x16x32_bf16 a[148:151], a[108:111], v[212:215], a[148:151]\n"
      "s_add_u32 m0, s62, 8192\n"
      "v_exp_f32 v115, v163\n"
      "v_mfma_f32_16x16x32_bf16 a[132:135], a[104:107], v[212:215], a[132:135]\n"
      "s_memrealtime s[68:69]\n"
      "buffer_load_dwordx4 v120, %[srdk], s60 offen nt lds\n"
      "v_cvt_pk_bf16_f32 v209, v114, v115\n"
      "v_xor_b32 v245, 0x10000, v245\n"
      "v_mfma_f32_16x16x32_bf16 a[192:195], a[204:207], v[200:203], a[192:195]\n"
      "s_add_u32 m0, s62, 9088\n"
      "v_exp_f32 v112, v164\n"
      "v_mfma_f32_16x16x32_bf16 v[52:55], a[100:103], v[212:215], v[52:55]\n"
      "s_add_u32 s62, s62, 0x8000\n"
      "buffer_load_dwordx4 v120, %[srdk], s60 offen offset:128 nt lds\n"
      "v_or_b32 v116, v208, v209\n"
      "v_xor_b32 v246, 0x10000, v246\n"
      "v_mfma_f32_16x16x32_bf16 v[36:39], a[96:99], v[212:215], v[36:39]\n"
      "s_and_b32 s62, s62, 0x1ffff\n"
      "v_exp_f32 v113, v165\n"
      "v_mfma_f32_16x16x32_bf16 a[192:195], a[208:211], v[204:207], a[192:195]\n"
      "s_add_u32 s60, s60, s78\n"
      "v_cvt_pk_bf16_f32 v210, v112, v113\n"
      "v_xor_b32 v247, 0x10000, v247\n"
      "s_waitcnt vmcnt(8)\n"
      "s_sub_u32 s63, %[n2], 1\n"
      "LOOP2_%=:\n"
      "s_waitcnt vmcnt(9) lgkmcnt(0)\n"
      "s_sub_u32 s67, s68, s90\n"
      "s_cmp_lt_i32 s67, 0\n"
      "s_cbranch_scc1 GW4_%=\n"
      "GX4_%=:\n"
      "s_barrier\n"
      "v_mfma_f32_16x16x32_bf16 v[40:43], a[96:99], v[216:219], v[40:43]\n"
      "s_memrealtime s[68:69]\n"
      "ds_read_b128 a[64:67], v242 offset:0\n"
      "v_exp_f32 v114, v166\n"
      "v_mfma_f32_16x16x32_bf16 v[44:47], a[96:99], v[220:223], v[44:47]\n"
      "s_add_u32 m0, s77, 16384\n"
      "ds_read_b128 a[68:71], v242 offset:4096\n"
      "ds_read_b128 a[72:75], v243 offset:0\n"
      "v_mfma_f32_16x16x32_bf16 v[60:63], a[100:103], v[220:223], v[60:63]\n"
      "buffer_load_dwordx4 v249, %[srdv], s61 offen nt lds\n"
      "v_exp_f32 v115, v167\n"
      "v_mfma_f32_16x16x32_bf16 v[56:59], a[100:103], v[216:219], v[56:59]\n"
      "s_add_u32 m0, s77, 17408\n"
      "ds_read_b128 a[76:79], v243 offset:4096\n"
      "v_cvt_pk_bf16_f32 v211, v114, v115\n"
      "v_mfma_f32_16x16x32_bf16 a[136:139], a[104:107], v[216:219], a[136:139]\n"
      "buffer_load_dwordx4 v121, %[srdv], s61 offen nt lds\n"
      "v_exp_f32 v112, v168\n"
      "v_mfma_f32_16x16x32_bf16 a[140:143], a[104:107], v[220:223], a[140:143]\n"
      "s_add_u32 m0, s77, 24576\n"
      "ds_read_b128 a[80:83], v242 offset:1024\n"
      "v_or3_b32 v116, v116, v210, v211\n"
      "v_mfma_f32_16x16x32_bf16 a[156:159], a[108:111], v[220:223], a[156:159]\n"
      "buffer_load_dwordx4 v122, %[srdv], s61 offen nt lds\n"
      "v_exp_f32 v113, v169\n"
      "v_mfma_f32_16x16x32_bf16 a[152:155], a[108:111], v[216:219], a[152:155]\n"
      "s_add_u32 m0, s77, 25600\n"
      "ds_read_b128 a[84:87], v242 offset:5120\n"
      "v_cvt_pk_bf16_f32 v212, v112, v113\n"
      "v_mfma_f32_16x16x32_bf16 v[72:75], a[112:115], v[216:219], v[72:75]\n"
      "s_add_u32 s77, s77, 0x8000\n"
      "buffer_load_dwordx4 v123, %[srdv], s61 offen nt lds\n"
      "v_exp_f32 v114, v170\n"
      "v_mfma_f32_16x16x32_bf16 v[76:79], a[112:115], v[220:223], v[76:79]\n"
      "s_and_b32 s77, s77, 0x1ffff\n"
      "ds_read_b128 a[88:91], v243 offset:1024\n"
      "ds_read_b128 a[92:95], v243 offset:5120\n"
      "v_mfma_f32_16x16x32_bf16 v[92:95], a[116:119], v[220:223], v[92:95]\n"
      "s_add_u32 s61, s61, s78\n"
      "v_exp_f32 v115, v171\n"
      "v_mfma_f32_16x16x32_bf16 v[88:91], a[116:119], v[216:219], v[88:91]\n"
      "v_cvt_pk_bf16_f32 v213, v114, v115\n"
      "v_mfma_f32_16x16x32_bf16 a[168:171], a[120:123], v[216:219], a[168:171]\n"
      "v_exp_f32 v112, v172\n"
      "v_mfma_f32_16x16x32_bf16 a[172:175], a[120:123], v[220:223], a[172:175]\n"
      "v_or3_b32 v116, v116, v212, v213\n"
      "v_mfma_f32_16x16x32_bf16 a[188:191], a[124:127], v[220:223], a[188:191]\n"
      "v_exp_f32 v113, v173\n"
      "v_mfma_f32_16x16x32_bf16 a[184:187], a[124:127], v[216:219], a[184:187]\n"
      "s_waitcnt lgkmcnt(0)\n"
      "v_cvt_pk_bf16_f32 v214, v112, v113\n"
      "v_mfma_f32_16x16x32_bf16 v[128:131], a[64:67], a[0:3], v[224:227]\n"
      "s_sub_u32 s67, s68, s66\n"
      "v_exp_f32 v114, v174\n"
      "v_mfma_f32_16x16x32_bf16 v[128:131], a[72:75], a[16:19], v[128:131]\n"
      "s_cmp_gt_i32 s67, s70\n"
      "v_mfma_f32_16x16x32_bf16 v[128:131], a[80:83], a[32:35], v[128:131]\n"
      "s_cselect_b32 s69, s86, 0\n"
      "v_exp_f32 v115, v175\n"
      "v_mfma_f32_16x16x32_bf16 v[128:131], a[88:91], a[48:51], v[128:131]\n"
      "s_add_u32 s66, s66, s69\n"
      "v_cvt_pk_bf16_f32 v215, v114, v115\n"
      "v_mfma_f32_16x16x32_bf16 v[132:135], a[92:95], a[48:51], v[224:227]\n"
      "s_add_u32 s67, s67, s101\n"
      "v_exp_f32 v112, v176\n"
      "v_mfma_f32_16x16x32_bf16 v[132:135], a[84:87], a[32:35], v[132:135]\n"
      "s_min_i32 s70, s70, s67\n"
      "v_or3_b32 v116, v116, v214, v215\n"
      "v_mfma_f32_16x16x32_bf16 v[132:135], a[76:79], a[16:19], v[132:135]\n"
      "s_max_i32 s70, s70, 85\n"
      "v_exp_f32 v113, v177\n"
      "v_mfma_f32_16x16x32_bf16 v[132:135], a[68:71], a[0:3], v[132:135]\n"
      "s_add_u32 s66, s66, s86\n"
      "v_cvt_pk_bf16_f32 v216, v112, v113\n"
      "v_mfma_f32_16x16x32_bf16 v[140:143], a[68:71], a[4:7], v[228:231]\n"
      "s_and_b32 s76, s80, 31\n"
      "v_exp_f32 v114, v178\n"
      "v_mfma_f32_16x16x32_bf16 v[140:143], a[76:79], a[20:23], v[140:143]\n"
      "s_lshr_b64 s[80:81], s[80:81], 5\n"
      "v_mfma_f32_16x16x32_bf16 v[140:143], a[84:87], a[36:39], v[140:143]\n"
      "s_add_u32 s66, s66, s76\n"
      "v_exp_f32 v115, v179\n"
      "v_mfma_f32_16x16x32_bf16 v[140:143], a[92:95], a[52:55], v[140:143]\n"
      "s_sub_u32 s67, s68, s82\n"
      "v_cvt_pk_bf16_f32 v217, v114, v115\n"
      "v_mfma_f32_16x16x32_bf16 v[136:139], a[88:91], a[52:55], v[228:231]\n"
      "s_min_i32 s76, s67, s83\n"
      "v_exp_f32 v112, v180\n"
      "s_waitcnt vmcnt(8)\n"
      "s_barrier\n"
      "v_mfma_f32_16x16x32_bf16 v[136:139], a[80:83], a[36:39], v[136:139]\n"
      "s_mov_b32 s83, s67\n"
      "ds_read_b64_tr_b16 a[96:97], v244 offset:0\n"
      "v_or3_b32 v116, v116, v216, v217\n"
      "v_mfma_f32_16x16x32_bf16 v[136:139], a[72:75], a[20:23], v[136:139]\n"
      "s_sub_u32 s76, s76, 36\n"
      "v_exp_f32 v113, v181\n"
      "v_mfma_f32_16x16x32_bf16 v[136:139], a[64:67], a[4:7], v[136:139]\n"
      "s_min_i32 s76, s76, 148\n"
      "ds_read_b64_tr_b16 a[98:99], v244 offset:4096\n"
      "v_cvt_pk_bf16_f32 v218, v112, v113\n"
      "v_mfma_f32_16x16x32_bf16 v[144:147], a[64:67], a[8:11], v[232:235]\n"
      "s_max_i32 s76, s76, 70\n"
      "v_exp_f32 v114, v182\n"
      "v_mfma_f32_16x16x32_bf16 v[144:147], a[72:75], a[24:27], v[144:147]\n"
      "s_mov_b32 s82, s68\n"
      "ds_read_b64_tr_b16 a[100:101], v245 offset:0\n"
      "ds_read_b64_tr_b16 a[102:103], v245 offset:4096\n"
      "v_mfma_f32_16x16x32_bf16 v[144:147], a[80:83], a[40:43], v[144:147]\n"
      "s_add_u32 s65, s82, s76\n"
      "v_exp_f32 v115, v183\n"
      "v_mfma_f32_16x16x32_bf16 v[144:147], a[88:91], a[56:59], v[144:147]\n"
      "s_sub_u32 s93, s93, 1\n"
      "ds_read_b64_tr_b16 a[104:105], v246 offset:0\n"
      "v_cvt_pk_bf16_f32 v219, v114, v115\n"
      "v_mfma_f32_16x16x32_bf16 v[148:151], a[92:95], a[56:59], v[232:235]\n"
      "s_cmp_le_i32 s93, 14\n"
      "v_exp_f32 v112, v184\n"
      "v_mfma_f32_16x16x32_bf16 v[148:151], a[84:87], a[40:43], v[148:151]\n"
      "s_cselect_b32 s86, 107, s86\n"
      "ds_read_b64_tr_b16 a[106:107], v246 offset:4096\n"
      "v_or3_b32 v116, v116, v218, v219\n"
      "v_mfma_f32_16x16x32_bf16 v[148:151], a[76:79], a[24:27], v[148:151]\n"
      "s_sub_u32 s67, s65, s66\n"
      "v_exp_f32 v113, v185\n"
      "v_mfma_f32_16x16x32_bf16 v[148:151], a[68:71], a[8:11], v[148:151]\n"
      "s_cmp_lt_i32 s67, 0\n"
      "ds_read_b64_tr_b16 a[108:109], v247 offset:0\n"
      "v_cvt_pk_bf16_f32 v220, v112, v113\n"
      "v_mfma_f32_16x16x32_bf16 v[156:159], a[68:71], a[12:15], v[236:239]\n"
      "s_cselect_b32 s88, s66, s65\n"
      "v_exp_f32 v114, v186\n"
      "v_mfma_f32_16x16x32_bf16 v[156:159], a[76:79], a[28:31], v[156:159]\n"
      "s_add_u32 s90, s88, s91\n"
      "ds_read_b64_tr_b16 a[110:111], v247 offset:4096\n"
      "ds_read_b64_tr_b16 a[112:113], v244 offset:1024\n"
      "v_mfma_f32_16x16x32_bf16 v[156:159], a[84:87], a[44:47], v[156:159]\n"
      "v_exp_f32 v115, v187\n"
      "v_mfma_f32_16x16x32_bf16 v[156:159], a[92:95], a[60:63], v[156:159]\n"
      "ds_read_b64_tr_b16 a[114:115], v244 offset:5120\n"
      "v_cvt_pk_bf16_f32 v221, v114, v115\n"
      "v_mfma_f32_16x16x32_bf16 v[152:155], a[88:91], a[60:63], v[236:239]\n"
      "v_exp_f32 v112, v188\n"
      "v_mfma_f32_16x16x32_bf16 v[152:155], a[80:83], a[44:47], v[152:155]\n"
      "ds_read_b64_tr_b16 a[116:117], v245 offset:1024\n"
      "v_or3_b32 v116, v116, v220, v221\n"
      "v_mfma_f32_16x16x32_bf16 v[152:155], a[72:75], a[28:31], v[152:155]\n"
      "v_exp_f32 v113, v189\n"
      "v_mfma_f32_16x16x32_bf16 v[152:155], a[64:67], a[12:15], v[152:155]\n"
      "ds_read_b64_tr_b16 a[118:119], v245 offset:5120\n"
      "v_cvt_pk_bf16_f32 v222, v112, v113\n"
      "s_waitcnt lgkmcnt(6)\n"
      "v_mfma_f32_16x16x32_bf16 v[32:35], a[96:99], v[192:195], v[32:35]\n"
      "v_exp_f32 v114, v190\n"
      "v_mfma_f32_16x16x32_bf16 v[48:51], a[100:103], v[192:195], v[48:51]\n"
      "ds_read_b64_tr_b16 a[120:121], v246 offset:1024\n"
      "ds_read_b64_tr_b16 a[122:123], v246 offset:5120\n"
      "v_mfma_f32_16x16x32_bf16 a[128:131], a[104:107], v[192:195], a[128:131]\n"
      "s_waitcnt lgkmcnt(4)\n"
      "v_exp_f32 v115, v191\n"
      "v_mfma_f32_16x16x32_bf16 a[144:147], a[108:111], v[192:195], a[144:147]\n"
      "ds_read_b64_tr_b16 a[124:125], v247 offset:1024\n"
      "v_cvt_pk_bf16_f32 v223, v114, v115\n"
      "v_mfma_f32_16x16x32_bf16 v[64:67], a[112:115], v[192:195], v[64:67]\n"
      "ds_read_b64_tr_b16 a[126:127], v247 offset:5120\n"
      "s_waitcnt lgkmcnt(2)\n"
      "v_mfma_f32_16x16x32_bf16 v[80:83], a[116:119], v[192:195], v[80:83]\n"
      "v_or3_b32 v116, v116, v222, v223\n"
      "v_mfma_f32_16x16x32_bf16 a[160:163], a[120:123], v[192:195], a[160:163]\n"
      "s_mov_b32 s64, 3\n"
      "s_waitcnt lgkmcnt(0)\n"
      "v_and_b32 v116, 0x40004000, v116\n"
      "v_mfma_f32_16x16x32_bf16 a[176:179], a[124:127], v[192:195], a[176:179]\n"
      "v_cmp_ne_u32 vcc, 0, v116\n"
      "s_cbranch_vccnz FB1_%=\n"
      "RET1_3_%=:\n"
      "v_mfma_f32_16x16x32_bf16 a[180:183], a[124:127], v[196:199], a[180:183]\n"
      "v_exp_f32 v112, v128\n"
      "v_mfma_f32_16x16x32_bf16 a[164:167], a[120:123], v[196:199], a[164:167]\n"
      "v_mfma_f32_16x16x32_bf16 a[192:195], a[196:199], v[208:211], a[192:195]\n"
      "s_add_u32 m0, s62, 0\n"
      "v_exp_f32 v113, v129\n"
      "v_mfma_f32_16x16x32_bf16 v[84:87], a[116:119], v[196:199], v[84:87]\n"
      "buffer_load_dwordx4 v248, %[srdk], s60 offen nt lds\n"
      "v_cvt_pk_bf16_f32 v192, v112, v113\n"
      "v_mfma_f32_16x16x32_bf16 v[68:71], a[112:115], v[196:199], v[68:71]\n"
      "s_add_u32 m0, s62, 896\n"
      "v_exp_f32 v114, v130\n"
      "v_mfma_f32_16x16x32_bf16 a[192:195], a[200:203], v[212:215], a[192:195]\n"
      "buffer_load_dwordx4 v248, %[srdk], s60 offen offset:128 nt lds\n"
      "v_mfma_f32_16x16x32_bf16 a[148:151], a[108:111], v[196:199], a[148:151]\n"
      "s_add_u32 m0, s62, 8192\n"
      "v_exp_f32 v115, v131\n"
      "v_mfma_f32_16x16x32_bf16 a[132:135], a[104:107], v[196:199], a[132:135]\n"
      "s_memrealtime s[68:69]\n"
      "buffer_load_dwordx4 v120, %[srdk], s60 offen nt lds\n"
      "v_cvt_pk_bf16_f32 v193, v114, v115\n"
      "v_mfma_f32_16x16x32_bf16 a[192:195], a[204:207], v[216:219], a[192:195]\n"
      "s_add_u32 m0, s62, 9088\n"
      "v_exp_f32 v112, v132\n"
      "v_mfma_f32_16x16x32_bf16 v[52:55], a[100:103], v[196:199], v[52:55]\n"
      "s_add_u32 s62, s62, 0x8000\n"
      "buffer_load_dwordx4 v120, %[srdk], s60 offen offset:128 nt lds\n"
      "v_or_b32 v116, v192, v193\n"
      "v_mfma_f32_16x16x32_bf16 v[36:39], a[96:99], v[196:199], v[36:39]\n"
      "s_and_b32 s62, s62, 0x1ffff\n"
      "v_exp_f32 v113, v133\n"
      "v_mfma_f32_16x16x32_bf16 a[192:195], a[208:211], v[220:223], a[192:195]\n"
      "s_add_u32 s60, s60, %[inc]\n"
      "v_cvt_pk_bf16_f32 v194, v112, v113\n"
      "s_waitcnt vmcnt(8) lgkmcnt(0)\n"
      "s_sub_u32 s67, s68, s90\n"
      "s_cmp_lt_i32 s67, 0\n"
      "s_cbranch_scc1 GW5_%=\n"
      "GX5_%=:\n"
      "s_barrier\n"
      "v_mfma_f32_16x16x32_bf16 v[40:43], a[96:99], v[200:203], v[40:43]\n"
      "s_memrealtime s[68:69]\n"
      "buffer_load_dword v250, v251, %[srdr], 0 offen sc1\n"
      "ds_read_b128 a[64:67], v242 offset:32768\n"
      "v_exp_f32 v114, v134\n"
      "v_mfma_f32_16x16x32_bf16 v[44:47], a[96:99], v[204:207], v[44:47]\n"
      "s_add_u32 m0, s77, 16384\n"
      "ds_read_b128 a[68:71], v242 offset:36864\n"
      "ds_read_b128 a[72:75], v243 offset:32768\n"
      "v_mfma_f32_16x16x32_bf16 v[60:63], a[100:103], v[204:207], v[60:63]\n"
      "buffer_load_dwordx4 v249, %[srdv], s61 offen nt lds\n"
      "v_exp_f32 v115, v135\n"
      "v_mfma_f32_16x16x32_bf16 v[56:59], a[100:103], v[200:203], v[56:59]\n"
      "s_add_u32 m0, s77, 17408\n"
      "ds_read_b128 a[76:79], v243 offset:36864\n"
      "v_cvt_pk_bf16_f32 v195, v114, v115\n"
      "v_mfma_f32_16x16x32_bf16 a[136:139], a[104:107], v[200:203], a[136:139]\n"
      "buffer_load_dwordx4 v121, %[srdv], s61 offen nt lds\n"
      "v_exp_f32 v112, v136\n"
      "v_mfma_f32_16x16x32_bf16 a[140:143], a[104:107], v[204:207], a[140:143]\n"
      "s_add_u32 m0, s77, 24576\n"
      "ds_read_b128 a[80:83], v242 offset:33792\n"
      "v_or3_b32 v116, v116, v194, v195\n"
      "v_mfma_f32_16x16x32_bf16 a[156:159], a[108:111], v[204:207], a[156:159]\n"
      "buffer_load_dwordx4 v122, %[srdv], s61 offen nt lds\n"
      "v_exp_f32 v113, v137\n"
      "v_mfma_f32_16x16x32_bf16 a[152:155], a[108:111], v[200:203], a[152:155]\n"
      "s_add_u32 m0, s77, 25600\n"
      "ds_read_b128 a[84:87], v242 offset:37888\n"
      "v_cvt_pk_bf16_f32 v196, v112, v113\n"
      "v_mfma_f32_16x16x32_bf16 v[72:75], a[112:115], v[200:203], v[72:75]\n"
      "s_add_u32 s77, s77, 0x8000\n"
      "buffer_load_dwordx4 v123, %[srdv], s61 offen nt lds\n"
      "v_exp_f32 v114, v138\n"
      "v_mfma_f32_16x16x32_bf16 v[76:79], a[112:115], v[204:207], v[76:79]\n"
      "s_and_b32 s77, s77, 0x1ffff\n"
      "ds_read_b128 a[88:91], v243 offset:33792\n"
      "ds_read_b128 a[92:95], v243 offset:37888\n"
      "v_mfma_f32_16x16x32_bf16 v[92:95], a[116:119], v[204:207], v[92:95]\n"
      "s_add_u32 s61, s61, %[inc]\n"
      "v_exp_f32 v115, v139\n"
      "v_mfma_f32_16x16x32_bf16 v[88:91], a[116:119], v[200:203], v[88:91]\n"
      "v_cvt_pk_bf16_f32 v197, v114, v115\n"
      "v_mfma_f32_16x16x32_bf16 a[168:171], a[120:123], v[200:203], a[168:171]\n"
      "v_exp_f32 v112, v140\n"
      "v_mfma_f32_16x16x32_bf16 a[172:175], a[120:123], v[204:207], a[172:175]\n"
      "v_or3_b32 v116, v116, v196, v197\n"
      "v_mfma_f32_16x16x32_bf16 a[188:191], a[124:127], v[204:207], a[188:191]\n"
      "v_exp_f32 v113, v141\n"
      "v_mfma_f32_16x16x32_bf16 a[184:187], a[124:127], v[200:203], a[184:187]\n"
      "s_waitcnt lgkmcnt(0)\n"
      "v_cvt_pk_bf16_f32 v198, v112, v113\n"
      "v_mfma_f32_16x16x32_bf16 v[160:163], a[64:67], a[0:3], v[224:227]\n"
      "s_sub_u32 s67, s68, s66\n"
      "v_exp_f32 v114, v142\n"
      "v_mfma_f32_16x16x32_bf16 v[160:163], a[72:75], a[16:19], v[160:163]\n"
      "s_cmp_gt_i32 s67, s70\n"
      "v_mfma_f32_16x16x32_bf16 v[160:163], a[80:83], a[32:35], v[160:163]\n"
      "s_cselect_b32 s69, s86, 0\n"
      "v_exp_f32 v115, v143\n"
      "v_mfma_f32_16x16x32_bf16 v[160:163], a[88:91], a[48:51], v[160:163]\n"
      "s_add_u32 s66, s66, s69\n"
      "v_cvt_pk_bf16_f32 v199, v114, v115\n"
      "v_mfma_f32_16x16x32_bf16 v[164:167], a[92:95], a[48:51], v[224:227]\n"
      "s_add_u32 s67, s67, s101\n"
      "v_exp_f32 v112, v144\n"
      "v_mfma_f32_16x16x32_bf16 v[164:167], a[84:87], a[32:35], v[164:167]\n"
      "s_min_i32 s70, s70, s67\n"
      "v_or3_b32 v116, v116, v198, v199\n"
      "v_mfma_f32_16x16x32_bf16 v[164:167], a[76:79], a[16:19], v[164:167]\n"
      "s_max_i32 s70, s70, 85\n"
      "v_exp_f32 v113, v145\n"
      "v_mfma_f32_16x16x32_bf16 v[164:167], a[68:71], a[0:3], v[164:167]\n"
      "s_add_u32 s66, s66, s86\n"
      "v_cvt_pk_bf16_f32 v200, v112, v113\n"
      "v_mfma_f32_16x16x32_bf16 v[172:175], a[68:71], a[4:7], v[228:231]\n"
      "s_and_b32 s76, s80, 31\n"
      "v_exp_f32 v114, v146\n"
      "v_mfma_f32_16x16x32_bf16 v[172:175], a[76:79], a[20:23], v[172:175]\n"
      "s_lshr_b64 s[80:81], s[80:81], 5\n"
      "v_mfma_f32_16x16x32_bf16 v[172:175], a[84:87], a[36:39], v[172:175]\n"
      "s_add_u32 s66, s66, s76\n"
      "v_exp_f32 v115, v147\n"
      "v_mfma_f32_16x16x32_bf16 v[172:175], a[92:95], a[52:55], v[172:175]\n"
      "s_sub_u32 s67, s68, s82\n"
      "v_cvt_pk_bf16_f32 v201, v114, v115\n"
      "v_mfma_f32_16x16x32_bf16 v[168:171], a[88:91], a[52:55], v[228:231]\n"
      "s_min_i32 s76, s67, s83\n"
      "v_exp_f32 v112, v148\n"
      "s_waitcnt vmcnt(9)\n"
      "s_barrier\n"
      "v_mfma_f32_16x16x32_bf16 v[168:171], a[80:83], a[36:39], v[168:171]\n"
      "s_mov_b32 s83, s67\n"
      "ds_read_b64_tr_b16 a[96:97], v244 offset:32768\n"
      "v_or3_b32 v116, v116, v200, v201\n"
      "v_mfma_f32_16x16x32_bf16 v[168:171], a[72:75], a[20:23], v[168:171]\n"
      "s_sub_u32 s76, s76, 36\n"
      "v_exp_f32 v113, v149\n"
      "v_mfma_f32_16x16x32_bf16 v[168:171], a[64:67], a[4:7], v[168:171]\n"
      "s_min_i32 s76, s76, 148\n"
      "ds_read_b64_tr_b16 a[98:99], v244 offset:36864\n"
      "v_cvt_pk_bf16_f32 v202, v112, v113\n"
      "v_mfma_f32_16x16x32_bf16 v[176:179], a[64:67], a[8:11], v[232:235]\n"
      "s_max_i32 s76, s76, 70\n"
      "v_exp_f32 v114, v150\n"
      "v_mfma_f32_16x16x32_bf16 v[176:179], a[72:75], a[24:27], v[176:179]\n"
      "s_mov_b32 s82, s68\n"
      "ds_read_b64_tr_b16 a[100:101], v245 offset:32768\n"
      "ds_read_b64_tr_b16 a[102:103], v245 offset:36864\n"
      "v_mfma_f32_16x16x32_bf16 v[176:179], a[80:83], a[40:43], v[176:179]\n"
      "s_add_u32 s65, s82, s76\n"
      "v_exp_f32 v115, v151\n"
      "v_mfma_f32_16x16x32_bf16 v[176:179], a[88:91], a[56:59], v[176:179]\n"
      "s_sub_u32 s93, s93, 1\n"
      "ds_read_b64_tr_b16 a[104:105], v246 offset:32768\n"
      "v_cvt_pk_bf16_f32 v203, v114, v115\n"
      "v_mfma_f32_16x16x32_bf16 v[180:183], a[92:95], a[56:59], v[232:235]\n"
      "s_cmp_le_i32 s93, 14\n"
      "v_exp_f32 v112, v152\n"
      "v_mfma_f32_16x16x32_bf16 v[180:183], a[84:87], a[40:43], v[180:183]\n"
      "s_cselect_b32 s86, 107, s86\n"
      "ds_read_b64_tr_b16 a[106:107], v246 offset:36864\n"
      "v_or3_b32 v116, v116, v202, v203\n"
      "v_mfma_f32_16x16x32_bf16 v[180:183], a[76:79], a[24:27], v[180:183]\n"
      "s_sub_u32 s67, s65, s66\n"
      "v_exp_f32 v113, v153\n"
      "v_mfma_f32_16x16x32_bf16 v[180:183], a[68:71], a[8:11], v[180:183]\n"
      "s_cmp_lt_i32 s67, 0\n"
      "ds_read_b64_tr_b16 a[108:109], v247 offset:32768\n"
      "v_cvt_pk_bf16_f32 v204, v112, v113\n"
      "v_mfma_f32_16x16x32_bf16 v[188:191], a[68:71], a[12:15], v[236:239]\n"
      "s_cselect_b32 s88, s66, s65\n"
      "v_exp_f32 v114, v154\n"
      "v_mfma_f32_16x16x32_bf16 v[188:191], a[76:79], a[28:31], v[188:191]\n"
      "s_add_u32 s90, s88, s91\n"
      "ds_read_b64_tr_b16 a[110:111], v247 offset:36864\n"
      "ds_read_b64_tr_b16 a[112:113], v244 offset:33792\n"
      "v_mfma_f32_16x16x32_bf16 v[188:191], a[84:87], a[44:47], v[188:191]\n"
      "v_exp_f32 v115, v155\n"
      "v_mfma_f32_16x16x32_bf16 v[188:191], a[92:95], a[60:63], v[188:191]\n"
      "ds_read_b64_tr_b16 a[114:115], v244 offset:37888\n"
      "v_cvt_pk_bf16_f32 v205, v114, v115\n"
      "v_mfma_f32_16x16x32_bf16 v[184:187], a[88:91], a[60:63], v[236:239]\n"
      "v_exp_f32 v112, v156\n"
      "v_mfma_f32_16x16x32_bf16 v[184:187], a[80:83], a[44:47], v[184:187]\n"
      "ds_read_b64_tr_b16 a[116:117], v245 offset:33792\n"
      "v_or3_b32 v116, v116, v204, v205\n"
      "v_mfma_f32_16x16x32_bf16 v[184:187], a[72:75], a[28:31], v[184:187]\n"
      "v_exp_f32 v113, v157\n"
      "v_mfma_f32_16x16x32_bf16 v[184:187], a[64:67], a[12:15], v[184:187]\n"
      "ds_read_b64_tr_b16 a[118:119], v245 offset:37888\n"
      "v_cvt_pk_bf16_f32 v206, v112, v113\n"
      "s_waitcnt lgkmcnt(6)\n"
      "v_mfma_f32_16x16x32_bf16 v[32:35], a[96:99], v[208:211], v[32:35]\n"
      "v_exp_f32 v114, v158\n"
      "v_mfma_f32_16x16x32_bf16 v[48:51], a[100:103], v[208:211], v[48:51]\n"
      "ds_read_b64_tr_b16 a[120:121], v246 offset:33792\n"
      "ds_read_b64_tr_b16 a[122:123], v246 offset:37888\n"
      "v_mfma_f32_16x16x32_bf16 a[128:131], a[104:107], v[208:211], a[128:131]\n"
      "s_waitcnt lgkmcnt(4)\n"
      "v_exp_f32 v115, v159\n"
      "v_mfma_f32_16x16x32_bf16 a[144:147], a[108:111], v[208:211], a[144:147]\n"
      "ds_read_b64_tr_b16 a[124:125], v247 offset:33792\n"
      "v_cvt_pk_bf16_f32 v207, v114, v115\n"
      "v_mfma_f32_16x16x32_bf16 v[64:67], a[112:115], v[208:211], v[64:67]\n"
      "ds_read_b64_tr_b16 a[126:127], v247 offset:37888\n"
      "s_waitcnt lgkmcnt(2)\n"
      "v_mfma_f32_16x16x32_bf16 v[80:83], a[116:119], v[208:211], v[80:83]\n"
      "v_or3_b32 v116, v116, v206, v207\n"
      "v_mfma_f32_16x16x32_bf16 a[160:163], a[120:123], v[208:211], a[160:163]\n"
      "s_mov_b32 s64, 3\n"
      "s_waitcnt lgkmcnt(0)\n"
      "v_and_b32 v116, 0x40004000, v116\n"
      "v_mfma_f32_16x16x32_bf16 a[176:179], a[124:127], v[208:211], a[176:179]\n"
      "v_cmp_ne_u32 vcc, 0, v116\n"
      "s_cbranch_vccnz FB0_%=\n"
      "RET0_3_%=:\n"
      "v_mfma_f32_16x16x32_bf16 a[180:183], a[124:127], v[212:215], a[180:183]\n"
      "v_exp_f32 v112, v160\n"
      "v_mfma_f32_16x16x32_bf16 a[164:167], a[120:123], v[212:215], a[164:167]\n"
      "v_xor_b32 v242, 0x10000, v242\n"
      "v_mfma_f32_16x16x32_bf16 a[192:195], a[196:199], v[192:195], a[192:195]\n"
      "s_add_u32 m0, s62, 0\n"
      "v_exp_f32 v113, v161\n"
      "v_mfma_f32_16x16x32_bf16 v[84:87], a[116:119], v[212:215], v[84:87]\n"
      "buffer_load_dwordx4 v248, %[srdk], s60 offen nt lds\n"
      "v_cvt_pk_bf16_f32 v208, v112, v113\n"
      "v_xor_b32 v243, 0x10000, v243\n"
      "v_mfma_f32_16x16x32_bf16 v[68:71], a[112:115], v[212:215], v[68:71]\n"
      "s_add_u32 m0, s62, 896\n"
      "v_exp_f32 v114, v162\n"
      "v_mfma_f32_16x16x32_bf16 a[192:195], a[200:203], v[196:199], a[192:195]\n"
      "buffer_load_dwordx4 v248, %[srdk], s60 offen offset:128 nt lds\n"
      "v_xor_b32 v244, 0x10000, v244\n"
      "v_mfma_f32_16x16x32_bf16 a[148:151], a[108:111], v[212:215], a[148:151]\n"
      "s_add_u32 m0, s62, 8192\n"
      "v_exp_f32 v115, v163\n"
      "v_mfma_f32_16x16x32_bf16 a[132:135], a[104:107], v[212:215], a[132:135]\n"
      "s_memrealtime s[68:69]\n"
      "buffer_load_dwordx4 v120, %[srdk], s60 offen nt lds\n"
      "v_cvt_pk_bf16_f32 v209, v114, v115\n"
      "v_xor_b32 v245, 0x10000, v245\n"
      "v_mfma_f32_16x16x32_bf16 a[192:195], a[204:207], v[200:203], a[192:195]\n"
      "s_add_u32 m0, s62, 9088\n"
      "v_exp_f32 v112, v164\n"
      "v_mfma_f32_16x16x32_bf16 v[52:55], a[100:103], v[212:215], v[52:55]\n"
      "s_add_u32 s62, s62, 0x8000\n"
      "buffer_load_dwordx4 v120, %[srdk], s60 offen offset:128 nt lds\n"
      "v_or_b32 v116, v208, v209\n"
      "v_xor_b32 v246, 0x10000, v246\n"
      "v_mfma_f32_16x16x32_bf16 v[36:39], a[96:99], v[212:215], v[36:39]\n"
      "s_and_b32 s62, s62, 0x1ffff\n"
      "s_add_u32 s60, s60, s78\n"
      "v_exp_f32 v113, v165\n"
      "v_mfma_f32_16x16x32_bf16 a[192:195], a[208:211], v[204:207], a[192:195]\n"
      "s_sub_u32 s63, s63, 1\n"
      "v_cvt_pk_bf16_f32 v210, v112, v113\n"
      "v_xor_b32 v247, 0x10000, v247\n"
      "s_cbranch_scc0 LOOP2_%=\n"
      "s_waitcnt vmcnt(9) lgkmcnt(0)\n"
      "s_sub_u32 s67, s68, s90\n"
      "s_cmp_lt_i32 s67, 0\n"
      "s_cbranch_scc1 GW6_%=\n"
      "GX6_%=:\n"
      "s_barrier\n"
      "v_mfma_f32_16x16x32_bf16 v[40:43], a[96:99], v[216:219], v[40:43]\n"
      "s_memrealtime s[68:69]\n"
      "ds_read_b128 a[64:67], v242 offset:0\n"
      "v_exp_f32 v114, v166\n"
      "v_mfma_f32_16x16x32_bf16 v[44:47], a[96:99], v[220:223], v[44:47]\n"
      "s_add_u32 m0, %[wb], 50176\n"
      "ds_read_b128 a[68:71], v242 offset:4096\n"
      "ds_read_b128 a[72:75], v243 offset:0\n"
      "v_mfma_f32_16x16x32_bf16 v[60:63], a[100:103], v[220:223], v[60:63]\n"
      "buffer_load_dwordx4 v121, %[srdv], s61 offen nt lds\n"
      "v_exp_f32 v115, v167\n"
      "v_mfma_f32_16x16x32_bf16 v[56:59], a[100:103], v[216:219], v[56:59]\n"
      "s_add_u32 m0, %[wb], 58368\n"
      "ds_read_b128 a[76:79], v243 offset:4096\n"
      "v_cvt_pk_bf16_f32 v211, v114, v115\n"
      "v_mfma_f32_16x16x32_bf16 a[136:139], a[104:107], v[216:219], a[136:139]\n"
      "buffer_load_dwordx4 v123, %[srdv], s61 offen nt lds\n"
      "v_exp_f32 v112, v168\n"
      "v_mfma_f32_16x16x32_bf16 a[140:143], a[104:107], v[220:223], a[140:143]\n"
      "s_add_u32 s94, s61, s78\n"
      "ds_read_b128 a[80:83], v242 offset:1024\n"
      "v_or3_b32 v116, v116, v210, v211\n"
      "v_mfma_f32_16x16x32_bf16 a[156:159], a[108:111], v[220:223], a[156:159]\n"
      "s_add_u32 s95, s61, 0x10000\n"
      "v_exp_f32 v113, v169\n"
      "v_mfma_f32_16x16x32_bf16 a[152:155], a[108:111], v[216:219], a[152:155]\n"
      "s_add_u32 s77, s94, 0x10000\n"
      "ds_read_b128 a[84:87], v242 offset:5120\n"
      "v_cvt_pk_bf16_f32 v212, v112, v113\n"
      "v_mfma_f32_16x16x32_bf16 v[72:75], a[112:115], v[216:219], v[72:75]\n"
      "s_add_u32 s78, s61, 0x20000\n"
      "v_exp_f32 v114, v170\n"
      "v_mfma_f32_16x16x32_bf16 v[76:79], a[112:115], v[220:223], v[76:79]\n"
      "ds_read_b128 a[88:91], v243 offset:1024\n"
      "ds_read_b128 a[92:95], v243 offset:5120\n"
      "v_mfma_f32_16x16x32_bf16 v[92:95], a[116:119], v[220:223], v[92:95]\n"
      "v_exp_f32 v115, v171\n"
      "v_mfma_f32_16x16x32_bf16 v[88:91], a[116:119], v[216:219], v[88:91]\n"
      "v_cvt_pk_bf16_f32 v213, v114, v115\n"
      "v_mfma_f32_16x16x32_bf16 a[168:171], a[120:123], v[216:219], a[168:171]\n"
      "v_exp_f32 v112, v172\n"
      "v_mfma_f32_16x16x32_bf16 a[172:175], a[120:123], v[220:223], a[172:175]\n"
      "v_or3_b32 v116, v116, v212, v213\n"
      "v_mfma_f32_16x16x32_bf16 a[188:191], a[124:127], v[220:223], a[188:191]\n"
      "v_exp_f32 v113, v173\n"
      "v_mfma_f32_16x16x32_bf16 a[184:187], a[124:127], v[216:219], a[184:187]\n"
      "s_waitcnt lgkmcnt(0)\n"
      "v_cvt_pk_bf16_f32 v214, v112, v113\n"
      "v_mfma_f32_16x16x32_bf16 v[128:131], a[64:67], a[0:3], v[224:227]\n"
      "s_sub_u32 s67, s68, s66\n"
      "v_exp_f32 v114, v174\n"
      "v_mfma_f32_16x16x32_bf16 v[128:131], a[72:75], a[16:19], v[128:131]\n"
      "s_cmp_gt_i32 s67, s70\n"
      "v_mfma_f32_16x16x32_bf16 v[128:131], a[80:83], a[32:35], v[128:131]\n"
      "s_cselect_b32 s69, s86, 0\n"
      "v_exp_f32 v115, v175\n"
      "v_mfma_f32_16x16x32_bf16 v[128:131], a[88:91], a[48:51], v[128:131]\n"
      "s_add_u32 s66, s66, s69\n"
      "v_cvt_pk_bf16_f32 v215, v114, v115\n"
      "v_mfma_f32_16x16x32_bf16 v[132:135], a[92:95], a[48:51], v[224:227]\n"
      "s_add_u32 s67, s67, s101\n"
      "v_exp_f32 v112, v176\n"
      "v_mfma_f32_16x16x32_bf16 v[132:135], a[84:87], a[32:35], v[132:135]\n"
      "s_min_i32 s70, s70, s67\n"
      "v_or3_b32 v116, v116, v214, v215\n"
      "v_mfma_f32_16x16x32_bf16 v[132:135], a[76:79], a[16:19], v[132:135]\n"
      "s_max_i32 s70, s70, 85\n"
      "v_exp_f32 v113, v177\n"
      "v_mfma_f32_16x16x32_bf16 v[132:135], a[68:71], a[0:3], v[132:135]\n"
      "s_add_u32 s66, s66, s86\n"
      "v_cvt_pk_bf16_f32 v216, v112, v113\n"
      "v_mfma_f32_16x16x32_bf16 v[140:143], a[68:71], a[4:7], v[228:231]\n"
      "s_and_b32 s76, s80, 31\n"
      "v_exp_f32 v114, v178\n"
      "v_mfma_f32_16x16x32_bf16 v[140:143], a[76:79], a[20:23], v[140:143]\n"
      "s_lshr_b64 s[80:81], s[80:81], 5\n"
      "v_mfma_f32_16x16x32_bf16 v[140:143], a[84:87], a[36:39], v[140:143]\n"
      "s_add_u32 s66, s66, s76\n"
      "v_exp_f32 v115, v179\n"
      "v_mfma_f32_16x16x32_bf16 v[140:143], a[92:95], a[52:55], v[140:143]\n"
      "s_sub_u32 s67, s68, s82\n"
      "v_cvt_pk_bf16_f32 v217, v114, v115\n"
      "v_mfma_f32_16x16x32_bf16 v[136:139], a[88:91], a[52:55], v[228:231]\n"
      "s_min_i32 s76, s67, s83\n"
      "v_exp_f32 v112, v180\n"
      "s_waitcnt vmcnt(6)\n"
      "s_barrier\n"
      "v_mfma_f32_16x16x32_bf16 v[136:139], a[80:83], a[36:39], v[136:139]\n"
      "s_mov_b32 s83, s67\n"
      "ds_read_b64_tr_b16 a[96:97], v244 offset:0\n"
      "v_or3_b32 v116, v116, v216, v217\n"
      "v_mfma_f32_16x16x32_bf16 v[136:139], a[72:75], a[20:23], v[136:139]\n"
      "s_add_u32 m0, s62, 0\n"
      "v_exp_f32 v113, v181\n"
      "v_mfma_f32_16x16x32_bf16 v[136:139], a[64:67], a[4:7], v[136:139]\n"
      "s_sub_u32 s76, s76, 36\n"
      "ds_read_b64_tr_b16 a[98:99], v244 offset:4096\n"
      "buffer_load_dwordx4 v248, %[srdk], s60 offen nt lds\n"
      "v_cvt_pk_bf16_f32 v218, v112, v113\n"
      "v_mfma_f32_16x16x32_bf16 v[144:147], a[64:67], a[8:11], v[232:235]\n"
      "s_add_u32 m0, s62, 896\n"
      "v_exp_f32 v114, v182\n"
      "v_mfma_f32_16x16x32_bf16 v[144:147], a[72:75], a[24:27], v[144:147]\n"
      "s_min_i32 s76, s76, 148\n"
      "ds_read_b64_tr_b16 a[100:101], v245 offset:0\n"
      "ds_read_b64_tr_b16 a[102:103], v245 offset:4096\n"
      "buffer_load_dwordx4 v248, %[srdk], s60 offen offset:128 nt lds\n"
      "v_mfma_f32_16x16x32_bf16 v[144:147], a[80:83], a[40:43], v[144:147]\n"
      "s_add_u32 m0, s62, 8192\n"
      "v_exp_f32 v115, v183\n"
      "v_mfma_f32_16x16x32_bf16 v[144:147], a[88:91], a[56:59], v[144:147]\n"
      "s_max_i32 s76, s76, 70\n"
      "ds_read_b64_tr_b16 a[104:105], v246 offset:0\n"
      "buffer_load_dwordx4 v120, %[srdk], s60 offen nt lds\n"
      "v_cvt_pk_bf16_f32 v219, v114, v115\n"
      "v_mfma_f32_16x16x32_bf16 v[148:151], a[92:95], a[56:59], v[232:235]\n"
      "s_add_u32 m0, s62, 9088\n"
      "v_exp_f32 v112, v184\n"
      "v_mfma_f32_16x16x32_bf16 v[148:151], a[84:87], a[40:43], v[148:151]\n"
      "s_add_u32 s62, s62, 0x8000\n"
      "ds_read_b64_tr_b16 a[106:107], v246 offset:4096\n"
      "buffer_load_dwordx4 v120, %[srdk], s60 offen offset:128 nt lds\n"
      "v_or3_b32 v116, v116, v218, v219\n"
      "v_mfma_f32_16x16x32_bf16 v[148:151], a[76:79], a[24:27], v[148:151]\n"
      "s_and_b32 s62, s62, 0x1ffff\n"
      "v_exp_f32 v113, v185\n"
      "v_mfma_f32_16x16x32_bf16 v[148:151], a[68:71], a[8:11], v[148:151]\n"
      "s_add_u32 s60, s60, %[inc]\n"
      "ds_read_b64_tr_b16 a[108:109], v247 offset:0\n"
      "v_cvt_pk_bf16_f32 v220, v112, v113\n"
      "v_mfma_f32_16x16x32_bf16 v[156:159], a[68:71], a[12:15], v[236:239]\n"
      "s_mov_b32 s82, s68\n"
      "v_exp_f32 v114, v186\n"
      "v_mfma_f32_16x16x32_bf16 v[156:159], a[76:79], a[28:31], v[156:159]\n"
      "s_sub_u32 s66, s66, s99\n"
      "ds_read_b64_tr_b16 a[110:111], v247 offset:4096\n"
      "ds_read_b64_tr_b16 a[112:113], v244 offset:1024\n"
      "v_mfma_f32_16x16x32_bf16 v[156:159], a[84:87], a[44:47], v[156:159]\n"
      "s_sub_u32 s82, s82, s99\n"
      "v_exp_f32 v115, v187\n"
      "v_mfma_f32_16x16x32_bf16 v[156:159], a[92:95], a[60:63], v[156:159]\n"
      "s_add_u32 s65, s82, s76\n"
      "ds_read_b64_tr_b16 a[114:115], v244 offset:5120\n"
      "v_cvt_pk_bf16_f32 v221, v114, v115\n"
      "v_mfma_f32_16x16x32_bf16 v[152:155], a[88:91], a[60:63], v[236:239]\n"
      "s_sub_u32 s93, s93, 1\n"
      "v_exp_f32 v112, v188\n"
      "v_mfma_f32_16x16x32_bf16 v[152:155], a[80:83], a[44:47], v[152:155]\n"
      "s_cmp_le_i32 s93, 14\n"
      "ds_read_b64_tr_b16 a[116:117], v245 offset:1024\n"
      "v_or3_b32 v116, v116, v220, v221\n"
      "v_mfma_f32_16x16x32_bf16 v[152:155], a[72:75], a[28:31], v[152:155]\n"
      "s_cselect_b32 s86, 107, s86\n"
      "v_exp_f32 v113, v189\n"
      "v_mfma_f32_16x16x32_bf16 v[152:155], a[64:67], a[12:15], v[152:155]\n"
      "ds_read_b64_tr_b16 a[118:119], v245 offset:5120\n"
      "v_cvt_pk_bf16_f32 v222, v112, v113\n"
      "s_waitcnt lgkmcnt(6)\n"
      "v_mfma_f32_16x16x32_bf16 v[32:35], a[96:99], v[192:195], v[32:35]\n"
      "s_sub_u32 s67, s65, s66\n"
      "v_exp_f32 v114, v190\n"
      "v_mfma_f32_16x16x32_bf16 v[48:51], a[100:103], v[192:195], v[48:51]\n"
      "s_cmp_lt_i32 s67, 0\n"
      "s_cselect_b32 s88, s66, s65\n"
      "ds_read_b64_tr_b16 a[120:121], v246 offset:1024\n"
      "ds_read_b64_tr_b16 a[122:123], v246 offset:5120\n"
      "v_mfma_f32_16x16x32_bf16 a[128:131], a[104:107], v[192:195], a[128:131]\n"
      "s_waitcnt lgkmcnt(4)\n"
      "v_exp_f32 v115, v191\n"
      "v_mfma_f32_16x16x32_bf16 a[144:147], a[108:111], v[192:195], a[144:147]\n"
      "s_add_u32 s90, s88, s91\n"
      "ds_read_b64_tr_b16 a[124:125], v247 offset:1024\n"
      "v_cvt_pk_bf16_f32 v223, v114, v115\n"
      "v_mfma_f32_16x16x32_bf16 v[64:67], a[112:115], v[192:195], v[64:67]\n"
      "ds_read_b64_tr_b16 a[126:127], v247 offset:5120\n"
      "s_waitcnt lgkmcnt(2)\n"
      "v_mfma_f32_16x16x32_bf16 v[80:83], a[116:119], v[192:195], v[80:83]\n"
      "v_or3_b32 v116, v116, v222, v223\n"
      "v_mfma_f32_16x16x32_bf16 a[160:163], a[120:123], v[192:195], a[160:163]\n"
      "s_mov_b32 s64, 4\n"
      "s_waitcnt lgkmcnt(0)\n"
      "v_and_b32 v116, 0x40004000, v116\n"
      "v_mfma_f32_16x16x32_bf16 a[176:179], a[124:127], v[192:195], a[176:179]\n"
      "v_cmp_ne_u32 vcc, 0, v116\n"
      "s_cbranch_vccnz FB1_%=\n"
      "RET1_4_%=:\n"
      "v_mfma_f32_16x16x32_bf16 a[180:183], a[124:127], v[196:199], a[180:183]\n"
      "v_exp_f32 v112, v128\n"
      "v_mfma_f32_16x16x32_bf16 a[164:167], a[120:123], v[196:199], a[164:167]\n"
      "v_xor_b32 v240, 0x10000, v244\n"
      "v_mfma_f32_16x16x32_bf16 a[192:195], a[196:199], v[208:211], a[192:195]\n"
      "v_exp_f32 v113, v129\n"
      "v_mfma_f32_16x16x32_bf16 v[84:87], a[116:119], v[196:199], v[84:87]\n"
      "v_cvt_pk_bf16_f32 v192, v112, v113\n"
      "v_xor_b32 v241, 0x10000, v245\n"
      "v_mfma_f32_16x16x32_bf16 v[68:71], a[112:115], v[196:199], v[68:71]\n"
      "v_exp_f32 v114, v130\n"
      "v_mfma_f32_16x16x32_bf16 a[192:195], a[200:203], v[212:215], a[192:195]\n"
      "v_xor_b32 v250, 0x10000, v246\n"
      "v_mfma_f32_16x16x32_bf16 a[148:151], a[108:111], v[196:199], a[148:151]\n"
      "v_exp_f32 v115, v131\n"
      "v_mfma_f32_16x16x32_bf16 a[132:135], a[104:107], v[196:199], a[132:135]\n"
      "s_memrealtime s[68:69]\n"
      "v_cvt_pk_bf16_f32 v193, v114, v115\n"
      "v_xor_b32 v251, 0x10000, v247\n"
      "v_mfma_f32_16x16x32_bf16 a[192:195], a[204:207], v[216:219], a[192:195]\n"
      "v_exp_f32 v112, v132\n"
      "v_mfma_f32_16x16x32_bf16 v[52:55], a[100:103], v[196:199], v[52:55]\n"
      "v_or_b32 v116, v192, v193\n"
      "v_mfma_f32_16x16x32_bf16 v[36:39], a[96:99], v[196:199], v[36:39]\n"
      "v_exp_f32 v113, v133\n"
      "v_mfma_f32_16x16x32_bf16 a[192:195], a[208:211], v[220:223], a[192:195]\n"
      "v_cvt_pk_bf16_f32 v194, v112, v113\n"
      "s_waitcnt vmcnt(6) lgkmcnt(0)\n"
      "s_sub_u32 s67, s68, s90\n"
      "s_cmp_lt_i32 s67, 0\n"
      "s_cbranch_scc1 GW7_%=\n"
      "GX7_%=:\n"
      "s_barrier\n"
      "v_mfma_f32_16x16x32_bf16 v[40:43], a[96:99], v[200:203], v[40:43]\n"
      "s_memrealtime s[68:69]\n"
      "ds_read_b128 a[64:67], v242 offset:32768\n"
      "v_exp_f32 v114, v134\n"
      "v_mfma_f32_16x16x32_bf16 v[44:47], a[96:99], v[204:207], v[44:47]\n"
      "s_add_u32 m0, %[wb], 82944\n"
      "ds_read_b128 a[68:71], v242 offset:36864\n"
      "v_mfma_f32_16x16x32_bf16 v[60:63], a[100:103], v[204:207], v[60:63]\n"
      "ds_read_b128 a[72:75], v243 offset:32768\n"
      "buffer_load_dwordx4 v121, %[srdv], s94 offen nt lds\n"
      "v_mfma_f32_16x16x32_bf16 v[56:59], a[100:103], v[200:203], v[56:59]\n"
      "s_add_u32 m0, %[wb], 91136\n"
      "ds_read_b128 a[76:79], v243 offset:36864\n"
      "v_mfma_f32_16x16x32_bf16 a[136:139], a[104:107], v[200:203], a[136:139]\n"
      "ds_read_b128 a[80:83], v242 offset:33792\n"
      "buffer_load_dwordx4 v123, %[srdv], s94 offen nt lds\n"
      "v_mfma_f32_16x16x32_bf16 a[140:143], a[104:107], v[204:207], a[140:143]\n"
      "ds_read_b128 a[84:87], v242 offset:37888\n"
      "v_mfma_f32_16x16x32_bf16 a[156:159], a[108:111], v[204:207], a[156:159]\n"
      "ds_read_b128 a[88:91], v243 offset:33792\n"
      "v_mfma_f32_16x16x32_bf16 a[152:155], a[108:111], v[200:203], a[152:155]\n"
      "ds_read_b128 a[92:95], v243 offset:37888\n"
      "v_mfma_f32_16x16x32_bf16 v[72:75], a[112:115], v[200:203], v[72:75]\n"
      "v_exp_f32 v115, v135\n"
      "v_mfma_f32_16x16x32_bf16 v[76:79], a[112:115], v[204:207], v[76:79]\n"
      "v_exp_f32 v112, v136\n"
      "v_mfma_f32_16x16x32_bf16 v[92:95], a[116:119], v[204:207], v[92:95]\n"
      "v_cvt_pk_bf16_f32 v195, v114, v115\n"
      "v_exp_f32 v113, v137\n"
      "v_mfma_f32_16x16x32_bf16 v[88:91], a[116:119], v[200:203], v[88:91]\n"
      "v_or3_b32 v116, v116, v194, v195\n"
      "v_mfma_f32_16x16x32_bf16 a[168:171], a[120:123], v[200:203], a[168:171]\n"
      "v_exp_f32 v114, v138\n"
      "v_mfma_f32_16x16x32_bf16 a[172:175], a[120:123], v[204:207], a[172:175]\n"
      "v_cvt_pk_bf16_f32 v196, v112, v113\n"
      "v_exp_f32 v115, v139\n"
      "v_mfma_f32_16x16x32_bf16 a[188:191], a[124:127], v[204:207], a[188:191]\n"
      "v_exp_f32 v112, v140\n"
      "v_mfma_f32_16x16x32_bf16 a[184:187], a[124:127], v[200:203], a[184:187]\n"
      "s_waitcnt lgkmcnt(0)\n"
      "v_cvt_pk_bf16_f32 v197, v114, v115\n"
      "v_mfma_f32_16x16x32_bf16 v[160:163], a[64:67], a[0:3], v[224:227]\n"
      "s_sub_u32 s67, s68, s66\n"
      "v_exp_f32 v113, v141\n"
      "v_mfma_f32_16x16x32_bf16 v[168:171], a[64:67], a[4:7], v[228:231]\n"
      "s_cmp_gt_i32 s67, s70\n"
      "v_or3_b32 v116, v116, v196, v197\n"
      "v_exp_f32 v114, v142\n"
      "v_mfma_f32_16x16x32_bf16 v[176:179], a[64:67], a[8:11], v[232:235]\n"
      "s_cselect_b32 s69, s86, 0\n"
      "v_cvt_pk_bf16_f32 v198, v112, v113\n"
      "v_mfma_f32_16x16x32_bf16 v[184:187], a[64:67], a[12:15], v[236:239]\n"
      "s_add_u32 s66, s66, s69\n"
      "v_exp_f32 v115, v143\n"
      "v_mfma_f32_16x16x32_bf16 v[164:167], a[68:71], a[0:3], v[224:227]\n"
      "s_add_u32 s67, s67, s101\n"
      "v_exp_f32 v112, v144\n"
      "v_mfma_f32_16x16x32_bf16 v[172:175], a[68:71], a[4:7], v[228:231]\n"
      "s_min_i32 s70, s70, s67\n"
      "v_cvt_pk_bf16_f32 v199, v114, v115\n"
      "v_exp_f32 v113, v145\n"
      "v_mfma_f32_16x16x32_bf16 v[180:183], a[68:71], a[8:11], v[232:235]\n"
      "s_max_i32 s70, s70, 85\n"
      "v_or3_b32 v116, v116, v198, v199\n"
      "v_mfma_f32_16x16x32_bf16 v[188:191], a[68:71], a[12:15], v[236:239]\n"
      "s_waitcnt lgkmcnt(4)\n"
      "v_exp_f32 v114, v146\n"
      "v_mfma_f32_16x16x32_bf16 v[160:163], a[72:75], a[16:19], v[160:163]\n"
      "s_add_u32 s66, s66, s86\n"
      "v_cvt_pk_bf16_f32 v200, v112, v113\n"
      "v_exp_f32 v115, v147\n"
      "v_mfma_f32_16x16x32_bf16 v[168:171], a[72:75], a[20:23], v[168:171]\n"
      "s_and_b32 s76, s80, 31\n"
      "v_exp_f32 v112, v148\n"
      "v_mfma_f32_16x16x32_bf16 v[176:179], a[72:75], a[24:27], v[176:179]\n"
      "s_lshr_b64 s[80:81], s[80:81], 5\n"
      "v_cvt_pk_bf16_f32 v201, v114, v115\n"
      "v_mfma_f32_16x16x32_bf16 v[184:187], a[72:75], a[28:31], v[184:187]\n"
      "s_add_u32 s66, s66, s76\n"
      "v_exp_f32 v113, v149\n"
      "v_mfma_f32_16x16x32_bf16 v[164:167], a[76:79], a[16:19], v[164:167]\n"
      "s_sub_u32 s67, s68, s82\n"
      "v_or3_b32 v116, v116, v200, v201\n"
      "v_exp_f32 v114, v150\n"
      "v_mfma_f32_16x16x32_bf16 v[172:175], a[76:79], a[20:23], v[172:175]\n"
      "s_min_i32 s76, s67, s83\n"
      "v_cvt_pk_bf16_f32 v202, v112, v113\n"
      "v_mfma_f32_16x16x32_bf16 v[180:183], a[76:79], a[24:27], v[180:183]\n"
      "s_add_u32 m0, s62, 0\n"
      "v_exp_f32 v115, v151\n"
      "v_mfma_f32_16x16x32_bf16 v[188:191], a[76:79], a[28:31], v[188:191]\n"
      "buffer_load_dwordx4 v248, %[srdk], s60 offen nt lds\n"
      "s_waitcnt lgkmcnt(2)\n"
      "v_mfma_f32_16x16x32_bf16 v[160:163], a[80:83], a[32:35], v[160:163]\n"
      "s_add_u32 m0, s62, 896\n"
      "v_exp_f32 v112, v152\n"
      "v_mfma_f32_16x16x32_bf16 v[168:171], a[80:83], a[36:39], v[168:171]\n"
      "s_mov_b32 s83, s67\n"
      "buffer_load_dwordx4 v248, %[srdk], s60 offen offset:128 nt lds\n"
      "v_mfma_f32_16x16x32_bf16 v[176:179], a[80:83], a[40:43], v[176:179]\n"
      "s_add_u32 m0, s62, 8192\n"
      "v_mfma_f32_16x16x32_bf16 v[184:187], a[80:83], a[44:47], v[184:187]\n"
      "s_sub_u32 s76, s76, 36\n"
      "buffer_load_dwordx4 v120, %[srdk], s60 offen nt lds\n"
      "s_waitcnt vmcnt(9)\n"
      "s_barrier\n"
      "v_mfma_f32_16x16x32_bf16 v[164:167], a[84:87], a[32:35], v[164:167]\n"
      "s_add_u32 m0, s62, 9088\n"
      "ds_read_b64_tr_b16 a[96:97], v246 offset:33792\n"
      "v_mfma_f32_16x16x32_bf16 v[172:175], a[84:87], a[36:39], v[172:175]\n"
      "s_min_i32 s76, s76, 148\n"
      "ds_read_b64_tr_b16 a[98:99], v246 offset:37888\n"
      "buffer_load_dwordx4 v120, %[srdk], s60 offen offset:128 nt lds\n"
      "v_mfma_f32_16x16x32_bf16 v[180:183], a[84:87], a[40:43], v[180:183]\n"
      "s_max_i32 s76, s76, 70\n"
      "ds_read_b64_tr_b16 a[100:101], v247 offset:33792\n"
      "v_mfma_f32_16x16x32_bf16 v[188:191], a[84:87], a[44:47], v[188:191]\n"
      "ds_read_b64_tr_b16 a[102:103], v247 offset:37888\n"
      "s_waitcnt lgkmcnt(4)\n"
      "v_mfma_f32_16x16x32_bf16 v[160:163], a[88:91], a[48:51], v[160:163]\n"
      "s_mov_b32 s82, s68\n"
      "ds_read_b64_tr_b16 a[104:105], v244 offset:33792\n"
      "v_cvt_pk_bf16_f32 v203, v114, v115\n"
      "v_mfma_f32_16x16x32_bf16 v[168:171], a[88:91], a[52:55], v[168:171]\n"
      "s_sub_u32 s66, s66, s99\n"
      "ds_read_b64_tr_b16 a[106:107], v244 offset:37888\n"
      "v_exp_f32 v113, v153\n"
      "v_mfma_f32_16x16x32_bf16 v[176:179], a[88:91], a[56:59], v[176:179]\n"
      "s_sub_u32 s82, s82, s99\n"
      "ds_read_b64_tr_b16 a[108:109], v245 offset:33792\n"
      "v_mfma_f32_16x16x32_bf16 v[184:187], a[88:91], a[60:63], v[184:187]\n"
      "s_add_u32 s65, s82, s76\n"
      "ds_read_b64_tr_b16 a[110:111], v245 offset:37888\n"
      "v_or3_b32 v116, v116, v202, v203\n"
      "v_mfma_f32_16x16x32_bf16 v[164:167], a[92:95], a[48:51], v[164:167]\n"
      "s_sub_u32 s93, s93, 1\n"
      "v_exp_f32 v114, v154\n"
      "v_mfma_f32_16x16x32_bf16 v[172:175], a[92:95], a[52:55], v[172:175]\n"
      "s_cmp_le_i32 s93, 14\n"
      "v_cvt_pk_bf16_f32 v204, v112, v113\n"
      "v_exp_f32 v115, v155\n"
      "v_mfma_f32_16x16x32_bf16 v[180:183], a[92:95], a[56:59], v[180:183]\n"
      "s_cselect_b32 s86, 107, s86\n"
      "v_exp_f32 v112, v156\n"
      "v_mfma_f32_16x16x32_bf16 v[188:191], a[92:95], a[60:63], v[188:191]\n"
      "s_waitcnt lgkmcnt(4)\n"
      "v_cvt_pk_bf16_f32 v205, v114, v115\n"
      "v_mfma_f32_16x16x32_bf16 a[160:163], a[96:99], v[208:211], a[160:163]\n"
      "s_sub_u32 s67, s65, s66\n"
      "v_exp_f32 v113, v157\n"
      "v_mfma_f32_16x16x32_bf16 a[164:167], a[96:99], v[212:215], a[164:167]\n"
      "s_cmp_lt_i32 s67, 0\n"
      "v_or3_b32 v116, v116, v204, v205\n"
      "v_exp_f32 v114, v158\n"
      "v_mfma_f32_16x16x32_bf16 a[168:171], a[96:99], v[216:219], a[168:171]\n"
      "s_cselect_b32 s88, s66, s65\n"
      "v_cvt_pk_bf16_f32 v206, v112, v113\n"
      "v_mfma_f32_16x16x32_bf16 a[172:175], a[96:99], v[220:223], a[172:175]\n"
      "s_add_u32 s90, s88, s91\n"
      "v_exp_f32 v115, v159\n"
      "s_nop 0\n"
      "v_mfma_f32_16x16x32_bf16 a[176:179], a[100:103], v[208:211], a[176:179]\n"
      "s_mov_b32 s64, 4\n"
      "v_cvt_pk_bf16_f32 v207, v114, v115\n"
      "v_or3_b32 v116, v116, v206, v207\n"
      "v_mfma_f32_16x16x32_bf16 a[180:183], a[100:103], v[212:215], a[180:183]\n"
      "v_and_b32 v116, 0x40004000, v116\n"
      "v_cmp_ne_u32 vcc, 0, v116\n"
      "s_cbranch_vccnz FB0_%=\n"
      "RET0_4_%=:\n"
      "v_mfma_f32_16x16x32_bf16 a[184:187], a[100:103], v[216:219], a[184:187]\n"
      "v_exp_f32 v112, v160\n"
      "v_mfma_f32_16x16x32_bf16 a[188:191], a[100:103], v[220:223], a[188:191]\n"
      "s_memrealtime s[68:69]\n"
      "v_xor_b32 v242, 0x10000, v242\n"
      "v_mfma_f32_16x16x32_bf16 a[192:195], a[196:199], v[192:195], a[192:195]\n"
      "v_exp_f32 v113, v161\n"
      "v_mfma_f32_16x16x32_bf16 a[192:195], a[200:203], v[196:199], a[192:195]\n"
      "v_xor_b32 v243, 0x10000, v243\n"
      "v_exp_f32 v114, v162\n"
      "v_mfma_f32_16x16x32_bf16 a[192:195], a[204:207], v[200:203], a[192:195]\n"
      "v_cvt_pk_bf16_f32 v0, v112, v113\n"
      "v_mfma_f32_16x16x32_bf16 a[192:195], a[208:211], v[204:207], a[192:195]\n"
      "v_exp_f32 v115, v163\n"
      "s_waitcnt vmcnt(6) lgkmcnt(0)\n"
      "s_sub_u32 s67, s68, s90\n"
      "s_cmp_lt_i32 s67, 0\n"
      "s_cbranch_scc1 GW8_%=\n"
      "GX8_%=:\n"
      "s_barrier\n"
      "v_mfma_f32_16x16x32_bf16 v[64:67], a[104:107], v[208:211], v[64:67]\n"
      "ds_read_b128 a[64:67], v242 offset:0\n"
      "ds_read_b128 a[68:71], v242 offset:4096\n"
      "v_mfma_f32_16x16x32_bf16 v[68:71], a[104:107], v[212:215], v[68:71]\n"
      "s_add_u32 m0, %[wb], 115712\n"
      "ds_read_b128 a[72:75], v243 offset:0\n"
      "ds_read_b128 a[76:79], v243 offset:4096\n"
      "v_mfma_f32_16x16x32_bf16 v[72:75], a[104:107], v[216:219], v[72:75]\n"
      "ds_read_b128 a[80:83], v242 offset:1024\n"
      "ds_read_b128 a[84:87], v242 offset:5120\n"
      "buffer_load_dwordx4 v121, %[srdv], s95 offen nt lds\n"
      "v_mfma_f32_16x16x32_bf16 v[76:79], a[104:107], v[220:223], v[76:79]\n"
      "s_add_u32 m0, %[wb], 123904\n"
      "ds_read_b128 a[88:91], v243 offset:1024\n"
      "ds_read_b128 a[92:95], v243 offset:5120\n"
      "v_mfma_f32_16x16x32_bf16 v[80:83], a[108:111], v[208:211], v[80:83]\n"
      "buffer_load_dwordx4 v123, %[srdv], s95 offen nt lds\n"
      "v_mfma_f32_16x16x32_bf16 v[84:87], a[108:111], v[212:215], v[84:87]\n"
      "s_add_u32 m0, %[wb], 17408\n"
      "v_mfma_f32_16x16x32_bf16 v[88:91], a[108:111], v[216:219], v[88:91]\n"
      "buffer_load_dwordx4 v121, %[srdv], s77 offen nt lds\n"
      "v_mfma_f32_16x16x32_bf16 v[92:95], a[108:111], v[220:223], v[92:95]\n"
      "s_waitcnt lgkmcnt(6)\n"
      "s_add_u32 m0, %[wb], 25600\n"
      "v_exp_f32 v112, v164\n"
      "v_mfma_f32_16x16x32_bf16 v[128:131], a[64:67], a[0:3], v[224:227]\n"
      "buffer_load_dwordx4 v123, %[srdv], s77 offen nt lds\n"
      "v_mfma_f32_16x16x32_bf16 v[136:139], a[64:67], a[4:7], v[228:231]\n"
      "v_cvt_pk_bf16_f32 v1, v114, v115\n"
      "v_exp_f32 v113, v165\n"
      "v_mfma_f32_16x16x32_bf16 v[144:147], a[64:67], a[8:11], v[232:235]\n"
      "s_add_u32 s88, s66, 0\n"
      "v_or_b32 v116, v0, v1\n"
      "v_mfma_f32_16x16x32_bf16 v[152:155], a[64:67], a[12:15], v[236:239]\n"
      "s_add_u32 s90, s88, s91\n"
      "v_exp_f32 v114, v166\n"
      "v_cvt_pk_bf16_f32 v2, v112, v113\n"
      "v_mfma_f32_16x16x32_bf16 v[132:135], a[68:71], a[0:3], v[224:227]\n"
      "v_exp_f32 v115, v167\n"
      "v_mfma_f32_16x16x32_bf16 v[140:143], a[68:71], a[4:7], v[228:231]\n"
      "v_exp_f32 v112, v168\n"
      "v_cvt_pk_bf16_f32 v3, v114, v115\n"
      "v_mfma_f32_16x16x32_bf16 v[148:151], a[68:71], a[8:11], v[232:235]\n"
      "v_exp_f32 v113, v169\n"
      "v_or3_b32 v116, v116, v2, v3\n"
      "v_mfma_f32_16x16x32_bf16 v[156:159], a[68:71], a[12:15], v[236:239]\n"
      "s_waitcnt lgkmcnt(4)\n"
      "v_exp_f32 v114, v170\n"
      "v_mfma_f32_16x16x32_bf16 v[128:131], a[72:75], a[16:19], v[128:131]\n"
      "v_cvt_pk_bf16_f32 v4, v112, v113\n"
      "v_exp_f32 v115, v171\n"
      "v_mfma_f32_16x16x32_bf16 v[136:139], a[72:75], a[20:23], v[136:139]\n"
      "v_exp_f32 v112, v172\n"
      "v_mfma_f32_16x16x32_bf16 v[144:147], a[72:75], a[24:27], v[144:147]\n"
      "v_cvt_pk_bf16_f32 v5, v114, v115\n"
      "v_exp_f32 v113, v173\n"
      "v_mfma_f32_16x16x32_bf16 v[152:155], a[72:75], a[28:31], v[152:155]\n"
      "v_or3_b32 v116, v116, v4, v5\n"
      "v_mfma_f32_16x16x32_bf16 v[132:135], a[76:79], a[16:19], v[132:135]\n"
      "v_exp_f32 v114, v174\n"
      "v_cvt_pk_bf16_f32 v6, v112, v113\n"
      "v_mfma_f32_16x16x32_bf16 v[140:143], a[76:79], a[20:23], v[140:143]\n"
      "v_exp_f32 v115, v175\n"
      "v_mfma_f32_16x16x32_bf16 v[148:151], a[76:79], a[24:27], v[148:151]\n"
      "v_exp_f32 v112, v176\n"
      "v_cvt_pk_bf16_f32 v7, v114, v115\n"
      "v_mfma_f32_16x16x32_bf16 v[156:159], a[76:79], a[28:31], v[156:159]\n"
      "s_waitcnt lgkmcnt(2)\n"
      "v_exp_f32 v113, v177\n"
      "v_mfma_f32_16x16x32_bf16 v[128:131], a[80:83], a[32:35], v[128:131]\n"
      "v_or3_b32 v116, v116, v6, v7\n"
      "v_exp_f32 v114, v178\n"
      "v_mfma_f32_16x16x32_bf16 v[136:139], a[80:83], a[36:39], v[136:139]\n"
      "v_cvt_pk_bf16_f32 v8, v112, v113\n"
      "v_exp_f32 v115, v179\n"
      "v_mfma_f32_16x16x32_bf16 v[144:147], a[80:83], a[40:43], v[144:147]\n"
      "v_exp_f32 v112, v180\n"
      "v_mfma_f32_16x16x32_bf16 v[152:155], a[80:83], a[44:47], v[152:155]\n"
      "v_cvt_pk_bf16_f32 v9, v114, v115\n"
      "v_exp_f32 v113, v181\n"
      "s_waitcnt vmcnt(8)\n"
      "s_barrier\n"
      "v_mfma_f32_16x16x32_bf16 v[132:135], a[84:87], a[32:35], v[132:135]\n"
      "ds_read_b64_tr_b16 a[112:113], v250 offset:1024\n"
      "v_or3_b32 v116, v116, v8, v9\n"
      "v_mfma_f32_16x16x32_bf16 v[140:143], a[84:87], a[36:39], v[140:143]\n"
      "ds_read_b64_tr_b16 a[114:115], v250 offset:5120\n"
      "v_exp_f32 v114, v182\n"
      "v_mfma_f32_16x16x32_bf16 v[148:151], a[84:87], a[40:43], v[148:151]\n"
      "ds_read_b64_tr_b16 a[116:117], v251 offset:1024\n"
      "v_mfma_f32_16x16x32_bf16 v[156:159], a[84:87], a[44:47], v[156:159]\n"
      "ds_read_b64_tr_b16 a[118:119], v251 offset:5120\n"
      "v_cvt_pk_bf16_f32 v10, v112, v113\n"
      "s_waitcnt lgkmcnt(4)\n"
      "v_mfma_f32_16x16x32_bf16 v[128:131], a[88:91], a[48:51], v[128:131]\n"
      "ds_read_b64_tr_b16 a[120:121], v240 offset:1024\n"
      "v_exp_f32 v115, v183\n"
      "v_mfma_f32_16x16x32_bf16 v[136:139], a[88:91], a[52:55], v[136:139]\n"
      "ds_read_b64_tr_b16 a[122:123], v240 offset:5120\n"
      "v_exp_f32 v112, v184\n"
      "v_mfma_f32_16x16x32_bf16 v[144:147], a[88:91], a[56:59], v[144:147]\n"
      "ds_read_b64_tr_b16 a[124:125], v241 offset:1024\n"
      "v_cvt_pk_bf16_f32 v11, v114, v115\n"
      "v_mfma_f32_16x16x32_bf16 v[152:155], a[88:91], a[60:63], v[152:155]\n"
      "ds_read_b64_tr_b16 a[126:127], v241 offset:5120\n"
      "v_exp_f32 v113, v185\n"
      "v_mfma_f32_16x16x32_bf16 v[132:135], a[92:95], a[48:51], v[132:135]\n"
      "v_or3_b32 v116, v116, v10, v11\n"
      "v_exp_f32 v114, v186\n"
      "v_mfma_f32_16x16x32_bf16 v[140:143], a[92:95], a[52:55], v[140:143]\n"
      "v_cvt_pk_bf16_f32 v12, v112, v113\n"
      "v_mfma_f32_16x16x32_bf16 v[148:151], a[92:95], a[56:59], v[148:151]\n"
      "v_exp_f32 v115, v187\n"
      "v_mfma_f32_16x16x32_bf16 v[156:159], a[92:95], a[60:63], v[156:159]\n"
      "s_waitcnt lgkmcnt(4)\n"
      "v_exp_f32 v112, v188\n"
      "v_cvt_pk_bf16_f32 v13, v114, v115\n"
      "v_mfma_f32_16x16x32_bf16 a[160:163], a[112:115], v[192:195], a[160:163]\n"
      "v_exp_f32 v113, v189\n"
      "v_or3_b32 v116, v116, v12, v13\n"
      "v_mfma_f32_16x16x32_bf16 a[164:167], a[112:115], v[196:199], a[164:167]\n"
      "v_exp_f32 v114, v190\n"
      "v_mfma_f32_16x16x32_bf16 a[168:171], a[112:115], v[200:203], a[168:171]\n"
      "v_cvt_pk_bf16_f32 v14, v112, v113\n"
      "v_exp_f32 v115, v191\n"
      "v_mfma_f32_16x16x32_bf16 a[172:175], a[112:115], v[204:207], a[172:175]\n"
      "s_add_u32 m0, %[wb], 50176\n"
      "s_nop 0\n"
      "v_cvt_pk_bf16_f32 v15, v114, v115\n"
      "v_or3_b32 v116, v116, v14, v15\n"
      "v_mfma_f32_16x16x32_bf16 a[176:179], a[116:119], v[192:195], a[176:179]\n"
      "s_mov_b32 s64, 5\n"
      "buffer_load_dwordx4 v121, %[srdv], s78 offen nt lds\n"
      "v_mfma_f32_16x16x32_bf16 a[180:183], a[116:119], v[196:199], a[180:183]\n"
      "s_add_u32 m0, %[wb], 58368\n"
      "v_and_b32 v116, 0x40004000, v116\n"
      "v_cmp_ne_u32 vcc, 0, v116\n"
      "s_cbranch_vccnz FB1_%=\n"
      "RET1_5_%=:\n"
      "v_mfma_f32_16x16x32_bf16 a[184:187], a[116:119], v[200:203], a[184:187]\n"
      "buffer_load_dwordx4 v123, %[srdv], s78 offen nt lds\n"
      "v_mfma_f32_16x16x32_bf16 a[188:191], a[116:119], v[204:207], a[188:191]\n"
      "s_memrealtime s[68:69]\n"
      "v_exp_f32 v112, v128\n"
      "v_exp_f32 v113, v129\n"
      "v_mfma_f32_16x16x32_bf16 a[192:195], a[196:199], v[0:3], a[192:195]\n"
      "v_exp_f32 v114, v130\n"
      "v_mfma_f32_16x16x32_bf16 a[192:195], a[200:203], v[4:7], a[192:195]\n"
      "v_cvt_pk_bf16_f32 v16, v112, v113\n"
      "v_exp_f32 v115, v131\n"
      "v_mfma_f32_16x16x32_bf16 a[192:195], a[204:207], v[8:11], a[192:195]\n"
      "v_exp_f32 v112, v132\n"
      "v_cvt_pk_bf16_f32 v17, v114, v115\n"
      "v_mfma_f32_16x16x32_bf16 a[192:195], a[208:211], v[12:15], a[192:195]\n"
      "v_exp_f32 v113, v133\n"
      "v_or_b32 v116, v16, v17\n"
      "s_add_u32 s72, s72, 0x7f4a7c15\n"
      "s_addc_u32 s76, s79, 0x9e3779b9\n"
      "s_waitcnt vmcnt(6) lgkmcnt(0)\n"
      "s_barrier\n"
      "v_mfma_f32_16x16x32_bf16 v[64:67], a[120:123], v[192:195], v[64:67]\n"
      "s_sub_u32 s80, 20480, %[xo]\n"
      "ds_read_b128 a[64:67], v242 offset:32768\n"
      "ds_read_b128 a[68:71], v242 offset:36864\n"
      "v_mfma_f32_16x16x32_bf16 v[68:71], a[120:123], v[196:199], v[68:71]\n"
      "s_add_u32 s79, s80, 0x1000\n"
      "ds_read_b128 a[72:75], v243 offset:32768\n"
      "ds_read_b128 a[76:79], v243 offset:36864\n"
      "v_exp_f32 v114, v134\n"
      "v_mfma_f32_16x16x32_bf16 v[72:75], a[120:123], v[200:203], v[72:75]\n"
      "s_add_u32 s81, %[xo], 0x1000\n"
      "ds_read_b128 a[80:83], v242 offset:33792\n"
      "ds_read_b128 a[84:87], v242 offset:37888\n"
      "v_cvt_pk_bf16_f32 v18, v112, v113\n"
      "v_mfma_f32_16x16x32_bf16 v[76:79], a[120:123], v[204:207], v[76:79]\n"
      "s_mov_b32 s63, 0x100000\n"
      "ds_read_b128 a[88:91], v243 offset:33792\n"
      "ds_read_b128 a[92:95], v243 offset:37888\n"
      "v_mfma_f32_16x16x32_bf16 v[80:83], a[124:127], v[192:195], v[80:83]\n"
      "s_mov_b32 s60, 0x928b2f\n"
      "v_exp_f32 v115, v135\n"
      "v_exp_f32 v112, v136\n"
      "v_mfma_f32_16x16x32_bf16 v[84:87], a[124:127], v[196:199], v[84:87]\n"
      "s_mov_b32 s62, 0x9818e9\n"
      "v_cvt_pk_bf16_f32 v19, v114, v115\n"
      "v_exp_f32 v113, v137\n"
      "v_mfma_f32_16x16x32_bf16 v[88:91], a[124:127], v[200:203], v[88:91]\n"
      "s_mov_b32 s70, 0xdd9dc9\n"
      "v_xor_b32 v248, 0x2000, v244\n"
      "v_or3_b32 v116, v116, v18, v19\n"
      "v_mfma_f32_16x16x32_bf16 v[92:95], a[124:127], v[204:207], v[92:95]\n"
      "s_waitcnt lgkmcnt(6)\n"
      "v_exp_f32 v114, v138\n"
      "v_cvt_pk_bf16_f32 v20, v112, v113\n"
      "v_mfma_f32_16x16x32_bf16 v[160:163], a[64:67], a[0:3], v[224:227]\n"
      "s_mov_b32 s71, 0x8ed905\n"
      "v_exp_f32 v115, v139\n"
      "v_mfma_f32_16x16x32_bf16 v[168:171], a[64:67], a[4:7], v[228:231]\n"
      "s_sub_u32 s88, %[cph], 319\n"
      "v_xor_b32 v120, 0x2000, v245\n"
      "v_exp_f32 v112, v140\n"
      "v_cvt_pk_bf16_f32 v21, v114, v115\n"
      "v_mfma_f32_16x16x32_bf16 v[176:179], a[64:67], a[8:11], v[232:235]\n"
      "s_mul_i32 s88, s88, -8\n"
      "v_exp_f32 v113, v141\n"
      "v_mfma_f32_16x16x32_bf16 v[184:187], a[64:67], a[12:15], v[236:239]\n"
      "s_add_u32 m0, %[wb], 49152\n"
      "v_or3_b32 v116, v116, v20, v21\n"
      "v_exp_f32 v114, v142\n"
      "v_mfma_f32_16x16x32_bf16 v[164:167], a[68:71], a[0:3], v[224:227]\n"
      "s_ashr_i32 s88, s88, 3\n"
      "buffer_load_dwordx4 v249, %[srdv], s61 offen nt lds\n"
      "v_mbcnt_lo_u32_b32 v127, -1, 0\n"
      "v_mfma_f32_16x16x32_bf16 v[172:175], a[68:71], a[4:7], v[228:231]\n"
      "s_add_u32 m0, %[wb], 57344\n"
      "v_cvt_pk_bf16_f32 v22, v112, v113\n"
      "v_mfma_f32_16x16x32_bf16 v[180:183], a[68:71], a[8:11], v[232:235]\n"
      "s_add_u32 s88, s88, s66\n"
      "buffer_load_dwordx4 v122, %[srdv], s61 offen nt lds\n"
      "v_mfma_f32_16x16x32_bf16 v[188:191], a[68:71], a[12:15], v[236:239]\n"
      "s_waitcnt lgkmcnt(4)\n"
      "v_mbcnt_hi_u32_b32 v127, -1, v127\n"
      "v_exp_f32 v115, v143\n"
      "v_mfma_f32_16x16x32_bf16 v[160:163], a[72:75], a[16:19], v[160:163]\n"
      "s_add_u32 s88, s88, s100\n"
      "v_exp_f32 v112, v144\n"
      "v_cvt_pk_bf16_f32 v23, v114, v115\n"
      "v_mfma_f32_16x16x32_bf16 v[168:171], a[72:75], a[20:23], v[168:171]\n"
      "v_exp_f32 v113, v145\n"
      "v_or3_b32 v116, v116, v22, v23\n"
      "v_mfma_f32_16x16x32_bf16 v[176:179], a[72:75], a[24:27], v[176:179]\n"
      "v_lshlrev_b32 v127, 4, v127\n"
      "v_exp_f32 v114, v146\n"
      "v_mfma_f32_16x16x32_bf16 v[184:187], a[72:75], a[28:31], v[184:187]\n"
      "v_cvt_pk_bf16_f32 v24, v112, v113\n"
      "v_exp_f32 v115, v147\n"
      "v_mfma_f32_16x16x32_bf16 v[164:167], a[76:79], a[16:19], v[164:167]\n"
      "v_exp_f32 v112, v148\n"
      "v_cvt_pk_bf16_f32 v25, v114, v115\n"
      "v_mfma_f32_16x16x32_bf16 v[172:175], a[76:79], a[20:23], v[172:175]\n"
      "v_add_u32 v125, %[xw], v127\n"
      "v_exp_f32 v113, v149\n"
      "v_mfma_f32_16x16x32_bf16 v[180:183], a[76:79], a[24:27], v[180:183]\n"
      "v_or3_b32 v116, v116, v24, v25\n"
      "v_exp_f32 v114, v150\n"
      "v_mfma_f32_16x16x32_bf16 v[188:191], a[76:79], a[28:31], v[188:191]\n"
      "s_waitcnt lgkmcnt(2)\n"
      "v_cvt_pk_bf16_f32 v26, v112, v113\n"
      "v_exp_f32 v115, v151\n"
      "v_mfma_f32_16x16x32_bf16 v[160:163], a[80:83], a[32:35], v[160:163]\n"
      "v_add_u32 v126, %[xr], v127\n"
      "v_exp_f32 v112, v152\n"
      "v_mfma_f32_16x16x32_bf16 v[168:171], a[80:83], a[36:39], v[168:171]\n"
      "s_add_u32 m0, %[wb], 81920\n"
      "v_cvt_pk_bf16_f32 v27, v114, v115\n"
      "v_exp_f32 v113, v153\n"
      "v_mfma_f32_16x16x32_bf16 v[176:179], a[80:83], a[40:43], v[176:179]\n"
      "buffer_load_dwordx4 v249, %[srdv], s94 offen nt lds\n"
      "v_mfma_f32_16x16x32_bf16 v[184:187], a[80:83], a[44:47], v[184:187]\n"
      "s_add_u32 m0, %[wb], 90112\n"
      "v_or3_b32 v116, v116, v26, v27\n"
      "v_mfma_f32_16x16x32_bf16 v[164:167], a[84:87], a[32:35], v[164:167]\n"
      "buffer_load_dwordx4 v122, %[srdv], s94 offen nt lds\n"
      "v_mfma_f32_16x16x32_bf16 v[172:175], a[84:87], a[36:39], v[172:175]\n"
      "v_exp_f32 v114, v154\n"
      "v_cvt_pk_bf16_f32 v28, v112, v113\n"
      "v_mfma_f32_16x16x32_bf16 v[180:183], a[84:87], a[40:43], v[180:183]\n"
      "v_exp_f32 v115, v155\n"
      "v_exp_f32 v112, v156\n"
      "v_mov_b32 v123, s88\n"
      "v_mfma_f32_16x16x32_bf16 v[188:191], a[84:87], a[44:47], v[188:191]\n"
      "s_waitcnt lgkmcnt(0)\n"
      "v_cvt_pk_bf16_f32 v29, v114, v115\n"
      "v_exp_f32 v113, v157\n"
      "s_waitcnt vmcnt(6)\n"
      "s_barrier\n"
      "v_mfma_f32_16x16x32_bf16 v[160:163], a[88:91], a[48:51], v[160:163]\n"
      "ds_read_b64_tr_b16 a[96:97], v250 offset:33792\n"
      "ds_read_b64_tr_b16 a[98:99], v250 offset:37888\n"
      "v_mfma_f32_16x16x32_bf16 v[168:171], a[88:91], a[52:55], v[168:171]\n"
      "ds_read_b64_tr_b16 a[100:101], v251 offset:33792\n"
      "ds_read_b64_tr_b16 a[102:103], v251 offset:37888\n"
      "v_or3_b32 v116, v116, v28, v29\n"
      "v_mfma_f32_16x16x32_bf16 v[176:179], a[88:91], a[56:59], v[176:179]\n"
      "ds_read_b64_tr_b16 a[112:113], v246 offset:1024\n"
      "ds_read_b64_tr_b16 a[114:115], v246 offset:5120\n"
      "v_mfma_f32_16x16x32_bf16 v[184:187], a[88:91], a[60:63], v[184:187]\n"
      "ds_read_b64_tr_b16 a[116:117], v247 offset:1024\n"
      "ds_read_b64_tr_b16 a[118:119], v247 offset:5120\n"
      "v_exp_f32 v114, v158\n"
      "v_mfma_f32_16x16x32_bf16 v[164:167], a[92:95], a[48:51], v[164:167]\n"
      "ds_read_b64_tr_b16 a[104:105], v240 offset:33792\n"
      "v_cvt_pk_bf16_f32 v30, v112, v113\n"
      "v_mfma_f32_16x16x32_bf16 v[172:175], a[92:95], a[52:55], v[172:175]\n"
      "s_mov_b32 s64, 5\n"
      "ds_read_b64_tr_b16 a[106:107], v240 offset:37888\n"
      "v_exp_f32 v115, v159\n"
      "s_nop 0\n"
      "v_mfma_f32_16x16x32_bf16 v[180:183], a[92:95], a[56:59], v[180:183]\n"
      "ds_read_b64_tr_b16 a[108:109], v241 offset:33792\n"
      "v_cvt_pk_bf16_f32 v31, v114, v115\n"
      "v_or3_b32 v116, v116, v30, v31\n"
      "v_mfma_f32_16x16x32_bf16 v[188:191], a[92:95], a[60:63], v[188:191]\n"
      "ds_read_b64_tr_b16 a[110:111], v241 offset:37888\n"
      "v_and_b32 v116, 0x40004000, v116\n"
      "v_cmp_ne_u32 vcc, 0, v116\n"
      "s_waitcnt lgkmcnt(4)\n"
      "s_cbranch_vccnz FB0_%=\n"
      "RET0_5_%=:\n"
      "v_mfma_f32_16x16x32_bf16 a[160:163], a[96:99], v[0:3], a[160:163]\n"
      "ds_read_b64_tr_b16 a[120:121], v244 offset:1024\n"
      "v_mov_b32 v117, v224\n"
      "v_xor_b32 v242, 0x10000, v242\n"
      "v_xor_b32 v243, 0x10000, v243\n"
      "v_mfma_f32_16x16x32_bf16 a[176:179], a[100:103], v[0:3], a[176:179]\n"
      "ds_read_b64_tr_b16 a[122:123], v244 offset:5120\n"
      "v_exp_f32 v112, v160\n"
      "v_exp_f32 v113, v161\n"
      "v_mfma_f32_16x16x32_bf16 a[160:163], a[112:115], v[16:19], a[160:163]\n"
      "s_mov_b32 s64, 6\n"
      "ds_read_b64_tr_b16 a[124:125], v245 offset:1024\n"
      "v_exp_f32 v114, v162\n"
      "v_mfma_f32_16x16x32_bf16 a[176:179], a[116:119], v[16:19], a[176:179]\n"
      "s_add_u32 m0, %[wb], 114688\n"
      "ds_read_b64_tr_b16 a[126:127], v245 offset:5120\n"
      "v_cvt_pk_bf16_f32 v96, v112, v113\n"
      "v_exp_f32 v115, v163\n"
      "v_mfma_f32_16x16x32_bf16 a[164:167], a[96:99], v[4:7], a[164:167]\n"
      "buffer_load_dwordx4 v249, %[srdv], s95 offen nt lds\n"
      "v_mov_b32_dpp v117, v228 quad_perm:[0,1,2,3] row_mask:0x2 bank_mask:0xf\n"
      "v_mfma_f32_16x16x32_bf16 a[180:183], a[100:103], v[4:7], a[180:183]\n"
      "s_add_u32 m0, %[wb], 122880\n"
      "v_exp_f32 v112, v164\n"
      "v_cvt_pk_bf16_f32 v97, v114, v115\n"
      "v_exp_f32 v113, v165\n"
      "v_mfma_f32_16x16x32_bf16 a[164:167], a[112:115], v[20:23], a[164:167]\n"
      "buffer_load_dwordx4 v122, %[srdv], s95 offen nt lds\n"
      "v_or_b32 v116, v96, v97\n"
      "v_mfma_f32_16x16x32_bf16 a[180:183], a[116:119], v[20:23], a[180:183]\n"
      "ds_write_b128 v125, a[160:163] offset:0\n"
      "v_mfma_f32_16x16x32_bf16 a[168:171], a[96:99], v[8:11], a[168:171]\n"
      "v_mov_b32_dpp v117, v232 quad_perm:[0,1,2,3] row_mask:0x4 bank_mask:0xf\n"
      "v_exp_f32 v114, v166\n"
      "v_cvt_pk_bf16_f32 v98, v112, v113\n"
      "v_mfma_f32_16x16x32_bf16 a[184:187], a[100:103], v[8:11], a[184:187]\n"
      "ds_write_b128 v125, a[176:179] offset:4096\n"
      "v_exp_f32 v115, v167\n"
      "v_mfma_f32_16x16x32_bf16 a[168:171], a[112:115], v[24:27], a[168:171]\n"
      "v_exp_f32 v112, v168\n"
      "v_cvt_pk_bf16_f32 v99, v114, v115\n"
      "v_exp_f32 v113, v169\n"
      "v_mfma_f32_16x16x32_bf16 a[184:187], a[116:119], v[24:27], a[184:187]\n"
      "v_or3_b32 v116, v116, v98, v99\n"
      "v_exp_f32 v114, v170\n"
      "v_mfma_f32_16x16x32_bf16 a[172:175], a[96:99], v[12:15], a[172:175]\n"
      "ds_write_b128 v125, v[96:99] offset:8192\n"
      "v_mov_b32_dpp v117, v236 quad_perm:[0,1,2,3] row_mask:0x8 bank_mask:0xf\n"
      "v_mfma_f32_16x16x32_bf16 a[188:191], a[100:103], v[12:15], a[188:191]\n"
      "v_cvt_pk_bf16_f32 v100, v112, v113\n"
      "v_exp_f32 v115, v171\n"
      "v_exp_f32 v112, v172\n"
      "v_mfma_f32_16x16x32_bf16 a[172:175], a[112:115], v[28:31], a[172:175]\n"
      "v_cvt_pk_bf16_f32 v101, v114, v115\n"
      "v_exp_f32 v113, v173\n"
      "v_mfma_f32_16x16x32_bf16 a[188:191], a[116:119], v[28:31], a[188:191]\n"
      "s_add_u32 m0, %[wb], 16384\n"
      "ds_write_b128 v125, a[164:167] offset:1024\n"
      "v_or3_b32 v116, v116, v100, v101\n"
      "v_mfma_f32_16x16x32_bf16 a[192:195], a[196:199], v[16:19], a[192:195]\n"
      "buffer_load_dwordx4 v249, %[srdv], s77 offen nt lds\n"
      "ds_write_b32 v125, v117 offset:12292\n"
      "v_mfma_f32_16x16x32_bf16 a[192:195], a[200:203], v[20:23], a[192:195]\n"
      "s_add_u32 m0, %[wb], 24576\n"
      "v_exp_f32 v114, v174\n"
      "v_cvt_pk_bf16_f32 v102, v112, v113\n"
      "v_exp_f32 v115, v175\n"
      "v_mfma_f32_16x16x32_bf16 a[192:195], a[204:207], v[24:27], a[192:195]\n"
      "buffer_load_dwordx4 v122, %[srdv], s77 offen nt lds\n"
      "ds_write_b128 v125, a[180:183] offset:5120\n"
      "ds_write_b32 v125, v123 offset:12296\n"
      "v_mfma_f32_16x16x32_bf16 a[192:195], a[208:211], v[28:31], a[192:195]\n"
      "s_waitcnt lgkmcnt(7)\n"
      "v_exp_f32 v112, v176\n"
      "v_mfma_f32_16x16x32_bf16 v[64:67], a[104:107], v[0:3], v[64:67]\n"
      "v_cvt_pk_bf16_f32 v103, v114, v115\n"
      "v_exp_f32 v113, v177\n"
      "v_or3_b32 v116, v116, v102, v103\n"
      "v_mfma_f32_16x16x32_bf16 v[68:71], a[104:107], v[4:7], v[68:71]\n"
      "ds_write_b128 v125, v[100:103] offset:9216\n"
      "v_exp_f32 v114, v178\n"
      "v_mfma_f32_16x16x32_bf16 v[72:75], a[104:107], v[8:11], v[72:75]\n"
      "v_cvt_pk_bf16_f32 v104, v112, v113\n"
      "v_exp_f32 v115, v179\n"
      "v_mfma_f32_16x16x32_bf16 v[76:79], a[104:107], v[12:15], v[76:79]\n"
      "ds_write_b128 v125, a[168:171] offset:2048\n"
      "v_exp_f32 v112, v180\n"
      "v_mfma_f32_16x16x32_bf16 a[228:231], a[196:199], v[96:99], a[192:195]\n"
      "v_cvt_pk_bf16_f32 v105, v114, v115\n"
      "v_exp_f32 v113, v181\n"
      "v_or3_b32 v116, v116, v104, v105\n"
      "v_mfma_f32_16x16x32_bf16 v[80:83], a[108:111], v[0:3], v[80:83]\n"
      "v_exp_f32 v114, v182\n"
      "v_cvt_pk_bf16_f32 v106, v112, v113\n"
      "s_waitcnt vmcnt(8)\n"
      "s_barrier\n"
      "v_mfma_f32_16x16x32_bf16 v[84:87], a[108:111], v[4:7], v[84:87]\n"
      "ds_write_b128 v125, a[184:187] offset:6144\n"
      "ds_read_b64_tr_b16 a[212:213], v244 offset:33792\n"
      "v_mfma_f32_16x16x32_bf16 v[88:91], a[108:111], v[8:11], v[88:91]\n"
      "ds_read_b64_tr_b16 a[214:215], v244 offset:37888\n"
      "v_exp_f32 v115, v183\n"
      "v_exp_f32 v112, v184\n"
      "v_mfma_f32_16x16x32_bf16 v[92:95], a[108:111], v[12:15], v[92:95]\n"
      "ds_read_b64_tr_b16 a[216:217], v245 offset:33792\n"
      "v_cvt_pk_bf16_f32 v107, v114, v115\n"
      "v_exp_f32 v113, v185\n"
      "v_mfma_f32_16x16x32_bf16 v[64:67], a[120:123], v[16:19], v[64:67]\n"
      "s_add_u32 m0, %[wb], 82944\n"
      "ds_write_b128 v125, v[104:107] offset:10240\n"
      "ds_read_b64_tr_b16 a[218:219], v245 offset:37888\n"
      "v_mfma_f32_16x16x32_bf16 v[68:71], a[120:123], v[20:23], v[68:71]\n"
      "buffer_load_dwordx4 v249, %[srdv], s78 offen nt lds\n"
      "ds_read_b64_tr_b16 a[220:221], v248 offset:33792\n"
      "v_mfma_f32_16x16x32_bf16 v[72:75], a[120:123], v[24:27], v[72:75]\n"
      "s_add_u32 m0, %[wb], 91136\n"
      "ds_read_b64_tr_b16 a[222:223], v248 offset:37888\n"
      "v_or3_b32 v116, v116, v106, v107\n"
      "v_exp_f32 v114, v186\n"
      "v_mfma_f32_16x16x32_bf16 v[76:79], a[120:123], v[28:31], v[76:79]\n"
      "buffer_load_dwordx4 v122, %[srdv], s78 offen nt lds\n"
      "ds_write_b128 v125, a[172:175] offset:3072\n"
      "ds_read_b64_tr_b16 a[224:225], v120 offset:33792\n"
      "v_mfma_f32_16x16x32_bf16 a[228:231], a[200:203], v[100:103], a[228:231]\n"
      "ds_read_b64_tr_b16 a[226:227], v120 offset:37888\n"
      "v_mfma_f32_16x16x32_bf16 v[80:83], a[124:127], v[16:19], v[80:83]\n"
      "v_cvt_pk_bf16_f32 v108, v112, v113\n"
      "v_exp_f32 v115, v187\n"
      "v_exp_f32 v112, v188\n"
      "v_mfma_f32_16x16x32_bf16 v[84:87], a[124:127], v[20:23], v[84:87]\n"
      "ds_write_b128 v125, a[188:191] offset:7168\n"
      "v_cvt_pk_bf16_f32 v109, v114, v115\n"
      "v_mfma_f32_16x16x32_bf16 v[88:91], a[124:127], v[24:27], v[88:91]\n"
      "v_exp_f32 v113, v189\n"
      "v_or3_b32 v116, v116, v108, v109\n"
      "v_mfma_f32_16x16x32_bf16 v[92:95], a[124:127], v[28:31], v[92:95]\n"
      "v_exp_f32 v114, v190\n"
      "v_cvt_pk_bf16_f32 v110, v112, v113\n"
      "v_exp_f32 v115, v191\n"
      "v_mfma_f32_16x16x32_bf16 a[228:231], a[204:207], v[104:107], a[228:231]\n"
      "s_nop 0\n"
      "v_cvt_pk_bf16_f32 v111, v114, v115\n"
      "v_or3_b32 v116, v116, v110, v111\n"
      "v_and_b32 v116, 0x40004000, v116\n"
      "v_cmp_ne_u32 vcc, 0, v116\n"
      "s_cbranch_vccnz FB1_%=\n"
      "RET1_6_%=:\n"
      "ds_write_b128 v125, v[108:111] offset:11264\n"
      "v_mfma_f32_16x16x32_bf16 a[228:231], a[208:211], v[108:111], a[228:231]\n"
      "s_waitcnt lgkmcnt(7)\n"
      "v_mfma_f32_16x16x32_bf16 v[64:67], a[212:215], v[96:99], v[64:67]\n"
      "v_mov_b32 v148, 1.0\n"
      "v_mfma_f32_16x16x32_bf16 v[80:83], a[216:219], v[96:99], v[80:83]\n"
      "v_mov_b32 v149, 1.0\n"
      "v_mfma_f32_16x16x32_bf16 v[68:71], a[212:215], v[100:103], v[68:71]\n"
      "v_mov_b32 v157, %[fo]\n"
      "v_mfma_f32_16x16x32_bf16 v[84:87], a[216:219], v[100:103], v[84:87]\n"
      "v_mov_b32 v150, s72\n"
      "v_mfma_f32_16x16x32_bf16 v[72:75], a[212:215], v[104:107], v[72:75]\n"
      "v_mov_b32 v151, s76\n"
      "ds_write_b32 v125, a228 offset:12288\n"
      "v_mfma_f32_16x16x32_bf16 v[88:91], a[216:219], v[104:107], v[88:91]\n"
      "v_lshrrev_b32 v159, 1, v127\n"
      "v_mfma_f32_16x16x32_bf16 v[76:79], a[212:215], v[108:111], v[76:79]\n"
      "v_xor_b32 v158, 32, v157\n"
      "v_mfma_f32_16x16x32_bf16 v[92:95], a[216:219], v[108:111], v[92:95]\n"
      "v_and_b32 v158, 0x60, v158\n"
      "s_waitcnt lgkmcnt(0)\n"
      "s_barrier\n"
      "s_memrealtime s[68:69]\n"
      "ds_read_b32 v237, v127 offset:12296\n"
      "ds_read_b64 v[152:153], v126 offset:12288\n"
      "ds_read_b128 v[160:163], v126 offset:0\n"
      "ds_read_b128 v[176:179], v126 offset:4096\n"
      "ds_read_b128 v[164:167], v126 offset:1024\n"
      "ds_read_b128 v[180:183], v126 offset:5120\n"
      "ds_read_b128 v[168:171], v126 offset:2048\n"
      "ds_read_b128 v[184:187], v126 offset:6144\n"
      "ds_read_b128 v[172:175], v126 offset:3072\n"
      "ds_read_b128 v[188:191], v126 offset:7168\n"
      "ds_read_b128 a[64:67], v126 offset:8192\n"
      "ds_read_b128 a[68:71], v126 offset:9216\n"
      "ds_read_b128 a[72:75], v126 offset:10240\n"
      "ds_read_b128 a[76:79], v126 offset:11264\n"
      "v_accvgpr_read_b32 v144, a228\n"
      "v_and_b32 v118, 0xf0, v127\n"
      "v_lshrrev_b32 v156, 3, v127\n"
      "v_and_b32 v156, 32, v156\n"
      "v_lshl_or_b32 v156, v118, 4, v156\n"
      "v_lshrrev_b32 v118, 5, v127\n"
      "v_and_or_b32 v156, v118, 16, v156\n"
      "v_add_u32 v156, 0x1000, v156\n"
      "v_add_u32 v155, 0x2000, v156\n"
      "v_xor_b32 v242, 0x2000, v240\n"
      "v_xor_b32 v243, 0x2000, v241\n"
      "v_mov_b32 v236, v150\n"
      "s_waitcnt lgkmcnt(12)\n"
      "v_cmp_neq_f32 vcc, v153, v117\n"
      "v_readfirstlane_b32 s88, v237\n"
      "s_cbranch_vccnz MIS_%=\n"
      "MR_%=:\n"
      "v_mul_f32 v144, v144, v148\n"
      "v_mov_b32 v145, v117\n"
      "v_fmac_f32 v144, v152, v149\n"
      "s_waitcnt lgkmcnt(10)\n"
      "v_pk_add_f32 v[64:65], v[64:65], v[160:161]\n"
      "v_pk_add_f32 v[66:67], v[66:67], v[162:163]\n"
      "v_pk_add_f32 v[80:81], v[80:81], v[176:177]\n"
      "v_pk_add_f32 v[82:83], v[82:83], v[178:179]\n"
      "s_waitcnt lgkmcnt(8)\n"
      "v_pk_add_f32 v[68:69], v[68:69], v[164:165]\n"
      "v_pk_add_f32 v[70:71], v[70:71], v[166:167]\n"
      "v_pk_add_f32 v[84:85], v[84:85], v[180:181]\n"
      "v_pk_add_f32 v[86:87], v[86:87], v[182:183]\n"
      "s_waitcnt lgkmcnt(6)\n"
      "v_pk_add_f32 v[72:73], v[72:73], v[168:169]\n"
      "v_pk_add_f32 v[74:75], v[74:75], v[170:171]\n"
      "v_pk_add_f32 v[88:89], v[88:89], v[184:185]\n"
      "v_pk_add_f32 v[90:91], v[90:91], v[186:187]\n"
      "s_waitcnt lgkmcnt(4)\n"
      "v_pk_add_f32 v[76:77], v[76:77], v[172:173]\n"
      "v_pk_add_f32 v[78:79], v[78:79], v[174:175]\n"
      "v_pk_add_f32 v[92:93], v[92:93], v[188:189]\n"
      "v_pk_add_f32 v[94:95], v[94:95], v[190:191]\n"
      "s_waitcnt lgkmcnt(0)\n"
      "s_add_u32 s65, s68, 36\n"
      "s_mov_b32 s66, s88\n"
      "s_sub_u32 s67, s65, s66\n"
      "s_cmp_lt_i32 s67, 0\n"
      "s_cselect_b32 s88, s66, s65\n"
      "v_mov_b32 v237, s88\n"
      "s_cmp_lg_u32 %[xw], 0\n"
      "s_cbranch_scc1 NH_%=\n"
      "s_mov_b64 exec, 1\n"
      "global_store_dwordx2 v157, v[236:237], %[line] sc1\n"
      "s_mov_b64 exec, -1\n"
      "NH_%=:\n"
      "v_mfma_f32_16x16x32_bf16 v[64:67], a[220:223], a[64:67], v[64:67]\n"
      "v_mfma_f32_16x16x32_bf16 v[80:83], a[224:227], a[64:67], v[80:83]\n"
      "v_mfma_f32_16x16x32_bf16 v[68:71], a[220:223], a[68:71], v[68:71]\n"
      "v_mfma_f32_16x16x32_bf16 v[84:87], a[224:227], a[68:71], v[84:87]\n"
      "v_mfma_f32_16x16x32_bf16 v[72:75], a[220:223], a[72:75], v[72:75]\n"
      "v_mfma_f32_16x16x32_bf16 v[88:91], a[224:227], a[72:75], v[88:91]\n"
      "v_mfma_f32_16x16x32_bf16 v[76:79], a[220:223], a[76:79], v[76:79]\n"
      "v_mfma_f32_16x16x32_bf16 v[92:95], a[224:227], a[76:79], v[92:95]\n"
      "s_nop 1\n"
      "s_memrealtime s[68:69]\n"
      "v_cvt_pk_bf16_f32 v128, v64, v65\n"
      "v_cvt_pk_bf16_f32 v129, v66, v67\n"
      "v_cvt_pk_bf16_f32 v130, v80, v81\n"
      "v_cvt_pk_bf16_f32 v131, v82, v83\n"
      "v_cvt_pk_bf16_f32 v132, v68, v69\n"
      "v_cvt_pk_bf16_f32 v133, v70, v71\n"
      "v_cvt_pk_bf16_f32 v134, v84, v85\n"
      "v_cvt_pk_bf16_f32 v135, v86, v87\n"
      "v_cvt_pk_bf16_f32 v136, v72, v73\n"
      "v_cvt_pk_bf16_f32 v137, v74, v75\n"
      "v_cvt_pk_bf16_f32 v138, v88, v89\n"
      "v_cvt_pk_bf16_f32 v139, v90, v91\n"
      "v_cvt_pk_bf16_f32 v140, v76, v77\n"
      "v_cvt_pk_bf16_f32 v141, v78, v79\n"
      "v_cvt_pk_bf16_f32 v142, v92, v93\n"
      "v_cvt_pk_bf16_f32 v143, v94, v95\n"
      "v_mad_u32_u24 v147, v144, s60, v151\n"
      "v_mad_u32_u24 v147, v145, s62, v147\n"
      "v_add3_u32 v146, v150, v144, v145\n"
      "v_mad_u32_u24 v147, v128, s60, v147\n"
      "v_mad_u32_u24 v147, v129, s62, v147\n"
      "v_add3_u32 v146, v146, v128, v129\n"
      "v_mad_u32_u24 v147, v130, s70, v147\n"
      "v_mad_u32_u24 v147, v131, s71, v147\n"
      "v_add3_u32 v146, v146, v130, v131\n"
      "v_mad_u32_u24 v147, v132, s60, v147\n"
      "v_mad_u32_u24 v147, v133, s62, v147\n"
      "v_add3_u32 v146, v146, v132, v133\n"
      "v_mad_u32_u24 v147, v134, s70, v147\n"
      "v_mad_u32_u24 v147, v135, s71, v147\n"
      "v_add3_u32 v146, v146, v134, v135\n"
      "v_mad_u32_u24 v147, v136, s60, v147\n"
      "v_mad_u32_u24 v147, v137, s62, v147\n"
      "v_add3_u32 v146, v146, v136, v137\n"
      "v_mad_u32_u24 v147, v138, s70, v147\n"
      "v_mad_u32_u24 v147, v139, s71, v147\n"
      "v_add3_u32 v146, v146, v138, v139\n"
      "v_mad_u32_u24 v147, v140, s60, v147\n"
      "v_mad_u32_u24 v147, v141, s62, v147\n"
      "v_add3_u32 v146, v146, v140, v141\n"
      "v_mad_u32_u24 v147, v142, s70, v147\n"
      "v_mad_u32_u24 v147, v143, s71, v147\n"
      "v_add3_u32 v146, v146, v142, v143\n"
      "s_waitcnt lgkmcnt(0)\n"
      "s_call_b64 s[84:85], SPIN_%=\n"
      "global_load_dwordx2 v[232:233], v158, %[line] sc1\n"
      "buffer_store_dwordx4 v[128:131], v127, %[srdx], %[xo] offen offset:0 sc1\n"
      "buffer_store_dwordx4 v[132:135], v127, %[srdx], %[xo] offen offset:1024 sc1\n"
      "buffer_store_dwordx4 v[136:139], v127, %[srdx], %[xo] offen offset:2048 sc1\n"
      "buffer_store_dwordx4 v[140:143], v127, %[srdx], %[xo] offen offset:3072 sc1\n"
      "buffer_store_dwordx4 v[144:147], v127, %[srdx], s81 offen sc1\n"
      "global_load_dwordx2 v[234:235], v158, %[line] sc1\n"
      "s_waitcnt vmcnt(7)\n"
      "s_barrier\n"
      "ds_read_b64_tr_b16 a[96:97], v246 offset:32768\n"
      "ds_read_b64_tr_b16 a[98:99], v246 offset:36864\n"
      "ds_read_b64_tr_b16 a[100:101], v247 offset:32768\n"
      "ds_read_b64_tr_b16 a[102:103], v247 offset:36864\n"
      "ds_read_b64_tr_b16 a[112:113], v250 offset:0\n"
      "ds_read_b64_tr_b16 a[114:115], v250 offset:4096\n"
      "ds_read_b64_tr_b16 a[116:117], v251 offset:0\n"
      "ds_read_b64_tr_b16 a[118:119], v251 offset:4096\n"
      "ds_read_b64_tr_b16 a[0:1], v250 offset:32768\n"
      "ds_read_b64_tr_b16 a[2:3], v250 offset:36864\n"
      "ds_read_b64_tr_b16 a[4:5], v251 offset:32768\n"
      "ds_read_b64_tr_b16 a[6:7], v251 offset:36864\n"
      "s_waitcnt lgkmcnt(8)\n"
      "v_mfma_f32_16x16x32_bf16 a[128:131], a[96:99], v[208:211], a[128:131]\n"
      "ds_read_b64_tr_b16 a[104:105], v244 offset:32768\n"
      "ds_read_b64_tr_b16 a[106:107], v244 offset:36864\n"
      "v_mfma_f32_16x16x32_bf16 a[144:147], a[100:103], v[208:211], a[144:147]\n"
      "ds_read_b64_tr_b16 a[108:109], v245 offset:32768\n"
      "ds_read_b64_tr_b16 a[110:111], v245 offset:36864\n"
      "s_waitcnt lgkmcnt(8)\n"
      "v_mfma_f32_16x16x32_bf16 a[128:131], a[112:115], v[192:195], a[128:131]\n"
      "ds_read_b64_tr_b16 a[120:121], v240 offset:0\n"
      "ds_read_b64_tr_b16 a[122:123], v240 offset:4096\n"
      "v_mfma_f32_16x16x32_bf16 a[144:147], a[116:119], v[192:195], a[144:147]\n"
      "ds_read_b64_tr_b16 a[124:125], v241 offset:0\n"
      "ds_read_b64_tr_b16 a[126:127], v241 offset:4096\n"
      "s_waitcnt lgkmcnt(8)\n"
      "v_mfma_f32_16x16x32_bf16 a[128:131], a[0:3], v[0:3], a[128:131]\n"
      "ds_read_b64_tr_b16 a[8:9], v240 offset:32768\n"
      "ds_read_b64_tr_b16 a[10:11], v240 offset:36864\n"
      "v_mfma_f32_16x16x32_bf16 a[144:147], a[4:7], v[0:3], a[144:147]\n"
      "ds_read_b64_tr_b16 a[12:13], v241 offset:32768\n"
      "ds_read_b64_tr_b16 a[14:15], v241 offset:36864\n"
      "v_mfma_f32_16x16x32_bf16 a[132:135], a[96:99], v[212:215], a[132:135]\n"
      "ds_write_b128 v125, v[16:19] offset:8192\n"
      "v_lshrrev_b32 v118, 2, v127\n"
      "v_mfma_f32_16x16x32_bf16 a[148:151], a[100:103], v[212:215], a[148:151]\n"
      "v_xor_b32 v118, v118, v127\n"
      "v_mfma_f32_16x16x32_bf16 a[132:135], a[112:115], v[196:199], a[132:135]\n"
      "v_and_b32 v118, 0xc0, v118\n"
      "v_mfma_f32_16x16x32_bf16 a[148:151], a[116:119], v[196:199], a[148:151]\n"
      "v_and_or_b32 v118, v127, 32, v118\n"
      "v_mfma_f32_16x16x32_bf16 a[132:135], a[0:3], v[4:7], a[132:135]\n"
      "ds_write_b128 v125, v[20:23] offset:9216\n"
      "v_and_b32 v119, 16, v127\n"
      "v_mfma_f32_16x16x32_bf16 a[148:151], a[4:7], v[4:7], a[148:151]\n"
      "v_mov_b32 v84, 0x3f80\n"
      "v_mfma_f32_16x16x32_bf16 a[136:139], a[96:99], v[216:219], a[136:139]\n"
      "v_lshlrev_b32 v119, v119, v84\n"
      "v_mfma_f32_16x16x32_bf16 a[152:155], a[100:103], v[216:219], a[152:155]\n"
      "v_cmp_eq_u32 vcc, 0, v118\n"
      "v_mfma_f32_16x16x32_bf16 a[136:139], a[112:115], v[200:203], a[136:139]\n"
      "ds_write_b128 v125, v[24:27] offset:10240\n"
      "v_mov_b32 v84, 0\n"
      "v_mfma_f32_16x16x32_bf16 a[152:155], a[116:119], v[200:203], a[152:155]\n"
      "v_cndmask_b32 v86, 0, v119, vcc\n"
      "v_mfma_f32_16x16x32_bf16 a[136:139], a[0:3], v[8:11], a[136:139]\n"
      "v_cmp_eq_u32 vcc, 32, v118\n"
      "v_mfma_f32_16x16x32_bf16 a[152:155], a[4:7], v[8:11], a[152:155]\n"
      "v_mov_b32 v85, 0\n"
      "v_mfma_f32_16x16x32_bf16 a[140:143], a[96:99], v[220:223], a[140:143]\n"
      "ds_write_b128 v125, v[28:31] offset:11264\n"
      "v_cndmask_b32 v87, 0, v119, vcc\n"
      "v_mfma_f32_16x16x32_bf16 a[156:159], a[100:103], v[220:223], a[156:159]\n"
      "v_mov_b32 v88, 0\n"
      "v_mfma_f32_16x16x32_bf16 a[140:143], a[112:115], v[204:207], a[140:143]\n"
      "v_mov_b32 v89, 0\n"
      "v_mfma_f32_16x16x32_bf16 a[156:159], a[116:119], v[204:207], a[156:159]\n"
      "v_mfma_f32_16x16x32_bf16 a[140:143], a[0:3], v[12:15], a[140:143]\n"
      "ds_write_b128 v125, a[128:131] offset:0\n"
      "v_mfma_f32_16x16x32_bf16 a[156:159], a[4:7], v[12:15], a[156:159]\n"
      "s_waitcnt lgkmcnt(13)\n"
      "v_mfma_f32_16x16x32_bf16 v[32:35], a[104:107], v[208:211], v[32:35]\n"
      "ds_read_b64_tr_b16 a[32:33], v240 offset:1024\n"
      "v_mfma_f32_16x16x32_bf16 v[36:39], a[104:107], v[212:215], v[36:39]\n"
      "ds_read_b64_tr_b16 a[34:35], v240 offset:5120\n"
      "v_mfma_f32_16x16x32_bf16 v[40:43], a[104:107], v[216:219], v[40:43]\n"
      "ds_write_b128 v125, a[144:147] offset:4096\n"
      "ds_read_b64_tr_b16 a[36:37], v241 offset:1024\n"
      "v_mfma_f32_16x16x32_bf16 v[44:47], a[104:107], v[220:223], v[44:47]\n"
      "ds_read_b64_tr_b16 a[38:39], v241 offset:5120\n"
      "v_mfma_f32_16x16x32_bf16 v[48:51], a[108:111], v[208:211], v[48:51]\n"
      "ds_read_b64_tr_b16 a[40:41], v242 offset:1024\n"
      "v_mfma_f32_16x16x32_bf16 v[52:55], a[108:111], v[212:215], v[52:55]\n"
      "ds_read_b64_tr_b16 a[42:43], v242 offset:5120\n"
      "v_mfma_f32_16x16x32_bf16 v[56:59], a[108:111], v[216:219], v[56:59]\n"
      "ds_write_b128 v125, a[132:135] offset:1024\n"
      "ds_read_b64_tr_b16 a[44:45], v243 offset:1024\n"
      "v_mfma_f32_16x16x32_bf16 v[60:63], a[108:111], v[220:223], v[60:63]\n"
      "ds_read_b64_tr_b16 a[46:47], v243 offset:5120\n"
      "s_waitcnt lgkmcnt(15)\n"
      "v_mfma_f32_16x16x32_bf16 v[32:35], a[120:123], v[192:195], v[32:35]\n"
      "ds_read_b64_tr_b16 a[16:17], v244 offset:0\n"
      "v_mfma_f32_16x16x32_bf16 v[36:39], a[120:123], v[196:199], v[36:39]\n"
      "ds_read_b64_tr_b16 a[18:19], v244 offset:4096\n"
      "v_mfma_f32_16x16x32_bf16 v[40:43], a[120:123], v[200:203], v[40:43]\n"
      "ds_write_b128 v125, a[148:151] offset:5120\n"
      "ds_read_b64_tr_b16 a[20:21], v245 offset:0\n"
      "v_mfma_f32_16x16x32_bf16 v[44:47], a[120:123], v[204:207], v[44:47]\n"
      "ds_read_b64_tr_b16 a[22:23], v245 offset:4096\n"
      "v_mfma_f32_16x16x32_bf16 v[48:51], a[124:127], v[192:195], v[48:51]\n"
      "ds_read_b64_tr_b16 a[24:25], v248 offset:0\n"
      "v_mfma_f32_16x16x32_bf16 v[52:55], a[124:127], v[196:199], v[52:55]\n"
      "ds_read_b64_tr_b16 a[26:27], v248 offset:4096\n"
      "v_mfma_f32_16x16x32_bf16 v[56:59], a[124:127], v[200:203], v[56:59]\n"
      "ds_write_b128 v125, a[136:139] offset:2048\n"
      "ds_read_b64_tr_b16 a[28:29], v120 offset:0\n"
      "v_mfma_f32_16x16x32_bf16 v[60:63], a[124:127], v[204:207], v[60:63]\n"
      "ds_read_b64_tr_b16 a[30:31], v120 offset:4096\n"
      "s_waitcnt lgkmcnt(15)\n"
      "v_mfma_f32_16x16x32_bf16 v[32:35], a[8:11], v[0:3], v[32:35]\n"
      "v_mfma_f32_16x16x32_bf16 v[36:39], a[8:11], v[4:7], v[36:39]\n"
      "v_mfma_f32_16x16x32_bf16 v[40:43], a[8:11], v[8:11], v[40:43]\n"
      "ds_write_b128 v125, a[152:155] offset:6144\n"
      "v_mfma_f32_16x16x32_bf16 v[44:47], a[8:11], v[12:15], v[44:47]\n"
      "s_waitcnt vmcnt(6)\n"
      "v_cmp_ne_u32 vcc, v232, v150\n"
      "s_memrealtime s[68:69]\n"
      "s_cbranch_vccnz POLL_%=\n"
      "GOT_%=:\n"
      "v_readfirstlane_b32 s88, v233\n"
      "s_waitcnt lgkmcnt(0)\n"
      "s_add_u32 s88, s88, 60\n"
      "s_sub_u32 s67, s88, s68\n"
      "s_cmp_gt_u32 s67, 1000\n"
      "s_cbranch_scc1 LD_%=\n"
      "s_call_b64 s[84:85], SPIN_%=\n"
      "LD_%=:\n"
      "buffer_load_dwordx4 v[80:83], v127, %[srdx], s79 offen sc1\n"
      "buffer_load_dwordx4 v[64:67], v127, %[srdx], s80 offen offset:0 sc1\n"
      "buffer_load_dwordx4 v[68:71], v127, %[srdx], s80 offen offset:1024 sc1\n"
      "buffer_load_dwordx4 v[72:75], v127, %[srdx], s80 offen offset:2048 sc1\n"
      "buffer_load_dwordx4 v[76:79], v127, %[srdx], s80 offen offset:3072 sc1\n"
      "v_mfma_f32_16x16x32_bf16 v[48:51], a[12:15], v[0:3], v[48:51]\n"
      "v_mfma_f32_16x16x32_bf16 v[52:55], a[12:15], v[4:7], v[52:55]\n"
      "v_mfma_f32_16x16x32_bf16 v[56:59], a[12:15], v[8:11], v[56:59]\n"
      "ds_write_b128 v125, a[140:143] offset:3072\n"
      "v_mfma_f32_16x16x32_bf16 v[60:63], a[12:15], v[12:15], v[60:63]\n"
      "s_waitcnt lgkmcnt(15)\n"
      "v_mfma_f32_16x16x32_bf16 v[32:35], a[32:35], v[96:99], v[32:35]\n"
      "v_mfma_f32_16x16x32_bf16 v[48:51], a[36:39], v[96:99], v[48:51]\n"
      "v_mfma_f32_16x16x32_bf16 v[36:39], a[32:35], v[100:103], v[36:39]\n"
      "ds_write_b128 v125, a[156:159] offset:7168\n"
      "v_mfma_f32_16x16x32_bf16 v[52:55], a[36:39], v[100:103], v[52:55]\n"
      "v_mfma_f32_16x16x32_bf16 v[40:43], a[32:35], v[104:107], v[40:43]\n"
      "v_mfma_f32_16x16x32_bf16 v[56:59], a[36:39], v[104:107], v[56:59]\n"
      "v_mfma_f32_16x16x32_bf16 v[44:47], a[32:35], v[108:111], v[44:47]\n"
      "v_mfma_f32_16x16x32_bf16 v[60:63], a[36:39], v[108:111], v[60:63]\n"
      "s_waitcnt lgkmcnt(13)\n"
      "v_mfma_f32_16x16x32_bf16 v[32:35], a[40:43], a[64:67], v[32:35]\n"
      "v_mfma_f32_16x16x32_bf16 v[48:51], a[44:47], a[64:67], v[48:51]\n"
      "v_mfma_f32_16x16x32_bf16 v[36:39], a[40:43], a[68:71], v[36:39]\n"
      "v_mfma_f32_16x16x32_bf16 v[52:55], a[44:47], a[68:71], v[52:55]\n"
      "v_mfma_f32_16x16x32_bf16 v[40:43], a[40:43], a[72:75], v[40:43]\n"
      "v_mfma_f32_16x16x32_bf16 v[56:59], a[44:47], a[72:75], v[56:59]\n"
      "v_mfma_f32_16x16x32_bf16 v[44:47], a[40:43], a[76:79], v[44:47]\n"
      "v_mfma_f32_16x16x32_bf16 v[60:63], a[44:47], a[76:79], v[60:63]\n"
      "s_waitcnt lgkmcnt(8)\n"
      "v_mfma_f32_16x16x32_bf16 v[32:35], a[16:19], v[16:19], v[32:35]\n"
      "v_mfma_f32_16x16x32_bf16 v[48:51], a[20:23], v[16:19], v[48:51]\n"
      "v_mfma_f32_16x16x32_bf16 v[36:39], a[16:19], v[20:23], v[36:39]\n"
      "v_mfma_f32_16x16x32_bf16 v[52:55], a[20:23], v[20:23], v[52:55]\n"
      "v_mfma_f32_16x16x32_bf16 v[40:43], a[16:19], v[24:27], v[40:43]\n"
      "v_mfma_f32_16x16x32_bf16 v[56:59], a[20:23], v[24:27], v[56:59]\n"
      "v_mfma_f32_16x16x32_bf16 v[44:47], a[16:19], v[28:31], v[44:47]\n"
      "v_mfma_f32_16x16x32_bf16 v[60:63], a[20:23], v[28:31], v[60:63]\n"
      "s_waitcnt lgkmcnt(0)\n"
      "s_barrier\n"
      "ds_read_b128 v[160:163], v126 offset:0\n"
      "ds_read_b128 v[176:179], v126 offset:4096\n"
      "ds_read_b128 v[164:167], v126 offset:1024\n"
      "ds_read_b128 v[180:183], v126 offset:5120\n"
      "ds_read_b128 v[168:171], v126 offset:2048\n"
      "ds_read_b128 v[184:187], v126 offset:6144\n"
      "ds_read_b128 v[172:175], v126 offset:3072\n"
      "ds_read_b128 v[188:191], v126 offset:7168\n"
      "ds_read_b128 a[80:83], v126 offset:8192\n"
      "ds_read_b128 a[84:87], v126 offset:9216\n"
      "ds_read_b128 a[88:91], v126 offset:10240\n"
      "ds_read_b128 a[92:95], v126 offset:11264\n"
      "s_waitcnt lgkmcnt(10)\n"
      "v_pk_add_f32 v[32:33], v[32:33], v[160:161]\n"
      "v_pk_add_f32 v[34:35], v[34:35], v[162:163]\n"
      "v_pk_add_f32 v[48:49], v[48:49], v[176:177]\n"
      "v_pk_add_f32 v[50:51], v[50:51], v[178:179]\n"
      "s_waitcnt lgkmcnt(8)\n"
      "v_pk_add_f32 v[36:37], v[36:37], v[164:165]\n"
      "v_pk_add_f32 v[38:39], v[38:39], v[166:167]\n"
      "v_pk_add_f32 v[52:53], v[52:53], v[180:181]\n"
      "v_pk_add_f32 v[54:55], v[54:55], v[182:183]\n"
      "s_waitcnt lgkmcnt(6)\n"
      "v_pk_add_f32 v[40:41], v[40:41], v[168:169]\n"
      "v_pk_add_f32 v[42:43], v[42:43], v[170:171]\n"
      "v_pk_add_f32 v[56:57], v[56:57], v[184:185]\n"
      "v_pk_add_f32 v[58:59], v[58:59], v[186:187]\n"
      "s_waitcnt lgkmcnt(4)\n"
      "v_pk_add_f32 v[44:45], v[44:45], v[172:173]\n"
      "v_pk_add_f32 v[46:47], v[46:47], v[174:175]\n"
      "v_pk_add_f32 v[60:61], v[60:61], v[188:189]\n"
      "v_pk_add_f32 v[62:63], v[62:63], v[190:191]\n"
      "s_waitcnt lgkmcnt(0)\n"
      "v_mfma_f32_16x16x32_bf16 v[32:35], a[24:27], a[80:83], v[32:35]\n"
      "v_mfma_f32_16x16x32_bf16 v[48:51], a[28:31], a[80:83], v[48:51]\n"
      "v_mfma_f32_16x16x32_bf16 v[36:39], a[24:27], a[84:87], v[36:39]\n"
      "v_mfma_f32_16x16x32_bf16 v[52:55], a[28:31], a[84:87], v[52:55]\n"
      "v_mfma_f32_16x16x32_bf16 v[40:43], a[24:27], a[88:91], v[40:43]\n"
      "v_mfma_f32_16x16x32_bf16 v[56:59], a[28:31], a[88:91], v[56:59]\n"
      "v_mfma_f32_16x16x32_bf16 v[44:47], a[24:27], a[92:95], v[44:47]\n"
      "v_mfma_f32_16x16x32_bf16 v[60:63], a[28:31], a[92:95], v[60:63]\n"
      "CHK_%=:\n"
      "s_waitcnt vmcnt(4)\n"
      "v_add_f32 v224, v144, v80\n"
      "v_rcp_f32 v224, v224\n"
      "v_mad_u32_u24 v119, v80, s60, v151\n"
      "v_mad_u32_u24 v119, v81, s62, v119\n"
      "v_add3_u32 v118, v150, v80, v81\n"
      "s_waitcnt vmcnt(3)\n"
      "v_mfma_f32_16x16x32_bf16 v[160:163], v[86:89], v[64:67], v[32:35]\n"
      "v_mad_u32_u24 v119, v64, s60, v119\n"
      "v_mad_u32_u24 v119, v65, s62, v119\n"
      "v_add3_u32 v118, v118, v64, v65\n"
      "v_mov_b32 v226, v224\n"
      "v_mfma_f32_16x16x32_bf16 v[176:179], v[84:87], v[64:67], v[48:51]\n"
      "v_mad_u32_u24 v119, v66, s70, v119\n"
      "v_mad_u32_u24 v119, v67, s71, v119\n"
      "v_add3_u32 v118, v118, v66, v67\n"
      "v_permlane32_swap_b32 v224, v226\n"
      "s_waitcnt vmcnt(2)\n"
      "v_mfma_f32_16x16x32_bf16 v[164:167], v[86:89], v[68:71], v[36:39]\n"
      "v_mad_u32_u24 v119, v68, s60, v119\n"
      "v_mad_u32_u24 v119, v69, s62, v119\n"
      "v_add3_u32 v118, v118, v68, v69\n"
      "v_mov_b32 v225, v224\n"
      "v_mov_b32 v227, v226\n"
      "v_mfma_f32_16x16x32_bf16 v[180:183], v[84:87], v[68:71], v[52:55]\n"
      "v_mad_u32_u24 v119, v70, s70, v119\n"
      "v_mad_u32_u24 v119, v71, s71, v119\n"
      "v_add3_u32 v118, v118, v70, v71\n"
      "v_permlane16_swap_b32 v224, v225\n"
      "s_waitcnt vmcnt(1)\n"
      "v_mfma_f32_16x16x32_bf16 v[168:171], v[86:89], v[72:75], v[40:43]\n"
      "v_mad_u32_u24 v119, v72, s60, v119\n"
      "v_mad_u32_u24 v119, v73, s62, v119\n"
      "v_add3_u32 v118, v118, v72, v73\n"
      "v_permlane16_swap_b32 v226, v227\n"
      "v_mfma_f32_16x16x32_bf16 v[184:187], v[84:87], v[72:75], v[56:59]\n"
      "v_mad_u32_u24 v119, v74, s70, v119\n"
      "v_mad_u32_u24 v119, v75, s71, v119\n"
      "v_add3_u32 v118, v118, v74, v75\n"
      "s_waitcnt vmcnt(0)\n"
      "v_mfma_f32_16x16x32_bf16 v[172:175], v[86:89], v[76:79], v[44:47]\n"
      "v_mad_u32_u24 v119, v76, s60, v119\n"
      "v_mad_u32_u24 v119, v77, s62, v119\n"
      "v_add3_u32 v118, v118, v76, v77\n"
      "v_mfma_f32_16x16x32_bf16 v[188:191], v[84:87], v[76:79], v[60:63]\n"
      "v_mad_u32_u24 v119, v78, s70, v119\n"
      "v_mad_u32_u24 v119, v79, s71, v119\n"
      "v_add3_u32 v118, v118, v78, v79\n"
      "v_cmp_ne_u64 vcc, v[118:119], v[82:83]\n"
      "s_cbranch_vccnz RETRY_%=\n"
      "OK_%=:\n"
      "v_cmp_neq_f32 vcc, v81, v117\n"
      "s_cbranch_vccnz FIX_%=\n"
      "FR_%=:\n"
      "v_pk_mul_f32 v[160:161], v[160:161], v[224:225] op_sel:[0,0] op_sel_hi:[1,0]\n"
      "v_pk_mul_f32 v[162:163], v[162:163], v[224:225] op_sel:[0,0] op_sel_hi:[1,0]\n"
      "v_pk_mul_f32 v[176:177], v[176:177], v[224:225] op_sel:[0,0] op_sel_hi:[1,0]\n"
      "v_pk_mul_f32 v[178:179], v[178:179], v[224:225] op_sel:[0,0] op_sel_hi:[1,0]\n"
      "v_cvt_pk_bf16_f32 v128, v160, v161\n"
      "v_cvt_pk_bf16_f32 v130, v176, v177\n"
      "v_cvt_pk_bf16_f32 v129, v162, v163\n"
      "v_cvt_pk_bf16_f32 v131, v178, v179\n"
      "v_pk_mul_f32 v[164:165], v[164:165], v[224:225] op_sel:[0,1] op_sel_hi:[1,1]\n"
      "v_permlane16_swap_b32 v128, v130\n"
      "v_permlane16_swap_b32 v129, v131\n"
      "global_store_dwordx4 v156, v[128:131], %[outp] offset:-4096 sc1\n"
      "v_pk_mul_f32 v[166:167], v[166:167], v[224:225] op_sel:[0,1] op_sel_hi:[1,1]\n"
      "v_pk_mul_f32 v[180:181], v[180:181], v[224:225] op_sel:[0,1] op_sel_hi:[1,1]\n"
      "v_pk_mul_f32 v[182:183], v[182:183], v[224:225] op_sel:[0,1] op_sel_hi:[1,1]\n"
      "v_cvt_pk_bf16_f32 v132, v164, v165\n"
      "v_cvt_pk_bf16_f32 v134, v180, v181\n"
      "v_cvt_pk_bf16_f32 v133, v166, v167\n"
      "v_cvt_pk_bf16_f32 v135, v182, v183\n"
      "v_pk_mul_f32 v[168:169], v[168:169], v[226:227] op_sel:[0,0] op_sel_hi:[1,0]\n"
      "v_permlane16_swap_b32 v132, v134\n"
      "v_permlane16_swap_b32 v133, v135\n"
      "global_store_dwordx4 v156, v[132:135], %[outp] offset:0 sc1\n"
      "v_pk_mul_f32 v[170:171], v[170:171], v[226:227] op_sel:[0,0] op_sel_hi:[1,0]\n"
      "v_pk_mul_f32 v[184:185], v[184:185], v[226:227] op_sel:[0,0] op_sel_hi:[1,0]\n"
      "v_pk_mul_f32 v[186:187], v[186:187], v[226:227] op_sel:[0,0] op_sel_hi:[1,0]\n"
      "v_cvt_pk_bf16_f32 v136, v168, v169\n"
      "v_cvt_pk_bf16_f32 v138, v184, v185\n"
      "v_cvt_pk_bf16_f32 v137, v170, v171\n"
      "v_cvt_pk_bf16_f32 v139, v186, v187\n"
      "v_pk_mul_f32 v[172:173], v[172:173], v[226:227] op_sel:[0,1] op_sel_hi:[1,1]\n"
      "v_permlane16_swap_b32 v136, v138\n"
      "v_permlane16_swap_b32 v137, v139\n"
      "global_store_dwordx4 v155, v[136:139], %[outp] offset:-4096 sc1\n"
      "v_pk_mul_f32 v[174:175], v[174:175], v[226:227] op_sel:[0,1] op_sel_hi:[1,1]\n"
      "v_pk_mul_f32 v[188:189], v[188:189], v[226:227] op_sel:[0,1] op_sel_hi:[1,1]\n"
      "v_pk_mul_f32 v[190:191], v[190:191], v[226:227] op_sel:[0,1] op_sel_hi:[1,1]\n"
      "v_cvt_pk_bf16_f32 v140, v172, v173\n"
      "v_cvt_pk_bf16_f32 v142, v188, v189\n"
      "v_cvt_pk_bf16_f32 v141, v174, v175\n"
      "v_cvt_pk_bf16_f32 v143, v190, v191\n"
      "v_mov_b32 v157, v159\n"
      "v_permlane16_swap_b32 v140, v142\n"
      "v_permlane16_swap_b32 v141, v143\n"
      "global_store_dwordx4 v155, v[140:143], %[outp] offset:0 sc1\n"
      "s_cmp_lg_u32 %[fo], 64\n"
      "s_cbranch_scc1 END_%=\n"
      "s_mov_b64 exec, 1\n"
      "global_store_dwordx2 v157, v[150:151], %[line] sc1\n"
      "s_mov_b64 exec, -1\n"
      "END_%=:\n"
      ".subsection 1\n"
      "POLL_%=:\n"
      "s_waitcnt vmcnt(0) lgkmcnt(0)\n"
      "v_cmp_ne_u32 vcc, v234, v150\n"
      "v_mov_b32 v233, v235\n"
      "s_memrealtime s[68:69]\n"
      "s_sub_u32 s63, s63, 1\n"
      "s_cbranch_vccz GOT_%=\n"
      "global_load_dwordx2 v[234:235], v158, %[line] sc1\n"
      "s_cbranch_scc0 POLL_%=\n"
      "s_branch GOT_%=\n"
      "RETRY_%=:\n"
      "s_sub_u32 s63, s63, 1\n"
      "s_cbranch_scc1 OK_%=\n"
      "s_sleep 4\n"
      "buffer_load_dwordx4 v[80:83], v127, %[srdx], s79 offen sc1\n"
      "buffer_load_dwordx4 v[64:67], v127, %[srdx], s80 offen offset:0 sc1\n"
      "buffer_load_dwordx4 v[68:71], v127, %[srdx], s80 offen offset:1024 sc1\n"
      "buffer_load_dwordx4 v[72:75], v127, %[srdx], s80 offen offset:2048 sc1\n"
      "buffer_load_dwordx4 v[76:79], v127, %[srdx], s80 offen offset:3072 sc1\n"
      "s_branch CHK_%=\n"
      "GW0_%=:\n"
      "s_call_b64 s[84:85], WAIT_%=\n"
      "s_branch GX0_%=\n"
      "GW1_%=:\n"
      "s_call_b64 s[84:85], WAIT_%=\n"
      "s_branch GX1_%=\n"
      "GW2_%=:\n"
      "s_call_b64 s[84:85], WAIT_%=\n"
      "s_branch GX2_%=\n"
      "GW3_%=:\n"
      "s_call_b64 s[84:85], WAIT_%=\n"
      "s_add_u32 s87, s87, 1\n"
      "s_branch GX3_%=\n"
      "GW4_%=:\n"
      "s_call_b64 s[84:85], WAIT_%=\n"
      "s_branch GX4_%=\n"
      "GW5_%=:\n"
      "s_call_b64 s[84:85], WAIT_%=\n"
      "s_branch GX5_%=\n"
      "GW6_%=:\n"
      "s_call_b64 s[84:85], WAIT_%=\n"
      "s_branch GX6_%=\n"
      "GW7_%=:\n"
      "s_call_b64 s[84:85], WAIT_%=\n"
      "s_branch GX7_%=\n"
      "GW8_%=:\n"
      "s_call_b64 s[84:85], WAIT_%=\n"
      "s_branch GX8_%=\n"
      "WAIT_%=:\n"
      "s_add_u32 s87, s87, 1\n"
      "s_bitcmp1_b32 s89, s87\n"
      "s_cbranch_scc1 H1_%=\n"
      "SPIN_%=:\n"
      "s_sub_u32 s67, s68, s88\n"
      "s_cmp_ge_i32 s67, 0\n"
      "s_cbranch_scc1 GO_%=\n"
      "s_sleep 1\n"
      "s_memrealtime s[68:69]\n"
      "s_waitcnt lgkmcnt(0)\n"
      "s_branch SPIN_%=\n"
      "GO_%=:\n"
      "s_setpc_b64 s[84:85]\n"
      "H1_%=:\n"
      "s_bitcmp1_b32 0xaaa80, s87\n"
      "s_cbranch_scc0 H2_%=\n"
      "s_add_u32 s67, s66, 400\n"
      "s_waitcnt vmcnt(8)\n"
      "v_sub_u32 v250, s67, v250\n"
      "v_cmp_gt_u32 vcc, 1200, v250\n"
      "s_add_u32 s76, s66, 100000\n"
      "s_sub_u32 s69, s66, s71\n"
      "s_cmp_eq_u32 s87, 7\n"
      "s_cselect_b32 s75, 22, 18\n"
      "s_bcnt1_i32_b32 s74, vcc_lo\n"
      "v_cmp_gt_u32 vcc, 730, v250\n"
      "s_cmp_lt_i32 s69, -1000\n"
      "s_cselect_b32 s75, 13, s75\n"
      "s_cmp_ge_u32 s87, 12\n"
      "s_cselect_b32 s101, 50, s101\n"
      "s_bcnt1_i32_b32 s67, vcc_lo\n"
      "s_cmp_le_u32 s67, 1\n"
      "s_cselect_b32 s86, 107, s86\n"
      "s_cselect_b32 s71, s76, s71\n"
      "s_cmp_ge_u32 s67, 5\n"
      "s_cselect_b32 s86, 112, s86\n"
      "s_cmp_ge_u32 s67, s75\n"
      "s_cselect_b32 s74, 32, s74\n"
      "s_cmp_ge_u32 s74, 26\n"
      "s_cbranch_scc1 H3_%=\n"
      "s_cmp_eq_u32 s87, 19\n"
      "s_cbranch_scc1 H4_%=\n"
      "s_cmp_lt_u32 %[rep], 128\n"
      "s_cbranch_scc0 SPIN_%=\n"
      "s_bitcmp1_b32 0x2aaa0, s87\n"
      "s_cbranch_scc0 SPIN_%=\n"
      "H5_%=:\n"
      "v_mov_b32 v127, s66\n"
      "RS_%=:\n"
      "s_sub_u32 s67, s68, s88\n"
      "s_cmp_ge_i32 s67, 0\n"
      "s_cbranch_scc1 RG_%=\n"
      "s_sleep 1\n"
      "s_memrealtime s[68:69]\n"
      "s_waitcnt lgkmcnt(0)\n"
      "s_branch RS_%=\n"
      "RG_%=:\n"
      "s_getreg_b32 s67, hwreg(HW_REG_IB_STS, 0, 4)\n"
      "s_cmp_le_u32 s67, 4\n"
      "s_cbranch_scc1 GO_%=\n"
      "buffer_store_dword v127, v124, %[srdr], %[rep] offen sc1\n"
      "s_setpc_b64 s[84:85]\n"
      "H3_%=:\n"
      "s_mul_i32 s76, s69, 110\n"
      "s_ashr_i32 s76, s76, 10\n"
      "s_min_i32 s76, s76, 40\n"
      "s_max_i32 s76, s76, -15\n"
      "s_add_u32 s66, s66, s76\n"
      "s_add_u32 s66, s66, 95\n"
      "s_mov_b32 s99, 16\n"
      "s_mov_b32 s100, 292\n"
      "s_add_u32 s93, s93, 99\n"
      "s_mov_b32 s86, 112\n"
      "s_sub_u32 s67, s65, s66\n"
      "s_cmp_lt_i32 s67, 0\n"
      "s_cselect_b32 s88, s66, s65\n"
      "H4_%=:\n"
      "s_mov_b32 s91, 0\n"
      "s_mov_b32 s89, 0\n"
      "s_mov_b32 s101, 50\n"
      "v_mov_b32 v251, 0x100000\n"
      "s_branch SPIN_%=\n"
      "H0_%=:\n"
      "buffer_load_dword v250, v251, %[srdr], 0 offen sc1\n"
      "s_branch SPIN_%=\n"
      "H2_%=:\n"
      "s_cmp_gt_u32 s87, 2\n"
      "s_cbranch_scc1 H5_%=\n"
      "s_cmp_eq_u32 s87, 1\n"
      "s_cbranch_scc1 H0_%=\n"
      "s_waitcnt vmcnt(8)\n"
      "v_subrev_u32 v250, s71, v250\n"
      "v_min_u32 v250, 0x258, v250\n"
      "v_mbcnt_lo_u32_b32 v251, -1, 0\n"
      "s_nop 0\n"
      "v_add_u32_dpp v250, v250, v250 row_shr:1 row_mask:0xf bank_mask:0xf bound_ctrl:0\n"
      "v_lshlrev_b32 v124, 10, v251\n"
      "v_and_b32 v251, 31, v251\n"
      "v_add_u32_dpp v250, v250, v250 row_shr:2 row_mask:0xf bank_mask:0xf bound_ctrl:0\n"
      "v_lshlrev_b32 v251, 2, v251\n"
      "v_add_u32 v124, 0x2000, v124\n"
      "v_add_u32_dpp v250, v250, v250 row_shr:4 row_mask:0xf bank_mask:0xf bound_ctrl:0\n"
      "v_add_u32 v251, %[vrd], v251\n"
      "v_readlane_b32 s66, v250, 7\n"
      "s_nop 0\n"
      "s_lshr_b32 s66, s66, 3\n"
      "s_add_u32 s66, s66, s71\n"
      "s_add_u32 s66, s66, %[cph]\n"
      "s_add_u32 s71, s66, 545\n"
      "s_sub_u32 s67, s65, s66\n"
      "s_cmp_lt_i32 s67, 0\n"
      "s_cselect_b32 s88, s66, s65\n"
      "s_branch SPIN_%=\n"
      "FB0_%=:\n"
      "s_nop 7\n"
      "s_nop 7\n"
      "v_xor_b32 v118, 0x0, v242\n"
      "v_xor_b32 v119, 0x0, v243\n"
      "ds_read_b128 a[64:67], v118 offset:0\n"
      "ds_read_b128 a[68:71], v118 offset:4096\n"
      "ds_read_b128 a[72:75], v119 offset:0\n"
      "ds_read_b128 a[76:79], v119 offset:4096\n"
      "ds_read_b128 a[80:83], v118 offset:1024\n"
      "ds_read_b128 a[84:87], v118 offset:5120\n"
      "ds_read_b128 a[88:91], v119 offset:1024\n"
      "ds_read_b128 a[92:95], v119 offset:5120\n"
      "s_waitcnt lgkmcnt(0)\n"
      "v_mfma_f32_16x16x32_bf16 v[128:131], a[64:67], a[0:3], 0\n"
      "v_mfma_f32_16x16x32_bf16 v[136:139], a[64:67], a[4:7], 0\n"
      "v_mfma_f32_16x16x32_bf16 v[144:147], a[64:67], a[8:11], 0\n"
      "v_mfma_f32_16x16x32_bf16 v[152:155], a[64:67], a[12:15], 0\n"
      "v_mfma_f32_16x16x32_bf16 v[132:135], a[68:71], a[0:3], 0\n"
      "v_mfma_f32_16x16x32_bf16 v[140:143], a[68:71], a[4:7], 0\n"
      "v_mfma_f32_16x16x32_bf16 v[148:151], a[68:71], a[8:11], 0\n"
      "v_mfma_f32_16x16x32_bf16 v[156:159], a[68:71], a[12:15], 0\n"
      "v_mfma_f32_16x16x32_bf16 v[128:131], a[72:75], a[16:19], v[128:131]\n"
      "v_mfma_f32_16x16x32_bf16 v[136:139], a[72:75], a[20:23], v[136:139]\n"
      "v_mfma_f32_16x16x32_bf16 v[144:147], a[72:75], a[24:27], v[144:147]\n"
      "v_mfma_f32_16x16x32_bf16 v[152:155], a[72:75], a[28:31], v[152:155]\n"
      "v_mfma_f32_16x16x32_bf16 v[132:135], a[76:79], a[16:19], v[132:135]\n"
      "v_mfma_f32_16x16x32_bf16 v[140:143], a[76:79], a[20:23], v[140:143]\n"
      "v_mfma_f32_16x16x32_bf16 v[148:151], a[76:79], a[24:27], v[148:151]\n"
      "v_mfma_f32_16x16x32_bf16 v[156:159], a[76:79], a[28:31], v[156:159]\n"
      "v_mfma_f32_16x16x32_bf16 v[128:131], a[80:83], a[32:35], v[128:131]\n"
      "v_mfma_f32_16x16x32_bf16 v[136:139], a[80:83], a[36:39], v[136:139]\n"
      "v_mfma_f32_16x16x32_bf16 v[144:147], a[80:83], a[40:43], v[144:147]\n"
      "v_mfma_f32_16x16x32_bf16 v[152:155], a[80:83], a[44:47], v[152:155]\n"
      "v_mfma_f32_16x16x32_bf16 v[132:135], a[84:87], a[32:35], v[132:135]\n"
      "v_mfma_f32_16x16x32_bf16 v[140:143], a[84:87], a[36:39], v[140:143]\n"
      "v_mfma_f32_16x16x32_bf16 v[148:151], a[84:87], a[40:43], v[148:151]\n"
      "v_mfma_f32_16x16x32_bf16 v[156:159], a[84:87], a[44:47], v[156:159]\n"
      "v_mfma_f32_16x16x32_bf16 v[128:131], a[88:91], a[48:51], v[128:131]\n"
      "v_mfma_f32_16x16x32_bf16 v[136:139], a[88:91], a[52:55], v[136:139]\n"
      "v_mfma_f32_16x16x32_bf16 v[144:147], a[88:91], a[56:59], v[144:147]\n"
      "v_mfma_f32_16x16x32_bf16 v[152:155], a[88:91], a[60:63], v[152:155]\n"
      "v_mfma_f32_16x16x32_bf16 v[132:135], a[92:95], a[48:51], v[132:135]\n"
      "v_mfma_f32_16x16x32_bf16 v[140:143], a[92:95], a[52:55], v[140:143]\n"
      "v_mfma_f32_16x16x32_bf16 v[148:151], a[92:95], a[56:59], v[148:151]\n"
      "v_mfma_f32_16x16x32_bf16 v[156:159], a[92:95], a[60:63], v[156:159]\n"
      "s_nop 7\n"
      "v_max3_f32 v112, v128, v129, v130\n"
      "v_max3_f32 v113, v136, v137, v138\n"
      "v_max3_f32 v114, v144, v145, v146\n"
      "v_max3_f32 v115, v152, v153, v154\n"
      "v_max3_f32 v112, v112, v131, v132\n"
      "v_max3_f32 v113, v113, v139, v140\n"
      "v_max3_f32 v114, v114, v147, v148\n"
      "v_max3_f32 v115, v115, v155, v156\n"
      "v_max3_f32 v112, v112, v133, v134\n"
      "v_max3_f32 v113, v113, v141, v142\n"
      "v_max3_f32 v114, v114, v149, v150\n"
      "v_max3_f32 v115, v115, v157, v158\n"
      "v_max_f32 v112, v112, v135\n"
      "v_max_f32 v113, v113, v143\n"
      "v_max_f32 v114, v114, v151\n"
      "v_max_f32 v115, v115, v159\n"
      "v_mov_b32 v160, v112\n"
      "v_mov_b32 v161, v113\n"
      "v_mov_b32 v162, v114\n"
      "v_mov_b32 v163, v115\n"
      "v_permlane16_swap_b32 v112, v160\n"
      "v_permlane16_swap_b32 v113, v161\n"
      "v_permlane16_swap_b32 v114, v162\n"
      "v_permlane16_swap_b32 v115, v163\n"
      "v_max_f32 v112, v112, v160\n"
      "v_max_f32 v113, v113, v161\n"
      "v_max_f32 v114, v114, v162\n"
      "v_max_f32 v115, v115, v163\n"
      "v_mov_b32 v160, v112\n"
      "v_mov_b32 v161, v113\n"
      "v_mov_b32 v162, v114\n"
      "v_mov_b32 v163, v115\n"
      "v_permlane32_swap_b32 v112, v160\n"
      "v_permlane32_swap_b32 v113, v161\n"
      "v_permlane32_swap_b32 v114, v162\n"
      "v_permlane32_swap_b32 v115, v163\n"
      "v_max_f32 v112, v112, v160\n"
      "v_max_f32 v113, v113, v161\n"
      "v_max_f32 v114, v114, v162\n"
      "v_max_f32 v115, v115, v163\n"
      "v_sub_f32 v112, 0xc1800000, v112\n"
      "v_sub_f32 v113, 0xc1800000, v113\n"
      "v_sub_f32 v114, 0xc1800000, v114\n"
      "v_sub_f32 v115, 0xc1800000, v115\n"
      "v_floor_f32 v112, v112\n"
      "v_floor_f32 v113, v113\n"
      "v_floor_f32 v114, v114\n"
      "v_floor_f32 v115, v115\n"
      "v_min_f32 v112, v112, v224\n"
      "v_min_f32 v113, v113, v228\n"
      "v_min_f32 v114, v114, v232\n"
      "v_min_f32 v115, v115, v236\n"
      "v_sub_f32 v164, v112, v224\n"
      "v_sub_f32 v165, v113, v228\n"
      "v_sub_f32 v166, v114, v232\n"
      "v_sub_f32 v167, v115, v236\n"
      "v_mov_b32 v224, v112\n"
      "v_mov_b32 v228, v113\n"
      "v_mov_b32 v232, v114\n"
      "v_mov_b32 v236, v115\n"
      "v_exp_f32 v112, v164\n"
      "v_exp_f32 v113, v165\n"
      "v_exp_f32 v114, v166\n"
      "v_exp_f32 v115, v167\n"
      "v_mov_b32 v225, v224\n"
      "v_mov_b32 v229, v228\n"
      "v_mov_b32 v233, v232\n"
      "v_mov_b32 v237, v236\n"
      "v_mov_b64 v[226:227], v[224:225]\n"
      "v_mov_b64 v[230:231], v[228:229]\n"
      "v_mov_b64 v[234:235], v[232:233]\n"
      "v_mov_b64 v[238:239], v[236:237]\n"
      "s_call_b64 s[84:85], SCALE_%=\n"
      "v_pk_add_f32 v[128:129], v[128:129], v[224:225]\n"
      "v_pk_add_f32 v[130:131], v[130:131], v[224:225]\n"
      "v_pk_add_f32 v[132:133], v[132:133], v[224:225]\n"
      "v_pk_add_f32 v[134:135], v[134:135], v[224:225]\n"
      "v_pk_add_f32 v[136:137], v[136:137], v[228:229]\n"
      "v_pk_add_f32 v[138:139], v[138:139], v[228:229]\n"
      "v_pk_add_f32 v[140:141], v[140:141], v[228:229]\n"
      "v_pk_add_f32 v[142:143], v[142:143], v[228:229]\n"
      "v_pk_add_f32 v[144:145], v[144:145], v[232:233]\n"
      "v_pk_add_f32 v[146:147], v[146:147], v[232:233]\n"
      "v_pk_add_f32 v[148:149], v[148:149], v[232:233]\n"
      "v_pk_add_f32 v[150:151], v[150:151], v[232:233]\n"
      "v_pk_add_f32 v[152:153], v[152:153], v[236:237]\n"
      "v_pk_add_f32 v[154:155], v[154:155], v[236:237]\n"
      "v_pk_add_f32 v[156:157], v[156:157], v[236:237]\n"
      "v_pk_add_f32 v[158:159], v[158:159], v[236:237]\n"
      "v_exp_f32 v112, v128\n"
      "v_exp_f32 v113, v129\n"
      "v_exp_f32 v114, v130\n"
      "v_cvt_pk_bf16_f32 v128, v112, v113\n"
      "v_exp_f32 v115, v131\n"
      "v_exp_f32 v112, v132\n"
      "v_cvt_pk_bf16_f32 v129, v114, v115\n"
      "v_exp_f32 v113, v133\n"
      "v_exp_f32 v114, v134\n"
      "v_cvt_pk_bf16_f32 v130, v112, v113\n"
      "v_exp_f32 v115, v135\n"
      "v_exp_f32 v112, v136\n"
      "v_cvt_pk_bf16_f32 v131, v114, v115\n"
      "v_exp_f32 v113, v137\n"
      "v_exp_f32 v114, v138\n"
      "v_cvt_pk_bf16_f32 v132, v112, v113\n"
      "v_exp_f32 v115, v139\n"
      "v_exp_f32 v112, v140\n"
      "v_cvt_pk_bf16_f32 v133, v114, v115\n"
      "v_exp_f32 v113, v141\n"
      "v_exp_f32 v114, v142\n"
      "v_cvt_pk_bf16_f32 v134, v112, v113\n"
      "v_exp_f32 v115, v143\n"
      "v_exp_f32 v112, v144\n"
      "v_cvt_pk_bf16_f32 v135, v114, v115\n"
      "v_exp_f32 v113, v145\n"
      "v_exp_f32 v114, v146\n"
      "v_cvt_pk_bf16_f32 v136, v112, v113\n"
      "v_exp_f32 v115, v147\n"
      "v_exp_f32 v112, v148\n"
      "v_cvt_pk_bf16_f32 v137, v114, v115\n"
      "v_exp_f32 v113, v149\n"
      "v_exp_f32 v114, v150\n"
      "v_cvt_pk_bf16_f32 v138, v112, v113\n"
      "v_exp_f32 v115, v151\n"
      "v_exp_f32 v112, v152\n"
      "v_cvt_pk_bf16_f32 v139, v114, v115\n"
      "v_exp_f32 v113, v153\n"
      "v_exp_f32 v114, v154\n"
      "v_cvt_pk_bf16_f32 v140, v112, v113\n"
      "v_exp_f32 v115, v155\n"
      "v_exp_f32 v112, v156\n"
      "v_cvt_pk_bf16_f32 v141, v114, v115\n"
      "v_exp_f32 v113, v157\n"
      "v_exp_f32 v114, v158\n"
      "v_cvt_pk_bf16_f32 v142, v112, v113\n"
      "v_exp_f32 v115, v159\n"
      "s_nop 0\n"
      "v_cvt_pk_bf16_f32 v143, v114, v115\n"
      "ds_read_b128 a[64:67], v242 offset:32768\n"
      "ds_read_b128 a[68:71], v242 offset:36864\n"
      "ds_read_b128 a[72:75], v243 offset:32768\n"
      "ds_read_b128 a[76:79], v243 offset:36864\n"
      "ds_read_b128 a[80:83], v242 offset:33792\n"
      "ds_read_b128 a[84:87], v242 offset:37888\n"
      "ds_read_b128 a[88:91], v243 offset:33792\n"
      "ds_read_b128 a[92:95], v243 offset:37888\n"
      "s_waitcnt lgkmcnt(0)\n"
      "v_mfma_f32_16x16x32_bf16 v[160:163], a[64:67], a[0:3], v[224:227]\n"
      "v_mfma_f32_16x16x32_bf16 v[168:171], a[64:67], a[4:7], v[228:231]\n"
      "v_mfma_f32_16x16x32_bf16 v[176:179], a[64:67], a[8:11], v[232:235]\n"
      "v_mfma_f32_16x16x32_bf16 v[184:187], a[64:67], a[12:15], v[236:239]\n"
      "v_mfma_f32_16x16x32_bf16 v[164:167], a[68:71], a[0:3], v[224:227]\n"
      "v_mfma_f32_16x16x32_bf16 v[172:175], a[68:71], a[4:7], v[228:231]\n"
      "v_mfma_f32_16x16x32_bf16 v[180:183], a[68:71], a[8:11], v[232:235]\n"
      "v_mfma_f32_16x16x32_bf16 v[188:191], a[68:71], a[12:15], v[236:239]\n"
      "v_mfma_f32_16x16x32_bf16 v[160:163], a[72:75], a[16:19], v[160:163]\n"
      "v_mfma_f32_16x16x32_bf16 v[168:171], a[72:75], a[20:23], v[168:171]\n"
      "v_mfma_f32_16x16x32_bf16 v[176:179], a[72:75], a[24:27], v[176:179]\n"
      "v_mfma_f32_16x16x32_bf16 v[184:187], a[72:75], a[28:31], v[184:187]\n"
      "v_mfma_f32_16x16x32_bf16 v[164:167], a[76:79], a[16:19], v[164:167]\n"
      "v_mfma_f32_16x16x32_bf16 v[172:175], a[76:79], a[20:23], v[172:175]\n"
      "v_mfma_f32_16x16x32_bf16 v[180:183], a[76:79], a[24:27], v[180:183]\n"
      "v_mfma_f32_16x16x32_bf16 v[188:191], a[76:79], a[28:31], v[188:191]\n"
      "v_mfma_f32_16x16x32_bf16 v[160:163], a[80:83], a[32:35], v[160:163]\n"
      "v_mfma_f32_16x16x32_bf16 v[168:171], a[80:83], a[36:39], v[168:171]\n"
      "v_mfma_f32_16x16x32_bf16 v[176:179], a[80:83], a[40:43], v[176:179]\n"
      "v_mfma_f32_16x16x32_bf16 v[184:187], a[80:83], a[44:47], v[184:187]\n"
      "v_mfma_f32_16x16x32_bf16 v[164:167], a[84:87], a[32:35], v[164:167]\n"
      "v_mfma_f32_16x16x32_bf16 v[172:175], a[84:87], a[36:39], v[172:175]\n"
      "v_mfma_f32_16x16x32_bf16 v[180:183], a[84:87], a[40:43], v[180:183]\n"
      "v_mfma_f32_16x16x32_bf16 v[188:191], a[84:87], a[44:47], v[188:191]\n"
      "v_mfma_f32_16x16x32_bf16 v[160:163], a[88:91], a[48:51], v[160:163]\n"
      "v_mfma_f32_16x16x32_bf16 v[168:171], a[88:91], a[52:55], v[168:171]\n"
      "v_mfma_f32_16x16x32_bf16 v[176:179], a[88:91], a[56:59], v[176:179]\n"
      "v_mfma_f32_16x16x32_bf16 v[184:187], a[88:91], a[60:63], v[184:187]\n"
      "v_mfma_f32_16x16x32_bf16 v[164:167], a[92:95], a[48:51], v[164:167]\n"
      "v_mfma_f32_16x16x32_bf16 v[172:175], a[92:95], a[52:55], v[172:175]\n"
      "v_mfma_f32_16x16x32_bf16 v[180:183], a[92:95], a[56:59], v[180:183]\n"
      "v_mfma_f32_16x16x32_bf16 v[188:191], a[92:95], a[60:63], v[188:191]\n"
      "s_cmp_eq_u32 s64, 0\n"
      "s_cbranch_scc0 NRET0_0_%=\n"
      "v_mov_b64 v[192:193], v[128:129]\n"
      "v_mov_b64 v[194:195], v[130:131]\n"
      "v_mov_b64 v[196:197], v[132:133]\n"
      "v_mov_b64 v[198:199], v[134:135]\n"
      "v_mov_b64 v[200:201], v[136:137]\n"
      "v_mov_b64 v[202:203], v[138:139]\n"
      "v_mov_b64 v[204:205], v[140:141]\n"
      "v_mov_b64 v[206:207], v[142:143]\n"
      "s_nop 1\n"
      "s_branch RET0_0_%=\n"
      "NRET0_0_%=:\n"
      "s_cmp_eq_u32 s64, 1\n"
      "s_cbranch_scc0 NRET0_1_%=\n"
      "v_mov_b64 v[192:193], v[128:129]\n"
      "v_mov_b64 v[194:195], v[130:131]\n"
      "v_mov_b64 v[196:197], v[132:133]\n"
      "v_mov_b64 v[198:199], v[134:135]\n"
      "v_mov_b64 v[200:201], v[136:137]\n"
      "v_mov_b64 v[202:203], v[138:139]\n"
      "v_mov_b64 v[204:205], v[140:141]\n"
      "v_mov_b64 v[206:207], v[142:143]\n"
      "s_nop 1\n"
      "s_branch RET0_1_%=\n"
      "NRET0_1_%=:\n"
      "s_cmp_eq_u32 s64, 2\n"
      "s_cbranch_scc0 NRET0_2_%=\n"
      "v_mov_b64 v[192:193], v[128:129]\n"
      "v_mov_b64 v[194:195], v[130:131]\n"
      "v_mov_b64 v[196:197], v[132:133]\n"
      "v_mov_b64 v[198:199], v[134:135]\n"
      "v_mov_b64 v[200:201], v[136:137]\n"
      "v_mov_b64 v[202:203], v[138:139]\n"
      "v_mov_b64 v[204:205], v[140:141]\n"
      "v_mov_b64 v[206:207], v[142:143]\n"
      "s_nop 1\n"
      "s_branch RET0_2_%=\n"
      "NRET0_2_%=:\n"
      "s_cmp_eq_u32 s64, 3\n"
      "s_cbranch_scc0 NRET0_3_%=\n"
      "v_mov_b64 v[192:193], v[128:129]\n"
      "v_mov_b64 v[194:195], v[130:131]\n"
      "v_mov_b64 v[196:197], v[132:133]\n"
      "v_mov_b64 v[198:199], v[134:135]\n"
      "v_mov_b64 v[200:201], v[136:137]\n"
      "v_mov_b64 v[202:203], v[138:139]\n"
      "v_mov_b64 v[204:205], v[140:141]\n"
      "v_mov_b64 v[206:207], v[142:143]\n"
      "s_nop 1\n"
      "s_branch RET0_3_%=\n"
      "NRET0_3_%=:\n"
      "s_cmp_eq_u32 s64, 4\n"
      "s_cbranch_scc0 NRET0_4_%=\n"
      "v_mov_b64 v[192:193], v[128:129]\n"
      "v_mov_b64 v[194:195], v[130:131]\n"
      "v_mov_b64 v[196:197], v[132:133]\n"
      "v_mov_b64 v[198:199], v[134:135]\n"
      "v_mov_b64 v[200:201], v[136:137]\n"
      "v_mov_b64 v[202:203], v[138:139]\n"
      "v_mov_b64 v[204:205], v[140:141]\n"
      "v_mov_b64 v[206:207], v[142:143]\n"
      "s_nop 1\n"
      "s_branch RET0_4_%=\n"
      "NRET0_4_%=:\n"
      "s_cmp_eq_u32 s64, 5\n"
      "s_cbranch_scc0 NRET0_5_%=\n"
      "v_mov_b64 v[16:17], v[128:129]\n"
      "v_mov_b64 v[18:19], v[130:131]\n"
      "v_mov_b64 v[20:21], v[132:133]\n"
      "v_mov_b64 v[22:23], v[134:135]\n"
      "v_mov_b64 v[24:25], v[136:137]\n"
      "v_mov_b64 v[26:27], v[138:139]\n"
      "v_mov_b64 v[28:29], v[140:141]\n"
      "v_mov_b64 v[30:31], v[142:143]\n"
      "s_nop 1\n"
      "s_branch RET0_5_%=\n"
      "NRET0_5_%=:\n"
      "FB1_%=:\n"
      "s_add_u32 s84, s63, 1\n"
      "s_sub_u32 s84, s84, %[n1]\n"
      "s_or_b32 s84, s84, s64\n"
      "s_cbranch_scc0 RET1_0_%=\n"
      "s_nop 7\n"
      "s_nop 7\n"
      "v_xor_b32 v118, 0x10000, v242\n"
      "v_xor_b32 v119, 0x10000, v243\n"
      "ds_read_b128 a[64:67], v118 offset:32768\n"
      "ds_read_b128 a[68:71], v118 offset:36864\n"
      "ds_read_b128 a[72:75], v119 offset:32768\n"
      "ds_read_b128 a[76:79], v119 offset:36864\n"
      "ds_read_b128 a[80:83], v118 offset:33792\n"
      "ds_read_b128 a[84:87], v118 offset:37888\n"
      "ds_read_b128 a[88:91], v119 offset:33792\n"
      "ds_read_b128 a[92:95], v119 offset:37888\n"
      "s_waitcnt lgkmcnt(0)\n"
      "v_mfma_f32_16x16x32_bf16 v[160:163], a[64:67], a[0:3], 0\n"
      "v_mfma_f32_16x16x32_bf16 v[168:171], a[64:67], a[4:7], 0\n"
      "v_mfma_f32_16x16x32_bf16 v[176:179], a[64:67], a[8:11], 0\n"
      "v_mfma_f32_16x16x32_bf16 v[184:187], a[64:67], a[12:15], 0\n"
      "v_mfma_f32_16x16x32_bf16 v[164:167], a[68:71], a[0:3], 0\n"
      "v_mfma_f32_16x16x32_bf16 v[172:175], a[68:71], a[4:7], 0\n"
      "v_mfma_f32_16x16x32_bf16 v[180:183], a[68:71], a[8:11], 0\n"
      "v_mfma_f32_16x16x32_bf16 v[188:191], a[68:71], a[12:15], 0\n"
      "v_mfma_f32_16x16x32_bf16 v[160:163], a[72:75], a[16:19], v[160:163]\n"
      "v_mfma_f32_16x16x32_bf16 v[168:171], a[72:75], a[20:23], v[168:171]\n"
      "v_mfma_f32_16x16x32_bf16 v[176:179], a[72:75], a[24:27], v[176:179]\n"
      "v_mfma_f32_16x16x32_bf16 v[184:187], a[72:75], a[28:31], v[184:187]\n"
      "v_mfma_f32_16x16x32_bf16 v[164:167], a[76:79], a[16:19], v[164:167]\n"
      "v_mfma_f32_16x16x32_bf16 v[172:175], a[76:79], a[20:23], v[172:175]\n"
      "v_mfma_f32_16x16x32_bf16 v[180:183], a[76:79], a[24:27], v[180:183]\n"
      "v_mfma_f32_16x16x32_bf16 v[188:191], a[76:79], a[28:31], v[188:191]\n"
      "v_mfma_f32_16x16x32_bf16 v[160:163], a[80:83], a[32:35], v[160:163]\n"
      "v_mfma_f32_16x16x32_bf16 v[168:171], a[80:83], a[36:39], v[168:171]\n"
      "v_mfma_f32_16x16x32_bf16 v[176:179], a[80:83], a[40:43], v[176:179]\n"
      "v_mfma_f32_16x16x32_bf16 v[184:187], a[80:83], a[44:47], v[184:187]\n"
      "v_mfma_f32_16x16x32_bf16 v[164:167], a[84:87], a[32:35], v[164:167]\n"
      "v_mfma_f32_16x16x32_bf16 v[172:175], a[84:87], a[36:39], v[172:175]\n"
      "v_mfma_f32_16x16x32_bf16 v[180:183], a[84:87], a[40:43], v[180:183]\n"
      "v_mfma_f32_16x16x32_bf16 v[188:191], a[84:87], a[44:47], v[188:191]\n"
      "v_mfma_f32_16x16x32_bf16 v[160:163], a[88:91], a[48:51], v[160:163]\n"
      "v_mfma_f32_16x16x32_bf16 v[168:171], a[88:91], a[52:55], v[168:171]\n"
      "v_mfma_f32_16x16x32_bf16 v[176:179], a[88:91], a[56:59], v[176:179]\n"
      "v_mfma_f32_16x16x32_bf16 v[184:187], a[88:91], a[60:63], v[184:187]\n"
      "v_mfma_f32_16x16x32_bf16 v[164:167], a[92:95], a[48:51], v[164:167]\n"
      "v_mfma_f32_16x16x32_bf16 v[172:175], a[92:95], a[52:55], v[172:175]\n"
      "v_mfma_f32_16x16x32_bf16 v[180:183], a[92:95], a[56:59], v[180:183]\n"
      "v_mfma_f32_16x16x32_bf16 v[188:191], a[92:95], a[60:63], v[188:191]\n"
      "s_nop 7\n"
      "v_max3_f32 v112, v160, v161, v162\n"
      "v_max3_f32 v113, v168, v169, v170\n"
      "v_max3_f32 v114, v176, v177, v178\n"
      "v_max3_f32 v115, v184, v185, v186\n"
      "v_max3_f32 v112, v112, v163, v164\n"
      "v_max3_f32 v113, v113, v171, v172\n"
      "v_max3_f32 v114, v114, v179, v180\n"
      "v_max3_f32 v115, v115, v187, v188\n"
      "v_max3_f32 v112, v112, v165, v166\n"
      "v_max3_f32 v113, v113, v173, v174\n"
      "v_max3_f32 v114, v114, v181, v182\n"
      "v_max3_f32 v115, v115, v189, v190\n"
      "v_max_f32 v112, v112, v167\n"
      "v_max_f32 v113, v113, v175\n"
      "v_max_f32 v114, v114, v183\n"
      "v_max_f32 v115, v115, v191\n"
      "v_mov_b32 v128, v112\n"
      "v_mov_b32 v129, v113\n"
      "v_mov_b32 v130, v114\n"
      "v_mov_b32 v131, v115\n"
      "v_permlane16_swap_b32 v112, v128\n"
      "v_permlane16_swap_b32 v113, v129\n"
      "v_permlane16_swap_b32 v114, v130\n"
      "v_permlane16_swap_b32 v115, v131\n"
      "v_max_f32 v112, v112, v128\n"
      "v_max_f32 v113, v113, v129\n"
      "v_max_f32 v114, v114, v130\n"
      "v_max_f32 v115, v115, v131\n"
      "v_mov_b32 v128, v112\n"
      "v_mov_b32 v129, v113\n"
      "v_mov_b32 v130, v114\n"
      "v_mov_b32 v131, v115\n"
      "v_permlane32_swap_b32 v112, v128\n"
      "v_permlane32_swap_b32 v113, v129\n"
      "v_permlane32_swap_b32 v114, v130\n"
      "v_permlane32_swap_b32 v115, v131\n"
      "v_max_f32 v112, v112, v128\n"
      "v_max_f32 v113, v113, v129\n"
      "v_max_f32 v114, v114, v130\n"
      "v_max_f32 v115, v115, v131\n"
      "v_sub_f32 v112, 0xc1800000, v112\n"
      "v_sub_f32 v113, 0xc1800000, v113\n"
      "v_sub_f32 v114, 0xc1800000, v114\n"
      "v_sub_f32 v115, 0xc1800000, v115\n"
      "v_floor_f32 v112, v112\n"
      "v_floor_f32 v113, v113\n"
      "v_floor_f32 v114, v114\n"
      "v_floor_f32 v115, v115\n"
      "v_min_f32 v112, v112, v224\n"
      "v_min_f32 v113, v113, v228\n"
      "v_min_f32 v114, v114, v232\n"
      "v_min_f32 v115, v115, v236\n"
      "v_sub_f32 v132, v112, v224\n"
      "v_sub_f32 v133, v113, v228\n"
      "v_sub_f32 v134, v114, v232\n"
      "v_sub_f32 v135, v115, v236\n"
      "v_mov_b32 v224, v112\n"
      "v_mov_b32 v228, v113\n"
      "v_mov_b32 v232, v114\n"
      "v_mov_b32 v236, v115\n"
      "v_exp_f32 v112, v132\n"
      "v_exp_f32 v113, v133\n"
      "v_exp_f32 v114, v134\n"
      "v_exp_f32 v115, v135\n"
      "v_mov_b32 v225, v224\n"
      "v_mov_b32 v229, v228\n"
      "v_mov_b32 v233, v232\n"
      "v_mov_b32 v237, v236\n"
      "v_mov_b64 v[226:227], v[224:225]\n"
      "v_mov_b64 v[230:231], v[228:229]\n"
      "v_mov_b64 v[234:235], v[232:233]\n"
      "v_mov_b64 v[238:239], v[236:237]\n"
      "s_call_b64 s[84:85], SCALE_%=\n"
      "v_pk_add_f32 v[160:161], v[160:161], v[224:225]\n"
      "v_pk_add_f32 v[162:163], v[162:163], v[224:225]\n"
      "v_pk_add_f32 v[164:165], v[164:165], v[224:225]\n"
      "v_pk_add_f32 v[166:167], v[166:167], v[224:225]\n"
      "v_pk_add_f32 v[168:169], v[168:169], v[228:229]\n"
      "v_pk_add_f32 v[170:171], v[170:171], v[228:229]\n"
      "v_pk_add_f32 v[172:173], v[172:173], v[228:229]\n"
      "v_pk_add_f32 v[174:175], v[174:175], v[228:229]\n"
      "v_pk_add_f32 v[176:177], v[176:177], v[232:233]\n"
      "v_pk_add_f32 v[178:179], v[178:179], v[232:233]\n"
      "v_pk_add_f32 v[180:181], v[180:181], v[232:233]\n"
      "v_pk_add_f32 v[182:183], v[182:183], v[232:233]\n"
      "v_pk_add_f32 v[184:185], v[184:185], v[236:237]\n"
      "v_pk_add_f32 v[186:187], v[186:187], v[236:237]\n"
      "v_pk_add_f32 v[188:189], v[188:189], v[236:237]\n"
      "v_pk_add_f32 v[190:191], v[190:191], v[236:237]\n"
      "v_exp_f32 v112, v160\n"
      "v_exp_f32 v113, v161\n"
      "v_exp_f32 v114, v162\n"
      "v_cvt_pk_bf16_f32 v160, v112, v113\n"
      "v_exp_f32 v115, v163\n"
      "v_exp_f32 v112, v164\n"
      "v_cvt_pk_bf16_f32 v161, v114, v115\n"
      "v_exp_f32 v113, v165\n"
      "v_exp_f32 v114, v166\n"
      "v_cvt_pk_bf16_f32 v162, v112, v113\n"
      "v_exp_f32 v115, v167\n"
      "v_exp_f32 v112, v168\n"
      "v_cvt_pk_bf16_f32 v163, v114, v115\n"
      "v_exp_f32 v113, v169\n"
      "v_exp_f32 v114, v170\n"
      "v_cvt_pk_bf16_f32 v164, v112, v113\n"
      "v_exp_f32 v115, v171\n"
      "v_exp_f32 v112, v172\n"
      "v_cvt_pk_bf16_f32 v165, v114, v115\n"
      "v_exp_f32 v113, v173\n"
      "v_exp_f32 v114, v174\n"
      "v_cvt_pk_bf16_f32 v166, v112, v113\n"
      "v_exp_f32 v115, v175\n"
      "v_exp_f32 v112, v176\n"
      "v_cvt_pk_bf16_f32 v167, v114, v115\n"
      "v_exp_f32 v113, v177\n"
      "v_exp_f32 v114, v178\n"
      "v_cvt_pk_bf16_f32 v168, v112, v113\n"
      "v_exp_f32 v115, v179\n"
      "v_exp_f32 v112, v180\n"
      "v_cvt_pk_bf16_f32 v169, v114, v115\n"
      "v_exp_f32 v113, v181\n"
      "v_exp_f32 v114, v182\n"
      "v_cvt_pk_bf16_f32 v170, v112, v113\n"
      "v_exp_f32 v115, v183\n"
      "v_exp_f32 v112, v184\n"
      "v_cvt_pk_bf16_f32 v171, v114, v115\n"
      "v_exp_f32 v113, v185\n"
      "v_exp_f32 v114, v186\n"
      "v_cvt_pk_bf16_f32 v172, v112, v113\n"
      "v_exp_f32 v115, v187\n"
      "v_exp_f32 v112, v188\n"
      "v_cvt_pk_bf16_f32 v173, v114, v115\n"
      "v_exp_f32 v113, v189\n"
      "v_exp_f32 v114, v190\n"
      "v_cvt_pk_bf16_f32 v174, v112, v113\n"
      "v_exp_f32 v115, v191\n"
      "s_nop 0\n"
      "v_cvt_pk_bf16_f32 v175, v114, v115\n"
      "ds_read_b128 a[64:67], v242 offset:0\n"
      "ds_read_b128 a[68:71], v242 offset:4096\n"
      "ds_read_b128 a[72:75], v243 offset:0\n"
      "ds_read_b128 a[76:79], v243 offset:4096\n"
      "ds_read_b128 a[80:83], v242 offset:1024\n"
      "ds_read_b128 a[84:87], v242 offset:5120\n"
      "ds_read_b128 a[88:91], v243 offset:1024\n"
      "ds_read_b128 a[92:95], v243 offset:5120\n"
      "s_waitcnt lgkmcnt(0)\n"
      "v_mfma_f32_16x16x32_bf16 v[128:131], a[64:67], a[0:3], v[224:227]\n"
      "v_mfma_f32_16x16x32_bf16 v[136:139], a[64:67], a[4:7], v[228:231]\n"
      "v_mfma_f32_16x16x32_bf16 v[144:147], a[64:67], a[8:11], v[232:235]\n"
      "v_mfma_f32_16x16x32_bf16 v[152:155], a[64:67], a[12:15], v[236:239]\n"
      "v_mfma_f32_16x16x32_bf16 v[132:135], a[68:71], a[0:3], v[224:227]\n"
      "v_mfma_f32_16x16x32_bf16 v[140:143], a[68:71], a[4:7], v[228:231]\n"
      "v_mfma_f32_16x16x32_bf16 v[148:151], a[68:71], a[8:11], v[232:235]\n"
      "v_mfma_f32_16x16x32_bf16 v[156:159], a[68:71], a[12:15], v[236:239]\n"
      "v_mfma_f32_16x16x32_bf16 v[128:131], a[72:75], a[16:19], v[128:131]\n"
      "v_mfma_f32_16x16x32_bf16 v[136:139], a[72:75], a[20:23], v[136:139]\n"
      "v_mfma_f32_16x16x32_bf16 v[144:147], a[72:75], a[24:27], v[144:147]\n"
      "v_mfma_f32_16x16x32_bf16 v[152:155], a[72:75], a[28:31], v[152:155]\n"
      "v_mfma_f32_16x16x32_bf16 v[132:135], a[76:79], a[16:19], v[132:135]\n"
      "v_mfma_f32_16x16x32_bf16 v[140:143], a[76:79], a[20:23], v[140:143]\n"
      "v_mfma_f32_16x16x32_bf16 v[148:151], a[76:79], a[24:27], v[148:151]\n"
      "v_mfma_f32_16x16x32_bf16 v[156:159], a[76:79], a[28:31], v[156:159]\n"
      "v_mfma_f32_16x16x32_bf16 v[128:131], a[80:83], a[32:35], v[128:131]\n"
      "v_mfma_f32_16x16x32_bf16 v[136:139], a[80:83], a[36:39], v[136:139]\n"
      "v_mfma_f32_16x16x32_bf16 v[144:147], a[80:83], a[40:43], v[144:147]\n"
      "v_mfma_f32_16x16x32_bf16 v[152:155], a[80:83], a[44:47], v[152:155]\n"
      "v_mfma_f32_16x16x32_bf16 v[132:135], a[84:87], a[32:35], v[132:135]\n"
      "v_mfma_f32_16x16x32_bf16 v[140:143], a[84:87], a[36:39], v[140:143]\n"
      "v_mfma_f32_16x16x32_bf16 v[148:151], a[84:87], a[40:43], v[148:151]\n"
      "v_mfma_f32_16x16x32_bf16 v[156:159], a[84:87], a[44:47], v[156:159]\n"
      "v_mfma_f32_16x16x32_bf16 v[128:131], a[88:91], a[48:51], v[128:131]\n"
      "v_mfma_f32_16x16x32_bf16 v[136:139], a[88:91], a[52:55], v[136:139]\n"
      "v_mfma_f32_16x16x32_bf16 v[144:147], a[88:91], a[56:59], v[144:147]\n"
      "v_mfma_f32_16x16x32_bf16 v[152:155], a[88:91], a[60:63], v[152:155]\n"
      "v_mfma_f32_16x16x32_bf16 v[132:135], a[92:95], a[48:51], v[132:135]\n"
      "v_mfma_f32_16x16x32_bf16 v[140:143], a[92:95], a[52:55], v[140:143]\n"
      "v_mfma_f32_16x16x32_bf16 v[148:151], a[92:95], a[56:59], v[148:151]\n"
      "v_mfma_f32_16x16x32_bf16 v[156:159], a[92:95], a[60:63], v[156:159]\n"
      "s_cmp_eq_u32 s64, 0\n"
      "s_cbranch_scc0 NRET1_0_%=\n"
      "v_mov_b64 v[208:209], v[160:161]\n"
      "v_mov_b64 v[210:211], v[162:163]\n"
      "v_mov_b64 v[212:213], v[164:165]\n"
      "v_mov_b64 v[214:215], v[166:167]\n"
      "v_mov_b64 v[216:217], v[168:169]\n"
      "v_mov_b64 v[218:219], v[170:171]\n"
      "v_mov_b64 v[220:221], v[172:173]\n"
      "v_mov_b64 v[222:223], v[174:175]\n"
      "s_nop 1\n"
      "s_branch RET1_0_%=\n"
      "NRET1_0_%=:\n"
      "s_cmp_eq_u32 s64, 1\n"
      "s_cbranch_scc0 NRET1_1_%=\n"
      "v_mov_b64 v[208:209], v[160:161]\n"
      "v_mov_b64 v[210:211], v[162:163]\n"
      "v_mov_b64 v[212:213], v[164:165]\n"
      "v_mov_b64 v[214:215], v[166:167]\n"
      "v_mov_b64 v[216:217], v[168:169]\n"
      "v_mov_b64 v[218:219], v[170:171]\n"
      "v_mov_b64 v[220:221], v[172:173]\n"
      "v_mov_b64 v[222:223], v[174:175]\n"
      "s_nop 1\n"
      "s_branch RET1_1_%=\n"
      "NRET1_1_%=:\n"
      "s_cmp_eq_u32 s64, 2\n"
      "s_cbranch_scc0 NRET1_2_%=\n"
      "v_mov_b64 v[208:209], v[160:161]\n"
      "v_mov_b64 v[210:211], v[162:163]\n"
      "v_mov_b64 v[212:213], v[164:165]\n"
      "v_mov_b64 v[214:215], v[166:167]\n"
      "v_mov_b64 v[216:217], v[168:169]\n"
      "v_mov_b64 v[218:219], v[170:171]\n"
      "v_mov_b64 v[220:221], v[172:173]\n"
      "v_mov_b64 v[222:223], v[174:175]\n"
      "s_nop 1\n"
      "s_branch RET1_2_%=\n"
      "NRET1_2_%=:\n"
      "s_cmp_eq_u32 s64, 3\n"
      "s_cbranch_scc0 NRET1_3_%=\n"
      "v_mov_b64 v[208:209], v[160:161]\n"
      "v_mov_b64 v[210:211], v[162:163]\n"
      "v_mov_b64 v[212:213], v[164:165]\n"
      "v_mov_b64 v[214:215], v[166:167]\n"
      "v_mov_b64 v[216:217], v[168:169]\n"
      "v_mov_b64 v[218:219], v[170:171]\n"
      "v_mov_b64 v[220:221], v[172:173]\n"
      "v_mov_b64 v[222:223], v[174:175]\n"
      "s_nop 1\n"
      "s_branch RET1_3_%=\n"
      "NRET1_3_%=:\n"
      "s_cmp_eq_u32 s64, 4\n"
      "s_cbranch_scc0 NRET1_4_%=\n"
      "v_mov_b64 v[208:209], v[160:161]\n"
      "v_mov_b64 v[210:211], v[162:163]\n"
      "v_mov_b64 v[212:213], v[164:165]\n"
      "v_mov_b64 v[214:215], v[166:167]\n"
      "v_mov_b64 v[216:217], v[168:169]\n"
      "v_mov_b64 v[218:219], v[170:171]\n"
      "v_mov_b64 v[220:221], v[172:173]\n"
      "v_mov_b64 v[222:223], v[174:175]\n"
      "s_nop 1\n"
      "s_branch RET1_4_%=\n"
      "NRET1_4_%=:\n"
      "s_cmp_eq_u32 s64, 5\n"
      "s_cbranch_scc0 NRET1_5_%=\n"
      "v_mov_b64 v[0:1], v[160:161]\n"
      "v_mov_b64 v[2:3], v[162:163]\n"
      "v_mov_b64 v[4:5], v[164:165]\n"
      "v_mov_b64 v[6:7], v[166:167]\n"
      "v_mov_b64 v[8:9], v[168:169]\n"
      "v_mov_b64 v[10:11], v[170:171]\n"
      "v_mov_b64 v[12:13], v[172:173]\n"
      "v_mov_b64 v[14:15], v[174:175]\n"
      "s_nop 1\n"
      "s_branch RET1_5_%=\n"
      "NRET1_5_%=:\n"
      "s_cmp_eq_u32 s64, 6\n"
      "s_cbranch_scc0 NRET1_6_%=\n"
      "v_mov_b64 v[96:97], v[160:161]\n"
      "v_mov_b64 v[98:99], v[162:163]\n"
      "v_mov_b64 v[100:101], v[164:165]\n"
      "v_mov_b64 v[102:103], v[166:167]\n"
      "v_mov_b64 v[104:105], v[168:169]\n"
      "v_mov_b64 v[106:107], v[170:171]\n"
      "v_mov_b64 v[108:109], v[172:173]\n"
      "v_mov_b64 v[110:111], v[174:175]\n"
      "v_mov_b32 v117, v224\n"
      "s_nop 1\n"
      "v_mov_b32_dpp v117, v228 quad_perm:[0,1,2,3] row_mask:0x2 bank_mask:0xf\n"
      "s_nop 1\n"
      "v_mov_b32_dpp v117, v232 quad_perm:[0,1,2,3] row_mask:0x4 bank_mask:0xf\n"
      "s_nop 1\n"
      "v_mov_b32_dpp v117, v236 quad_perm:[0,1,2,3] row_mask:0x8 bank_mask:0xf\n"
      "v_mfma_f32_16x16x32_bf16 a[228:231], a[196:199], v[96:99], a[192:195]\n"
      "v_mfma_f32_16x16x32_bf16 a[228:231], a[200:203], v[100:103], a[228:231]\n"
      "v_mfma_f32_16x16x32_bf16 a[228:231], a[204:207], v[104:107], a[228:231]\n"
      "ds_write_b128 v125, a[160:163] offset:0\n"
      "ds_write_b128 v125, a[164:167] offset:1024\n"
      "ds_write_b128 v125, a[168:171] offset:2048\n"
      "ds_write_b128 v125, a[172:175] offset:3072\n"
      "ds_write_b128 v125, a[176:179] offset:4096\n"
      "ds_write_b128 v125, a[180:183] offset:5120\n"
      "ds_write_b128 v125, a[184:187] offset:6144\n"
      "ds_write_b128 v125, a[188:191] offset:7168\n"
      "ds_write_b128 v125, v[96:99] offset:8192\n"
      "ds_write_b128 v125, v[100:103] offset:9216\n"
      "ds_write_b128 v125, v[104:107] offset:10240\n"
      "ds_write_b32 v125, v117 offset:12292\n"
      "s_nop 1\n"
      "s_branch RET1_6_%=\n"
      "NRET1_6_%=:\n"
      "SCALE_%=:\n"
      "v_pk_mul_f32 v[32:33], v[32:33], v[112:113] op_sel:[0,0] op_sel_hi:[1,0]\n"
      "v_pk_mul_f32 v[34:35], v[34:35], v[112:113] op_sel:[0,0] op_sel_hi:[1,0]\n"
      "v_pk_mul_f32 v[36:37], v[36:37], v[112:113] op_sel:[0,1] op_sel_hi:[1,1]\n"
      "v_pk_mul_f32 v[38:39], v[38:39], v[112:113] op_sel:[0,1] op_sel_hi:[1,1]\n"
      "v_pk_mul_f32 v[40:41], v[40:41], v[114:115] op_sel:[0,0] op_sel_hi:[1,0]\n"
      "v_pk_mul_f32 v[42:43], v[42:43], v[114:115] op_sel:[0,0] op_sel_hi:[1,0]\n"
      "v_pk_mul_f32 v[44:45], v[44:45], v[114:115] op_sel:[0,1] op_sel_hi:[1,1]\n"
      "v_pk_mul_f32 v[46:47], v[46:47], v[114:115] op_sel:[0,1] op_sel_hi:[1,1]\n"
      "v_pk_mul_f32 v[48:49], v[48:49], v[112:113] op_sel:[0,0] op_sel_hi:[1,0]\n"
      "v_pk_mul_f32 v[50:51], v[50:51], v[112:113] op_sel:[0,0] op_sel_hi:[1,0]\n"
      "v_pk_mul_f32 v[52:53], v[52:53], v[112:113] op_sel:[0,1] op_sel_hi:[1,1]\n"
      "v_pk_mul_f32 v[54:55], v[54:55], v[112:113] op_sel:[0,1] op_sel_hi:[1,1]\n"
      "v_pk_mul_f32 v[56:57], v[56:57], v[114:115] op_sel:[0,0] op_sel_hi:[1,0]\n"
      "v_pk_mul_f32 v[58:59], v[58:59], v[114:115] op_sel:[0,0] op_sel_hi:[1,0]\n"
      "v_pk_mul_f32 v[60:61], v[60:61], v[114:115] op_sel:[0,1] op_sel_hi:[1,1]\n"
      "v_pk_mul_f32 v[62:63], v[62:63], v[114:115] op_sel:[0,1] op_sel_hi:[1,1]\n"
      "v_pk_mul_f32 v[64:65], v[64:65], v[112:113] op_sel:[0,0] op_sel_hi:[1,0]\n"
      "v_pk_mul_f32 v[66:67], v[66:67], v[112:113] op_sel:[0,0] op_sel_hi:[1,0]\n"
      "v_pk_mul_f32 v[68:69], v[68:69], v[112:113] op_sel:[0,1] op_sel_hi:[1,1]\n"
      "v_pk_mul_f32 v[70:71], v[70:71], v[112:113] op_sel:[0,1] op_sel_hi:[1,1]\n"
      "v_pk_mul_f32 v[72:73], v[72:73], v[114:115] op_sel:[0,0] op_sel_hi:[1,0]\n"
      "v_pk_mul_f32 v[74:75], v[74:75], v[114:115] op_sel:[0,0] op_sel_hi:[1,0]\n"
      "v_pk_mul_f32 v[76:77], v[76:77], v[114:115] op_sel:[0,1] op_sel_hi:[1,1]\n"
      "v_pk_mul_f32 v[78:79], v[78:79], v[114:115] op_sel:[0,1] op_sel_hi:[1,1]\n"
      "v_pk_mul_f32 v[80:81], v[80:81], v[112:113] op_sel:[0,0] op_sel_hi:[1,0]\n"
      "v_pk_mul_f32 v[82:83], v[82:83], v[112:113] op_sel:[0,0] op_sel_hi:[1,0]\n"
      "v_pk_mul_f32 v[84:85], v[84:85], v[112:113] op_sel:[0,1] op_sel_hi:[1,1]\n"
      "v_pk_mul_f32 v[86:87], v[86:87], v[112:113] op_sel:[0,1] op_sel_hi:[1,1]\n"
      "v_pk_mul_f32 v[88:89], v[88:89], v[114:115] op_sel:[0,0] op_sel_hi:[1,0]\n"
      "v_pk_mul_f32 v[90:91], v[90:91], v[114:115] op_sel:[0,0] op_sel_hi:[1,0]\n"
      "v_pk_mul_f32 v[92:93], v[92:93], v[114:115] op_sel:[0,1] op_sel_hi:[1,1]\n"
      "v_pk_mul_f32 v[94:95], v[94:95], v[114:115] op_sel:[0,1] op_sel_hi:[1,1]\n"
      "v_accvgpr_read_b32 v118, a128\n"
      "v_accvgpr_read_b32 v119, a129\n"
      "v_pk_mul_f32 v[118:119], v[118:119], v[112:113] op_sel:[0,0] op_sel_hi:[1,0]\n"
      "v_accvgpr_write_b32 a128, v118\n"
      "v_accvgpr_write_b32 a129, v119\n"
      "v_accvgpr_read_b32 v118, a130\n"
      "v_accvgpr_read_b32 v119, a131\n"
      "v_pk_mul_f32 v[118:119], v[118:119], v[112:113] op_sel:[0,0] op_sel_hi:[1,0]\n"
      "v_accvgpr_write_b32 a130, v118\n"
      "v_accvgpr_write_b32 a131, v119\n"
      "v_accvgpr_read_b32 v118, a132\n"
      "v_accvgpr_read_b32 v119, a133\n"
      "v_pk_mul_f32 v[118:119], v[118:119], v[112:113] op_sel:[0,1] op_sel_hi:[1,1]\n"
      "v_accvgpr_write_b32 a132, v118\n"
      "v_accvgpr_write_b32 a133, v119\n"
      "v_accvgpr_read_b32 v118, a134\n"
      "v_accvgpr_read_b32 v119, a135\n"
      "v_pk_mul_f32 v[118:119], v[118:119], v[112:113] op_sel:[0,1] op_sel_hi:[1,1]\n"
      "v_accvgpr_write_b32 a134, v118\n"
      "v_accvgpr_write_b32 a135, v119\n"
      "v_accvgpr_read_b32 v118, a136\n"
      "v_accvgpr_read_b32 v119, a137\n"
      "v_pk_mul_f32 v[118:119], v[118:119], v[114:115] op_sel:[0,0] op_sel_hi:[1,0]\n"
      "v_accvgpr_write_b32 a136, v118\n"
      "v_accvgpr_write_b32 a137, v119\n"
      "v_accvgpr_read_b32 v118, a138\n"
      "v_accvgpr_read_b32 v119, a139\n"
      "v_pk_mul_f32 v[118:119], v[118:119], v[114:115] op_sel:[0,0] op_sel_hi:[1,0]\n"
      "v_accvgpr_write_b32 a138, v118\n"
      "v_accvgpr_write_b32 a139, v119\n"
      "v_accvgpr_read_b32 v118, a140\n"
      "v_accvgpr_read_b32 v119, a141\n"
      "v_pk_mul_f32 v[118:119], v[118:119], v[114:115] op_sel:[0,1] op_sel_hi:[1,1]\n"
      "v_accvgpr_write_b32 a140, v118\n"
      "v_accvgpr_write_b32 a141, v119\n"
      "v_accvgpr_read_b32 v118, a142\n"
      "v_accvgpr_read_b32 v119, a143\n"
      "v_pk_mul_f32 v[118:119], v[118:119], v[114:115] op_sel:[0,1] op_sel_hi:[1,1]\n"
      "v_accvgpr_write_b32 a142, v118\n"
      "v_accvgpr_write_b32 a143, v119\n"
      "v_accvgpr_read_b32 v118, a144\n"
      "v_accvgpr_read_b32 v119, a145\n"
      "v_pk_mul_f32 v[118:119], v[118:119], v[112:113] op_sel:[0,0] op_sel_hi:[1,0]\n"
      "v_accvgpr_write_b32 a144, v118\n"
      "v_accvgpr_write_b32 a145, v119\n"
      "v_accvgpr_read_b32 v118, a146\n"
      "v_accvgpr_read_b32 v119, a147\n"
      "v_pk_mul_f32 v[118:119], v[118:119], v[112:113] op_sel:[0,0] op_sel_hi:[1,0]\n"
      "v_accvgpr_write_b32 a146, v118\n"
      "v_accvgpr_write_b32 a147, v119\n"
      "v_accvgpr_read_b32 v118, a148\n"
      "v_accvgpr_read_b32 v119, a149\n"
      "v_pk_mul_f32 v[118:119], v[118:119], v[112:113] op_sel:[0,1] op_sel_hi:[1,1]\n"
      "v_accvgpr_write_b32 a148, v118\n"
      "v_accvgpr_write_b32 a149, v119\n"
      "v_accvgpr_read_b32 v118, a150\n"
      "v_accvgpr_read_b32 v119, a151\n"
      "v_pk_mul_f32 v[118:119], v[118:119], v[112:113] op_sel:[0,1] op_sel_hi:[1,1]\n"
      "v_accvgpr_write_b32 a150, v118\n"
      "v_accvgpr_write_b32 a151, v119\n"
      "v_accvgpr_read_b32 v118, a152\n"
      "v_accvgpr_read_b32 v119, a153\n"
      "v_pk_mul_f32 v[118:119], v[118:119], v[114:115] op_sel:[0,0] op_sel_hi:[1,0]\n"
      "v_accvgpr_write_b32 a152, v118\n"
      "v_accvgpr_write_b32 a153, v119\n"
      "v_accvgpr_read_b32 v118, a154\n"
      "v_accvgpr_read_b32 v119, a155\n"
      "v_pk_mul_f32 v[118:119], v[118:119], v[114:115] op_sel:[0,0] op_sel_hi:[1,0]\n"
      "v_accvgpr_write_b32 a154, v118\n"
      "v_accvgpr_write_b32 a155, v119\n"
      "v_accvgpr_read_b32 v118, a156\n"
      "v_accvgpr_read_b32 v119, a157\n"
      "v_pk_mul_f32 v[118:119], v[118:119], v[114:115] op_sel:[0,1] op_sel_hi:[1,1]\n"
      "v_accvgpr_write_b32 a156, v118\n"
      "v_accvgpr_write_b32 a157, v119\n"
      "v_accvgpr_read_b32 v118, a158\n"
      "v_accvgpr_read_b32 v119, a159\n"
      "v_pk_mul_f32 v[118:119], v[118:119], v[114:115] op_sel:[0,1] op_sel_hi:[1,1]\n"
      "v_accvgpr_write_b32 a158, v118\n"
      "v_accvgpr_write_b32 a159, v119\n"
      "v_accvgpr_read_b32 v118, a160\n"
      "v_accvgpr_read_b32 v119, a161\n"
      "v_pk_mul_f32 v[118:119], v[118:119], v[112:113] op_sel:[0,0] op_sel_hi:[1,0]\n"
      "v_accvgpr_write_b32 a160, v118\n"
      "v_accvgpr_write_b32 a161, v119\n"
      "v_accvgpr_read_b32 v118, a162\n"
      "v_accvgpr_read_b32 v119, a163\n"
      "v_pk_mul_f32 v[118:119], v[118:119], v[112:113] op_sel:[0,0] op_sel_hi:[1,0]\n"
      "v_accvgpr_write_b32 a162, v118\n"
      "v_accvgpr_write_b32 a163, v119\n"
      "v_accvgpr_read_b32 v118, a164\n"
      "v_accvgpr_read_b32 v119, a165\n"
      "v_pk_mul_f32 v[118:119], v[118:119], v[112:113] op_sel:[0,1] op_sel_hi:[1,1]\n"
      "v_accvgpr_write_b32 a164, v118\n"
      "v_accvgpr_write_b32 a165, v119\n"
      "v_accvgpr_read_b32 v118, a166\n"
      "v_accvgpr_read_b32 v119, a167\n"
      "v_pk_mul_f32 v[118:119], v[118:119], v[112:113] op_sel:[0,1] op_sel_hi:[1,1]\n"
      "v_accvgpr_write_b32 a166, v118\n"
      "v_accvgpr_write_b32 a167, v119\n"
      "v_accvgpr_read_b32 v118, a168\n"
      "v_accvgpr_read_b32 v119, a169\n"
      "v_pk_mul_f32 v[118:119], v[118:119], v[114:115] op_sel:[0,0] op_sel_hi:[1,0]\n"
      "v_accvgpr_write_b32 a168, v118\n"
      "v_accvgpr_write_b32 a169, v119\n"
      "v_accvgpr_read_b32 v118, a170\n"
      "v_accvgpr_read_b32 v119, a171\n"
      "v_pk_mul_f32 v[118:119], v[118:119], v[114:115] op_sel:[0,0] op_sel_hi:[1,0]\n"
      "v_accvgpr_write_b32 a170, v118\n"
      "v_accvgpr_write_b32 a171, v119\n"
      "v_accvgpr_read_b32 v118, a172\n"
      "v_accvgpr_read_b32 v119, a173\n"
      "v_pk_mul_f32 v[118:119], v[118:119], v[114:115] op_sel:[0,1] op_sel_hi:[1,1]\n"
      "v_accvgpr_write_b32 a172, v118\n"
      "v_accvgpr_write_b32 a173, v119\n"
      "v_accvgpr_read_b32 v118, a174\n"
      "v_accvgpr_read_b32 v119, a175\n"
      "v_pk_mul_f32 v[118:119], v[118:119], v[114:115] op_sel:[0,1] op_sel_hi:[1,1]\n"
      "v_accvgpr_write_b32 a174, v118\n"
      "v_accvgpr_write_b32 a175, v119\n"
      "v_accvgpr_read_b32 v118, a176\n"
      "v_accvgpr_read_b32 v119, a177\n"
      "v_pk_mul_f32 v[118:119], v[118:119], v[112:113] op_sel:[0,0] op_sel_hi:[1,0]\n"
      "v_accvgpr_write_b32 a176, v118\n"
      "v_accvgpr_write_b32 a177, v119\n"
      "v_accvgpr_read_b32 v118, a178\n"
      "v_accvgpr_read_b32 v119, a179\n"
      "v_pk_mul_f32 v[118:119], v[118:119], v[112:113] op_sel:[0,0] op_sel_hi:[1,0]\n"
      "v_accvgpr_write_b32 a178, v118\n"
      "v_accvgpr_write_b32 a179, v119\n"
      "v_accvgpr_read_b32 v118, a180\n"
      "v_accvgpr_read_b32 v119, a181\n"
      "v_pk_mul_f32 v[118:119], v[118:119], v[112:113] op_sel:[0,1] op_sel_hi:[1,1]\n"
      "v_accvgpr_write_b32 a180, v118\n"
      "v_accvgpr_write_b32 a181, v119\n"
      "v_accvgpr_read_b32 v118, a182\n"
      "v_accvgpr_read_b32 v119, a183\n"
      "v_pk_mul_f32 v[118:119], v[118:119], v[112:113] op_sel:[0,1] op_sel_hi:[1,1]\n"
      "v_accvgpr_write_b32 a182, v118\n"
      "v_accvgpr_write_b32 a183, v119\n"
      "v_accvgpr_read_b32 v118, a184\n"
      "v_accvgpr_read_b32 v119, a185\n"
      "v_pk_mul_f32 v[118:119], v[118:119], v[114:115] op_sel:[0,0] op_sel_hi:[1,0]\n"
      "v_accvgpr_write_b32 a184, v118\n"
      "v_accvgpr_write_b32 a185, v119\n"
      "v_accvgpr_read_b32 v118, a186\n"
      "v_accvgpr_read_b32 v119, a187\n"
      "v_pk_mul_f32 v[118:119], v[118:119], v[114:115] op_sel:[0,0] op_sel_hi:[1,0]\n"
      "v_accvgpr_write_b32 a186, v118\n"
      "v_accvgpr_write_b32 a187, v119\n"
      "v_accvgpr_read_b32 v118, a188\n"
      "v_accvgpr_read_b32 v119, a189\n"
      "v_pk_mul_f32 v[118:119], v[118:119], v[114:115] op_sel:[0,1] op_sel_hi:[1,1]\n"
      "v_accvgpr_write_b32 a188, v118\n"
      "v_accvgpr_write_b32 a189, v119\n"
      "v_accvgpr_read_b32 v118, a190\n"
      "v_accvgpr_read_b32 v119, a191\n"
      "v_pk_mul_f32 v[118:119], v[118:119], v[114:115] op_sel:[0,1] op_sel_hi:[1,1]\n"
      "v_accvgpr_write_b32 a190, v118\n"
      "v_accvgpr_write_b32 a191, v119\n"
      "v_mov_b32 v118, v112\n"
      "s_nop 1\n"
      "v_mov_b32_dpp v118, v113 quad_perm:[0,1,2,3] row_mask:0x2 bank_mask:0xf\n"
      "s_nop 1\n"
      "v_mov_b32_dpp v118, v114 quad_perm:[0,1,2,3] row_mask:0x4 bank_mask:0xf\n"
      "s_nop 1\n"
      "v_mov_b32_dpp v118, v115 quad_perm:[0,1,2,3] row_mask:0x8 bank_mask:0xf\n"
      "v_accvgpr_read_b32 v119, a192\n"
      "v_mul_f32 v118, v118, v119\n"
      "v_accvgpr_write_b32 a192, v118\n"
      "v_lshlrev_b32 v118, 16, v208\n"
      "v_and_b32 v119, 0xffff0000, v208\n"
      "v_pk_mul_f32 v[118:119], v[118:119], v[112:113] op_sel:[0,0] op_sel_hi:[1,0]\n"
      "v_cvt_pk_bf16_f32 v208, v118, v119\n"
      "v_lshlrev_b32 v118, 16, v209\n"
      "v_and_b32 v119, 0xffff0000, v209\n"
      "v_pk_mul_f32 v[118:119], v[118:119], v[112:113] op_sel:[0,0] op_sel_hi:[1,0]\n"
      "v_cvt_pk_bf16_f32 v209, v118, v119\n"
      "v_lshlrev_b32 v118, 16, v210\n"
      "v_and_b32 v119, 0xffff0000, v210\n"
      "v_pk_mul_f32 v[118:119], v[118:119], v[112:113] op_sel:[0,0] op_sel_hi:[1,0]\n"
      "v_cvt_pk_bf16_f32 v210, v118, v119\n"
      "v_lshlrev_b32 v118, 16, v211\n"
      "v_and_b32 v119, 0xffff0000, v211\n"
      "v_pk_mul_f32 v[118:119], v[118:119], v[112:113] op_sel:[0,0] op_sel_hi:[1,0]\n"
      "v_cvt_pk_bf16_f32 v211, v118, v119\n"
      "v_lshlrev_b32 v118, 16, v212\n"
      "v_and_b32 v119, 0xffff0000, v212\n"
      "v_pk_mul_f32 v[118:119], v[118:119], v[112:113] op_sel:[0,1] op_sel_hi:[1,1]\n"
      "v_cvt_pk_bf16_f32 v212, v118, v119\n"
      "v_lshlrev_b32 v118, 16, v213\n"
      "v_and_b32 v119, 0xffff0000, v213\n"
      "v_pk_mul_f32 v[118:119], v[118:119], v[112:113] op_sel:[0,1] op_sel_hi:[1,1]\n"
      "v_cvt_pk_bf16_f32 v213, v118, v119\n"
      "v_lshlrev_b32 v118, 16, v214\n"
      "v_and_b32 v119, 0xffff0000, v214\n"
      "v_pk_mul_f32 v[118:119], v[118:119], v[112:113] op_sel:[0,1] op_sel_hi:[1,1]\n"
      "v_cvt_pk_bf16_f32 v214, v118, v119\n"
      "v_lshlrev_b32 v118, 16, v215\n"
      "v_and_b32 v119, 0xffff0000, v215\n"
      "v_pk_mul_f32 v[118:119], v[118:119], v[112:113] op_sel:[0,1] op_sel_hi:[1,1]\n"
      "v_cvt_pk_bf16_f32 v215, v118, v119\n"
      "v_lshlrev_b32 v118, 16, v216\n"
      "v_and_b32 v119, 0xffff0000, v216\n"
      "v_pk_mul_f32 v[118:119], v[118:119], v[114:115] op_sel:[0,0] op_sel_hi:[1,0]\n"
      "v_cvt_pk_bf16_f32 v216, v118, v119\n"
      "v_lshlrev_b32 v118, 16, v217\n"
      "v_and_b32 v119, 0xffff0000, v217\n"
      "v_pk_mul_f32 v[118:119], v[118:119], v[114:115] op_sel:[0,0] op_sel_hi:[1,0]\n"
      "v_cvt_pk_bf16_f32 v217, v118, v119\n"
      "v_lshlrev_b32 v118, 16, v218\n"
      "v_and_b32 v119, 0xffff0000, v218\n"
      "v_pk_mul_f32 v[118:119], v[118:119], v[114:115] op_sel:[0,0] op_sel_hi:[1,0]\n"
      "v_cvt_pk_bf16_f32 v218, v118, v119\n"
      "v_lshlrev_b32 v118, 16, v219\n"
      "v_and_b32 v119, 0xffff0000, v219\n"
      "v_pk_mul_f32 v[118:119], v[118:119], v[114:115] op_sel:[0,0] op_sel_hi:[1,0]\n"
      "v_cvt_pk_bf16_f32 v219, v118, v119\n"
      "v_lshlrev_b32 v118, 16, v220\n"
      "v_and_b32 v119, 0xffff0000, v220\n"
      "v_pk_mul_f32 v[118:119], v[118:119], v[114:115] op_sel:[0,1] op_sel_hi:[1,1]\n"
      "v_cvt_pk_bf16_f32 v220, v118, v119\n"
      "v_lshlrev_b32 v118, 16, v221\n"
      "v_and_b32 v119, 0xffff0000, v221\n"
      "v_pk_mul_f32 v[118:119], v[118:119], v[114:115] op_sel:[0,1] op_sel_hi:[1,1]\n"
      "v_cvt_pk_bf16_f32 v221, v118, v119\n"
      "v_lshlrev_b32 v118, 16, v222\n"
      "v_and_b32 v119, 0xffff0000, v222\n"
      "v_pk_mul_f32 v[118:119], v[118:119], v[114:115] op_sel:[0,1] op_sel_hi:[1,1]\n"
      "v_cvt_pk_bf16_f32 v222, v118, v119\n"
      "v_lshlrev_b32 v118, 16, v223\n"
      "v_and_b32 v119, 0xffff0000, v223\n"
      "v_pk_mul_f32 v[118:119], v[118:119], v[114:115] op_sel:[0,1] op_sel_hi:[1,1]\n"
      "v_cvt_pk_bf16_f32 v223, v118, v119\n"
      "v_lshlrev_b32 v118, 16, v192\n"
      "v_and_b32 v119, 0xffff0000, v192\n"
      "v_pk_mul_f32 v[118:119], v[118:119], v[112:113] op_sel:[0,0] op_sel_hi:[1,0]\n"
      "v_cvt_pk_bf16_f32 v192, v118, v119\n"
      "v_lshlrev_b32 v118, 16, v193\n"
      "v_and_b32 v119, 0xffff0000, v193\n"
      "v_pk_mul_f32 v[118:119], v[118:119], v[112:113] op_sel:[0,0] op_sel_hi:[1,0]\n"
      "v_cvt_pk_bf16_f32 v193, v118, v119\n"
      "v_lshlrev_b32 v118, 16, v194\n"
      "v_and_b32 v119, 0xffff0000, v194\n"
      "v_pk_mul_f32 v[118:119], v[118:119], v[112:113] op_sel:[0,0] op_sel_hi:[1,0]\n"
      "v_cvt_pk_bf16_f32 v194, v118, v119\n"
      "v_lshlrev_b32 v118, 16, v195\n"
      "v_and_b32 v119, 0xffff0000, v195\n"
      "v_pk_mul_f32 v[118:119], v[118:119], v[112:113] op_sel:[0,0] op_sel_hi:[1,0]\n"
      "v_cvt_pk_bf16_f32 v195, v118, v119\n"
      "v_lshlrev_b32 v118, 16, v196\n"
      "v_and_b32 v119, 0xffff0000, v196\n"
      "v_pk_mul_f32 v[118:119], v[118:119], v[112:113] op_sel:[0,1] op_sel_hi:[1,1]\n"
      "v_cvt_pk_bf16_f32 v196, v118, v119\n"
      "v_lshlrev_b32 v118, 16, v197\n"
      "v_and_b32 v119, 0xffff0000, v197\n"
      "v_pk_mul_f32 v[118:119], v[118:119], v[112:113] op_sel:[0,1] op_sel_hi:[1,1]\n"
      "v_cvt_pk_bf16_f32 v197, v118, v119\n"
      "v_lshlrev_b32 v118, 16, v198\n"
      "v_and_b32 v119, 0xffff0000, v198\n"
      "v_pk_mul_f32 v[118:119], v[118:119], v[112:113] op_sel:[0,1] op_sel_hi:[1,1]\n"
      "v_cvt_pk_bf16_f32 v198, v118, v119\n"
      "v_lshlrev_b32 v118, 16, v199\n"
      "v_and_b32 v119, 0xffff0000, v199\n"
      "v_pk_mul_f32 v[118:119], v[118:119], v[112:113] op_sel:[0,1] op_sel_hi:[1,1]\n"
      "v_cvt_pk_bf16_f32 v199, v118, v119\n"
      "v_lshlrev_b32 v118, 16, v200\n"
      "v_and_b32 v119, 0xffff0000, v200\n"
      "v_pk_mul_f32 v[118:119], v[118:119], v[114:115] op_sel:[0,0] op_sel_hi:[1,0]\n"
      "v_cvt_pk_bf16_f32 v200, v118, v119\n"
      "v_lshlrev_b32 v118, 16, v201\n"
      "v_and_b32 v119, 0xffff0000, v201\n"
      "v_pk_mul_f32 v[118:119], v[118:119], v[114:115] op_sel:[0,0] op_sel_hi:[1,0]\n"
      "v_cvt_pk_bf16_f32 v201, v118, v119\n"
      "v_lshlrev_b32 v118, 16, v202\n"
      "v_and_b32 v119, 0xffff0000, v202\n"
      "v_pk_mul_f32 v[118:119], v[118:119], v[114:115] op_sel:[0,0] op_sel_hi:[1,0]\n"
      "v_cvt_pk_bf16_f32 v202, v118, v119\n"
      "v_lshlrev_b32 v118, 16, v203\n"
      "v_and_b32 v119, 0xffff0000, v203\n"
      "v_pk_mul_f32 v[118:119], v[118:119], v[114:115] op_sel:[0,0] op_sel_hi:[1,0]\n"
      "v_cvt_pk_bf16_f32 v203, v118, v119\n"
      "v_lshlrev_b32 v118, 16, v204\n"
      "v_and_b32 v119, 0xffff0000, v204\n"
      "v_pk_mul_f32 v[118:119], v[118:119], v[114:115] op_sel:[0,1] op_sel_hi:[1,1]\n"
      "v_cvt_pk_bf16_f32 v204, v118, v119\n"
      "v_lshlrev_b32 v118, 16, v205\n"
      "v_and_b32 v119, 0xffff0000, v205\n"
      "v_pk_mul_f32 v[118:119], v[118:119], v[114:115] op_sel:[0,1] op_sel_hi:[1,1]\n"
      "v_cvt_pk_bf16_f32 v205, v118, v119\n"
      "v_lshlrev_b32 v118, 16, v206\n"
      "v_and_b32 v119, 0xffff0000, v206\n"
      "v_pk_mul_f32 v[118:119], v[118:119], v[114:115] op_sel:[0,1] op_sel_hi:[1,1]\n"
      "v_cvt_pk_bf16_f32 v206, v118, v119\n"
      "v_lshlrev_b32 v118, 16, v207\n"
      "v_and_b32 v119, 0xffff0000, v207\n"
      "v_pk_mul_f32 v[118:119], v[118:119], v[114:115] op_sel:[0,1] op_sel_hi:[1,1]\n"
      "v_cvt_pk_bf16_f32 v207, v118, v119\n"
      "v_lshlrev_b32 v118, 16, v0\n"
      "v_and_b32 v119, 0xffff0000, v0\n"
      "v_pk_mul_f32 v[118:119], v[118:119], v[112:113] op_sel:[0,0] op_sel_hi:[1,0]\n"
      "v_cvt_pk_bf16_f32 v0, v118, v119\n"
      "v_lshlrev_b32 v118, 16, v1\n"
      "v_and_b32 v119, 0xffff0000, v1\n"
      "v_pk_mul_f32 v[118:119], v[118:119], v[112:113] op_sel:[0,0] op_sel_hi:[1,0]\n"
      "v_cvt_pk_bf16_f32 v1, v118, v119\n"
      "v_lshlrev_b32 v118, 16, v2\n"
      "v_and_b32 v119, 0xffff0000, v2\n"
      "v_pk_mul_f32 v[118:119], v[118:119], v[112:113] op_sel:[0,0] op_sel_hi:[1,0]\n"
      "v_cvt_pk_bf16_f32 v2, v118, v119\n"
      "v_lshlrev_b32 v118, 16, v3\n"
      "v_and_b32 v119, 0xffff0000, v3\n"
      "v_pk_mul_f32 v[118:119], v[118:119], v[112:113] op_sel:[0,0] op_sel_hi:[1,0]\n"
      "v_cvt_pk_bf16_f32 v3, v118, v119\n"
      "v_lshlrev_b32 v118, 16, v4\n"
      "v_and_b32 v119, 0xffff0000, v4\n"
      "v_pk_mul_f32 v[118:119], v[118:119], v[112:113] op_sel:[0,1] op_sel_hi:[1,1]\n"
      "v_cvt_pk_bf16_f32 v4, v118, v119\n"
      "v_lshlrev_b32 v118, 16, v5\n"
      "v_and_b32 v119, 0xffff0000, v5\n"
      "v_pk_mul_f32 v[118:119], v[118:119], v[112:113] op_sel:[0,1] op_sel_hi:[1,1]\n"
      "v_cvt_pk_bf16_f32 v5, v118, v119\n"
      "v_lshlrev_b32 v118, 16, v6\n"
      "v_and_b32 v119, 0xffff0000, v6\n"
      "v_pk_mul_f32 v[118:119], v[118:119], v[112:113] op_sel:[0,1] op_sel_hi:[1,1]\n"
      "v_cvt_pk_bf16_f32 v6, v118, v119\n"
      "v_lshlrev_b32 v118, 16, v7\n"
      "v_and_b32 v119, 0xffff0000, v7\n"
      "v_pk_mul_f32 v[118:119], v[118:119], v[112:113] op_sel:[0,1] op_sel_hi:[1,1]\n"
      "v_cvt_pk_bf16_f32 v7, v118, v119\n"
      "v_lshlrev_b32 v118, 16, v8\n"
      "v_and_b32 v119, 0xffff0000, v8\n"
      "v_pk_mul_f32 v[118:119], v[118:119], v[114:115] op_sel:[0,0] op_sel_hi:[1,0]\n"
      "v_cvt_pk_bf16_f32 v8, v118, v119\n"
      "v_lshlrev_b32 v118, 16, v9\n"
      "v_and_b32 v119, 0xffff0000, v9\n"
      "v_pk_mul_f32 v[118:119], v[118:119], v[114:115] op_sel:[0,0] op_sel_hi:[1,0]\n"
      "v_cvt_pk_bf16_f32 v9, v118, v119\n"
      "v_lshlrev_b32 v118, 16, v10\n"
      "v_and_b32 v119, 0xffff0000, v10\n"
      "v_pk_mul_f32 v[118:119], v[118:119], v[114:115] op_sel:[0,0] op_sel_hi:[1,0]\n"
      "v_cvt_pk_bf16_f32 v10, v118, v119\n"
      "v_lshlrev_b32 v118, 16, v11\n"
      "v_and_b32 v119, 0xffff0000, v11\n"
      "v_pk_mul_f32 v[118:119], v[118:119], v[114:115] op_sel:[0,0] op_sel_hi:[1,0]\n"
      "v_cvt_pk_bf16_f32 v11, v118, v119\n"
      "v_lshlrev_b32 v118, 16, v12\n"
      "v_and_b32 v119, 0xffff0000, v12\n"
      "v_pk_mul_f32 v[118:119], v[118:119], v[114:115] op_sel:[0,1] op_sel_hi:[1,1]\n"
      "v_cvt_pk_bf16_f32 v12, v118, v119\n"
      "v_lshlrev_b32 v118, 16, v13\n"
      "v_and_b32 v119, 0xffff0000, v13\n"
      "v_pk_mul_f32 v[118:119], v[118:119], v[114:115] op_sel:[0,1] op_sel_hi:[1,1]\n"
      "v_cvt_pk_bf16_f32 v13, v118, v119\n"
      "v_lshlrev_b32 v118, 16, v14\n"
      "v_and_b32 v119, 0xffff0000, v14\n"
      "v_pk_mul_f32 v[118:119], v[118:119], v[114:115] op_sel:[0,1] op_sel_hi:[1,1]\n"
      "v_cvt_pk_bf16_f32 v14, v118, v119\n"
      "v_lshlrev_b32 v118, 16, v15\n"
      "v_and_b32 v119, 0xffff0000, v15\n"
      "v_pk_mul_f32 v[118:119], v[118:119], v[114:115] op_sel:[0,1] op_sel_hi:[1,1]\n"
      "v_cvt_pk_bf16_f32 v15, v118, v119\n"
      "v_lshlrev_b32 v118, 16, v16\n"
      "v_and_b32 v119, 0xffff0000, v16\n"
      "v_pk_mul_f32 v[118:119], v[118:119], v[112:113] op_sel:[0,0] op_sel_hi:[1,0]\n"
      "v_cvt_pk_bf16_f32 v16, v118, v119\n"
      "v_lshlrev_b32 v118, 16, v17\n"
      "v_and_b32 v119, 0xffff0000, v17\n"
      "v_pk_mul_f32 v[118:119], v[118:119], v[112:113] op_sel:[0,0] op_sel_hi:[1,0]\n"
      "v_cvt_pk_bf16_f32 v17, v118, v119\n"
      "v_lshlrev_b32 v118, 16, v18\n"
      "v_and_b32 v119, 0xffff0000, v18\n"
      "v_pk_mul_f32 v[118:119], v[118:119], v[112:113] op_sel:[0,0] op_sel_hi:[1,0]\n"
      "v_cvt_pk_bf16_f32 v18, v118, v119\n"
      "v_lshlrev_b32 v118, 16, v19\n"
      "v_and_b32 v119, 0xffff0000, v19\n"
      "v_pk_mul_f32 v[118:119], v[118:119], v[112:113] op_sel:[0,0] op_sel_hi:[1,0]\n"
      "v_cvt_pk_bf16_f32 v19, v118, v119\n"
      "v_lshlrev_b32 v118, 16, v20\n"
      "v_and_b32 v119, 0xffff0000, v20\n"
      "v_pk_mul_f32 v[118:119], v[118:119], v[112:113] op_sel:[0,1] op_sel_hi:[1,1]\n"
      "v_cvt_pk_bf16_f32 v20, v118, v119\n"
      "v_lshlrev_b32 v118, 16, v21\n"
      "v_and_b32 v119, 0xffff0000, v21\n"
      "v_pk_mul_f32 v[118:119], v[118:119], v[112:113] op_sel:[0,1] op_sel_hi:[1,1]\n"
      "v_cvt_pk_bf16_f32 v21, v118, v119\n"
      "v_lshlrev_b32 v118, 16, v22\n"
      "v_and_b32 v119, 0xffff0000, v22\n"
      "v_pk_mul_f32 v[118:119], v[118:119], v[112:113] op_sel:[0,1] op_sel_hi:[1,1]\n"
      "v_cvt_pk_bf16_f32 v22, v118, v119\n"
      "v_lshlrev_b32 v118, 16, v23\n"
      "v_and_b32 v119, 0xffff0000, v23\n"
      "v_pk_mul_f32 v[118:119], v[118:119], v[112:113] op_sel:[0,1] op_sel_hi:[1,1]\n"
      "v_cvt_pk_bf16_f32 v23, v118, v119\n"
      "v_lshlrev_b32 v118, 16, v24\n"
      "v_and_b32 v119, 0xffff0000, v24\n"
      "v_pk_mul_f32 v[118:119], v[118:119], v[114:115] op_sel:[0,0] op_sel_hi:[1,0]\n"
      "v_cvt_pk_bf16_f32 v24, v118, v119\n"
      "v_lshlrev_b32 v118, 16, v25\n"
      "v_and_b32 v119, 0xffff0000, v25\n"
      "v_pk_mul_f32 v[118:119], v[118:119], v[114:115] op_sel:[0,0] op_sel_hi:[1,0]\n"
      "v_cvt_pk_bf16_f32 v25, v118, v119\n"
      "v_lshlrev_b32 v118, 16, v26\n"
      "v_and_b32 v119, 0xffff0000, v26\n"
      "v_pk_mul_f32 v[118:119], v[118:119], v[114:115] op_sel:[0,0] op_sel_hi:[1,0]\n"
      "v_cvt_pk_bf16_f32 v26, v118, v119\n"
      "v_lshlrev_b32 v118, 16, v27\n"
      "v_and_b32 v119, 0xffff0000, v27\n"
      "v_pk_mul_f32 v[118:119], v[118:119], v[114:115] op_sel:[0,0] op_sel_hi:[1,0]\n"
      "v_cvt_pk_bf16_f32 v27, v118, v119\n"
      "v_lshlrev_b32 v118, 16, v28\n"
      "v_and_b32 v119, 0xffff0000, v28\n"
      "v_pk_mul_f32 v[118:119], v[118:119], v[114:115] op_sel:[0,1] op_sel_hi:[1,1]\n"
      "v_cvt_pk_bf16_f32 v28, v118, v119\n"
      "v_lshlrev_b32 v118, 16, v29\n"
      "v_and_b32 v119, 0xffff0000, v29\n"
      "v_pk_mul_f32 v[118:119], v[118:119], v[114:115] op_sel:[0,1] op_sel_hi:[1,1]\n"
      "v_cvt_pk_bf16_f32 v29, v118, v119\n"
      "v_lshlrev_b32 v118, 16, v30\n"
      "v_and_b32 v119, 0xffff0000, v30\n"
      "v_pk_mul_f32 v[118:119], v[118:119], v[114:115] op_sel:[0,1] op_sel_hi:[1,1]\n"
      "v_cvt_pk_bf16_f32 v30, v118, v119\n"
      "v_lshlrev_b32 v118, 16, v31\n"
      "v_and_b32 v119, 0xffff0000, v31\n"
      "v_pk_mul_f32 v[118:119], v[118:119], v[114:115] op_sel:[0,1] op_sel_hi:[1,1]\n"
      "v_cvt_pk_bf16_f32 v31, v118, v119\n"
      "v_lshlrev_b32 v118, 16, v96\n"
      "v_and_b32 v119, 0xffff0000, v96\n"
      "v_pk_mul_f32 v[118:119], v[118:119], v[112:113] op_sel:[0,0] op_sel_hi:[1,0]\n"
      "v_cvt_pk_bf16_f32 v96, v118, v119\n"
      "v_lshlrev_b32 v118, 16, v97\n"
      "v_and_b32 v119, 0xffff0000, v97\n"
      "v_pk_mul_f32 v[118:119], v[118:119], v[112:113] op_sel:[0,0] op_sel_hi:[1,0]\n"
      "v_cvt_pk_bf16_f32 v97, v118, v119\n"
      "v_lshlrev_b32 v118, 16, v98\n"
      "v_and_b32 v119, 0xffff0000, v98\n"
      "v_pk_mul_f32 v[118:119], v[118:119], v[112:113] op_sel:[0,0] op_sel_hi:[1,0]\n"
      "v_cvt_pk_bf16_f32 v98, v118, v119\n"
      "v_lshlrev_b32 v118, 16, v99\n"
      "v_and_b32 v119, 0xffff0000, v99\n"
      "v_pk_mul_f32 v[118:119], v[118:119], v[112:113] op_sel:[0,0] op_sel_hi:[1,0]\n"
      "v_cvt_pk_bf16_f32 v99, v118, v119\n"
      "v_lshlrev_b32 v118, 16, v100\n"
      "v_and_b32 v119, 0xffff0000, v100\n"
      "v_pk_mul_f32 v[118:119], v[118:119], v[112:113] op_sel:[0,1] op_sel_hi:[1,1]\n"
      "v_cvt_pk_bf16_f32 v100, v118, v119\n"
      "v_lshlrev_b32 v118, 16, v101\n"
      "v_and_b32 v119, 0xffff0000, v101\n"
      "v_pk_mul_f32 v[118:119], v[118:119], v[112:113] op_sel:[0,1] op_sel_hi:[1,1]\n"
      "v_cvt_pk_bf16_f32 v101, v118, v119\n"
      "v_lshlrev_b32 v118, 16, v102\n"
      "v_and_b32 v119, 0xffff0000, v102\n"
      "v_pk_mul_f32 v[118:119], v[118:119], v[112:113] op_sel:[0,1] op_sel_hi:[1,1]\n"
      "v_cvt_pk_bf16_f32 v102, v118, v119\n"
      "v_lshlrev_b32 v118, 16, v103\n"
      "v_and_b32 v119, 0xffff0000, v103\n"
      "v_pk_mul_f32 v[118:119], v[118:119], v[112:113] op_sel:[0,1] op_sel_hi:[1,1]\n"
      "v_cvt_pk_bf16_f32 v103, v118, v119\n"
      "v_lshlrev_b32 v118, 16, v104\n"
      "v_and_b32 v119, 0xffff0000, v104\n"
      "v_pk_mul_f32 v[118:119], v[118:119], v[114:115] op_sel:[0,0] op_sel_hi:[1,0]\n"
      "v_cvt_pk_bf16_f32 v104, v118, v119\n"
      "v_lshlrev_b32 v118, 16, v105\n"
      "v_and_b32 v119, 0xffff0000, v105\n"
      "v_pk_mul_f32 v[118:119], v[118:119], v[114:115] op_sel:[0,0] op_sel_hi:[1,0]\n"
      "v_cvt_pk_bf16_f32 v105, v118, v119\n"
      "v_lshlrev_b32 v118, 16, v106\n"
      "v_and_b32 v119, 0xffff0000, v106\n"
      "v_pk_mul_f32 v[118:119], v[118:119], v[114:115] op_sel:[0,0] op_sel_hi:[1,0]\n"
      "v_cvt_pk_bf16_f32 v106, v118, v119\n"
      "v_lshlrev_b32 v118, 16, v107\n"
      "v_and_b32 v119, 0xffff0000, v107\n"
      "v_pk_mul_f32 v[118:119], v[118:119], v[114:115] op_sel:[0,0] op_sel_hi:[1,0]\n"
      "v_cvt_pk_bf16_f32 v107, v118, v119\n"
      "v_lshlrev_b32 v118, 16, v108\n"
      "v_and_b32 v119, 0xffff0000, v108\n"
      "v_pk_mul_f32 v[118:119], v[118:119], v[114:115] op_sel:[0,1] op_sel_hi:[1,1]\n"
      "v_cvt_pk_bf16_f32 v108, v118, v119\n"
      "v_lshlrev_b32 v118, 16, v109\n"
      "v_and_b32 v119, 0xffff0000, v109\n"
      "v_pk_mul_f32 v[118:119], v[118:119], v[114:115] op_sel:[0,1] op_sel_hi:[1,1]\n"
      "v_cvt_pk_bf16_f32 v109, v118, v119\n"
      "v_lshlrev_b32 v118, 16, v110\n"
      "v_and_b32 v119, 0xffff0000, v110\n"
      "v_pk_mul_f32 v[118:119], v[118:119], v[114:115] op_sel:[0,1] op_sel_hi:[1,1]\n"
      "v_cvt_pk_bf16_f32 v110, v118, v119\n"
      "v_lshlrev_b32 v118, 16, v111\n"
      "v_and_b32 v119, 0xffff0000, v111\n"
      "v_pk_mul_f32 v[118:119], v[118:119], v[114:115] op_sel:[0,1] op_sel_hi:[1,1]\n"
      "v_cvt_pk_bf16_f32 v111, v118, v119\n"
      "s_setpc_b64 s[84:85]\n"
      "MIS_%=:\n"
      "s_waitcnt lgkmcnt(0)\n"
      "v_sub_f32 v148, v153, v117\n"
      "v_min_f32 v149, 0, -v148\n"
      "v_min_f32 v148, 0, v148\n"
      "v_exp_f32 v149, v149\n"
      "v_exp_f32 v148, v148\n"
      "v_min_f32 v117, v117, v153\n"
      "v_mov_b32 v112, v148\n"
      "v_mov_b32 v228, v149\n"
      "v_mov_b32 v114, v112\n"
      "v_mov_b32 v230, v228\n"
      "s_nop 0\n"
      "v_permlane32_swap_b32 v112, v114\n"
      "v_permlane32_swap_b32 v228, v230\n"
      "v_mov_b32 v113, v112\n"
      "v_mov_b32 v229, v228\n"
      "v_mov_b32 v115, v114\n"
      "v_mov_b32 v231, v230\n"
      "v_permlane16_swap_b32 v112, v113\n"
      "v_permlane16_swap_b32 v228, v229\n"
      "v_permlane16_swap_b32 v114, v115\n"
      "v_permlane16_swap_b32 v230, v231\n"
      "s_call_b64 s[84:85], SCALE_%=\n"
      "v_pk_mul_f32 v[160:161], v[160:161], v[228:229] op_sel:[0,0] op_sel_hi:[1,0]\n"
      "v_pk_mul_f32 v[162:163], v[162:163], v[228:229] op_sel:[0,0] op_sel_hi:[1,0]\n"
      "v_pk_mul_f32 v[164:165], v[164:165], v[228:229] op_sel:[0,1] op_sel_hi:[1,1]\n"
      "v_pk_mul_f32 v[166:167], v[166:167], v[228:229] op_sel:[0,1] op_sel_hi:[1,1]\n"
      "v_pk_mul_f32 v[168:169], v[168:169], v[230:231] op_sel:[0,0] op_sel_hi:[1,0]\n"
      "v_pk_mul_f32 v[170:171], v[170:171], v[230:231] op_sel:[0,0] op_sel_hi:[1,0]\n"
      "v_pk_mul_f32 v[172:173], v[172:173], v[230:231] op_sel:[0,1] op_sel_hi:[1,1]\n"
      "v_pk_mul_f32 v[174:175], v[174:175], v[230:231] op_sel:[0,1] op_sel_hi:[1,1]\n"
      "v_pk_mul_f32 v[176:177], v[176:177], v[228:229] op_sel:[0,0] op_sel_hi:[1,0]\n"
      "v_pk_mul_f32 v[178:179], v[178:179], v[228:229] op_sel:[0,0] op_sel_hi:[1,0]\n"
      "v_pk_mul_f32 v[180:181], v[180:181], v[228:229] op_sel:[0,1] op_sel_hi:[1,1]\n"
      "v_pk_mul_f32 v[182:183], v[182:183], v[228:229] op_sel:[0,1] op_sel_hi:[1,1]\n"
      "v_pk_mul_f32 v[184:185], v[184:185], v[230:231] op_sel:[0,0] op_sel_hi:[1,0]\n"
      "v_pk_mul_f32 v[186:187], v[186:187], v[230:231] op_sel:[0,0] op_sel_hi:[1,0]\n"
      "v_pk_mul_f32 v[188:189], v[188:189], v[230:231] op_sel:[0,1] op_sel_hi:[1,1]\n"
      "v_pk_mul_f32 v[190:191], v[190:191], v[230:231] op_sel:[0,1] op_sel_hi:[1,1]\n"
      "v_mov_b32 v112, v228\n"
      "v_mov_b32 v113, v229\n"
      "v_mov_b32 v114, v230\n"
      "v_mov_b32 v115, v231\n"
      "v_accvgpr_read_b32 v128, a64\n"
      "v_accvgpr_read_b32 v129, a65\n"
      "v_accvgpr_read_b32 v130, a66\n"
      "v_accvgpr_read_b32 v131, a67\n"
      "v_accvgpr_read_b32 v132, a68\n"
      "v_accvgpr_read_b32 v133, a69\n"
      "v_accvgpr_read_b32 v134, a70\n"
      "v_accvgpr_read_b32 v135, a71\n"
      "v_accvgpr_read_b32 v136, a72\n"
      "v_accvgpr_read_b32 v137, a73\n"
      "v_accvgpr_read_b32 v138, a74\n"
      "v_accvgpr_read_b32 v139, a75\n"
      "v_accvgpr_read_b32 v140, a76\n"
      "v_accvgpr_read_b32 v141, a77\n"
      "v_accvgpr_read_b32 v142, a78\n"
      "v_accvgpr_read_b32 v143, a79\n"
      "v_lshlrev_b32 v118, 16, v128\n"
      "v_and_b32 v119, 0xffff0000, v128\n"
      "v_pk_mul_f32 v[118:119], v[118:119], v[112:113] op_sel:[0,0] op_sel_hi:[1,0]\n"
      "v_cvt_pk_bf16_f32 v128, v118, v119\n"
      "v_lshlrev_b32 v118, 16, v129\n"
      "v_and_b32 v119, 0xffff0000, v129\n"
      "v_pk_mul_f32 v[118:119], v[118:119], v[112:113] op_sel:[0,0] op_sel_hi:[1,0]\n"
      "v_cvt_pk_bf16_f32 v129, v118, v119\n"
      "v_lshlrev_b32 v118, 16, v130\n"
      "v_and_b32 v119, 0xffff0000, v130\n"
      "v_pk_mul_f32 v[118:119], v[118:119], v[112:113] op_sel:[0,0] op_sel_hi:[1,0]\n"
      "v_cvt_pk_bf16_f32 v130, v118, v119\n"
      "v_lshlrev_b32 v118, 16, v131\n"
      "v_and_b32 v119, 0xffff0000, v131\n"
      "v_pk_mul_f32 v[118:119], v[118:119], v[112:113] op_sel:[0,0] op_sel_hi:[1,0]\n"
      "v_cvt_pk_bf16_f32 v131, v118, v119\n"
      "v_lshlrev_b32 v118, 16, v132\n"
      "v_and_b32 v119, 0xffff0000, v132\n"
      "v_pk_mul_f32 v[118:119], v[118:119], v[112:113] op_sel:[0,1] op_sel_hi:[1,1]\n"
      "v_cvt_pk_bf16_f32 v132, v118, v119\n"
      "v_lshlrev_b32 v118, 16, v133\n"
      "v_and_b32 v119, 0xffff0000, v133\n"
      "v_pk_mul_f32 v[118:119], v[118:119], v[112:113] op_sel:[0,1] op_sel_hi:[1,1]\n"
      "v_cvt_pk_bf16_f32 v133, v118, v119\n"
      "v_lshlrev_b32 v118, 16, v134\n"
      "v_and_b32 v119, 0xffff0000, v134\n"
      "v_pk_mul_f32 v[118:119], v[118:119], v[112:113] op_sel:[0,1] op_sel_hi:[1,1]\n"
      "v_cvt_pk_bf16_f32 v134, v118, v119\n"
      "v_lshlrev_b32 v118, 16, v135\n"
      "v_and_b32 v119, 0xffff0000, v135\n"
      "v_pk_mul_f32 v[118:119], v[118:119], v[112:113] op_sel:[0,1] op_sel_hi:[1,1]\n"
      "v_cvt_pk_bf16_f32 v135, v118, v119\n"
      "v_lshlrev_b32 v118, 16, v136\n"
      "v_and_b32 v119, 0xffff0000, v136\n"
      "v_pk_mul_f32 v[118:119], v[118:119], v[114:115] op_sel:[0,0] op_sel_hi:[1,0]\n"
      "v_cvt_pk_bf16_f32 v136, v118, v119\n"
      "v_lshlrev_b32 v118, 16, v137\n"
      "v_and_b32 v119, 0xffff0000, v137\n"
      "v_pk_mul_f32 v[118:119], v[118:119], v[114:115] op_sel:[0,0] op_sel_hi:[1,0]\n"
      "v_cvt_pk_bf16_f32 v137, v118, v119\n"
      "v_lshlrev_b32 v118, 16, v138\n"
      "v_and_b32 v119, 0xffff0000, v138\n"
      "v_pk_mul_f32 v[118:119], v[118:119], v[114:115] op_sel:[0,0] op_sel_hi:[1,0]\n"
      "v_cvt_pk_bf16_f32 v138, v118, v119\n"
      "v_lshlrev_b32 v118, 16, v139\n"
      "v_and_b32 v119, 0xffff0000, v139\n"
      "v_pk_mul_f32 v[118:119], v[118:119], v[114:115] op_sel:[0,0] op_sel_hi:[1,0]\n"
      "v_cvt_pk_bf16_f32 v139, v118, v119\n"
      "v_lshlrev_b32 v118, 16, v140\n"
      "v_and_b32 v119, 0xffff0000, v140\n"
      "v_pk_mul_f32 v[118:119], v[118:119], v[114:115] op_sel:[0,1] op_sel_hi:[1,1]\n"
      "v_cvt_pk_bf16_f32 v140, v118, v119\n"
      "v_lshlrev_b32 v118, 16, v141\n"
      "v_and_b32 v119, 0xffff0000, v141\n"
      "v_pk_mul_f32 v[118:119], v[118:119], v[114:115] op_sel:[0,1] op_sel_hi:[1,1]\n"
      "v_cvt_pk_bf16_f32 v141, v118, v119\n"
      "v_lshlrev_b32 v118, 16, v142\n"
      "v_and_b32 v119, 0xffff0000, v142\n"
      "v_pk_mul_f32 v[118:119], v[118:119], v[114:115] op_sel:[0,1] op_sel_hi:[1,1]\n"
      "v_cvt_pk_bf16_f32 v142, v118, v119\n"
      "v_lshlrev_b32 v118, 16, v143\n"
      "v_and_b32 v119, 0xffff0000, v143\n"
      "v_pk_mul_f32 v[118:119], v[118:119], v[114:115] op_sel:[0,1] op_sel_hi:[1,1]\n"
      "v_cvt_pk_bf16_f32 v143, v118, v119\n"
      "v_accvgpr_write_b32 a64, v128\n"
      "v_accvgpr_write_b32 a65, v129\n"
      "v_accvgpr_write_b32 a66, v130\n"
      "v_accvgpr_write_b32 a67, v131\n"
      "v_accvgpr_write_b32 a68, v132\n"
      "v_accvgpr_write_b32 a69, v133\n"
      "v_accvgpr_write_b32 a70, v134\n"
      "v_accvgpr_write_b32 a71, v135\n"
      "v_accvgpr_write_b32 a72, v136\n"
      "v_accvgpr_write_b32 a73, v137\n"
      "v_accvgpr_write_b32 a74, v138\n"
      "v_accvgpr_write_b32 a75, v139\n"
      "v_accvgpr_write_b32 a76, v140\n"
      "v_accvgpr_write_b32 a77, v141\n"
      "v_accvgpr_write_b32 a78, v142\n"
      "v_accvgpr_write_b32 a79, v143\n"
      "s_branch MR_%=\n"
      "FIX_%=:\n"
      "s_nop 7\n"
      "v_sub_f32 v224, v81, v117\n"
      "v_min_f32 v112, 0, -v224\n"
      "v_min_f32 v224, 0, v224\n"
      "v_exp_f32 v112, v112\n"
      "v_exp_f32 v224, v224\n"
      "s_nop 0\n"
      "v_mul_f32 v144, v144, v224\n"
      "v_mul_f32 v80, v80, v112\n"
      "v_mov_b32 v226, v224\n"
      "v_mov_b32 v114, v112\n"
      "s_nop 0\n"
      "v_permlane32_swap_b32 v224, v226\n"
      "v_permlane32_swap_b32 v112, v114\n"
      "v_mov_b32 v225, v224\n"
      "v_mov_b32 v113, v112\n"
      "v_mov_b32 v227, v226\n"
      "v_mov_b32 v115, v114\n"
      "v_permlane16_swap_b32 v224, v225\n"
      "v_permlane16_swap_b32 v112, v113\n"
      "v_permlane16_swap_b32 v226, v227\n"
      "v_permlane16_swap_b32 v114, v115\n"
      "v_pk_mul_f32 v[32:33], v[32:33], v[224:225] op_sel:[0,0] op_sel_hi:[1,0]\n"
      "v_pk_mul_f32 v[34:35], v[34:35], v[224:225] op_sel:[0,0] op_sel_hi:[1,0]\n"
      "v_pk_mul_f32 v[36:37], v[36:37], v[224:225] op_sel:[0,1] op_sel_hi:[1,1]\n"
      "v_pk_mul_f32 v[38:39], v[38:39], v[224:225] op_sel:[0,1] op_sel_hi:[1,1]\n"
      "v_pk_mul_f32 v[40:41], v[40:41], v[226:227] op_sel:[0,0] op_sel_hi:[1,0]\n"
      "v_pk_mul_f32 v[42:43], v[42:43], v[226:227] op_sel:[0,0] op_sel_hi:[1,0]\n"
      "v_pk_mul_f32 v[44:45], v[44:45], v[226:227] op_sel:[0,1] op_sel_hi:[1,1]\n"
      "v_pk_mul_f32 v[46:47], v[46:47], v[226:227] op_sel:[0,1] op_sel_hi:[1,1]\n"
      "v_pk_mul_f32 v[48:49], v[48:49], v[224:225] op_sel:[0,0] op_sel_hi:[1,0]\n"
      "v_pk_mul_f32 v[50:51], v[50:51], v[224:225] op_sel:[0,0] op_sel_hi:[1,0]\n"
      "v_pk_mul_f32 v[52:53], v[52:53], v[224:225] op_sel:[0,1] op_sel_hi:[1,1]\n"
      "v_pk_mul_f32 v[54:55], v[54:55], v[224:225] op_sel:[0,1] op_sel_hi:[1,1]\n"
      "v_pk_mul_f32 v[56:57], v[56:57], v[226:227] op_sel:[0,0] op_sel_hi:[1,0]\n"
      "v_pk_mul_f32 v[58:59], v[58:59], v[226:227] op_sel:[0,0] op_sel_hi:[1,0]\n"
      "v_pk_mul_f32 v[60:61], v[60:61], v[226:227] op_sel:[0,1] op_sel_hi:[1,1]\n"
      "v_pk_mul_f32 v[62:63], v[62:63], v[226:227] op_sel:[0,1] op_sel_hi:[1,1]\n"
      "v_lshlrev_b32 v118, 16, v64\n"
      "v_and_b32 v119, 0xffff0000, v64\n"
      "v_pk_mul_f32 v[118:119], v[118:119], v[112:113] op_sel:[0,0] op_sel_hi:[1,0]\n"
      "v_cvt_pk_bf16_f32 v64, v118, v119\n"
      "v_lshlrev_b32 v118, 16, v65\n"
      "v_and_b32 v119, 0xffff0000, v65\n"
      "v_pk_mul_f32 v[118:119], v[118:119], v[112:113] op_sel:[0,0] op_sel_hi:[1,0]\n"
      "v_cvt_pk_bf16_f32 v65, v118, v119\n"
      "v_lshlrev_b32 v118, 16, v66\n"
      "v_and_b32 v119, 0xffff0000, v66\n"
      "v_pk_mul_f32 v[118:119], v[118:119], v[112:113] op_sel:[0,0] op_sel_hi:[1,0]\n"
      "v_cvt_pk_bf16_f32 v66, v118, v119\n"
      "v_lshlrev_b32 v118, 16, v67\n"
      "v_and_b32 v119, 0xffff0000, v67\n"
      "v_pk_mul_f32 v[118:119], v[118:119], v[112:113] op_sel:[0,0] op_sel_hi:[1,0]\n"
      "v_cvt_pk_bf16_f32 v67, v118, v119\n"
      "v_lshlrev_b32 v118, 16, v68\n"
      "v_and_b32 v119, 0xffff0000, v68\n"
      "v_pk_mul_f32 v[118:119], v[118:119], v[112:113] op_sel:[0,1] op_sel_hi:[1,1]\n"
      "v_cvt_pk_bf16_f32 v68, v118, v119\n"
      "v_lshlrev_b32 v118, 16, v69\n"
      "v_and_b32 v119, 0xffff0000, v69\n"
      "v_pk_mul_f32 v[118:119], v[118:119], v[112:113] op_sel:[0,1] op_sel_hi:[1,1]\n"
      "v_cvt_pk_bf16_f32 v69, v118, v119\n"
      "v_lshlrev_b32 v118, 16, v70\n"
      "v_and_b32 v119, 0xffff0000, v70\n"
      "v_pk_mul_f32 v[118:119], v[118:119], v[112:113] op_sel:[0,1] op_sel_hi:[1,1]\n"
      "v_cvt_pk_bf16_f32 v70, v118, v119\n"
      "v_lshlrev_b32 v118, 16, v71\n"
      "v_and_b32 v119, 0xffff0000, v71\n"
      "v_pk_mul_f32 v[118:119], v[118:119], v[112:113] op_sel:[0,1] op_sel_hi:[1,1]\n"
      "v_cvt_pk_bf16_f32 v71, v118, v119\n"
      "v_lshlrev_b32 v118, 16, v72\n"
      "v_and_b32 v119, 0xffff0000, v72\n"
      "v_pk_mul_f32 v[118:119], v[118:119], v[114:115] op_sel:[0,0] op_sel_hi:[1,0]\n"
      "v_cvt_pk_bf16_f32 v72, v118, v119\n"
      "v_lshlrev_b32 v118, 16, v73\n"
      "v_and_b32 v119, 0xffff0000, v73\n"
      "v_pk_mul_f32 v[118:119], v[118:119], v[114:115] op_sel:[0,0] op_sel_hi:[1,0]\n"
      "v_cvt_pk_bf16_f32 v73, v118, v119\n"
      "v_lshlrev_b32 v118, 16, v74\n"
      "v_and_b32 v119, 0xffff0000, v74\n"
      "v_pk_mul_f32 v[118:119], v[118:119], v[114:115] op_sel:[0,0] op_sel_hi:[1,0]\n"
      "v_cvt_pk_bf16_f32 v74, v118, v119\n"
      "v_lshlrev_b32 v118, 16, v75\n"
      "v_and_b32 v119, 0xffff0000, v75\n"
      "v_pk_mul_f32 v[118:119], v[118:119], v[114:115] op_sel:[0,0] op_sel_hi:[1,0]\n"
      "v_cvt_pk_bf16_f32 v75, v118, v119\n"
      "v_lshlrev_b32 v118, 16, v76\n"
      "v_and_b32 v119, 0xffff0000, v76\n"
      "v_pk_mul_f32 v[118:119], v[118:119], v[114:115] op_sel:[0,1] op_sel_hi:[1,1]\n"
      "v_cvt_pk_bf16_f32 v76, v118, v119\n"
      "v_lshlrev_b32 v118, 16, v77\n"
      "v_and_b32 v119, 0xffff0000, v77\n"
      "v_pk_mul_f32 v[118:119], v[118:119], v[114:115] op_sel:[0,1] op_sel_hi:[1,1]\n"
      "v_cvt_pk_bf16_f32 v77, v118, v119\n"
      "v_lshlrev_b32 v118, 16, v78\n"
      "v_and_b32 v119, 0xffff0000, v78\n"
      "v_pk_mul_f32 v[118:119], v[118:119], v[114:115] op_sel:[0,1] op_sel_hi:[1,1]\n"
      "v_cvt_pk_bf16_f32 v78, v118, v119\n"
      "v_lshlrev_b32 v118, 16, v79\n"
      "v_and_b32 v119, 0xffff0000, v79\n"
      "v_pk_mul_f32 v[118:119], v[118:119], v[114:115] op_sel:[0,1] op_sel_hi:[1,1]\n"
      "v_cvt_pk_bf16_f32 v79, v118, v119\n"
      "v_add_f32 v224, v144, v80\n"
      "v_rcp_f32 v224, v224\n"
      "v_mfma_f32_16x16x32_bf16 v[160:163], v[86:89], v[64:67], v[32:35]\n"
      "v_mfma_f32_16x16x32_bf16 v[176:179], v[84:87], v[64:67], v[48:51]\n"
      "v_mfma_f32_16x16x32_bf16 v[164:167], v[86:89], v[68:71], v[36:39]\n"
      "v_mfma_f32_16x16x32_bf16 v[180:183], v[84:87], v[68:71], v[52:55]\n"
      "v_mfma_f32_16x16x32_bf16 v[168:171], v[86:89], v[72:75], v[40:43]\n"
      "v_mfma_f32_16x16x32_bf16 v[184:187], v[84:87], v[72:75], v[56:59]\n"
      "v_mfma_f32_16x16x32_bf16 v[172:175], v[86:89], v[76:79], v[44:47]\n"
      "v_mfma_f32_16x16x32_bf16 v[188:191], v[84:87], v[76:79], v[60:63]\n"
      "v_mov_b32 v226, v224\n"
      "s_nop 1\n"
      "s_nop 0\n"
      "s_nop 1\n"
      "v_permlane32_swap_b32 v224, v226\n"
      "s_nop 1\n"
      "v_mov_b32 v225, v224\n"
      "s_nop 1\n"
      "v_mov_b32 v227, v226\n"
      "s_nop 1\n"
      "v_permlane16_swap_b32 v224, v225\n"
      "s_nop 1\n"
      "v_permlane16_swap_b32 v226, v227\n"
      "s_nop 1\n"
      "s_branch FR_%=\n"
      ".subsection 0\n"
      : "+{s92}"(tch), "+{v[208:215]}"(mk), "+{v[240:241]}"(epoch), "+{v[242:249]}"(ad), "+{v[250:251]}"(ra)
      : [srdk] "s"(srdK), [srdv] "s"(srdV), [srdr] "s"(srdR), [srdx] "s"(srdX), [srdt] "s"(srdVt), [line] "s"(line),
        [outp] "s"((char*)out + ((size_t)bk * 128 + rh * 64) * (D_ * 2) + (2 * split + kh) * 64),
        [fo] "s"(FL ? 128 + split * 128 : 64 + split * 32 + wid * 8), [xo] "s"(split * 4 * (unsigned)XW_BYTES), [xw] "s"(xarea(wid)), [xr] "s"(xarea(wid ^ 2)),
        [so0] "s"(so0), [inc] "s"(inc), [wb] "s"(wbase), [tv] "s"(wbase + STAGE_BYTES + KIMG + split * 8192), [n1] "s"(n1), [n2] "s"(12 - n1),
        [qoff] "s"(Q_OFF + rh * 16384 - kh * (8192 - 1024)), [toff] "s"(TREE_OFF - kh * (8192 - 1024)),
        [neg] "s"((wid ^ (3 * split)) ? 0xff800000u : 0u),
        [lead] "s"(blockIdx.x * 4 + wid * 32), [cph] "s"(C2 + (unsigned)((jj & ~3) * 8 + xg * 4) * WIN / 256u),
        [rep] "s"((unsigned)(blockIdx.x - 160) * 4), [vrd] "s"(8192 + (jj & (8 - 1)) * (8192 / 8))
      : "v0", "v1", "v2", "v3", "v4", "v5", "v6", "v7", "v8", "v9", "v10", "v11", "v12", "v13", "v14", "v15", "v16", "v17", "v18", "v19", "v20", "v21", "v22", "v23", "v24", "v25", "v26", "v27", "v28", "v29", "v30", "v31", "v32", "v33", "v34", "v35", "v36", "v37", "v38", "v39", "v40", "v41", "v42", "v43", "v44", "v45", "v46", "v47", "v48", "v49", "v50", "v51", "v52", "v53", "v54", "v55", "v56", "v57", "v58", "v59", "v60", "v61", "v62", "v63", "v64", "v65", "v66", "v67", "v68", "v69", "v70", "v71", "v72", "v73", "v74", "v75", "v76", "v77", "v78", "v79", "v80", "v81", "v82", "v83", "v84", "v85", "v86", "v87", "v88", "v89", "v90", "v91", "v92", "v93", "v94", "v95", "v96", "v97", "v98", "v99", "v100", "v101", "v102", "v103", "v104", "v105", "v106", "v107", "v108", "v109", "v110", "v111", "v112", "v113", "v114", "v115", "v116", "v117", "v118", "v119", "v120", "v121", "v122", "v123", "v124", "v125", "v126", "v127", "v128", "v129", "v130", "v131", "v132", "v133", "v134", "v135", "v136", "v137", "v138", "v139", "v140", "v141", "v142", "v143", "v144", "v145", "v146", "v147", "v148", "v149", "v150", "v151", "v152", "v153", "v154", "v155", "v156", "v157", "v158", "v159", "v160", "v161", "v162", "v163", "v164", "v165", "v166", "v167", "v168", "v169", "v170", "v171", "v172", "v173", "v174", "v175", "v176", "v177", "v178", "v179", "v180", "v181", "v182", "v183", "v184", "v185", "v186", "v187", "v188", "v189", "v190", "v191", "v192", "v193", "v194", "v195", "v196", "v197", "v198", "v199", "v200", "v201", "v202", "v203", "v204", "v205", "v206", "v207", "v216", "v217", "v218", "v219", "v220", "v221", "v222", "v223", "v224", "v225", "v226", "v227", "v228", "v229", "v230", "v231", "v232", "v233", "v234", "v235", "v236", "v237", "v238", "v239", "a0", "a1", "a2", "a3", "a4", "a5", "a6", "a7", "a8", "a9", "a10", "a11", "a12", "a13", "a14", "a15", "a16", "a17", "a18", "a19", "a20", "a21", "a22", "a23", "a24", "a25", "a26", "a27", "a28", "a29", "a30", "a31", "a32", "a33", "a34", "a35", "a36", "a37", "a38", "a39", "a40", "a41", "a42", "a43", "a44", "a45", "a46", "a47", "a48", "a49", "a50", "a51", "a52", "a53", "a54", "a55", "a56", "a57", "a58", "a59", "a60", "a61", "a62", "a63", "a64", "a65", "a66", "a67", "a68", "a69", "a70", "a71", "a72", "a73", "a74", "a75", "a76", "a77", "a78", "a79", "a80", "a81", "a82", "a83", "a84", "a85", "a86", "a87", "a88", "a89", "a90", "a91", "a92", "a93", "a94", "a95", "a96", "a97", "a98", "a99", "a100", "a101", "a102", "a103", "a104", "a105", "a106", "a107", "a108", "a109", "a110", "a111", "a112", "a113", "a114", "a115", "a116", "a117", "a118", "a119", "a120", "a121", "a122", "a123", "a124", "a125", "a126", "a127", "a128", "a129", "a130", "a131", "a132", "a133", "a134", "a135", "a136", "a137", "a138", "a139", "a140", "a141", "a142", "a143", "a144", "a145", "a146", "a147", "a148", "a149", "a150", "a151", "a152", "a153", "a154", "a155", "a156", "a157", "a158", "a159", "a160", "a161", "a162", "a163", "a164", "a165", "a166", "a167", "a168", "a169", "a170", "a171", "a172", "a173", "a174", "a175", "a176", "a177", "a178", "a179", "a180", "a181", "a182", "a183", "a184", "a185", "a186", "a187", "a188", "a189", "a190", "a191", "a192", "a193", "a194", "a195", "a196", "a197", "a198", "a199", "a200", "a201", "a202", "a203", "a204", "a205", "a206", "a207", "a208", "a209", "a210", "a211", "a212", "a213", "a214", "a215", "a216", "a217", "a218", "a219", "a220", "a221", "a222", "a223", "a224", "a225", "a226", "a227", "a228", "a229", "a230", "a231", "s60", "s61", "s62", "s63", "s64", "s65", "s66", "s67", "s68", "s69", "s70", "s71", "s72", "s73", "s74", "s75", "s76", "s77", "s78", "s79", "s80", "s81", "s82", "s83", "s84", "s85", "s86", "s87", "s88", "s89", "s90", "s91", "s93", "s94", "s95", "s99", "s100", "s101", "vcc", "scc", "memory");
}

}

extern "C" void kernel_launch(void* const* d_in, const int* in_sizes, int n_in, void* d_out, int out_size,
                              void* d_ws, size_t ws_size, hipStream_t stream) {
  if (n_in != 6 || ws_size < WS_END) return;
  const __hip_bfloat16* q = (const __hip_bfloat16*)d_in[0];
  const __hip_bfloat16* kc = (const __hip_bfloat16*)d_in[1];
  const __hip_bfloat16* vc = (const __hip_bfloat16*)d_in[2];
  const __hip_bfloat16* kt = (const __hip_bfloat16*)d_in[3];
  const __hip_bfloat16* vt = (const __hip_bfloat16*)d_in[4];
  const __hip_bfloat16* tm = (const __hip_bfloat16*)d_in[5];
  hipLaunchKernelGGL(tree_attn_main, dim3(B_ * HKV_ * 2), dim3(256), 0, stream, q, kc, vc, kt, vt, tm,
                     (__hip_bfloat16*)d_out, (char*)d_ws);
}
```

```cpp
#include <hip/hip_runtime.h>
#include <hip/hip_bf16.h>
#include <stdint.h>

#pragma clang diagnostic ignored "-Wint-to-pointer-cast"
#pragma clang diagnostic ignored "-Winline-asm"

namespace {

constexpr int B_ = 16, HQ_ = 32, HKV_ = 8, T_ = 32, C_ = 4096, D_ = 128;
constexpr int TILE = 64;
constexpr int KIMG = TILE * D_ * 2;
constexpr int STAGE_BYTES = 2 * KIMG;
constexpr int TREE_OFF = 4 * STAGE_BYTES;
constexpr int Q_OFF = 3 * STAGE_BYTES;
constexpr int LDS_BYTES = TREE_OFF + 2 * 13312;
constexpr unsigned C2 = 319, WIN = 52;
constexpr int CODE_BYTES = 26624;

constexpr size_t WS_EPOCH = 0;
constexpr size_t WS_REF = WS_EPOCH + 128 * 128;
constexpr size_t WS_X = WS_REF + 16384;
constexpr size_t XW_BYTES = 4096 + 1024;
constexpr size_t WS_END = WS_X + (size_t)128 * 2 * 4 * XW_BYTES;

typedef float    f32x4  __attribute__((ext_vector_type(4)));
typedef unsigned long long u64x8 __attribute__((ext_vector_type(8)));
typedef unsigned u32x8  __attribute__((ext_vector_type(8)));
typedef unsigned u32x4  __attribute__((ext_vector_type(4)));
typedef unsigned u32x2  __attribute__((ext_vector_type(2)));
typedef __attribute__((address_space(3))) f32x4 lds_f32x4;
typedef __attribute__((address_space(1))) unsigned long long gu64;

#define DI __device__ __forceinline__

DI void dma16(unsigned lds_off, unsigned voff, __amdgpu_buffer_rsrc_t rsrc, unsigned soff) {
  asm volatile("s_mov_b32 m0, %0\n\ts_nop 0\n\tbuffer_load_dwordx4 %1, %2, %3 offen nt lds"
               :: "s"(lds_off), "v"(voff), "s"(rsrc), "s"(soff) : "memory");
}

__global__ __launch_bounds__(256, 1)
void tree_attn_main(const __hip_bfloat16*, const __hip_bfloat16*, const __hip_bfloat16*, const __hip_bfloat16*,
                    const __hip_bfloat16*, const __hip_bfloat16*, __hip_bfloat16*, char*) {
  u64x8 ka;
  asm volatile("s_load_dwordx16 s[4:19], %1, 0x0" : "={s[4:19]}"(ka) : "s"(__builtin_amdgcn_kernarg_segment_ptr()) : "memory");
  __shared__ __attribute__((aligned(1024))) char smem[LDS_BYTES];
  unsigned long long pc;
  asm volatile("s_getpc_b64 %0" : "=s"(pc));
  const int tid = threadIdx.x, lane = tid & 63;
  const int wid = __builtin_amdgcn_readfirstlane(tid >> 6);
  const int rh = wid & 1, kh = wid >> 1;
  const int xg = blockIdx.x & 7, jj = blockIdx.x >> 3;
  const int bk = xg * 16 + (jj >> 1), split = jj & 1;
  const int c16 = lane & 15, g = lane >> 4;
  const unsigned lds0 = (unsigned)(uintptr_t)smem;
  if (wid >= 2) {
    __amdgpu_buffer_rsrc_t srdC = __builtin_amdgcn_make_buffer_rsrc((void*)(pc & ~63ull), 0, CODE_BYTES, 0x00020000);
    asm volatile("s_mov_b32 m0, %0\n\ts_nop 0\n\tbuffer_load_dwordx4 %1, %2, %3 offen lds"
                 :: "s"(lds0 + TREE_OFF + 8192), "v"(lane * 16), "s"(srdC), "s"((unsigned)(wid == 3 ? min(jj, CODE_BYTES / 1024 - 1) : jj & 3) * 1024) : "memory");
  }

  const unsigned voffK = (unsigned)((8 * wid + (lane >> 3)) * 256 + (((lane & 7) ^ (4 * (wid & 1) + g)) << 4));
  const unsigned voffV = (unsigned)((8 * wid + (lane >> 3)) * 256 + (((lane & 7) ^ (g << 1)) << 4));
  const unsigned wbase = lds0 + (unsigned)wid * 2048;

#pragma unroll
  for (int j = 0; j < 4; ++j) *(lds_f32x4*)(lds0 + KIMG + 4096 * j + tid * 16) = f32x4{0.f, 0.f, 0.f, 0.f};
#pragma unroll
  for (int j = 0; j < 2; ++j) *(lds_f32x4*)(lds0 + STAGE_BYTES + KIMG + (1 - split) * 8192 + 4096 * j + tid * 16) = f32x4{0.f, 0.f, 0.f, 0.f};
  u32x8 ad;
#pragma unroll
  for (int j = 0; j < 2; ++j)
    ad[j] = lds0 + (unsigned)(2048 * (c16 >> 3) + 128 * (c16 & 7) + (((4 * j + g) ^ (c16 >> 1)) << 4) + kh * 8192);
  {
    const int qq = c16 >> 2, p = lane & 3;
#pragma unroll
    for (int j = 0; j < 4; ++j)
      ad[2 + j] = lds0 + (unsigned)(2048 * (g >> 1) + 128 * (4 * (g & 1) + qq) + kh * 8192 + KIMG +
                                    (((2 * (j ^ (2 * kh)) + (p >> 1)) ^ ((2 * (g & 1) + (qq >> 1)) << 1)) << 4) + 8 * (p & 1));
  }
  ad[6] = voffK; ad[7] = voffV + 128 * split;
  u32x2 ra = {(unsigned)(lane < 4 ? lane * 512 : 1 << 20), (unsigned)((jj & (4 - 1)) * 512 + (lane & 7) * 4 + (wid << 20))};
  unsigned mko = (unsigned)(((bk >> 3) * T_ + c16) * (T_ * 2) + g * 8);
  size_t qo = (size_t)bk * 128 * D_ * 2;
  asm volatile("" : "+{v[242:249]}"(ad), "+{v[250:251]}"(ra), "+v"(mko), "+s"(qo) :: "memory");
  asm volatile("s_waitcnt lgkmcnt(0)" : "+{s[4:19]}"(ka) :: "memory");
  const char *qg = (const char*)ka[0], *kc = (const char*)ka[1], *vc = (const char*)ka[2], *kt = (const char*)ka[3],
             *vt = (const char*)ka[4], *tmask = (const char*)ka[5];
  char *out = (char*)ka[6], *ws = (char*)ka[7];
  __amdgpu_buffer_rsrc_t srdQ = __builtin_amdgcn_make_buffer_rsrc((void*)((const char*)qg + qo), 0, 128 * D_ * 2, 0x00020000);
#pragma unroll
  for (int i = 0; i < 8; ++i) dma16(wbase + Q_OFF + (i >> 1) * 8192 + (i & 1) * 1024, ad[6], srdQ, (i >> 1) * 8192 + (i & 1) * 128);
  u32x8 mk;
  asm volatile("global_load_dwordx2 v[208:209], %1, %2\n\tglobal_load_dwordx2 v[210:211], %1, %2 offset:32\n\t"
               "global_load_dwordx2 v[212:213], %1, %2 offset:1024\n\tglobal_load_dwordx2 v[214:215], %1, %2 offset:1056"
               : "=&{v[208:215]}"(mk) : "v"(mko), "s"(tmask) : "memory");
  unsigned tch;
  asm volatile("s_load_dword s92, %1, 0x0\n\ts_load_dword s92, %2, 0x0"
               : "={s92}"(tch) : "s"(kc + qo * 32), "s"(vc + qo * 32) : "memory");
  const size_t kvoff = ((size_t)bk * C_ + (size_t)split * 2 * TILE) * D_ * 2;
  const int kvbytes = (C_ - split * 2 * TILE) * D_ * 2;
  const unsigned so0 = (bk & 1) * KIMG, inc = KIMG - 2 * so0;
  __amdgpu_buffer_rsrc_t srdK = __builtin_amdgcn_make_buffer_rsrc((void*)((const char*)kc + kvoff), 0, kvbytes, 0x00020000);
  __amdgpu_buffer_rsrc_t srdV = __builtin_amdgcn_make_buffer_rsrc((void*)((const char*)vc + kvoff), 0, kvbytes, 0x00020000);
  __amdgpu_buffer_rsrc_t srdKt = __builtin_amdgcn_make_buffer_rsrc((void*)((const char*)kt + (size_t)bk * T_ * D_ * 2), 0, T_ * D_ * 2, 0x00020000);
  dma16(wbase + TREE_OFF, ad[6], srdKt, 0);
  dma16(wbase + TREE_OFF + 1024, ad[6], srdKt, 128);
  char* line = ws + WS_EPOCH + (size_t)bk * 128;
  u32x2 epoch;
  asm volatile("global_load_dwordx2 v[240:241], %1, %2 sc1" : "=&{v[240:241]}"(epoch) : "v"(0), "s"(line) : "memory");
  __amdgpu_buffer_rsrc_t srdVt = __builtin_amdgcn_make_buffer_rsrc((void*)((const char*)vt + (size_t)bk * T_ * D_ * 2), 0, T_ * D_ * 2, 0x00020000);
  __amdgpu_buffer_rsrc_t srdR = __builtin_amdgcn_make_buffer_rsrc((void*)(ws + WS_REF), 0, 16384, 0x00020000);
  __amdgpu_buffer_rsrc_t srdX = __builtin_amdgcn_make_buffer_rsrc((void*)(ws + WS_X + (size_t)(bk * 8 + wid) * XW_BYTES), 0, (int)(5 * XW_BYTES), 0x00020000);
  auto xarea = [&](int w) { return lds0 + ((0x8d802000u >> (8 * w)) & 255u) * 1024; };
  const unsigned pg = jj >> 2, n1 = 4 + (pg == 7 ? 7u : pg == 160 / 32 ? 6u : (pg - (pg > 160 / 32) + xg * 5) % 6u);
  asm volatile(
      "s_mov_b32 s60, %[so0]\n"
      "s_mov_b32 s62, %[wb]\n"
      "v_add_u32 v120, 0x2000, v248\n"
      "s_memrealtime s[82:83]\n"
      "s_add_u32 m0, s62, 0\n"
      "s_nop 0\n"
      "buffer_load_dwordx4 v248, %[srdk], s60 offen nt lds\n"
      "v_mov_b64 v[216:217], 0\n"
      "v_mov_b64 v[218:219], 0\n"
      "v_xor_b32 v121, 0x80, v249\n"
      "v_add_u32 v122, 0x2000, v249\n"
      "v_mfma_f32_32x32x16_bf16 v[32:47], v[216:219], v[216:219], 0\n"
      "v_add_u32 v123, 0x2000, v121\n"
      "s_mov_b32 s61, %[so0]\n"
      "s_sub_u32 s78, 0x10000, %[inc]\n"
      "s_add_u32 m0, s62, 896\n"
      "s_nop 0\n"
      "buffer_load_dwordx4 v248, %[srdk], s60 offen offset:128 nt lds\n"
      "s_add_u32 s77, %[wb], 0x10000\n"
      "v_mfma_f32_32x32x16_bf16 v[48:63], v[216:219], v[216:219], 0\n"
      "s_mov_b32 s70, 300\n"
      "s_mov_b32 s80, 0x443212a\n"
      "s_mov_b32 s81, 0x0\n"
      "v_mfma_f32_32x32x16_bf16 v[64:79], v[216:219], v[216:219], 0\n"
      "s_mov_b32 s86, 109\n"
      "s_mov_b32 s87, 0\n"
      "s_add_u32 m0, s62, 8192\n"
      "s_nop 0\n"
      "buffer_load_dwordx4 v120, %[srdk], s60 offen nt lds\n"
      "s_mov_b32 s101, 1000\n"
      "s_mov_b32 s93, 28\n"
      "s_mov_b32 s99, 21\n"
      "s_mov_b32 s100, 282\n"
      "v_mfma_f32_32x32x16_bf16 v[80:95], v[216:219], v[216:219], 0\n"
      "s_lshl_b32 s91, %[wb], 18\n"
      "s_sub_u32 s91, 0x10000000, s91\n"
      "s_mov_b32 s94, 0x3e0293ee\n"
      "s_add_u32 m0, s62, 9088\n"
      "s_nop 0\n"
      "buffer_load_dwordx4 v120, %[srdk], s60 offen offset:128 nt lds\n"
      "s_add_u32 s62, s62, 0x8000\n"
      "s_and_b32 s62, s62, 0x1ffff\n"
      "s_add_u32 s60, s60, %[inc]\n"
      "s_mov_b32 s95, 0xffff0000\n"
      "v_mfma_f32_32x32x16_bf16 v[192:207], v[216:219], v[216:219], 0\n"
      "s_mov_b32 s76, 0xd0289660\n"
      "v_mbcnt_lo_u32_b32 v112, -1, 0\n"
      "v_mbcnt_hi_u32_b32 v112, -1, v112\n"
      "v_and_b32 v112, 15, v112\n"
      "v_mov_b32 v113, 0x3f803f80\n"
      "v_accvgpr_write_b32 a192, 0\n"
      "s_add_u32 m0, %[tv], 0\n"
      "s_nop 0\n"
      "buffer_load_dwordx4 v249, %[srdt], 0 offen nt lds\n"
      "v_add_u32 v224, %[qoff], v242\n"
      "v_add_u32 v228, %[toff], v242\n"
      "v_xor_b32 v226, 0x400, v224\n"
      "v_xor_b32 v230, 0x400, v228\n"
      "v_and_b32 v118, 0xfffffbff, v224\n"
      "v_add_u32 v225, %[qoff], v243\n"
      "v_add_u32 v229, %[toff], v243\n"
      "v_xor_b32 v227, 0x400, v225\n"
      "s_add_u32 m0, %[tv], 1024\n"
      "s_nop 0\n"
      "buffer_load_dwordx4 v121, %[srdt], 0 offen nt lds\n"
      "v_xor_b32 v231, 0x400, v229\n"
      "v_and_b32 v119, 0xfffffbff, v225\n"
      "v_cmp_eq_u32 vcc, 0, v112\n"
      "v_mfma_f32_32x32x16_bf16 a[128:143], v[216:219], v[216:219], 0\n"
      "s_nop 0\n"
      "v_cndmask_b32 v114, 0, v113, vcc\n"
      "v_accvgpr_write_b32 a196, v114\n"
      "v_accvgpr_write_b32 a197, v114\n"
      "s_add_u32 m0, s62, 0\n"
      "s_nop 0\n"
      "buffer_load_dwordx4 v248, %[srdk], s60 offen nt lds\n"
      "v_accvgpr_write_b32 a198, v114\n"
      "v_accvgpr_write_b32 a199, v114\n"
      "v_cmp_eq_u32 vcc, 4, v112\n"
      "v_mfma_f32_32x32x16_bf16 a[144:159], v[216:219], v[216:219], 0\n"
      "s_nop 0\n"
      "v_cndmask_b32 v114, 0, v113, vcc\n"
      "v_accvgpr_write_b32 a200, v114\n"
      "v_accvgpr_write_b32 a201, v114\n"
      "s_add_u32 m0, s62, 896\n"
      "s_nop 0\n"
      "buffer_load_dwordx4 v248, %[srdk], s60 offen offset:128 nt lds\n"
      "v_accvgpr_write_b32 a202, v114\n"
      "v_accvgpr_write_b32 a203, v114\n"
      "v_cmp_eq_u32 vcc, 8, v112\n"
      "v_mfma_f32_32x32x16_bf16 a[160:175], v[216:219], v[216:219], 0\n"
      "s_nop 0\n"
      "v_cndmask_b32 v114, 0, v113, vcc\n"
      "v_accvgpr_write_b32 a204, v114\n"
      "v_accvgpr_write_b32 a205, v114\n"
      "s_add_u32 m0, s62, 8192\n"
      "s_nop 0\n"
      "buffer_load_dwordx4 v120, %[srdk], s60 offen nt lds\n"
      "v_accvgpr_write_b32 a206, v114\n"
      "v_accvgpr_write_b32 a207, v114\n"
      "v_cmp_eq_u32 vcc, 12, v112\n"
      "v_mfma_f32_32x32x16_bf16 a[176:191], v[216:219], v[216:219], 0\n"
      "s_nop 0\n"
      "v_cndmask_b32 v114, 0, v113, vcc\n"
      "v_accvgpr_write_b32 a208, v114\n"
      "v_accvgpr_write_b32 a209, v114\n"
      "s_add_u32 m0, s62, 9088\n"
      "s_nop 0\n"
      "buffer_load_dwordx4 v120, %[srdk], s60 offen offset:128 nt lds\n"
      "s_add_u32 s62, s62, 0x8000\n"
      "s_and_b32 s62, s62, 0x1ffff\n"
      "s_add_u32 s60, s60, s78\n"
      "v_accvgpr_write_b32 a210, v114\n"
      "v_accvgpr_write_b32 a211, v114\n"
      "s_cmp_lt_u32 %[rep], 128\n"
      "s_cselect_b32 s89, 0x2ffe, 0\n"
      "s_or_b32 s89, s89, 0x2aae\n"
      "s_waitcnt vmcnt(11)\n"
      "s_barrier\n"
      "s_memrealtime s[68:69]\n"
      "ds_read_b128 a[64:67], v228 offset:0\n"
      "ds_read_b128 a[68:71], v228 offset:4096\n"
      "ds_read_b128 v[128:131], v224 offset:0\n"
      "ds_read_b128 v[132:135], v224 offset:4096\n"
      "ds_read_b128 v[136:139], v224 offset:8192\n"
      "ds_read_b128 v[140:143], v224 offset:12288\n"
      "ds_read_b128 a[72:75], v229 offset:0\n"
      "ds_read_b128 a[76:79], v229 offset:4096\n"
      "ds_read_b128 v[144:147], v225 offset:0\n"
      "ds_read_b128 v[148:151], v225 offset:4096\n"
      "ds_read_b128 v[152:155], v225 offset:8192\n"
      "ds_read_b128 v[156:159], v225 offset:12288\n"
      "v_lshlrev_b32 v96, 16, v208\n"
      "v_and_b32 v97, 0xffff0000, v208\n"
      "v_lshlrev_b32 v98, 16, v209\n"
      "v_and_b32 v99, 0xffff0000, v209\n"
      "v_lshlrev_b32 v100, 16, v210\n"
      "v_and_b32 v101, 0xffff0000, v210\n"
      "v_lshlrev_b32 v102, 16, v211\n"
      "v_and_b32 v103, 0xffff0000, v211\n"
      "v_lshlrev_b32 v104, 16, v212\n"
      "v_and_b32 v105, 0xffff0000, v212\n"
      "v_lshlrev_b32 v106, 16, v213\n"
      "v_and_b32 v107, 0xffff0000, v213\n"
      "v_lshlrev_b32 v108, 16, v214\n"
      "v_and_b32 v109, 0xffff0000, v214\n"
      "v_lshlrev_b32 v110, 16, v215\n"
      "v_and_b32 v111, 0xffff0000, v215\n"
      "v_fma_f32 v96, -v96, s76, s76\n"
      "v_fma_f32 v97, -v97, s76, s76\n"
      "v_fma_f32 v98, -v98, s76, s76\n"
      "v_fma_f32 v99, -v99, s76, s76\n"
      "v_fma_f32 v100, -v100, s76, s76\n"
      "v_fma_f32 v101, -v101, s76, s76\n"
      "v_fma_f32 v102, -v102, s76, s76\n"
      "v_fma_f32 v103, -v103, s76, s76\n"
      "v_fma_f32 v104, -v104, s76, s76\n"
      "v_fma_f32 v105, -v105, s76, s76\n"
      "v_fma_f32 v106, -v106, s76, s76\n"
      "v_fma_f32 v107, -v107, s76, s76\n"
      "v_fma_f32 v108, -v108, s76, s76\n"
      "v_fma_f32 v109, -v109, s76, s76\n"
      "v_fma_f32 v110, -v110, s76, s76\n"
      "v_fma_f32 v111, -v111, s76, s76\n"
      "s_waitcnt lgkmcnt(0)\n"
      "ds_read_b128 a[80:83], v230 offset:0\n"
      "ds_read_b128 a[84:87], v230 offset:4096\n"
      "ds_read_b128 a[32:35], v226 offset:0\n"
      "ds_read_b128 a[36:39], v226 offset:4096\n"
      "ds_read_b128 a[40:43], v226 offset:8192\n"
      "ds_read_b128 a[44:47], v226 offset:12288\n"
      "ds_read_b128 a[88:91], v231 offset:0\n"
      "ds_read_b128 a[92:95], v231 offset:4096\n"
      "ds_read_b128 a[48:51], v227 offset:0\n"
      "ds_read_b128 a[52:55], v227 offset:4096\n"
      "ds_read_b128 a[56:59], v227 offset:8192\n"
      "ds_read_b128 a[60:63], v227 offset:12288\n"
      "s_cmp_lt_u32 %[lead], 32\n"
      "s_cbranch_scc0 NOLEAD_%=\n"
      "v_mov_b32 v112, s68\n"
      "buffer_store_dword v112, v250, %[srdr], %[lead] offen sc1\n"
      "NOLEAD_%=:\n"
      "v_mfma_f32_16x16x32_bf16 v[160:163], a[64:67], v[128:131], v[96:99]\n"
      "s_mov_b32 s83, 0\n"
      "v_lshlrev_b32 v232, 16, v128\n"
      "v_and_b32 v233, s95, v128\n"
      "v_mfma_f32_16x16x32_bf16 v[168:171], a[64:67], v[132:135], v[104:107]\n"
      "s_sub_u32 s67, s68, s82\n"
      "v_lshlrev_b32 v234, 16, v129\n"
      "v_and_b32 v235, s95, v129\n"
      "v_mul_f32 v232, s94, v232\n"
      "v_mul_f32 v233, s94, v233\n"
      "v_mfma_f32_16x16x32_bf16 v[176:179], a[64:67], v[136:139], v[96:99]\n"
      "s_min_u32 s67, s67, 140\n"
      "v_lshlrev_b32 v236, 16, v130\n"
      "v_and_b32 v237, s95, v130\n"
      "v_mul_f32 v234, s94, v234\n"
      "v_mul_f32 v235, s94, v235\n"
      "v_cvt_pk_bf16_f32 v0, v232, v233\n"
      "v_mfma_f32_16x16x32_bf16 v[184:187], a[64:67], v[140:143], v[104:107]\n"
      "s_add_u32 s66, s82, s67\n"
      "v_lshlrev_b32 v238, 16, v131\n"
      "v_and_b32 v239, s95, v131\n"
      "v_mul_f32 v236, s94, v236\n"
      "v_mul_f32 v237, s94, v237\n"
      "v_cvt_pk_bf16_f32 v1, v234, v235\n"
      "v_mfma_f32_16x16x32_bf16 v[164:167], a[68:71], v[128:131], v[100:103]\n"
      "s_add_u32 s66, s66, 108\n"
      "v_lshlrev_b32 v232, 16, v132\n"
      "v_and_b32 v233, s95, v132\n"
      "v_mul_f32 v238, s94, v238\n"
      "v_mul_f32 v239, s94, v239\n"
      "v_cvt_pk_bf16_f32 v2, v236, v237\n"
      "v_mfma_f32_16x16x32_bf16 v[172:175], a[68:71], v[132:135], v[108:111]\n"
      "s_sub_u32 s71, s68, 400\n"
      "v_lshlrev_b32 v234, 16, v133\n"
      "v_and_b32 v235, s95, v133\n"
      "v_mul_f32 v232, s94, v232\n"
      "v_mul_f32 v233, s94, v233\n"
      "v_cvt_pk_bf16_f32 v3, v238, v239\n"
      "v_mfma_f32_16x16x32_bf16 v[180:183], a[68:71], v[136:139], v[100:103]\n"
      "s_mov_b32 s68, s66\n"
      "v_lshlrev_b32 v236, 16, v134\n"
      "v_and_b32 v237, s95, v134\n"
      "v_mul_f32 v234, s94, v234\n"
      "v_mul_f32 v235, s94, v235\n"
      "v_cvt_pk_bf16_f32 v4, v232, v233\n"
      "v_mfma_f32_16x16x32_bf16 v[188:191], a[68:71], v[140:143], v[108:111]\n"
      "s_mov_b32 s82, s66\n"
      "v_lshlrev_b32 v238, 16, v135\n"
      "v_and_b32 v239, s95, v135\n"
      "v_mul_f32 v236, s94, v236\n"
      "v_mul_f32 v237, s94, v237\n"
      "v_cvt_pk_bf16_f32 v5, v234, v235\n"
      "v_mfma_f32_16x16x32_bf16 v[160:163], a[72:75], v[144:147], v[160:163]\n"
      "v_lshlrev_b32 v232, 16, v136\n"
      "v_and_b32 v233, s95, v136\n"
      "v_mul_f32 v238, s94, v238\n"
      "v_mul_f32 v239, s94, v239\n"
      "v_cvt_pk_bf16_f32 v6, v236, v237\n"
      "v_mfma_f32_16x16x32_bf16 v[168:171], a[72:75], v[148:151], v[168:171]\n"
      "v_lshlrev_b32 v234, 16, v137\n"
      "v_and_b32 v235, s95, v137\n"
      "v_mul_f32 v232, s94, v232\n"
      "v_mul_f32 v233, s94, v233\n"
      "v_cvt_pk_bf16_f32 v7, v238, v239\n"
      "s_waitcnt lgkmcnt(0)\n"
      "s_barrier\n"
      "v_mfma_f32_16x16x32_bf16 v[176:179], a[72:75], v[152:155], v[176:179]\n"
      "v_lshlrev_b32 v236, 16, v138\n"
      "v_and_b32 v237, s95, v138\n"
      "v_mul_f32 v234, s94, v234\n"
      "v_mul_f32 v235, s94, v235\n"
      "v_cvt_pk_bf16_f32 v8, v232, v233\n"
      "ds_write_b128 v224, v[0:3] offset:0\n"
      "v_mfma_f32_16x16x32_bf16 v[184:187], a[72:75], v[156:159], v[184:187]\n"
      "s_add_u32 m0, s62, 0\n"
      "v_lshlrev_b32 v238, 16, v139\n"
      "v_and_b32 v239, s95, v139\n"
      "v_mul_f32 v236, s94, v236\n"
      "v_mul_f32 v237, s94, v237\n"
      "v_cvt_pk_bf16_f32 v9, v234, v235\n"
      "v_mfma_f32_16x16x32_bf16 v[164:167], a[76:79], v[144:147], v[164:167]\n"
      "v_lshlrev_b32 v232, 16, v140\n"
      "v_and_b32 v233, s95, v140\n"
      "v_mul_f32 v238, s94, v238\n"
      "v_mul_f32 v239, s94, v239\n"
      "v_cvt_pk_bf16_f32 v10, v236, v237\n"
      "buffer_load_dwordx4 v248, %[srdk], s60 offen nt lds\n"
      "ds_write_b128 v224, v[4:7] offset:4096\n"
      "v_mfma_f32_16x16x32_bf16 v[172:175], a[76:79], v[148:151], v[172:175]\n"
      "s_add_u32 m0, s62, 896\n"
      "v_lshlrev_b32 v234, 16, v141\n"
      "v_and_b32 v235, s95, v141\n"
      "v_mul_f32 v232, s94, v232\n"
      "v_mul_f32 v233, s94, v233\n"
      "v_cvt_pk_bf16_f32 v11, v238, v239\n"
      "v_mfma_f32_16x16x32_bf16 v[180:183], a[76:79], v[152:155], v[180:183]\n"
      "v_lshlrev_b32 v236, 16, v142\n"
      "v_and_b32 v237, s95, v142\n"
      "v_mul_f32 v234, s94, v234\n"
      "v_mul_f32 v235, s94, v235\n"
      "v_cvt_pk_bf16_f32 v12, v232, v233\n"
      "buffer_load_dwordx4 v248, %[srdk], s60 offen offset:128 nt lds\n"
      "ds_write_b128 v224, v[8:11] offset:8192\n"
      "v_mfma_f32_16x16x32_bf16 v[188:191], a[76:79], v[156:159], v[188:191]\n"
      "s_add_u32 m0, s62, 8192\n"
      "v_lshlrev_b32 v238, 16, v143\n"
      "v_and_b32 v239, s95, v143\n"
      "v_mul_f32 v236, s94, v236\n"
      "v_mul_f32 v237, s94, v237\n"
      "v_cvt_pk_bf16_f32 v13, v234, v235\n"
      "v_mfma_f32_16x16x32_bf16 v[160:163], a[80:83], a[32:35], v[160:163]\n"
      "v_lshlrev_b32 v232, 16, v144\n"
      "v_and_b32 v233, s95, v144\n"
      "v_mul_f32 v238, s94, v238\n"
      "v_mul_f32 v239, s94, v239\n"
      "v_cvt_pk_bf16_f32 v14, v236, v237\n"
      "buffer_load_dwordx4 v120, %[srdk], s60 offen nt lds\n"
      "v_mfma_f32_16x16x32_bf16 v[168:171], a[80:83], a[36:39], v[168:171]\n"
      "s_add_u32 m0, s62, 9088\n"
      "v_lshlrev_b32 v234, 16, v145\n"
      "v_and_b32 v235, s95, v145\n"
      "v_mul_f32 v232, s94, v232\n"
      "v_mul_f32 v233, s94, v233\n"
      "v_cvt_pk_bf16_f32 v15, v238, v239\n"
      "ds_write_b128 v224, v[12:15] offset:12288\n"
      "v_mfma_f32_16x16x32_bf16 v[176:179], a[80:83], a[40:43], v[176:179]\n"
      "s_add_u32 s62, s62, 0x8000\n"
      "v_lshlrev_b32 v236, 16, v146\n"
      "v_and_b32 v237, s95, v146\n"
      "v_mul_f32 v234, s94, v234\n"
      "v_mul_f32 v235, s94, v235\n"
      "v_cvt_pk_bf16_f32 v16, v232, v233\n"
      "buffer_load_dwordx4 v120, %[srdk], s60 offen offset:128 nt lds\n"
      "v_mfma_f32_16x16x32_bf16 v[184:187], a[80:83], a[44:47], v[184:187]\n"
      "s_and_b32 s62, s62, 0x1ffff\n"
      "v_lshlrev_b32 v238, 16, v147\n"
      "v_and_b32 v239, s95, v147\n"
      "v_mul_f32 v236, s94, v236\n"
      "v_mul_f32 v237, s94, v237\n"
      "v_cvt_pk_bf16_f32 v17, v234, v235\n"
      "v_mfma_f32_16x16x32_bf16 v[164:167], a[84:87], a[32:35], v[164:167]\n"
      "s_add_u32 s60, s60, %[inc]\n"
      "v_lshlrev_b32 v232, 16, v148\n"
      "v_and_b32 v233, s95, v148\n"
      "v_mul_f32 v238, s94, v238\n"
      "v_mul_f32 v239, s94, v239\n"
      "v_cvt_pk_bf16_f32 v18, v236, v237\n"
      "v_mfma_f32_16x16x32_bf16 v[172:175], a[84:87], a[36:39], v[172:175]\n"
      "v_lshlrev_b32 v234, 16, v149\n"
      "v_and_b32 v235, s95, v149\n"
      "v_mul_f32 v232, s94, v232\n"
      "v_mul_f32 v233, s94, v233\n"
      "v_cvt_pk_bf16_f32 v19, v238, v239\n"
      "ds_write_b128 v225, v[16:19] offset:0\n"
      "v_mfma_f32_16x16x32_bf16 v[180:183], a[84:87], a[40:43], v[180:183]\n"
      "v_lshlrev_b32 v236, 16, v150\n"
      "v_and_b32 v237, s95, v150\n"
      "v_mul_f32 v234, s94, v234\n"
      "v_mul_f32 v235, s94, v235\n"
      "v_cvt_pk_bf16_f32 v20, v232, v233\n"
      "v_mfma_f32_16x16x32_bf16 v[188:191], a[84:87], a[44:47], v[188:191]\n"
      "v_lshlrev_b32 v238, 16, v151\n"
      "v_and_b32 v239, s95, v151\n"
      "v_mul_f32 v236, s94, v236\n"
      "v_mul_f32 v237, s94, v237\n"
      "v_cvt_pk_bf16_f32 v21, v234, v235\n"
      "v_mfma_f32_16x16x32_bf16 v[160:163], a[88:91], a[48:51], v[160:163]\n"
      "v_lshlrev_b32 v232, 16, v152\n"
      "v_and_b32 v233, s95, v152\n"
      "v_mul_f32 v238, s94, v238\n"
      "v_mul_f32 v239, s94, v239\n"
      "v_cvt_pk_bf16_f32 v22, v236, v237\n"
      "v_mfma_f32_16x16x32_bf16 v[168:171], a[88:91], a[52:55], v[168:171]\n"
      "v_lshlrev_b32 v234, 16, v153\n"
      "v_and_b32 v235, s95, v153\n"
      "v_mul_f32 v232, s94, v232\n"
      "v_mul_f32 v233, s94, v233\n"
      "v_cvt_pk_bf16_f32 v23, v238, v239\n"
      "ds_write_b128 v225, v[20:23] offset:4096\n"
      "v_mfma_f32_16x16x32_bf16 v[176:179], a[88:91], a[56:59], v[176:179]\n"
      "v_lshlrev_b32 v236, 16, v154\n"
      "v_and_b32 v237, s95, v154\n"
      "v_mul_f32 v234, s94, v234\n"
      "v_mul_f32 v235, s94, v235\n"
      "v_cvt_pk_bf16_f32 v24, v232, v233\n"
      "v_mfma_f32_16x16x32_bf16 v[184:187], a[88:91], a[60:63], v[184:187]\n"
      "v_lshlrev_b32 v238, 16, v155\n"
      "v_and_b32 v239, s95, v155\n"
      "v_mul_f32 v236, s94, v236\n"
      "v_mul_f32 v237, s94, v237\n"
      "v_cvt_pk_bf16_f32 v25, v234, v235\n"
      "v_mfma_f32_16x16x32_bf16 v[164:167], a[92:95], a[48:51], v[164:167]\n"
      "v_lshlrev_b32 v232, 16, v156\n"
      "v_and_b32 v233, s95, v156\n"
      "v_mul_f32 v238, s94, v238\n"
      "v_mul_f32 v239, s94, v239\n"
      "v_cvt_pk_bf16_f32 v26, v236, v237\n"
      "v_mfma_f32_16x16x32_bf16 v[172:175], a[92:95], a[52:55], v[172:175]\n"
      "v_lshlrev_b32 v234, 16, v157\n"
      "v_and_b32 v235, s95, v157\n"
      "v_mul_f32 v232, s94, v232\n"
      "v_mul_f32 v233, s94, v233\n"
      "v_cvt_pk_bf16_f32 v27, v238, v239\n"
      "ds_write_b128 v225, v[24:27] offset:8192\n"
      "v_mfma_f32_16x16x32_bf16 v[180:183], a[92:95], a[56:59], v[180:183]\n"
      "v_lshlrev_b32 v236, 16, v158\n"
      "v_and_b32 v237, s95, v158\n"
      "v_mul_f32 v234, s94, v234\n"
      "v_mul_f32 v235, s94, v235\n"
      "v_cvt_pk_bf16_f32 v28, v232, v233\n"
      "v_mfma_f32_16x16x32_bf16 v[188:191], a[92:95], a[60:63], v[188:191]\n"
      "v_lshlrev_b32 v238, 16, v159\n"
      "v_and_b32 v239, s95, v159\n"
      "v_mul_f32 v236, s94, v236\n"
      "v_mul_f32 v237, s94, v237\n"
      "v_cvt_pk_bf16_f32 v29, v234, v235\n"
      "v_mul_f32 v238, s94, v238\n"
      "v_mul_f32 v239, s94, v239\n"
      "v_cvt_pk_bf16_f32 v30, v236, v237\n"
      "v_cvt_pk_bf16_f32 v31, v238, v239\n"
      "ds_write_b128 v225, v[28:31] offset:12288\n"
      "v_max3_f32 v112, v160, v161, v162\n"
      "v_max3_f32 v113, v168, v169, v170\n"
      "v_max3_f32 v114, v176, v177, v178\n"
      "v_max3_f32 v115, v184, v185, v186\n"
      "v_max3_f32 v112, v112, v163, v164\n"
      "v_max3_f32 v113, v113, v171, v172\n"
      "v_max3_f32 v114, v114, v179, v180\n"
      "v_max3_f32 v115, v115, v187, v188\n"
      "v_max3_f32 v112, v112, v165, v166\n"
      "v_max3_f32 v113, v113, v173, v174\n"
      "v_max3_f32 v114, v114, v181, v182\n"
      "v_max3_f32 v115, v115, v189, v190\n"
      "s_waitcnt lgkmcnt(0)\n"
      "s_barrier\n"
      "ds_read_b128 a[0:3], v118 offset:0\n"
      "ds_read_b128 a[4:7], v118 offset:4096\n"
      "ds_read_b128 a[8:11], v118 offset:8192\n"
      "ds_read_b128 a[12:15], v118 offset:12288\n"
      "ds_read_b128 a[16:19], v119 offset:0\n"
      "ds_read_b128 a[20:23], v119 offset:4096\n"
      "ds_read_b128 a[24:27], v119 offset:8192\n"
      "ds_read_b128 a[28:31], v119 offset:12288\n"
      "ds_read_b128 a[32:35], v118 offset:1024\n"
      "ds_read_b128 a[36:39], v118 offset:5120\n"
      "ds_read_b128 a[40:43], v118 offset:9216\n"
      "ds_read_b128 a[44:47], v118 offset:13312\n"
      "v_max_f32 v112, v112, v167\n"
      "v_max_f32 v113, v113, v175\n"
      "v_max_f32 v114, v114, v183\n"
      "v_max_f32 v115, v115, v191\n"
      "v_mov_b32 v208, v112\n"
      "v_mov_b32 v209, v113\n"
      "v_mov_b32 v210, v114\n"
      "v_mov_b32 v211, v115\n"
      "v_permlane16_swap_b32 v112, v208\n"
      "v_permlane16_swap_b32 v113, v209\n"
      "v_permlane16_swap_b32 v114, v210\n"
      "v_permlane16_swap_b32 v115, v211\n"
      "s_waitcnt lgkmcnt(8)\n"
      "ds_read_b128 a[48:51], v119 offset:1024\n"
      "ds_read_b128 a[52:55], v119 offset:5120\n"
      "ds_read_b128 a[56:59], v119 offset:9216\n"
      "ds_read_b128 a[60:63], v119 offset:13312\n"
      "v_max_f32 v112, v112, v208\n"
      "v_max_f32 v113, v113, v209\n"
      "v_max_f32 v114, v114, v210\n"
      "v_max_f32 v115, v115, v211\n"
      "v_mov_b32 v208, v112\n"
      "v_mov_b32 v209, v113\n"
      "v_mov_b32 v210, v114\n"
      "v_mov_b32 v211, v115\n"
      "v_permlane32_swap_b32 v112, v208\n"
      "v_permlane32_swap_b32 v113, v209\n"
      "v_permlane32_swap_b32 v114, v210\n"
      "v_permlane32_swap_b32 v115, v211\n"
      "v_max_f32 v112, v112, v208\n"
      "v_max_f32 v113, v113, v209\n"
      "v_max_f32 v114, v114, v210\n"
      "v_max_f32 v115, v115, v211\n"
      "v_mov_b32 v116, 0xc1800000\n"
      "v_fmamk_f32 v112, v112, 0xbe0293ee, v116\n"
      "v_fmamk_f32 v113, v113, 0xbe0293ee, v116\n"
      "v_fmamk_f32 v114, v114, 0xbe0293ee, v116\n"
      "v_fmamk_f32 v115, v115, 0xbe0293ee, v116\n"
      "v_floor_f32 v112, v112\n"
      "v_floor_f32 v113, v113\n"
      "v_floor_f32 v114, v114\n"
      "v_floor_f32 v115, v115\n"
      "s_waitcnt vmcnt(10) lgkmcnt(0)\n"
      "s_barrier\n"
      "ds_read_b128 a[64:67], v242 offset:0\n"
      "ds_read_b128 a[68:71], v242 offset:4096\n"
      "ds_read_b128 a[72:75], v243 offset:0\n"
      "ds_read_b128 a[76:79], v243 offset:4096\n"
      "ds_read_b128 a[80:83], v242 offset:1024\n"
      "ds_read_b128 a[84:87], v242 offset:5120\n"
      "ds_read_b128 a[88:91], v243 offset:1024\n"
      "ds_read_b128 a[92:95], v243 offset:5120\n"
      "v_min_f32 v224, 0x7f000000, v112\n"
      "v_min_f32 v228, 0x7f000000, v113\n"
      "v_min_f32 v232, 0x7f000000, v114\n"
      "v_min_f32 v236, 0x7f000000, v115\n"
      "v_add_f32 v112, %[neg], v224\n"
      "v_add_f32 v113, %[neg], v228\n"
      "v_add_f32 v114, %[neg], v232\n"
      "v_add_f32 v115, %[neg], v236\n"
      "v_mov_b32 v225, v224\n"
      "v_mov_b32 v229, v228\n"
      "v_mov_b32 v233, v232\n"
      "v_mov_b32 v237, v236\n"
      "v_mov_b64 v[226:227], v[224:225]\n"
      "v_mov_b64 v[230:231], v[228:229]\n"
      "v_mov_b64 v[234:235], v[232:233]\n"
      "v_mov_b64 v[238:239], v[236:237]\n"
      "s_mov_b32 s95, s94\n"
      "v_readfirstlane_b32 s72, v240\n"
      "v_readfirstlane_b32 s79, v241\n"
      "s_add_u32 m0, s77, 16384\n"
      "v_pk_fma_f32 v[160:161], v[160:161], s[94:95], v[112:113] op_sel:[0,0,0] op_sel_hi:[1,1,0]\n"
      "buffer_load_dwordx4 v249, %[srdv], s61 offen nt lds\n"
      "v_pk_fma_f32 v[162:163], v[162:163], s[94:95], v[112:113] op_sel:[0,0,0] op_sel_hi:[1,1,0]\n"
      "v_pk_fma_f32 v[164:165], v[164:165], s[94:95], v[112:113] op_sel:[0,0,0] op_sel_hi:[1,1,0]\n"
      "v_pk_fma_f32 v[166:167], v[166:167], s[94:95], v[112:113] op_sel:[0,0,0] op_sel_hi:[1,1,0]\n"
      "s_add_u32 m0, s77, 17408\n"
      "v_pk_fma_f32 v[168:169], v[168:169], s[94:95], v[112:113] op_sel:[0,0,1] op_sel_hi:[1,1,1]\n"
      "buffer_load_dwordx4 v121, %[srdv], s61 offen nt lds\n"
      "v_pk_fma_f32 v[170:171], v[170:171], s[94:95], v[112:113] op_sel:[0,0,1] op_sel_hi:[1,1,1]\n"
      "v_pk_fma_f32 v[172:173], v[172:173], s[94:95], v[112:113] op_sel:[0,0,1] op_sel_hi:[1,1,1]\n"
      "v_pk_fma_f32 v[174:175], v[174:175], s[94:95], v[112:113] op_sel:[0,0,1] op_sel_hi:[1,1,1]\n"
      "s_add_u32 m0, s77, 24576\n"
      "v_pk_fma_f32 v[176:177], v[176:177], s[94:95], v[114:115] op_sel:[0,0,0] op_sel_hi:[1,1,0]\n"
      "buffer_load_dwordx4 v122, %[srdv], s61 offen nt lds\n"
      "v_pk_fma_f32 v[178:179], v[178:179], s[94:95], v[114:115] op_sel:[0,0,0] op_sel_hi:[1,1,0]\n"
      "v_pk_fma_f32 v[180:181], v[180:181], s[94:95], v[114:115] op_sel:[0,0,0] op_sel_hi:[1,1,0]\n"
      "v_pk_fma_f32 v[182:183], v[182:183], s[94:95], v[114:115] op_sel:[0,0,0] op_sel_hi:[1,1,0]\n"
      "s_add_u32 m0, s77, 25600\n"
      "v_pk_fma_f32 v[184:185], v[184:185], s[94:95], v[114:115] op_sel:[0,0,1] op_sel_hi:[1,1,1]\n"
      "buffer_load_dwordx4 v123, %[srdv], s61 offen nt lds\n"
      "v_pk_fma_f32 v[186:187], v[186:187], s[94:95], v[114:115] op_sel:[0,0,1] op_sel_hi:[1,1,1]\n"
      "v_pk_fma_f32 v[188:189], v[188:189], s[94:95], v[114:115] op_sel:[0,0,1] op_sel_hi:[1,1,1]\n"
      "v_pk_fma_f32 v[190:191], v[190:191], s[94:95], v[114:115] op_sel:[0,0,1] op_sel_hi:[1,1,1]\n"
      "s_add_u32 s77, s77, 0x8000\n"
      "s_and_b32 s77, s77, 0x1ffff\n"
      "s_add_u32 s61, s61, %[inc]\n"
      "v_exp_f32 v112, v160\n"
      "v_exp_f32 v113, v161\n"
      "v_exp_f32 v114, v162\n"
      "v_cvt_pk_bf16_f32 v208, v112, v113\n"
      "v_exp_f32 v115, v163\n"
      "v_exp_f32 v112, v164\n"
      "v_cvt_pk_bf16_f32 v209, v114, v115\n"
      "v_exp_f32 v113, v165\n"
      "v_or_b32 v116, v208, v209\n"
      "v_exp_f32 v114, v166\n"
      "v_cvt_pk_bf16_f32 v210, v112, v113\n"
      "v_exp_f32 v115, v167\n"
      "v_exp_f32 v112, v168\n"
      "v_cvt_pk_bf16_f32 v211, v114, v115\n"
      "v_exp_f32 v113, v169\n"
      "v_or3_b32 v116, v116, v210, v211\n"
      "v_exp_f32 v114, v170\n"
      "v_cvt_pk_bf16_f32 v212, v112, v113\n"
      "v_exp_f32 v115, v171\n"
      "v_exp_f32 v112, v172\n"
      "v_cvt_pk_bf16_f32 v213, v114, v115\n"
      "v_exp_f32 v113, v173\n"
      "v_or3_b32 v116, v116, v212, v213\n"
      "s_waitcnt lgkmcnt(0)\n"
      "v_cvt_pk_bf16_f32 v214, v112, v113\n"
      "s_sub_u32 s63, %[n1], 1\n"
      "s_branch ENTRY_%=\n"
      "LOOP1_%=:\n"
      "s_waitcnt vmcnt(12) lgkmcnt(0)\n"
      "s_sub_u32 s67, s68, s90\n"
      "s_cmp_lt_i32 s67, 0\n"
      "s_cbranch_scc1 GW0_%=\n"
      "GX0_%=:\n"
      "s_barrier\n"
      "v_mfma_f32_16x16x32_bf16 v[40:43], a[96:99], v[216:219], v[40:43]\n"
      "s_memrealtime s[68:69]\n"
      "buffer_load_dword v250, v251, %[srdr], 0 offen sc1\n"
      "ds_read_b128 a[64:67], v242 offset:0\n"
      "v_exp_f32 v114, v166\n"
      "v_mfma_f32_16x16x32_bf16 v[44:47], a[96:99], v[220:223], v[44:47]\n"
      "s_add_u32 m0, s77, 16384\n"
      "ds_read_b128 a[68:71], v242 offset:4096\n"
      "ds_read_b128 a[72:75], v243 offset:0\n"
      "v_mfma_f32_16x16x32_bf16 v[60:63], a[100:103], v[220:223], v[60:63]\n"
      "buffer_load_dwordx4 v249, %[srdv], s61 offen nt lds\n"
      "v_exp_f32 v115, v167\n"
      "v_mfma_f32_16x16x32_bf16 v[56:59], a[100:103], v[216:219], v[56:59]\n"
      "s_add_u32 m0, s77, 17408\n"
      "ds_read_b128 a[76:79], v243 offset:4096\n"
      "v_cvt_pk_bf16_f32 v211, v114, v115\n"
      "v_mfma_f32_16x16x32_bf16 a[136:139], a[104:107], v[216:219], a[136:139]\n"
      "buffer_load_dwordx4 v121, %[srdv], s61 offen nt lds\n"
      "v_exp_f32 v112, v168\n"
      "v_mfma_f32_16x16x32_bf16 a[140:143], a[104:107], v[220:223], a[140:143]\n"
      "s_add_u32 m0, s77, 24576\n"
      "ds_read_b128 a[80:83], v242 offset:1024\n"
      "v_or3_b32 v116, v116, v210, v211\n"
      "v_mfma_f32_16x16x32_bf16 a[156:159], a[108:111], v[220:223], a[156:159]\n"
      "buffer_load_dwordx4 v122, %[srdv], s61 offen nt lds\n"
      "v_exp_f32 v113, v169\n"
      "v_mfma_f32_16x16x32_bf16 a[152:155], a[108:111], v[216:219], a[152:155]\n"
      "s_add_u32 m0, s77, 25600\n"
      "ds_read_b128 a[84:87], v242 offset:5120\n"
      "v_cvt_pk_bf16_f32 v212, v112, v113\n"
      "v_mfma_f32_16x16x32_bf16 v[72:75], a[112:115], v[216:219], v[72:75]\n"
      "s_add_u32 s77, s77, 0x8000\n"
      "buffer_load_dwordx4 v123, %[srdv], s61 offen nt lds\n"
      "v_exp_f32 v114, v170\n"
      "v_mfma_f32_16x16x32_bf16 v[76:79], a[112:115], v[220:223], v[76:79]\n"
      "s_and_b32 s77, s77, 0x1ffff\n"
      "ds_read_b128 a[88:91], v243 offset:1024\n"
      "ds_read_b128 a[92:95], v243 offset:5120\n"
      "v_mfma_f32_16x16x32_bf16 v[92:95], a[116:119], v[220:223], v[92:95]\n"
      "s_add_u32 s61, s61, %[inc]\n"
      "v_exp_f32 v115, v171\n"
      "v_mfma_f32_16x16x32_bf16 v[88:91], a[116:119], v[216:219], v[88:91]\n"
      "v_cvt_pk_bf16_f32 v213, v114, v115\n"
      "v_mfma_f32_16x16x32_bf16 a[168:171], a[120:123], v[216:219], a[168:171]\n"
      "v_exp_f32 v112, v172\n"
      "v_mfma_f32_16x16x32_bf16 a[172:175], a[120:123], v[220:223], a[172:175]\n"
      "v_or3_b32 v116, v116, v212, v213\n"
      "v_mfma_f32_16x16x32_bf16 a[188:191], a[124:127], v[220:223], a[188:191]\n"
      "v_exp_f32 v113, v173\n"
      "v_mfma_f32_16x16x32_bf16 a[184:187], a[124:127], v[216:219], a[184:187]\n"
      "s_waitcnt lgkmcnt(0)\n"
      "v_cvt_pk_bf16_f32 v214, v112, v113\n"
      "ENTRY_%=:\n"
      "v_mfma_f32_16x16x32_bf16 v[128:131], a[64:67], a[0:3], v[224:227]\n"
      "s_sub_u32 s67, s68, s66\n"
      "v_exp_f32 v114, v174\n"
      "v_mfma_f32_16x16x32_bf16 v[128:131], a[72:75], a[16:19], v[128:131]\n"
      "s_cmp_gt_i32 s67, s70\n"
      "ds_read_b64_tr_b16 a[96:97], v244 offset:0\n"
      "ds_read_b64_tr_b16 a[98:99], v244 offset:4096\n"
      "v_mfma_f32_16x16x32_bf16 v[128:131], a[80:83], a[32:35], v[128:131]\n"
      "s_cselect_b32 s69, s86, 0\n"
      "v_exp_f32 v115, v175\n"
      "v_mfma_f32_16x16x32_bf16 v[128:131], a[88:91], a[48:51], v[128:131]\n"
      "s_add_u32 s66, s66, s69\n"
      "ds_read_b64_tr_b16 a[100:101], v245 offset:0\n"
      "v_cvt_pk_bf16_f32 v215, v114, v115\n"
      "v_mfma_f32_16x16x32_bf16 v[132:135], a[92:95], a[48:51], v[224:227]\n"
      "s_add_u32 s67, s67, s101\n"
      "v_exp_f32 v112, v176\n"
      "v_mfma_f32_16x16x32_bf16 v[132:135], a[84:87], a[32:35], v[132:135]\n"
      "s_min_i32 s70, s70, s67\n"
      "ds_read_b64_tr_b16 a[102:103], v245 offset:4096\n"
      "v_or3_b32 v116, v116, v214, v215\n"
      "v_mfma_f32_16x16x32_bf16 v[132:135], a[76:79], a[16:19], v[132:135]\n"
      "s_max_i32 s70, s70, 85\n"
      "v_exp_f32 v113, v177\n"
      "v_mfma_f32_16x16x32_bf16 v[132:135], a[68:71], a[0:3], v[132:135]\n"
      "s_add_u32 s66, s66, s86\n"
      "ds_read_b64_tr_b16 a[104:105], v246 offset:0\n"
      "v_cvt_pk_bf16_f32 v216, v112, v113\n"
      "v_mfma_f32_16x16x32_bf16 v[140:143], a[68:71], a[4:7], v[228:231]\n"
      "s_and_b32 s76, s80, 31\n"
      "v_exp_f32 v114, v178\n"
      "v_mfma_f32_16x16x32_bf16 v[140:143], a[76:79], a[20:23], v[140:143]\n"
      "s_lshr_b64 s[80:81], s[80:81], 5\n"
      "ds_read_b64_tr_b16 a[106:107], v246 offset:4096\n"
      "ds_read_b64_tr_b16 a[108:109], v247 offset:0\n"
      "v_mfma_f32_16x16x32_bf16 v[140:143], a[84:87], a[36:39], v[140:143]\n"
      "s_add_u32 s66, s66, s76\n"
      "v_exp_f32 v115, v179\n"
      "v_mfma_f32_16x16x32_bf16 v[140:143], a[92:95], a[52:55], v[140:143]\n"
      "s_sub_u32 s67, s68, s82\n"
      "ds_read_b64_tr_b16 a[110:111], v247 offset:4096\n"
      "v_cvt_pk_bf16_f32 v217, v114, v115\n"
      "v_mfma_f32_16x16x32_bf16 v[136:139], a[88:91], a[52:55], v[228:231]\n"
      "s_min_i32 s76, s67, s83\n"
      "v_exp_f32 v112, v180\n"
      "v_mfma_f32_16x16x32_bf16 v[136:139], a[80:83], a[36:39], v[136:139]\n"
      "s_mov_b32 s83, s67\n"
      "ds_read_b64_tr_b16 a[112:113], v244 offset:1024\n"
      "v_or3_b32 v116, v116, v216, v217\n"
      "v_mfma_f32_16x16x32_bf16 v[136:139], a[72:75], a[20:23], v[136:139]\n"
      "s_sub_u32 s76, s76, 36\n"
      "v_exp_f32 v113, v181\n"
      "v_mfma_f32_16x16x32_bf16 v[136:139], a[64:67], a[4:7], v[136:139]\n"
      "s_min_i32 s76, s76, 148\n"
      "ds_read_b64_tr_b16 a[114:115], v244 offset:5120\n"
      "v_cvt_pk_bf16_f32 v218, v112, v113\n"
      "v_mfma_f32_16x16x32_bf16 v[144:147], a[64:67], a[8:11], v[232:235]\n"
      "s_max_i32 s76, s76, 70\n"
      "v_exp_f32 v114, v182\n"
      "v_mfma_f32_16x16x32_bf16 v[144:147], a[72:75], a[24:27], v[144:147]\n"
      "s_mov_b32 s82, s68\n"
      "ds_read_b64_tr_b16 a[116:117], v245 offset:1024\n"
      "ds_read_b64_tr_b16 a[118:119], v245 offset:5120\n"
      "v_mfma_f32_16x16x32_bf16 v[144:147], a[80:83], a[40:43], v[144:147]\n"
      "s_add_u32 s65, s82, s76\n"
      "v_exp_f32 v115, v183\n"
      "v_mfma_f32_16x16x32_bf16 v[144:147], a[88:91], a[56:59], v[144:147]\n"
      "s_sub_u32 s93, s93, 1\n"
      "ds_read_b64_tr_b16 a[120:121], v246 offset:1024\n"
      "v_cvt_pk_bf16_f32 v219, v114, v115\n"
      "v_mfma_f32_16x16x32_bf16 v[148:151], a[92:95], a[56:59], v[232:235]\n"
      "s_cmp_le_i32 s93, 14\n"
      "v_exp_f32 v112, v184\n"
      "v_mfma_f32_16x16x32_bf16 v[148:151], a[84:87], a[40:43], v[148:151]\n"
      "s_cselect_b32 s86, 107, s86\n"
      "ds_read_b64_tr_b16 a[122:123], v246 offset:5120\n"
      "v_or3_b32 v116, v116, v218, v219\n"
      "v_mfma_f32_16x16x32_bf16 v[148:151], a[76:79], a[24:27], v[148:151]\n"
      "s_sub_u32 s67, s65, s66\n"
      "v_exp_f32 v113, v185\n"
      "v_mfma_f32_16x16x32_bf16 v[148:151], a[68:71], a[8:11], v[148:151]\n"
      "s_cmp_lt_i32 s67, 0\n"
      "ds_read_b64_tr_b16 a[124:125], v247 offset:1024\n"
      "v_cvt_pk_bf16_f32 v220, v112, v113\n"
      "v_mfma_f32_16x16x32_bf16 v[156:159], a[68:71], a[12:15], v[236:239]\n"
      "s_cselect_b32 s88, s66, s65\n"
      "v_exp_f32 v114, v186\n"
      "v_mfma_f32_16x16x32_bf16 v[156:159], a[76:79], a[28:31], v[156:159]\n"
      "s_add_u32 s90, s88, s91\n"
      "ds_read_b64_tr_b16 a[126:127], v247 offset:5120\n"
      "v_mfma_f32_16x16x32_bf16 v[156:159], a[84:87], a[44:47], v[156:159]\n"
      "v_exp_f32 v115, v187\n"
      "v_mfma_f32_16x16x32_bf16 v[156:159], a[92:95], a[60:63], v[156:159]\n"
      "v_cvt_pk_bf16_f32 v221, v114, v115\n"
      "v_mfma_f32_16x16x32_bf16 v[152:155], a[88:91], a[60:63], v[236:239]\n"
      "v_exp_f32 v112, v188\n"
      "v_mfma_f32_16x16x32_bf16 v[152:155], a[80:83], a[44:47], v[152:155]\n"
      "v_or3_b32 v116, v116, v220, v221\n"
      "v_mfma_f32_16x16x32_bf16 v[152:155], a[72:75], a[28:31], v[152:155]\n"
      "v_exp_f32 v113, v189\n"
      "v_mfma_f32_16x16x32_bf16 v[152:155], a[64:67], a[12:15], v[152:155]\n"
      "s_waitcnt lgkmcnt(10)\n"
      "v_cvt_pk_bf16_f32 v222, v112, v113\n"
      "v_mfma_f32_16x16x32_bf16 v[32:35], a[96:99], v[192:195], v[32:35]\n"
      "v_exp_f32 v114, v190\n"
      "v_mfma_f32_16x16x32_bf16 v[48:51], a[100:103], v[192:195], v[48:51]\n"
      "v_mfma_f32_16x16x32_bf16 a[128:131], a[104:107], v[192:195], a[128:131]\n"
      "s_waitcnt lgkmcnt(6)\n"
      "v_exp_f32 v115, v191\n"
      "v_mfma_f32_16x16x32_bf16 a[144:147], a[108:111], v[192:195], a[144:147]\n"
      "v_cvt_pk_bf16_f32 v223, v114, v115\n"
      "v_mfma_f32_16x16x32_bf16 v[64:67], a[112:115], v[192:195], v[64:67]\n"
      "s_waitcnt lgkmcnt(2)\n"
      "v_mfma_f32_16x16x32_bf16 v[80:83], a[116:119], v[192:195], v[80:83]\n"
      "v_or3_b32 v116, v116, v222, v223\n"
      "v_mfma_f32_16x16x32_bf16 a[160:163], a[120:123], v[192:195], a[160:163]\n"
      "s_mov_b32 s64, 0\n"
      "s_waitcnt lgkmcnt(0)\n"
      "v_and_b32 v116, 0x40004000, v116\n"
      "v_mfma_f32_16x16x32_bf16 a[176:179], a[124:127], v[192:195], a[176:179]\n"
      "v_cmp_ne_u32 vcc, 0, v116\n"
      "s_cbranch_vccnz FB1_%=\n"
      "RET1_0_%=:\n"
      "v_mfma_f32_16x16x32_bf16 a[180:183], a[124:127], v[196:199], a[180:183]\n"
      "v_exp_f32 v112, v128\n"
      "s_barrier\n"
      "v_mfma_f32_16x16x32_bf16 a[164:167], a[120:123], v[196:199], a[164:167]\n"
      "v_mfma_f32_16x16x32_bf16 a[192:195], a[196:199], v[208:211], a[192:195]\n"
      "s_add_u32 m0, s62, 0\n"
      "v_exp_f32 v113, v129\n"
      "v_mfma_f32_16x16x32_bf16 v[84:87], a[116:119], v[196:199], v[84:87]\n"
      "buffer_load_dwordx4 v248, %[srdk], s60 offen nt lds\n"
      "v_cvt_pk_bf16_f32 v192, v112, v113\n"
      "v_mfma_f32_16x16x32_bf16 v[68:71], a[112:115], v[196:199], v[68:71]\n"
      "s_add_u32 m0, s62, 896\n"
      "v_exp_f32 v114, v130\n"
      "v_mfma_f32_16x16x32_bf16 a[192:195], a[200:203], v[212:215], a[192:195]\n"
      "buffer_load_dwordx4 v248, %[srdk], s60 offen offset:128 nt lds\n"
      "v_mfma_f32_16x16x32_bf16 a[148:151], a[108:111], v[196:199], a[148:151]\n"
      "s_add_u32 m0, s62, 8192\n"
      "v_exp_f32 v115, v131\n"
      "v_mfma_f32_16x16x32_bf16 a[132:135], a[104:107], v[196:199], a[132:135]\n"
      "s_memrealtime s[68:69]\n"
      "buffer_load_dwordx4 v120, %[srdk], s60 offen nt lds\n"
      "v_cvt_pk_bf16_f32 v193, v114, v115\n"
      "v_mfma_f32_16x16x32_bf16 a[192:195], a[204:207], v[216:219], a[192:195]\n"
      "s_add_u32 m0, s62, 9088\n"
      "v_exp_f32 v112, v132\n"
      "v_mfma_f32_16x16x32_bf16 v[52:55], a[100:103], v[196:199], v[52:55]\n"
      "s_add_u32 s62, s62, 0x8000\n"
      "buffer_load_dwordx4 v120, %[srdk], s60 offen offset:128 nt lds\n"
      "v_or_b32 v116, v192, v193\n"
      "v_mfma_f32_16x16x32_bf16 v[36:39], a[96:99], v[196:199], v[36:39]\n"
      "s_and_b32 s62, s62, 0x1ffff\n"
      "v_exp_f32 v113, v133\n"
      "v_mfma_f32_16x16x32_bf16 a[192:195], a[208:211], v[220:223], a[192:195]\n"
      "s_add_u32 s60, s60, s78\n"
      "v_cvt_pk_bf16_f32 v194, v112, v113\n"
      "s_waitcnt vmcnt(12) lgkmcnt(0)\n"
      "s_sub_u32 s67, s68, s90\n"
      "s_cmp_lt_i32 s67, 0\n"
      "s_cbranch_scc1 GW1_%=\n"
      "GX1_%=:\n"
      "s_barrier\n"
      "v_mfma_f32_16x16x32_bf16 v[40:43], a[96:99], v[200:203], v[40:43]\n"
      "s_memrealtime s[68:69]\n"
      "ds_read_b128 a[64:67], v242 offset:32768\n"
      "v_exp_f32 v114, v134\n"
      "v_mfma_f32_16x16x32_bf16 v[44:47], a[96:99], v[204:207], v[44:47]\n"
      "s_add_u32 m0, s77, 16384\n"
      "ds_read_b128 a[68:71], v242 offset:36864\n"
      "ds_read_b128 a[72:75], v243 offset:32768\n"
      "v_mfma_f32_16x16x32_bf16 v[60:63], a[100:103], v[204:207], v[60:63]\n"
      "buffer_load_dwordx4 v249, %[srdv], s61 offen nt lds\n"
      "v_exp_f32 v115, v135\n"
      "v_mfma_f32_16x16x32_bf16 v[56:59], a[100:103], v[200:203], v[56:59]\n"
      "s_add_u32 m0, s77, 17408\n"
      "ds_read_b128 a[76:79], v243 offset:36864\n"
      "v_cvt_pk_bf16_f32 v195, v114, v115\n"
      "v_mfma_f32_16x16x32_bf16 a[136:139], a[104:107], v[200:203], a[136:139]\n"
      "buffer_load_dwordx4 v121, %[srdv], s61 offen nt lds\n"
      "v_exp_f32 v112, v136\n"
      "v_mfma_f32_16x16x32_bf16 a[140:143], a[104:107], v[204:207], a[140:143]\n"
      "s_add_u32 m0, s77, 24576\n"
      "ds_read_b128 a[80:83], v242 offset:33792\n"
      "v_or3_b32 v116, v116, v194, v195\n"
      "v_mfma_f32_16x16x32_bf16 a[156:159], a[108:111], v[204:207], a[156:159]\n"
      "buffer_load_dwordx4 v122, %[srdv], s61 offen nt lds\n"
      "v_exp_f32 v113, v137\n"
      "v_mfma_f32_16x16x32_bf16 a[152:155], a[108:111], v[200:203], a[152:155]\n"
      "s_add_u32 m0, s77, 25600\n"
      "ds_read_b128 a[84:87], v242 offset:37888\n"
      "v_cvt_pk_bf16_f32 v196, v112, v113\n"
      "v_mfma_f32_16x16x32_bf16 v[72:75], a[112:115], v[200:203], v[72:75]\n"
      "s_add_u32 s77, s77, 0x8000\n"
      "buffer_load_dwordx4 v123, %[srdv], s61 offen nt lds\n"
      "v_exp_f32 v114, v138\n"
      "v_mfma_f32_16x16x32_bf16 v[76:79], a[112:115], v[204:207], v[76:79]\n"
      "s_and_b32 s77, s77, 0x1ffff\n"
      "ds_read_b128 a[88:91], v243 offset:33792\n"
      "ds_read_b128 a[92:95], v243 offset:37888\n"
      "v_mfma_f32_16x16x32_bf16 v[92:95], a[116:119], v[204:207], v[92:95]\n"
      "s_add_u32 s61, s61, s78\n"
      "v_exp_f32 v115, v139\n"
      "v_mfma_f32_16x16x32_bf16 v[88:91], a[116:119], v[200:203], v[88:91]\n"
      "v_cvt_pk_bf16_f32 v197, v114, v115\n"
      "v_mfma_f32_16x16x32_bf16 a[168:171], a[120:123], v[200:203], a[168:171]\n"
      "v_exp_f32 v112, v140\n"
      "v_mfma_f32_16x16x32_bf16 a[172:175], a[120:123], v[204:207], a[172:175]\n"
      "v_or3_b32 v116, v116, v196, v197\n"
      "v_mfma_f32_16x16x32_bf16 a[188:191], a[124:127], v[204:207], a[188:191]\n"
      "v_exp_f32 v113, v141\n"
      "v_mfma_f32_16x16x32_bf16 a[184:187], a[124:127], v[200:203], a[184:187]\n"
      "s_waitcnt lgkmcnt(0)\n"
      "v_cvt_pk_bf16_f32 v198, v112, v113\n"
      "v_mfma_f32_16x16x32_bf16 v[160:163], a[64:67], a[0:3], v[224:227]\n"
      "s_sub_u32 s67, s68, s66\n"
      "v_exp_f32 v114, v142\n"
      "v_mfma_f32_16x16x32_bf16 v[160:163], a[72:75], a[16:19], v[160:163]\n"
      "s_cmp_gt_i32 s67, s70\n"
      "ds_read_b64_tr_b16 a[96:97], v244 offset:32768\n"
      "ds_read_b64_tr_b16 a[98:99], v244 offset:36864\n"
      "v_mfma_f32_16x16x32_bf16 v[160:163], a[80:83], a[32:35], v[160:163]\n"
      "s_cselect_b32 s69, s86, 0\n"
      "v_exp_f32 v115, v143\n"
      "v_mfma_f32_16x16x32_bf16 v[160:163], a[88:91], a[48:51], v[160:163]\n"
      "s_add_u32 s66, s66, s69\n"
      "ds_read_b64_tr_b16 a[100:101], v245 offset:32768\n"
      "v_cvt_pk_bf16_f32 v199, v114, v115\n"
      "v_mfma_f32_16x16x32_bf16 v[164:167], a[92:95], a[48:51], v[224:227]\n"
      "s_add_u32 s67, s67, s101\n"
      "v_exp_f32 v112, v144\n"
      "v_mfma_f32_16x16x32_bf16 v[164:167], a[84:87], a[32:35], v[164:167]\n"
      "s_min_i32 s70, s70, s67\n"
      "ds_read_b64_tr_b16 a[102:103], v245 offset:36864\n"
      "v_or3_b32 v116, v116, v198, v199\n"
      "v_mfma_f32_16x16x32_bf16 v[164:167], a[76:79], a[16:19], v[164:167]\n"
      "s_max_i32 s70, s70, 85\n"
      "v_exp_f32 v113, v145\n"
      "v_mfma_f32_16x16x32_bf16 v[164:167], a[68:71], a[0:3], v[164:167]\n"
      "s_add_u32 s66, s66, s86\n"
      "ds_read_b64_tr_b16 a[104:105], v246 offset:32768\n"
      "v_cvt_pk_bf16_f32 v200, v112, v113\n"
      "v_mfma_f32_16x16x32_bf16 v[172:175], a[68:71], a[4:7], v[228:231]\n"
      "s_and_b32 s76, s80, 31\n"
      "v_exp_f32 v114, v146\n"
      "v_mfma_f32_16x16x32_bf16 v[172:175], a[76:79], a[20:23], v[172:175]\n"
      "s_lshr_b64 s[80:81], s[80:81], 5\n"
      "ds_read_b64_tr_b16 a[106:107], v246 offset:36864\n"
      "ds_read_b64_tr_b16 a[108:109], v247 offset:32768\n"
      "v_mfma_f32_16x16x32_bf16 v[172:175], a[84:87], a[36:39], v[172:175]\n"
      "s_add_u32 s66, s66, s76\n"
      "v_exp_f32 v115, v147\n"
      "v_mfma_f32_16x16x32_bf16 v[172:175], a[92:95], a[52:55], v[172:175]\n"
      "s_sub_u32 s67, s68, s82\n"
      "ds_read_b64_tr_b16 a[110:111], v247 offset:36864\n"
      "v_cvt_pk_bf16_f32 v201, v114, v115\n"
      "v_mfma_f32_16x16x32_bf16 v[168:171], a[88:91], a[52:55], v[228:231]\n"
      "s_min_i32 s76, s67, s83\n"
      "v_exp_f32 v112, v148\n"
      "v_mfma_f32_16x16x32_bf16 v[168:171], a[80:83], a[36:39], v[168:171]\n"
      "s_mov_b32 s83, s67\n"
      "ds_read_b64_tr_b16 a[112:113], v244 offset:33792\n"
      "v_or3_b32 v116, v116, v200, v201\n"
      "v_mfma_f32_16x16x32_bf16 v[168:171], a[72:75], a[20:23], v[168:171]\n"
      "s_sub_u32 s76, s76, 36\n"
      "v_exp_f32 v113, v149\n"
      "v_mfma_f32_16x16x32_bf16 v[168:171], a[64:67], a[4:7], v[168:171]\n"
      "s_min_i32 s76, s76, 148\n"
      "ds_read_b64_tr_b16 a[114:115], v244 offset:37888\n"
      "v_cvt_pk_bf16_f32 v202, v112, v113\n"
      "v_mfma_f32_16x16x32_bf16 v[176:179], a[64:67], a[8:11], v[232:235]\n"
      "s_max_i32 s76, s76, 70\n"
      "v_exp_f32 v114, v150\n"
      "v_mfma_f32_16x16x32_bf16 v[176:179], a[72:75], a[24:27], v[176:179]\n"
      "s_mov_b32 s82, s68\n"
      "ds_read_b64_tr_b16 a[116:117], v245 offset:33792\n"
      "ds_read_b64_tr_b16 a[118:119], v245 offset:37888\n"
      "v_mfma_f32_16x16x32_bf16 v[176:179], a[80:83], a[40:43], v[176:179]\n"
      "s_add_u32 s65, s82, s76\n"
      "v_exp_f32 v115, v151\n"
      "v_mfma_f32_16x16x32_bf16 v[176:179], a[88:91], a[56:59], v[176:179]\n"
      "s_sub_u32 s93, s93, 1\n"
      "ds_read_b64_tr_b16 a[120:121], v246 offset:33792\n"
      "v_cvt_pk_bf16_f32 v203, v114, v115\n"
      "v_mfma_f32_16x16x32_bf16 v[180:183], a[92:95], a[56:59], v[232:235]\n"
      "s_cmp_le_i32 s93, 14\n"
      "v_exp_f32 v112, v152\n"
      "v_mfma_f32_16x16x32_bf16 v[180:183], a[84:87], a[40:43], v[180:183]\n"
      "s_cselect_b32 s86, 107, s86\n"
      "ds_read_b64_tr_b16 a[122:123], v246 offset:37888\n"
      "v_or3_b32 v116, v116, v202, v203\n"
      "v_mfma_f32_16x16x32_bf16 v[180:183], a[76:79], a[24:27], v[180:183]\n"
      "s_sub_u32 s67, s65, s66\n"
      "v_exp_f32 v113, v153\n"
      "v_mfma_f32_16x16x32_bf16 v[180:183], a[68:71], a[8:11], v[180:183]\n"
      "s_cmp_lt_i32 s67, 0\n"
      "ds_read_b64_tr_b16 a[124:125], v247 offset:33792\n"
      "v_cvt_pk_bf16_f32 v204, v112, v113\n"
      "v_mfma_f32_16x16x32_bf16 v[188:191], a[68:71], a[12:15], v[236:239]\n"
      "s_cselect_b32 s88, s66, s65\n"
      "v_exp_f32 v114, v154\n"
      "v_mfma_f32_16x16x32_bf16 v[188:191], a[76:79], a[28:31], v[188:191]\n"
      "s_add_u32 s90, s88, s91\n"
      "ds_read_b64_tr_b16 a[126:127], v247 offset:37888\n"
      "v_mfma_f32_16x16x32_bf16 v[188:191], a[84:87], a[44:47], v[188:191]\n"
      "v_exp_f32 v115, v155\n"
      "v_mfma_f32_16x16x32_bf16 v[188:191], a[92:95], a[60:63], v[188:191]\n"
      "v_cvt_pk_bf16_f32 v205, v114, v115\n"
      "v_mfma_f32_16x16x32_bf16 v[184:187], a[88:91], a[60:63], v[236:239]\n"
      "v_exp_f32 v112, v156\n"
      "v_mfma_f32_16x16x32_bf16 v[184:187], a[80:83], a[44:47], v[184:187]\n"
      "v_or3_b32 v116, v116, v204, v205\n"
      "v_mfma_f32_16x16x32_bf16 v[184:187], a[72:75], a[28:31], v[184:187]\n"
      "v_exp_f32 v113, v157\n"
      "v_mfma_f32_16x16x32_bf16 v[184:187], a[64:67], a[12:15], v[184:187]\n"
      "s_waitcnt lgkmcnt(10)\n"
      "v_cvt_pk_bf16_f32 v206, v112, v113\n"
      "v_mfma_f32_16x16x32_bf16 v[32:35], a[96:99], v[208:211], v[32:35]\n"
      "v_exp_f32 v114, v158\n"
      "v_mfma_f32_16x16x32_bf16 v[48:51], a[100:103], v[208:211], v[48:51]\n"
      "v_mfma_f32_16x16x32_bf16 a[128:131], a[104:107], v[208:211], a[128:131]\n"
      "s_waitcnt lgkmcnt(6)\n"
      "v_exp_f32 v115, v159\n"
      "v_mfma_f32_16x16x32_bf16 a[144:147], a[108:111], v[208:211], a[144:147]\n"
      "v_cvt_pk_bf16_f32 v207, v114, v115\n"
      "v_mfma_f32_16x16x32_bf16 v[64:67], a[112:115], v[208:211], v[64:67]\n"
      "s_waitcnt lgkmcnt(2)\n"
      "v_mfma_f32_16x16x32_bf16 v[80:83], a[116:119], v[208:211], v[80:83]\n"
      "v_or3_b32 v116, v116, v206, v207\n"
      "v_mfma_f32_16x16x32_bf16 a[160:163], a[120:123], v[208:211], a[160:163]\n"
      "s_mov_b32 s64, 0\n"
      "s_waitcnt lgkmcnt(0)\n"
      "v_and_b32 v116, 0x40004000, v116\n"
      "v_mfma_f32_16x16x32_bf16 a[176:179], a[124:127], v[208:211], a[176:179]\n"
      "v_cmp_ne_u32 vcc, 0, v116\n"
      "s_cbranch_vccnz FB0_%=\n"
      "RET0_0_%=:\n"
      "v_mfma_f32_16x16x32_bf16 a[180:183], a[124:127], v[212:215], a[180:183]\n"
      "v_exp_f32 v112, v160\n"
      "s_barrier\n"
      "v_mfma_f32_16x16x32_bf16 a[164:167], a[120:123], v[212:215], a[164:167]\n"
      "v_xor_b32 v242, 0x10000, v242\n"
      "v_mfma_f32_16x16x32_bf16 a[192:195], a[196:199], v[192:195], a[192:195]\n"
      "s_add_u32 m0, s62, 0\n"
      "v_exp_f32 v113, v161\n"
      "v_mfma_f32_16x16x32_bf16 v[84:87], a[116:119], v[212:215], v[84:87]\n"
      "buffer_load_dwordx4 v248, %[srdk], s60 offen nt lds\n"
      "v_cvt_pk_bf16_f32 v208, v112, v113\n"
      "v_xor_b32 v243, 0x10000, v243\n"
      "v_mfma_f32_16x16x32_bf16 v[68:71], a[112:115], v[212:215], v[68:71]\n"
      "s_add_u32 m0, s62, 896\n"
      "v_exp_f32 v114, v162\n"
      "v_mfma_f32_16x16x32_bf16 a[192:195], a[200:203], v[196:199], a[192:195]\n"
      "buffer_load_dwordx4 v248, %[srdk], s60 offen offset:128 nt lds\n"
      "v_xor_b32 v244, 0x10000, v244\n"
      "v_mfma_f32_16x16x32_bf16 a[148:151], a[108:111], v[212:215], a[148:151]\n"
      "s_add_u32 m0, s62, 8192\n"
      "v_exp_f32 v115, v163\n"
      "v_mfma_f32_16x16x32_bf16 a[132:135], a[104:107], v[212:215], a[132:135]\n"
      "s_memrealtime s[68:69]\n"
      "buffer_load_dwordx4 v120, %[srdk], s60 offen nt lds\n"
      "v_cvt_pk_bf16_f32 v209, v114, v115\n"
      "v_xor_b32 v245, 0x10000, v245\n"
      "v_mfma_f32_16x16x32_bf16 a[192:195], a[204:207], v[200:203], a[192:195]\n"
      "s_add_u32 m0, s62, 9088\n"
      "v_exp_f32 v112, v164\n"
      "v_mfma_f32_16x16x32_bf16 v[52:55], a[100:103], v[212:215], v[52:55]\n"
      "s_add_u32 s62, s62, 0x8000\n"
      "buffer_load_dwordx4 v120, %[srdk], s60 offen offset:128 nt lds\n"
      "v_or_b32 v116, v208, v209\n"
      "v_xor_b32 v246, 0x10000, v246\n"
      "v_mfma_f32_16x16x32_bf16 v[36:39], a[96:99], v[212:215], v[36:39]\n"
      "s_and_b32 s62, s62, 0x1ffff\n"
      "s_add_u32 s60, s60, %[inc]\n"
      "v_exp_f32 v113, v165\n"
      "v_mfma_f32_16x16x32_bf16 a[192:195], a[208:211], v[204:207], a[192:195]\n"
      "s_sub_u32 s63, s63, 1\n"
      "v_cvt_pk_bf16_f32 v210, v112, v113\n"
      "v_xor_b32 v247, 0x10000, v247\n"
      "s_cbranch_scc0 LOOP1_%=\n"
      "s_waitcnt vmcnt(12) lgkmcnt(0)\n"
      "s_sub_u32 s67, s68, s90\n"
      "s_cmp_lt_i32 s67, 0\n"
      "s_cbranch_scc1 GW2_%=\n"
      "GX2_%=:\n"
      "s_barrier\n"
      "v_mfma_f32_16x16x32_bf16 v[40:43], a[96:99], v[216:219], v[40:43]\n"
      "s_memrealtime s[68:69]\n"
      "buffer_load_dword v250, v251, %[srdr], 0 offen sc1\n"
      "ds_read_b128 a[64:67], v242 offset:0\n"
      "v_exp_f32 v114, v166\n"
      "v_mfma_f32_16x16x32_bf16 v[44:47], a[96:99], v[220:223], v[44:47]\n"
      "s_add_u32 m0, s77, 16384\n"
      "ds_read_b128 a[68:71], v242 offset:4096\n"
      "ds_read_b128 a[72:75], v243 offset:0\n"
      "v_mfma_f32_16x16x32_bf16 v[60:63], a[100:103], v[220:223], v[60:63]\n"
      "buffer_load_dwordx4 v249, %[srdv], s61 offen nt lds\n"
      "v_exp_f32 v115, v167\n"
      "v_mfma_f32_16x16x32_bf16 v[56:59], a[100:103], v[216:219], v[56:59]\n"
      "s_add_u32 m0, s77, 17408\n"
      "ds_read_b128 a[76:79], v243 offset:4096\n"
      "v_cvt_pk_bf16_f32 v211, v114, v115\n"
      "v_mfma_f32_16x16x32_bf16 a[136:139], a[104:107], v[216:219], a[136:139]\n"
      "buffer_load_dwordx4 v121, %[srdv], s61 offen nt lds\n"
      "v_exp_f32 v112, v168\n"
      "v_mfma_f32_16x16x32_bf16 a[140:143], a[104:107], v[220:223], a[140:143]\n"
      "s_add_u32 m0, s77, 24576\n"
      "ds_read_b128 a[80:83], v242 offset:1024\n"
      "v_or3_b32 v116, v116, v210, v211\n"
      "v_mfma_f32_16x16x32_bf16 a[156:159], a[108:111], v[220:223], a[156:159]\n"
      "buffer_load_dwordx4 v122, %[srdv], s61 offen nt lds\n"
      "v_exp_f32 v113, v169\n"
      "v_mfma_f32_16x16x32_bf16 a[152:155], a[108:111], v[216:219], a[152:155]\n"
      "s_add_u32 m0, s77, 25600\n"
      "ds_read_b128 a[84:87], v242 offset:5120\n"
      "v_cvt_pk_bf16_f32 v212, v112, v113\n"
      "v_mfma_f32_16x16x32_bf16 v[72:75], a[112:115], v[216:219], v[72:75]\n"
      "s_add_u32 s77, s77, 0x8000\n"
      "buffer_load_dwordx4 v123, %[srdv], s61 offen nt lds\n"
      "v_exp_f32 v114, v170\n"
      "v_mfma_f32_16x16x32_bf16 v[76:79], a[112:115], v[220:223], v[76:79]\n"
      "s_and_b32 s77, s77, 0x1ffff\n"
      "ds_read_b128 a[88:91], v243 offset:1024\n"
      "ds_read_b128 a[92:95], v243 offset:5120\n"
      "v_mfma_f32_16x16x32_bf16 v[92:95], a[116:119], v[220:223], v[92:95]\n"
      "s_add_u32 s61, s61, %[inc]\n"
      "v_exp_f32 v115, v171\n"
      "v_mfma_f32_16x16x32_bf16 v[88:91], a[116:119], v[216:219], v[88:91]\n"
      "v_cvt_pk_bf16_f32 v213, v114, v115\n"
      "v_mfma_f32_16x16x32_bf16 a[168:171], a[120:123], v[216:219], a[168:171]\n"
      "v_exp_f32 v112, v172\n"
      "v_mfma_f32_16x16x32_bf16 a[172:175], a[120:123], v[220:223], a[172:175]\n"
      "v_or3_b32 v116, v116, v212, v213\n"
      "v_mfma_f32_16x16x32_bf16 a[188:191], a[124:127], v[220:223], a[188:191]\n"
      "v_exp_f32 v113, v173\n"
      "v_mfma_f32_16x16x32_bf16 a[184:187], a[124:127], v[216:219], a[184:187]\n"
      "s_waitcnt lgkmcnt(0)\n"
      "v_cvt_pk_bf16_f32 v214, v112, v113\n"
      "v_mfma_f32_16x16x32_bf16 v[128:131], a[64:67], a[0:3], v[224:227]\n"
      "s_sub_u32 s67, s68, s66\n"
      "v_exp_f32 v114, v174\n"
      "v_mfma_f32_16x16x32_bf16 v[128:131], a[72:75], a[16:19], v[128:131]\n"
      "s_cmp_gt_i32 s67, s70\n"
      "ds_read_b64_tr_b16 a[96:97], v244 offset:0\n"
      "ds_read_b64_tr_b16 a[98:99], v244 offset:4096\n"
      "v_mfma_f32_16x16x32_bf16 v[128:131], a[80:83], a[32:35], v[128:131]\n"
      "s_cselect_b32 s69, s86, 0\n"
      "v_exp_f32 v115, v175\n"
      "v_mfma_f32_16x16x32_bf16 v[128:131], a[88:91], a[48:51], v[128:131]\n"
      "s_add_u32 s66, s66, s69\n"
      "ds_read_b64_tr_b16 a[100:101], v245 offset:0\n"
      "v_cvt_pk_bf16_f32 v215, v114, v115\n"
      "v_mfma_f32_16x16x32_bf16 v[132:135], a[92:95], a[48:51], v[224:227]\n"
      "s_add_u32 s67, s67, s101\n"
      "v_exp_f32 v112, v176\n"
      "v_mfma_f32_16x16x32_bf16 v[132:135], a[84:87], a[32:35], v[132:135]\n"
      "s_min_i32 s70, s70, s67\n"
      "ds_read_b64_tr_b16 a[102:103], v245 offset:4096\n"
      "v_or3_b32 v116, v116, v214, v215\n"
      "v_mfma_f32_16x16x32_bf16 v[132:135], a[76:79], a[16:19], v[132:135]\n"
      "s_max_i32 s70, s70, 85\n"
      "v_exp_f32 v113, v177\n"
      "v_mfma_f32_16x16x32_bf16 v[132:135], a[68:71], a[0:3], v[132:135]\n"
      "s_add_u32 s66, s66, s86\n"
      "ds_read_b64_tr_b16 a[104:105], v246 offset:0\n"
      "v_cvt_pk_bf16_f32 v216, v112, v113\n"
      "v_mfma_f32_16x16x32_bf16 v[140:143], a[68:71], a[4:7], v[228:231]\n"
      "s_and_b32 s76, s80, 31\n"
      "v_exp_f32 v114, v178\n"
      "v_mfma_f32_16x16x32_bf16 v[140:143], a[76:79], a[20:23], v[140:143]\n"
      "s_lshr_b64 s[80:81], s[80:81], 5\n"
      "ds_read_b64_tr_b16 a[106:107], v246 offset:4096\n"
      "ds_read_b64_tr_b16 a[108:109], v247 offset:0\n"
      "v_mfma_f32_16x16x32_bf16 v[140:143], a[84:87], a[36:39], v[140:143]\n"
      "s_add_u32 s66, s66, s76\n"
      "v_exp_f32 v115, v179\n"
      "v_mfma_f32_16x16x32_bf16 v[140:143], a[92:95], a[52:55], v[140:143]\n"
      "s_sub_u32 s67, s68, s82\n"
      "ds_read_b64_tr_b16 a[110:111], v247 offset:4096\n"
      "v_cvt_pk_bf16_f32 v217, v114, v115\n"
      "v_mfma_f32_16x16x32_bf16 v[136:139], a[88:91], a[52:55], v[228:231]\n"
      "s_min_i32 s76, s67, s83\n"
      "v_exp_f32 v112, v180\n"
      "v_mfma_f32_16x16x32_bf16 v[136:139], a[80:83], a[36:39], v[136:139]\n"
      "s_mov_b32 s83, s67\n"
      "ds_read_b64_tr_b16 a[112:113], v244 offset:1024\n"
      "v_or3_b32 v116, v116, v216, v217\n"
      "v_mfma_f32_16x16x32_bf16 v[136:139], a[72:75], a[20:23], v[136:139]\n"
      "s_sub_u32 s76, s76, 36\n"
      "v_exp_f32 v113, v181\n"
      "v_mfma_f32_16x16x32_bf16 v[136:139], a[64:67], a[4:7], v[136:139]\n"
      "s_min_i32 s76, s76, 148\n"
      "ds_read_b64_tr_b16 a[114:115], v244 offset:5120\n"
      "v_cvt_pk_bf16_f32 v218, v112, v113\n"
      "v_mfma_f32_16x16x32_bf16 v[144:147], a[64:67], a[8:11], v[232:235]\n"
      "s_max_i32 s76, s76, 70\n"
      "v_exp_f32 v114, v182\n"
      "v_mfma_f32_16x16x32_bf16 v[144:147], a[72:75], a[24:27], v[144:147]\n"
      "s_mov_b32 s82, s68\n"
      "ds_read_b64_tr_b16 a[116:117], v245 offset:1024\n"
      "ds_read_b64_tr_b16 a[118:119], v245 offset:5120\n"
      "v_mfma_f32_16x16x32_bf16 v[144:147], a[80:83], a[40:43], v[144:147]\n"
      "s_lshr_b32 s69, s86, 1\n"
      "v_exp_f32 v115, v183\n"
      "v_mfma_f32_16x16x32_bf16 v[144:147], a[88:91], a[56:59], v[144:147]\n"
      "s_add_u32 s66, s66, s69\n"
      "ds_read_b64_tr_b16 a[120:121], v246 offset:1024\n"
      "v_cvt_pk_bf16_f32 v219, v114, v115\n"
      "v_mfma_f32_16x16x32_bf16 v[148:151], a[92:95], a[56:59], v[232:235]\n"
      "s_add_u32 s82, s82, s69\n"
      "v_exp_f32 v112, v184\n"
      "v_mfma_f32_16x16x32_bf16 v[148:151], a[84:87], a[40:43], v[148:151]\n"
      "s_add_u32 s65, s82, s76\n"
      "ds_read_b64_tr_b16 a[122:123], v246 offset:5120\n"
      "v_or3_b32 v116, v116, v218, v219\n"
      "v_mfma_f32_16x16x32_bf16 v[148:151], a[76:79], a[24:27], v[148:151]\n"
      "s_sub_u32 s93, s93, 1\n"
      "v_exp_f32 v113, v185\n"
      "v_mfma_f32_16x16x32_bf16 v[148:151], a[68:71], a[8:11], v[148:151]\n"
      "s_cmp_le_i32 s93, 14\n"
      "ds_read_b64_tr_b16 a[124:125], v247 offset:1024\n"
      "v_cvt_pk_bf16_f32 v220, v112, v113\n"
      "v_mfma_f32_16x16x32_bf16 v[156:159], a[68:71], a[12:15], v[236:239]\n"
      "s_cselect_b32 s86, 107, s86\n"
      "v_exp_f32 v114, v186\n"
      "v_mfma_f32_16x16x32_bf16 v[156:159], a[76:79], a[28:31], v[156:159]\n"
      "s_sub_u32 s67, s65, s66\n"
      "ds_read_b64_tr_b16 a[126:127], v247 offset:5120\n"
      "v_mfma_f32_16x16x32_bf16 v[156:159], a[84:87], a[44:47], v[156:159]\n"
      "s_cmp_lt_i32 s67, 0\n"
      "v_exp_f32 v115, v187\n"
      "v_mfma_f32_16x16x32_bf16 v[156:159], a[92:95], a[60:63], v[156:159]\n"
      "s_cselect_b32 s88, s66, s65\n"
      "v_cvt_pk_bf16_f32 v221, v114, v115\n"
      "v_mfma_f32_16x16x32_bf16 v[152:155], a[88:91], a[60:63], v[236:239]\n"
      "s_add_u32 s90, s88, s91\n"
      "v_exp_f32 v112, v188\n"
      "v_mfma_f32_16x16x32_bf16 v[152:155], a[80:83], a[44:47], v[152:155]\n"
      "v_or3_b32 v116, v116, v220, v221\n"
      "v_mfma_f32_16x16x32_bf16 v[152:155], a[72:75], a[28:31], v[152:155]\n"
      "v_exp_f32 v113, v189\n"
      "v_mfma_f32_16x16x32_bf16 v[152:155], a[64:67], a[12:15], v[152:155]\n"
      "s_waitcnt lgkmcnt(10)\n"
      "v_cvt_pk_bf16_f32 v222, v112, v113\n"
      "v_mfma_f32_16x16x32_bf16 v[32:35], a[96:99], v[192:195], v[32:35]\n"
      "v_exp_f32 v114, v190\n"
      "v_mfma_f32_16x16x32_bf16 v[48:51], a[100:103], v[192:195], v[48:51]\n"
      "v_mfma_f32_16x16x32_bf16 a[128:131], a[104:107], v[192:195], a[128:131]\n"
      "s_waitcnt lgkmcnt(6)\n"
      "v_exp_f32 v115, v191\n"
      "v_mfma_f32_16x16x32_bf16 a[144:147], a[108:111], v[192:195], a[144:147]\n"
      "v_cvt_pk_bf16_f32 v223, v114, v115\n"
      "v_mfma_f32_16x16x32_bf16 v[64:67], a[112:115], v[192:195], v[64:67]\n"
      "s_waitcnt lgkmcnt(2)\n"
      "v_mfma_f32_16x16x32_bf16 v[80:83], a[116:119], v[192:195], v[80:83]\n"
      "v_or3_b32 v116, v116, v222, v223\n"
      "v_mfma_f32_16x16x32_bf16 a[160:163], a[120:123], v[192:195], a[160:163]\n"
      "s_mov_b32 s64, 1\n"
      "s_waitcnt lgkmcnt(0)\n"
      "v_and_b32 v116, 0x40004000, v116\n"
      "v_mfma_f32_16x16x32_bf16 a[176:179], a[124:127], v[192:195], a[176:179]\n"
      "v_cmp_ne_u32 vcc, 0, v116\n"
      "s_cbranch_vccnz FB1_%=\n"
      "RET1_1_%=:\n"
      "v_mfma_f32_16x16x32_bf16 a[180:183], a[124:127], v[196:199], a[180:183]\n"
      "v_exp_f32 v112, v128\n"
      "s_barrier\n"
      "v_mfma_f32_16x16x32_bf16 a[164:167], a[120:123], v[196:199], a[164:167]\n"
      "v_mfma_f32_16x16x32_bf16 a[192:195], a[196:199], v[208:211], a[192:195]\n"
      "s_add_u32 m0, s62, 0\n"
      "v_exp_f32 v113, v129\n"
      "v_mfma_f32_16x16x32_bf16 v[84:87], a[116:119], v[196:199], v[84:87]\n"
      "buffer_load_dwordx4 v248, %[srdk], s60 offen nt lds\n"
      "v_cvt_pk_bf16_f32 v192, v112, v113\n"
      "v_mfma_f32_16x16x32_bf16 v[68:71], a[112:115], v[196:199], v[68:71]\n"
      "s_add_u32 m0, s62, 896\n"
      "v_exp_f32 v114, v130\n"
      "v_mfma_f32_16x16x32_bf16 a[192:195], a[200:203], v[212:215], a[192:195]\n"
      "buffer_load_dwordx4 v248, %[srdk], s60 offen offset:128 nt lds\n"
      "v_mfma_f32_16x16x32_bf16 a[148:151], a[108:111], v[196:199], a[148:151]\n"
      "s_add_u32 m0, s62, 8192\n"
      "v_exp_f32 v115, v131\n"
      "v_mfma_f32_16x16x32_bf16 a[132:135], a[104:107], v[196:199], a[132:135]\n"
      "s_memrealtime s[68:69]\n"
      "buffer_load_dwordx4 v120, %[srdk], s60 offen nt lds\n"
      "v_cvt_pk_bf16_f32 v193, v114, v115\n"
      "v_mfma_f32_16x16x32_bf16 a[192:195], a[204:207], v[216:219], a[192:195]\n"
      "s_add_u32 m0, s62, 9088\n"
      "v_exp_f32 v112, v132\n"
      "v_mfma_f32_16x16x32_bf16 v[52:55], a[100:103], v[196:199], v[52:55]\n"
      "s_add_u32 s62, s62, 0x8000\n"
      "buffer_load_dwordx4 v120, %[srdk], s60 offen offset:128 nt lds\n"
      "v_or_b32 v116, v192, v193\n"
      "v_mfma_f32_16x16x32_bf16 v[36:39], a[96:99], v[196:199], v[36:39]\n"
      "s_and_b32 s62, s62, 0x1ffff\n"
      "v_exp_f32 v113, v133\n"
      "v_mfma_f32_16x16x32_bf16 a[192:195], a[208:211], v[220:223], a[192:195]\n"
      "s_add_u32 s60, s60, s78\n"
      "v_cvt_pk_bf16_f32 v194, v112, v113\n"
      "s_waitcnt vmcnt(12) lgkmcnt(0)\n"
      "s_barrier\n"
      "v_mfma_f32_16x16x32_bf16 v[40:43], a[96:99], v[200:203], v[40:43]\n"
      "ds_read_b128 a[64:67], v242 offset:32768\n"
      "v_exp_f32 v114, v134\n"
      "v_mfma_f32_16x16x32_bf16 v[44:47], a[96:99], v[204:207], v[44:47]\n"
      "ds_read_b128 a[68:71], v242 offset:36864\n"
      "ds_read_b128 a[72:75], v243 offset:32768\n"
      "v_mfma_f32_16x16x32_bf16 v[60:63], a[100:103], v[204:207], v[60:63]\n"
      "v_exp_f32 v115, v135\n"
      "v_mfma_f32_16x16x32_bf16 v[56:59], a[100:103], v[200:203], v[56:59]\n"
      "ds_read_b128 a[76:79], v243 offset:36864\n"
      "v_cvt_pk_bf16_f32 v195, v114, v115\n"
      "v_mfma_f32_16x16x32_bf16 a[136:139], a[104:107], v[200:203], a[136:139]\n"
      "v_exp_f32 v112, v136\n"
      "v_mfma_f32_16x16x32_bf16 a[140:143], a[104:107], v[204:207], a[140:143]\n"
      "ds_read_b128 a[80:83], v242 offset:33792\n"
      "v_or3_b32 v116, v116, v194, v195\n"
      "v_mfma_f32_16x16x32_bf16 a[156:159], a[108:111], v[204:207], a[156:159]\n"
      "v_exp_f32 v113, v137\n"
      "v_mfma_f32_16x16x32_bf16 a[152:155], a[108:111], v[200:203], a[152:155]\n"
      "ds_read_b128 a[84:87], v242 offset:37888\n"
      "v_cvt_pk_bf16_f32 v196, v112, v113\n"
      "v_mfma_f32_16x16x32_bf16 v[72:75], a[112:115], v[200:203], v[72:75]\n"
      "v_exp_f32 v114, v138\n"
      "v_mfma_f32_16x16x32_bf16 v[76:79], a[112:115], v[204:207], v[76:79]\n"
      "ds_read_b128 a[88:91], v243 offset:33792\n"
      "ds_read_b128 a[92:95], v243 offset:37888\n"
      "v_mfma_f32_16x16x32_bf16 v[92:95], a[116:119], v[204:207], v[92:95]\n"
      "v_exp_f32 v115, v139\n"
      "v_mfma_f32_16x16x32_bf16 v[88:91], a[116:119], v[200:203], v[88:91]\n"
      "v_cvt_pk_bf16_f32 v197, v114, v115\n"
      "v_mfma_f32_16x16x32_bf16 a[168:171], a[120:123], v[200:203], a[168:171]\n"
      "v_exp_f32 v112, v140\n"
      "v_mfma_f32_16x16x32_bf16 a[172:175], a[120:123], v[204:207], a[172:175]\n"
      "v_or3_b32 v116, v116, v196, v197\n"
      "v_mfma_f32_16x16x32_bf16 a[188:191], a[124:127], v[204:207], a[188:191]\n"
      "v_exp_f32 v113, v141\n"
      "v_mfma_f32_16x16x32_bf16 a[184:187], a[124:127], v[200:203], a[184:187]\n"
      "s_waitcnt lgkmcnt(0)\n"
      "v_cvt_pk_bf16_f32 v198, v112, v113\n"
      "v_mfma_f32_16x16x32_bf16 v[160:163], a[64:67], a[0:3], v[224:227]\n"
      "v_exp_f32 v114, v142\n"
      "v_mfma_f32_16x16x32_bf16 v[160:163], a[72:75], a[16:19], v[160:163]\n"
      "ds_read_b64_tr_b16 a[96:97], v244 offset:32768\n"
      "ds_read_b64_tr_b16 a[98:99], v244 offset:36864\n"
      "v_mfma_f32_16x16x32_bf16 v[160:163], a[80:83], a[32:35], v[160:163]\n"
      "v_exp_f32 v115, v143\n"
      "v_mfma_f32_16x16x32_bf16 v[160:163], a[88:91], a[48:51], v[160:163]\n"
      "ds_read_b64_tr_b16 a[100:101], v245 offset:32768\n"
      "v_cvt_pk_bf16_f32 v199, v114, v115\n"
      "v_mfma_f32_16x16x32_bf16 v[164:167], a[92:95], a[48:51], v[224:227]\n"
      "v_exp_f32 v112, v144\n"
      "v_mfma_f32_16x16x32_bf16 v[164:167], a[84:87], a[32:35], v[164:167]\n"
      "ds_read_b64_tr_b16 a[102:103], v245 offset:36864\n"
      "v_or3_b32 v116, v116, v198, v199\n"
      "v_mfma_f32_16x16x32_bf16 v[164:167], a[76:79], a[16:19], v[164:167]\n"
      "v_exp_f32 v113, v145\n"
      "v_mfma_f32_16x16x32_bf16 v[164:167], a[68:71], a[0:3], v[164:167]\n"
      "ds_read_b64_tr_b16 a[104:105], v246 offset:32768\n"
      "v_cvt_pk_bf16_f32 v200, v112, v113\n"
      "v_mfma_f32_16x16x32_bf16 v[172:175], a[68:71], a[4:7], v[228:231]\n"
      "v_exp_f32 v114, v146\n"
      "v_mfma_f32_16x16x32_bf16 v[172:175], a[76:79], a[20:23], v[172:175]\n"
      "ds_read_b64_tr_b16 a[106:107], v246 offset:36864\n"
      "ds_read_b64_tr_b16 a[108:109], v247 offset:32768\n"
      "v_mfma_f32_16x16x32_bf16 v[172:175], a[84:87], a[36:39], v[172:175]\n"
      "v_exp_f32 v115, v147\n"
      "v_mfma_f32_16x16x32_bf16 v[172:175], a[92:95], a[52:55], v[172:175]\n"
      "ds_read_b64_tr_b16 a[110:111], v247 offset:36864\n"
      "v_cvt_pk_bf16_f32 v201, v114, v115\n"
      "v_mfma_f32_16x16x32_bf16 v[168:171], a[88:91], a[52:55], v[228:231]\n"
      "v_exp_f32 v112, v148\n"
      "v_mfma_f32_16x16x32_bf16 v[168:171], a[80:83], a[36:39], v[168:171]\n"
      "ds_read_b64_tr_b16 a[112:113], v244 offset:33792\n"
      "v_or3_b32 v116, v116, v200, v201\n"
      "v_mfma_f32_16x16x32_bf16 v[168:171], a[72:75], a[20:23], v[168:171]\n"
      "v_exp_f32 v113, v149\n"
      "v_mfma_f32_16x16x32_bf16 v[168:171], a[64:67], a[4:7], v[168:171]\n"
      "ds_read_b64_tr_b16 a[114:115], v244 offset:37888\n"
      "v_cvt_pk_bf16_f32 v202, v112, v113\n"
      "v_mfma_f32_16x16x32_bf16 v[176:179], a[64:67], a[8:11], v[232:235]\n"
      "v_exp_f32 v114, v150\n"
      "v_mfma_f32_16x16x32_bf16 v[176:179], a[72:75], a[24:27], v[176:179]\n"
      "ds_read_b64_tr_b16 a[116:117], v245 offset:33792\n"
      "ds_read_b64_tr_b16 a[118:119], v245 offset:37888\n"
      "v_mfma_f32_16x16x32_bf16 v[176:179], a[80:83], a[40:43], v[176:179]\n"
      "v_exp_f32 v115, v151\n"
      "v_mfma_f32_16x16x32_bf16 v[176:179], a[88:91], a[56:59], v[176:179]\n"
      "ds_read_b64_tr_b16 a[120:121], v246 offset:33792\n"
      "v_cvt_pk_bf16_f32 v203, v114, v115\n"
      "v_mfma_f32_16x16x32_bf16 v[180:183], a[92:95], a[56:59], v[232:235]\n"
      "v_exp_f32 v112, v152\n"
      "v_mfma_f32_16x16x32_bf16 v[180:183], a[84:87], a[40:43], v[180:183]\n"
      "ds_read_b64_tr_b16 a[122:123], v246 offset:37888\n"
      "v_or3_b32 v116, v116, v202, v203\n"
      "v_mfma_f32_16x16x32_bf16 v[180:183], a[76:79], a[24:27], v[180:183]\n"
      "v_exp_f32 v113, v153\n"
      "v_mfma_f32_16x16x32_bf16 v[180:183], a[68:71], a[8:11], v[180:183]\n"
      "ds_read_b64_tr_b16 a[124:125], v247 offset:33792\n"
      "v_cvt_pk_bf16_f32 v204, v112, v113\n"
      "v_mfma_f32_16x16x32_bf16 v[188:191], a[68:71], a[12:15], v[236:239]\n"
      "v_exp_f32 v114, v154\n"
      "v_mfma_f32_16x16x32_bf16 v[188:191], a[76:79], a[28:31], v[188:191]\n"
      "ds_read_b64_tr_b16 a[126:127], v247 offset:37888\n"
      "v_mfma_f32_16x16x32_bf16 v[188:191], a[84:87], a[44:47], v[188:191]\n"
      "v_exp_f32 v115, v155\n"
      "v_mfma_f32_16x16x32_bf16 v[188:191], a[92:95], a[60:63], v[188:191]\n"
      "v_cvt_pk_bf16_f32 v205, v114, v115\n"
      "v_mfma_f32_16x16x32_bf16 v[184:187], a[88:91], a[60:63], v[236:239]\n"
      "v_exp_f32 v112, v156\n"
      "v_mfma_f32_16x16x32_bf16 v[184:187], a[80:83], a[44:47], v[184:187]\n"
      "v_or3_b32 v116, v116, v204, v205\n"
      "v_mfma_f32_16x16x32_bf16 v[184:187], a[72:75], a[28:31], v[184:187]\n"
      "v_exp_f32 v113, v157\n"
      "v_mfma_f32_16x16x32_bf16 v[184:187], a[64:67], a[12:15], v[184:187]\n"
      "s_waitcnt lgkmcnt(10)\n"
      "v_cvt_pk_bf16_f32 v206, v112, v113\n"
      "v_mfma_f32_16x16x32_bf16 v[32:35], a[96:99], v[208:211], v[32:35]\n"
      "v_exp_f32 v114, v158\n"
      "v_mfma_f32_16x16x32_bf16 v[48:51], a[100:103], v[208:211], v[48:51]\n"
      "v_mfma_f32_16x16x32_bf16 a[128:131], a[104:107], v[208:211], a[128:131]\n"
      "s_waitcnt lgkmcnt(6)\n"
      "v_exp_f32 v115, v159\n"
      "v_mfma_f32_16x16x32_bf16 a[144:147], a[108:111], v[208:211], a[144:147]\n"
      "v_cvt_pk_bf16_f32 v207, v114, v115\n"
      "v_mfma_f32_16x16x32_bf16 v[64:67], a[112:115], v[208:211], v[64:67]\n"
      "s_waitcnt lgkmcnt(2)\n"
      "v_mfma_f32_16x16x32_bf16 v[80:83], a[116:119], v[208:211], v[80:83]\n"
      "v_or3_b32 v116, v116, v206, v207\n"
      "v_mfma_f32_16x16x32_bf16 a[160:163], a[120:123], v[208:211], a[160:163]\n"
      "s_mov_b32 s64, 1\n"
      "s_waitcnt lgkmcnt(0)\n"
      "v_and_b32 v116, 0x40004000, v116\n"
      "v_mfma_f32_16x16x32_bf16 a[176:179], a[124:127], v[208:211], a[176:179]\n"
      "v_cmp_ne_u32 vcc, 0, v116\n"
      "s_cbranch_vccnz FB0_%=\n"
      "RET0_1_%=:\n"
      "v_mfma_f32_16x16x32_bf16 a[180:183], a[124:127], v[212:215], a[180:183]\n"
      "v_exp_f32 v112, v160\n"
      "v_mfma_f32_16x16x32_bf16 a[164:167], a[120:123], v[212:215], a[164:167]\n"
      "v_xor_b32 v242, 0x10000, v242\n"
      "v_mfma_f32_16x16x32_bf16 a[192:195], a[196:199], v[192:195], a[192:195]\n"
      "s_add_u32 m0, s77, 16384\n"
      "v_exp_f32 v113, v161\n"
      "v_mfma_f32_16x16x32_bf16 v[84:87], a[116:119], v[212:215], v[84:87]\n"
      "buffer_load_dwordx4 v249, %[srdv], s61 offen nt lds\n"
      "v_cvt_pk_bf16_f32 v208, v112, v113\n"
      "v_xor_b32 v243, 0x10000, v243\n"
      "v_mfma_f32_16x16x32_bf16 v[68:71], a[112:115], v[212:215], v[68:71]\n"
      "s_add_u32 m0, s77, 17408\n"
      "v_exp_f32 v114, v162\n"
      "v_mfma_f32_16x16x32_bf16 a[192:195], a[200:203], v[196:199], a[192:195]\n"
      "buffer_load_dwordx4 v121, %[srdv], s61 offen nt lds\n"
      "v_xor_b32 v244, 0x10000, v244\n"
      "v_mfma_f32_16x16x32_bf16 a[148:151], a[108:111], v[212:215], a[148:151]\n"
      "s_add_u32 m0, s77, 24576\n"
      "v_exp_f32 v115, v163\n"
      "v_mfma_f32_16x16x32_bf16 a[132:135], a[104:107], v[212:215], a[132:135]\n"
      "s_memrealtime s[68:69]\n"
      "buffer_load_dwordx4 v122, %[srdv], s61 offen nt lds\n"
      "v_cvt_pk_bf16_f32 v209, v114, v115\n"
      "v_xor_b32 v245, 0x10000, v245\n"
      "v_mfma_f32_16x16x32_bf16 a[192:195], a[204:207], v[200:203], a[192:195]\n"
      "s_add_u32 m0, s77, 25600\n"
      "v_exp_f32 v112, v164\n"
      "v_mfma_f32_16x16x32_bf16 v[52:55], a[100:103], v[212:215], v[52:55]\n"
      "s_add_u32 s77, s77, 0x8000\n"
      "buffer_load_dwordx4 v123, %[srdv], s61 offen nt lds\n"
      "v_or_b32 v116, v208, v209\n"
      "v_xor_b32 v246, 0x10000, v246\n"
      "v_mfma_f32_16x16x32_bf16 v[36:39], a[96:99], v[212:215], v[36:39]\n"
      "s_and_b32 s77, s77, 0x1ffff\n"
      "v_exp_f32 v113, v165\n"
      "v_mfma_f32_16x16x32_bf16 a[192:195], a[208:211], v[204:207], a[192:195]\n"
      "s_add_u32 s61, s61, s78\n"
      "v_cvt_pk_bf16_f32 v210, v112, v113\n"
      "v_xor_b32 v247, 0x10000, v247\n"
      "s_waitcnt vmcnt(8) lgkmcnt(0)\n"
      "s_sub_u32 s67, s68, s90\n"
      "s_cmp_lt_i32 s67, 0\n"
      "s_cbranch_scc1 GW3_%=\n"
      "GX3_%=:\n"
      "s_barrier\n"
      "v_mfma_f32_16x16x32_bf16 v[40:43], a[96:99], v[216:219], v[40:43]\n"
      "s_memrealtime s[68:69]\n"
      "buffer_load_dword v250, v251, %[srdr], 0 offen sc1\n"
      "ds_read_b128 a[64:67], v242 offset:0\n"
      "v_exp_f32 v114, v166\n"
      "v_mfma_f32_16x16x32_bf16 v[44:47], a[96:99], v[220:223], v[44:47]\n"
      "s_add_u32 m0, s62, 0\n"
      "ds_read_b128 a[68:71], v242 offset:4096\n"
      "ds_read_b128 a[72:75], v243 offset:0\n"
      "v_mfma_f32_16x16x32_bf16 v[60:63], a[100:103], v[220:223], v[60:63]\n"
      "buffer_load_dwordx4 v248, %[srdk], s60 offen nt lds\n"
      "v_exp_f32 v115, v167\n"
      "v_mfma_f32_16x16x32_bf16 v[56:59], a[100:103], v[216:219], v[56:59]\n"
      "s_add_u32 m0, s62, 896\n"
      "ds_read_b128 a[76:79], v243 offset:4096\n"
      "v_cvt_pk_bf16_f32 v211, v114, v115\n"
      "v_mfma_f32_16x16x32_bf16 a[136:139], a[104:107], v[216:219], a[136:139]\n"
      "buffer_load_dwordx4 v248, %[srdk], s60 offen offset:128 nt lds\n"
      "v_exp_f32 v112, v168\n"
      "v_mfma_f32_16x16x32_bf16 a[140:143], a[104:107], v[220:223], a[140:143]\n"
      "s_add_u32 m0, s62, 8192\n"
      "ds_read_b128 a[80:83], v242 offset:1024\n"
      "v_or3_b32 v116, v116, v210, v211\n"
      "v_mfma_f32_16x16x32_bf16 a[156:159], a[108:111], v[220:223], a[156:159]\n"
      "buffer_load_dwordx4 v120, %[srdk], s60 offen nt lds\n"
      "v_exp_f32 v113, v169\n"
      "v_mfma_f32_16x16x32_bf16 a[152:155], a[108:111], v[216:219], a[152:155]\n"
      "s_add_u32 m0, s62, 9088\n"
      "ds_read_b128 a[84:87], v242 offset:5120\n"
      "v_cvt_pk_bf16_f32 v212, v112, v113\n"
      "v_mfma_f32_16x16x32_bf16 v[72:75], a[112:115], v[216:219], v[72:75]\n"
      "s_add_u32 s62, s62, 0x8000\n"
      "buffer_load_dwordx4 v120, %[srdk], s60 offen offset:128 nt lds\n"
      "v_exp_f32 v114, v170\n"
      "v_mfma_f32_16x16x32_bf16 v[76:79], a[112:115], v[220:223], v[76:79]\n"
      "s_and_b32 s62, s62, 0x1ffff\n"
      "ds_read_b128 a[88:91], v243 offset:1024\n"
      "ds_read_b128 a[92:95], v243 offset:5120\n"
      "v_mfma_f32_16x16x32_bf16 v[92:95], a[116:119], v[220:223], v[92:95]\n"
      "s_add_u32 s60, s60, %[inc]\n"
      "v_exp_f32 v115, v171\n"
      "v_mfma_f32_16x16x32_bf16 v[88:91], a[116:119], v[216:219], v[88:91]\n"
      "v_cvt_pk_bf16_f32 v213, v114, v115\n"
      "v_mfma_f32_16x16x32_bf16 a[168:171], a[120:123], v[216:219], a[168:171]\n"
      "v_exp_f32 v112, v172\n"
      "v_mfma_f32_16x16x32_bf16 a[172:175], a[120:123], v[220:223], a[172:175]\n"
      "v_or3_b32 v116, v116, v212, v213\n"
      "v_mfma_f32_16x16x32_bf16 a[188:191], a[124:127], v[220:223], a[188:191]\n"
      "v_exp_f32 v113, v173\n"
      "v_mfma_f32_16x16x32_bf16 a[184:187], a[124:127], v[216:219], a[184:187]\n"
      "s_waitcnt lgkmcnt(0)\n"
      "v_cvt_pk_bf16_f32 v214, v112, v113\n"
      "v_mfma_f32_16x16x32_bf16 v[128:131], a[64:67], a[0:3], v[224:227]\n"
      "s_sub_u32 s67, s68, s66\n"
      "v_exp_f32 v114, v174\n"
      "v_mfma_f32_16x16x32_bf16 v[128:131], a[72:75], a[16:19], v[128:131]\n"
      "s_cmp_gt_i32 s67, s70\n"
      "ds_read_b64_tr_b16 a[96:97], v244 offset:0\n"
      "ds_read_b64_tr_b16 a[98:99], v244 offset:4096\n"
      "v_mfma_f32_16x16x32_bf16 v[128:131], a[80:83], a[32:35], v[128:131]\n"
      "s_cselect_b32 s69, s86, 0\n"
      "v_exp_f32 v115, v175\n"
      "v_mfma_f32_16x16x32_bf16 v[128:131], a[88:91], a[48:51], v[128:131]\n"
      "s_add_u32 s66, s66, s69\n"
      "ds_read_b64_tr_b16 a[100:101], v245 offset:0\n"
      "v_cvt_pk_bf16_f32 v215, v114, v115\n"
      "v_mfma_f32_16x16x32_bf16 v[132:135], a[92:95], a[48:51], v[224:227]\n"
      "s_add_u32 s67, s67, s101\n"
      "v_exp_f32 v112, v176\n"
      "v_mfma_f32_16x16x32_bf16 v[132:135], a[84:87], a[32:35], v[132:135]\n"
      "s_min_i32 s70, s70, s67\n"
      "ds_read_b64_tr_b16 a[102:103], v245 offset:4096\n"
      "v_or3_b32 v116, v116, v214, v215\n"
      "v_mfma_f32_16x16x32_bf16 v[132:135], a[76:79], a[16:19], v[132:135]\n"
      "s_max_i32 s70, s70, 85\n"
      "v_exp_f32 v113, v177\n"
      "v_mfma_f32_16x16x32_bf16 v[132:135], a[68:71], a[0:3], v[132:135]\n"
      "s_add_u32 s66, s66, s86\n"
      "ds_read_b64_tr_b16 a[104:105], v246 offset:0\n"
      "v_cvt_pk_bf16_f32 v216, v112, v113\n"
      "v_mfma_f32_16x16x32_bf16 v[140:143], a[68:71], a[4:7], v[228:231]\n"
      "s_and_b32 s76, s80, 31\n"
      "v_exp_f32 v114, v178\n"
      "v_mfma_f32_16x16x32_bf16 v[140:143], a[76:79], a[20:23], v[140:143]\n"
      "s_lshr_b64 s[80:81], s[80:81], 5\n"
      "ds_read_b64_tr_b16 a[106:107], v246 offset:4096\n"
      "ds_read_b64_tr_b16 a[108:109], v247 offset:0\n"
      "v_mfma_f32_16x16x32_bf16 v[140:143], a[84:87], a[36:39], v[140:143]\n"
      "s_add_u32 s66, s66, s76\n"
      "v_exp_f32 v115, v179\n"
      "v_mfma_f32_16x16x32_bf16 v[140:143], a[92:95], a[52:55], v[140:143]\n"
      "s_sub_u32 s67, s68, s82\n"
      "ds_read_b64_tr_b16 a[110:111], v247 offset:4096\n"
      "v_cvt_pk_bf16_f32 v217, v114, v115\n"
      "v_mfma_f32_16x16x32_bf16 v[136:139], a[88:91], a[52:55], v[228:231]\n"
      "s_min_i32 s76, s67, s83\n"
      "v_exp_f32 v112, v180\n"
      "v_mfma_f32_16x16x32_bf16 v[136:139], a[80:83], a[36:39], v[136:139]\n"
      "s_mov_b32 s83, s67\n"
      "ds_read_b64_tr_b16 a[112:113], v244 offset:1024\n"
      "v_or3_b32 v116, v116, v216, v217\n"
      "v_mfma_f32_16x16x32_bf16 v[136:139], a[72:75], a[20:23], v[136:139]\n"
      "s_sub_u32 s76, s76, 36\n"
      "v_exp_f32 v113, v181\n"
      "v_mfma_f32_16x16x32_bf16 v[136:139], a[64:67], a[4:7], v[136:139]\n"
      "s_min_i32 s76, s76, 148\n"
      "ds_read_b64_tr_b16 a[114:115], v244 offset:5120\n"
      "v_cvt_pk_bf16_f32 v218, v112, v113\n"
      "v_mfma_f32_16x16x32_bf16 v[144:147], a[64:67], a[8:11], v[232:235]\n"
      "s_max_i32 s76, s76, 70\n"
      "v_exp_f32 v114, v182\n"
      "v_mfma_f32_16x16x32_bf16 v[144:147], a[72:75], a[24:27], v[144:147]\n"
      "s_mov_b32 s82, s68\n"
      "ds_read_b64_tr_b16 a[116:117], v245 offset:1024\n"
      "ds_read_b64_tr_b16 a[118:119], v245 offset:5120\n"
      "v_mfma_f32_16x16x32_bf16 v[144:147], a[80:83], a[40:43], v[144:147]\n"
      "s_lshr_b32 s69, s86, 1\n"
      "v_exp_f32 v115, v183\n"
      "v_mfma_f32_16x16x32_bf16 v[144:147], a[88:91], a[56:59], v[144:147]\n"
      "s_add_u32 s66, s66, s69\n"
      "ds_read_b64_tr_b16 a[120:121], v246 offset:1024\n"
      "v_cvt_pk_bf16_f32 v219, v114, v115\n"
      "v_mfma_f32_16x16x32_bf16 v[148:151], a[92:95], a[56:59], v[232:235]\n"
      "s_add_u32 s82, s82, s69\n"
      "v_exp_f32 v112, v184\n"
      "v_mfma_f32_16x16x32_bf16 v[148:151], a[84:87], a[40:43], v[148:151]\n"
      "s_add_u32 s65, s82, s76\n"
      "ds_read_b64_tr_b16 a[122:123], v246 offset:5120\n"
      "v_or3_b32 v116, v116, v218, v219\n"
      "v_mfma_f32_16x16x32_bf16 v[148:151], a[76:79], a[24:27], v[148:151]\n"
      "s_sub_u32 s93, s93, 1\n"
      "v_exp_f32 v113, v185\n"
      "v_mfma_f32_16x16x32_bf16 v[148:151], a[68:71], a[8:11], v[148:151]\n"
      "s_cmp_le_i32 s93, 14\n"
      "ds_read_b64_tr_b16 a[124:125], v247 offset:1024\n"
      "v_cvt_pk_bf16_f32 v220, v112, v113\n"
      "v_mfma_f32_16x16x32_bf16 v[156:159], a[68:71], a[12:15], v[236:239]\n"
      "s_cselect_b32 s86, 107, s86\n"
      "v_exp_f32 v114, v186\n"
      "v_mfma_f32_16x16x32_bf16 v[156:159], a[76:79], a[28:31], v[156:159]\n"
      "s_sub_u32 s67, s65, s66\n"
      "ds_read_b64_tr_b16 a[126:127], v247 offset:5120\n"
      "v_mfma_f32_16x16x32_bf16 v[156:159], a[84:87], a[44:47], v[156:159]\n"
      "s_cmp_lt_i32 s67, 0\n"
      "v_exp_f32 v115, v187\n"
      "v_mfma_f32_16x16x32_bf16 v[156:159], a[92:95], a[60:63], v[156:159]\n"
      "s_cselect_b32 s88, s66, s65\n"
      "v_cvt_pk_bf16_f32 v221, v114, v115\n"
      "v_mfma_f32_16x16x32_bf16 v[152:155], a[88:91], a[60:63], v[236:239]\n"
      "s_add_u32 s90, s88, s91\n"
      "v_exp_f32 v112, v188\n"
      "v_mfma_f32_16x16x32_bf16 v[152:155], a[80:83], a[44:47], v[152:155]\n"
      "v_or3_b32 v116, v116, v220, v221\n"
      "v_mfma_f32_16x16x32_bf16 v[152:155], a[72:75], a[28:31], v[152:155]\n"
      "v_exp_f32 v113, v189\n"
      "v_mfma_f32_16x16x32_bf16 v[152:155], a[64:67], a[12:15], v[152:155]\n"
      "s_waitcnt lgkmcnt(10)\n"
      "v_cvt_pk_bf16_f32 v222, v112, v113\n"
      "v_mfma_f32_16x16x32_bf16 v[32:35], a[96:99], v[192:195], v[32:35]\n"
      "v_exp_f32 v114, v190\n"
      "v_mfma_f32_16x16x32_bf16 v[48:51], a[100:103], v[192:195], v[48:51]\n"
      "v_mfma_f32_16x16x32_bf16 a[128:131], a[104:107], v[192:195], a[128:131]\n"
      "s_waitcnt lgkmcnt(6)\n"
      "v_exp_f32 v115, v191\n"
      "v_mfma_f32_16x16x32_bf16 a[144:147], a[108:111], v[192:195], a[144:147]\n"
      "v_cvt_pk_bf16_f32 v223, v114, v115\n"
      "v_mfma_f32_16x16x32_bf16 v[64:67], a[112:115], v[192:195], v[64:67]\n"
      "s_waitcnt lgkmcnt(2)\n"
      "v_mfma_f32_16x16x32_bf16 v[80:83], a[116:119], v[192:195], v[80:83]\n"
      "v_or3_b32 v116, v116, v222, v223\n"
      "v_mfma_f32_16x16x32_bf16 a[160:163], a[120:123], v[192:195], a[160:163]\n"
      "s_mov_b32 s64, 2\n"
      "s_waitcnt lgkmcnt(0)\n"
      "v_and_b32 v116, 0x40004000, v116\n"
      "v_mfma_f32_16x16x32_bf16 a[176:179], a[124:127], v[192:195], a[176:179]\n"
      "v_cmp_ne_u32 vcc, 0, v116\n"
      "s_cbranch_vccnz FB1_%=\n"
      "RET1_2_%=:\n"
      "v_mfma_f32_16x16x32_bf16 a[180:183], a[124:127], v[196:199], a[180:183]\n"
      "v_exp_f32 v112, v128\n"
      "v_mfma_f32_16x16x32_bf16 a[164:167], a[120:123], v[196:199], a[164:167]\n"
      "v_mfma_f32_16x16x32_bf16 a[192:195], a[196:199], v[208:211], a[192:195]\n"
      "s_add_u32 m0, s77, 16384\n"
      "v_exp_f32 v113, v129\n"
      "v_mfma_f32_16x16x32_bf16 v[84:87], a[116:119], v[196:199], v[84:87]\n"
      "buffer_load_dwordx4 v249, %[srdv], s61 offen nt lds\n"
      "v_cvt_pk_bf16_f32 v192, v112, v113\n"
      "v_mfma_f32_16x16x32_bf16 v[68:71], a[112:115], v[196:199], v[68:71]\n"
      "s_add_u32 m0, s77, 17408\n"
      "v_exp_f32 v114, v130\n"
      "v_mfma_f32_16x16x32_bf16 a[192:195], a[200:203], v[212:215], a[192:195]\n"
      "buffer_load_dwordx4 v121, %[srdv], s61 offen nt lds\n"
      "v_mfma_f32_16x16x32_bf16 a[148:151], a[108:111], v[196:199], a[148:151]\n"
      "s_add_u32 m0, s77, 24576\n"
      "v_exp_f32 v115, v131\n"
      "v_mfma_f32_16x16x32_bf16 a[132:135], a[104:107], v[196:199], a[132:135]\n"
      "s_memrealtime s[68:69]\n"
      "buffer_load_dwordx4 v122, %[srdv], s61 offen nt lds\n"
      "v_cvt_pk_bf16_f32 v193, v114, v115\n"
      "v_mfma_f32_16x16x32_bf16 a[192:195], a[204:207], v[216:219], a[192:195]\n"
      "s_add_u32 m0, s77, 25600\n"
      "v_exp_f32 v112, v132\n"
      "v_mfma_f32_16x16x32_bf16 v[52:55], a[100:103], v[196:199], v[52:55]\n"
      "s_add_u32 s77, s77, 0x8000\n"
      "buffer_load_dwordx4 v123, %[srdv], s61 offen nt lds\n"
      "v_or_b32 v116, v192, v193\n"
      "v_mfma_f32_16x16x32_bf16 v[36:39], a[96:99], v[196:199], v[36:39]\n"
      "s_and_b32 s77, s77, 0x1ffff\n"
      "v_exp_f32 v113, v133\n"
      "v_mfma_f32_16x16x32_bf16 a[192:195], a[208:211], v[220:223], a[192:195]\n"
      "s_add_u32 s61, s61, %[inc]\n"
      "v_cvt_pk_bf16_f32 v194, v112, v113\n"
      "s_waitcnt vmcnt(9) lgkmcnt(0)\n"
      "s_barrier\n"
      "v_mfma_f32_16x16x32_bf16 v[40:43], a[96:99], v[200:203], v[40:43]\n"
      "ds_read_b128 a[64:67], v242 offset:32768\n"
      "v_exp_f32 v114, v134\n"
      "v_mfma_f32_16x16x32_bf16 v[44:47], a[96:99], v[204:207], v[44:47]\n"
      "ds_read_b128 a[68:71], v242 offset:36864\n"
      "ds_read_b128 a[72:75], v243 offset:32768\n"
      "v_mfma_f32_16x16x32_bf16 v[60:63], a[100:103], v[204:207], v[60:63]\n"
      "v_exp_f32 v115, v135\n"
      "v_mfma_f32_16x16x32_bf16 v[56:59], a[100:103], v[200:203], v[56:59]\n"
      "ds_read_b128 a[76:79], v243 offset:36864\n"
      "v_cvt_pk_bf16_f32 v195, v114, v115\n"
      "v_mfma_f32_16x16x32_bf16 a[136:139], a[104:107], v[200:203], a[136:139]\n"
      "v_exp_f32 v112, v136\n"
      "v_mfma_f32_16x16x32_bf16 a[140:143], a[104:107], v[204:207], a[140:143]\n"
      "ds_read_b128 a[80:83], v242 offset:33792\n"
      "v_or3_b32 v116, v116, v194, v195\n"
      "v_mfma_f32_16x16x32_bf16 a[156:159], a[108:111], v[204:207], a[156:159]\n"
      "v_exp_f32 v113, v137\n"
      "v_mfma_f32_16x16x32_bf16 a[152:155], a[108:111], v[200:203], a[152:155]\n"
      "ds_read_b128 a[84:87], v242 offset:37888\n"
      "v_cvt_pk_bf16_f32 v196, v112, v113\n"
      "v_mfma_f32_16x16x32_bf16 v[72:75], a[112:115], v[200:203], v[72:75]\n"
      "v_exp_f32 v114, v138\n"
      "v_mfma_f32_16x16x32_bf16 v[76:79], a[112:115], v[204:207], v[76:79]\n"
      "ds_read_b128 a[88:91], v243 offset:33792\n"
      "ds_read_b128 a[92:95], v243 offset:37888\n"
      "v_mfma_f32_16x16x32_bf16 v[92:95], a[116:119], v[204:207], v[92:95]\n"
      "v_exp_f32 v115, v139\n"
      "v_mfma_f32_16x16x32_bf16 v[88:91], a[116:119], v[200:203], v[88:91]\n"
      "v_cvt_pk_bf16_f32 v197, v114, v115\n"
      "v_mfma_f32_16x16x32_bf16 a[168:171], a[120:123], v[200:203], a[168:171]\n"
      "v_exp_f32 v112, v140\n"
      "v_mfma_f32_16x16x32_bf16 a[172:175], a[120:123], v[204:207], a[172:175]\n"
      "v_or3_b32 v116, v116, v196, v197\n"
      "v_mfma_f32_16x16x32_bf16 a[188:191], a[124:127], v[204:207], a[188:191]\n"
      "v_exp_f32 v113, v141\n"
      "v_mfma_f32_16x16x32_bf16 a[184:187], a[124:127], v[200:203], a[184:187]\n"
      "s_waitcnt lgkmcnt(0)\n"
      "v_cvt_pk_bf16_f32 v198, v112, v113\n"
      "v_mfma_f32_16x16x32_bf16 v[160:163], a[64:67], a[0:3], v[224:227]\n"
      "v_exp_f32 v114, v142\n"
      "v_mfma_f32_16x16x32_bf16 v[160:163], a[72:75], a[16:19], v[160:163]\n"
      "ds_read_b64_tr_b16 a[96:97], v244 offset:32768\n"
      "ds_read_b64_tr_b16 a[98:99], v244 offset:36864\n"
      "v_mfma_f32_16x16x32_bf16 v[160:163], a[80:83], a[32:35], v[160:163]\n"
      "v_exp_f32 v115, v143\n"
      "v_mfma_f32_16x16x32_bf16 v[160:163], a[88:91], a[48:51], v[160:163]\n"
      "ds_read_b64_tr_b16 a[100:101], v245 offset:32768\n"
      "v_cvt_pk_bf16_f32 v199, v114, v115\n"
      "v_mfma_f32_16x16x32_bf16 v[164:167], a[92:95], a[48:51], v[224:227]\n"
      "v_exp_f32 v112, v144\n"
      "v_mfma_f32_16x16x32_bf16 v[164:167], a[84:87], a[32:35], v[164:167]\n"
      "ds_read_b64_tr_b16 a[102:103], v245 offset:36864\n"
      "v_or3_b32 v116, v116, v198, v199\n"
      "v_mfma_f32_16x16x32_bf16 v[164:167], a[76:79], a[16:19], v[164:167]\n"
      "v_exp_f32 v113, v145\n"
      "v_mfma_f32_16x16x32_bf16 v[164:167], a[68:71], a[0:3], v[164:167]\n"
      "ds_read_b64_tr_b16 a[104:105], v246 offset:32768\n"
      "v_cvt_pk_bf16_f32 v200, v112, v113\n"
      "v_mfma_f32_16x16x32_bf16 v[172:175], a[68:71], a[4:7], v[228:231]\n"
      "v_exp_f32 v114, v146\n"
      "v_mfma_f32_16x16x32_bf16 v[172:175], a[76:79], a[20:23], v[172:175]\n"
      "ds_read_b64_tr_b16 a[106:107], v246 offset:36864\n"
      "ds_read_b64_tr_b16 a[108:109], v247 offset:32768\n"
      "v_mfma_f32_16x16x32_bf16 v[172:175], a[84:87], a[36:39], v[172:175]\n"
      "v_exp_f32 v115, v147\n"
      "v_mfma_f32_16x16x32_bf16 v[172:175], a[92:95], a[52:55], v[172:175]\n"
      "ds_read_b64_tr_b16 a[110:111], v247 offset:36864\n"
      "v_cvt_pk_bf16_f32 v201, v114, v115\n"
      "v_mfma_f32_16x16x32_bf16 v[168:171], a[88:91], a[52:55], v[228:231]\n"
      "v_exp_f32 v112, v148\n"
      "v_mfma_f32_16x16x32_bf16 v[168:171], a[80:83], a[36:39], v[168:171]\n"
      "ds_read_b64_tr_b16 a[112:113], v244 offset:33792\n"
      "v_or3_b32 v116, v116, v200, v201\n"
      "v_mfma_f32_16x16x32_bf16 v[168:171], a[72:75], a[20:23], v[168:171]\n"
      "v_exp_f32 v113, v149\n"
      "v_mfma_f32_16x16x32_bf16 v[168:171], a[64:67], a[4:7], v[168:171]\n"
      "ds_read_b64_tr_b16 a[114:115], v244 offset:37888\n"
      "v_cvt_pk_bf16_f32 v202, v112, v113\n"
      "v_mfma_f32_16x16x32_bf16 v[176:179], a[64:67], a[8:11], v[232:235]\n"
      "v_exp_f32 v114, v150\n"
      "v_mfma_f32_16x16x32_bf16 v[176:179], a[72:75], a[24:27], v[176:179]\n"
      "ds_read_b64_tr_b16 a[116:117], v245 offset:33792\n"
      "ds_read_b64_tr_b16 a[118:119], v245 offset:37888\n"
      "v_mfma_f32_16x16x32_bf16 v[176:179], a[80:83], a[40:43], v[176:179]\n"
      "v_exp_f32 v115, v151\n"
      "v_mfma_f32_16x16x32_bf16 v[176:179], a[88:91], a[56:59], v[176:179]\n"
      "ds_read_b64_tr_b16 a[120:121], v246 offset:33792\n"
      "v_cvt_pk_bf16_f32 v203, v114, v115\n"
      "v_mfma_f32_16x16x32_bf16 v[180:183], a[92:95], a[56:59], v[232:235]\n"
      "v_exp_f32 v112, v152\n"
      "v_mfma_f32_16x16x32_bf16 v[180:183], a[84:87], a[40:43], v[180:183]\n"
      "ds_read_b64_tr_b16 a[122:123], v246 offset:37888\n"
      "v_or3_b32 v116, v116, v202, v203\n"
      "v_mfma_f32_16x16x32_bf16 v[180:183], a[76:79], a[24:27], v[180:183]\n"
      "v_exp_f32 v113, v153\n"
      "v_mfma_f32_16x16x32_bf16 v[180:183], a[68:71], a[8:11], v[180:183]\n"
      "ds_read_b64_tr_b16 a[124:125], v247 offset:33792\n"
      "v_cvt_pk_bf16_f32 v204, v112, v113\n"
      "v_mfma_f32_16x16x32_bf16 v[188:191], a[68:71], a[12:15], v[236:239]\n"
      "v_exp_f32 v114, v154\n"
      "v_mfma_f32_16x16x32_bf16 v[188:191], a[76:79], a[28:31], v[188:191]\n"
      "ds_read_b64_tr_b16 a[126:127], v247 offset:37888\n"
      "v_mfma_f32_16x16x32_bf16 v[188:191], a[84:87], a[44:47], v[188:191]\n"
      "v_exp_f32 v115, v155\n"
      "v_mfma_f32_16x16x32_bf16 v[188:191], a[92:95], a[60:63], v[188:191]\n"
      "v_cvt_pk_bf16_f32 v205, v114, v115\n"
      "v_mfma_f32_16x16x32_bf16 v[184:187], a[88:91], a[60:63], v[236:239]\n"
      "v_exp_f32 v112, v156\n"
      "v_mfma_f32_16x16x32_bf16 v[184:187], a[80:83], a[44:47], v[184:187]\n"
      "v_or3_b32 v116, v116, v204, v205\n"
      "v_mfma_f32_16x16x32_bf16 v[184:187], a[72:75], a[28:31], v[184:187]\n"
      "v_exp_f32 v113, v157\n"
      "v_mfma_f32_16x16x32_bf16 v[184:187], a[64:67], a[12:15], v[184:187]\n"
      "s_waitcnt lgkmcnt(10)\n"
      "v_cvt_pk_bf16_f32 v206, v112, v113\n"
      "v_mfma_f32_16x16x32_bf16 v[32:35], a[96:99], v[208:211], v[32:35]\n"
      "v_exp_f32 v114, v158\n"
      "v_mfma_f32_16x16x32_bf16 v[48:51], a[100:103], v[208:211], v[48:51]\n"
      "v_mfma_f32_16x16x32_bf16 a[128:131], a[104:107], v[208:211], a[128:131]\n"
      "s_waitcnt lgkmcnt(6)\n"
      "v_exp_f32 v115, v159\n"
      "v_mfma_f32_16x16x32_bf16 a[144:147], a[108:111], v[208:211], a[144:147]\n"
      "v_cvt_pk_bf16_f32 v207, v114, v115\n"
      "v_mfma_f32_16x16x32_bf16 v[64:67], a[112:115], v[208:211], v[64:67]\n"
      "s_waitcnt lgkmcnt(2)\n"
      "v_mfma_f32_16x16x32_bf16 v[80:83], a[116:119], v[208:211], v[80:83]\n"
      "v_or3_b32 v116, v116, v206, v207\n"
      "v_mfma_f32_16x16x32_bf16 a[160:163], a[120:123], v[208:211], a[160:163]\n"
      "s_mov_b32 s64, 2\n"
      "s_waitcnt lgkmcnt(0)\n"
      "v_and_b32 v116, 0x40004000, v116\n"
      "v_mfma_f32_16x16x32_bf16 a[176:179], a[124:127], v[208:211], a[176:179]\n"
      "v_cmp_ne_u32 vcc, 0, v116\n"
      "s_cbranch_vccnz FB0_%=\n"
      "RET0_2_%=:\n"
      "v_mfma_f32_16x16x32_bf16 a[180:183], a[124:127], v[212:215], a[180:183]\n"
      "v_exp_f32 v112, v160\n"
      "v_mfma_f32_16x16x32_bf16 a[164:167], a[120:123], v[212:215], a[164:167]\n"
      "v_xor_b32 v242, 0x10000, v242\n"
      "v_mfma_f32_16x16x32_bf16 a[192:195], a[196:199], v[192:195], a[192:195]\n"
      "s_add_u32 m0, s62, 0\n"
      "v_exp_f32 v113, v161\n"
      "v_mfma_f32_16x16x32_bf16 v[84:87], a[116:119], v[212:215], v[84:87]\n"
      "buffer_load_dwordx4 v248, %[srdk], s60 offen nt lds\n"
      "v_cvt_pk_bf16_f32 v208, v112, v113\n"
      "v_xor_b32 v243, 0x10000, v243\n"
      "v_mfma_f32_16x16x32_bf16 v[68:71], a[112:115], v[212:215], v[68:71]\n"
      "s_add_u32 m0, s62, 896\n"
      "v_exp_f32 v114, v162\n"
      "v_mfma_f32_16x16x32_bf16 a[192:195], a[200:203], v[196:199], a[192:195]\n"
      "buffer_load_dwordx4 v248, %[srdk], s60 offen offset:128 nt lds\n"
      "v_xor_b32 v244, 0x10000, v244\n"
      "v_mfma_f32_16x16x32_bf16 a[148:151], a[108:111], v[212:215], a[148:151]\n"
      "s_add_u32 m0, s62, 8192\n"
      "v_exp_f32 v115, v163\n"
      "v_mfma_f32_16x16x32_bf16 a[132:135], a[104:107], v[212:215], a[132:135]\n"
      "s_memrealtime s[68:69]\n"
      "buffer_load_dwordx4 v120, %[srdk], s60 offen nt lds\n"
      "v_cvt_pk_bf16_f32 v209, v114, v115\n"
      "v_xor_b32 v245, 0x10000, v245\n"
      "v_mfma_f32_16x16x32_bf16 a[192:195], a[204:207], v[200:203], a[192:195]\n"
      "s_add_u32 m0, s62, 9088\n"
      "v_exp_f32 v112, v164\n"
      "v_mfma_f32_16x16x32_bf16 v[52:55], a[100:103], v[212:215], v[52:55]\n"
      "s_add_u32 s62, s62, 0x8000\n"
      "buffer_load_dwordx4 v120, %[srdk], s60 offen offset:128 nt lds\n"
      "v_or_b32 v116, v208, v209\n"
      "v_xor_b32 v246, 0x10000, v246\n"
      "v_mfma_f32_16x16x32_bf16 v[36:39], a[96:99], v[212:215], v[36:39]\n"
      "s_and_b32 s62, s62, 0x1ffff\n"
      "v_exp_f32 v113, v165\n"
      "v_mfma_f32_16x16x32_bf16 a[192:195], a[208:211], v[204:207], a[192:195]\n"
      "s_add_u32 s60, s60, s78\n"
      "v_cvt_pk_bf16_f32 v210, v112, v113\n"
      "v_xor_b32 v247, 0x10000, v247\n"
      "s_waitcnt vmcnt(8)\n"
      "s_sub_u32 s63, %[n2], 1\n"
      "LOOP2_%=:\n"
      "s_waitcnt vmcnt(9) lgkmcnt(0)\n"
      "s_sub_u32 s67, s68, s90\n"
      "s_cmp_lt_i32 s67, 0\n"
      "s_cbranch_scc1 GW4_%=\n"
      "GX4_%=:\n"
      "s_barrier\n"
      "v_mfma_f32_16x16x32_bf16 v[40:43], a[96:99], v[216:219], v[40:43]\n"
      "s_memrealtime s[68:69]\n"
      "ds_read_b128 a[64:67], v242 offset:0\n"
      "v_exp_f32 v114, v166\n"
      "v_mfma_f32_16x16x32_bf16 v[44:47], a[96:99], v[220:223], v[44:47]\n"
      "s_add_u32 m0, s77, 16384\n"
      "ds_read_b128 a[68:71], v242 offset:4096\n"
      "ds_read_b128 a[72:75], v243 offset:0\n"
      "v_mfma_f32_16x16x32_bf16 v[60:63], a[100:103], v[220:223], v[60:63]\n"
      "buffer_load_dwordx4 v249, %[srdv], s61 offen nt lds\n"
      "v_exp_f32 v115, v167\n"
      "v_mfma_f32_16x16x32_bf16 v[56:59], a[100:103], v[216:219], v[56:59]\n"
      "s_add_u32 m0, s77, 17408\n"
      "ds_read_b128 a[76:79], v243 offset:4096\n"
      "v_cvt_pk_bf16_f32 v211, v114, v115\n"
      "v_mfma_f32_16x16x32_bf16 a[136:139], a[104:107], v[216:219], a[136:139]\n"
      "buffer_load_dwordx4 v121, %[srdv], s61 offen nt lds\n"
      "v_exp_f32 v112, v168\n"
      "v_mfma_f32_16x16x32_bf16 a[140:143], a[104:107], v[220:223], a[140:143]\n"
      "s_add_u32 m0, s77, 24576\n"
      "ds_read_b128 a[80:83], v242 offset:1024\n"
      "v_or3_b32 v116, v116, v210, v211\n"
      "v_mfma_f32_16x16x32_bf16 a[156:159], a[108:111], v[220:223], a[156:159]\n"
      "buffer_load_dwordx4 v122, %[srdv], s61 offen nt lds\n"
      "v_exp_f32 v113, v169\n"
      "v_mfma_f32_16x16x32_bf16 a[152:155], a[108:111], v[216:219], a[152:155]\n"
      "s_add_u32 m0, s77, 25600\n"
      "ds_read_b128 a[84:87], v242 offset:5120\n"
      "v_cvt_pk_bf16_f32 v212, v112, v113\n"
      "v_mfma_f32_16x16x32_bf16 v[72:75], a[112:115], v[216:219], v[72:75]\n"
      "s_add_u32 s77, s77, 0x8000\n"
      "buffer_load_dwordx4 v123, %[srdv], s61 offen nt lds\n"
      "v_exp_f32 v114, v170\n"
      "v_mfma_f32_16x16x32_bf16 v[76:79], a[112:115], v[220:223], v[76:79]\n"
      "s_and_b32 s77, s77, 0x1ffff\n"
      "ds_read_b128 a[88:91], v243 offset:1024\n"
      "ds_read_b128 a[92:95], v243 offset:5120\n"
      "v_mfma_f32_16x16x32_bf16 v[92:95], a[116:119], v[220:223], v[92:95]\n"
      "s_add_u32 s61, s61, s78\n"
      "v_exp_f32 v115, v171\n"
      "v_mfma_f32_16x16x32_bf16 v[88:91], a[116:119], v[216:219], v[88:91]\n"
      "v_cvt_pk_bf16_f32 v213, v114, v115\n"
      "v_mfma_f32_16x16x32_bf16 a[168:171], a[120:123], v[216:219], a[168:171]\n"
      "v_exp_f32 v112, v172\n"
      "v_mfma_f32_16x16x32_bf16 a[172:175], a[120:123], v[220:223], a[172:175]\n"
      "v_or3_b32 v116, v116, v212, v213\n"
      "v_mfma_f32_16x16x32_bf16 a[188:191], a[124:127], v[220:223], a[188:191]\n"
      "v_exp_f32 v113, v173\n"
      "v_mfma_f32_16x16x32_bf16 a[184:187], a[124:127], v[216:219], a[184:187]\n"
      "s_waitcnt lgkmcnt(0)\n"
      "v_cvt_pk_bf16_f32 v214, v112, v113\n"
      "v_mfma_f32_16x16x32_bf16 v[128:131], a[64:67], a[0:3], v[224:227]\n"
      "s_sub_u32 s67, s68, s66\n"
      "v_exp_f32 v114, v174\n"
      "v_mfma_f32_16x16x32_bf16 v[128:131], a[72:75], a[16:19], v[128:131]\n"
      "s_cmp_gt_i32 s67, s70\n"
      "v_mfma_f32_16x16x32_bf16 v[128:131], a[80:83], a[32:35], v[128:131]\n"
      "s_cselect_b32 s69, s86, 0\n"
      "v_exp_f32 v115, v175\n"
      "v_mfma_f32_16x16x32_bf16 v[128:131], a[88:91], a[48:51], v[128:131]\n"
      "s_add_u32 s66, s66, s69\n"
      "v_cvt_pk_bf16_f32 v215, v114, v115\n"
      "v_mfma_f32_16x16x32_bf16 v[132:135], a[92:95], a[48:51], v[224:227]\n"
      "s_add_u32 s67, s67, s101\n"
      "v_exp_f32 v112, v176\n"
      "v_mfma_f32_16x16x32_bf16 v[132:135], a[84:87], a[32:35], v[132:135]\n"
      "s_min_i32 s70, s70, s67\n"
      "v_or3_b32 v116, v116, v214, v215\n"
      "v_mfma_f32_16x16x32_bf16 v[132:135], a[76:79], a[16:19], v[132:135]\n"
      "s_max_i32 s70, s70, 85\n"
      "v_exp_f32 v113, v177\n"
      "v_mfma_f32_16x16x32_bf16 v[132:135], a[68:71], a[0:3], v[132:135]\n"
      "s_add_u32 s66, s66, s86\n"
      "v_cvt_pk_bf16_f32 v216, v112, v113\n"
      "v_mfma_f32_16x16x32_bf16 v[140:143], a[68:71], a[4:7], v[228:231]\n"
      "s_and_b32 s76, s80, 31\n"
      "v_exp_f32 v114, v178\n"
      "v_mfma_f32_16x16x32_bf16 v[140:143], a[76:79], a[20:23], v[140:143]\n"
      "s_lshr_b64 s[80:81], s[80:81], 5\n"
      "v_mfma_f32_16x16x32_bf16 v[140:143], a[84:87], a[36:39], v[140:143]\n"
      "s_add_u32 s66, s66, s76\n"
      "v_exp_f32 v115, v179\n"
      "v_mfma_f32_16x16x32_bf16 v[140:143], a[92:95], a[52:55], v[140:143]\n"
      "s_sub_u32 s67, s68, s82\n"
      "v_cvt_pk_bf16_f32 v217, v114, v115\n"
      "v_mfma_f32_16x16x32_bf16 v[136:139], a[88:91], a[52:55], v[228:231]\n"
      "s_min_i32 s76, s67, s83\n"
      "v_exp_f32 v112, v180\n"
      "s_waitcnt vmcnt(8)\n"
      "s_barrier\n"
      "v_mfma_f32_16x16x32_bf16 v[136:139], a[80:83], a[36:39], v[136:139]\n"
      "s_mov_b32 s83, s67\n"
      "ds_read_b64_tr_b16 a[96:97], v244 offset:0\n"
      "v_or3_b32 v116, v116, v216, v217\n"
      "v_mfma_f32_16x16x32_bf16 v[136:139], a[72:75], a[20:23], v[136:139]\n"
      "s_sub_u32 s76, s76, 36\n"
      "v_exp_f32 v113, v181\n"
      "v_mfma_f32_16x16x32_bf16 v[136:139], a[64:67], a[4:7], v[136:139]\n"
      "s_min_i32 s76, s76, 148\n"
      "ds_read_b64_tr_b16 a[98:99], v244 offset:4096\n"
      "v_cvt_pk_bf16_f32 v218, v112, v113\n"
      "v_mfma_f32_16x16x32_bf16 v[144:147], a[64:67], a[8:11], v[232:235]\n"
      "s_max_i32 s76, s76, 70\n"
      "v_exp_f32 v114, v182\n"
      "v_mfma_f32_16x16x32_bf16 v[144:147], a[72:75], a[24:27], v[144:147]\n"
      "s_mov_b32 s82, s68\n"
      "ds_read_b64_tr_b16 a[100:101], v245 offset:0\n"
      "ds_read_b64_tr_b16 a[102:103], v245 offset:4096\n"
      "v_mfma_f32_16x16x32_bf16 v[144:147], a[80:83], a[40:43], v[144:147]\n"
      "s_add_u32 s65, s82, s76\n"
      "v_exp_f32 v115, v183\n"
      "v_mfma_f32_16x16x32_bf16 v[144:147], a[88:91], a[56:59], v[144:147]\n"
      "s_sub_u32 s93, s93, 1\n"
      "ds_read_b64_tr_b16 a[104:105], v246 offset:0\n"
      "v_cvt_pk_bf16_f32 v219, v114, v115\n"
      "v_mfma_f32_16x16x32_bf16 v[148:151], a[92:95], a[56:59], v[232:235]\n"
      "s_cmp_le_i32 s93, 14\n"
      "v_exp_f32 v112, v184\n"
      "v_mfma_f32_16x16x32_bf16 v[148:151], a[84:87], a[40:43], v[148:151]\n"
      "s_cselect_b32 s86, 107, s86\n"
      "ds_read_b64_tr_b16 a[106:107], v246 offset:4096\n"
      "v_or3_b32 v116, v116, v218, v219\n"
      "v_mfma_f32_16x16x32_bf16 v[148:151], a[76:79], a[24:27], v[148:151]\n"
      "s_sub_u32 s67, s65, s66\n"
      "v_exp_f32 v113, v185\n"
      "v_mfma_f32_16x16x32_bf16 v[148:151], a[68:71], a[8:11], v[148:151]\n"
      "s_cmp_lt_i32 s67, 0\n"
      "ds_read_b64_tr_b16 a[108:109], v247 offset:0\n"
      "v_cvt_pk_bf16_f32 v220, v112, v113\n"
      "v_mfma_f32_16x16x32_bf16 v[156:159], a[68:71], a[12:15], v[236:239]\n"
      "s_cselect_b32 s88, s66, s65\n"
      "v_exp_f32 v114, v186\n"
      "v_mfma_f32_16x16x32_bf16 v[156:159], a[76:79], a[28:31], v[156:159]\n"
      "s_add_u32 s90, s88, s91\n"
      "ds_read_b64_tr_b16 a[110:111], v247 offset:4096\n"
      "ds_read_b64_tr_b16 a[112:113], v244 offset:1024\n"
      "v_mfma_f32_16x16x32_bf16 v[156:159], a[84:87], a[44:47], v[156:159]\n"
      "v_exp_f32 v115, v187\n"
      "v_mfma_f32_16x16x32_bf16 v[156:159], a[92:95], a[60:63], v[156:159]\n"
      "ds_read_b64_tr_b16 a[114:115], v244 offset:5120\n"
      "v_cvt_pk_bf16_f32 v221, v114, v115\n"
      "v_mfma_f32_16x16x32_bf16 v[152:155], a[88:91], a[60:63], v[236:239]\n"
      "v_exp_f32 v112, v188\n"
      "v_mfma_f32_16x16x32_bf16 v[152:155], a[80:83], a[44:47], v[152:155]\n"
      "ds_read_b64_tr_b16 a[116:117], v245 offset:1024\n"
      "v_or3_b32 v116, v116, v220, v221\n"
      "v_mfma_f32_16x16x32_bf16 v[152:155], a[72:75], a[28:31], v[152:155]\n"
      "v_exp_f32 v113, v189\n"
      "v_mfma_f32_16x16x32_bf16 v[152:155], a[64:67], a[12:15], v[152:155]\n"
      "ds_read_b64_tr_b16 a[118:119], v245 offset:5120\n"
      "v_cvt_pk_bf16_f32 v222, v112, v113\n"
      "s_waitcnt lgkmcnt(6)\n"
      "v_mfma_f32_16x16x32_bf16 v[32:35], a[96:99], v[192:195], v[32:35]\n"
      "v_exp_f32 v114, v190\n"
      "v_mfma_f32_16x16x32_bf16 v[48:51], a[100:103], v[192:195], v[48:51]\n"
      "ds_read_b64_tr_b16 a[120:121], v246 offset:1024\n"
      "ds_read_b64_tr_b16 a[122:123], v246 offset:5120\n"
      "v_mfma_f32_16x16x32_bf16 a[128:131], a[104:107], v[192:195], a[128:131]\n"
      "s_waitcnt lgkmcnt(4)\n"
      "v_exp_f32 v115, v191\n"
      "v_mfma_f32_16x16x32_bf16 a[144:147], a[108:111], v[192:195], a[144:147]\n"
      "ds_read_b64_tr_b16 a[124:125], v247 offset:1024\n"
      "v_cvt_pk_bf16_f32 v223, v114, v115\n"
      "v_mfma_f32_16x16x32_bf16 v[64:67], a[112:115], v[192:195], v[64:67]\n"
      "ds_read_b64_tr_b16 a[126:127], v247 offset:5120\n"
      "s_waitcnt lgkmcnt(2)\n"
      "v_mfma_f32_16x16x32_bf16 v[80:83], a[116:119], v[192:195], v[80:83]\n"
      "v_or3_b32 v116, v116, v222, v223\n"
      "v_mfma_f32_16x16x32_bf16 a[160:163], a[120:123], v[192:195], a[160:163]\n"
      "s_mov_b32 s64, 3\n"
      "s_waitcnt lgkmcnt(0)\n"
      "v_and_b32 v116, 0x40004000, v116\n"
      "v_mfma_f32_16x16x32_bf16 a[176:179], a[124:127], v[192:195], a[176:179]\n"
      "v_cmp_ne_u32 vcc, 0, v116\n"
      "s_cbranch_vccnz FB1_%=\n"
      "RET1_3_%=:\n"
      "v_mfma_f32_16x16x32_bf16 a[180:183], a[124:127], v[196:199], a[180:183]\n"
      "v_exp_f32 v112, v128\n"
      "v_mfma_f32_16x16x32_bf16 a[164:167], a[120:123], v[196:199], a[164:167]\n"
      "v_mfma_f32_16x16x32_bf16 a[192:195], a[196:199], v[208:211], a[192:195]\n"
      "s_add_u32 m0, s62, 0\n"
      "v_exp_f32 v113, v129\n"
      "v_mfma_f32_16x16x32_bf16 v[84:87], a[116:119], v[196:199], v[84:87]\n"
      "buffer_load_dwordx4 v248, %[srdk], s60 offen nt lds\n"
      "v_cvt_pk_bf16_f32 v192, v112, v113\n"
      "v_mfma_f32_16x16x32_bf16 v[68:71], a[112:115], v[196:199], v[68:71]\n"
      "s_add_u32 m0, s62, 896\n"
      "v_exp_f32 v114, v130\n"
      "v_mfma_f32_16x16x32_bf16 a[192:195], a[200:203], v[212:215], a[192:195]\n"
      "buffer_load_dwordx4 v248, %[srdk], s60 offen offset:128 nt lds\n"
      "v_mfma_f32_16x16x32_bf16 a[148:151], a[108:111], v[196:199], a[148:151]\n"
      "s_add_u32 m0, s62, 8192\n"
      "v_exp_f32 v115, v131\n"
      "v_mfma_f32_16x16x32_bf16 a[132:135], a[104:107], v[196:199], a[132:135]\n"
      "s_memrealtime s[68:69]\n"
      "buffer_load_dwordx4 v120, %[srdk], s60 offen nt lds\n"
      "v_cvt_pk_bf16_f32 v193, v114, v115\n"
      "v_mfma_f32_16x16x32_bf16 a[192:195], a[204:207], v[216:219], a[192:195]\n"
      "s_add_u32 m0, s62, 9088\n"
      "v_exp_f32 v112, v132\n"
      "v_mfma_f32_16x16x32_bf16 v[52:55], a[100:103], v[196:199], v[52:55]\n"
      "s_add_u32 s62, s62, 0x8000\n"
      "buffer_load_dwordx4 v120, %[srdk], s60 offen offset:128 nt lds\n"
      "v_or_b32 v116, v192, v193\n"
      "v_mfma_f32_16x16x32_bf16 v[36:39], a[96:99], v[196:199], v[36:39]\n"
      "s_and_b32 s62, s62, 0x1ffff\n"
      "v_exp_f32 v113, v133\n"
      "v_mfma_f32_16x16x32_bf16 a[192:195], a[208:211], v[220:223], a[192:195]\n"
      "s_add_u32 s60, s60, %[inc]\n"
      "v_cvt_pk_bf16_f32 v194, v112, v113\n"
      "s_waitcnt vmcnt(8) lgkmcnt(0)\n"
      "s_sub_u32 s67, s68, s90\n"
      "s_cmp_lt_i32 s67, 0\n"
      "s_cbranch_scc1 GW5_%=\n"
      "GX5_%=:\n"
      "s_barrier\n"
      "v_mfma_f32_16x16x32_bf16 v[40:43], a[96:99], v[200:203], v[40:43]\n"
      "s_memrealtime s[68:69]\n"
      "buffer_load_dword v250, v251, %[srdr], 0 offen sc1\n"
      "ds_read_b128 a[64:67], v242 offset:32768\n"
      "v_exp_f32 v114, v134\n"
      "v_mfma_f32_16x16x32_bf16 v[44:47], a[96:99], v[204:207], v[44:47]\n"
      "s_add_u32 m0, s77, 16384\n"
      "ds_read_b128 a[68:71], v242 offset:36864\n"
      "ds_read_b128 a[72:75], v243 offset:32768\n"
      "v_mfma_f32_16x16x32_bf16 v[60:63], a[100:103], v[204:207], v[60:63]\n"
      "buffer_load_dwordx4 v249, %[srdv], s61 offen nt lds\n"
      "v_exp_f32 v115, v135\n"
      "v_mfma_f32_16x16x32_bf16 v[56:59], a[100:103], v[200:203], v[56:59]\n"
      "s_add_u32 m0, s77, 17408\n"
      "ds_read_b128 a[76:79], v243 offset:36864\n"
      "v_cvt_pk_bf16_f32 v195, v114, v115\n"
      "v_mfma_f32_16x16x32_bf16 a[136:139], a[104:107], v[200:203], a[136:139]\n"
      "buffer_load_dwordx4 v121, %[srdv], s61 offen nt lds\n"
      "v_exp_f32 v112, v136\n"
      "v_mfma_f32_16x16x32_bf16 a[140:143], a[104:107], v[204:207], a[140:143]\n"
      "s_add_u32 m0, s77, 24576\n"
      "ds_read_b128 a[80:83], v242 offset:33792\n"
      "v_or3_b32 v116, v116, v194, v195\n"
      "v_mfma_f32_16x16x32_bf16 a[156:159], a[108:111], v[204:207], a[156:159]\n"
      "buffer_load_dwordx4 v122, %[srdv], s61 offen nt lds\n"
      "v_exp_f32 v113, v137\n"
      "v_mfma_f32_16x16x32_bf16 a[152:155], a[108:111], v[200:203], a[152:155]\n"
      "s_add_u32 m0, s77, 25600\n"
      "ds_read_b128 a[84:87], v242 offset:37888\n"
      "v_cvt_pk_bf16_f32 v196, v112, v113\n"
      "v_mfma_f32_16x16x32_bf16 v[72:75], a[112:115], v[200:203], v[72:75]\n"
      "s_add_u32 s77, s77, 0x8000\n"
      "buffer_load_dwordx4 v123, %[srdv], s61 offen nt lds\n"
      "v_exp_f32 v114, v138\n"
      "v_mfma_f32_16x16x32_bf16 v[76:79], a[112:115], v[204:207], v[76:79]\n"
      "s_and_b32 s77, s77, 0x1ffff\n"
      "ds_read_b128 a[88:91], v243 offset:33792\n"
      "ds_read_b128 a[92:95], v243 offset:37888\n"
      "v_mfma_f32_16x16x32_bf16 v[92:95], a[116:119], v[204:207], v[92:95]\n"
      "s_add_u32 s61, s61, %[inc]\n"
      "v_exp_f32 v115, v139\n"
      "v_mfma_f32_16x16x32_bf16 v[88:91], a[116:119], v[200:203], v[88:91]\n"
      "v_cvt_pk_bf16_f32 v197, v114, v115\n"
      "v_mfma_f32_16x16x32_bf16 a[168:171], a[120:123], v[200:203], a[168:171]\n"
      "v_exp_f32 v112, v140\n"
      "v_mfma_f32_16x16x32_bf16 a[172:175], a[120:123], v[204:207], a[172:175]\n"
      "v_or3_b32 v116, v116, v196, v197\n"
      "v_mfma_f32_16x16x32_bf16 a[188:191], a[124:127], v[204:207], a[188:191]\n"
      "v_exp_f32 v113, v141\n"
      "v_mfma_f32_16x16x32_bf16 a[184:187], a[124:127], v[200:203], a[184:187]\n"
      "s_waitcnt lgkmcnt(0)\n"
      "v_cvt_pk_bf16_f32 v198, v112, v113\n"
      "v_mfma_f32_16x16x32_bf16 v[160:163], a[64:67], a[0:3], v[224:227]\n"
      "s_sub_u32 s67, s68, s66\n"
      "v_exp_f32 v114, v142\n"
      "v_mfma_f32_16x16x32_bf16 v[160:163], a[72:75], a[16:19], v[160:163]\n"
      "s_cmp_gt_i32 s67, s70\n"
      "v_mfma_f32_16x16x32_bf16 v[160:163], a[80:83], a[32:35], v[160:163]\n"
      "s_cselect_b32 s69, s86, 0\n"
      "v_exp_f32 v115, v143\n"
      "v_mfma_f32_16x16x32_bf16 v[160:163], a[88:91], a[48:51], v[160:163]\n"
      "s_add_u32 s66, s66, s69\n"
      "v_cvt_pk_bf16_f32 v199, v114, v115\n"
      "v_mfma_f32_16x16x32_bf16 v[164:167], a[92:95], a[48:51], v[224:227]\n"
      "s_add_u32 s67, s67, s101\n"
      "v_exp_f32 v112, v144\n"
      "v_mfma_f32_16x16x32_bf16 v[164:167], a[84:87], a[32:35], v[164:167]\n"
      "s_min_i32 s70, s70, s67\n"
      "v_or3_b32 v116, v116, v198, v199\n"
      "v_mfma_f32_16x16x32_bf16 v[164:167], a[76:79], a[16:19], v[164:167]\n"
      "s_max_i32 s70, s70, 85\n"
      "v_exp_f32 v113, v145\n"
      "v_mfma_f32_16x16x32_bf16 v[164:167], a[68:71], a[0:3], v[164:167]\n"
      "s_add_u32 s66, s66, s86\n"
      "v_cvt_pk_bf16_f32 v200, v112, v113\n"
      "v_mfma_f32_16x16x32_bf16 v[172:175], a[68:71], a[4:7], v[228:231]\n"
      "s_and_b32 s76, s80, 31\n"
      "v_exp_f32 v114, v146\n"
      "v_mfma_f32_16x16x32_bf16 v[172:175], a[76:79], a[20:23], v[172:175]\n"
      "s_lshr_b64 s[80:81], s[80:81], 5\n"
      "v_mfma_f32_16x16x32_bf16 v[172:175], a[84:87], a[36:39], v[172:175]\n"
      "s_add_u32 s66, s66, s76\n"
      "v_exp_f32 v115, v147\n"
      "v_mfma_f32_16x16x32_bf16 v[172:175], a[92:95], a[52:55], v[172:175]\n"
      "s_sub_u32 s67, s68, s82\n"
      "v_cvt_pk_bf16_f32 v201, v114, v115\n"
      "v_mfma_f32_16x16x32_bf16 v[168:171], a[88:91], a[52:55], v[228:231]\n"
      "s_min_i32 s76, s67, s83\n"
      "v_exp_f32 v112, v148\n"
      "s_waitcnt vmcnt(9)\n"
      "s_barrier\n"
      "v_mfma_f32_16x16x32_bf16 v[168:171], a[80:83], a[36:39], v[168:171]\n"
      "s_mov_b32 s83, s67\n"
      "ds_read_b64_tr_b16 a[96:97], v244 offset:32768\n"
      "v_or3_b32 v116, v116, v200, v201\n"
      "v_mfma_f32_16x16x32_bf16 v[168:171], a[72:75], a[20:23], v[168:171]\n"
      "s_sub_u32 s76, s76, 36\n"
      "v_exp_f32 v113, v149\n"
      "v_mfma_f32_16x16x32_bf16 v[168:171], a[64:67], a[4:7], v[168:171]\n"
      "s_min_i32 s76, s76, 148\n"
      "ds_read_b64_tr_b16 a[98:99], v244 offset:36864\n"
      "v_cvt_pk_bf16_f32 v202, v112, v113\n"
      "v_mfma_f32_16x16x32_bf16 v[176:179], a[64:67], a[8:11], v[232:235]\n"
      "s_max_i32 s76, s76, 70\n"
      "v_exp_f32 v114, v150\n"
      "v_mfma_f32_16x16x32_bf16 v[176:179], a[72:75], a[24:27], v[176:179]\n"
      "s_mov_b32 s82, s68\n"
      "ds_read_b64_tr_b16 a[100:101], v245 offset:32768\n"
      "ds_read_b64_tr_b16 a[102:103], v245 offset:36864\n"
      "v_mfma_f32_16x16x32_bf16 v[176:179], a[80:83], a[40:43], v[176:179]\n"
      "s_add_u32 s65, s82, s76\n"
      "v_exp_f32 v115, v151\n"
      "v_mfma_f32_16x16x32_bf16 v[176:179], a[88:91], a[56:59], v[176:179]\n"
      "s_sub_u32 s93, s93, 1\n"
      "ds_read_b64_tr_b16 a[104:105], v246 offset:32768\n"
      "v_cvt_pk_bf16_f32 v203, v114, v115\n"
      "v_mfma_f32_16x16x32_bf16 v[180:183], a[92:95], a[56:59], v[232:235]\n"
      "s_cmp_le_i32 s93, 14\n"
      "v_exp_f32 v112, v152\n"
      "v_mfma_f32_16x16x32_bf16 v[180:183], a[84:87], a[40:43], v[180:183]\n"
      "s_cselect_b32 s86, 107, s86\n"
      "ds_read_b64_tr_b16 a[106:107], v246 offset:36864\n"
      "v_or3_b32 v116, v116, v202, v203\n"
      "v_mfma_f32_16x16x32_bf16 v[180:183], a[76:79], a[24:27], v[180:183]\n"
      "s_sub_u32 s67, s65, s66\n"
      "v_exp_f32 v113, v153\n"
      "v_mfma_f32_16x16x32_bf16 v[180:183], a[68:71], a[8:11], v[180:183]\n"
      "s_cmp_lt_i32 s67, 0\n"
      "ds_read_b64_tr_b16 a[108:109], v247 offset:32768\n"
      "v_cvt_pk_bf16_f32 v204, v112, v113\n"
      "v_mfma_f32_16x16x32_bf16 v[188:191], a[68:71], a[12:15], v[236:239]\n"
      "s_cselect_b32 s88, s66, s65\n"
      "v_exp_f32 v114, v154\n"
      "v_mfma_f32_16x16x32_bf16 v[188:191], a[76:79], a[28:31], v[188:191]\n"
      "s_add_u32 s90, s88, s91\n"
      "ds_read_b64_tr_b16 a[110:111], v247 offset:36864\n"
      "ds_read_b64_tr_b16 a[112:113], v244 offset:33792\n"
      "v_mfma_f32_16x16x32_bf16 v[188:191], a[84:87], a[44:47], v[188:191]\n"
      "v_exp_f32 v115, v155\n"
      "v_mfma_f32_16x16x32_bf16 v[188:191], a[92:95], a[60:63], v[188:191]\n"
      "ds_read_b64_tr_b16 a[114:115], v244 offset:37888\n"
      "v_cvt_pk_bf16_f32 v205, v114, v115\n"
      "v_mfma_f32_16x16x32_bf16 v[184:187], a[88:91], a[60:63], v[236:239]\n"
      "v_exp_f32 v112, v156\n"
      "v_mfma_f32_16x16x32_bf16 v[184:187], a[80:83], a[44:47], v[184:187]\n"
      "ds_read_b64_tr_b16 a[116:117], v245 offset:33792\n"
      "v_or3_b32 v116, v116, v204, v205\n"
      "v_mfma_f32_16x16x32_bf16 v[184:187], a[72:75], a[28:31], v[184:187]\n"
      "v_exp_f32 v113, v157\n"
      "v_mfma_f32_16x16x32_bf16 v[184:187], a[64:67], a[12:15], v[184:187]\n"
      "ds_read_b64_tr_b16 a[118:119], v245 offset:37888\n"
      "v_cvt_pk_bf16_f32 v206, v112, v113\n"
      "s_waitcnt lgkmcnt(6)\n"
      "v_mfma_f32_16x16x32_bf16 v[32:35], a[96:99], v[208:211], v[32:35]\n"
      "v_exp_f32 v114, v158\n"
      "v_mfma_f32_16x16x32_bf16 v[48:51], a[100:103], v[208:211], v[48:51]\n"
      "ds_read_b64_tr_b16 a[120:121], v246 offset:33792\n"
      "ds_read_b64_tr_b16 a[122:123], v246 offset:37888\n"
      "v_mfma_f32_16x16x32_bf16 a[128:131], a[104:107], v[208:211], a[128:131]\n"
      "s_waitcnt lgkmcnt(4)\n"
      "v_exp_f32 v115, v159\n"
      "v_mfma_f32_16x16x32_bf16 a[144:147], a[108:111], v[208:211], a[144:147]\n"
      "ds_read_b64_tr_b16 a[124:125], v247 offset:33792\n"
      "v_cvt_pk_bf16_f32 v207, v114, v115\n"
      "v_mfma_f32_16x16x32_bf16 v[64:67], a[112:115], v[208:211], v[64:67]\n"
      "ds_read_b64_tr_b16 a[126:127], v247 offset:37888\n"
      "s_waitcnt lgkmcnt(2)\n"
      "v_mfma_f32_16x16x32_bf16 v[80:83], a[116:119], v[208:211], v[80:83]\n"
      "v_or3_b32 v116, v116, v206, v207\n"
      "v_mfma_f32_16x16x32_bf16 a[160:163], a[120:123], v[208:211], a[160:163]\n"
      "s_mov_b32 s64, 3\n"
      "s_waitcnt lgkmcnt(0)\n"
      "v_and_b32 v116, 0x40004000, v116\n"
      "v_mfma_f32_16x16x32_bf16 a[176:179], a[124:127], v[208:211], a[176:179]\n"
      "v_cmp_ne_u32 vcc, 0, v116\n"
      "s_cbranch_vccnz FB0_%=\n"
      "RET0_3_%=:\n"
      "v_mfma_f32_16x16x32_bf16 a[180:183], a[124:127], v[212:215], a[180:183]\n"
      "v_exp_f32 v112, v160\n"
      "v_mfma_f32_16x16x32_bf16 a[164:167], a[120:123], v[212:215], a[164:167]\n"
      "v_xor_b32 v242, 0x10000, v242\n"
      "v_mfma_f32_16x16x32_bf16 a[192:195], a[196:199], v[192:195], a[192:195]\n"
      "s_add_u32 m0, s62, 0\n"
      "v_exp_f32 v113, v161\n"
      "v_mfma_f32_16x16x32_bf16 v[84:87], a[116:119], v[212:215], v[84:87]\n"
      "buffer_load_dwordx4 v248, %[srdk], s60 offen nt lds\n"
      "v_cvt_pk_bf16_f32 v208, v112, v113\n"
      "v_xor_b32 v243, 0x10000, v243\n"
      "v_mfma_f32_16x16x32_bf16 v[68:71], a[112:115], v[212:215], v[68:71]\n"
      "s_add_u32 m0, s62, 896\n"
      "v_exp_f32 v114, v162\n"
      "v_mfma_f32_16x16x32_bf16 a[192:195], a[200:203], v[196:199], a[192:195]\n"
      "buffer_load_dwordx4 v248, %[srdk], s60 offen offset:128 nt lds\n"
      "v_xor_b32 v244, 0x10000, v244\n"
      "v_mfma_f32_16x16x32_bf16 a[148:151], a[108:111], v[212:215], a[148:151]\n"
      "s_add_u32 m0, s62, 8192\n"
      "v_exp_f32 v115, v163\n"
      "v_mfma_f32_16x16x32_bf16 a[132:135], a[104:107], v[212:215], a[132:135]\n"
      "s_memrealtime s[68:69]\n"
      "buffer_load_dwordx4 v120, %[srdk], s60 offen nt lds\n"
      "v_cvt_pk_bf16_f32 v209, v114, v115\n"
      "v_xor_b32 v245, 0x10000, v245\n"
      "v_mfma_f32_16x16x32_bf16 a[192:195], a[204:207], v[200:203], a[192:195]\n"
      "s_add_u32 m0, s62, 9088\n"
      "v_exp_f32 v112, v164\n"
      "v_mfma_f32_16x16x32_bf16 v[52:55], a[100:103], v[212:215], v[52:55]\n"
      "s_add_u32 s62, s62, 0x8000\n"
      "buffer_load_dwordx4 v120, %[srdk], s60 offen offset:128 nt lds\n"
      "v_or_b32 v116, v208, v209\n"
      "v_xor_b32 v246, 0x10000, v246\n"
      "v_mfma_f32_16x16x32_bf16 v[36:39], a[96:99], v[212:215], v[36:39]\n"
      "s_and_b32 s62, s62, 0x1ffff\n"
      "s_add_u32 s60, s60, s78\n"
      "v_exp_f32 v113, v165\n"
      "v_mfma_f32_16x16x32_bf16 a[192:195], a[208:211], v[204:207], a[192:195]\n"
      "s_sub_u32 s63, s63, 1\n"
      "v_cvt_pk_bf16_f32 v210, v112, v113\n"
      "v_xor_b32 v247, 0x10000, v247\n"
      "s_cbranch_scc0 LOOP2_%=\n"
      "s_waitcnt vmcnt(9) lgkmcnt(0)\n"
      "s_sub_u32 s67, s68, s90\n"
      "s_cmp_lt_i32 s67, 0\n"
      "s_cbranch_scc1 GW6_%=\n"
      "GX6_%=:\n"
      "s_barrier\n"
      "v_mfma_f32_16x16x32_bf16 v[40:43], a[96:99], v[216:219], v[40:43]\n"
      "s_memrealtime s[68:69]\n"
      "ds_read_b128 a[64:67], v242 offset:0\n"
      "v_exp_f32 v114, v166\n"
      "v_mfma_f32_16x16x32_bf16 v[44:47], a[96:99], v[220:223], v[44:47]\n"
      "s_add_u32 m0, %[wb], 50176\n"
      "ds_read_b128 a[68:71], v242 offset:4096\n"
      "ds_read_b128 a[72:75], v243 offset:0\n"
      "v_mfma_f32_16x16x32_bf16 v[60:63], a[100:103], v[220:223], v[60:63]\n"
      "s_add_u32 s94, s61, s78\n"
      "buffer_load_dwordx4 v121, %[srdv], s61 offen nt lds\n"
      "v_exp_f32 v115, v167\n"
      "v_mfma_f32_16x16x32_bf16 v[56:59], a[100:103], v[216:219], v[56:59]\n"
      "s_add_u32 m0, %[wb], 58368\n"
      "ds_read_b128 a[76:79], v243 offset:4096\n"
      "v_cvt_pk_bf16_f32 v211, v114, v115\n"
      "v_mfma_f32_16x16x32_bf16 a[136:139], a[104:107], v[216:219], a[136:139]\n"
      "s_add_u32 s95, s61, 0x10000\n"
      "buffer_load_dwordx4 v123, %[srdv], s61 offen nt lds\n"
      "v_exp_f32 v112, v168\n"
      "v_mfma_f32_16x16x32_bf16 a[140:143], a[104:107], v[220:223], a[140:143]\n"
      "s_add_u32 s77, s94, 0x10000\n"
      "ds_read_b128 a[80:83], v242 offset:1024\n"
      "v_or3_b32 v116, v116, v210, v211\n"
      "v_mfma_f32_16x16x32_bf16 a[156:159], a[108:111], v[220:223], a[156:159]\n"
      "s_add_u32 s78, s61, 0x20000\n"
      "v_exp_f32 v113, v169\n"
      "v_mfma_f32_16x16x32_bf16 a[152:155], a[108:111], v[216:219], a[152:155]\n"
      "ds_read_b128 a[84:87], v242 offset:5120\n"
      "v_cvt_pk_bf16_f32 v212, v112, v113\n"
      "v_mfma_f32_16x16x32_bf16 v[72:75], a[112:115], v[216:219], v[72:75]\n"
      "v_exp_f32 v114, v170\n"
      "v_mfma_f32_16x16x32_bf16 v[76:79], a[112:115], v[220:223], v[76:79]\n"
      "ds_read_b128 a[88:91], v243 offset:1024\n"
      "ds_read_b128 a[92:95], v243 offset:5120\n"
      "v_mfma_f32_16x16x32_bf16 v[92:95], a[116:119], v[220:223], v[92:95]\n"
      "v_exp_f32 v115, v171\n"
      "v_mfma_f32_16x16x32_bf16 v[88:91], a[116:119], v[216:219], v[88:91]\n"
      "v_cvt_pk_bf16_f32 v213, v114, v115\n"
      "v_mfma_f32_16x16x32_bf16 a[168:171], a[120:123], v[216:219], a[168:171]\n"
      "v_exp_f32 v112, v172\n"
      "v_mfma_f32_16x16x32_bf16 a[172:175], a[120:123], v[220:223], a[172:175]\n"
      "v_or3_b32 v116, v116, v212, v213\n"
      "v_mfma_f32_16x16x32_bf16 a[188:191], a[124:127], v[220:223], a[188:191]\n"
      "v_exp_f32 v113, v173\n"
      "v_mfma_f32_16x16x32_bf16 a[184:187], a[124:127], v[216:219], a[184:187]\n"
      "s_waitcnt lgkmcnt(0)\n"
      "v_cvt_pk_bf16_f32 v214, v112, v113\n"
      "v_mfma_f32_16x16x32_bf16 v[128:131], a[64:67], a[0:3], v[224:227]\n"
      "s_sub_u32 s67, s68, s66\n"
      "v_exp_f32 v114, v174\n"
      "v_mfma_f32_16x16x32_bf16 v[128:131], a[72:75], a[16:19], v[128:131]\n"
      "s_cmp_gt_i32 s67, s70\n"
      "v_mfma_f32_16x16x32_bf16 v[128:131], a[80:83], a[32:35], v[128:131]\n"
      "s_cselect_b32 s69, s86, 0\n"
      "v_exp_f32 v115, v175\n"
      "v_mfma_f32_16x16x32_bf16 v[128:131], a[88:91], a[48:51], v[128:131]\n"
      "s_add_u32 s66, s66, s69\n"
      "v_cvt_pk_bf16_f32 v215, v114, v115\n"
      "v_mfma_f32_16x16x32_bf16 v[132:135], a[92:95], a[48:51], v[224:227]\n"
      "s_add_u32 s67, s67, s101\n"
      "v_exp_f32 v112, v176\n"
      "v_mfma_f32_16x16x32_bf16 v[132:135], a[84:87], a[32:35], v[132:135]\n"
      "s_min_i32 s70, s70, s67\n"
      "v_or3_b32 v116, v116, v214, v215\n"
      "v_mfma_f32_16x16x32_bf16 v[132:135], a[76:79], a[16:19], v[132:135]\n"
      "s_max_i32 s70, s70, 85\n"
      "v_exp_f32 v113, v177\n"
      "v_mfma_f32_16x16x32_bf16 v[132:135], a[68:71], a[0:3], v[132:135]\n"
      "s_add_u32 s66, s66, s86\n"
      "v_cvt_pk_bf16_f32 v216, v112, v113\n"
      "v_mfma_f32_16x16x32_bf16 v[140:143], a[68:71], a[4:7], v[228:231]\n"
      "s_and_b32 s76, s80, 31\n"
      "v_exp_f32 v114, v178\n"
      "v_mfma_f32_16x16x32_bf16 v[140:143], a[76:79], a[20:23], v[140:143]\n"
      "s_lshr_b64 s[80:81], s[80:81], 5\n"
      "v_mfma_f32_16x16x32_bf16 v[140:143], a[84:87], a[36:39], v[140:143]\n"
      "s_add_u32 s66, s66, s76\n"
      "v_exp_f32 v115, v179\n"
      "v_mfma_f32_16x16x32_bf16 v[140:143], a[92:95], a[52:55], v[140:143]\n"
      "s_sub_u32 s67, s68, s82\n"
      "v_cvt_pk_bf16_f32 v217, v114, v115\n"
      "v_mfma_f32_16x16x32_bf16 v[136:139], a[88:91], a[52:55], v[228:231]\n"
      "s_min_i32 s76, s67, s83\n"
      "v_exp_f32 v112, v180\n"
      "s_waitcnt vmcnt(6)\n"
      "s_barrier\n"
      "v_mfma_f32_16x16x32_bf16 v[136:139], a[80:83], a[36:39], v[136:139]\n"
      "s_mov_b32 s83, s67\n"
      "ds_read_b64_tr_b16 a[96:97], v244 offset:0\n"
      "v_or3_b32 v116, v116, v216, v217\n"
      "v_mfma_f32_16x16x32_bf16 v[136:139], a[72:75], a[20:23], v[136:139]\n"
      "s_add_u32 m0, s62, 0\n"
      "v_exp_f32 v113, v181\n"
      "v_mfma_f32_16x16x32_bf16 v[136:139], a[64:67], a[4:7], v[136:139]\n"
      "s_sub_u32 s76, s76, 36\n"
      "ds_read_b64_tr_b16 a[98:99], v244 offset:4096\n"
      "buffer_load_dwordx4 v248, %[srdk], s60 offen nt lds\n"
      "v_cvt_pk_bf16_f32 v218, v112, v113\n"
      "v_mfma_f32_16x16x32_bf16 v[144:147], a[64:67], a[8:11], v[232:235]\n"
      "s_add_u32 m0, s62, 896\n"
      "v_exp_f32 v114, v182\n"
      "v_mfma_f32_16x16x32_bf16 v[144:147], a[72:75], a[24:27], v[144:147]\n"
      "s_min_i32 s76, s76, 148\n"
      "ds_read_b64_tr_b16 a[100:101], v245 offset:0\n"
      "ds_read_b64_tr_b16 a[102:103], v245 offset:4096\n"
      "buffer_load_dwordx4 v248, %[srdk], s60 offen offset:128 nt lds\n"
      "v_mfma_f32_16x16x32_bf16 v[144:147], a[80:83], a[40:43], v[144:147]\n"
      "s_add_u32 m0, s62, 8192\n"
      "v_exp_f32 v115, v183\n"
      "v_mfma_f32_16x16x32_bf16 v[144:147], a[88:91], a[56:59], v[144:147]\n"
      "s_max_i32 s76, s76, 70\n"
      "ds_read_b64_tr_b16 a[104:105], v246 offset:0\n"
      "buffer_load_dwordx4 v120, %[srdk], s60 offen nt lds\n"
      "v_cvt_pk_bf16_f32 v219, v114, v115\n"
      "v_mfma_f32_16x16x32_bf16 v[148:151], a[92:95], a[56:59], v[232:235]\n"
      "s_add_u32 m0, s62, 9088\n"
      "v_exp_f32 v112, v184\n"
      "v_mfma_f32_16x16x32_bf16 v[148:151], a[84:87], a[40:43], v[148:151]\n"
      "s_add_u32 s62, s62, 0x8000\n"
      "ds_read_b64_tr_b16 a[106:107], v246 offset:4096\n"
      "buffer_load_dwordx4 v120, %[srdk], s60 offen offset:128 nt lds\n"
      "v_or3_b32 v116, v116, v218, v219\n"
      "v_mfma_f32_16x16x32_bf16 v[148:151], a[76:79], a[24:27], v[148:151]\n"
      "s_and_b32 s62, s62, 0x1ffff\n"
      "v_exp_f32 v113, v185\n"
      "v_mfma_f32_16x16x32_bf16 v[148:151], a[68:71], a[8:11], v[148:151]\n"
      "s_add_u32 s60, s60, %[inc]\n"
      "ds_read_b64_tr_b16 a[108:109], v247 offset:0\n"
      "v_cvt_pk_bf16_f32 v220, v112, v113\n"
      "v_mfma_f32_16x16x32_bf16 v[156:159], a[68:71], a[12:15], v[236:239]\n"
      "s_mov_b32 s82, s68\n"
      "v_exp_f32 v114, v186\n"
      "v_mfma_f32_16x16x32_bf16 v[156:159], a[76:79], a[28:31], v[156:159]\n"
      "s_sub_u32 s66, s66, s99\n"
      "ds_read_b64_tr_b16 a[110:111], v247 offset:4096\n"
      "ds_read_b64_tr_b16 a[112:113], v244 offset:1024\n"
      "v_mfma_f32_16x16x32_bf16 v[156:159], a[84:87], a[44:47], v[156:159]\n"
      "s_sub_u32 s82, s82, s99\n"
      "v_exp_f32 v115, v187\n"
      "v_mfma_f32_16x16x32_bf16 v[156:159], a[92:95], a[60:63], v[156:159]\n"
      "s_add_u32 s65, s82, s76\n"
      "ds_read_b64_tr_b16 a[114:115], v244 offset:5120\n"
      "v_cvt_pk_bf16_f32 v221, v114, v115\n"
      "v_mfma_f32_16x16x32_bf16 v[152:155], a[88:91], a[60:63], v[236:239]\n"
      "s_sub_u32 s93, s93, 1\n"
      "v_exp_f32 v112, v188\n"
      "v_mfma_f32_16x16x32_bf16 v[152:155], a[80:83], a[44:47], v[152:155]\n"
      "s_cmp_le_i32 s93, 14\n"
      "ds_read_b64_tr_b16 a[116:117], v245 offset:1024\n"
      "v_or3_b32 v116, v116, v220, v221\n"
      "v_mfma_f32_16x16x32_bf16 v[152:155], a[72:75], a[28:31], v[152:155]\n"
      "s_cselect_b32 s86, 107, s86\n"
      "v_exp_f32 v113, v189\n"
      "v_mfma_f32_16x16x32_bf16 v[152:155], a[64:67], a[12:15], v[152:155]\n"
      "ds_read_b64_tr_b16 a[118:119], v245 offset:5120\n"
      "v_cvt_pk_bf16_f32 v222, v112, v113\n"
      "s_waitcnt lgkmcnt(6)\n"
      "v_mfma_f32_16x16x32_bf16 v[32:35], a[96:99], v[192:195], v[32:35]\n"
      "s_sub_u32 s67, s65, s66\n"
      "v_exp_f32 v114, v190\n"
      "v_mfma_f32_16x16x32_bf16 v[48:51], a[100:103], v[192:195], v[48:51]\n"
      "s_cmp_lt_i32 s67, 0\n"
      "s_cselect_b32 s88, s66, s65\n"
      "ds_read_b64_tr_b16 a[120:121], v246 offset:1024\n"
      "ds_read_b64_tr_b16 a[122:123], v246 offset:5120\n"
      "v_mfma_f32_16x16x32_bf16 a[128:131], a[104:107], v[192:195], a[128:131]\n"
      "s_waitcnt lgkmcnt(4)\n"
      "v_exp_f32 v115, v191\n"
      "v_mfma_f32_16x16x32_bf16 a[144:147], a[108:111], v[192:195], a[144:147]\n"
      "s_add_u32 s90, s88, s91\n"
      "ds_read_b64_tr_b16 a[124:125], v247 offset:1024\n"
      "v_cvt_pk_bf16_f32 v223, v114, v115\n"
      "v_mfma_f32_16x16x32_bf16 v[64:67], a[112:115], v[192:195], v[64:67]\n"
      "ds_read_b64_tr_b16 a[126:127], v247 offset:5120\n"
      "s_waitcnt lgkmcnt(2)\n"
      "v_mfma_f32_16x16x32_bf16 v[80:83], a[116:119], v[192:195], v[80:83]\n"
      "v_or3_b32 v116, v116, v222, v223\n"
      "v_mfma_f32_16x16x32_bf16 a[160:163], a[120:123], v[192:195], a[160:163]\n"
      "s_mov_b32 s64, 4\n"
      "s_waitcnt lgkmcnt(0)\n"
      "v_and_b32 v116, 0x40004000, v116\n"
      "v_mfma_f32_16x16x32_bf16 a[176:179], a[124:127], v[192:195], a[176:179]\n"
      "v_cmp_ne_u32 vcc, 0, v116\n"
      "s_cbranch_vccnz FB1_%=\n"
      "RET1_4_%=:\n"
      "v_mfma_f32_16x16x32_bf16 a[180:183], a[124:127], v[196:199], a[180:183]\n"
      "v_exp_f32 v112, v128\n"
      "v_mfma_f32_16x16x32_bf16 a[164:167], a[120:123], v[196:199], a[164:167]\n"
      "v_xor_b32 v240, 0x10000, v244\n"
      "v_mfma_f32_16x16x32_bf16 a[192:195], a[196:199], v[208:211], a[192:195]\n"
      "v_exp_f32 v113, v129\n"
      "v_mfma_f32_16x16x32_bf16 v[84:87], a[116:119], v[196:199], v[84:87]\n"
      "v_cvt_pk_bf16_f32 v192, v112, v113\n"
      "v_xor_b32 v241, 0x10000, v245\n"
      "v_mfma_f32_16x16x32_bf16 v[68:71], a[112:115], v[196:199], v[68:71]\n"
      "v_exp_f32 v114, v130\n"
      "v_mfma_f32_16x16x32_bf16 a[192:195], a[200:203], v[212:215], a[192:195]\n"
      "v_xor_b32 v250, 0x10000, v246\n"
      "v_mfma_f32_16x16x32_bf16 a[148:151], a[108:111], v[196:199], a[148:151]\n"
      "v_exp_f32 v115, v131\n"
      "v_mfma_f32_16x16x32_bf16 a[132:135], a[104:107], v[196:199], a[132:135]\n"
      "s_memrealtime s[68:69]\n"
      "v_cvt_pk_bf16_f32 v193, v114, v115\n"
      "v_xor_b32 v251, 0x10000, v247\n"
      "v_mfma_f32_16x16x32_bf16 a[192:195], a[204:207], v[216:219], a[192:195]\n"
      "v_exp_f32 v112, v132\n"
      "v_mfma_f32_16x16x32_bf16 v[52:55], a[100:103], v[196:199], v[52:55]\n"
      "v_or_b32 v116, v192, v193\n"
      "v_mfma_f32_16x16x32_bf16 v[36:39], a[96:99], v[196:199], v[36:39]\n"
      "v_exp_f32 v113, v133\n"
      "v_mfma_f32_16x16x32_bf16 a[192:195], a[208:211], v[220:223], a[192:195]\n"
      "v_cvt_pk_bf16_f32 v194, v112, v113\n"
      "s_waitcnt vmcnt(6) lgkmcnt(0)\n"
      "s_sub_u32 s67, s68, s90\n"
      "s_cmp_lt_i32 s67, 0\n"
      "s_cbranch_scc1 GW7_%=\n"
      "GX7_%=:\n"
      "s_barrier\n"
      "v_mfma_f32_16x16x32_bf16 v[40:43], a[96:99], v[200:203], v[40:43]\n"
      "s_memrealtime s[68:69]\n"
      "ds_read_b128 a[64:67], v242 offset:32768\n"
      "v_exp_f32 v114, v134\n"
      "v_mfma_f32_16x16x32_bf16 v[44:47], a[96:99], v[204:207], v[44:47]\n"
      "s_add_u32 m0, %[wb], 82944\n"
      "ds_read_b128 a[68:71], v242 offset:36864\n"
      "v_mfma_f32_16x16x32_bf16 v[60:63], a[100:103], v[204:207], v[60:63]\n"
      "ds_read_b128 a[72:75], v243 offset:32768\n"
      "buffer_load_dwordx4 v121, %[srdv], s94 offen nt lds\n"
      "v_mfma_f32_16x16x32_bf16 v[56:59], a[100:103], v[200:203], v[56:59]\n"
      "s_add_u32 m0, %[wb], 91136\n"
      "ds_read_b128 a[76:79], v243 offset:36864\n"
      "v_mfma_f32_16x16x32_bf16 a[136:139], a[104:107], v[200:203], a[136:139]\n"
      "ds_read_b128 a[80:83], v242 offset:33792\n"
      "buffer_load_dwordx4 v123, %[srdv], s94 offen nt lds\n"
      "v_mfma_f32_16x16x32_bf16 a[140:143], a[104:107], v[204:207], a[140:143]\n"
      "ds_read_b128 a[84:87], v242 offset:37888\n"
      "v_mfma_f32_16x16x32_bf16 a[156:159], a[108:111], v[204:207], a[156:159]\n"
      "ds_read_b128 a[88:91], v243 offset:33792\n"
      "v_mfma_f32_16x16x32_bf16 a[152:155], a[108:111], v[200:203], a[152:155]\n"
      "ds_read_b128 a[92:95], v243 offset:37888\n"
      "v_mfma_f32_16x16x32_bf16 v[72:75], a[112:115], v[200:203], v[72:75]\n"
      "v_exp_f32 v115, v135\n"
      "v_mfma_f32_16x16x32_bf16 v[76:79], a[112:115], v[204:207], v[76:79]\n"
      "v_exp_f32 v112, v136\n"
      "v_mfma_f32_16x16x32_bf16 v[92:95], a[116:119], v[204:207], v[92:95]\n"
      "v_cvt_pk_bf16_f32 v195, v114, v115\n"
      "v_exp_f32 v113, v137\n"
      "v_mfma_f32_16x16x32_bf16 v[88:91], a[116:119], v[200:203], v[88:91]\n"
      "v_or3_b32 v116, v116, v194, v195\n"
      "v_mfma_f32_16x16x32_bf16 a[168:171], a[120:123], v[200:203], a[168:171]\n"
      "v_exp_f32 v114, v138\n"
      "v_mfma_f32_16x16x32_bf16 a[172:175], a[120:123], v[204:207], a[172:175]\n"
      "v_cvt_pk_bf16_f32 v196, v112, v113\n"
      "v_exp_f32 v115, v139\n"
      "v_mfma_f32_16x16x32_bf16 a[188:191], a[124:127], v[204:207], a[188:191]\n"
      "v_exp_f32 v112, v140\n"
      "v_mfma_f32_16x16x32_bf16 a[184:187], a[124:127], v[200:203], a[184:187]\n"
      "s_waitcnt lgkmcnt(0)\n"
      "v_cvt_pk_bf16_f32 v197, v114, v115\n"
      "v_mfma_f32_16x16x32_bf16 v[160:163], a[64:67], a[0:3], v[224:227]\n"
      "s_sub_u32 s67, s68, s66\n"
      "v_exp_f32 v113, v141\n"
      "v_mfma_f32_16x16x32_bf16 v[168:171], a[64:67], a[4:7], v[228:231]\n"
      "s_cmp_gt_i32 s67, s70\n"
      "v_or3_b32 v116, v116, v196, v197\n"
      "v_exp_f32 v114, v142\n"
      "v_mfma_f32_16x16x32_bf16 v[176:179], a[64:67], a[8:11], v[232:235]\n"
      "s_cselect_b32 s69, s86, 0\n"
      "v_cvt_pk_bf16_f32 v198, v112, v113\n"
      "v_mfma_f32_16x16x32_bf16 v[184:187], a[64:67], a[12:15], v[236:239]\n"
      "s_add_u32 s66, s66, s69\n"
      "v_exp_f32 v115, v143\n"
      "v_mfma_f32_16x16x32_bf16 v[164:167], a[68:71], a[0:3], v[224:227]\n"
      "s_add_u32 s67, s67, s101\n"
      "v_exp_f32 v112, v144\n"
      "v_mfma_f32_16x16x32_bf16 v[172:175], a[68:71], a[4:7], v[228:231]\n"
      "s_min_i32 s70, s70, s67\n"
      "v_cvt_pk_bf16_f32 v199, v114, v115\n"
      "v_exp_f32 v113, v145\n"
      "v_mfma_f32_16x16x32_bf16 v[180:183], a[68:71], a[8:11], v[232:235]\n"
      "s_max_i32 s70, s70, 85\n"
      "v_or3_b32 v116, v116, v198, v199\n"
      "v_mfma_f32_16x16x32_bf16 v[188:191], a[68:71], a[12:15], v[236:239]\n"
      "s_waitcnt lgkmcnt(4)\n"
      "v_exp_f32 v114, v146\n"
      "v_mfma_f32_16x16x32_bf16 v[160:163], a[72:75], a[16:19], v[160:163]\n"
      "s_add_u32 s66, s66, s86\n"
      "v_cvt_pk_bf16_f32 v200, v112, v113\n"
      "v_exp_f32 v115, v147\n"
      "v_mfma_f32_16x16x32_bf16 v[168:171], a[72:75], a[20:23], v[168:171]\n"
      "s_and_b32 s76, s80, 31\n"
      "v_exp_f32 v112, v148\n"
      "v_mfma_f32_16x16x32_bf16 v[176:179], a[72:75], a[24:27], v[176:179]\n"
      "s_lshr_b64 s[80:81], s[80:81], 5\n"
      "v_cvt_pk_bf16_f32 v201, v114, v115\n"
      "v_mfma_f32_16x16x32_bf16 v[184:187], a[72:75], a[28:31], v[184:187]\n"
      "s_add_u32 s66, s66, s76\n"
      "v_exp_f32 v113, v149\n"
      "v_mfma_f32_16x16x32_bf16 v[164:167], a[76:79], a[16:19], v[164:167]\n"
      "s_sub_u32 s67, s68, s82\n"
      "v_or3_b32 v116, v116, v200, v201\n"
      "v_exp_f32 v114, v150\n"
      "v_mfma_f32_16x16x32_bf16 v[172:175], a[76:79], a[20:23], v[172:175]\n"
      "s_min_i32 s76, s67, s83\n"
      "v_cvt_pk_bf16_f32 v202, v112, v113\n"
      "v_mfma_f32_16x16x32_bf16 v[180:183], a[76:79], a[24:27], v[180:183]\n"
      "s_add_u32 m0, s62, 0\n"
      "v_exp_f32 v115, v151\n"
      "v_mfma_f32_16x16x32_bf16 v[188:191], a[76:79], a[28:31], v[188:191]\n"
      "buffer_load_dwordx4 v248, %[srdk], s60 offen nt lds\n"
      "s_waitcnt lgkmcnt(2)\n"
      "v_mfma_f32_16x16x32_bf16 v[160:163], a[80:83], a[32:35], v[160:163]\n"
      "s_add_u32 m0, s62, 896\n"
      "v_exp_f32 v112, v152\n"
      "v_mfma_f32_16x16x32_bf16 v[168:171], a[80:83], a[36:39], v[168:171]\n"
      "s_mov_b32 s83, s67\n"
      "buffer_load_dwordx4 v248, %[srdk], s60 offen offset:128 nt lds\n"
      "v_mfma_f32_16x16x32_bf16 v[176:179], a[80:83], a[40:43], v[176:179]\n"
      "s_add_u32 m0, s62, 8192\n"
      "v_mfma_f32_16x16x32_bf16 v[184:187], a[80:83], a[44:47], v[184:187]\n"
      "s_sub_u32 s76, s76, 36\n"
      "buffer_load_dwordx4 v120, %[srdk], s60 offen nt lds\n"
      "s_waitcnt vmcnt(9)\n"
      "s_barrier\n"
      "v_mfma_f32_16x16x32_bf16 v[164:167], a[84:87], a[32:35], v[164:167]\n"
      "s_add_u32 m0, s62, 9088\n"
      "ds_read_b64_tr_b16 a[96:97], v246 offset:33792\n"
      "v_mfma_f32_16x16x32_bf16 v[172:175], a[84:87], a[36:39], v[172:175]\n"
      "s_min_i32 s76, s76, 148\n"
      "ds_read_b64_tr_b16 a[98:99], v246 offset:37888\n"
      "buffer_load_dwordx4 v120, %[srdk], s60 offen offset:128 nt lds\n"
      "v_mfma_f32_16x16x32_bf16 v[180:183], a[84:87], a[40:43], v[180:183]\n"
      "s_max_i32 s76, s76, 70\n"
      "ds_read_b64_tr_b16 a[100:101], v247 offset:33792\n"
      "v_mfma_f32_16x16x32_bf16 v[188:191], a[84:87], a[44:47], v[188:191]\n"
      "ds_read_b64_tr_b16 a[102:103], v247 offset:37888\n"
      "s_waitcnt lgkmcnt(4)\n"
      "v_mfma_f32_16x16x32_bf16 v[160:163], a[88:91], a[48:51], v[160:163]\n"
      "s_mov_b32 s82, s68\n"
      "ds_read_b64_tr_b16 a[104:105], v244 offset:33792\n"
      "v_cvt_pk_bf16_f32 v203, v114, v115\n"
      "v_mfma_f32_16x16x32_bf16 v[168:171], a[88:91], a[52:55], v[168:171]\n"
      "s_sub_u32 s66, s66, s99\n"
      "ds_read_b64_tr_b16 a[106:107], v244 offset:37888\n"
      "v_exp_f32 v113, v153\n"
      "v_mfma_f32_16x16x32_bf16 v[176:179], a[88:91], a[56:59], v[176:179]\n"
      "s_sub_u32 s82, s82, s99\n"
      "ds_read_b64_tr_b16 a[108:109], v245 offset:33792\n"
      "v_mfma_f32_16x16x32_bf16 v[184:187], a[88:91], a[60:63], v[184:187]\n"
      "s_add_u32 s65, s82, s76\n"
      "ds_read_b64_tr_b16 a[110:111], v245 offset:37888\n"
      "v_or3_b32 v116, v116, v202, v203\n"
      "v_mfma_f32_16x16x32_bf16 v[164:167], a[92:95], a[48:51], v[164:167]\n"
      "s_sub_u32 s93, s93, 1\n"
      "v_exp_f32 v114, v154\n"
      "v_mfma_f32_16x16x32_bf16 v[172:175], a[92:95], a[52:55], v[172:175]\n"
      "s_cmp_le_i32 s93, 14\n"
      "v_cvt_pk_bf16_f32 v204, v112, v113\n"
      "v_exp_f32 v115, v155\n"
      "v_mfma_f32_16x16x32_bf16 v[180:183], a[92:95], a[56:59], v[180:183]\n"
      "s_cselect_b32 s86, 107, s86\n"
      "v_exp_f32 v112, v156\n"
      "v_mfma_f32_16x16x32_bf16 v[188:191], a[92:95], a[60:63], v[188:191]\n"
      "s_waitcnt lgkmcnt(4)\n"
      "v_cvt_pk_bf16_f32 v205, v114, v115\n"
      "v_mfma_f32_16x16x32_bf16 a[160:163], a[96:99], v[208:211], a[160:163]\n"
      "s_sub_u32 s67, s65, s66\n"
      "v_exp_f32 v113, v157\n"
      "v_mfma_f32_16x16x32_bf16 a[164:167], a[96:99], v[212:215], a[164:167]\n"
      "s_cmp_lt_i32 s67, 0\n"
      "v_or3_b32 v116, v116, v204, v205\n"
      "v_exp_f32 v114, v158\n"
      "v_mfma_f32_16x16x32_bf16 a[168:171], a[96:99], v[216:219], a[168:171]\n"
      "s_cselect_b32 s88, s66, s65\n"
      "v_cvt_pk_bf16_f32 v206, v112, v113\n"
      "v_mfma_f32_16x16x32_bf16 a[172:175], a[96:99], v[220:223], a[172:175]\n"
      "s_add_u32 s90, s88, s91\n"
      "v_exp_f32 v115, v159\n"
      "s_nop 0\n"
      "v_mfma_f32_16x16x32_bf16 a[176:179], a[100:103], v[208:211], a[176:179]\n"
      "s_mov_b32 s64, 4\n"
      "v_cvt_pk_bf16_f32 v207, v114, v115\n"
      "v_or3_b32 v116, v116, v206, v207\n"
      "v_mfma_f32_16x16x32_bf16 a[180:183], a[100:103], v[212:215], a[180:183]\n"
      "v_and_b32 v116, 0x40004000, v116\n"
      "v_cmp_ne_u32 vcc, 0, v116\n"
      "s_cbranch_vccnz FB0_%=\n"
      "RET0_4_%=:\n"
      "v_mfma_f32_16x16x32_bf16 a[184:187], a[100:103], v[216:219], a[184:187]\n"
      "v_exp_f32 v112, v160\n"
      "v_mfma_f32_16x16x32_bf16 a[188:191], a[100:103], v[220:223], a[188:191]\n"
      "s_memrealtime s[68:69]\n"
      "v_xor_b32 v242, 0x10000, v242\n"
      "v_mfma_f32_16x16x32_bf16 a[192:195], a[196:199], v[192:195], a[192:195]\n"
      "v_exp_f32 v113, v161\n"
      "v_mfma_f32_16x16x32_bf16 a[192:195], a[200:203], v[196:199], a[192:195]\n"
      "v_xor_b32 v243, 0x10000, v243\n"
      "v_exp_f32 v114, v162\n"
      "v_mfma_f32_16x16x32_bf16 a[192:195], a[204:207], v[200:203], a[192:195]\n"
      "v_cvt_pk_bf16_f32 v0, v112, v113\n"
      "v_mfma_f32_16x16x32_bf16 a[192:195], a[208:211], v[204:207], a[192:195]\n"
      "v_exp_f32 v115, v163\n"
      "s_waitcnt vmcnt(6) lgkmcnt(0)\n"
      "s_sub_u32 s67, s68, s90\n"
      "s_cmp_lt_i32 s67, 0\n"
      "s_cbranch_scc1 GW8_%=\n"
      "GX8_%=:\n"
      "s_barrier\n"
      "v_mfma_f32_16x16x32_bf16 v[64:67], a[104:107], v[208:211], v[64:67]\n"
      "ds_read_b128 a[64:67], v242 offset:0\n"
      "ds_read_b128 a[68:71], v242 offset:4096\n"
      "v_mfma_f32_16x16x32_bf16 v[68:71], a[104:107], v[212:215], v[68:71]\n"
      "s_add_u32 m0, %[wb], 115712\n"
      "ds_read_b128 a[72:75], v243 offset:0\n"
      "ds_read_b128 a[76:79], v243 offset:4096\n"
      "v_mfma_f32_16x16x32_bf16 v[72:75], a[104:107], v[216:219], v[72:75]\n"
      "ds_read_b128 a[80:83], v242 offset:1024\n"
      "ds_read_b128 a[84:87], v242 offset:5120\n"
      "buffer_load_dwordx4 v121, %[srdv], s95 offen nt lds\n"
      "v_mfma_f32_16x16x32_bf16 v[76:79], a[104:107], v[220:223], v[76:79]\n"
      "s_add_u32 m0, %[wb], 123904\n"
      "ds_read_b128 a[88:91], v243 offset:1024\n"
      "ds_read_b128 a[92:95], v243 offset:5120\n"
      "v_mfma_f32_16x16x32_bf16 v[80:83], a[108:111], v[208:211], v[80:83]\n"
      "buffer_load_dwordx4 v123, %[srdv], s95 offen nt lds\n"
      "v_mfma_f32_16x16x32_bf16 v[84:87], a[108:111], v[212:215], v[84:87]\n"
      "s_add_u32 m0, %[wb], 17408\n"
      "v_mfma_f32_16x16x32_bf16 v[88:91], a[108:111], v[216:219], v[88:91]\n"
      "buffer_load_dwordx4 v121, %[srdv], s77 offen nt lds\n"
      "v_mfma_f32_16x16x32_bf16 v[92:95], a[108:111], v[220:223], v[92:95]\n"
      "s_waitcnt lgkmcnt(6)\n"
      "s_add_u32 m0, %[wb], 25600\n"
      "v_exp_f32 v112, v164\n"
      "v_mfma_f32_16x16x32_bf16 v[128:131], a[64:67], a[0:3], v[224:227]\n"
      "buffer_load_dwordx4 v123, %[srdv], s77 offen nt lds\n"
      "v_mfma_f32_16x16x32_bf16 v[136:139], a[64:67], a[4:7], v[228:231]\n"
      "v_cvt_pk_bf16_f32 v1, v114, v115\n"
      "v_exp_f32 v113, v165\n"
      "v_mfma_f32_16x16x32_bf16 v[144:147], a[64:67], a[8:11], v[232:235]\n"
      "v_or_b32 v116, v0, v1\n"
      "v_mfma_f32_16x16x32_bf16 v[152:155], a[64:67], a[12:15], v[236:239]\n"
      "v_exp_f32 v114, v166\n"
      "v_cvt_pk_bf16_f32 v2, v112, v113\n"
      "v_mfma_f32_16x16x32_bf16 v[132:135], a[68:71], a[0:3], v[224:227]\n"
      "v_exp_f32 v115, v167\n"
      "v_mfma_f32_16x16x32_bf16 v[140:143], a[68:71], a[4:7], v[228:231]\n"
      "v_exp_f32 v112, v168\n"
      "v_cvt_pk_bf16_f32 v3, v114, v115\n"
      "v_mfma_f32_16x16x32_bf16 v[148:151], a[68:71], a[8:11], v[232:235]\n"
      "v_exp_f32 v113, v169\n"
      "v_or3_b32 v116, v116, v2, v3\n"
      "v_mfma_f32_16x16x32_bf16 v[156:159], a[68:71], a[12:15], v[236:239]\n"
      "s_waitcnt lgkmcnt(4)\n"
      "v_exp_f32 v114, v170\n"
      "v_mfma_f32_16x16x32_bf16 v[128:131], a[72:75], a[16:19], v[128:131]\n"
      "v_cvt_pk_bf16_f32 v4, v112, v113\n"
      "v_exp_f32 v115, v171\n"
      "v_mfma_f32_16x16x32_bf16 v[136:139], a[72:75], a[20:23], v[136:139]\n"
      "v_exp_f32 v112, v172\n"
      "v_mfma_f32_16x16x32_bf16 v[144:147], a[72:75], a[24:27], v[144:147]\n"
      "v_cvt_pk_bf16_f32 v5, v114, v115\n"
      "v_exp_f32 v113, v173\n"
      "v_mfma_f32_16x16x32_bf16 v[152:155], a[72:75], a[28:31], v[152:155]\n"
      "v_or3_b32 v116, v116, v4, v5\n"
      "v_mfma_f32_16x16x32_bf16 v[132:135], a[76:79], a[16:19], v[132:135]\n"
      "v_exp_f32 v114, v174\n"
      "v_cvt_pk_bf16_f32 v6, v112, v113\n"
      "v_mfma_f32_16x16x32_bf16 v[140:143], a[76:79], a[20:23], v[140:143]\n"
      "v_exp_f32 v115, v175\n"
      "v_mfma_f32_16x16x32_bf16 v[148:151], a[76:79], a[24:27], v[148:151]\n"
      "v_exp_f32 v112, v176\n"
      "v_cvt_pk_bf16_f32 v7, v114, v115\n"
      "v_mfma_f32_16x16x32_bf16 v[156:159], a[76:79], a[28:31], v[156:159]\n"
      "s_waitcnt lgkmcnt(2)\n"
      "v_exp_f32 v113, v177\n"
      "v_mfma_f32_16x16x32_bf16 v[128:131], a[80:83], a[32:35], v[128:131]\n"
      "v_or3_b32 v116, v116, v6, v7\n"
      "v_exp_f32 v114, v178\n"
      "v_mfma_f32_16x16x32_bf16 v[136:139], a[80:83], a[36:39], v[136:139]\n"
      "v_cvt_pk_bf16_f32 v8, v112, v113\n"
      "v_exp_f32 v115, v179\n"
      "v_mfma_f32_16x16x32_bf16 v[144:147], a[80:83], a[40:43], v[144:147]\n"
      "v_exp_f32 v112, v180\n"
      "v_mfma_f32_16x16x32_bf16 v[152:155], a[80:83], a[44:47], v[152:155]\n"
      "v_cvt_pk_bf16_f32 v9, v114, v115\n"
      "v_exp_f32 v113, v181\n"
      "s_waitcnt vmcnt(8)\n"
      "s_barrier\n"
      "v_mfma_f32_16x16x32_bf16 v[132:135], a[84:87], a[32:35], v[132:135]\n"
      "ds_read_b64_tr_b16 a[112:113], v250 offset:1024\n"
      "v_or3_b32 v116, v116, v8, v9\n"
      "v_mfma_f32_16x16x32_bf16 v[140:143], a[84:87], a[36:39], v[140:143]\n"
      "ds_read_b64_tr_b16 a[114:115], v250 offset:5120\n"
      "v_exp_f32 v114, v182\n"
      "v_mfma_f32_16x16x32_bf16 v[148:151], a[84:87], a[40:43], v[148:151]\n"
      "ds_read_b64_tr_b16 a[116:117], v251 offset:1024\n"
      "v_mfma_f32_16x16x32_bf16 v[156:159], a[84:87], a[44:47], v[156:159]\n"
      "ds_read_b64_tr_b16 a[118:119], v251 offset:5120\n"
      "v_cvt_pk_bf16_f32 v10, v112, v113\n"
      "s_waitcnt lgkmcnt(4)\n"
      "v_mfma_f32_16x16x32_bf16 v[128:131], a[88:91], a[48:51], v[128:131]\n"
      "ds_read_b64_tr_b16 a[120:121], v240 offset:1024\n"
      "v_exp_f32 v115, v183\n"
      "v_mfma_f32_16x16x32_bf16 v[136:139], a[88:91], a[52:55], v[136:139]\n"
      "ds_read_b64_tr_b16 a[122:123], v240 offset:5120\n"
      "v_exp_f32 v112, v184\n"
      "v_mfma_f32_16x16x32_bf16 v[144:147], a[88:91], a[56:59], v[144:147]\n"
      "ds_read_b64_tr_b16 a[124:125], v241 offset:1024\n"
      "v_cvt_pk_bf16_f32 v11, v114, v115\n"
      "v_mfma_f32_16x16x32_bf16 v[152:155], a[88:91], a[60:63], v[152:155]\n"
      "ds_read_b64_tr_b16 a[126:127], v241 offset:5120\n"
      "v_exp_f32 v113, v185\n"
      "v_mfma_f32_16x16x32_bf16 v[132:135], a[92:95], a[48:51], v[132:135]\n"
      "v_or3_b32 v116, v116, v10, v11\n"
      "v_exp_f32 v114, v186\n"
      "v_mfma_f32_16x16x32_bf16 v[140:143], a[92:95], a[52:55], v[140:143]\n"
      "v_cvt_pk_bf16_f32 v12, v112, v113\n"
      "v_mfma_f32_16x16x32_bf16 v[148:151], a[92:95], a[56:59], v[148:151]\n"
      "v_exp_f32 v115, v187\n"
      "v_mfma_f32_16x16x32_bf16 v[156:159], a[92:95], a[60:63], v[156:159]\n"
      "s_waitcnt lgkmcnt(4)\n"
      "v_exp_f32 v112, v188\n"
      "v_cvt_pk_bf16_f32 v13, v114, v115\n"
      "v_mfma_f32_16x16x32_bf16 a[160:163], a[112:115], v[192:195], a[160:163]\n"
      "v_exp_f32 v113, v189\n"
      "v_or3_b32 v116, v116, v12, v13\n"
      "v_mfma_f32_16x16x32_bf16 a[164:167], a[112:115], v[196:199], a[164:167]\n"
      "v_exp_f32 v114, v190\n"
      "v_mfma_f32_16x16x32_bf16 a[168:171], a[112:115], v[200:203], a[168:171]\n"
      "v_cvt_pk_bf16_f32 v14, v112, v113\n"
      "v_exp_f32 v115, v191\n"
      "v_mfma_f32_16x16x32_bf16 a[172:175], a[112:115], v[204:207], a[172:175]\n"
      "s_add_u32 m0, %[wb], 50176\n"
      "s_nop 0\n"
      "v_cvt_pk_bf16_f32 v15, v114, v115\n"
      "v_or3_b32 v116, v116, v14, v15\n"
      "v_mfma_f32_16x16x32_bf16 a[176:179], a[116:119], v[192:195], a[176:179]\n"
      "s_mov_b32 s64, 5\n"
      "buffer_load_dwordx4 v121, %[srdv], s78 offen nt lds\n"
      "v_mfma_f32_16x16x32_bf16 a[180:183], a[116:119], v[196:199], a[180:183]\n"
      "s_add_u32 m0, %[wb], 58368\n"
      "v_and_b32 v116, 0x40004000, v116\n"
      "v_cmp_ne_u32 vcc, 0, v116\n"
      "s_cbranch_vccnz FB1_%=\n"
      "RET1_5_%=:\n"
      "v_mfma_f32_16x16x32_bf16 a[184:187], a[116:119], v[200:203], a[184:187]\n"
      "buffer_load_dwordx4 v123, %[srdv], s78 offen nt lds\n"
      "v_mfma_f32_16x16x32_bf16 a[188:191], a[116:119], v[204:207], a[188:191]\n"
      "s_memrealtime s[68:69]\n"
      "v_exp_f32 v112, v128\n"
      "v_exp_f32 v113, v129\n"
      "v_mfma_f32_16x16x32_bf16 a[192:195], a[196:199], v[0:3], a[192:195]\n"
      "v_exp_f32 v114, v130\n"
      "v_mfma_f32_16x16x32_bf16 a[192:195], a[200:203], v[4:7], a[192:195]\n"
      "v_cvt_pk_bf16_f32 v16, v112, v113\n"
      "v_exp_f32 v115, v131\n"
      "v_mfma_f32_16x16x32_bf16 a[192:195], a[204:207], v[8:11], a[192:195]\n"
      "v_exp_f32 v112, v132\n"
      "v_cvt_pk_bf16_f32 v17, v114, v115\n"
      "v_mfma_f32_16x16x32_bf16 a[192:195], a[208:211], v[12:15], a[192:195]\n"
      "v_exp_f32 v113, v133\n"
      "v_or_b32 v116, v16, v17\n"
      "s_add_u32 s72, s72, 0x7f4a7c15\n"
      "s_addc_u32 s76, s79, 0x9e3779b9\n"
      "s_waitcnt vmcnt(6) lgkmcnt(0)\n"
      "s_barrier\n"
      "v_mfma_f32_16x16x32_bf16 v[64:67], a[120:123], v[192:195], v[64:67]\n"
      "s_sub_u32 s80, 20480, %[xo]\n"
      "ds_read_b128 a[64:67], v242 offset:32768\n"
      "ds_read_b128 a[68:71], v242 offset:36864\n"
      "v_mfma_f32_16x16x32_bf16 v[68:71], a[120:123], v[196:199], v[68:71]\n"
      "s_add_u32 s79, s80, 0x1000\n"
      "ds_read_b128 a[72:75], v243 offset:32768\n"
      "ds_read_b128 a[76:79], v243 offset:36864\n"
      "v_exp_f32 v114, v134\n"
      "v_mfma_f32_16x16x32_bf16 v[72:75], a[120:123], v[200:203], v[72:75]\n"
      "s_add_u32 s81, %[xo], 0x1000\n"
      "ds_read_b128 a[80:83], v242 offset:33792\n"
      "ds_read_b128 a[84:87], v242 offset:37888\n"
      "v_cvt_pk_bf16_f32 v18, v112, v113\n"
      "v_mfma_f32_16x16x32_bf16 v[76:79], a[120:123], v[204:207], v[76:79]\n"
      "s_mov_b32 s63, 0x100000\n"
      "ds_read_b128 a[88:91], v243 offset:33792\n"
      "ds_read_b128 a[92:95], v243 offset:37888\n"
      "v_mfma_f32_16x16x32_bf16 v[80:83], a[124:127], v[192:195], v[80:83]\n"
      "s_mov_b32 s60, 0x928b2f\n"
      "v_exp_f32 v115, v135\n"
      "v_exp_f32 v112, v136\n"
      "v_mfma_f32_16x16x32_bf16 v[84:87], a[124:127], v[196:199], v[84:87]\n"
      "s_mov_b32 s62, 0x9818e9\n"
      "v_cvt_pk_bf16_f32 v19, v114, v115\n"
      "v_exp_f32 v113, v137\n"
      "v_mfma_f32_16x16x32_bf16 v[88:91], a[124:127], v[200:203], v[88:91]\n"
      "s_mov_b32 s70, 0xdd9dc9\n"
      "v_xor_b32 v248, 0x2000, v244\n"
      "v_or3_b32 v116, v116, v18, v19\n"
      "v_mfma_f32_16x16x32_bf16 v[92:95], a[124:127], v[204:207], v[92:95]\n"
      "s_waitcnt lgkmcnt(6)\n"
      "v_exp_f32 v114, v138\n"
      "v_cvt_pk_bf16_f32 v20, v112, v113\n"
      "v_mfma_f32_16x16x32_bf16 v[160:163], a[64:67], a[0:3], v[224:227]\n"
      "s_mov_b32 s71, 0x8ed905\n"
      "v_exp_f32 v115, v139\n"
      "v_mfma_f32_16x16x32_bf16 v[168:171], a[64:67], a[4:7], v[228:231]\n"
      "s_sub_u32 s88, 319, %[cph]\n"
      "v_xor_b32 v120, 0x2000, v245\n"
      "v_exp_f32 v112, v140\n"
      "v_cvt_pk_bf16_f32 v21, v114, v115\n"
      "v_mfma_f32_16x16x32_bf16 v[176:179], a[64:67], a[8:11], v[232:235]\n"
      "s_add_u32 s88, s88, s66\n"
      "v_exp_f32 v113, v141\n"
      "v_mfma_f32_16x16x32_bf16 v[184:187], a[64:67], a[12:15], v[236:239]\n"
      "s_add_u32 m0, %[wb], 49152\n"
      "v_or3_b32 v116, v116, v20, v21\n"
      "v_exp_f32 v114, v142\n"
      "v_mfma_f32_16x16x32_bf16 v[164:167], a[68:71], a[0:3], v[224:227]\n"
      "s_add_u32 s88, s88, s100\n"
      "buffer_load_dwordx4 v249, %[srdv], s61 offen nt lds\n"
      "v_mbcnt_lo_u32_b32 v127, -1, 0\n"
      "v_mfma_f32_16x16x32_bf16 v[172:175], a[68:71], a[4:7], v[228:231]\n"
      "s_add_u32 m0, %[wb], 57344\n"
      "v_cvt_pk_bf16_f32 v22, v112, v113\n"
      "v_mfma_f32_16x16x32_bf16 v[180:183], a[68:71], a[8:11], v[232:235]\n"
      "buffer_load_dwordx4 v122, %[srdv], s61 offen nt lds\n"
      "v_mfma_f32_16x16x32_bf16 v[188:191], a[68:71], a[12:15], v[236:239]\n"
      "s_waitcnt lgkmcnt(4)\n"
      "v_mbcnt_hi_u32_b32 v127, -1, v127\n"
      "v_exp_f32 v115, v143\n"
      "v_mfma_f32_16x16x32_bf16 v[160:163], a[72:75], a[16:19], v[160:163]\n"
      "v_exp_f32 v112, v144\n"
      "v_cvt_pk_bf16_f32 v23, v114, v115\n"
      "v_mfma_f32_16x16x32_bf16 v[168:171], a[72:75], a[20:23], v[168:171]\n"
      "v_exp_f32 v113, v145\n"
      "v_or3_b32 v116, v116, v22, v23\n"
      "v_mfma_f32_16x16x32_bf16 v[176:179], a[72:75], a[24:27], v[176:179]\n"
      "v_lshlrev_b32 v127, 4, v127\n"
      "v_exp_f32 v114, v146\n"
      "v_mfma_f32_16x16x32_bf16 v[184:187], a[72:75], a[28:31], v[184:187]\n"
      "v_cvt_pk_bf16_f32 v24, v112, v113\n"
      "v_exp_f32 v115, v147\n"
      "v_mfma_f32_16x16x32_bf16 v[164:167], a[76:79], a[16:19], v[164:167]\n"
      "v_exp_f32 v112, v148\n"
      "v_cvt_pk_bf16_f32 v25, v114, v115\n"
      "v_mfma_f32_16x16x32_bf16 v[172:175], a[76:79], a[20:23], v[172:175]\n"
      "v_add_u32 v125, %[xw], v127\n"
      "v_exp_f32 v113, v149\n"
      "v_mfma_f32_16x16x32_bf16 v[180:183], a[76:79], a[24:27], v[180:183]\n"
      "v_or3_b32 v116, v116, v24, v25\n"
      "v_exp_f32 v114, v150\n"
      "v_mfma_f32_16x16x32_bf16 v[188:191], a[76:79], a[28:31], v[188:191]\n"
      "s_waitcnt lgkmcnt(2)\n"
      "v_cvt_pk_bf16_f32 v26, v112, v113\n"
      "v_exp_f32 v115, v151\n"
      "v_mfma_f32_16x16x32_bf16 v[160:163], a[80:83], a[32:35], v[160:163]\n"
      "v_add_u32 v126, %[xr], v127\n"
      "v_exp_f32 v112, v152\n"
      "v_mfma_f32_16x16x32_bf16 v[168:171], a[80:83], a[36:39], v[168:171]\n"
      "s_add_u32 m0, %[wb], 81920\n"
      "v_cvt_pk_bf16_f32 v27, v114, v115\n"
      "v_exp_f32 v113, v153\n"
      "v_mfma_f32_16x16x32_bf16 v[176:179], a[80:83], a[40:43], v[176:179]\n"
      "buffer_load_dwordx4 v249, %[srdv], s94 offen nt lds\n"
      "v_mfma_f32_16x16x32_bf16 v[184:187], a[80:83], a[44:47], v[184:187]\n"
      "s_add_u32 m0, %[wb], 90112\n"
      "v_or3_b32 v116, v116, v26, v27\n"
      "v_mfma_f32_16x16x32_bf16 v[164:167], a[84:87], a[32:35], v[164:167]\n"
      "buffer_load_dwordx4 v122, %[srdv], s94 offen nt lds\n"
      "v_mfma_f32_16x16x32_bf16 v[172:175], a[84:87], a[36:39], v[172:175]\n"
      "v_exp_f32 v114, v154\n"
      "v_cvt_pk_bf16_f32 v28, v112, v113\n"
      "v_mfma_f32_16x16x32_bf16 v[180:183], a[84:87], a[40:43], v[180:183]\n"
      "v_exp_f32 v115, v155\n"
      "v_exp_f32 v112, v156\n"
      "v_mov_b32 v123, s88\n"
      "v_mfma_f32_16x16x32_bf16 v[188:191], a[84:87], a[44:47], v[188:191]\n"
      "s_waitcnt lgkmcnt(0)\n"
      "v_cvt_pk_bf16_f32 v29, v114, v115\n"
      "v_exp_f32 v113, v157\n"
      "s_waitcnt vmcnt(6)\n"
      "s_barrier\n"
      "v_mfma_f32_16x16x32_bf16 v[160:163], a[88:91], a[48:51], v[160:163]\n"
      "ds_read_b64_tr_b16 a[96:97], v250 offset:33792\n"
      "ds_read_b64_tr_b16 a[98:99], v250 offset:37888\n"
      "v_mfma_f32_16x16x32_bf16 v[168:171], a[88:91], a[52:55], v[168:171]\n"
      "ds_read_b64_tr_b16 a[100:101], v251 offset:33792\n"
      "ds_read_b64_tr_b16 a[102:103], v251 offset:37888\n"
      "v_or3_b32 v116, v116, v28, v29\n"
      "v_mfma_f32_16x16x32_bf16 v[176:179], a[88:91], a[56:59], v[176:179]\n"
      "ds_read_b64_tr_b16 a[112:113], v246 offset:1024\n"
      "ds_read_b64_tr_b16 a[114:115], v246 offset:5120\n"
      "v_mfma_f32_16x16x32_bf16 v[184:187], a[88:91], a[60:63], v[184:187]\n"
      "ds_read_b64_tr_b16 a[116:117], v247 offset:1024\n"
      "ds_read_b64_tr_b16 a[118:119], v247 offset:5120\n"
      "v_exp_f32 v114, v158\n"
      "v_mfma_f32_16x16x32_bf16 v[164:167], a[92:95], a[48:51], v[164:167]\n"
      "ds_read_b64_tr_b16 a[104:105], v240 offset:33792\n"
      "v_cvt_pk_bf16_f32 v30, v112, v113\n"
      "v_mfma_f32_16x16x32_bf16 v[172:175], a[92:95], a[52:55], v[172:175]\n"
      "s_mov_b32 s64, 5\n"
      "ds_read_b64_tr_b16 a[106:107], v240 offset:37888\n"
      "v_exp_f32 v115, v159\n"
      "s_nop 0\n"
      "v_mfma_f32_16x16x32_bf16 v[180:183], a[92:95], a[56:59], v[180:183]\n"
      "ds_read_b64_tr_b16 a[108:109], v241 offset:33792\n"
      "v_cvt_pk_bf16_f32 v31, v114, v115\n"
      "v_or3_b32 v116, v116, v30, v31\n"
      "v_mfma_f32_16x16x32_bf16 v[188:191], a[92:95], a[60:63], v[188:191]\n"
      "ds_read_b64_tr_b16 a[110:111], v241 offset:37888\n"
      "v_and_b32 v116, 0x40004000, v116\n"
      "v_cmp_ne_u32 vcc, 0, v116\n"
      "s_waitcnt lgkmcnt(4)\n"
      "s_cbranch_vccnz FB0_%=\n"
      "RET0_5_%=:\n"
      "v_mfma_f32_16x16x32_bf16 a[160:163], a[96:99], v[0:3], a[160:163]\n"
      "ds_read_b64_tr_b16 a[120:121], v244 offset:1024\n"
      "v_mov_b32 v117, v224\n"
      "v_xor_b32 v242, 0x10000, v242\n"
      "v_xor_b32 v243, 0x10000, v243\n"
      "v_mfma_f32_16x16x32_bf16 a[176:179], a[100:103], v[0:3], a[176:179]\n"
      "ds_read_b64_tr_b16 a[122:123], v244 offset:5120\n"
      "v_exp_f32 v112, v160\n"
      "v_exp_f32 v113, v161\n"
      "v_mfma_f32_16x16x32_bf16 a[160:163], a[112:115], v[16:19], a[160:163]\n"
      "s_mov_b32 s64, 6\n"
      "ds_read_b64_tr_b16 a[124:125], v245 offset:1024\n"
      "v_exp_f32 v114, v162\n"
      "v_mfma_f32_16x16x32_bf16 a[176:179], a[116:119], v[16:19], a[176:179]\n"
      "s_add_u32 m0, %[wb], 114688\n"
      "ds_read_b64_tr_b16 a[126:127], v245 offset:5120\n"
      "v_cvt_pk_bf16_f32 v96, v112, v113\n"
      "v_exp_f32 v115, v163\n"
      "v_mfma_f32_16x16x32_bf16 a[164:167], a[96:99], v[4:7], a[164:167]\n"
      "buffer_load_dwordx4 v249, %[srdv], s95 offen nt lds\n"
      "v_mov_b32_dpp v117, v228 quad_perm:[0,1,2,3] row_mask:0x2 bank_mask:0xf\n"
      "v_mfma_f32_16x16x32_bf16 a[180:183], a[100:103], v[4:7], a[180:183]\n"
      "s_add_u32 m0, %[wb], 122880\n"
      "v_exp_f32 v112, v164\n"
      "v_cvt_pk_bf16_f32 v97, v114, v115\n"
      "v_exp_f32 v113, v165\n"
      "v_mfma_f32_16x16x32_bf16 a[164:167], a[112:115], v[20:23], a[164:167]\n"
      "buffer_load_dwordx4 v122, %[srdv], s95 offen nt lds\n"
      "v_or_b32 v116, v96, v97\n"
      "v_mfma_f32_16x16x32_bf16 a[180:183], a[116:119], v[20:23], a[180:183]\n"
      "ds_write_b128 v125, a[160:163] offset:0\n"
      "v_mfma_f32_16x16x32_bf16 a[168:171], a[96:99], v[8:11], a[168:171]\n"
      "v_mov_b32_dpp v117, v232 quad_perm:[0,1,2,3] row_mask:0x4 bank_mask:0xf\n"
      "v_exp_f32 v114, v166\n"
      "v_cvt_pk_bf16_f32 v98, v112, v113\n"
      "v_mfma_f32_16x16x32_bf16 a[184:187], a[100:103], v[8:11], a[184:187]\n"
      "ds_write_b128 v125, a[176:179] offset:4096\n"
      "v_exp_f32 v115, v167\n"
      "v_mfma_f32_16x16x32_bf16 a[168:171], a[112:115], v[24:27], a[168:171]\n"
      "v_exp_f32 v112, v168\n"
      "v_cvt_pk_bf16_f32 v99, v114, v115\n"
      "v_exp_f32 v113, v169\n"
      "v_mfma_f32_16x16x32_bf16 a[184:187], a[116:119], v[24:27], a[184:187]\n"
      "v_or3_b32 v116, v116, v98, v99\n"
      "v_exp_f32 v114, v170\n"
      "v_mfma_f32_16x16x32_bf16 a[172:175], a[96:99], v[12:15], a[172:175]\n"
      "ds_write_b128 v125, v[96:99] offset:8192\n"
      "v_mov_b32_dpp v117, v236 quad_perm:[0,1,2,3] row_mask:0x8 bank_mask:0xf\n"
      "v_mfma_f32_16x16x32_bf16 a[188:191], a[100:103], v[12:15], a[188:191]\n"
      "v_cvt_pk_bf16_f32 v100, v112, v113\n"
      "v_exp_f32 v115, v171\n"
      "v_exp_f32 v112, v172\n"
      "v_mfma_f32_16x16x32_bf16 a[172:175], a[112:115], v[28:31], a[172:175]\n"
      "v_cvt_pk_bf16_f32 v101, v114, v115\n"
      "v_exp_f32 v113, v173\n"
      "v_mfma_f32_16x16x32_bf16 a[188:191], a[116:119], v[28:31], a[188:191]\n"
      "s_add_u32 m0, %[wb], 16384\n"
      "ds_write_b128 v125, a[164:167] offset:1024\n"
      "v_or3_b32 v116, v116, v100, v101\n"
      "v_mfma_f32_16x16x32_bf16 a[192:195], a[196:199], v[16:19], a[192:195]\n"
      "buffer_load_dwordx4 v249, %[srdv], s77 offen nt lds\n"
      "ds_write_b32 v125, v117 offset:12292\n"
      "v_mfma_f32_16x16x32_bf16 a[192:195], a[200:203], v[20:23], a[192:195]\n"
      "s_add_u32 m0, %[wb], 24576\n"
      "v_exp_f32 v114, v174\n"
      "v_cvt_pk_bf16_f32 v102, v112, v113\n"
      "v_exp_f32 v115, v175\n"
      "v_mfma_f32_16x16x32_bf16 a[192:195], a[204:207], v[24:27], a[192:195]\n"
      "buffer_load_dwordx4 v122, %[srdv], s77 offen nt lds\n"
      "ds_write_b128 v125, a[180:183] offset:5120\n"
      "ds_write_b32 v125, v123 offset:12296\n"
      "v_mfma_f32_16x16x32_bf16 a[192:195], a[208:211], v[28:31], a[192:195]\n"
      "s_waitcnt lgkmcnt(7)\n"
      "v_exp_f32 v112, v176\n"
      "v_mfma_f32_16x16x32_bf16 v[64:67], a[104:107], v[0:3], v[64:67]\n"
      "v_cvt_pk_bf16_f32 v103, v114, v115\n"
      "v_exp_f32 v113, v177\n"
      "v_or3_b32 v116, v116, v102, v103\n"
      "v_mfma_f32_16x16x32_bf16 v[68:71], a[104:107], v[4:7], v[68:71]\n"
      "ds_write_b128 v125, v[100:103] offset:9216\n"
      "v_exp_f32 v114, v178\n"
      "v_mfma_f32_16x16x32_bf16 v[72:75], a[104:107], v[8:11], v[72:75]\n"
      "v_cvt_pk_bf16_f32 v104, v112, v113\n"
      "v_exp_f32 v115, v179\n"
      "v_mfma_f32_16x16x32_bf16 v[76:79], a[104:107], v[12:15], v[76:79]\n"
      "ds_write_b128 v125, a[168:171] offset:2048\n"
      "v_exp_f32 v112, v180\n"
      "v_mfma_f32_16x16x32_bf16 a[228:231], a[196:199], v[96:99], a[192:195]\n"
      "v_cvt_pk_bf16_f32 v105, v114, v115\n"
      "v_exp_f32 v113, v181\n"
      "v_or3_b32 v116, v116, v104, v105\n"
      "v_mfma_f32_16x16x32_bf16 v[80:83], a[108:111], v[0:3], v[80:83]\n"
      "v_exp_f32 v114, v182\n"
      "v_cvt_pk_bf16_f32 v106, v112, v113\n"
      "s_waitcnt vmcnt(8)\n"
      "s_barrier\n"
      "v_mfma_f32_16x16x32_bf16 v[84:87], a[108:111], v[4:7], v[84:87]\n"
      "ds_write_b128 v125, a[184:187] offset:6144\n"
      "ds_read_b64_tr_b16 a[212:213], v244 offset:33792\n"
      "v_mfma_f32_16x16x32_bf16 v[88:91], a[108:111], v[8:11], v[88:91]\n"
      "ds_read_b64_tr_b16 a[214:215], v244 offset:37888\n"
      "v_exp_f32 v115, v183\n"
      "v_exp_f32 v112, v184\n"
      "v_mfma_f32_16x16x32_bf16 v[92:95], a[108:111], v[12:15], v[92:95]\n"
      "ds_read_b64_tr_b16 a[216:217], v245 offset:33792\n"
      "v_cvt_pk_bf16_f32 v107, v114, v115\n"
      "v_exp_f32 v113, v185\n"
      "v_mfma_f32_16x16x32_bf16 v[64:67], a[120:123], v[16:19], v[64:67]\n"
      "s_add_u32 m0, %[wb], 82944\n"
      "ds_write_b128 v125, v[104:107] offset:10240\n"
      "ds_read_b64_tr_b16 a[218:219], v245 offset:37888\n"
      "v_mfma_f32_16x16x32_bf16 v[68:71], a[120:123], v[20:23], v[68:71]\n"
      "buffer_load_dwordx4 v249, %[srdv], s78 offen nt lds\n"
      "ds_read_b64_tr_b16 a[220:221], v248 offset:33792\n"
      "v_mfma_f32_16x16x32_bf16 v[72:75], a[120:123], v[24:27], v[72:75]\n"
      "s_add_u32 m0, %[wb], 91136\n"
      "ds_read_b64_tr_b16 a[222:223], v248 offset:37888\n"
      "v_or3_b32 v116, v116, v106, v107\n"
      "v_exp_f32 v114, v186\n"
      "v_mfma_f32_16x16x32_bf16 v[76:79], a[120:123], v[28:31], v[76:79]\n"
      "buffer_load_dwordx4 v122, %[srdv], s78 offen nt lds\n"
      "ds_write_b128 v125, a[172:175] offset:3072\n"
      "ds_read_b64_tr_b16 a[224:225], v120 offset:33792\n"
      "v_mfma_f32_16x16x32_bf16 a[228:231], a[200:203], v[100:103], a[228:231]\n"
      "ds_read_b64_tr_b16 a[226:227], v120 offset:37888\n"
      "v_mfma_f32_16x16x32_bf16 v[80:83], a[124:127], v[16:19], v[80:83]\n"
      "v_cvt_pk_bf16_f32 v108, v112, v113\n"
      "v_exp_f32 v115, v187\n"
      "v_exp_f32 v112, v188\n"
      "v_mfma_f32_16x16x32_bf16 v[84:87], a[124:127], v[20:23], v[84:87]\n"
      "ds_write_b128 v125, a[188:191] offset:7168\n"
      "v_cvt_pk_bf16_f32 v109, v114, v115\n"
      "v_mfma_f32_16x16x32_bf16 v[88:91], a[124:127], v[24:27], v[88:91]\n"
      "v_exp_f32 v113, v189\n"
      "v_or3_b32 v116, v116, v108, v109\n"
      "v_mfma_f32_16x16x32_bf16 v[92:95], a[124:127], v[28:31], v[92:95]\n"
      "v_exp_f32 v114, v190\n"
      "v_cvt_pk_bf16_f32 v110, v112, v113\n"
      "v_exp_f32 v115, v191\n"
      "v_mfma_f32_16x16x32_bf16 a[228:231], a[204:207], v[104:107], a[228:231]\n"
      "s_nop 0\n"
      "v_cvt_pk_bf16_f32 v111, v114, v115\n"
      "v_or3_b32 v116, v116, v110, v111\n"
      "v_and_b32 v116, 0x40004000, v116\n"
      "v_cmp_ne_u32 vcc, 0, v116\n"
      "s_cbranch_vccnz FB1_%=\n"
      "RET1_6_%=:\n"
      "ds_write_b128 v125, v[108:111] offset:11264\n"
      "v_mfma_f32_16x16x32_bf16 a[228:231], a[208:211], v[108:111], a[228:231]\n"
      "s_waitcnt lgkmcnt(7)\n"
      "v_mfma_f32_16x16x32_bf16 v[64:67], a[212:215], v[96:99], v[64:67]\n"
      "v_mov_b32 v148, 1.0\n"
      "v_mfma_f32_16x16x32_bf16 v[80:83], a[216:219], v[96:99], v[80:83]\n"
      "v_mov_b32 v149, 1.0\n"
      "v_mfma_f32_16x16x32_bf16 v[68:71], a[212:215], v[100:103], v[68:71]\n"
      "v_mov_b32 v157, %[fo]\n"
      "v_mfma_f32_16x16x32_bf16 v[84:87], a[216:219], v[100:103], v[84:87]\n"
      "v_mov_b32 v150, s72\n"
      "v_mfma_f32_16x16x32_bf16 v[72:75], a[212:215], v[104:107], v[72:75]\n"
      "v_mov_b32 v151, s76\n"
      "ds_write_b32 v125, a228 offset:12288\n"
      "v_mfma_f32_16x16x32_bf16 v[88:91], a[216:219], v[104:107], v[88:91]\n"
      "v_xor_b32 v158, 8, v157\n"
      "v_mfma_f32_16x16x32_bf16 v[76:79], a[212:215], v[108:111], v[76:79]\n"
      "v_mfma_f32_16x16x32_bf16 v[92:95], a[216:219], v[108:111], v[92:95]\n"
      "s_waitcnt lgkmcnt(0)\n"
      "s_barrier\n"
      "s_memrealtime s[68:69]\n"
      "ds_read_b32 v237, v127 offset:12296\n"
      "ds_read_b64 v[152:153], v126 offset:12288\n"
      "ds_read_b128 v[160:163], v126 offset:0\n"
      "ds_read_b128 v[176:179], v126 offset:4096\n"
      "ds_read_b128 v[164:167], v126 offset:1024\n"
      "ds_read_b128 v[180:183], v126 offset:5120\n"
      "ds_read_b128 v[168:171], v126 offset:2048\n"
      "ds_read_b128 v[184:187], v126 offset:6144\n"
      "ds_read_b128 v[172:175], v126 offset:3072\n"
      "ds_read_b128 v[188:191], v126 offset:7168\n"
      "ds_read_b128 a[64:67], v126 offset:8192\n"
      "ds_read_b128 a[68:71], v126 offset:9216\n"
      "ds_read_b128 a[72:75], v126 offset:10240\n"
      "ds_read_b128 a[76:79], v126 offset:11264\n"
      "v_accvgpr_read_b32 v144, a228\n"
      "v_and_b32 v118, 0xf0, v127\n"
      "v_lshrrev_b32 v156, 3, v127\n"
      "v_and_b32 v156, 32, v156\n"
      "v_lshl_or_b32 v156, v118, 4, v156\n"
      "v_lshrrev_b32 v118, 5, v127\n"
      "v_and_or_b32 v156, v118, 16, v156\n"
      "v_add_u32 v156, 0x1000, v156\n"
      "v_add_u32 v155, 0x2000, v156\n"
      "v_xor_b32 v242, 0x2000, v240\n"
      "v_xor_b32 v243, 0x2000, v241\n"
      "v_mov_b32 v236, v150\n"
      "s_waitcnt lgkmcnt(12)\n"
      "v_cmp_neq_f32 vcc, v153, v117\n"
      "v_readfirstlane_b32 s88, v237\n"
      "s_cbranch_vccnz MIS_%=\n"
      "MR_%=:\n"
      "v_mul_f32 v144, v144, v148\n"
      "v_mov_b32 v145, v117\n"
      "v_fmac_f32 v144, v152, v149\n"
      "s_waitcnt lgkmcnt(10)\n"
      "v_pk_add_f32 v[64:65], v[64:65], v[160:161]\n"
      "v_pk_add_f32 v[66:67], v[66:67], v[162:163]\n"
      "v_pk_add_f32 v[80:81], v[80:81], v[176:177]\n"
      "v_pk_add_f32 v[82:83], v[82:83], v[178:179]\n"
      "s_waitcnt lgkmcnt(8)\n"
      "v_pk_add_f32 v[68:69], v[68:69], v[164:165]\n"
      "v_pk_add_f32 v[70:71], v[70:71], v[166:167]\n"
      "v_pk_add_f32 v[84:85], v[84:85], v[180:181]\n"
      "v_pk_add_f32 v[86:87], v[86:87], v[182:183]\n"
      "s_waitcnt lgkmcnt(6)\n"
      "v_pk_add_f32 v[72:73], v[72:73], v[168:169]\n"
      "v_pk_add_f32 v[74:75], v[74:75], v[170:171]\n"
      "v_pk_add_f32 v[88:89], v[88:89], v[184:185]\n"
      "v_pk_add_f32 v[90:91], v[90:91], v[186:187]\n"
      "s_waitcnt lgkmcnt(4)\n"
      "v_pk_add_f32 v[76:77], v[76:77], v[172:173]\n"
      "v_pk_add_f32 v[78:79], v[78:79], v[174:175]\n"
      "v_pk_add_f32 v[92:93], v[92:93], v[188:189]\n"
      "v_pk_add_f32 v[94:95], v[94:95], v[190:191]\n"
      "s_waitcnt lgkmcnt(0)\n"
      "s_add_u32 s65, s68, 36\n"
      "s_mov_b32 s66, s88\n"
      "s_sub_u32 s67, s65, s66\n"
      "s_cmp_lt_i32 s67, 0\n"
      "s_cselect_b32 s88, s66, s65\n"
      "v_mov_b32 v237, s88\n"
      "s_cmp_lg_u32 %[xw], 0\n"
      "s_cbranch_scc1 NH_%=\n"
      "s_mov_b64 exec, 1\n"
      "global_store_dwordx2 v157, v[236:237], %[line] sc1\n"
      "s_mov_b64 exec, -1\n"
      "NH_%=:\n"
      "v_mfma_f32_16x16x32_bf16 v[64:67], a[220:223], a[64:67], v[64:67]\n"
      "v_mfma_f32_16x16x32_bf16 v[80:83], a[224:227], a[64:67], v[80:83]\n"
      "v_mfma_f32_16x16x32_bf16 v[68:71], a[220:223], a[68:71], v[68:71]\n"
      "v_mfma_f32_16x16x32_bf16 v[84:87], a[224:227], a[68:71], v[84:87]\n"
      "v_mfma_f32_16x16x32_bf16 v[72:75], a[220:223], a[72:75], v[72:75]\n"
      "v_mfma_f32_16x16x32_bf16 v[88:91], a[224:227], a[72:75], v[88:91]\n"
      "v_mfma_f32_16x16x32_bf16 v[76:79], a[220:223], a[76:79], v[76:79]\n"
      "v_mfma_f32_16x16x32_bf16 v[92:95], a[224:227], a[76:79], v[92:95]\n"
      "s_nop 1\n"
      "s_memrealtime s[68:69]\n"
      "v_cvt_pk_bf16_f32 v128, v64, v65\n"
      "v_cvt_pk_bf16_f32 v129, v66, v67\n"
      "v_cvt_pk_bf16_f32 v130, v80, v81\n"
      "v_cvt_pk_bf16_f32 v131, v82, v83\n"
      "v_cvt_pk_bf16_f32 v132, v68, v69\n"
      "v_cvt_pk_bf16_f32 v133, v70, v71\n"
      "v_cvt_pk_bf16_f32 v134, v84, v85\n"
      "v_cvt_pk_bf16_f32 v135, v86, v87\n"
      "v_cvt_pk_bf16_f32 v136, v72, v73\n"
      "v_cvt_pk_bf16_f32 v137, v74, v75\n"
      "v_cvt_pk_bf16_f32 v138, v88, v89\n"
      "v_cvt_pk_bf16_f32 v139, v90, v91\n"
      "v_cvt_pk_bf16_f32 v140, v76, v77\n"
      "v_cvt_pk_bf16_f32 v141, v78, v79\n"
      "v_cvt_pk_bf16_f32 v142, v92, v93\n"
      "v_cvt_pk_bf16_f32 v143, v94, v95\n"
      "v_mad_u32_u24 v147, v144, s60, v151\n"
      "v_mad_u32_u24 v147, v145, s62, v147\n"
      "v_add3_u32 v146, v150, v144, v145\n"
      "v_mad_u32_u24 v147, v128, s60, v147\n"
      "v_mad_u32_u24 v147, v129, s62, v147\n"
      "v_add3_u32 v146, v146, v128, v129\n"
      "v_mad_u32_u24 v147, v130, s70, v147\n"
      "v_mad_u32_u24 v147, v131, s71, v147\n"
      "v_add3_u32 v146, v146, v130, v131\n"
      "v_mad_u32_u24 v147, v132, s60, v147\n"
      "v_mad_u32_u24 v147, v133, s62, v147\n"
      "v_add3_u32 v146, v146, v132, v133\n"
      "v_mad_u32_u24 v147, v134, s70, v147\n"
      "v_mad_u32_u24 v147, v135, s71, v147\n"
      "v_add3_u32 v146, v146, v134, v135\n"
      "v_mad_u32_u24 v147, v136, s60, v147\n"
      "v_mad_u32_u24 v147, v137, s62, v147\n"
      "v_add3_u32 v146, v146, v136, v137\n"
      "v_mad_u32_u24 v147, v138, s70, v147\n"
      "v_mad_u32_u24 v147, v139, s71, v147\n"
      "v_add3_u32 v146, v146, v138, v139\n"
      "v_mad_u32_u24 v147, v140, s60, v147\n"
      "v_mad_u32_u24 v147, v141, s62, v147\n"
      "v_add3_u32 v146, v146, v140, v141\n"
      "v_mad_u32_u24 v147, v142, s70, v147\n"
      "v_mad_u32_u24 v147, v143, s71, v147\n"
      "v_add3_u32 v146, v146, v142, v143\n"
      "s_waitcnt lgkmcnt(0)\n"
      "s_call_b64 s[84:85], SPIN_%=\n"
      "global_load_dwordx2 v[232:233], v158, %[line] sc1\n"
      "buffer_store_dwordx4 v[128:131], v127, %[srdx], %[xo] offen offset:0 sc1\n"
      "buffer_store_dwordx4 v[132:135], v127, %[srdx], %[xo] offen offset:1024 sc1\n"
      "buffer_store_dwordx4 v[136:139], v127, %[srdx], %[xo] offen offset:2048 sc1\n"
      "buffer_store_dwordx4 v[140:143], v127, %[srdx], %[xo] offen offset:3072 sc1\n"
      "buffer_store_dwordx4 v[144:147], v127, %[srdx], s81 offen sc1\n"
      "global_load_dwordx2 v[234:235], v158, %[line] sc1\n"
      "s_waitcnt vmcnt(7)\n"
      "s_barrier\n"
      "ds_read_b64_tr_b16 a[96:97], v246 offset:32768\n"
      "ds_read_b64_tr_b16 a[98:99], v246 offset:36864\n"
      "ds_read_b64_tr_b16 a[100:101], v247 offset:32768\n"
      "ds_read_b64_tr_b16 a[102:103], v247 offset:36864\n"
      "ds_read_b64_tr_b16 a[112:113], v250 offset:0\n"
      "ds_read_b64_tr_b16 a[114:115], v250 offset:4096\n"
      "ds_read_b64_tr_b16 a[116:117], v251 offset:0\n"
      "ds_read_b64_tr_b16 a[118:119], v251 offset:4096\n"
      "ds_read_b64_tr_b16 a[0:1], v250 offset:32768\n"
      "ds_read_b64_tr_b16 a[2:3], v250 offset:36864\n"
      "ds_read_b64_tr_b16 a[4:5], v251 offset:32768\n"
      "ds_read_b64_tr_b16 a[6:7], v251 offset:36864\n"
      "s_waitcnt lgkmcnt(8)\n"
      "v_mfma_f32_16x16x32_bf16 a[128:131], a[96:99], v[208:211], a[128:131]\n"
      "ds_read_b64_tr_b16 a[104:105], v244 offset:32768\n"
      "ds_read_b64_tr_b16 a[106:107], v244 offset:36864\n"
      "v_mfma_f32_16x16x32_bf16 a[144:147], a[100:103], v[208:211], a[144:147]\n"
      "ds_read_b64_tr_b16 a[108:109], v245 offset:32768\n"
      "ds_read_b64_tr_b16 a[110:111], v245 offset:36864\n"
      "s_waitcnt lgkmcnt(8)\n"
      "v_mfma_f32_16x16x32_bf16 a[128:131], a[112:115], v[192:195], a[128:131]\n"
      "ds_read_b64_tr_b16 a[120:121], v240 offset:0\n"
      "ds_read_b64_tr_b16 a[122:123], v240 offset:4096\n"
      "v_mfma_f32_16x16x32_bf16 a[144:147], a[116:119], v[192:195], a[144:147]\n"
      "ds_read_b64_tr_b16 a[124:125], v241 offset:0\n"
      "ds_read_b64_tr_b16 a[126:127], v241 offset:4096\n"
      "s_waitcnt lgkmcnt(8)\n"
      "v_mfma_f32_16x16x32_bf16 a[128:131], a[0:3], v[0:3], a[128:131]\n"
      "ds_read_b64_tr_b16 a[8:9], v240 offset:32768\n"
      "ds_read_b64_tr_b16 a[10:11], v240 offset:36864\n"
      "v_mfma_f32_16x16x32_bf16 a[144:147], a[4:7], v[0:3], a[144:147]\n"
      "ds_read_b64_tr_b16 a[12:13], v241 offset:32768\n"
      "ds_read_b64_tr_b16 a[14:15], v241 offset:36864\n"
      "v_mfma_f32_16x16x32_bf16 a[132:135], a[96:99], v[212:215], a[132:135]\n"
      "ds_write_b128 v125, v[16:19] offset:8192\n"
      "v_lshrrev_b32 v118, 2, v127\n"
      "v_mfma_f32_16x16x32_bf16 a[148:151], a[100:103], v[212:215], a[148:151]\n"
      "v_xor_b32 v118, v118, v127\n"
      "v_mfma_f32_16x16x32_bf16 a[132:135], a[112:115], v[196:199], a[132:135]\n"
      "v_and_b32 v118, 0xc0, v118\n"
      "v_mfma_f32_16x16x32_bf16 a[148:151], a[116:119], v[196:199], a[148:151]\n"
      "v_and_or_b32 v118, v127, 32, v118\n"
      "v_mfma_f32_16x16x32_bf16 a[132:135], a[0:3], v[4:7], a[132:135]\n"
      "ds_write_b128 v125, v[20:23] offset:9216\n"
      "v_and_b32 v119, 16, v127\n"
      "v_mfma_f32_16x16x32_bf16 a[148:151], a[4:7], v[4:7], a[148:151]\n"
      "v_mov_b32 v84, 0x3f80\n"
      "v_mfma_f32_16x16x32_bf16 a[136:139], a[96:99], v[216:219], a[136:139]\n"
      "v_lshlrev_b32 v119, v119, v84\n"
      "v_mfma_f32_16x16x32_bf16 a[152:155], a[100:103], v[216:219], a[152:155]\n"
      "v_cmp_eq_u32 vcc, 0, v118\n"
      "v_mfma_f32_16x16x32_bf16 a[136:139], a[112:115], v[200:203], a[136:139]\n"
      "ds_write_b128 v125, v[24:27] offset:10240\n"
      "v_mov_b32 v84, 0\n"
      "v_mfma_f32_16x16x32_bf16 a[152:155], a[116:119], v[200:203], a[152:155]\n"
      "v_cndmask_b32 v86, 0, v119, vcc\n"
      "v_mfma_f32_16x16x32_bf16 a[136:139], a[0:3], v[8:11], a[136:139]\n"
      "v_cmp_eq_u32 vcc, 32, v118\n"
      "v_mfma_f32_16x16x32_bf16 a[152:155], a[4:7], v[8:11], a[152:155]\n"
      "v_mov_b32 v85, 0\n"
      "v_mfma_f32_16x16x32_bf16 a[140:143], a[96:99], v[220:223], a[140:143]\n"
      "ds_write_b128 v125, v[28:31] offset:11264\n"
      "v_cndmask_b32 v87, 0, v119, vcc\n"
      "v_mfma_f32_16x16x32_bf16 a[156:159], a[100:103], v[220:223], a[156:159]\n"
      "v_mov_b32 v88, 0\n"
      "v_mfma_f32_16x16x32_bf16 a[140:143], a[112:115], v[204:207], a[140:143]\n"
      "v_mov_b32 v89, 0\n"
      "v_mfma_f32_16x16x32_bf16 a[156:159], a[116:119], v[204:207], a[156:159]\n"
      "v_mfma_f32_16x16x32_bf16 a[140:143], a[0:3], v[12:15], a[140:143]\n"
      "ds_write_b128 v125, a[128:131] offset:0\n"
      "v_mfma_f32_16x16x32_bf16 a[156:159], a[4:7], v[12:15], a[156:159]\n"
      "s_waitcnt lgkmcnt(13)\n"
      "v_mfma_f32_16x16x32_bf16 v[32:35], a[104:107], v[208:211], v[32:35]\n"
      "ds_read_b64_tr_b16 a[32:33], v240 offset:1024\n"
      "v_mfma_f32_16x16x32_bf16 v[36:39], a[104:107], v[212:215], v[36:39]\n"
      "ds_read_b64_tr_b16 a[34:35], v240 offset:5120\n"
      "v_mfma_f32_16x16x32_bf16 v[40:43], a[104:107], v[216:219], v[40:43]\n"
      "ds_write_b128 v125, a[144:147] offset:4096\n"
      "ds_read_b64_tr_b16 a[36:37], v241 offset:1024\n"
      "v_mfma_f32_16x16x32_bf16 v[44:47], a[104:107], v[220:223], v[44:47]\n"
      "ds_read_b64_tr_b16 a[38:39], v241 offset:5120\n"
      "v_mfma_f32_16x16x32_bf16 v[48:51], a[108:111], v[208:211], v[48:51]\n"
      "ds_read_b64_tr_b16 a[40:41], v242 offset:1024\n"
      "v_mfma_f32_16x16x32_bf16 v[52:55], a[108:111], v[212:215], v[52:55]\n"
      "ds_read_b64_tr_b16 a[42:43], v242 offset:5120\n"
      "v_mfma_f32_16x16x32_bf16 v[56:59], a[108:111], v[216:219], v[56:59]\n"
      "ds_write_b128 v125, a[132:135] offset:1024\n"
      "ds_read_b64_tr_b16 a[44:45], v243 offset:1024\n"
      "v_mfma_f32_16x16x32_bf16 v[60:63], a[108:111], v[220:223], v[60:63]\n"
      "ds_read_b64_tr_b16 a[46:47], v243 offset:5120\n"
      "s_waitcnt lgkmcnt(15)\n"
      "v_mfma_f32_16x16x32_bf16 v[32:35], a[120:123], v[192:195], v[32:35]\n"
      "ds_read_b64_tr_b16 a[16:17], v244 offset:0\n"
      "v_mfma_f32_16x16x32_bf16 v[36:39], a[120:123], v[196:199], v[36:39]\n"
      "ds_read_b64_tr_b16 a[18:19], v244 offset:4096\n"
      "v_mfma_f32_16x16x32_bf16 v[40:43], a[120:123], v[200:203], v[40:43]\n"
      "ds_write_b128 v125, a[148:151] offset:5120\n"
      "ds_read_b64_tr_b16 a[20:21], v245 offset:0\n"
      "v_mfma_f32_16x16x32_bf16 v[44:47], a[120:123], v[204:207], v[44:47]\n"
      "ds_read_b64_tr_b16 a[22:23], v245 offset:4096\n"
      "v_mfma_f32_16x16x32_bf16 v[48:51], a[124:127], v[192:195], v[48:51]\n"
      "ds_read_b64_tr_b16 a[24:25], v248 offset:0\n"
      "v_mfma_f32_16x16x32_bf16 v[52:55], a[124:127], v[196:199], v[52:55]\n"
      "ds_read_b64_tr_b16 a[26:27], v248 offset:4096\n"
      "v_mfma_f32_16x16x32_bf16 v[56:59], a[124:127], v[200:203], v[56:59]\n"
      "ds_write_b128 v125, a[136:139] offset:2048\n"
      "ds_read_b64_tr_b16 a[28:29], v120 offset:0\n"
      "v_mfma_f32_16x16x32_bf16 v[60:63], a[124:127], v[204:207], v[60:63]\n"
      "ds_read_b64_tr_b16 a[30:31], v120 offset:4096\n"
      "s_waitcnt lgkmcnt(15)\n"
      "v_mfma_f32_16x16x32_bf16 v[32:35], a[8:11], v[0:3], v[32:35]\n"
      "v_mfma_f32_16x16x32_bf16 v[36:39], a[8:11], v[4:7], v[36:39]\n"
      "v_mfma_f32_16x16x32_bf16 v[40:43], a[8:11], v[8:11], v[40:43]\n"
      "ds_write_b128 v125, a[152:155] offset:6144\n"
      "v_mfma_f32_16x16x32_bf16 v[44:47], a[8:11], v[12:15], v[44:47]\n"
      "s_waitcnt vmcnt(6)\n"
      "v_cmp_ne_u32 vcc, v232, v150\n"
      "s_memrealtime s[68:69]\n"
      "s_cbranch_vccnz POLL_%=\n"
      "GOT_%=:\n"
      "v_readfirstlane_b32 s88, v233\n"
      "s_waitcnt lgkmcnt(0)\n"
      "s_add_u32 s88, s88, 60\n"
      "s_sub_u32 s67, s88, s68\n"
      "s_cmp_gt_u32 s67, 1000\n"
      "s_cbranch_scc1 LD_%=\n"
      "s_call_b64 s[84:85], SPIN_%=\n"
      "LD_%=:\n"
      "buffer_load_dwordx4 v[80:83], v127, %[srdx], s79 offen sc1\n"
      "buffer_load_dwordx4 v[64:67], v127, %[srdx], s80 offen offset:0 sc1\n"
      "buffer_load_dwordx4 v[68:71], v127, %[srdx], s80 offen offset:1024 sc1\n"
      "buffer_load_dwordx4 v[72:75], v127, %[srdx], s80 offen offset:2048 sc1\n"
      "buffer_load_dwordx4 v[76:79], v127, %[srdx], s80 offen offset:3072 sc1\n"
      "v_mfma_f32_16x16x32_bf16 v[48:51], a[12:15], v[0:3], v[48:51]\n"
      "v_mfma_f32_16x16x32_bf16 v[52:55], a[12:15], v[4:7], v[52:55]\n"
      "v_mfma_f32_16x16x32_bf16 v[56:59], a[12:15], v[8:11], v[56:59]\n"
      "ds_write_b128 v125, a[140:143] offset:3072\n"
      "v_mfma_f32_16x16x32_bf16 v[60:63], a[12:15], v[12:15], v[60:63]\n"
      "s_waitcnt lgkmcnt(15)\n"
      "v_mfma_f32_16x16x32_bf16 v[32:35], a[32:35], v[96:99], v[32:35]\n"
      "v_mfma_f32_16x16x32_bf16 v[48:51], a[36:39], v[96:99], v[48:51]\n"
      "v_mfma_f32_16x16x32_bf16 v[36:39], a[32:35], v[100:103], v[36:39]\n"
      "ds_write_b128 v125, a[156:159] offset:7168\n"
      "v_mfma_f32_16x16x32_bf16 v[52:55], a[36:39], v[100:103], v[52:55]\n"
      "v_mfma_f32_16x16x32_bf16 v[40:43], a[32:35], v[104:107], v[40:43]\n"
      "v_mfma_f32_16x16x32_bf16 v[56:59], a[36:39], v[104:107], v[56:59]\n"
      "v_mfma_f32_16x16x32_bf16 v[44:47], a[32:35], v[108:111], v[44:47]\n"
      "v_mfma_f32_16x16x32_bf16 v[60:63], a[36:39], v[108:111], v[60:63]\n"
      "s_waitcnt lgkmcnt(13)\n"
      "v_mfma_f32_16x16x32_bf16 v[32:35], a[40:43], a[64:67], v[32:35]\n"
      "v_mfma_f32_16x16x32_bf16 v[48:51], a[44:47], a[64:67], v[48:51]\n"
      "v_mfma_f32_16x16x32_bf16 v[36:39], a[40:43], a[68:71], v[36:39]\n"
      "v_mfma_f32_16x16x32_bf16 v[52:55], a[44:47], a[68:71], v[52:55]\n"
      "v_mfma_f32_16x16x32_bf16 v[40:43], a[40:43], a[72:75], v[40:43]\n"
      "v_mfma_f32_16x16x32_bf16 v[56:59], a[44:47], a[72:75], v[56:59]\n"
      "v_mfma_f32_16x16x32_bf16 v[44:47], a[40:43], a[76:79], v[44:47]\n"
      "v_mfma_f32_16x16x32_bf16 v[60:63], a[44:47], a[76:79], v[60:63]\n"
      "s_waitcnt lgkmcnt(8)\n"
      "v_mfma_f32_16x16x32_bf16 v[32:35], a[16:19], v[16:19], v[32:35]\n"
      "v_mfma_f32_16x16x32_bf16 v[48:51], a[20:23], v[16:19], v[48:51]\n"
      "v_mfma_f32_16x16x32_bf16 v[36:39], a[16:19], v[20:23], v[36:39]\n"
      "v_mfma_f32_16x16x32_bf16 v[52:55], a[20:23], v[20:23], v[52:55]\n"
      "v_mfma_f32_16x16x32_bf16 v[40:43], a[16:19], v[24:27], v[40:43]\n"
      "v_mfma_f32_16x16x32_bf16 v[56:59], a[20:23], v[24:27], v[56:59]\n"
      "v_mfma_f32_16x16x32_bf16 v[44:47], a[16:19], v[28:31], v[44:47]\n"
      "v_mfma_f32_16x16x32_bf16 v[60:63], a[20:23], v[28:31], v[60:63]\n"
      "s_waitcnt lgkmcnt(0)\n"
      "s_barrier\n"
      "ds_read_b128 v[160:163], v126 offset:0\n"
      "ds_read_b128 v[176:179], v126 offset:4096\n"
      "ds_read_b128 v[164:167], v126 offset:1024\n"
      "ds_read_b128 v[180:183], v126 offset:5120\n"
      "ds_read_b128 v[168:171], v126 offset:2048\n"
      "ds_read_b128 v[184:187], v126 offset:6144\n"
      "ds_read_b128 v[172:175], v126 offset:3072\n"
      "ds_read_b128 v[188:191], v126 offset:7168\n"
      "ds_read_b128 a[80:83], v126 offset:8192\n"
      "ds_read_b128 a[84:87], v126 offset:9216\n"
      "ds_read_b128 a[88:91], v126 offset:10240\n"
      "ds_read_b128 a[92:95], v126 offset:11264\n"
      "s_waitcnt lgkmcnt(10)\n"
      "v_pk_add_f32 v[32:33], v[32:33], v[160:161]\n"
      "v_pk_add_f32 v[34:35], v[34:35], v[162:163]\n"
      "v_pk_add_f32 v[48:49], v[48:49], v[176:177]\n"
      "v_pk_add_f32 v[50:51], v[50:51], v[178:179]\n"
      "s_waitcnt lgkmcnt(8)\n"
      "v_pk_add_f32 v[36:37], v[36:37], v[164:165]\n"
      "v_pk_add_f32 v[38:39], v[38:39], v[166:167]\n"
      "v_pk_add_f32 v[52:53], v[52:53], v[180:181]\n"
      "v_pk_add_f32 v[54:55], v[54:55], v[182:183]\n"
      "s_waitcnt lgkmcnt(6)\n"
      "v_pk_add_f32 v[40:41], v[40:41], v[168:169]\n"
      "v_pk_add_f32 v[42:43], v[42:43], v[170:171]\n"
      "v_pk_add_f32 v[56:57], v[56:57], v[184:185]\n"
      "v_pk_add_f32 v[58:59], v[58:59], v[186:187]\n"
      "s_waitcnt lgkmcnt(4)\n"
      "v_pk_add_f32 v[44:45], v[44:45], v[172:173]\n"
      "v_pk_add_f32 v[46:47], v[46:47], v[174:175]\n"
      "v_pk_add_f32 v[60:61], v[60:61], v[188:189]\n"
      "v_pk_add_f32 v[62:63], v[62:63], v[190:191]\n"
      "s_waitcnt lgkmcnt(0)\n"
      "v_mfma_f32_16x16x32_bf16 v[32:35], a[24:27], a[80:83], v[32:35]\n"
      "v_mfma_f32_16x16x32_bf16 v[48:51], a[28:31], a[80:83], v[48:51]\n"
      "v_mfma_f32_16x16x32_bf16 v[36:39], a[24:27], a[84:87], v[36:39]\n"
      "v_mfma_f32_16x16x32_bf16 v[52:55], a[28:31], a[84:87], v[52:55]\n"
      "v_mfma_f32_16x16x32_bf16 v[40:43], a[24:27], a[88:91], v[40:43]\n"
      "v_mfma_f32_16x16x32_bf16 v[56:59], a[28:31], a[88:91], v[56:59]\n"
      "v_mfma_f32_16x16x32_bf16 v[44:47], a[24:27], a[92:95], v[44:47]\n"
      "v_mfma_f32_16x16x32_bf16 v[60:63], a[28:31], a[92:95], v[60:63]\n"
      "CHK_%=:\n"
      "s_waitcnt vmcnt(4)\n"
      "v_add_f32 v224, v144, v80\n"
      "v_rcp_f32 v224, v224\n"
      "v_mad_u32_u24 v119, v80, s60, v151\n"
      "v_mad_u32_u24 v119, v81, s62, v119\n"
      "v_add3_u32 v118, v150, v80, v81\n"
      "s_waitcnt vmcnt(3)\n"
      "v_mfma_f32_16x16x32_bf16 v[160:163], v[86:89], v[64:67], v[32:35]\n"
      "v_mad_u32_u24 v119, v64, s60, v119\n"
      "v_mad_u32_u24 v119, v65, s62, v119\n"
      "v_add3_u32 v118, v118, v64, v65\n"
      "v_mov_b32 v226, v224\n"
      "v_mfma_f32_16x16x32_bf16 v[176:179], v[84:87], v[64:67], v[48:51]\n"
      "v_mad_u32_u24 v119, v66, s70, v119\n"
      "v_mad_u32_u24 v119, v67, s71, v119\n"
      "v_add3_u32 v118, v118, v66, v67\n"
      "v_permlane32_swap_b32 v224, v226\n"
      "s_waitcnt vmcnt(2)\n"
      "v_mfma_f32_16x16x32_bf16 v[164:167], v[86:89], v[68:71], v[36:39]\n"
      "v_mad_u32_u24 v119, v68, s60, v119\n"
      "v_mad_u32_u24 v119, v69, s62, v119\n"
      "v_add3_u32 v118, v118, v68, v69\n"
      "v_mov_b32 v225, v224\n"
      "v_mov_b32 v227, v226\n"
      "v_mfma_f32_16x16x32_bf16 v[180:183], v[84:87], v[68:71], v[52:55]\n"
      "v_mad_u32_u24 v119, v70, s70, v119\n"
      "v_mad_u32_u24 v119, v71, s71, v119\n"
      "v_add3_u32 v118, v118, v70, v71\n"
      "v_permlane16_swap_b32 v224, v225\n"
      "s_waitcnt vmcnt(1)\n"
      "v_mfma_f32_16x16x32_bf16 v[168:171], v[86:89], v[72:75], v[40:43]\n"
      "v_mad_u32_u24 v119, v72, s60, v119\n"
      "v_mad_u32_u24 v119, v73, s62, v119\n"
      "v_add3_u32 v118, v118, v72, v73\n"
      "v_permlane16_swap_b32 v226, v227\n"
      "v_mfma_f32_16x16x32_bf16 v[184:187], v[84:87], v[72:75], v[56:59]\n"
      "v_mad_u32_u24 v119, v74, s70, v119\n"
      "v_mad_u32_u24 v119, v75, s71, v119\n"
      "v_add3_u32 v118, v118, v74, v75\n"
      "s_waitcnt vmcnt(0)\n"
      "v_mfma_f32_16x16x32_bf16 v[172:175], v[86:89], v[76:79], v[44:47]\n"
      "v_mad_u32_u24 v119, v76, s60, v119\n"
      "v_mad_u32_u24 v119, v77, s62, v119\n"
      "v_add3_u32 v118, v118, v76, v77\n"
      "v_mfma_f32_16x16x32_bf16 v[188:191], v[84:87], v[76:79], v[60:63]\n"
      "v_mad_u32_u24 v119, v78, s70, v119\n"
      "v_mad_u32_u24 v119, v79, s71, v119\n"
      "v_add3_u32 v118, v118, v78, v79\n"
      "v_cmp_ne_u64 vcc, v[118:119], v[82:83]\n"
      "s_cbranch_vccnz RETRY_%=\n"
      "OK_%=:\n"
      "v_cmp_neq_f32 vcc, v81, v117\n"
      "s_cbranch_vccnz FIX_%=\n"
      "FR_%=:\n"
      "v_pk_mul_f32 v[160:161], v[160:161], v[224:225] op_sel:[0,0] op_sel_hi:[1,0]\n"
      "v_pk_mul_f32 v[162:163], v[162:163], v[224:225] op_sel:[0,0] op_sel_hi:[1,0]\n"
      "v_pk_mul_f32 v[176:177], v[176:177], v[224:225] op_sel:[0,0] op_sel_hi:[1,0]\n"
      "v_pk_mul_f32 v[178:179], v[178:179], v[224:225] op_sel:[0,0] op_sel_hi:[1,0]\n"
      "v_cvt_pk_bf16_f32 v128, v160, v161\n"
      "v_cvt_pk_bf16_f32 v130, v176, v177\n"
      "v_cvt_pk_bf16_f32 v129, v162, v163\n"
      "v_cvt_pk_bf16_f32 v131, v178, v179\n"
      "v_pk_mul_f32 v[164:165], v[164:165], v[224:225] op_sel:[0,1] op_sel_hi:[1,1]\n"
      "v_permlane16_swap_b32 v128, v130\n"
      "v_permlane16_swap_b32 v129, v131\n"
      "global_store_dwordx4 v156, v[128:131], %[outp] offset:-4096 sc1\n"
      "v_pk_mul_f32 v[166:167], v[166:167], v[224:225] op_sel:[0,1] op_sel_hi:[1,1]\n"
      "v_pk_mul_f32 v[180:181], v[180:181], v[224:225] op_sel:[0,1] op_sel_hi:[1,1]\n"
      "v_pk_mul_f32 v[182:183], v[182:183], v[224:225] op_sel:[0,1] op_sel_hi:[1,1]\n"
      "v_cvt_pk_bf16_f32 v132, v164, v165\n"
      "v_cvt_pk_bf16_f32 v134, v180, v181\n"
      "v_cvt_pk_bf16_f32 v133, v166, v167\n"
      "v_cvt_pk_bf16_f32 v135, v182, v183\n"
      "v_pk_mul_f32 v[168:169], v[168:169], v[226:227] op_sel:[0,0] op_sel_hi:[1,0]\n"
      "v_permlane16_swap_b32 v132, v134\n"
      "v_permlane16_swap_b32 v133, v135\n"
      "global_store_dwordx4 v156, v[132:135], %[outp] offset:0 sc1\n"
      "v_pk_mul_f32 v[170:171], v[170:171], v[226:227] op_sel:[0,0] op_sel_hi:[1,0]\n"
      "v_pk_mul_f32 v[184:185], v[184:185], v[226:227] op_sel:[0,0] op_sel_hi:[1,0]\n"
      "v_pk_mul_f32 v[186:187], v[186:187], v[226:227] op_sel:[0,0] op_sel_hi:[1,0]\n"
      "v_cvt_pk_bf16_f32 v136, v168, v169\n"
      "v_cvt_pk_bf16_f32 v138, v184, v185\n"
      "v_cvt_pk_bf16_f32 v137, v170, v171\n"
      "v_cvt_pk_bf16_f32 v139, v186, v187\n"
      "v_pk_mul_f32 v[172:173], v[172:173], v[226:227] op_sel:[0,1] op_sel_hi:[1,1]\n"
      "v_permlane16_swap_b32 v136, v138\n"
      "v_permlane16_swap_b32 v137, v139\n"
      "global_store_dwordx4 v155, v[136:139], %[outp] offset:-4096 sc1\n"
      "v_pk_mul_f32 v[174:175], v[174:175], v[226:227] op_sel:[0,1] op_sel_hi:[1,1]\n"
      "v_pk_mul_f32 v[188:189], v[188:189], v[226:227] op_sel:[0,1] op_sel_hi:[1,1]\n"
      "v_pk_mul_f32 v[190:191], v[190:191], v[226:227] op_sel:[0,1] op_sel_hi:[1,1]\n"
      "v_cvt_pk_bf16_f32 v140, v172, v173\n"
      "v_cvt_pk_bf16_f32 v142, v188, v189\n"
      "v_cvt_pk_bf16_f32 v141, v174, v175\n"
      "v_cvt_pk_bf16_f32 v143, v190, v191\n"
      "v_mov_b32 v157, 0\n"
      "v_permlane16_swap_b32 v140, v142\n"
      "v_permlane16_swap_b32 v141, v143\n"
      "global_store_dwordx4 v155, v[140:143], %[outp] offset:0 sc1\n"
      "s_or_b32 s67, %[xw], %[xo]\n"
      "s_cbranch_scc1 END_%=\n"
      "s_mov_b64 exec, 1\n"
      "global_store_dwordx2 v157, v[150:151], %[line] sc1\n"
      "s_mov_b64 exec, -1\n"
      "END_%=:\n"
      ".subsection 1\n"
      "POLL_%=:\n"
      "s_waitcnt vmcnt(0) lgkmcnt(0)\n"
      "v_cmp_ne_u32 vcc, v234, v150\n"
      "v_mov_b32 v233, v235\n"
      "s_memrealtime s[68:69]\n"
      "s_sub_u32 s63, s63, 1\n"
      "s_cbranch_vccz GOT_%=\n"
      "global_load_dwordx2 v[234:235], v158, %[line] sc1\n"
      "s_cbranch_scc0 POLL_%=\n"
      "s_branch GOT_%=\n"
      "RETRY_%=:\n"
      "s_sub_u32 s63, s63, 1\n"
      "s_cbranch_scc1 OK_%=\n"
      "s_sleep 4\n"
      "buffer_load_dwordx4 v[80:83], v127, %[srdx], s79 offen sc1\n"
      "buffer_load_dwordx4 v[64:67], v127, %[srdx], s80 offen offset:0 sc1\n"
      "buffer_load_dwordx4 v[68:71], v127, %[srdx], s80 offen offset:1024 sc1\n"
      "buffer_load_dwordx4 v[72:75], v127, %[srdx], s80 offen offset:2048 sc1\n"
      "buffer_load_dwordx4 v[76:79], v127, %[srdx], s80 offen offset:3072 sc1\n"
      "s_branch CHK_%=\n"
      "GW0_%=:\n"
      "s_call_b64 s[84:85], WAIT_%=\n"
      "s_branch GX0_%=\n"
      "GW1_%=:\n"
      "s_call_b64 s[84:85], WAIT_%=\n"
      "s_branch GX1_%=\n"
      "GW2_%=:\n"
      "s_call_b64 s[84:85], WAIT_%=\n"
      "s_branch GX2_%=\n"
      "GW3_%=:\n"
      "s_call_b64 s[84:85], WAIT_%=\n"
      "s_add_u32 s87, s87, 1\n"
      "s_branch GX3_%=\n"
      "GW4_%=:\n"
      "s_call_b64 s[84:85], WAIT_%=\n"
      "s_branch GX4_%=\n"
      "GW5_%=:\n"
      "s_call_b64 s[84:85], WAIT_%=\n"
      "s_branch GX5_%=\n"
      "GW6_%=:\n"
      "s_call_b64 s[84:85], WAIT_%=\n"
      "s_branch GX6_%=\n"
      "GW7_%=:\n"
      "s_call_b64 s[84:85], WAIT_%=\n"
      "s_branch GX7_%=\n"
      "GW8_%=:\n"
      "s_call_b64 s[84:85], WAIT_%=\n"
      "s_branch GX8_%=\n"
      "WAIT_%=:\n"
      "s_add_u32 s87, s87, 1\n"
      "s_bitcmp1_b32 s89, s87\n"
      "s_cbranch_scc1 H1_%=\n"
      "SPIN_%=:\n"
      "s_sub_u32 s67, s68, s88\n"
      "s_cmp_ge_i32 s67, 0\n"
      "s_cbranch_scc1 GO_%=\n"
      "s_sleep 1\n"
      "s_memrealtime s[68:69]\n"
      "s_waitcnt lgkmcnt(0)\n"
      "s_branch SPIN_%=\n"
      "GO_%=:\n"
      "s_setpc_b64 s[84:85]\n"
      "H1_%=:\n"
      "s_cmp_gt_i32 s93, 50\n"
      "s_cbranch_scc1 HS_%=\n"
      "s_bitcmp1_b32 0x2a80, s87\n"
      "s_cbranch_scc0 H2_%=\n"
      "s_add_u32 s67, s66, 400\n"
      "s_waitcnt vmcnt(8)\n"
      "v_sub_u32 v250, s67, v250\n"
      "v_cmp_gt_u32 vcc, 900, v250\n"
      "v_add_u32 v250, 0xa00, v250\n"
      "v_xor_b32 v251, 0x100, v251\n"
      "s_sub_u32 s69, s66, s71\n"
      "s_cmp_ge_u32 s87, 12\n"
      "s_cselect_b32 s101, 50, s101\n"
      "s_bcnt1_i32_b32 s74, vcc_lo\n"
      "s_bcnt1_i32_b32 s75, vcc_hi\n"
      "v_cmp_gt_u32 vcc, 0x800, v250\n"
      "s_add_u32 s67, s74, s75\n"
      "s_cmp_le_u32 s67, 2\n"
      "s_cselect_b32 s86, 107, s86\n"
      "s_cmp_ge_u32 s67, 9\n"
      "s_cselect_b32 s86, 112, s86\n"
      "s_cmp_ge_u32 s75, 14\n"
      "s_cselect_b32 s76, 16, 33\n"
      "s_cmp_eq_u32 s87, 7\n"
      "s_cselect_b32 s76, s76, 13\n"
      "s_cbranch_vccnz H6_%=\n"
      "s_cmp_ge_u32 s74, s76\n"
      "s_cbranch_scc1 H3_%=\n"
      "s_cmp_eq_u32 s87, 13\n"
      "s_cbranch_scc1 H4_%=\n"
      "H8_%=:\n"
      "s_lshr_b32 s67, %[rep], 7\n"
      "s_cmp_eq_u32 s67, 1\n"
      "s_cbranch_scc0 SPIN_%=\n"
      "H5_%=:\n"
      "v_mov_b32 v127, s66\n"
      "s_add_u32 s74, s87, 2\n"
      "s_bfe_u32 s74, s74, 0x10001\n"
      "s_lshl_b32 s74, s74, 8\n"
      "s_add_u32 s74, s74, %[rep]\n"
      "RS_%=:\n"
      "s_sub_u32 s67, s68, s88\n"
      "s_cmp_ge_i32 s67, 0\n"
      "s_cbranch_scc1 RG_%=\n"
      "s_sleep 1\n"
      "s_memrealtime s[68:69]\n"
      "s_waitcnt lgkmcnt(0)\n"
      "s_branch RS_%=\n"
      "RG_%=:\n"
      "s_getreg_b32 s67, hwreg(HW_REG_IB_STS, 0, 4)\n"
      "s_cmp_le_u32 s67, 4\n"
      "s_cbranch_scc1 GO_%=\n"
      "buffer_store_dword v127, v124, %[srdr], s74 offen sc1\n"
      "s_setpc_b64 s[84:85]\n"
      "H6_%=:\n"
      "s_ff1_i32_b64 s67, vcc\n"
      "v_sub_u32 v250, s66, v250\n"
      "s_nop 3\n"
      "v_readlane_b32 s76, v250, s67\n"
      "s_add_u32 s76, s76, 2960\n"
      "s_and_b32 s76, s76, 31\n"
      "s_lshl_b32 s76, s76, 3\n"
      "s_lshl_b32 s89, 4, s87\n"
      "s_branch H7_%=\n"
      "H3_%=:\n"
      "s_mul_i32 s76, s69, 110\n"
      "s_ashr_i32 s76, s76, 10\n"
      "s_min_i32 s76, s76, 40\n"
      "s_add_u32 s76, s76, 96\n"
      "s_cmp_lt_u32 s75, 6\n"
      "s_cselect_b32 s76, 48, s76\n"
      "s_add_u32 s67, s66, 2000\n"
      "s_and_b32 s67, s67, -32\n"
      "s_lshr_b32 s75, s76, 3\n"
      "s_or_b32 s67, s67, s75\n"
      "v_mov_b32 v127, s67\n"
      "s_add_u32 s74, s87, 2\n"
      "s_bfe_u32 s74, s74, 0x10001\n"
      "s_lshl_b32 s74, s74, 8\n"
      "s_add_u32 s74, s74, %[rep]\n"
      "s_cmp_lt_u32 %[rep], 128\n"
      "s_cselect_b32 s89, 22, 20\n"
      "s_lshl_b32 s89, s89, s87\n"
      "s_sub_u32 s75, %[rep], 128\n"
      "s_cmp_lt_u32 s75, 32\n"
      "s_cbranch_scc0 H7_%=\n"
      "buffer_store_dword v127, v124, %[srdr], s74 offen sc1\n"
      "H7_%=:\n"
      "s_add_u32 s66, s66, s76\n"
      "s_mov_b32 s99, 13\n"
      "s_mov_b32 s100, 286\n"
      "s_add_u32 s93, s93, 99\n"
      "s_mov_b32 s86, 111\n"
      "s_sub_u32 s67, s65, s66\n"
      "s_cmp_lt_i32 s67, 0\n"
      "s_cselect_b32 s88, s66, s65\n"
      "s_mov_b32 s101, 200\n"
      "s_branch SPIN_%=\n"
      "H4_%=:\n"
      "s_mov_b32 s91, 0\n"
      "s_mov_b32 s89, 0\n"
      "v_mov_b32 v251, 0x100000\n"
      "s_branch SPIN_%=\n"
      "HS_%=:\n"
      "s_bitcmp1_b32 s87, 0\n"
      "s_cbranch_scc0 H5_%=\n"
      "v_xor_b32 v251, 0x100, v251\n"
      "s_lshr_b32 s67, s89, s87\n"
      "s_cmp_gt_u32 s67, 1\n"
      "s_cbranch_scc1 SPIN_%=\n"
      "s_add_u32 s67, s66, 400\n"
      "s_waitcnt vmcnt(8)\n"
      "v_sub_u32 v250, s67, v250\n"
      "v_cmp_gt_u32 vcc, 900, v250\n"
      "s_nop 3\n"
      "s_bcnt1_i32_b32 s74, vcc_lo\n"
      "s_cmp_gt_u32 s74, 2\n"
      "s_cbranch_scc1 H4_%=\n"
      "s_mov_b32 s99, 21\n"
      "s_mov_b32 s100, 282\n"
      "s_mov_b32 s86, 107\n"
      "s_branch H4_%=\n"
      "H0_%=:\n"
      "buffer_load_dword v250, v251, %[srdr], 0 offen sc1\n"
      "s_branch SPIN_%=\n"
      "H9_%=:\n"
      "s_bitcmp1_b32 s87, 0\n"
      "s_cbranch_scc0 H5_%=\n"
      "v_xor_b32 v251, 0x100, v251\n"
      "s_cmp_eq_u32 s87, 5\n"
      "s_cbranch_scc1 H8_%=\n"
      "s_branch SPIN_%=\n"
      "H2_%=:\n"
      "s_cmp_gt_u32 s87, 2\n"
      "s_cbranch_scc1 H9_%=\n"
      "s_cmp_eq_u32 s87, 1\n"
      "s_cbranch_scc1 H0_%=\n"
      "s_waitcnt vmcnt(8)\n"
      "v_subrev_u32 v250, s71, v250\n"
      "v_min_u32 v250, 0x258, v250\n"
      "v_mbcnt_lo_u32_b32 v251, -1, 0\n"
      "v_mbcnt_hi_u32_b32 v251, -1, v251\n"
      "v_add_u32_dpp v250, v250, v250 row_shr:1 row_mask:0xf bank_mask:0xf bound_ctrl:0\n"
      "v_lshlrev_b32 v124, 12, v251\n"
      "s_nop 0\n"
      "v_add_u32_dpp v250, v250, v250 row_shr:2 row_mask:0xf bank_mask:0xf bound_ctrl:0\n"
      "v_lshlrev_b32 v251, 2, v251\n"
      "v_add_u32 v124, 0x2000, v124\n"
      "v_add_u32_dpp v250, v250, v250 row_shr:4 row_mask:0xf bank_mask:0xf bound_ctrl:0\n"
      "v_add_u32 v251, %[vrd], v251\n"
      "v_readlane_b32 s66, v250, 7\n"
      "s_nop 0\n"
      "s_lshr_b32 s66, s66, 3\n"
      "s_add_u32 s66, s66, s71\n"
      "s_add_u32 s66, s66, %[cph]\n"
      "s_add_u32 s71, s66, 545\n"
      "s_sub_u32 s67, s65, s66\n"
      "s_cmp_lt_i32 s67, 0\n"
      "s_cselect_b32 s88, s66, s65\n"
      "s_branch SPIN_%=\n"
      "FB0_%=:\n"
      "s_nop 7\n"
      "s_nop 7\n"
      "v_xor_b32 v118, 0x0, v242\n"
      "v_xor_b32 v119, 0x0, v243\n"
      "ds_read_b128 a[64:67], v118 offset:0\n"
      "ds_read_b128 a[68:71], v118 offset:4096\n"
      "ds_read_b128 a[72:75], v119 offset:0\n"
      "ds_read_b128 a[76:79], v119 offset:4096\n"
      "ds_read_b128 a[80:83], v118 offset:1024\n"
      "ds_read_b128 a[84:87], v118 offset:5120\n"
      "ds_read_b128 a[88:91], v119 offset:1024\n"
      "ds_read_b128 a[92:95], v119 offset:5120\n"
      "s_waitcnt lgkmcnt(0)\n"
      "v_mfma_f32_16x16x32_bf16 v[128:131], a[64:67], a[0:3], 0\n"
      "v_mfma_f32_16x16x32_bf16 v[136:139], a[64:67], a[4:7], 0\n"
      "v_mfma_f32_16x16x32_bf16 v[144:147], a[64:67], a[8:11], 0\n"
      "v_mfma_f32_16x16x32_bf16 v[152:155], a[64:67], a[12:15], 0\n"
      "v_mfma_f32_16x16x32_bf16 v[132:135], a[68:71], a[0:3], 0\n"
      "v_mfma_f32_16x16x32_bf16 v[140:143], a[68:71], a[4:7], 0\n"
      "v_mfma_f32_16x16x32_bf16 v[148:151], a[68:71], a[8:11], 0\n"
      "v_mfma_f32_16x16x32_bf16 v[156:159], a[68:71], a[12:15], 0\n"
      "v_mfma_f32_16x16x32_bf16 v[128:131], a[72:75], a[16:19], v[128:131]\n"
      "v_mfma_f32_16x16x32_bf16 v[136:139], a[72:75], a[20:23], v[136:139]\n"
      "v_mfma_f32_16x16x32_bf16 v[144:147], a[72:75], a[24:27], v[144:147]\n"
      "v_mfma_f32_16x16x32_bf16 v[152:155], a[72:75], a[28:31], v[152:155]\n"
      "v_mfma_f32_16x16x32_bf16 v[132:135], a[76:79], a[16:19], v[132:135]\n"
      "v_mfma_f32_16x16x32_bf16 v[140:143], a[76:79], a[20:23], v[140:143]\n"
      "v_mfma_f32_16x16x32_bf16 v[148:151], a[76:79], a[24:27], v[148:151]\n"
      "v_mfma_f32_16x16x32_bf16 v[156:159], a[76:79], a[28:31], v[156:159]\n"
      "v_mfma_f32_16x16x32_bf16 v[128:131], a[80:83], a[32:35], v[128:131]\n"
      "v_mfma_f32_16x16x32_bf16 v[136:139], a[80:83], a[36:39], v[136:139]\n"
      "v_mfma_f32_16x16x32_bf16 v[144:147], a[80:83], a[40:43], v[144:147]\n"
      "v_mfma_f32_16x16x32_bf16 v[152:155], a[80:83], a[44:47], v[152:155]\n"
      "v_mfma_f32_16x16x32_bf16 v[132:135], a[84:87], a[32:35], v[132:135]\n"
      "v_mfma_f32_16x16x32_bf16 v[140:143], a[84:87], a[36:39], v[140:143]\n"
      "v_mfma_f32_16x16x32_bf16 v[148:151], a[84:87], a[40:43], v[148:151]\n"
      "v_mfma_f32_16x16x32_bf16 v[156:159], a[84:87], a[44:47], v[156:159]\n"
      "v_mfma_f32_16x16x32_bf16 v[128:131], a[88:91], a[48:51], v[128:131]\n"
      "v_mfma_f32_16x16x32_bf16 v[136:139], a[88:91], a[52:55], v[136:139]\n"
      "v_mfma_f32_16x16x32_bf16 v[144:147], a[88:91], a[56:59], v[144:147]\n"
      "v_mfma_f32_16x16x32_bf16 v[152:155], a[88:91], a[60:63], v[152:155]\n"
      "v_mfma_f32_16x16x32_bf16 v[132:135], a[92:95], a[48:51], v[132:135]\n"
      "v_mfma_f32_16x16x32_bf16 v[140:143], a[92:95], a[52:55], v[140:143]\n"
      "v_mfma_f32_16x16x32_bf16 v[148:151], a[92:95], a[56:59], v[148:151]\n"
      "v_mfma_f32_16x16x32_bf16 v[156:159], a[92:95], a[60:63], v[156:159]\n"
      "s_nop 7\n"
      "v_max3_f32 v112, v128, v129, v130\n"
      "v_max3_f32 v113, v136, v137, v138\n"
      "v_max3_f32 v114, v144, v145, v146\n"
      "v_max3_f32 v115, v152, v153, v154\n"
      "v_max3_f32 v112, v112, v131, v132\n"
      "v_max3_f32 v113, v113, v139, v140\n"
      "v_max3_f32 v114, v114, v147, v148\n"
      "v_max3_f32 v115, v115, v155, v156\n"
      "v_max3_f32 v112, v112, v133, v134\n"
      "v_max3_f32 v113, v113, v141, v142\n"
      "v_max3_f32 v114, v114, v149, v150\n"
      "v_max3_f32 v115, v115, v157, v158\n"
      "v_max_f32 v112, v112, v135\n"
      "v_max_f32 v113, v113, v143\n"
      "v_max_f32 v114, v114, v151\n"
      "v_max_f32 v115, v115, v159\n"
      "v_mov_b32 v160, v112\n"
      "v_mov_b32 v161, v113\n"
      "v_mov_b32 v162, v114\n"
      "v_mov_b32 v163, v115\n"
      "v_permlane16_swap_b32 v112, v160\n"
      "v_permlane16_swap_b32 v113, v161\n"
      "v_permlane16_swap_b32 v114, v162\n"
      "v_permlane16_swap_b32 v115, v163\n"
      "v_max_f32 v112, v112, v160\n"
      "v_max_f32 v113, v113, v161\n"
      "v_max_f32 v114, v114, v162\n"
      "v_max_f32 v115, v115, v163\n"
      "v_mov_b32 v160, v112\n"
      "v_mov_b32 v161, v113\n"
      "v_mov_b32 v162, v114\n"
      "v_mov_b32 v163, v115\n"
      "v_permlane32_swap_b32 v112, v160\n"
      "v_permlane32_swap_b32 v113, v161\n"
      "v_permlane32_swap_b32 v114, v162\n"
      "v_permlane32_swap_b32 v115, v163\n"
      "v_max_f32 v112, v112, v160\n"
      "v_max_f32 v113, v113, v161\n"
      "v_max_f32 v114, v114, v162\n"
      "v_max_f32 v115, v115, v163\n"
      "v_sub_f32 v112, 0xc1800000, v112\n"
      "v_sub_f32 v113, 0xc1800000, v113\n"
      "v_sub_f32 v114, 0xc1800000, v114\n"
      "v_sub_f32 v115, 0xc1800000, v115\n"
      "v_floor_f32 v112, v112\n"
      "v_floor_f32 v113, v113\n"
      "v_floor_f32 v114, v114\n"
      "v_floor_f32 v115, v115\n"
      "v_min_f32 v112, v112, v224\n"
      "v_min_f32 v113, v113, v228\n"
      "v_min_f32 v114, v114, v232\n"
      "v_min_f32 v115, v115, v236\n"
      "v_sub_f32 v164, v112, v224\n"
      "v_sub_f32 v165, v113, v228\n"
      "v_sub_f32 v166, v114, v232\n"
      "v_sub_f32 v167, v115, v236\n"
      "v_mov_b32 v224, v112\n"
      "v_mov_b32 v228, v113\n"
      "v_mov_b32 v232, v114\n"
      "v_mov_b32 v236, v115\n"
      "v_exp_f32 v112, v164\n"
      "v_exp_f32 v113, v165\n"
      "v_exp_f32 v114, v166\n"
      "v_exp_f32 v115, v167\n"
      "v_mov_b32 v225, v224\n"
      "v_mov_b32 v229, v228\n"
      "v_mov_b32 v233, v232\n"
      "v_mov_b32 v237, v236\n"
      "v_mov_b64 v[226:227], v[224:225]\n"
      "v_mov_b64 v[230:231], v[228:229]\n"
      "v_mov_b64 v[234:235], v[232:233]\n"
      "v_mov_b64 v[238:239], v[236:237]\n"
      "s_call_b64 s[84:85], SCALE_%=\n"
      "v_pk_add_f32 v[128:129], v[128:129], v[224:225]\n"
      "v_pk_add_f32 v[130:131], v[130:131], v[224:225]\n"
      "v_pk_add_f32 v[132:133], v[132:133], v[224:225]\n"
      "v_pk_add_f32 v[134:135], v[134:135], v[224:225]\n"
      "v_pk_add_f32 v[136:137], v[136:137], v[228:229]\n"
      "v_pk_add_f32 v[138:139], v[138:139], v[228:229]\n"
      "v_pk_add_f32 v[140:141], v[140:141], v[228:229]\n"
      "v_pk_add_f32 v[142:143], v[142:143], v[228:229]\n"
      "v_pk_add_f32 v[144:145], v[144:145], v[232:233]\n"
      "v_pk_add_f32 v[146:147], v[146:147], v[232:233]\n"
      "v_pk_add_f32 v[148:149], v[148:149], v[232:233]\n"
      "v_pk_add_f32 v[150:151], v[150:151], v[232:233]\n"
      "v_pk_add_f32 v[152:153], v[152:153], v[236:237]\n"
      "v_pk_add_f32 v[154:155], v[154:155], v[236:237]\n"
      "v_pk_add_f32 v[156:157], v[156:157], v[236:237]\n"
      "v_pk_add_f32 v[158:159], v[158:159], v[236:237]\n"
      "v_exp_f32 v112, v128\n"
      "v_exp_f32 v113, v129\n"
      "v_exp_f32 v114, v130\n"
      "v_cvt_pk_bf16_f32 v128, v112, v113\n"
      "v_exp_f32 v115, v131\n"
      "v_exp_f32 v112, v132\n"
      "v_cvt_pk_bf16_f32 v129, v114, v115\n"
      "v_exp_f32 v113, v133\n"
      "v_exp_f32 v114, v134\n"
      "v_cvt_pk_bf16_f32 v130, v112, v113\n"
      "v_exp_f32 v115, v135\n"
      "v_exp_f32 v112, v136\n"
      "v_cvt_pk_bf16_f32 v131, v114, v115\n"
      "v_exp_f32 v113, v137\n"
      "v_exp_f32 v114, v138\n"
      "v_cvt_pk_bf16_f32 v132, v112, v113\n"
      "v_exp_f32 v115, v139\n"
      "v_exp_f32 v112, v140\n"
      "v_cvt_pk_bf16_f32 v133, v114, v115\n"
      "v_exp_f32 v113, v141\n"
      "v_exp_f32 v114, v142\n"
      "v_cvt_pk_bf16_f32 v134, v112, v113\n"
      "v_exp_f32 v115, v143\n"
      "v_exp_f32 v112, v144\n"
      "v_cvt_pk_bf16_f32 v135, v114, v115\n"
      "v_exp_f32 v113, v145\n"
      "v_exp_f32 v114, v146\n"
      "v_cvt_pk_bf16_f32 v136, v112, v113\n"
      "v_exp_f32 v115, v147\n"
      "v_exp_f32 v112, v148\n"
      "v_cvt_pk_bf16_f32 v137, v114, v115\n"
      "v_exp_f32 v113, v149\n"
      "v_exp_f32 v114, v150\n"
      "v_cvt_pk_bf16_f32 v138, v112, v113\n"
      "v_exp_f32 v115, v151\n"
      "v_exp_f32 v112, v152\n"
      "v_cvt_pk_bf16_f32 v139, v114, v115\n"
      "v_exp_f32 v113, v153\n"
      "v_exp_f32 v114, v154\n"
      "v_cvt_pk_bf16_f32 v140, v112, v113\n"
      "v_exp_f32 v115, v155\n"
      "v_exp_f32 v112, v156\n"
      "v_cvt_pk_bf16_f32 v141, v114, v115\n"
      "v_exp_f32 v113, v157\n"
      "v_exp_f32 v114, v158\n"
      "v_cvt_pk_bf16_f32 v142, v112, v113\n"
      "v_exp_f32 v115, v159\n"
      "s_nop 0\n"
      "v_cvt_pk_bf16_f32 v143, v114, v115\n"
      "ds_read_b128 a[64:67], v242 offset:32768\n"
      "ds_read_b128 a[68:71], v242 offset:36864\n"
      "ds_read_b128 a[72:75], v243 offset:32768\n"
      "ds_read_b128 a[76:79], v243 offset:36864\n"
      "ds_read_b128 a[80:83], v242 offset:33792\n"
      "ds_read_b128 a[84:87], v242 offset:37888\n"
      "ds_read_b128 a[88:91], v243 offset:33792\n"
      "ds_read_b128 a[92:95], v243 offset:37888\n"
      "s_waitcnt lgkmcnt(0)\n"
      "v_mfma_f32_16x16x32_bf16 v[160:163], a[64:67], a[0:3], v[224:227]\n"
      "v_mfma_f32_16x16x32_bf16 v[168:171], a[64:67], a[4:7], v[228:231]\n"
      "v_mfma_f32_16x16x32_bf16 v[176:179], a[64:67], a[8:11], v[232:235]\n"
      "v_mfma_f32_16x16x32_bf16 v[184:187], a[64:67], a[12:15], v[236:239]\n"
      "v_mfma_f32_16x16x32_bf16 v[164:167], a[68:71], a[0:3], v[224:227]\n"
      "v_mfma_f32_16x16x32_bf16 v[172:175], a[68:71], a[4:7], v[228:231]\n"
      "v_mfma_f32_16x16x32_bf16 v[180:183], a[68:71], a[8:11], v[232:235]\n"
      "v_mfma_f32_16x16x32_bf16 v[188:191], a[68:71], a[12:15], v[236:239]\n"
      "v_mfma_f32_16x16x32_bf16 v[160:163], a[72:75], a[16:19], v[160:163]\n"
      "v_mfma_f32_16x16x32_bf16 v[168:171], a[72:75], a[20:23], v[168:171]\n"
      "v_mfma_f32_16x16x32_bf16 v[176:179], a[72:75], a[24:27], v[176:179]\n"
      "v_mfma_f32_16x16x32_bf16 v[184:187], a[72:75], a[28:31], v[184:187]\n"
      "v_mfma_f32_16x16x32_bf16 v[164:167], a[76:79], a[16:19], v[164:167]\n"
      "v_mfma_f32_16x16x32_bf16 v[172:175], a[76:79], a[20:23], v[172:175]\n"
      "v_mfma_f32_16x16x32_bf16 v[180:183], a[76:79], a[24:27], v[180:183]\n"
      "v_mfma_f32_16x16x32_bf16 v[188:191], a[76:79], a[28:31], v[188:191]\n"
      "v_mfma_f32_16x16x32_bf16 v[160:163], a[80:83], a[32:35], v[160:163]\n"
      "v_mfma_f32_16x16x32_bf16 v[168:171], a[80:83], a[36:39], v[168:171]\n"
      "v_mfma_f32_16x16x32_bf16 v[176:179], a[80:83], a[40:43], v[176:179]\n"
      "v_mfma_f32_16x16x32_bf16 v[184:187], a[80:83], a[44:47], v[184:187]\n"
      "v_mfma_f32_16x16x32_bf16 v[164:167], a[84:87], a[32:35], v[164:167]\n"
      "v_mfma_f32_16x16x32_bf16 v[172:175], a[84:87], a[36:39], v[172:175]\n"
      "v_mfma_f32_16x16x32_bf16 v[180:183], a[84:87], a[40:43], v[180:183]\n"
      "v_mfma_f32_16x16x32_bf16 v[188:191], a[84:87], a[44:47], v[188:191]\n"
      "v_mfma_f32_16x16x32_bf16 v[160:163], a[88:91], a[48:51], v[160:163]\n"
      "v_mfma_f32_16x16x32_bf16 v[168:171], a[88:91], a[52:55], v[168:171]\n"
      "v_mfma_f32_16x16x32_bf16 v[176:179], a[88:91], a[56:59], v[176:179]\n"
      "v_mfma_f32_16x16x32_bf16 v[184:187], a[88:91], a[60:63], v[184:187]\n"
      "v_mfma_f32_16x16x32_bf16 v[164:167], a[92:95], a[48:51], v[164:167]\n"
      "v_mfma_f32_16x16x32_bf16 v[172:175], a[92:95], a[52:55], v[172:175]\n"
      "v_mfma_f32_16x16x32_bf16 v[180:183], a[92:95], a[56:59], v[180:183]\n"
      "v_mfma_f32_16x16x32_bf16 v[188:191], a[92:95], a[60:63], v[188:191]\n"
      "s_cmp_eq_u32 s64, 0\n"
      "s_cbranch_scc0 NRET0_0_%=\n"
      "v_mov_b64 v[192:193], v[128:129]\n"
      "v_mov_b64 v[194:195], v[130:131]\n"
      "v_mov_b64 v[196:197], v[132:133]\n"
      "v_mov_b64 v[198:199], v[134:135]\n"
      "v_mov_b64 v[200:201], v[136:137]\n"
      "v_mov_b64 v[202:203], v[138:139]\n"
      "v_mov_b64 v[204:205], v[140:141]\n"
      "v_mov_b64 v[206:207], v[142:143]\n"
      "s_nop 1\n"
      "s_branch RET0_0_%=\n"
      "NRET0_0_%=:\n"
      "s_cmp_eq_u32 s64, 1\n"
      "s_cbranch_scc0 NRET0_1_%=\n"
      "v_mov_b64 v[192:193], v[128:129]\n"
      "v_mov_b64 v[194:195], v[130:131]\n"
      "v_mov_b64 v[196:197], v[132:133]\n"
      "v_mov_b64 v[198:199], v[134:135]\n"
      "v_mov_b64 v[200:201], v[136:137]\n"
      "v_mov_b64 v[202:203], v[138:139]\n"
      "v_mov_b64 v[204:205], v[140:141]\n"
      "v_mov_b64 v[206:207], v[142:143]\n"
      "s_nop 1\n"
      "s_branch RET0_1_%=\n"
      "NRET0_1_%=:\n"
      "s_cmp_eq_u32 s64, 2\n"
      "s_cbranch_scc0 NRET0_2_%=\n"
      "v_mov_b64 v[192:193], v[128:129]\n"
      "v_mov_b64 v[194:195], v[130:131]\n"
      "v_mov_b64 v[196:197], v[132:133]\n"
      "v_mov_b64 v[198:199], v[134:135]\n"
      "v_mov_b64 v[200:201], v[136:137]\n"
      "v_mov_b64 v[202:203], v[138:139]\n"
      "v_mov_b64 v[204:205], v[140:141]\n"
      "v_mov_b64 v[206:207], v[142:143]\n"
      "s_nop 1\n"
      "s_branch RET0_2_%=\n"
      "NRET0_2_%=:\n"
      "s_cmp_eq_u32 s64, 3\n"
      "s_cbranch_scc0 NRET0_3_%=\n"
      "v_mov_b64 v[192:193], v[128:129]\n"
      "v_mov_b64 v[194:195], v[130:131]\n"
      "v_mov_b64 v[196:197], v[132:133]\n"
      "v_mov_b64 v[198:199], v[134:135]\n"
      "v_mov_b64 v[200:201], v[136:137]\n"
      "v_mov_b64 v[202:203], v[138:139]\n"
      "v_mov_b64 v[204:205], v[140:141]\n"
      "v_mov_b64 v[206:207], v[142:143]\n"
      "s_nop 1\n"
      "s_branch RET0_3_%=\n"
      "NRET0_3_%=:\n"
      "s_cmp_eq_u32 s64, 4\n"
      "s_cbranch_scc0 NRET0_4_%=\n"
      "v_mov_b64 v[192:193], v[128:129]\n"
      "v_mov_b64 v[194:195], v[130:131]\n"
      "v_mov_b64 v[196:197], v[132:133]\n"
      "v_mov_b64 v[198:199], v[134:135]\n"
      "v_mov_b64 v[200:201], v[136:137]\n"
      "v_mov_b64 v[202:203], v[138:139]\n"
      "v_mov_b64 v[204:205], v[140:141]\n"
      "v_mov_b64 v[206:207], v[142:143]\n"
      "s_nop 1\n"
      "s_branch RET0_4_%=\n"
      "NRET0_4_%=:\n"
      "s_cmp_eq_u32 s64, 5\n"
      "s_cbranch_scc0 NRET0_5_%=\n"
      "v_mov_b64 v[16:17], v[128:129]\n"
      "v_mov_b64 v[18:19], v[130:131]\n"
      "v_mov_b64 v[20:21], v[132:133]\n"
      "v_mov_b64 v[22:23], v[134:135]\n"
      "v_mov_b64 v[24:25], v[136:137]\n"
      "v_mov_b64 v[26:27], v[138:139]\n"
      "v_mov_b64 v[28:29], v[140:141]\n"
      "v_mov_b64 v[30:31], v[142:143]\n"
      "s_nop 1\n"
      "s_branch RET0_5_%=\n"
      "NRET0_5_%=:\n"
      "FB1_%=:\n"
      "s_add_u32 s84, s63, 1\n"
      "s_sub_u32 s84, s84, %[n1]\n"
      "s_or_b32 s84, s84, s64\n"
      "s_cbranch_scc0 RET1_0_%=\n"
      "s_nop 7\n"
      "s_nop 7\n"
      "v_xor_b32 v118, 0x10000, v242\n"
      "v_xor_b32 v119, 0x10000, v243\n"
      "ds_read_b128 a[64:67], v118 offset:32768\n"
      "ds_read_b128 a[68:71], v118 offset:36864\n"
      "ds_read_b128 a[72:75], v119 offset:32768\n"
      "ds_read_b128 a[76:79], v119 offset:36864\n"
      "ds_read_b128 a[80:83], v118 offset:33792\n"
      "ds_read_b128 a[84:87], v118 offset:37888\n"
      "ds_read_b128 a[88:91], v119 offset:33792\n"
      "ds_read_b128 a[92:95], v119 offset:37888\n"
      "s_waitcnt lgkmcnt(0)\n"
      "v_mfma_f32_16x16x32_bf16 v[160:163], a[64:67], a[0:3], 0\n"
      "v_mfma_f32_16x16x32_bf16 v[168:171], a[64:67], a[4:7], 0\n"
      "v_mfma_f32_16x16x32_bf16 v[176:179], a[64:67], a[8:11], 0\n"
      "v_mfma_f32_16x16x32_bf16 v[184:187], a[64:67], a[12:15], 0\n"
      "v_mfma_f32_16x16x32_bf16 v[164:167], a[68:71], a[0:3], 0\n"
      "v_mfma_f32_16x16x32_bf16 v[172:175], a[68:71], a[4:7], 0\n"
      "v_mfma_f32_16x16x32_bf16 v[180:183], a[68:71], a[8:11], 0\n"
      "v_mfma_f32_16x16x32_bf16 v[188:191], a[68:71], a[12:15], 0\n"
      "v_mfma_f32_16x16x32_bf16 v[160:163], a[72:75], a[16:19], v[160:163]\n"
      "v_mfma_f32_16x16x32_bf16 v[168:171], a[72:75], a[20:23], v[168:171]\n"
      "v_mfma_f32_16x16x32_bf16 v[176:179], a[72:75], a[24:27], v[176:179]\n"
      "v_mfma_f32_16x16x32_bf16 v[184:187], a[72:75], a[28:31], v[184:187]\n"
      "v_mfma_f32_16x16x32_bf16 v[164:167], a[76:79], a[16:19], v[164:167]\n"
      "v_mfma_f32_16x16x32_bf16 v[172:175], a[76:79], a[20:23], v[172:175]\n"
      "v_mfma_f32_16x16x32_bf16 v[180:183], a[76:79], a[24:27], v[180:183]\n"
      "v_mfma_f32_16x16x32_bf16 v[188:191], a[76:79], a[28:31], v[188:191]\n"
      "v_mfma_f32_16x16x32_bf16 v[160:163], a[80:83], a[32:35], v[160:163]\n"
      "v_mfma_f32_16x16x32_bf16 v[168:171], a[80:83], a[36:39], v[168:171]\n"
      "v_mfma_f32_16x16x32_bf16 v[176:179], a[80:83], a[40:43], v[176:179]\n"
      "v_mfma_f32_16x16x32_bf16 v[184:187], a[80:83], a[44:47], v[184:187]\n"
      "v_mfma_f32_16x16x32_bf16 v[164:167], a[84:87], a[32:35], v[164:167]\n"
      "v_mfma_f32_16x16x32_bf16 v[172:175], a[84:87], a[36:39], v[172:175]\n"
      "v_mfma_f32_16x16x32_bf16 v[180:183], a[84:87], a[40:43], v[180:183]\n"
      "v_mfma_f32_16x16x32_bf16 v[188:191], a[84:87], a[44:47], v[188:191]\n"
      "v_mfma_f32_16x16x32_bf16 v[160:163], a[88:91], a[48:51], v[160:163]\n"
      "v_mfma_f32_16x16x32_bf16 v[168:171], a[88:91], a[52:55], v[168:171]\n"
      "v_mfma_f32_16x16x32_bf16 v[176:179], a[88:91], a[56:59], v[176:179]\n"
      "v_mfma_f32_16x16x32_bf16 v[184:187], a[88:91], a[60:63], v[184:187]\n"
      "v_mfma_f32_16x16x32_bf16 v[164:167], a[92:95], a[48:51], v[164:167]\n"
      "v_mfma_f32_16x16x32_bf16 v[172:175], a[92:95], a[52:55], v[172:175]\n"
      "v_mfma_f32_16x16x32_bf16 v[180:183], a[92:95], a[56:59], v[180:183]\n"
      "v_mfma_f32_16x16x32_bf16 v[188:191], a[92:95], a[60:63], v[188:191]\n"
      "s_nop 7\n"
      "v_max3_f32 v112, v160, v161, v162\n"
      "v_max3_f32 v113, v168, v169, v170\n"
      "v_max3_f32 v114, v176, v177, v178\n"
      "v_max3_f32 v115, v184, v185, v186\n"
      "v_max3_f32 v112, v112, v163, v164\n"
      "v_max3_f32 v113, v113, v171, v172\n"
      "v_max3_f32 v114, v114, v179, v180\n"
      "v_max3_f32 v115, v115, v187, v188\n"
      "v_max3_f32 v112, v112, v165, v166\n"
      "v_max3_f32 v113, v113, v173, v174\n"
      "v_max3_f32 v114, v114, v181, v182\n"
      "v_max3_f32 v115, v115, v189, v190\n"
      "v_max_f32 v112, v112, v167\n"
      "v_max_f32 v113, v113, v175\n"
      "v_max_f32 v114, v114, v183\n"
      "v_max_f32 v115, v115, v191\n"
      "v_mov_b32 v128, v112\n"
      "v_mov_b32 v129, v113\n"
      "v_mov_b32 v130, v114\n"
      "v_mov_b32 v131, v115\n"
      "v_permlane16_swap_b32 v112, v128\n"
      "v_permlane16_swap_b32 v113, v129\n"
      "v_permlane16_swap_b32 v114, v130\n"
      "v_permlane16_swap_b32 v115, v131\n"
      "v_max_f32 v112, v112, v128\n"
      "v_max_f32 v113, v113, v129\n"
      "v_max_f32 v114, v114, v130\n"
      "v_max_f32 v115, v115, v131\n"
      "v_mov_b32 v128, v112\n"
      "v_mov_b32 v129, v113\n"
      "v_mov_b32 v130, v114\n"
      "v_mov_b32 v131, v115\n"
      "v_permlane32_swap_b32 v112, v128\n"
      "v_permlane32_swap_b32 v113, v129\n"
      "v_permlane32_swap_b32 v114, v130\n"
      "v_permlane32_swap_b32 v115, v131\n"
      "v_max_f32 v112, v112, v128\n"
      "v_max_f32 v113, v113, v129\n"
      "v_max_f32 v114, v114, v130\n"
      "v_max_f32 v115, v115, v131\n"
      "v_sub_f32 v112, 0xc1800000, v112\n"
      "v_sub_f32 v113, 0xc1800000, v113\n"
      "v_sub_f32 v114, 0xc1800000, v114\n"
      "v_sub_f32 v115, 0xc1800000, v115\n"
      "v_floor_f32 v112, v112\n"
      "v_floor_f32 v113, v113\n"
      "v_floor_f32 v114, v114\n"
      "v_floor_f32 v115, v115\n"
      "v_min_f32 v112, v112, v224\n"
      "v_min_f32 v113, v113, v228\n"
      "v_min_f32 v114, v114, v232\n"
      "v_min_f32 v115, v115, v236\n"
      "v_sub_f32 v132, v112, v224\n"
      "v_sub_f32 v133, v113, v228\n"
      "v_sub_f32 v134, v114, v232\n"
      "v_sub_f32 v135, v115, v236\n"
      "v_mov_b32 v224, v112\n"
      "v_mov_b32 v228, v113\n"
      "v_mov_b32 v232, v114\n"
      "v_mov_b32 v236, v115\n"
      "v_exp_f32 v112, v132\n"
      "v_exp_f32 v113, v133\n"
      "v_exp_f32 v114, v134\n"
      "v_exp_f32 v115, v135\n"
      "v_mov_b32 v225, v224\n"
      "v_mov_b32 v229, v228\n"
      "v_mov_b32 v233, v232\n"
      "v_mov_b32 v237, v236\n"
      "v_mov_b64 v[226:227], v[224:225]\n"
      "v_mov_b64 v[230:231], v[228:229]\n"
      "v_mov_b64 v[234:235], v[232:233]\n"
      "v_mov_b64 v[238:239], v[236:237]\n"
      "s_call_b64 s[84:85], SCALE_%=\n"
      "v_pk_add_f32 v[160:161], v[160:161], v[224:225]\n"
      "v_pk_add_f32 v[162:163], v[162:163], v[224:225]\n"
      "v_pk_add_f32 v[164:165], v[164:165], v[224:225]\n"
      "v_pk_add_f32 v[166:167], v[166:167], v[224:225]\n"
      "v_pk_add_f32 v[168:169], v[168:169], v[228:229]\n"
      "v_pk_add_f32 v[170:171], v[170:171], v[228:229]\n"
      "v_pk_add_f32 v[172:173], v[172:173], v[228:229]\n"
      "v_pk_add_f32 v[174:175], v[174:175], v[228:229]\n"
      "v_pk_add_f32 v[176:177], v[176:177], v[232:233]\n"
      "v_pk_add_f32 v[178:179], v[178:179], v[232:233]\n"
      "v_pk_add_f32 v[180:181], v[180:181], v[232:233]\n"
      "v_pk_add_f32 v[182:183], v[182:183], v[232:233]\n"
      "v_pk_add_f32 v[184:185], v[184:185], v[236:237]\n"
      "v_pk_add_f32 v[186:187], v[186:187], v[236:237]\n"
      "v_pk_add_f32 v[188:189], v[188:189], v[236:237]\n"
      "v_pk_add_f32 v[190:191], v[190:191], v[236:237]\n"
      "v_exp_f32 v112, v160\n"
      "v_exp_f32 v113, v161\n"
      "v_exp_f32 v114, v162\n"
      "v_cvt_pk_bf16_f32 v160, v112, v113\n"
      "v_exp_f32 v115, v163\n"
      "v_exp_f32 v112, v164\n"
      "v_cvt_pk_bf16_f32 v161, v114, v115\n"
      "v_exp_f32 v113, v165\n"
      "v_exp_f32 v114, v166\n"
      "v_cvt_pk_bf16_f32 v162, v112, v113\n"
      "v_exp_f32 v115, v167\n"
      "v_exp_f32 v112, v168\n"
      "v_cvt_pk_bf16_f32 v163, v114, v115\n"
      "v_exp_f32 v113, v169\n"
      "v_exp_f32 v114, v170\n"
      "v_cvt_pk_bf16_f32 v164, v112, v113\n"
      "v_exp_f32 v115, v171\n"
      "v_exp_f32 v112, v172\n"
      "v_cvt_pk_bf16_f32 v165, v114, v115\n"
      "v_exp_f32 v113, v173\n"
      "v_exp_f32 v114, v174\n"
      "v_cvt_pk_bf16_f32 v166, v112, v113\n"
      "v_exp_f32 v115, v175\n"
      "v_exp_f32 v112, v176\n"
      "v_cvt_pk_bf16_f32 v167, v114, v115\n"
      "v_exp_f32 v113, v177\n"
      "v_exp_f32 v114, v178\n"
      "v_cvt_pk_bf16_f32 v168, v112, v113\n"
      "v_exp_f32 v115, v179\n"
      "v_exp_f32 v112, v180\n"
      "v_cvt_pk_bf16_f32 v169, v114, v115\n"
      "v_exp_f32 v113, v181\n"
      "v_exp_f32 v114, v182\n"
      "v_cvt_pk_bf16_f32 v170, v112, v113\n"
      "v_exp_f32 v115, v183\n"
      "v_exp_f32 v112, v184\n"
      "v_cvt_pk_bf16_f32 v171, v114, v115\n"
      "v_exp_f32 v113, v185\n"
      "v_exp_f32 v114, v186\n"
      "v_cvt_pk_bf16_f32 v172, v112, v113\n"
      "v_exp_f32 v115, v187\n"
      "v_exp_f32 v112, v188\n"
      "v_cvt_pk_bf16_f32 v173, v114, v115\n"
      "v_exp_f32 v113, v189\n"
      "v_exp_f32 v114, v190\n"
      "v_cvt_pk_bf16_f32 v174, v112, v113\n"
      "v_exp_f32 v115, v191\n"
      "s_nop 0\n"
      "v_cvt_pk_bf16_f32 v175, v114, v115\n"
      "ds_read_b128 a[64:67], v242 offset:0\n"
      "ds_read_b128 a[68:71], v242 offset:4096\n"
      "ds_read_b128 a[72:75], v243 offset:0\n"
      "ds_read_b128 a[76:79], v243 offset:4096\n"
      "ds_read_b128 a[80:83], v242 offset:1024\n"
      "ds_read_b128 a[84:87], v242 offset:5120\n"
      "ds_read_b128 a[88:91], v243 offset:1024\n"
      "ds_read_b128 a[92:95], v243 offset:5120\n"
      "s_waitcnt lgkmcnt(0)\n"
      "v_mfma_f32_16x16x32_bf16 v[128:131], a[64:67], a[0:3], v[224:227]\n"
      "v_mfma_f32_16x16x32_bf16 v[136:139], a[64:67], a[4:7], v[228:231]\n"
      "v_mfma_f32_16x16x32_bf16 v[144:147], a[64:67], a[8:11], v[232:235]\n"
      "v_mfma_f32_16x16x32_bf16 v[152:155], a[64:67], a[12:15], v[236:239]\n"
      "v_mfma_f32_16x16x32_bf16 v[132:135], a[68:71], a[0:3], v[224:227]\n"
      "v_mfma_f32_16x16x32_bf16 v[140:143], a[68:71], a[4:7], v[228:231]\n"
      "v_mfma_f32_16x16x32_bf16 v[148:151], a[68:71], a[8:11], v[232:235]\n"
      "v_mfma_f32_16x16x32_bf16 v[156:159], a[68:71], a[12:15], v[236:239]\n"
      "v_mfma_f32_16x16x32_bf16 v[128:131], a[72:75], a[16:19], v[128:131]\n"
      "v_mfma_f32_16x16x32_bf16 v[136:139], a[72:75], a[20:23], v[136:139]\n"
      "v_mfma_f32_16x16x32_bf16 v[144:147], a[72:75], a[24:27], v[144:147]\n"
      "v_mfma_f32_16x16x32_bf16 v[152:155], a[72:75], a[28:31], v[152:155]\n"
      "v_mfma_f32_16x16x32_bf16 v[132:135], a[76:79], a[16:19], v[132:135]\n"
      "v_mfma_f32_16x16x32_bf16 v[140:143], a[76:79], a[20:23], v[140:143]\n"
      "v_mfma_f32_16x16x32_bf16 v[148:151], a[76:79], a[24:27], v[148:151]\n"
      "v_mfma_f32_16x16x32_bf16 v[156:159], a[76:79], a[28:31], v[156:159]\n"
      "v_mfma_f32_16x16x32_bf16 v[128:131], a[80:83], a[32:35], v[128:131]\n"
      "v_mfma_f32_16x16x32_bf16 v[136:139], a[80:83], a[36:39], v[136:139]\n"
      "v_mfma_f32_16x16x32_bf16 v[144:147], a[80:83], a[40:43], v[144:147]\n"
      "v_mfma_f32_16x16x32_bf16 v[152:155], a[80:83], a[44:47], v[152:155]\n"
      "v_mfma_f32_16x16x32_bf16 v[132:135], a[84:87], a[32:35], v[132:135]\n"
      "v_mfma_f32_16x16x32_bf16 v[140:143], a[84:87], a[36:39], v[140:143]\n"
      "v_mfma_f32_16x16x32_bf16 v[148:151], a[84:87], a[40:43], v[148:151]\n"
      "v_mfma_f32_16x16x32_bf16 v[156:159], a[84:87], a[44:47], v[156:159]\n"
      "v_mfma_f32_16x16x32_bf16 v[128:131], a[88:91], a[48:51], v[128:131]\n"
      "v_mfma_f32_16x16x32_bf16 v[136:139], a[88:91], a[52:55], v[136:139]\n"
      "v_mfma_f32_16x16x32_bf16 v[144:147], a[88:91], a[56:59], v[144:147]\n"
      "v_mfma_f32_16x16x32_bf16 v[152:155], a[88:91], a[60:63], v[152:155]\n"
      "v_mfma_f32_16x16x32_bf16 v[132:135], a[92:95], a[48:51], v[132:135]\n"
      "v_mfma_f32_16x16x32_bf16 v[140:143], a[92:95], a[52:55], v[140:143]\n"
      "v_mfma_f32_16x16x32_bf16 v[148:151], a[92:95], a[56:59], v[148:151]\n"
      "v_mfma_f32_16x16x32_bf16 v[156:159], a[92:95], a[60:63], v[156:159]\n"
      "s_cmp_eq_u32 s64, 0\n"
      "s_cbranch_scc0 NRET1_0_%=\n"
      "v_mov_b64 v[208:209], v[160:161]\n"
      "v_mov_b64 v[210:211], v[162:163]\n"
      "v_mov_b64 v[212:213], v[164:165]\n"
      "v_mov_b64 v[214:215], v[166:167]\n"
      "v_mov_b64 v[216:217], v[168:169]\n"
      "v_mov_b64 v[218:219], v[170:171]\n"
      "v_mov_b64 v[220:221], v[172:173]\n"
      "v_mov_b64 v[222:223], v[174:175]\n"
      "s_nop 1\n"
      "s_branch RET1_0_%=\n"
      "NRET1_0_%=:\n"
      "s_cmp_eq_u32 s64, 1\n"
      "s_cbranch_scc0 NRET1_1_%=\n"
      "v_mov_b64 v[208:209], v[160:161]\n"
      "v_mov_b64 v[210:211], v[162:163]\n"
      "v_mov_b64 v[212:213], v[164:165]\n"
      "v_mov_b64 v[214:215], v[166:167]\n"
      "v_mov_b64 v[216:217], v[168:169]\n"
      "v_mov_b64 v[218:219], v[170:171]\n"
      "v_mov_b64 v[220:221], v[172:173]\n"
      "v_mov_b64 v[222:223], v[174:175]\n"
      "s_nop 1\n"
      "s_branch RET1_1_%=\n"
      "NRET1_1_%=:\n"
      "s_cmp_eq_u32 s64, 2\n"
      "s_cbranch_scc0 NRET1_2_%=\n"
      "v_mov_b64 v[208:209], v[160:161]\n"
      "v_mov_b64 v[210:211], v[162:163]\n"
      "v_mov_b64 v[212:213], v[164:165]\n"
      "v_mov_b64 v[214:215], v[166:167]\n"
      "v_mov_b64 v[216:217], v[168:169]\n"
      "v_mov_b64 v[218:219], v[170:171]\n"
      "v_mov_b64 v[220:221], v[172:173]\n"
      "v_mov_b64 v[222:223], v[174:175]\n"
      "s_nop 1\n"
      "s_branch RET1_2_%=\n"
      "NRET1_2_%=:\n"
      "s_cmp_eq_u32 s64, 3\n"
      "s_cbranch_scc0 NRET1_3_%=\n"
      "v_mov_b64 v[208:209], v[160:161]\n"
      "v_mov_b64 v[210:211], v[162:163]\n"
      "v_mov_b64 v[212:213], v[164:165]\n"
      "v_mov_b64 v[214:215], v[166:167]\n"
      "v_mov_b64 v[216:217], v[168:169]\n"
      "v_mov_b64 v[218:219], v[170:171]\n"
      "v_mov_b64 v[220:221], v[172:173]\n"
      "v_mov_b64 v[222:223], v[174:175]\n"
      "s_nop 1\n"
      "s_branch RET1_3_%=\n"
      "NRET1_3_%=:\n"
      "s_cmp_eq_u32 s64, 4\n"
      "s_cbranch_scc0 NRET1_4_%=\n"
      "v_mov_b64 v[208:209], v[160:161]\n"
      "v_mov_b64 v[210:211], v[162:163]\n"
      "v_mov_b64 v[212:213], v[164:165]\n"
      "v_mov_b64 v[214:215], v[166:167]\n"
      "v_mov_b64 v[216:217], v[168:169]\n"
      "v_mov_b64 v[218:219], v[170:171]\n"
      "v_mov_b64 v[220:221], v[172:173]\n"
      "v_mov_b64 v[222:223], v[174:175]\n"
      "s_nop 1\n"
      "s_branch RET1_4_%=\n"
      "NRET1_4_%=:\n"
      "s_cmp_eq_u32 s64, 5\n"
      "s_cbranch_scc0 NRET1_5_%=\n"
      "v_mov_b64 v[0:1], v[160:161]\n"
      "v_mov_b64 v[2:3], v[162:163]\n"
      "v_mov_b64 v[4:5], v[164:165]\n"
      "v_mov_b64 v[6:7], v[166:167]\n"
      "v_mov_b64 v[8:9], v[168:169]\n"
      "v_mov_b64 v[10:11], v[170:171]\n"
      "v_mov_b64 v[12:13], v[172:173]\n"
      "v_mov_b64 v[14:15], v[174:175]\n"
      "s_nop 1\n"
      "s_branch RET1_5_%=\n"
      "NRET1_5_%=:\n"
      "s_cmp_eq_u32 s64, 6\n"
      "s_cbranch_scc0 NRET1_6_%=\n"
      "v_mov_b64 v[96:97], v[160:161]\n"
      "v_mov_b64 v[98:99], v[162:163]\n"
      "v_mov_b64 v[100:101], v[164:165]\n"
      "v_mov_b64 v[102:103], v[166:167]\n"
      "v_mov_b64 v[104:105], v[168:169]\n"
      "v_mov_b64 v[106:107], v[170:171]\n"
      "v_mov_b64 v[108:109], v[172:173]\n"
      "v_mov_b64 v[110:111], v[174:175]\n"
      "v_mov_b32 v117, v224\n"
      "s_nop 1\n"
      "v_mov_b32_dpp v117, v228 quad_perm:[0,1,2,3] row_mask:0x2 bank_mask:0xf\n"
      "s_nop 1\n"
      "v_mov_b32_dpp v117, v232 quad_perm:[0,1,2,3] row_mask:0x4 bank_mask:0xf\n"
      "s_nop 1\n"
      "v_mov_b32_dpp v117, v236 quad_perm:[0,1,2,3] row_mask:0x8 bank_mask:0xf\n"
      "v_mfma_f32_16x16x32_bf16 a[228:231], a[196:199], v[96:99], a[192:195]\n"
      "v_mfma_f32_16x16x32_bf16 a[228:231], a[200:203], v[100:103], a[228:231]\n"
      "v_mfma_f32_16x16x32_bf16 a[228:231], a[204:207], v[104:107], a[228:231]\n"
      "ds_write_b128 v125, a[160:163] offset:0\n"
      "ds_write_b128 v125, a[164:167] offset:1024\n"
      "ds_write_b128 v125, a[168:171] offset:2048\n"
      "ds_write_b128 v125, a[172:175] offset:3072\n"
      "ds_write_b128 v125, a[176:179] offset:4096\n"
      "ds_write_b128 v125, a[180:183] offset:5120\n"
      "ds_write_b128 v125, a[184:187] offset:6144\n"
      "ds_write_b128 v125, a[188:191] offset:7168\n"
      "ds_write_b128 v125, v[96:99] offset:8192\n"
      "ds_write_b128 v125, v[100:103] offset:9216\n"
      "ds_write_b128 v125, v[104:107] offset:10240\n"
      "ds_write_b32 v125, v117 offset:12292\n"
      "s_nop 1\n"
      "s_branch RET1_6_%=\n"
      "NRET1_6_%=:\n"
      "SCALE_%=:\n"
      "v_pk_mul_f32 v[32:33], v[32:33], v[112:113] op_sel:[0,0] op_sel_hi:[1,0]\n"
      "v_pk_mul_f32 v[34:35], v[34:35], v[112:113] op_sel:[0,0] op_sel_hi:[1,0]\n"
      "v_pk_mul_f32 v[36:37], v[36:37], v[112:113] op_sel:[0,1] op_sel_hi:[1,1]\n"
      "v_pk_mul_f32 v[38:39], v[38:39], v[112:113] op_sel:[0,1] op_sel_hi:[1,1]\n"
      "v_pk_mul_f32 v[40:41], v[40:41], v[114:115] op_sel:[0,0] op_sel_hi:[1,0]\n"
      "v_pk_mul_f32 v[42:43], v[42:43], v[114:115] op_sel:[0,0] op_sel_hi:[1,0]\n"
      "v_pk_mul_f32 v[44:45], v[44:45], v[114:115] op_sel:[0,1] op_sel_hi:[1,1]\n"
      "v_pk_mul_f32 v[46:47], v[46:47], v[114:115] op_sel:[0,1] op_sel_hi:[1,1]\n"
      "v_pk_mul_f32 v[48:49], v[48:49], v[112:113] op_sel:[0,0] op_sel_hi:[1,0]\n"
      "v_pk_mul_f32 v[50:51], v[50:51], v[112:113] op_sel:[0,0] op_sel_hi:[1,0]\n"
      "v_pk_mul_f32 v[52:53], v[52:53], v[112:113] op_sel:[0,1] op_sel_hi:[1,1]\n"
      "v_pk_mul_f32 v[54:55], v[54:55], v[112:113] op_sel:[0,1] op_sel_hi:[1,1]\n"
      "v_pk_mul_f32 v[56:57], v[56:57], v[114:115] op_sel:[0,0] op_sel_hi:[1,0]\n"
      "v_pk_mul_f32 v[58:59], v[58:59], v[114:115] op_sel:[0,0] op_sel_hi:[1,0]\n"
      "v_pk_mul_f32 v[60:61], v[60:61], v[114:115] op_sel:[0,1] op_sel_hi:[1,1]\n"
      "v_pk_mul_f32 v[62:63], v[62:63], v[114:115] op_sel:[0,1] op_sel_hi:[1,1]\n"
      "v_pk_mul_f32 v[64:65], v[64:65], v[112:113] op_sel:[0,0] op_sel_hi:[1,0]\n"
      "v_pk_mul_f32 v[66:67], v[66:67], v[112:113] op_sel:[0,0] op_sel_hi:[1,0]\n"
      "v_pk_mul_f32 v[68:69], v[68:69], v[112:113] op_sel:[0,1] op_sel_hi:[1,1]\n"
      "v_pk_mul_f32 v[70:71], v[70:71], v[112:113] op_sel:[0,1] op_sel_hi:[1,1]\n"
      "v_pk_mul_f32 v[72:73], v[72:73], v[114:115] op_sel:[0,0] op_sel_hi:[1,0]\n"
      "v_pk_mul_f32 v[74:75], v[74:75], v[114:115] op_sel:[0,0] op_sel_hi:[1,0]\n"
      "v_pk_mul_f32 v[76:77], v[76:77], v[114:115] op_sel:[0,1] op_sel_hi:[1,1]\n"
      "v_pk_mul_f32 v[78:79], v[78:79], v[114:115] op_sel:[0,1] op_sel_hi:[1,1]\n"
      "v_pk_mul_f32 v[80:81], v[80:81], v[112:113] op_sel:[0,0] op_sel_hi:[1,0]\n"
      "v_pk_mul_f32 v[82:83], v[82:83], v[112:113] op_sel:[0,0] op_sel_hi:[1,0]\n"
      "v_pk_mul_f32 v[84:85], v[84:85], v[112:113] op_sel:[0,1] op_sel_hi:[1,1]\n"
      "v_pk_mul_f32 v[86:87], v[86:87], v[112:113] op_sel:[0,1] op_sel_hi:[1,1]\n"
      "v_pk_mul_f32 v[88:89], v[88:89], v[114:115] op_sel:[0,0] op_sel_hi:[1,0]\n"
      "v_pk_mul_f32 v[90:91], v[90:91], v[114:115] op_sel:[0,0] op_sel_hi:[1,0]\n"
      "v_pk_mul_f32 v[92:93], v[92:93], v[114:115] op_sel:[0,1] op_sel_hi:[1,1]\n"
      "v_pk_mul_f32 v[94:95], v[94:95], v[114:115] op_sel:[0,1] op_sel_hi:[1,1]\n"
      "v_accvgpr_read_b32 v118, a128\n"
      "v_accvgpr_read_b32 v119, a129\n"
      "v_pk_mul_f32 v[118:119], v[118:119], v[112:113] op_sel:[0,0] op_sel_hi:[1,0]\n"
      "v_accvgpr_write_b32 a128, v118\n"
      "v_accvgpr_write_b32 a129, v119\n"
      "v_accvgpr_read_b32 v118, a130\n"
      "v_accvgpr_read_b32 v119, a131\n"
      "v_pk_mul_f32 v[118:119], v[118:119], v[112:113] op_sel:[0,0] op_sel_hi:[1,0]\n"
      "v_accvgpr_write_b32 a130, v118\n"
      "v_accvgpr_write_b32 a131, v119\n"
      "v_accvgpr_read_b32 v118, a132\n"
      "v_accvgpr_read_b32 v119, a133\n"
      "v_pk_mul_f32 v[118:119], v[118:119], v[112:113] op_sel:[0,1] op_sel_hi:[1,1]\n"
      "v_accvgpr_write_b32 a132, v118\n"
      "v_accvgpr_write_b32 a133, v119\n"
      "v_accvgpr_read_b32 v118, a134\n"
      "v_accvgpr_read_b32 v119, a135\n"
      "v_pk_mul_f32 v[118:119], v[118:119], v[112:113] op_sel:[0,1] op_sel_hi:[1,1]\n"
      "v_accvgpr_write_b32 a134, v118\n"
      "v_accvgpr_write_b32 a135, v119\n"
      "v_accvgpr_read_b32 v118, a136\n"
      "v_accvgpr_read_b32 v119, a137\n"
      "v_pk_mul_f32 v[118:119], v[118:119], v[114:115] op_sel:[0,0] op_sel_hi:[1,0]\n"
      "v_accvgpr_write_b32 a136, v118\n"
      "v_accvgpr_write_b32 a137, v119\n"
      "v_accvgpr_read_b32 v118, a138\n"
      "v_accvgpr_read_b32 v119, a139\n"
      "v_pk_mul_f32 v[118:119], v[118:119], v[114:115] op_sel:[0,0] op_sel_hi:[1,0]\n"
      "v_accvgpr_write_b32 a138, v118\n"
      "v_accvgpr_write_b32 a139, v119\n"
      "v_accvgpr_read_b32 v118, a140\n"
      "v_accvgpr_read_b32 v119, a141\n"
      "v_pk_mul_f32 v[118:119], v[118:119], v[114:115] op_sel:[0,1] op_sel_hi:[1,1]\n"
      "v_accvgpr_write_b32 a140, v118\n"
      "v_accvgpr_write_b32 a141, v119\n"
      "v_accvgpr_read_b32 v118, a142\n"
      "v_accvgpr_read_b32 v119, a143\n"
      "v_pk_mul_f32 v[118:119], v[118:119], v[114:115] op_sel:[0,1] op_sel_hi:[1,1]\n"
      "v_accvgpr_write_b32 a142, v118\n"
      "v_accvgpr_write_b32 a143, v119\n"
      "v_accvgpr_read_b32 v118, a144\n"
      "v_accvgpr_read_b32 v119, a145\n"
      "v_pk_mul_f32 v[118:119], v[118:119], v[112:113] op_sel:[0,0] op_sel_hi:[1,0]\n"
      "v_accvgpr_write_b32 a144, v118\n"
      "v_accvgpr_write_b32 a145, v119\n"
      "v_accvgpr_read_b32 v118, a146\n"
      "v_accvgpr_read_b32 v119, a147\n"
      "v_pk_mul_f32 v[118:119], v[118:119], v[112:113] op_sel:[0,0] op_sel_hi:[1,0]\n"
      "v_accvgpr_write_b32 a146, v118\n"
      "v_accvgpr_write_b32 a147, v119\n"
      "v_accvgpr_read_b32 v118, a148\n"
      "v_accvgpr_read_b32 v119, a149\n"
      "v_pk_mul_f32 v[118:119], v[118:119], v[112:113] op_sel:[0,1] op_sel_hi:[1,1]\n"
      "v_accvgpr_write_b32 a148, v118\n"
      "v_accvgpr_write_b32 a149, v119\n"
      "v_accvgpr_read_b32 v118, a150\n"
      "v_accvgpr_read_b32 v119, a151\n"
      "v_pk_mul_f32 v[118:119], v[118:119], v[112:113] op_sel:[0,1] op_sel_hi:[1,1]\n"
      "v_accvgpr_write_b32 a150, v118\n"
      "v_accvgpr_write_b32 a151, v119\n"
      "v_accvgpr_read_b32 v118, a152\n"
      "v_accvgpr_read_b32 v119, a153\n"
      "v_pk_mul_f32 v[118:119], v[118:119], v[114:115] op_sel:[0,0] op_sel_hi:[1,0]\n"
      "v_accvgpr_write_b32 a152, v118\n"
      "v_accvgpr_write_b32 a153, v119\n"
      "v_accvgpr_read_b32 v118, a154\n"
      "v_accvgpr_read_b32 v119, a155\n"
      "v_pk_mul_f32 v[118:119], v[118:119], v[114:115] op_sel:[0,0] op_sel_hi:[1,0]\n"
      "v_accvgpr_write_b32 a154, v118\n"
      "v_accvgpr_write_b32 a155, v119\n"
      "v_accvgpr_read_b32 v118, a156\n"
      "v_accvgpr_read_b32 v119, a157\n"
      "v_pk_mul_f32 v[118:119], v[118:119], v[114:115] op_sel:[0,1] op_sel_hi:[1,1]\n"
      "v_accvgpr_write_b32 a156, v118\n"
      "v_accvgpr_write_b32 a157, v119\n"
      "v_accvgpr_read_b32 v118, a158\n"
      "v_accvgpr_read_b32 v119, a159\n"
      "v_pk_mul_f32 v[118:119], v[118:119], v[114:115] op_sel:[0,1] op_sel_hi:[1,1]\n"
      "v_accvgpr_write_b32 a158, v118\n"
      "v_accvgpr_write_b32 a159, v119\n"
      "v_accvgpr_read_b32 v118, a160\n"
      "v_accvgpr_read_b32 v119, a161\n"
      "v_pk_mul_f32 v[118:119], v[118:119], v[112:113] op_sel:[0,0] op_sel_hi:[1,0]\n"
      "v_accvgpr_write_b32 a160, v118\n"
      "v_accvgpr_write_b32 a161, v119\n"
      "v_accvgpr_read_b32 v118, a162\n"
      "v_accvgpr_read_b32 v119, a163\n"
      "v_pk_mul_f32 v[118:119], v[118:119], v[112:113] op_sel:[0,0] op_sel_hi:[1,0]\n"
      "v_accvgpr_write_b32 a162, v118\n"
      "v_accvgpr_write_b32 a163, v119\n"
      "v_accvgpr_read_b32 v118, a164\n"
      "v_accvgpr_read_b32 v119, a165\n"
      "v_pk_mul_f32 v[118:119], v[118:119], v[112:113] op_sel:[0,1] op_sel_hi:[1,1]\n"
      "v_accvgpr_write_b32 a164, v118\n"
      "v_accvgpr_write_b32 a165, v119\n"
      "v_accvgpr_read_b32 v118, a166\n"
      "v_accvgpr_read_b32 v119, a167\n"
      "v_pk_mul_f32 v[118:119], v[118:119], v[112:113] op_sel:[0,1] op_sel_hi:[1,1]\n"
      "v_accvgpr_write_b32 a166, v118\n"
      "v_accvgpr_write_b32 a167, v119\n"
      "v_accvgpr_read_b32 v118, a168\n"
      "v_accvgpr_read_b32 v119, a169\n"
      "v_pk_mul_f32 v[118:119], v[118:119], v[114:115] op_sel:[0,0] op_sel_hi:[1,0]\n"
      "v_accvgpr_write_b32 a168, v118\n"
      "v_accvgpr_write_b32 a169, v119\n"
      "v_accvgpr_read_b32 v118, a170\n"
      "v_accvgpr_read_b32 v119, a171\n"
      "v_pk_mul_f32 v[118:119], v[118:119], v[114:115] op_sel:[0,0] op_sel_hi:[1,0]\n"
      "v_accvgpr_write_b32 a170, v118\n"
      "v_accvgpr_write_b32 a171, v119\n"
      "v_accvgpr_read_b32 v118, a172\n"
      "v_accvgpr_read_b32 v119, a173\n"
      "v_pk_mul_f32 v[118:119], v[118:119], v[114:115] op_sel:[0,1] op_sel_hi:[1,1]\n"
      "v_accvgpr_write_b32 a172, v118\n"
      "v_accvgpr_write_b32 a173, v119\n"
      "v_accvgpr_read_b32 v118, a174\n"
      "v_accvgpr_read_b32 v119, a175\n"
      "v_pk_mul_f32 v[118:119], v[118:119], v[114:115] op_sel:[0,1] op_sel_hi:[1,1]\n"
      "v_accvgpr_write_b32 a174, v118\n"
      "v_accvgpr_write_b32 a175, v119\n"
      "v_accvgpr_read_b32 v118, a176\n"
      "v_accvgpr_read_b32 v119, a177\n"
      "v_pk_mul_f32 v[118:119], v[118:119], v[112:113] op_sel:[0,0] op_sel_hi:[1,0]\n"
      "v_accvgpr_write_b32 a176, v118\n"
      "v_accvgpr_write_b32 a177, v119\n"
      "v_accvgpr_read_b32 v118, a178\n"
      "v_accvgpr_read_b32 v119, a179\n"
      "v_pk_mul_f32 v[118:119], v[118:119], v[112:113] op_sel:[0,0] op_sel_hi:[1,0]\n"
      "v_accvgpr_write_b32 a178, v118\n"
      "v_accvgpr_write_b32 a179, v119\n"
      "v_accvgpr_read_b32 v118, a180\n"
      "v_accvgpr_read_b32 v119, a181\n"
      "v_pk_mul_f32 v[118:119], v[118:119], v[112:113] op_sel:[0,1] op_sel_hi:[1,1]\n"
      "v_accvgpr_write_b32 a180, v118\n"
      "v_accvgpr_write_b32 a181, v119\n"
      "v_accvgpr_read_b32 v118, a182\n"
      "v_accvgpr_read_b32 v119, a183\n"
      "v_pk_mul_f32 v[118:119], v[118:119], v[112:113] op_sel:[0,1] op_sel_hi:[1,1]\n"
      "v_accvgpr_write_b32 a182, v118\n"
      "v_accvgpr_write_b32 a183, v119\n"
      "v_accvgpr_read_b32 v118, a184\n"
      "v_accvgpr_read_b32 v119, a185\n"
      "v_pk_mul_f32 v[118:119], v[118:119], v[114:115] op_sel:[0,0] op_sel_hi:[1,0]\n"
      "v_accvgpr_write_b32 a184, v118\n"
      "v_accvgpr_write_b32 a185, v119\n"
      "v_accvgpr_read_b32 v118, a186\n"
      "v_accvgpr_read_b32 v119, a187\n"
      "v_pk_mul_f32 v[118:119], v[118:119], v[114:115] op_sel:[0,0] op_sel_hi:[1,0]\n"
      "v_accvgpr_write_b32 a186, v118\n"
      "v_accvgpr_write_b32 a187, v119\n"
      "v_accvgpr_read_b32 v118, a188\n"
      "v_accvgpr_read_b32 v119, a189\n"
      "v_pk_mul_f32 v[118:119], v[118:119], v[114:115] op_sel:[0,1] op_sel_hi:[1,1]\n"
      "v_accvgpr_write_b32 a188, v118\n"
      "v_accvgpr_write_b32 a189, v119\n"
      "v_accvgpr_read_b32 v118, a190\n"
      "v_accvgpr_read_b32 v119, a191\n"
      "v_pk_mul_f32 v[118:119], v[118:119], v[114:115] op_sel:[0,1] op_sel_hi:[1,1]\n"
      "v_accvgpr_write_b32 a190, v118\n"
      "v_accvgpr_write_b32 a191, v119\n"
      "v_mov_b32 v118, v112\n"
      "s_nop 1\n"
      "v_mov_b32_dpp v118, v113 quad_perm:[0,1,2,3] row_mask:0x2 bank_mask:0xf\n"
      "s_nop 1\n"
      "v_mov_b32_dpp v118, v114 quad_perm:[0,1,2,3] row_mask:0x4 bank_mask:0xf\n"
      "s_nop 1\n"
      "v_mov_b32_dpp v118, v115 quad_perm:[0,1,2,3] row_mask:0x8 bank_mask:0xf\n"
      "v_accvgpr_read_b32 v119, a192\n"
      "v_mul_f32 v118, v118, v119\n"
      "v_accvgpr_write_b32 a192, v118\n"
      "v_lshlrev_b32 v118, 16, v208\n"
      "v_and_b32 v119, 0xffff0000, v208\n"
      "v_pk_mul_f32 v[118:119], v[118:119], v[112:113] op_sel:[0,0] op_sel_hi:[1,0]\n"
      "v_cvt_pk_bf16_f32 v208, v118, v119\n"
      "v_lshlrev_b32 v118, 16, v209\n"
      "v_and_b32 v119, 0xffff0000, v209\n"
      "v_pk_mul_f32 v[118:119], v[118:119], v[112:113] op_sel:[0,0] op_sel_hi:[1,0]\n"
      "v_cvt_pk_bf16_f32 v209, v118, v119\n"
      "v_lshlrev_b32 v118, 16, v210\n"
      "v_and_b32 v119, 0xffff0000, v210\n"
      "v_pk_mul_f32 v[118:119], v[118:119], v[112:113] op_sel:[0,0] op_sel_hi:[1,0]\n"
      "v_cvt_pk_bf16_f32 v210, v118, v119\n"
      "v_lshlrev_b32 v118, 16, v211\n"
      "v_and_b32 v119, 0xffff0000, v211\n"
      "v_pk_mul_f32 v[118:119], v[118:119], v[112:113] op_sel:[0,0] op_sel_hi:[1,0]\n"
      "v_cvt_pk_bf16_f32 v211, v118, v119\n"
      "v_lshlrev_b32 v118, 16, v212\n"
      "v_and_b32 v119, 0xffff0000, v212\n"
      "v_pk_mul_f32 v[118:119], v[118:119], v[112:113] op_sel:[0,1] op_sel_hi:[1,1]\n"
      "v_cvt_pk_bf16_f32 v212, v118, v119\n"
      "v_lshlrev_b32 v118, 16, v213\n"
      "v_and_b32 v119, 0xffff0000, v213\n"
      "v_pk_mul_f32 v[118:119], v[118:119], v[112:113] op_sel:[0,1] op_sel_hi:[1,1]\n"
      "v_cvt_pk_bf16_f32 v213, v118, v119\n"
      "v_lshlrev_b32 v118, 16, v214\n"
      "v_and_b32 v119, 0xffff0000, v214\n"
      "v_pk_mul_f32 v[118:119], v[118:119], v[112:113] op_sel:[0,1] op_sel_hi:[1,1]\n"
      "v_cvt_pk_bf16_f32 v214, v118, v119\n"
      "v_lshlrev_b32 v118, 16, v215\n"
      "v_and_b32 v119, 0xffff0000, v215\n"
      "v_pk_mul_f32 v[118:119], v[118:119], v[112:113] op_sel:[0,1] op_sel_hi:[1,1]\n"
      "v_cvt_pk_bf16_f32 v215, v118, v119\n"
      "v_lshlrev_b32 v118, 16, v216\n"
      "v_and_b32 v119, 0xffff0000, v216\n"
      "v_pk_mul_f32 v[118:119], v[118:119], v[114:115] op_sel:[0,0] op_sel_hi:[1,0]\n"
      "v_cvt_pk_bf16_f32 v216, v118, v119\n"
      "v_lshlrev_b32 v118, 16, v217\n"
      "v_and_b32 v119, 0xffff0000, v217\n"
      "v_pk_mul_f32 v[118:119], v[118:119], v[114:115] op_sel:[0,0] op_sel_hi:[1,0]\n"
      "v_cvt_pk_bf16_f32 v217, v118, v119\n"
      "v_lshlrev_b32 v118, 16, v218\n"
      "v_and_b32 v119, 0xffff0000, v218\n"
      "v_pk_mul_f32 v[118:119], v[118:119], v[114:115] op_sel:[0,0] op_sel_hi:[1,0]\n"
      "v_cvt_pk_bf16_f32 v218, v118, v119\n"
      "v_lshlrev_b32 v118, 16, v219\n"
      "v_and_b32 v119, 0xffff0000, v219\n"
      "v_pk_mul_f32 v[118:119], v[118:119], v[114:115] op_sel:[0,0] op_sel_hi:[1,0]\n"
      "v_cvt_pk_bf16_f32 v219, v118, v119\n"
      "v_lshlrev_b32 v118, 16, v220\n"
      "v_and_b32 v119, 0xffff0000, v220\n"
      "v_pk_mul_f32 v[118:119], v[118:119], v[114:115] op_sel:[0,1] op_sel_hi:[1,1]\n"
      "v_cvt_pk_bf16_f32 v220, v118, v119\n"
      "v_lshlrev_b32 v118, 16, v221\n"
      "v_and_b32 v119, 0xffff0000, v221\n"
      "v_pk_mul_f32 v[118:119], v[118:119], v[114:115] op_sel:[0,1] op_sel_hi:[1,1]\n"
      "v_cvt_pk_bf16_f32 v221, v118, v119\n"
      "v_lshlrev_b32 v118, 16, v222\n"
      "v_and_b32 v119, 0xffff0000, v222\n"
      "v_pk_mul_f32 v[118:119], v[118:119], v[114:115] op_sel:[0,1] op_sel_hi:[1,1]\n"
      "v_cvt_pk_bf16_f32 v222, v118, v119\n"
      "v_lshlrev_b32 v118, 16, v223\n"
      "v_and_b32 v119, 0xffff0000, v223\n"
      "v_pk_mul_f32 v[118:119], v[118:119], v[114:115] op_sel:[0,1] op_sel_hi:[1,1]\n"
      "v_cvt_pk_bf16_f32 v223, v118, v119\n"
      "v_lshlrev_b32 v118, 16, v192\n"
      "v_and_b32 v119, 0xffff0000, v192\n"
      "v_pk_mul_f32 v[118:119], v[118:119], v[112:113] op_sel:[0,0] op_sel_hi:[1,0]\n"
      "v_cvt_pk_bf16_f32 v192, v118, v119\n"
      "v_lshlrev_b32 v118, 16, v193\n"
      "v_and_b32 v119, 0xffff0000, v193\n"
      "v_pk_mul_f32 v[118:119], v[118:119], v[112:113] op_sel:[0,0] op_sel_hi:[1,0]\n"
      "v_cvt_pk_bf16_f32 v193, v118, v119\n"
      "v_lshlrev_b32 v118, 16, v194\n"
      "v_and_b32 v119, 0xffff0000, v194\n"
      "v_pk_mul_f32 v[118:119], v[118:119], v[112:113] op_sel:[0,0] op_sel_hi:[1,0]\n"
      "v_cvt_pk_bf16_f32 v194, v118, v119\n"
      "v_lshlrev_b32 v118, 16, v195\n"
      "v_and_b32 v119, 0xffff0000, v195\n"
      "v_pk_mul_f32 v[118:119], v[118:119], v[112:113] op_sel:[0,0] op_sel_hi:[1,0]\n"
      "v_cvt_pk_bf16_f32 v195, v118, v119\n"
      "v_lshlrev_b32 v118, 16, v196\n"
      "v_and_b32 v119, 0xffff0000, v196\n"
      "v_pk_mul_f32 v[118:119], v[118:119], v[112:113] op_sel:[0,1] op_sel_hi:[1,1]\n"
      "v_cvt_pk_bf16_f32 v196, v118, v119\n"
      "v_lshlrev_b32 v118, 16, v197\n"
      "v_and_b32 v119, 0xffff0000, v197\n"
      "v_pk_mul_f32 v[118:119], v[118:119], v[112:113] op_sel:[0,1] op_sel_hi:[1,1]\n"
      "v_cvt_pk_bf16_f32 v197, v118, v119\n"
      "v_lshlrev_b32 v118, 16, v198\n"
      "v_and_b32 v119, 0xffff0000, v198\n"
      "v_pk_mul_f32 v[118:119], v[118:119], v[112:113] op_sel:[0,1] op_sel_hi:[1,1]\n"
      "v_cvt_pk_bf16_f32 v198, v118, v119\n"
      "v_lshlrev_b32 v118, 16, v199\n"
      "v_and_b32 v119, 0xffff0000, v199\n"
      "v_pk_mul_f32 v[118:119], v[118:119], v[112:113] op_sel:[0,1] op_sel_hi:[1,1]\n"
      "v_cvt_pk_bf16_f32 v199, v118, v119\n"
      "v_lshlrev_b32 v118, 16, v200\n"
      "v_and_b32 v119, 0xffff0000, v200\n"
      "v_pk_mul_f32 v[118:119], v[118:119], v[114:115] op_sel:[0,0] op_sel_hi:[1,0]\n"
      "v_cvt_pk_bf16_f32 v200, v118, v119\n"
      "v_lshlrev_b32 v118, 16, v201\n"
      "v_and_b32 v119, 0xffff0000, v201\n"
      "v_pk_mul_f32 v[118:119], v[118:119], v[114:115] op_sel:[0,0] op_sel_hi:[1,0]\n"
      "v_cvt_pk_bf16_f32 v201, v118, v119\n"
      "v_lshlrev_b32 v118, 16, v202\n"
      "v_and_b32 v119, 0xffff0000, v202\n"
      "v_pk_mul_f32 v[118:119], v[118:119], v[114:115] op_sel:[0,0] op_sel_hi:[1,0]\n"
      "v_cvt_pk_bf16_f32 v202, v118, v119\n"
      "v_lshlrev_b32 v118, 16, v203\n"
      "v_and_b32 v119, 0xffff0000, v203\n"
      "v_pk_mul_f32 v[118:119], v[118:119], v[114:115] op_sel:[0,0] op_sel_hi:[1,0]\n"
      "v_cvt_pk_bf16_f32 v203, v118, v119\n"
      "v_lshlrev_b32 v118, 16, v204\n"
      "v_and_b32 v119, 0xffff0000, v204\n"
      "v_pk_mul_f32 v[118:119], v[118:119], v[114:115] op_sel:[0,1] op_sel_hi:[1,1]\n"
      "v_cvt_pk_bf16_f32 v204, v118, v119\n"
      "v_lshlrev_b32 v118, 16, v205\n"
      "v_and_b32 v119, 0xffff0000, v205\n"
      "v_pk_mul_f32 v[118:119], v[118:119], v[114:115] op_sel:[0,1] op_sel_hi:[1,1]\n"
      "v_cvt_pk_bf16_f32 v205, v118, v119\n"
      "v_lshlrev_b32 v118, 16, v206\n"
      "v_and_b32 v119, 0xffff0000, v206\n"
      "v_pk_mul_f32 v[118:119], v[118:119], v[114:115] op_sel:[0,1] op_sel_hi:[1,1]\n"
      "v_cvt_pk_bf16_f32 v206, v118, v119\n"
      "v_lshlrev_b32 v118, 16, v207\n"
      "v_and_b32 v119, 0xffff0000, v207\n"
      "v_pk_mul_f32 v[118:119], v[118:119], v[114:115] op_sel:[0,1] op_sel_hi:[1,1]\n"
      "v_cvt_pk_bf16_f32 v207, v118, v119\n"
      "v_lshlrev_b32 v118, 16, v0\n"
      "v_and_b32 v119, 0xffff0000, v0\n"
      "v_pk_mul_f32 v[118:119], v[118:119], v[112:113] op_sel:[0,0] op_sel_hi:[1,0]\n"
      "v_cvt_pk_bf16_f32 v0, v118, v119\n"
      "v_lshlrev_b32 v118, 16, v1\n"
      "v_and_b32 v119, 0xffff0000, v1\n"
      "v_pk_mul_f32 v[118:119], v[118:119], v[112:113] op_sel:[0,0] op_sel_hi:[1,0]\n"
      "v_cvt_pk_bf16_f32 v1, v118, v119\n"
      "v_lshlrev_b32 v118, 16, v2\n"
      "v_and_b32 v119, 0xffff0000, v2\n"
      "v_pk_mul_f32 v[118:119], v[118:119], v[112:113] op_sel:[0,0] op_sel_hi:[1,0]\n"
      "v_cvt_pk_bf16_f32 v2, v118, v119\n"
      "v_lshlrev_b32 v118, 16, v3\n"
      "v_and_b32 v119, 0xffff0000, v3\n"
      "v_pk_mul_f32 v[118:119], v[118:119], v[112:113] op_sel:[0,0] op_sel_hi:[1,0]\n"
      "v_cvt_pk_bf16_f32 v3, v118, v119\n"
      "v_lshlrev_b32 v118, 16, v4\n"
      "v_and_b32 v119, 0xffff0000, v4\n"
      "v_pk_mul_f32 v[118:119], v[118:119], v[112:113] op_sel:[0,1] op_sel_hi:[1,1]\n"
      "v_cvt_pk_bf16_f32 v4, v118, v119\n"
      "v_lshlrev_b32 v118, 16, v5\n"
      "v_and_b32 v119, 0xffff0000, v5\n"
      "v_pk_mul_f32 v[118:119], v[118:119], v[112:113] op_sel:[0,1] op_sel_hi:[1,1]\n"
      "v_cvt_pk_bf16_f32 v5, v118, v119\n"
      "v_lshlrev_b32 v118, 16, v6\n"
      "v_and_b32 v119, 0xffff0000, v6\n"
      "v_pk_mul_f32 v[118:119], v[118:119], v[112:113] op_sel:[0,1] op_sel_hi:[1,1]\n"
      "v_cvt_pk_bf16_f32 v6, v118, v119\n"
      "v_lshlrev_b32 v118, 16, v7\n"
      "v_and_b32 v119, 0xffff0000, v7\n"
      "v_pk_mul_f32 v[118:119], v[118:119], v[112:113] op_sel:[0,1] op_sel_hi:[1,1]\n"
      "v_cvt_pk_bf16_f32 v7, v118, v119\n"
      "v_lshlrev_b32 v118, 16, v8\n"
      "v_and_b32 v119, 0xffff0000, v8\n"
      "v_pk_mul_f32 v[118:119], v[118:119], v[114:115] op_sel:[0,0] op_sel_hi:[1,0]\n"
      "v_cvt_pk_bf16_f32 v8, v118, v119\n"
      "v_lshlrev_b32 v118, 16, v9\n"
      "v_and_b32 v119, 0xffff0000, v9\n"
      "v_pk_mul_f32 v[118:119], v[118:119], v[114:115] op_sel:[0,0] op_sel_hi:[1,0]\n"
      "v_cvt_pk_bf16_f32 v9, v118, v119\n"
      "v_lshlrev_b32 v118, 16, v10\n"
      "v_and_b32 v119, 0xffff0000, v10\n"
      "v_pk_mul_f32 v[118:119], v[118:119], v[114:115] op_sel:[0,0] op_sel_hi:[1,0]\n"
      "v_cvt_pk_bf16_f32 v10, v118, v119\n"
      "v_lshlrev_b32 v118, 16, v11\n"
      "v_and_b32 v119, 0xffff0000, v11\n"
      "v_pk_mul_f32 v[118:119], v[118:119], v[114:115] op_sel:[0,0] op_sel_hi:[1,0]\n"
      "v_cvt_pk_bf16_f32 v11, v118, v119\n"
      "v_lshlrev_b32 v118, 16, v12\n"
      "v_and_b32 v119, 0xffff0000, v12\n"
      "v_pk_mul_f32 v[118:119], v[118:119], v[114:115] op_sel:[0,1] op_sel_hi:[1,1]\n"
      "v_cvt_pk_bf16_f32 v12, v118, v119\n"
      "v_lshlrev_b32 v118, 16, v13\n"
      "v_and_b32 v119, 0xffff0000, v13\n"
      "v_pk_mul_f32 v[118:119], v[118:119], v[114:115] op_sel:[0,1] op_sel_hi:[1,1]\n"
      "v_cvt_pk_bf16_f32 v13, v118, v119\n"
      "v_lshlrev_b32 v118, 16, v14\n"
      "v_and_b32 v119, 0xffff0000, v14\n"
      "v_pk_mul_f32 v[118:119], v[118:119], v[114:115] op_sel:[0,1] op_sel_hi:[1,1]\n"
      "v_cvt_pk_bf16_f32 v14, v118, v119\n"
      "v_lshlrev_b32 v118, 16, v15\n"
      "v_and_b32 v119, 0xffff0000, v15\n"
      "v_pk_mul_f32 v[118:119], v[118:119], v[114:115] op_sel:[0,1] op_sel_hi:[1,1]\n"
      "v_cvt_pk_bf16_f32 v15, v118, v119\n"
      "v_lshlrev_b32 v118, 16, v16\n"
      "v_and_b32 v119, 0xffff0000, v16\n"
      "v_pk_mul_f32 v[118:119], v[118:119], v[112:113] op_sel:[0,0] op_sel_hi:[1,0]\n"
      "v_cvt_pk_bf16_f32 v16, v118, v119\n"
      "v_lshlrev_b32 v118, 16, v17\n"
      "v_and_b32 v119, 0xffff0000, v17\n"
      "v_pk_mul_f32 v[118:119], v[118:119], v[112:113] op_sel:[0,0] op_sel_hi:[1,0]\n"
      "v_cvt_pk_bf16_f32 v17, v118, v119\n"
      "v_lshlrev_b32 v118, 16, v18\n"
      "v_and_b32 v119, 0xffff0000, v18\n"
      "v_pk_mul_f32 v[118:119], v[118:119], v[112:113] op_sel:[0,0] op_sel_hi:[1,0]\n"
      "v_cvt_pk_bf16_f32 v18, v118, v119\n"
      "v_lshlrev_b32 v118, 16, v19\n"
      "v_and_b32 v119, 0xffff0000, v19\n"
      "v_pk_mul_f32 v[118:119], v[118:119], v[112:113] op_sel:[0,0] op_sel_hi:[1,0]\n"
      "v_cvt_pk_bf16_f32 v19, v118, v119\n"
      "v_lshlrev_b32 v118, 16, v20\n"
      "v_and_b32 v119, 0xffff0000, v20\n"
      "v_pk_mul_f32 v[118:119], v[118:119], v[112:113] op_sel:[0,1] op_sel_hi:[1,1]\n"
      "v_cvt_pk_bf16_f32 v20, v118, v119\n"
      "v_lshlrev_b32 v118, 16, v21\n"
      "v_and_b32 v119, 0xffff0000, v21\n"
      "v_pk_mul_f32 v[118:119], v[118:119], v[112:113] op_sel:[0,1] op_sel_hi:[1,1]\n"
      "v_cvt_pk_bf16_f32 v21, v118, v119\n"
      "v_lshlrev_b32 v118, 16, v22\n"
      "v_and_b32 v119, 0xffff0000, v22\n"
      "v_pk_mul_f32 v[118:119], v[118:119], v[112:113] op_sel:[0,1] op_sel_hi:[1,1]\n"
      "v_cvt_pk_bf16_f32 v22, v118, v119\n"
      "v_lshlrev_b32 v118, 16, v23\n"
      "v_and_b32 v119, 0xffff0000, v23\n"
      "v_pk_mul_f32 v[118:119], v[118:119], v[112:113] op_sel:[0,1] op_sel_hi:[1,1]\n"
      "v_cvt_pk_bf16_f32 v23, v118, v119\n"
      "v_lshlrev_b32 v118, 16, v24\n"
      "v_and_b32 v119, 0xffff0000, v24\n"
      "v_pk_mul_f32 v[118:119], v[118:119], v[114:115] op_sel:[0,0] op_sel_hi:[1,0]\n"
      "v_cvt_pk_bf16_f32 v24, v118, v119\n"
      "v_lshlrev_b32 v118, 16, v25\n"
      "v_and_b32 v119, 0xffff0000, v25\n"
      "v_pk_mul_f32 v[118:119], v[118:119], v[114:115] op_sel:[0,0] op_sel_hi:[1,0]\n"
      "v_cvt_pk_bf16_f32 v25, v118, v119\n"
      "v_lshlrev_b32 v118, 16, v26\n"
      "v_and_b32 v119, 0xffff0000, v26\n"
      "v_pk_mul_f32 v[118:119], v[118:119], v[114:115] op_sel:[0,0] op_sel_hi:[1,0]\n"
      "v_cvt_pk_bf16_f32 v26, v118, v119\n"
      "v_lshlrev_b32 v118, 16, v27\n"
      "v_and_b32 v119, 0xffff0000, v27\n"
      "v_pk_mul_f32 v[118:119], v[118:119], v[114:115] op_sel:[0,0] op_sel_hi:[1,0]\n"
      "v_cvt_pk_bf16_f32 v27, v118, v119\n"
      "v_lshlrev_b32 v118, 16, v28\n"
      "v_and_b32 v119, 0xffff0000, v28\n"
      "v_pk_mul_f32 v[118:119], v[118:119], v[114:115] op_sel:[0,1] op_sel_hi:[1,1]\n"
      "v_cvt_pk_bf16_f32 v28, v118, v119\n"
      "v_lshlrev_b32 v118, 16, v29\n"
      "v_and_b32 v119, 0xffff0000, v29\n"
      "v_pk_mul_f32 v[118:119], v[118:119], v[114:115] op_sel:[0,1] op_sel_hi:[1,1]\n"
      "v_cvt_pk_bf16_f32 v29, v118, v119\n"
      "v_lshlrev_b32 v118, 16, v30\n"
      "v_and_b32 v119, 0xffff0000, v30\n"
      "v_pk_mul_f32 v[118:119], v[118:119], v[114:115] op_sel:[0,1] op_sel_hi:[1,1]\n"
      "v_cvt_pk_bf16_f32 v30, v118, v119\n"
      "v_lshlrev_b32 v118, 16, v31\n"
      "v_and_b32 v119, 0xffff0000, v31\n"
      "v_pk_mul_f32 v[118:119], v[118:119], v[114:115] op_sel:[0,1] op_sel_hi:[1,1]\n"
      "v_cvt_pk_bf16_f32 v31, v118, v119\n"
      "v_lshlrev_b32 v118, 16, v96\n"
      "v_and_b32 v119, 0xffff0000, v96\n"
      "v_pk_mul_f32 v[118:119], v[118:119], v[112:113] op_sel:[0,0] op_sel_hi:[1,0]\n"
      "v_cvt_pk_bf16_f32 v96, v118, v119\n"
      "v_lshlrev_b32 v118, 16, v97\n"
      "v_and_b32 v119, 0xffff0000, v97\n"
      "v_pk_mul_f32 v[118:119], v[118:119], v[112:113] op_sel:[0,0] op_sel_hi:[1,0]\n"
      "v_cvt_pk_bf16_f32 v97, v118, v119\n"
      "v_lshlrev_b32 v118, 16, v98\n"
      "v_and_b32 v119, 0xffff0000, v98\n"
      "v_pk_mul_f32 v[118:119], v[118:119], v[112:113] op_sel:[0,0] op_sel_hi:[1,0]\n"
      "v_cvt_pk_bf16_f32 v98, v118, v119\n"
      "v_lshlrev_b32 v118, 16, v99\n"
      "v_and_b32 v119, 0xffff0000, v99\n"
      "v_pk_mul_f32 v[118:119], v[118:119], v[112:113] op_sel:[0,0] op_sel_hi:[1,0]\n"
      "v_cvt_pk_bf16_f32 v99, v118, v119\n"
      "v_lshlrev_b32 v118, 16, v100\n"
      "v_and_b32 v119, 0xffff0000, v100\n"
      "v_pk_mul_f32 v[118:119], v[118:119], v[112:113] op_sel:[0,1] op_sel_hi:[1,1]\n"
      "v_cvt_pk_bf16_f32 v100, v118, v119\n"
      "v_lshlrev_b32 v118, 16, v101\n"
      "v_and_b32 v119, 0xffff0000, v101\n"
      "v_pk_mul_f32 v[118:119], v[118:119], v[112:113] op_sel:[0,1] op_sel_hi:[1,1]\n"
      "v_cvt_pk_bf16_f32 v101, v118, v119\n"
      "v_lshlrev_b32 v118, 16, v102\n"
      "v_and_b32 v119, 0xffff0000, v102\n"
      "v_pk_mul_f32 v[118:119], v[118:119], v[112:113] op_sel:[0,1] op_sel_hi:[1,1]\n"
      "v_cvt_pk_bf16_f32 v102, v118, v119\n"
      "v_lshlrev_b32 v118, 16, v103\n"
      "v_and_b32 v119, 0xffff0000, v103\n"
      "v_pk_mul_f32 v[118:119], v[118:119], v[112:113] op_sel:[0,1] op_sel_hi:[1,1]\n"
      "v_cvt_pk_bf16_f32 v103, v118, v119\n"
      "v_lshlrev_b32 v118, 16, v104\n"
      "v_and_b32 v119, 0xffff0000, v104\n"
      "v_pk_mul_f32 v[118:119], v[118:119], v[114:115] op_sel:[0,0] op_sel_hi:[1,0]\n"
      "v_cvt_pk_bf16_f32 v104, v118, v119\n"
      "v_lshlrev_b32 v118, 16, v105\n"
      "v_and_b32 v119, 0xffff0000, v105\n"
      "v_pk_mul_f32 v[118:119], v[118:119], v[114:115] op_sel:[0,0] op_sel_hi:[1,0]\n"
      "v_cvt_pk_bf16_f32 v105, v118, v119\n"
      "v_lshlrev_b32 v118, 16, v106\n"
      "v_and_b32 v119, 0xffff0000, v106\n"
      "v_pk_mul_f32 v[118:119], v[118:119], v[114:115] op_sel:[0,0] op_sel_hi:[1,0]\n"
      "v_cvt_pk_bf16_f32 v106, v118, v119\n"
      "v_lshlrev_b32 v118, 16, v107\n"
      "v_and_b32 v119, 0xffff0000, v107\n"
      "v_pk_mul_f32 v[118:119], v[118:119], v[114:115] op_sel:[0,0] op_sel_hi:[1,0]\n"
      "v_cvt_pk_bf16_f32 v107, v118, v119\n"
      "v_lshlrev_b32 v118, 16, v108\n"
      "v_and_b32 v119, 0xffff0000, v108\n"
      "v_pk_mul_f32 v[118:119], v[118:119], v[114:115] op_sel:[0,1] op_sel_hi:[1,1]\n"
      "v_cvt_pk_bf16_f32 v108, v118, v119\n"
      "v_lshlrev_b32 v118, 16, v109\n"
      "v_and_b32 v119, 0xffff0000, v109\n"
      "v_pk_mul_f32 v[118:119], v[118:119], v[114:115] op_sel:[0,1] op_sel_hi:[1,1]\n"
      "v_cvt_pk_bf16_f32 v109, v118, v119\n"
      "v_lshlrev_b32 v118, 16, v110\n"
      "v_and_b32 v119, 0xffff0000, v110\n"
      "v_pk_mul_f32 v[118:119], v[118:119], v[114:115] op_sel:[0,1] op_sel_hi:[1,1]\n"
      "v_cvt_pk_bf16_f32 v110, v118, v119\n"
      "v_lshlrev_b32 v118, 16, v111\n"
      "v_and_b32 v119, 0xffff0000, v111\n"
      "v_pk_mul_f32 v[118:119], v[118:119], v[114:115] op_sel:[0,1] op_sel_hi:[1,1]\n"
      "v_cvt_pk_bf16_f32 v111, v118, v119\n"
      "s_setpc_b64 s[84:85]\n"
      "MIS_%=:\n"
      "s_waitcnt lgkmcnt(0)\n"
      "v_sub_f32 v148, v153, v117\n"
      "v_min_f32 v149, 0, -v148\n"
      "v_min_f32 v148, 0, v148\n"
      "v_exp_f32 v149, v149\n"
      "v_exp_f32 v148, v148\n"
      "v_min_f32 v117, v117, v153\n"
      "v_mov_b32 v112, v148\n"
      "v_mov_b32 v228, v149\n"
      "v_mov_b32 v114, v112\n"
      "v_mov_b32 v230, v228\n"
      "s_nop 0\n"
      "v_permlane32_swap_b32 v112, v114\n"
      "v_permlane32_swap_b32 v228, v230\n"
      "v_mov_b32 v113, v112\n"
      "v_mov_b32 v229, v228\n"
      "v_mov_b32 v115, v114\n"
      "v_mov_b32 v231, v230\n"
      "v_permlane16_swap_b32 v112, v113\n"
      "v_permlane16_swap_b32 v228, v229\n"
      "v_permlane16_swap_b32 v114, v115\n"
      "v_permlane16_swap_b32 v230, v231\n"
      "s_call_b64 s[84:85], SCALE_%=\n"
      "v_pk_mul_f32 v[160:161], v[160:161], v[228:229] op_sel:[0,0] op_sel_hi:[1,0]\n"
      "v_pk_mul_f32 v[162:163], v[162:163], v[228:229] op_sel:[0,0] op_sel_hi:[1,0]\n"
      "v_pk_mul_f32 v[164:165], v[164:165], v[228:229] op_sel:[0,1] op_sel_hi:[1,1]\n"
      "v_pk_mul_f32 v[166:167], v[166:167], v[228:229] op_sel:[0,1] op_sel_hi:[1,1]\n"
      "v_pk_mul_f32 v[168:169], v[168:169], v[230:231] op_sel:[0,0] op_sel_hi:[1,0]\n"
      "v_pk_mul_f32 v[170:171], v[170:171], v[230:231] op_sel:[0,0] op_sel_hi:[1,0]\n"
      "v_pk_mul_f32 v[172:173], v[172:173], v[230:231] op_sel:[0,1] op_sel_hi:[1,1]\n"
      "v_pk_mul_f32 v[174:175], v[174:175], v[230:231] op_sel:[0,1] op_sel_hi:[1,1]\n"
      "v_pk_mul_f32 v[176:177], v[176:177], v[228:229] op_sel:[0,0] op_sel_hi:[1,0]\n"
      "v_pk_mul_f32 v[178:179], v[178:179], v[228:229] op_sel:[0,0] op_sel_hi:[1,0]\n"
      "v_pk_mul_f32 v[180:181], v[180:181], v[228:229] op_sel:[0,1] op_sel_hi:[1,1]\n"
      "v_pk_mul_f32 v[182:183], v[182:183], v[228:229] op_sel:[0,1] op_sel_hi:[1,1]\n"
      "v_pk_mul_f32 v[184:185], v[184:185], v[230:231] op_sel:[0,0] op_sel_hi:[1,0]\n"
      "v_pk_mul_f32 v[186:187], v[186:187], v[230:231] op_sel:[0,0] op_sel_hi:[1,0]\n"
      "v_pk_mul_f32 v[188:189], v[188:189], v[230:231] op_sel:[0,1] op_sel_hi:[1,1]\n"
      "v_pk_mul_f32 v[190:191], v[190:191], v[230:231] op_sel:[0,1] op_sel_hi:[1,1]\n"
      "v_mov_b32 v112, v228\n"
      "v_mov_b32 v113, v229\n"
      "v_mov_b32 v114, v230\n"
      "v_mov_b32 v115, v231\n"
      "v_accvgpr_read_b32 v128, a64\n"
      "v_accvgpr_read_b32 v129, a65\n"
      "v_accvgpr_read_b32 v130, a66\n"
      "v_accvgpr_read_b32 v131, a67\n"
      "v_accvgpr_read_b32 v132, a68\n"
      "v_accvgpr_read_b32 v133, a69\n"
      "v_accvgpr_read_b32 v134, a70\n"
      "v_accvgpr_read_b32 v135, a71\n"
      "v_accvgpr_read_b32 v136, a72\n"
      "v_accvgpr_read_b32 v137, a73\n"
      "v_accvgpr_read_b32 v138, a74\n"
      "v_accvgpr_read_b32 v139, a75\n"
      "v_accvgpr_read_b32 v140, a76\n"
      "v_accvgpr_read_b32 v141, a77\n"
      "v_accvgpr_read_b32 v142, a78\n"
      "v_accvgpr_read_b32 v143, a79\n"
      "v_lshlrev_b32 v118, 16, v128\n"
      "v_and_b32 v119, 0xffff0000, v128\n"
      "v_pk_mul_f32 v[118:119], v[118:119], v[112:113] op_sel:[0,0] op_sel_hi:[1,0]\n"
      "v_cvt_pk_bf16_f32 v128, v118, v119\n"
      "v_lshlrev_b32 v118, 16, v129\n"
      "v_and_b32 v119, 0xffff0000, v129\n"
      "v_pk_mul_f32 v[118:119], v[118:119], v[112:113] op_sel:[0,0] op_sel_hi:[1,0]\n"
      "v_cvt_pk_bf16_f32 v129, v118, v119\n"
      "v_lshlrev_b32 v118, 16, v130\n"
      "v_and_b32 v119, 0xffff0000, v130\n"
      "v_pk_mul_f32 v[118:119], v[118:119], v[112:113] op_sel:[0,0] op_sel_hi:[1,0]\n"
      "v_cvt_pk_bf16_f32 v130, v118, v119\n"
      "v_lshlrev_b32 v118, 16, v131\n"
      "v_and_b32 v119, 0xffff0000, v131\n"
      "v_pk_mul_f32 v[118:119], v[118:119], v[112:113] op_sel:[0,0] op_sel_hi:[1,0]\n"
      "v_cvt_pk_bf16_f32 v131, v118, v119\n"
      "v_lshlrev_b32 v118, 16, v132\n"
      "v_and_b32 v119, 0xffff0000, v132\n"
      "v_pk_mul_f32 v[118:119], v[118:119], v[112:113] op_sel:[0,1] op_sel_hi:[1,1]\n"
      "v_cvt_pk_bf16_f32 v132, v118, v119\n"
      "v_lshlrev_b32 v118, 16, v133\n"
      "v_and_b32 v119, 0xffff0000, v133\n"
      "v_pk_mul_f32 v[118:119], v[118:119], v[112:113] op_sel:[0,1] op_sel_hi:[1,1]\n"
      "v_cvt_pk_bf16_f32 v133, v118, v119\n"
      "v_lshlrev_b32 v118, 16, v134\n"
      "v_and_b32 v119, 0xffff0000, v134\n"
      "v_pk_mul_f32 v[118:119], v[118:119], v[112:113] op_sel:[0,1] op_sel_hi:[1,1]\n"
      "v_cvt_pk_bf16_f32 v134, v118, v119\n"
      "v_lshlrev_b32 v118, 16, v135\n"
      "v_and_b32 v119, 0xffff0000, v135\n"
      "v_pk_mul_f32 v[118:119], v[118:119], v[112:113] op_sel:[0,1] op_sel_hi:[1,1]\n"
      "v_cvt_pk_bf16_f32 v135, v118, v119\n"
      "v_lshlrev_b32 v118, 16, v136\n"
      "v_and_b32 v119, 0xffff0000, v136\n"
      "v_pk_mul_f32 v[118:119], v[118:119], v[114:115] op_sel:[0,0] op_sel_hi:[1,0]\n"
      "v_cvt_pk_bf16_f32 v136, v118, v119\n"
      "v_lshlrev_b32 v118, 16, v137\n"
      "v_and_b32 v119, 0xffff0000, v137\n"
      "v_pk_mul_f32 v[118:119], v[118:119], v[114:115] op_sel:[0,0] op_sel_hi:[1,0]\n"
      "v_cvt_pk_bf16_f32 v137, v118, v119\n"
      "v_lshlrev_b32 v118, 16, v138\n"
      "v_and_b32 v119, 0xffff0000, v138\n"
      "v_pk_mul_f32 v[118:119], v[118:119], v[114:115] op_sel:[0,0] op_sel_hi:[1,0]\n"
      "v_cvt_pk_bf16_f32 v138, v118, v119\n"
      "v_lshlrev_b32 v118, 16, v139\n"
      "v_and_b32 v119, 0xffff0000, v139\n"
      "v_pk_mul_f32 v[118:119], v[118:119], v[114:115] op_sel:[0,0] op_sel_hi:[1,0]\n"
      "v_cvt_pk_bf16_f32 v139, v118, v119\n"
      "v_lshlrev_b32 v118, 16, v140\n"
      "v_and_b32 v119, 0xffff0000, v140\n"
      "v_pk_mul_f32 v[118:119], v[118:119], v[114:115] op_sel:[0,1] op_sel_hi:[1,1]\n"
      "v_cvt_pk_bf16_f32 v140, v118, v119\n"
      "v_lshlrev_b32 v118, 16, v141\n"
      "v_and_b32 v119, 0xffff0000, v141\n"
      "v_pk_mul_f32 v[118:119], v[118:119], v[114:115] op_sel:[0,1] op_sel_hi:[1,1]\n"
      "v_cvt_pk_bf16_f32 v141, v118, v119\n"
      "v_lshlrev_b32 v118, 16, v142\n"
      "v_and_b32 v119, 0xffff0000, v142\n"
      "v_pk_mul_f32 v[118:119], v[118:119], v[114:115] op_sel:[0,1] op_sel_hi:[1,1]\n"
      "v_cvt_pk_bf16_f32 v142, v118, v119\n"
      "v_lshlrev_b32 v118, 16, v143\n"
      "v_and_b32 v119, 0xffff0000, v143\n"
      "v_pk_mul_f32 v[118:119], v[118:119], v[114:115] op_sel:[0,1] op_sel_hi:[1,1]\n"
      "v_cvt_pk_bf16_f32 v143, v118, v119\n"
      "v_accvgpr_write_b32 a64, v128\n"
      "v_accvgpr_write_b32 a65, v129\n"
      "v_accvgpr_write_b32 a66, v130\n"
      "v_accvgpr_write_b32 a67, v131\n"
      "v_accvgpr_write_b32 a68, v132\n"
      "v_accvgpr_write_b32 a69, v133\n"
      "v_accvgpr_write_b32 a70, v134\n"
      "v_accvgpr_write_b32 a71, v135\n"
      "v_accvgpr_write_b32 a72, v136\n"
      "v_accvgpr_write_b32 a73, v137\n"
      "v_accvgpr_write_b32 a74, v138\n"
      "v_accvgpr_write_b32 a75, v139\n"
      "v_accvgpr_write_b32 a76, v140\n"
      "v_accvgpr_write_b32 a77, v141\n"
      "v_accvgpr_write_b32 a78, v142\n"
      "v_accvgpr_write_b32 a79, v143\n"
      "s_branch MR_%=\n"
      "FIX_%=:\n"
      "s_nop 7\n"
      "v_sub_f32 v224, v81, v117\n"
      "v_min_f32 v112, 0, -v224\n"
      "v_min_f32 v224, 0, v224\n"
      "v_exp_f32 v112, v112\n"
      "v_exp_f32 v224, v224\n"
      "s_nop 0\n"
      "v_mul_f32 v144, v144, v224\n"
      "v_mul_f32 v80, v80, v112\n"
      "v_mov_b32 v226, v224\n"
      "v_mov_b32 v114, v112\n"
      "s_nop 0\n"
      "v_permlane32_swap_b32 v224, v226\n"
      "v_permlane32_swap_b32 v112, v114\n"
      "v_mov_b32 v225, v224\n"
      "v_mov_b32 v113, v112\n"
      "v_mov_b32 v227, v226\n"
      "v_mov_b32 v115, v114\n"
      "v_permlane16_swap_b32 v224, v225\n"
      "v_permlane16_swap_b32 v112, v113\n"
      "v_permlane16_swap_b32 v226, v227\n"
      "v_permlane16_swap_b32 v114, v115\n"
      "v_pk_mul_f32 v[32:33], v[32:33], v[224:225] op_sel:[0,0] op_sel_hi:[1,0]\n"
      "v_pk_mul_f32 v[34:35], v[34:35], v[224:225] op_sel:[0,0] op_sel_hi:[1,0]\n"
      "v_pk_mul_f32 v[36:37], v[36:37], v[224:225] op_sel:[0,1] op_sel_hi:[1,1]\n"
      "v_pk_mul_f32 v[38:39], v[38:39], v[224:225] op_sel:[0,1] op_sel_hi:[1,1]\n"
      "v_pk_mul_f32 v[40:41], v[40:41], v[226:227] op_sel:[0,0] op_sel_hi:[1,0]\n"
      "v_pk_mul_f32 v[42:43], v[42:43], v[226:227] op_sel:[0,0] op_sel_hi:[1,0]\n"
      "v_pk_mul_f32 v[44:45], v[44:45], v[226:227] op_sel:[0,1] op_sel_hi:[1,1]\n"
      "v_pk_mul_f32 v[46:47], v[46:47], v[226:227] op_sel:[0,1] op_sel_hi:[1,1]\n"
      "v_pk_mul_f32 v[48:49], v[48:49], v[224:225] op_sel:[0,0] op_sel_hi:[1,0]\n"
      "v_pk_mul_f32 v[50:51], v[50:51], v[224:225] op_sel:[0,0] op_sel_hi:[1,0]\n"
      "v_pk_mul_f32 v[52:53], v[52:53], v[224:225] op_sel:[0,1] op_sel_hi:[1,1]\n"
      "v_pk_mul_f32 v[54:55], v[54:55], v[224:225] op_sel:[0,1] op_sel_hi:[1,1]\n"
      "v_pk_mul_f32 v[56:57], v[56:57], v[226:227] op_sel:[0,0] op_sel_hi:[1,0]\n"
      "v_pk_mul_f32 v[58:59], v[58:59], v[226:227] op_sel:[0,0] op_sel_hi:[1,0]\n"
      "v_pk_mul_f32 v[60:61], v[60:61], v[226:227] op_sel:[0,1] op_sel_hi:[1,1]\n"
      "v_pk_mul_f32 v[62:63], v[62:63], v[226:227] op_sel:[0,1] op_sel_hi:[1,1]\n"
      "v_lshlrev_b32 v118, 16, v64\n"
      "v_and_b32 v119, 0xffff0000, v64\n"
      "v_pk_mul_f32 v[118:119], v[118:119], v[112:113] op_sel:[0,0] op_sel_hi:[1,0]\n"
      "v_cvt_pk_bf16_f32 v64, v118, v119\n"
      "v_lshlrev_b32 v118, 16, v65\n"
      "v_and_b32 v119, 0xffff0000, v65\n"
      "v_pk_mul_f32 v[118:119], v[118:119], v[112:113] op_sel:[0,0] op_sel_hi:[1,0]\n"
      "v_cvt_pk_bf16_f32 v65, v118, v119\n"
      "v_lshlrev_b32 v118, 16, v66\n"
      "v_and_b32 v119, 0xffff0000, v66\n"
      "v_pk_mul_f32 v[118:119], v[118:119], v[112:113] op_sel:[0,0] op_sel_hi:[1,0]\n"
      "v_cvt_pk_bf16_f32 v66, v118, v119\n"
      "v_lshlrev_b32 v118, 16, v67\n"
      "v_and_b32 v119, 0xffff0000, v67\n"
      "v_pk_mul_f32 v[118:119], v[118:119], v[112:113] op_sel:[0,0] op_sel_hi:[1,0]\n"
      "v_cvt_pk_bf16_f32 v67, v118, v119\n"
      "v_lshlrev_b32 v118, 16, v68\n"
      "v_and_b32 v119, 0xffff0000, v68\n"
      "v_pk_mul_f32 v[118:119], v[118:119], v[112:113] op_sel:[0,1] op_sel_hi:[1,1]\n"
      "v_cvt_pk_bf16_f32 v68, v118, v119\n"
      "v_lshlrev_b32 v118, 16, v69\n"
      "v_and_b32 v119, 0xffff0000, v69\n"
      "v_pk_mul_f32 v[118:119], v[118:119], v[112:113] op_sel:[0,1] op_sel_hi:[1,1]\n"
      "v_cvt_pk_bf16_f32 v69, v118, v119\n"
      "v_lshlrev_b32 v118, 16, v70\n"
      "v_and_b32 v119, 0xffff0000, v70\n"
      "v_pk_mul_f32 v[118:119], v[118:119], v[112:113] op_sel:[0,1] op_sel_hi:[1,1]\n"
      "v_cvt_pk_bf16_f32 v70, v118, v119\n"
      "v_lshlrev_b32 v118, 16, v71\n"
      "v_and_b32 v119, 0xffff0000, v71\n"
      "v_pk_mul_f32 v[118:119], v[118:119], v[112:113] op_sel:[0,1] op_sel_hi:[1,1]\n"
      "v_cvt_pk_bf16_f32 v71, v118, v119\n"
      "v_lshlrev_b32 v118, 16, v72\n"
      "v_and_b32 v119, 0xffff0000, v72\n"
      "v_pk_mul_f32 v[118:119], v[118:119], v[114:115] op_sel:[0,0] op_sel_hi:[1,0]\n"
      "v_cvt_pk_bf16_f32 v72, v118, v119\n"
      "v_lshlrev_b32 v118, 16, v73\n"
      "v_and_b32 v119, 0xffff0000, v73\n"
      "v_pk_mul_f32 v[118:119], v[118:119], v[114:115] op_sel:[0,0] op_sel_hi:[1,0]\n"
      "v_cvt_pk_bf16_f32 v73, v118, v119\n"
      "v_lshlrev_b32 v118, 16, v74\n"
      "v_and_b32 v119, 0xffff0000, v74\n"
      "v_pk_mul_f32 v[118:119], v[118:119], v[114:115] op_sel:[0,0] op_sel_hi:[1,0]\n"
      "v_cvt_pk_bf16_f32 v74, v118, v119\n"
      "v_lshlrev_b32 v118, 16, v75\n"
      "v_and_b32 v119, 0xffff0000, v75\n"
      "v_pk_mul_f32 v[118:119], v[118:119], v[114:115] op_sel:[0,0] op_sel_hi:[1,0]\n"
      "v_cvt_pk_bf16_f32 v75, v118, v119\n"
      "v_lshlrev_b32 v118, 16, v76\n"
      "v_and_b32 v119, 0xffff0000, v76\n"
      "v_pk_mul_f32 v[118:119], v[118:119], v[114:115] op_sel:[0,1] op_sel_hi:[1,1]\n"
      "v_cvt_pk_bf16_f32 v76, v118, v119\n"
      "v_lshlrev_b32 v118, 16, v77\n"
      "v_and_b32 v119, 0xffff0000, v77\n"
      "v_pk_mul_f32 v[118:119], v[118:119], v[114:115] op_sel:[0,1] op_sel_hi:[1,1]\n"
      "v_cvt_pk_bf16_f32 v77, v118, v119\n"
      "v_lshlrev_b32 v118, 16, v78\n"
      "v_and_b32 v119, 0xffff0000, v78\n"
      "v_pk_mul_f32 v[118:119], v[118:119], v[114:115] op_sel:[0,1] op_sel_hi:[1,1]\n"
      "v_cvt_pk_bf16_f32 v78, v118, v119\n"
      "v_lshlrev_b32 v118, 16, v79\n"
      "v_and_b32 v119, 0xffff0000, v79\n"
      "v_pk_mul_f32 v[118:119], v[118:119], v[114:115] op_sel:[0,1] op_sel_hi:[1,1]\n"
      "v_cvt_pk_bf16_f32 v79, v118, v119\n"
      "v_add_f32 v224, v144, v80\n"
      "v_rcp_f32 v224, v224\n"
      "v_mfma_f32_16x16x32_bf16 v[160:163], v[86:89], v[64:67], v[32:35]\n"
      "v_mfma_f32_16x16x32_bf16 v[176:179], v[84:87], v[64:67], v[48:51]\n"
      "v_mfma_f32_16x16x32_bf16 v[164:167], v[86:89], v[68:71], v[36:39]\n"
      "v_mfma_f32_16x16x32_bf16 v[180:183], v[84:87], v[68:71], v[52:55]\n"
      "v_mfma_f32_16x16x32_bf16 v[168:171], v[86:89], v[72:75], v[40:43]\n"
      "v_mfma_f32_16x16x32_bf16 v[184:187], v[84:87], v[72:75], v[56:59]\n"
      "v_mfma_f32_16x16x32_bf16 v[172:175], v[86:89], v[76:79], v[44:47]\n"
      "v_mfma_f32_16x16x32_bf16 v[188:191], v[84:87], v[76:79], v[60:63]\n"
      "v_mov_b32 v226, v224\n"
      "s_nop 1\n"
      "s_nop 0\n"
      "s_nop 1\n"
      "v_permlane32_swap_b32 v224, v226\n"
      "s_nop 1\n"
      "v_mov_b32 v225, v224\n"
      "s_nop 1\n"
      "v_mov_b32 v227, v226\n"
      "s_nop 1\n"
      "v_permlane16_swap_b32 v224, v225\n"
      "s_nop 1\n"
      "v_permlane16_swap_b32 v226, v227\n"
      "s_nop 1\n"
      "s_branch FR_%=\n"
      ".subsection 0\n"
      : "+{s92}"(tch), "+{v[208:215]}"(mk), "+{v[240:241]}"(epoch), "+{v[242:249]}"(ad), "+{v[250:251]}"(ra)
      : [srdk] "s"(srdK), [srdv] "s"(srdV), [srdr] "s"(srdR), [srdx] "s"(srdX), [srdt] "s"(srdVt), [line] "s"(line),
        [outp] "s"((char*)out + ((size_t)bk * 128 + rh * 64) * (D_ * 2) + (2 * split + kh) * 64),
        [fo] "s"(64 + split * 8), [xo] "s"(split * 4 * (unsigned)XW_BYTES), [xw] "s"(xarea(wid)), [xr] "s"(xarea(wid ^ 2)),
        [so0] "s"(so0), [inc] "s"(inc), [wb] "s"(wbase), [tv] "s"(wbase + STAGE_BYTES + KIMG + split * 8192), [n1] "s"(n1), [n2] "s"(12 - n1),
        [qoff] "s"(Q_OFF + rh * 16384 - kh * (8192 - 1024)), [toff] "s"(TREE_OFF - kh * (8192 - 1024)),
        [neg] "s"((wid ^ (3 * split)) ? 0xff800000u : 0u),
        [lead] "s"(blockIdx.x * 4 + wid * 32), [cph] "s"(C2 + (unsigned)((jj & ~3) * 8 + xg * 4) * WIN / 256u),
        [rep] "s"((jj >> 2) == 160 / 32 ? 128 + (blockIdx.x & 31) * 4 : (unsigned)(blockIdx.x - 224) * 4), [vrd] "s"(8192 + 256 + (jj & (2 - 1)) * (8192 / 2))
      : "v0", "v1", "v2", "v3", "v4", "v5", "v6", "v7", "v8", "v9", "v10", "v11", "v12", "v13", "v14", "v15", "v16", "v17", "v18", "v19", "v20", "v21", "v22", "v23", "v24", "v25", "v26", "v27", "v28", "v29", "v30", "v31", "v32", "v33", "v34", "v35", "v36", "v37", "v38", "v39", "v40", "v41", "v42", "v43", "v44", "v45", "v46", "v47", "v48", "v49", "v50", "v51", "v52", "v53", "v54", "v55", "v56", "v57", "v58", "v59", "v60", "v61", "v62", "v63", "v64", "v65", "v66", "v67", "v68", "v69", "v70", "v71", "v72", "v73", "v74", "v75", "v76", "v77", "v78", "v79", "v80", "v81", "v82", "v83", "v84", "v85", "v86", "v87", "v88", "v89", "v90", "v91", "v92", "v93", "v94", "v95", "v96", "v97", "v98", "v99", "v100", "v101", "v102", "v103", "v104", "v105", "v106", "v107", "v108", "v109", "v110", "v111", "v112", "v113", "v114", "v115", "v116", "v117", "v118", "v119", "v120", "v121", "v122", "v123", "v124", "v125", "v126", "v127", "v128", "v129", "v130", "v131", "v132", "v133", "v134", "v135", "v136", "v137", "v138", "v139", "v140", "v141", "v142", "v143", "v144", "v145", "v146", "v147", "v148", "v149", "v150", "v151", "v152", "v153", "v154", "v155", "v156", "v157", "v158", "v159", "v160", "v161", "v162", "v163", "v164", "v165", "v166", "v167", "v168", "v169", "v170", "v171", "v172", "v173", "v174", "v175", "v176", "v177", "v178", "v179", "v180", "v181", "v182", "v183", "v184", "v185", "v186", "v187", "v188", "v189", "v190", "v191", "v192", "v193", "v194", "v195", "v196", "v197", "v198", "v199", "v200", "v201", "v202", "v203", "v204", "v205", "v206", "v207", "v216", "v217", "v218", "v219", "v220", "v221", "v222", "v223", "v224", "v225", "v226", "v227", "v228", "v229", "v230", "v231", "v232", "v233", "v234", "v235", "v236", "v237", "v238", "v239", "a0", "a1", "a2", "a3", "a4", "a5", "a6", "a7", "a8", "a9", "a10", "a11", "a12", "a13", "a14", "a15", "a16", "a17", "a18", "a19", "a20", "a21", "a22", "a23", "a24", "a25", "a26", "a27", "a28", "a29", "a30", "a31", "a32", "a33", "a34", "a35", "a36", "a37", "a38", "a39", "a40", "a41", "a42", "a43", "a44", "a45", "a46", "a47", "a48", "a49", "a50", "a51", "a52", "a53", "a54", "a55", "a56", "a57", "a58", "a59", "a60", "a61", "a62", "a63", "a64", "a65", "a66", "a67", "a68", "a69", "a70", "a71", "a72", "a73", "a74", "a75", "a76", "a77", "a78", "a79", "a80", "a81", "a82", "a83", "a84", "a85", "a86", "a87", "a88", "a89", "a90", "a91", "a92", "a93", "a94", "a95", "a96", "a97", "a98", "a99", "a100", "a101", "a102", "a103", "a104", "a105", "a106", "a107", "a108", "a109", "a110", "a111", "a112", "a113", "a114", "a115", "a116", "a117", "a118", "a119", "a120", "a121", "a122", "a123", "a124", "a125", "a126", "a127", "a128", "a129", "a130", "a131", "a132", "a133", "a134", "a135", "a136", "a137", "a138", "a139", "a140", "a141", "a142", "a143", "a144", "a145", "a146", "a147", "a148", "a149", "a150", "a151", "a152", "a153", "a154", "a155", "a156", "a157", "a158", "a159", "a160", "a161", "a162", "a163", "a164", "a165", "a166", "a167", "a168", "a169", "a170", "a171", "a172", "a173", "a174", "a175", "a176", "a177", "a178", "a179", "a180", "a181", "a182", "a183", "a184", "a185", "a186", "a187", "a188", "a189", "a190", "a191", "a192", "a193", "a194", "a195", "a196", "a197", "a198", "a199", "a200", "a201", "a202", "a203", "a204", "a205", "a206", "a207", "a208", "a209", "a210", "a211", "a212", "a213", "a214", "a215", "a216", "a217", "a218", "a219", "a220", "a221", "a222", "a223", "a224", "a225", "a226", "a227", "a228", "a229", "a230", "a231", "s60", "s61", "s62", "s63", "s64", "s65", "s66", "s67", "s68", "s69", "s70", "s71", "s72", "s73", "s74", "s75", "s76", "s77", "s78", "s79", "s80", "s81", "s82", "s83", "s84", "s85", "s86", "s87", "s88", "s89", "s90", "s91", "s93", "s94", "s95", "s99", "s100", "s101", "vcc", "scc", "memory");
}

}

extern "C" void kernel_launch(void* const* d_in, const int* in_sizes, int n_in, void* d_out, int out_size,
                              void* d_ws, size_t ws_size, hipStream_t stream) {
  if (n_in != 6 || ws_size < WS_END) return;
  const __hip_bfloat16* q = (const __hip_bfloat16*)d_in[0];
  const __hip_bfloat16* kc = (const __hip_bfloat16*)d_in[1];
  const __hip_bfloat16* vc = (const __hip_bfloat16*)d_in[2];
  const __hip_bfloat16* kt = (const __hip_bfloat16*)d_in[3];
  const __hip_bfloat16* vt = (const __hip_bfloat16*)d_in[4];
  const __hip_bfloat16* tm = (const __hip_bfloat16*)d_in[5];
  hipLaunchKernelGGL(tree_attn_main, dim3(B_ * HKV_ * 2), dim3(256), 0, stream, q, kc, vc, kt, vt, tm,
                     (__hip_bfloat16*)d_out, (char*)d_ws);
}
```

```cpp
#include <hip/hip_runtime.h>
#include <hip/hip_bf16.h>
#include <stdint.h>

#pragma clang diagnostic ignored "-Wint-to-pointer-cast"
#pragma clang diagnostic ignored "-Winline-asm"

namespace {

constexpr int B_ = 16, HQ_ = 32, HKV_ = 8, T_ = 32, C_ = 4096, D_ = 128;
constexpr int TILE = 64;
constexpr int KIMG = TILE * D_ * 2;
constexpr int STAGE_BYTES = 2 * KIMG;
constexpr int TREE_OFF = 4 * STAGE_BYTES;
constexpr int Q_OFF = 3 * STAGE_BYTES;
constexpr int LDS_BYTES = TREE_OFF + 2 * 13312;
constexpr unsigned C2 = 324, WIN = 52;

constexpr size_t WS_EPOCH = 0;
constexpr size_t WS_REF = WS_EPOCH + 128 * 128;
constexpr size_t WS_X = WS_REF + 16384, WS_X1 = 4 << 20;
constexpr size_t XW_BYTES = 4096 + 1024;
constexpr size_t WS_END = 0x680000 + WS_X;
static_assert(WS_X1 + WS_X + 128 * 4 * XW_BYTES <= WS_END, "");

typedef float    f32x4  __attribute__((ext_vector_type(4)));
typedef unsigned long long u64x8 __attribute__((ext_vector_type(8)));
typedef unsigned u32x8  __attribute__((ext_vector_type(8)));
typedef unsigned u32x4  __attribute__((ext_vector_type(4)));
typedef unsigned u32x2  __attribute__((ext_vector_type(2)));
typedef __attribute__((address_space(3))) f32x4 lds_f32x4;
typedef __attribute__((address_space(1))) unsigned long long gu64;

__global__ __launch_bounds__(256, 1)
void tree_attn_main(const __hip_bfloat16*, const __hip_bfloat16*, const __hip_bfloat16*, const __hip_bfloat16*,
                    const __hip_bfloat16*, const __hip_bfloat16*, __hip_bfloat16*, char*) {
  __shared__ __attribute__((aligned(1024))) char smem[LDS_BYTES];
  unsigned blk = blockIdx.x;
  u64x8 ka; u32x4 pre; unsigned tch, voffK; int tid = threadIdx.x;
  asm volatile(
      "s_load_dwordx16 s[4:19], %[kp], 0x0\n"
      "s_getpc_b64 s[84:85]\n"
      "v_lshlrev_b32 v112, 4, %[tid]\n"
      "s_lshl_b32 s69, %[blk], 7\n"
      "s_and_b32 s69, s69, 0x7c00\n"
      "v_lshl_add_u32 v120, %[tid], 2, s69\n"
      "global_load_dword v128, v120, s[84:85]\n"
      "v_readfirstlane_b32 s77, %[tid]\n"
      "v_lshrrev_b32 %[vk], 4, %[tid]\n"
      "s_and_b32 s78, %[blk], 7\n"
      "v_xor_b32 %[vk], %[vk], %[tid]\n"
      "s_lshr_b32 s74, %[blk], 4\n"
      "v_and_b32 %[vk], 7, %[vk]\n"
      "s_lshl4_add_u32 s78, s78, s74\n"
      "v_lshrrev_b32 v120, 3, %[tid]\n"
      "s_lshl_b32 s78, s78, 15\n"
      "v_lshlrev_b32 %[vk], 4, %[vk]\n"
      "s_lshl_b32 s77, s77, 5\n"
      "v_lshl_or_b32 %[vk], v120, 8, %[vk]\n"
      "s_bfm_b32 s66, 1, 22\n"
      "s_bfm_b32 s67, 1, 17\n"
      "s_add_u32 m0, s77, 0x18000\n"
      "s_waitcnt lgkmcnt(0)\n"
      "s_mov_b32 s64, s4\n"
      "s_and_b32 s65, s5, 0xffff\n"
      "buffer_load_dwordx4 %[vk], s[64:67], s78 offen nt lds\n"
      "s_add_u32 m0, s77, 99200\n"
      "v_add_u32 v120, 0x2000, %[vk]\n"
      "buffer_load_dwordx4 %[vk], s[64:67], s78 offen offset:128 nt lds\n"
      "s_add_u32 m0, s77, 106496\n"
      "s_add_u32 s74, s78, 0x4000\n"
      "s_lshl_b32 s75, s78, 5\n"
      "buffer_load_dwordx4 v120, s[64:67], s78 offen nt lds\n"
      "s_add_u32 m0, s77, 107392\n"
      "s_nop 0\n"
      "buffer_load_dwordx4 v120, s[64:67], s78 offen offset:128 nt lds\n"
      "s_add_u32 m0, s77, 114688\n"
      "s_load_dword s92, s[6:7], s75\n"
      "s_load_dword s92, s[8:9], s75\n"
      "buffer_load_dwordx4 %[vk], s[64:67], s74 offen nt lds\n"
      "s_add_u32 m0, s77, 115584\n"
      "s_mov_b32 s84, s10\n"
      "s_and_b32 s85, s11, 0xffff\n"
      "buffer_load_dwordx4 %[vk], s[64:67], s74 offen offset:128 nt lds\n"
      "s_add_u32 m0, s77, 122880\n"
      "s_mov_b32 s86, 0x100000\n"
      "s_mov_b32 s87, 0x20000\n"
      "s_lshr_b32 s75, s78, 2\n"
      "buffer_load_dwordx4 v120, s[64:67], s74 offen nt lds\n"
      "s_add_u32 m0, s77, 123776\n"
      "s_lshr_b32 s76, s78, 18\n"
      "s_lshl_b32 s76, s76, 11\n"
      "buffer_load_dwordx4 v120, s[64:67], s74 offen offset:128 nt lds\n"
      "s_add_u32 m0, s77, 131072\n"
      "s_lshr_b32 s69, s77, 1\n"
      "buffer_load_dwordx4 %[vk], s[84:87], s75 offen nt lds\n"
      "s_add_u32 m0, s77, 131968\n"
      "s_cmp_gt_u32 s77, 2048\n"
      "buffer_load_dwordx4 %[vk], s[84:87], s75 offen offset:128 nt lds\n"
      "s_cbranch_scc1 NOMASK_%=\n"
      "s_add_u32 m0, s69, 140288\n"
      "s_mov_b32 s64, s14\n"
      "s_and_b32 s65, s15, 0xffff\n"
      "s_mov_b32 s66, 0x8000\n"
      "buffer_load_dwordx4 v112, s[64:67], s76 offen lds\n"
      "NOMASK_%=:\n"
      : "=&{s[4:19]}"(ka), "=&{v[128:131]}"(pre), "=&{s92}"(tch), [vk] "=&v"(voffK), [tid] "+v"(tid)
      : [kp] "s"(__builtin_amdgcn_kernarg_segment_ptr()), [blk] "s"(blk)
      : "v112", "v120", "s64", "s65", "s66", "s67", "s69", "s70", "s71", "s74", "s75", "s76", "s77", "s78", "s84", "s85", "s86", "s87", "s96", "s97", "scc", "memory");
  asm volatile("" : "+s"(blk));
  const int lane = tid & 63;
  const int wid = __builtin_amdgcn_readfirstlane(tid >> 6);
  const unsigned wbase = wid * 2048;
  const int rh = wid & 1, kh = wid >> 1;
  const int xg = blk & 7, jj = blk >> 3, bk = xg * 16 + (jj >> 1), split = jj & 1;
  const int c16 = lane & 15, g = lane >> 4;
  const unsigned lds0 = (unsigned)(uintptr_t)smem;

  const unsigned voffV = (unsigned)((8 * wid + (lane >> 3)) * 256 + (((lane & 7) ^ (g << 1)) << 4));
  const unsigned so0 = (unsigned)(bk * C_ + split * 2 * TILE) * (D_ * 2) + (bk & 1) * KIMG, inc = KIMG - 2 * (bk & 1) * KIMG;
#pragma unroll
  for (int j = 0; j < 4; ++j) *(lds_f32x4*)(lds0 + KIMG + 4096 * j + tid * 16) = f32x4{0.f, 0.f, 0.f, 0.f};
#pragma unroll
  for (int j = 0; j < 2; ++j) *(lds_f32x4*)(lds0 + STAGE_BYTES + KIMG + (1 - split) * 8192 + 4096 * j + tid * 16) = f32x4{0.f, 0.f, 0.f, 0.f};
  u32x8 ad;
#pragma unroll
  for (int j = 0; j < 2; ++j)
    ad[j] = lds0 + (unsigned)(2048 * (c16 >> 3) + 128 * (c16 & 7) + (((4 * j + g) ^ (c16 >> 1)) << 4) + kh * 8192);
  {
    const int qq = c16 >> 2, p = lane & 3;
#pragma unroll
    for (int j = 0; j < 4; ++j)
      ad[2 + j] = lds0 + (unsigned)(2048 * (g >> 1) + 128 * (4 * (g & 1) + qq) + kh * 8192 + KIMG +
                                    (((2 * (j ^ (2 * kh)) + (p >> 1)) ^ ((2 * (g & 1) + (qq >> 1)) << 1)) << 4) + 8 * (p & 1));
  }
  ad[6] = voffK; ad[7] = voffV + 128 * split;
  u32x2 ra = {(unsigned)(lane < 4 ? WS_REF + lane * 512 : 1 << 26), (unsigned)(WS_REF + (jj & (4 - 1)) * 512 + (lane & 7) * 4 + (wid << 26))};
  auto xarea = [&](int w) { return lds0 + ((0x8d802000u >> (8 * w)) & 255u) * 1024; };
  const unsigned pg = jj >> 2, px = pg - (pg > 160 / 32) + xg * 5;
  const unsigned n1 = 4 + max(px - 6 * (px * 43 >> 8), ((0x70000000u | 6u << 160 / 8) >> 4 * pg) & 7);
  asm volatile(
      "s_memrealtime s[82:83]\n"
      "s_mov_b32 s60, %[so0]\n"
      "s_mov_b32 s62, %[wb]\n"
      "v_add_u32 v120, 0x2000, v248\n"
      "s_and_b32 s80, %[fo], 0xffffff80\n"
      "v_mov_b32 v114, s80\n"
      "s_mov_b32 s22, 0x8000000\n"
      "s_mov_b32 s23, 0x20000\n"
      "s_mov_b32 s20, s6\n"
      "s_and_b32 s21, s7, 0xffff\n"
      "s_add_u32 m0, s62, 0\n"
      "global_load_dwordx2 v[240:241], v114, s[18:19] sc1\n"
      "buffer_load_dwordx4 v248, s[20:23], s60 offen nt lds\n"
      "s_mov_b32 s86, 0x100000\n"
      "s_mov_b32 s87, 0x20000\n"
      "s_lshr_b32 s75, %[qo], 2\n"
      "s_mov_b32 s84, s12\n"
      "s_and_b32 s85, s13, 0xffff\n"
      "v_mov_b64 v[216:217], 0\n"
      "v_mov_b64 v[218:219], 0\n"
      "v_xor_b32 v121, 0x80, v249\n"
      "v_add_u32 v122, 0x2000, v249\n"
      "s_add_u32 m0, s62, 896\n"
      "s_nop 0\n"
      "buffer_load_dwordx4 v248, s[20:23], s60 offen offset:128 nt lds\n"
      "v_mfma_f32_32x32x16_bf16 v[32:47], v[216:219], v[216:219], 0\n"
      "v_add_u32 v123, 0x2000, v121\n"
      "s_mov_b32 s61, %[so0]\n"
      "s_sub_u32 s78, 0x10000, %[inc]\n"
      "s_add_u32 s77, %[wb], 0x10000\n"
      "v_mfma_f32_32x32x16_bf16 v[48:63], v[216:219], v[216:219], 0\n"
      "s_mov_b32 s70, 300\n"
      "s_mov_b32 s80, 0x443212a\n"
      "s_mov_b32 s81, 0x0\n"
      "s_add_u32 m0, s62, 8192\n"
      "s_nop 0\n"
      "buffer_load_dwordx4 v120, s[20:23], s60 offen nt lds\n"
      "v_mfma_f32_32x32x16_bf16 v[64:79], v[216:219], v[216:219], 0\n"
      "s_mov_b32 s101, 1000\n"
      "s_mov_b32 s93, 28\n"
      "s_mov_b32 s99, 21\n"
      "s_mov_b32 s100, 282\n"
      "v_mfma_f32_32x32x16_bf16 v[80:95], v[216:219], v[216:219], 0\n"
      "s_lshl_b32 s91, %[wb], 18\n"
      "s_sub_u32 s91, 0x10000000, s91\n"
      "s_mov_b32 s94, 0x3e0293ee\n"
      "s_add_u32 m0, s62, 9088\n"
      "s_nop 0\n"
      "buffer_load_dwordx4 v120, s[20:23], s60 offen offset:128 nt lds\n"
      "s_add_u32 s62, s62, 0x8000\n"
      "s_and_b32 s62, s62, 0x1ffff\n"
      "s_add_u32 s60, s60, %[inc]\n"
      "s_mov_b32 s95, 0xffff0000\n"
      "v_mfma_f32_32x32x16_bf16 v[192:207], v[216:219], v[216:219], 0\n"
      "s_mov_b32 s76, 0xd0289660\n"
      "v_mbcnt_lo_u32_b32 v112, -1, 0\n"
      "v_mbcnt_hi_u32_b32 v112, -1, v112\n"
      "v_lshrrev_b32 v115, 4, v112\n"
      "v_and_b32 v112, 15, v112\n"
      "v_lshlrev_b32 v115, 3, v115\n"
      "v_lshl_add_u32 v115, v112, 6, v115\n"
      "s_add_u32 m0, s62, 0\n"
      "s_nop 0\n"
      "buffer_load_dwordx4 v248, s[20:23], s60 offen nt lds\n"
      "v_add_u32 v115, 140288, v115\n"
      "s_mov_b32 s28, s18\n"
      "s_and_b32 s29, s19, 0xffff\n"
      "s_mov_b32 s24, s8\n"
      "s_and_b32 s25, s9, 0xffff\n"
      "s_mov_b32 s26, 0x8000000\n"
      "s_mov_b32 s27, 0x20000\n"
      "s_mov_b32 s30, 0x688000\n"
      "s_mov_b32 s31, 0x20000\n"
      "s_add_u32 m0, s62, 896\n"
      "s_nop 0\n"
      "buffer_load_dwordx4 v248, s[20:23], s60 offen offset:128 nt lds\n"
      "v_mov_b32 v113, 0x3f803f80\n"
      "v_accvgpr_write_b32 a192, 0\n"
      "v_add_u32 v224, %[qoff], v242\n"
      "v_add_u32 v228, %[toff], v242\n"
      "v_xor_b32 v226, 0x400, v224\n"
      "v_xor_b32 v230, 0x400, v228\n"
      "v_and_b32 v118, 0xfffffbff, v224\n"
      "v_add_u32 v225, %[qoff], v243\n"
      "v_add_u32 v229, %[toff], v243\n"
      "s_add_u32 m0, s62, 8192\n"
      "s_nop 0\n"
      "buffer_load_dwordx4 v120, s[20:23], s60 offen nt lds\n"
      "v_xor_b32 v227, 0x400, v225\n"
      "v_xor_b32 v231, 0x400, v229\n"
      "v_and_b32 v119, 0xfffffbff, v225\n"
      "v_cmp_eq_u32 vcc, 0, v112\n"
      "v_mfma_f32_32x32x16_bf16 a[128:143], v[216:219], v[216:219], 0\n"
      "s_nop 0\n"
      "v_cndmask_b32 v114, 0, v113, vcc\n"
      "v_accvgpr_write_b32 a196, v114\n"
      "v_accvgpr_write_b32 a197, v114\n"
      "s_add_u32 m0, s62, 9088\n"
      "s_nop 0\n"
      "buffer_load_dwordx4 v120, s[20:23], s60 offen offset:128 nt lds\n"
      "v_accvgpr_write_b32 a198, v114\n"
      "v_accvgpr_write_b32 a199, v114\n"
      "v_cmp_eq_u32 vcc, 4, v112\n"
      "v_mfma_f32_32x32x16_bf16 a[144:159], v[216:219], v[216:219], 0\n"
      "s_nop 0\n"
      "v_cndmask_b32 v114, 0, v113, vcc\n"
      "v_accvgpr_write_b32 a200, v114\n"
      "v_accvgpr_write_b32 a201, v114\n"
      "v_accvgpr_write_b32 a202, v114\n"
      "s_add_u32 m0, %[tv], 0\n"
      "s_nop 0\n"
      "buffer_load_dwordx4 v249, s[84:87], s75 offen nt lds\n"
      "v_accvgpr_write_b32 a203, v114\n"
      "v_cmp_eq_u32 vcc, 8, v112\n"
      "v_mfma_f32_32x32x16_bf16 a[160:175], v[216:219], v[216:219], 0\n"
      "s_nop 0\n"
      "v_cndmask_b32 v114, 0, v113, vcc\n"
      "v_accvgpr_write_b32 a204, v114\n"
      "v_accvgpr_write_b32 a205, v114\n"
      "v_accvgpr_write_b32 a206, v114\n"
      "v_accvgpr_write_b32 a207, v114\n"
      "s_add_u32 m0, %[tv], 1024\n"
      "s_nop 0\n"
      "buffer_load_dwordx4 v121, s[84:87], s75 offen nt lds\n"
      "v_cmp_eq_u32 vcc, 12, v112\n"
      "v_mfma_f32_32x32x16_bf16 a[176:191], v[216:219], v[216:219], 0\n"
      "s_nop 0\n"
      "v_cndmask_b32 v114, 0, v113, vcc\n"
      "v_accvgpr_write_b32 a208, v114\n"
      "v_accvgpr_write_b32 a209, v114\n"
      "v_accvgpr_write_b32 a210, v114\n"
      "v_accvgpr_write_b32 a211, v114\n"
      "s_add_u32 s62, s62, 0x8000\n"
      "s_and_b32 s62, s62, 0x1ffff\n"
      "s_add_u32 s60, s60, s78\n"
      "s_cmp_lt_u32 %[rep], 128\n"
      "s_cselect_b32 s89, 0x2ffe, 0\n"
      "s_or_b32 s89, s89, 0x2aae\n"
      "s_waitcnt vmcnt(11)\n"
      "s_barrier\n"
      "s_memrealtime s[68:69]\n"
      "ds_read_b64 v[208:209], v115 offset:0\n"
      "ds_read_b64 v[210:211], v115 offset:32\n"
      "ds_read_b64 v[212:213], v115 offset:1024\n"
      "ds_read_b64 v[214:215], v115 offset:1056\n"
      "ds_read_b128 a[64:67], v228 offset:0\n"
      "ds_read_b128 a[68:71], v228 offset:4096\n"
      "ds_read_b128 v[128:131], v224 offset:0\n"
      "ds_read_b128 v[132:135], v224 offset:4096\n"
      "ds_read_b128 v[136:139], v224 offset:8192\n"
      "ds_read_b128 v[140:143], v224 offset:12288\n"
      "ds_read_b128 a[72:75], v229 offset:0\n"
      "ds_read_b128 a[76:79], v229 offset:4096\n"
      "ds_read_b128 v[144:147], v225 offset:0\n"
      "ds_read_b128 v[148:151], v225 offset:4096\n"
      "s_waitcnt lgkmcnt(10)\n"
      "ds_read_b128 v[152:155], v225 offset:8192\n"
      "ds_read_b128 v[156:159], v225 offset:12288\n"
      "v_lshlrev_b32 v96, 16, v208\n"
      "v_and_b32 v97, 0xffff0000, v208\n"
      "v_lshlrev_b32 v98, 16, v209\n"
      "v_and_b32 v99, 0xffff0000, v209\n"
      "v_lshlrev_b32 v100, 16, v210\n"
      "v_and_b32 v101, 0xffff0000, v210\n"
      "v_lshlrev_b32 v102, 16, v211\n"
      "v_and_b32 v103, 0xffff0000, v211\n"
      "v_lshlrev_b32 v104, 16, v212\n"
      "v_and_b32 v105, 0xffff0000, v212\n"
      "v_lshlrev_b32 v106, 16, v213\n"
      "v_and_b32 v107, 0xffff0000, v213\n"
      "v_lshlrev_b32 v108, 16, v214\n"
      "v_and_b32 v109, 0xffff0000, v214\n"
      "v_lshlrev_b32 v110, 16, v215\n"
      "v_and_b32 v111, 0xffff0000, v215\n"
      "v_fma_f32 v96, -v96, s76, s76\n"
      "v_fma_f32 v97, -v97, s76, s76\n"
      "v_fma_f32 v98, -v98, s76, s76\n"
      "v_fma_f32 v99, -v99, s76, s76\n"
      "v_fma_f32 v100, -v100, s76, s76\n"
      "v_fma_f32 v101, -v101, s76, s76\n"
      "v_fma_f32 v102, -v102, s76, s76\n"
      "v_fma_f32 v103, -v103, s76, s76\n"
      "v_fma_f32 v104, -v104, s76, s76\n"
      "v_fma_f32 v105, -v105, s76, s76\n"
      "v_fma_f32 v106, -v106, s76, s76\n"
      "v_fma_f32 v107, -v107, s76, s76\n"
      "v_fma_f32 v108, -v108, s76, s76\n"
      "v_fma_f32 v109, -v109, s76, s76\n"
      "v_fma_f32 v110, -v110, s76, s76\n"
      "v_fma_f32 v111, -v111, s76, s76\n"
      "s_mov_b32 s86, 109\n"
      "s_mov_b32 s87, 0\n"
      "s_waitcnt lgkmcnt(0)\n"
      "ds_read_b128 a[80:83], v230 offset:0\n"
      "ds_read_b128 a[84:87], v230 offset:4096\n"
      "ds_read_b128 a[32:35], v226 offset:0\n"
      "ds_read_b128 a[36:39], v226 offset:4096\n"
      "ds_read_b128 a[40:43], v226 offset:8192\n"
      "ds_read_b128 a[44:47], v226 offset:12288\n"
      "ds_read_b128 a[88:91], v231 offset:0\n"
      "ds_read_b128 a[92:95], v231 offset:4096\n"
      "ds_read_b128 a[48:51], v227 offset:0\n"
      "ds_read_b128 a[52:55], v227 offset:4096\n"
      "ds_read_b128 a[56:59], v227 offset:8192\n"
      "ds_read_b128 a[60:63], v227 offset:12288\n"
      "s_cmp_lt_u32 %[lead], 32\n"
      "s_cbranch_scc0 NOLEAD_%=\n"
      "v_mov_b32 v112, s68\n"
      "buffer_store_dword v112, v250, s[28:31], %[lead] offen sc1\n"
      "NOLEAD_%=:\n"
      "v_mfma_f32_16x16x32_bf16 v[160:163], a[64:67], v[128:131], v[96:99]\n"
      "s_mov_b32 s83, 0\n"
      "v_lshlrev_b32 v232, 16, v128\n"
      "v_and_b32 v233, s95, v128\n"
      "v_mfma_f32_16x16x32_bf16 v[168:171], a[64:67], v[132:135], v[104:107]\n"
      "s_sub_u32 s67, s68, s82\n"
      "v_lshlrev_b32 v234, 16, v129\n"
      "v_and_b32 v235, s95, v129\n"
      "v_mul_f32 v232, s94, v232\n"
      "v_mul_f32 v233, s94, v233\n"
      "v_mfma_f32_16x16x32_bf16 v[176:179], a[64:67], v[136:139], v[96:99]\n"
      "s_min_u32 s67, s67, 140\n"
      "v_lshlrev_b32 v236, 16, v130\n"
      "v_and_b32 v237, s95, v130\n"
      "v_mul_f32 v234, s94, v234\n"
      "v_mul_f32 v235, s94, v235\n"
      "v_cvt_pk_bf16_f32 v0, v232, v233\n"
      "v_mfma_f32_16x16x32_bf16 v[184:187], a[64:67], v[140:143], v[104:107]\n"
      "s_add_u32 s66, s82, s67\n"
      "v_lshlrev_b32 v238, 16, v131\n"
      "v_and_b32 v239, s95, v131\n"
      "v_mul_f32 v236, s94, v236\n"
      "v_mul_f32 v237, s94, v237\n"
      "v_cvt_pk_bf16_f32 v1, v234, v235\n"
      "v_mfma_f32_16x16x32_bf16 v[164:167], a[68:71], v[128:131], v[100:103]\n"
      "s_add_u32 s66, s66, 108\n"
      "v_lshlrev_b32 v232, 16, v132\n"
      "v_and_b32 v233, s95, v132\n"
      "v_mul_f32 v238, s94, v238\n"
      "v_mul_f32 v239, s94, v239\n"
      "v_cvt_pk_bf16_f32 v2, v236, v237\n"
      "v_mfma_f32_16x16x32_bf16 v[172:175], a[68:71], v[132:135], v[108:111]\n"
      "s_sub_u32 s71, s68, 400\n"
      "v_lshlrev_b32 v234, 16, v133\n"
      "v_and_b32 v235, s95, v133\n"
      "v_mul_f32 v232, s94, v232\n"
      "v_mul_f32 v233, s94, v233\n"
      "v_cvt_pk_bf16_f32 v3, v238, v239\n"
      "v_mfma_f32_16x16x32_bf16 v[180:183], a[68:71], v[136:139], v[100:103]\n"
      "s_mov_b32 s68, s66\n"
      "v_lshlrev_b32 v236, 16, v134\n"
      "v_and_b32 v237, s95, v134\n"
      "v_mul_f32 v234, s94, v234\n"
      "v_mul_f32 v235, s94, v235\n"
      "v_cvt_pk_bf16_f32 v4, v232, v233\n"
      "v_mfma_f32_16x16x32_bf16 v[188:191], a[68:71], v[140:143], v[108:111]\n"
      "s_mov_b32 s82, s66\n"
      "v_lshlrev_b32 v238, 16, v135\n"
      "v_and_b32 v239, s95, v135\n"
      "v_mul_f32 v236, s94, v236\n"
      "v_mul_f32 v237, s94, v237\n"
      "v_cvt_pk_bf16_f32 v5, v234, v235\n"
      "v_mfma_f32_16x16x32_bf16 v[160:163], a[72:75], v[144:147], v[160:163]\n"
      "v_lshlrev_b32 v232, 16, v136\n"
      "v_and_b32 v233, s95, v136\n"
      "v_mul_f32 v238, s94, v238\n"
      "v_mul_f32 v239, s94, v239\n"
      "v_cvt_pk_bf16_f32 v6, v236, v237\n"
      "v_mfma_f32_16x16x32_bf16 v[168:171], a[72:75], v[148:151], v[168:171]\n"
      "v_lshlrev_b32 v234, 16, v137\n"
      "v_and_b32 v235, s95, v137\n"
      "v_mul_f32 v232, s94, v232\n"
      "v_mul_f32 v233, s94, v233\n"
      "v_cvt_pk_bf16_f32 v7, v238, v239\n"
      "s_waitcnt lgkmcnt(0)\n"
      "s_barrier\n"
      "v_mfma_f32_16x16x32_bf16 v[176:179], a[72:75], v[152:155], v[176:179]\n"
      "v_lshlrev_b32 v236, 16, v138\n"
      "v_and_b32 v237, s95, v138\n"
      "v_mul_f32 v234, s94, v234\n"
      "v_mul_f32 v235, s94, v235\n"
      "v_cvt_pk_bf16_f32 v8, v232, v233\n"
      "ds_write_b128 v224, v[0:3] offset:0\n"
      "v_mfma_f32_16x16x32_bf16 v[184:187], a[72:75], v[156:159], v[184:187]\n"
      "s_add_u32 m0, s62, 0\n"
      "v_lshlrev_b32 v238, 16, v139\n"
      "v_and_b32 v239, s95, v139\n"
      "v_mul_f32 v236, s94, v236\n"
      "v_mul_f32 v237, s94, v237\n"
      "v_cvt_pk_bf16_f32 v9, v234, v235\n"
      "v_mfma_f32_16x16x32_bf16 v[164:167], a[76:79], v[144:147], v[164:167]\n"
      "v_lshlrev_b32 v232, 16, v140\n"
      "v_and_b32 v233, s95, v140\n"
      "v_mul_f32 v238, s94, v238\n"
      "v_mul_f32 v239, s94, v239\n"
      "v_cvt_pk_bf16_f32 v10, v236, v237\n"
      "buffer_load_dwordx4 v248, s[20:23], s60 offen nt lds\n"
      "ds_write_b128 v224, v[4:7] offset:4096\n"
      "v_mfma_f32_16x16x32_bf16 v[172:175], a[76:79], v[148:151], v[172:175]\n"
      "s_add_u32 m0, s62, 896\n"
      "v_lshlrev_b32 v234, 16, v141\n"
      "v_and_b32 v235, s95, v141\n"
      "v_mul_f32 v232, s94, v232\n"
      "v_mul_f32 v233, s94, v233\n"
      "v_cvt_pk_bf16_f32 v11, v238, v239\n"
      "v_mfma_f32_16x16x32_bf16 v[180:183], a[76:79], v[152:155], v[180:183]\n"
      "v_lshlrev_b32 v236, 16, v142\n"
      "v_and_b32 v237, s95, v142\n"
      "v_mul_f32 v234, s94, v234\n"
      "v_mul_f32 v235, s94, v235\n"
      "v_cvt_pk_bf16_f32 v12, v232, v233\n"
      "buffer_load_dwordx4 v248, s[20:23], s60 offen offset:128 nt lds\n"
      "ds_write_b128 v224, v[8:11] offset:8192\n"
      "v_mfma_f32_16x16x32_bf16 v[188:191], a[76:79], v[156:159], v[188:191]\n"
      "s_add_u32 m0, s62, 8192\n"
      "v_lshlrev_b32 v238, 16, v143\n"
      "v_and_b32 v239, s95, v143\n"
      "v_mul_f32 v236, s94, v236\n"
      "v_mul_f32 v237, s94, v237\n"
      "v_cvt_pk_bf16_f32 v13, v234, v235\n"
      "v_mfma_f32_16x16x32_bf16 v[160:163], a[80:83], a[32:35], v[160:163]\n"
      "v_lshlrev_b32 v232, 16, v144\n"
      "v_and_b32 v233, s95, v144\n"
      "v_mul_f32 v238, s94, v238\n"
      "v_mul_f32 v239, s94, v239\n"
      "v_cvt_pk_bf16_f32 v14, v236, v237\n"
      "buffer_load_dwordx4 v120, s[20:23], s60 offen nt lds\n"
      "v_mfma_f32_16x16x32_bf16 v[168:171], a[80:83], a[36:39], v[168:171]\n"
      "s_add_u32 m0, s62, 9088\n"
      "v_lshlrev_b32 v234, 16, v145\n"
      "v_and_b32 v235, s95, v145\n"
      "v_mul_f32 v232, s94, v232\n"
      "v_mul_f32 v233, s94, v233\n"
      "v_cvt_pk_bf16_f32 v15, v238, v239\n"
      "ds_write_b128 v224, v[12:15] offset:12288\n"
      "v_mfma_f32_16x16x32_bf16 v[176:179], a[80:83], a[40:43], v[176:179]\n"
      "s_add_u32 s62, s62, 0x8000\n"
      "v_lshlrev_b32 v236, 16, v146\n"
      "v_and_b32 v237, s95, v146\n"
      "v_mul_f32 v234, s94, v234\n"
      "v_mul_f32 v235, s94, v235\n"
      "v_cvt_pk_bf16_f32 v16, v232, v233\n"
      "buffer_load_dwordx4 v120, s[20:23], s60 offen offset:128 nt lds\n"
      "v_mfma_f32_16x16x32_bf16 v[184:187], a[80:83], a[44:47], v[184:187]\n"
      "s_and_b32 s62, s62, 0x1ffff\n"
      "v_lshlrev_b32 v238, 16, v147\n"
      "v_and_b32 v239, s95, v147\n"
      "v_mul_f32 v236, s94, v236\n"
      "v_mul_f32 v237, s94, v237\n"
      "v_cvt_pk_bf16_f32 v17, v234, v235\n"
      "v_mfma_f32_16x16x32_bf16 v[164:167], a[84:87], a[32:35], v[164:167]\n"
      "s_add_u32 s60, s60, %[inc]\n"
      "v_lshlrev_b32 v232, 16, v148\n"
      "v_and_b32 v233, s95, v148\n"
      "v_mul_f32 v238, s94, v238\n"
      "v_mul_f32 v239, s94, v239\n"
      "v_cvt_pk_bf16_f32 v18, v236, v237\n"
      "v_mfma_f32_16x16x32_bf16 v[172:175], a[84:87], a[36:39], v[172:175]\n"
      "v_lshlrev_b32 v234, 16, v149\n"
      "v_and_b32 v235, s95, v149\n"
      "v_mul_f32 v232, s94, v232\n"
      "v_mul_f32 v233, s94, v233\n"
      "v_cvt_pk_bf16_f32 v19, v238, v239\n"
      "ds_write_b128 v225, v[16:19] offset:0\n"
      "v_mfma_f32_16x16x32_bf16 v[180:183], a[84:87], a[40:43], v[180:183]\n"
      "v_lshlrev_b32 v236, 16, v150\n"
      "v_and_b32 v237, s95, v150\n"
      "v_mul_f32 v234, s94, v234\n"
      "v_mul_f32 v235, s94, v235\n"
      "v_cvt_pk_bf16_f32 v20, v232, v233\n"
      "v_mfma_f32_16x16x32_bf16 v[188:191], a[84:87], a[44:47], v[188:191]\n"
      "v_lshlrev_b32 v238, 16, v151\n"
      "v_and_b32 v239, s95, v151\n"
      "v_mul_f32 v236, s94, v236\n"
      "v_mul_f32 v237, s94, v237\n"
      "v_cvt_pk_bf16_f32 v21, v234, v235\n"
      "v_mfma_f32_16x16x32_bf16 v[160:163], a[88:91], a[48:51], v[160:163]\n"
      "v_lshlrev_b32 v232, 16, v152\n"
      "v_and_b32 v233, s95, v152\n"
      "v_mul_f32 v238, s94, v238\n"
      "v_mul_f32 v239, s94, v239\n"
      "v_cvt_pk_bf16_f32 v22, v236, v237\n"
      "v_mfma_f32_16x16x32_bf16 v[168:171], a[88:91], a[52:55], v[168:171]\n"
      "v_lshlrev_b32 v234, 16, v153\n"
      "v_and_b32 v235, s95, v153\n"
      "v_mul_f32 v232, s94, v232\n"
      "v_mul_f32 v233, s94, v233\n"
      "v_cvt_pk_bf16_f32 v23, v238, v239\n"
      "ds_write_b128 v225, v[20:23] offset:4096\n"
      "v_mfma_f32_16x16x32_bf16 v[176:179], a[88:91], a[56:59], v[176:179]\n"
      "v_lshlrev_b32 v236, 16, v154\n"
      "v_and_b32 v237, s95, v154\n"
      "v_mul_f32 v234, s94, v234\n"
      "v_mul_f32 v235, s94, v235\n"
      "v_cvt_pk_bf16_f32 v24, v232, v233\n"
      "v_mfma_f32_16x16x32_bf16 v[184:187], a[88:91], a[60:63], v[184:187]\n"
      "v_lshlrev_b32 v238, 16, v155\n"
      "v_and_b32 v239, s95, v155\n"
      "v_mul_f32 v236, s94, v236\n"
      "v_mul_f32 v237, s94, v237\n"
      "v_cvt_pk_bf16_f32 v25, v234, v235\n"
      "v_mfma_f32_16x16x32_bf16 v[164:167], a[92:95], a[48:51], v[164:167]\n"
      "v_lshlrev_b32 v232, 16, v156\n"
      "v_and_b32 v233, s95, v156\n"
      "v_mul_f32 v238, s94, v238\n"
      "v_mul_f32 v239, s94, v239\n"
      "v_cvt_pk_bf16_f32 v26, v236, v237\n"
      "v_mfma_f32_16x16x32_bf16 v[172:175], a[92:95], a[52:55], v[172:175]\n"
      "v_lshlrev_b32 v234, 16, v157\n"
      "v_and_b32 v235, s95, v157\n"
      "v_mul_f32 v232, s94, v232\n"
      "v_mul_f32 v233, s94, v233\n"
      "v_cvt_pk_bf16_f32 v27, v238, v239\n"
      "ds_write_b128 v225, v[24:27] offset:8192\n"
      "v_mfma_f32_16x16x32_bf16 v[180:183], a[92:95], a[56:59], v[180:183]\n"
      "v_lshlrev_b32 v236, 16, v158\n"
      "v_and_b32 v237, s95, v158\n"
      "v_mul_f32 v234, s94, v234\n"
      "v_mul_f32 v235, s94, v235\n"
      "v_cvt_pk_bf16_f32 v28, v232, v233\n"
      "v_mfma_f32_16x16x32_bf16 v[188:191], a[92:95], a[60:63], v[188:191]\n"
      "v_lshlrev_b32 v238, 16, v159\n"
      "v_and_b32 v239, s95, v159\n"
      "v_mul_f32 v236, s94, v236\n"
      "v_mul_f32 v237, s94, v237\n"
      "v_cvt_pk_bf16_f32 v29, v234, v235\n"
      "v_mul_f32 v238, s94, v238\n"
      "v_mul_f32 v239, s94, v239\n"
      "v_cvt_pk_bf16_f32 v30, v236, v237\n"
      "v_cvt_pk_bf16_f32 v31, v238, v239\n"
      "ds_write_b128 v225, v[28:31] offset:12288\n"
      "v_max3_f32 v112, v160, v161, v162\n"
      "v_max3_f32 v113, v168, v169, v170\n"
      "v_max3_f32 v114, v176, v177, v178\n"
      "v_max3_f32 v115, v184, v185, v186\n"
      "v_max3_f32 v112, v112, v163, v164\n"
      "v_max3_f32 v113, v113, v171, v172\n"
      "v_max3_f32 v114, v114, v179, v180\n"
      "v_max3_f32 v115, v115, v187, v188\n"
      "v_max3_f32 v112, v112, v165, v166\n"
      "v_max3_f32 v113, v113, v173, v174\n"
      "v_max3_f32 v114, v114, v181, v182\n"
      "v_max3_f32 v115, v115, v189, v190\n"
      "s_waitcnt lgkmcnt(0)\n"
      "s_barrier\n"
      "ds_read_b128 a[0:3], v118 offset:0\n"
      "ds_read_b128 a[4:7], v118 offset:4096\n"
      "ds_read_b128 a[8:11], v118 offset:8192\n"
      "ds_read_b128 a[12:15], v118 offset:12288\n"
      "ds_read_b128 a[16:19], v119 offset:0\n"
      "ds_read_b128 a[20:23], v119 offset:4096\n"
      "ds_read_b128 a[24:27], v119 offset:8192\n"
      "ds_read_b128 a[28:31], v119 offset:12288\n"
      "ds_read_b128 a[32:35], v118 offset:1024\n"
      "ds_read_b128 a[36:39], v118 offset:5120\n"
      "ds_read_b128 a[40:43], v118 offset:9216\n"
      "ds_read_b128 a[44:47], v118 offset:13312\n"
      "v_max_f32 v112, v112, v167\n"
      "v_max_f32 v113, v113, v175\n"
      "v_max_f32 v114, v114, v183\n"
      "v_max_f32 v115, v115, v191\n"
      "v_mov_b32 v208, v112\n"
      "v_mov_b32 v209, v113\n"
      "v_mov_b32 v210, v114\n"
      "v_mov_b32 v211, v115\n"
      "v_permlane16_swap_b32 v112, v208\n"
      "v_permlane16_swap_b32 v113, v209\n"
      "v_permlane16_swap_b32 v114, v210\n"
      "v_permlane16_swap_b32 v115, v211\n"
      "s_waitcnt lgkmcnt(8)\n"
      "ds_read_b128 a[48:51], v119 offset:1024\n"
      "ds_read_b128 a[52:55], v119 offset:5120\n"
      "ds_read_b128 a[56:59], v119 offset:9216\n"
      "ds_read_b128 a[60:63], v119 offset:13312\n"
      "v_max_f32 v112, v112, v208\n"
      "v_max_f32 v113, v113, v209\n"
      "v_max_f32 v114, v114, v210\n"
      "v_max_f32 v115, v115, v211\n"
      "v_mov_b32 v208, v112\n"
      "v_mov_b32 v209, v113\n"
      "v_mov_b32 v210, v114\n"
      "v_mov_b32 v211, v115\n"
      "v_permlane32_swap_b32 v112, v208\n"
      "v_permlane32_swap_b32 v113, v209\n"
      "v_permlane32_swap_b32 v114, v210\n"
      "v_permlane32_swap_b32 v115, v211\n"
      "v_max_f32 v112, v112, v208\n"
      "v_max_f32 v113, v113, v209\n"
      "v_max_f32 v114, v114, v210\n"
      "v_max_f32 v115, v115, v211\n"
      "v_mov_b32 v116, 0xc1800000\n"
      "v_fmamk_f32 v112, v112, 0xbe0293ee, v116\n"
      "v_fmamk_f32 v113, v113, 0xbe0293ee, v116\n"
      "v_fmamk_f32 v114, v114, 0xbe0293ee, v116\n"
      "v_fmamk_f32 v115, v115, 0xbe0293ee, v116\n"
      "v_floor_f32 v112, v112\n"
      "v_floor_f32 v113, v113\n"
      "v_floor_f32 v114, v114\n"
      "v_floor_f32 v115, v115\n"
      "s_waitcnt vmcnt(10) lgkmcnt(0)\n"
      "s_barrier\n"
      "ds_read_b128 a[64:67], v242 offset:0\n"
      "ds_read_b128 a[68:71], v242 offset:4096\n"
      "ds_read_b128 a[72:75], v243 offset:0\n"
      "ds_read_b128 a[76:79], v243 offset:4096\n"
      "ds_read_b128 a[80:83], v242 offset:1024\n"
      "ds_read_b128 a[84:87], v242 offset:5120\n"
      "ds_read_b128 a[88:91], v243 offset:1024\n"
      "ds_read_b128 a[92:95], v243 offset:5120\n"
      "v_min_f32 v224, 0x7f000000, v112\n"
      "v_min_f32 v228, 0x7f000000, v113\n"
      "v_min_f32 v232, 0x7f000000, v114\n"
      "v_min_f32 v236, 0x7f000000, v115\n"
      "v_add_f32 v112, %[neg], v224\n"
      "v_add_f32 v113, %[neg], v228\n"
      "v_add_f32 v114, %[neg], v232\n"
      "v_add_f32 v115, %[neg], v236\n"
      "v_mov_b32 v225, v224\n"
      "v_mov_b32 v229, v228\n"
      "v_mov_b32 v233, v232\n"
      "v_mov_b32 v237, v236\n"
      "v_mov_b64 v[226:227], v[224:225]\n"
      "v_mov_b64 v[230:231], v[228:229]\n"
      "v_mov_b64 v[234:235], v[232:233]\n"
      "v_mov_b64 v[238:239], v[236:237]\n"
      "s_mov_b32 s95, s94\n"
      "v_readfirstlane_b32 s72, v240\n"
      "v_readfirstlane_b32 s79, v241\n"
      "s_add_u32 m0, s77, 16384\n"
      "v_pk_fma_f32 v[160:161], v[160:161], s[94:95], v[112:113] op_sel:[0,0,0] op_sel_hi:[1,1,0]\n"
      "buffer_load_dwordx4 v249, s[24:27], s61 offen nt lds\n"
      "v_pk_fma_f32 v[162:163], v[162:163], s[94:95], v[112:113] op_sel:[0,0,0] op_sel_hi:[1,1,0]\n"
      "v_pk_fma_f32 v[164:165], v[164:165], s[94:95], v[112:113] op_sel:[0,0,0] op_sel_hi:[1,1,0]\n"
      "v_pk_fma_f32 v[166:167], v[166:167], s[94:95], v[112:113] op_sel:[0,0,0] op_sel_hi:[1,1,0]\n"
      "s_add_u32 m0, s77, 17408\n"
      "v_pk_fma_f32 v[168:169], v[168:169], s[94:95], v[112:113] op_sel:[0,0,1] op_sel_hi:[1,1,1]\n"
      "buffer_load_dwordx4 v121, s[24:27], s61 offen nt lds\n"
      "v_pk_fma_f32 v[170:171], v[170:171], s[94:95], v[112:113] op_sel:[0,0,1] op_sel_hi:[1,1,1]\n"
      "v_pk_fma_f32 v[172:173], v[172:173], s[94:95], v[112:113] op_sel:[0,0,1] op_sel_hi:[1,1,1]\n"
      "v_pk_fma_f32 v[174:175], v[174:175], s[94:95], v[112:113] op_sel:[0,0,1] op_sel_hi:[1,1,1]\n"
      "s_add_u32 m0, s77, 24576\n"
      "v_pk_fma_f32 v[176:177], v[176:177], s[94:95], v[114:115] op_sel:[0,0,0] op_sel_hi:[1,1,0]\n"
      "buffer_load_dwordx4 v122, s[24:27], s61 offen nt lds\n"
      "v_pk_fma_f32 v[178:179], v[178:179], s[94:95], v[114:115] op_sel:[0,0,0] op_sel_hi:[1,1,0]\n"
      "v_pk_fma_f32 v[180:181], v[180:181], s[94:95], v[114:115] op_sel:[0,0,0] op_sel_hi:[1,1,0]\n"
      "v_pk_fma_f32 v[182:183], v[182:183], s[94:95], v[114:115] op_sel:[0,0,0] op_sel_hi:[1,1,0]\n"
      "s_add_u32 m0, s77, 25600\n"
      "v_pk_fma_f32 v[184:185], v[184:185], s[94:95], v[114:115] op_sel:[0,0,1] op_sel_hi:[1,1,1]\n"
      "buffer_load_dwordx4 v123, s[24:27], s61 offen nt lds\n"
      "v_pk_fma_f32 v[186:187], v[186:187], s[94:95], v[114:115] op_sel:[0,0,1] op_sel_hi:[1,1,1]\n"
      "v_pk_fma_f32 v[188:189], v[188:189], s[94:95], v[114:115] op_sel:[0,0,1] op_sel_hi:[1,1,1]\n"
      "v_pk_fma_f32 v[190:191], v[190:191], s[94:95], v[114:115] op_sel:[0,0,1] op_sel_hi:[1,1,1]\n"
      "s_add_u32 s77, s77, 0x8000\n"
      "s_and_b32 s77, s77, 0x1ffff\n"
      "s_add_u32 s61, s61, %[inc]\n"
      "v_exp_f32 v112, v160\n"
      "v_exp_f32 v113, v161\n"
      "v_exp_f32 v114, v162\n"
      "v_cvt_pk_bf16_f32 v208, v112, v113\n"
      "v_exp_f32 v115, v163\n"
      "v_exp_f32 v112, v164\n"
      "v_cvt_pk_bf16_f32 v209, v114, v115\n"
      "v_exp_f32 v113, v165\n"
      "v_or_b32 v116, v208, v209\n"
      "v_exp_f32 v114, v166\n"
      "v_cvt_pk_bf16_f32 v210, v112, v113\n"
      "v_exp_f32 v115, v167\n"
      "v_exp_f32 v112, v168\n"
      "v_cvt_pk_bf16_f32 v211, v114, v115\n"
      "v_exp_f32 v113, v169\n"
      "v_or3_b32 v116, v116, v210, v211\n"
      "v_exp_f32 v114, v170\n"
      "v_cvt_pk_bf16_f32 v212, v112, v113\n"
      "v_exp_f32 v115, v171\n"
      "v_exp_f32 v112, v172\n"
      "v_cvt_pk_bf16_f32 v213, v114, v115\n"
      "v_exp_f32 v113, v173\n"
      "v_or3_b32 v116, v116, v212, v213\n"
      "s_waitcnt lgkmcnt(0)\n"
      "v_cvt_pk_bf16_f32 v214, v112, v113\n"
      "s_sub_u32 s63, %[n1], 1\n"
      "s_branch ENTRY_%=\n"
      "LOOP1_%=:\n"
      "s_waitcnt vmcnt(12) lgkmcnt(0)\n"
      "s_sub_u32 s67, s68, s90\n"
      "s_cmp_lt_i32 s67, 0\n"
      "s_cbranch_scc1 GW0_%=\n"
      "GX0_%=:\n"
      "s_barrier\n"
      "v_mfma_f32_16x16x32_bf16 v[40:43], a[96:99], v[216:219], v[40:43]\n"
      "s_memrealtime s[68:69]\n"
      "buffer_load_dword v250, v251, s[28:31], 0 offen sc1\n"
      "ds_read_b128 a[64:67], v242 offset:0\n"
      "v_exp_f32 v114, v166\n"
      "v_mfma_f32_16x16x32_bf16 v[44:47], a[96:99], v[220:223], v[44:47]\n"
      "s_add_u32 m0, s77, 16384\n"
      "ds_read_b128 a[68:71], v242 offset:4096\n"
      "ds_read_b128 a[72:75], v243 offset:0\n"
      "v_mfma_f32_16x16x32_bf16 v[60:63], a[100:103], v[220:223], v[60:63]\n"
      "buffer_load_dwordx4 v249, s[24:27], s61 offen nt lds\n"
      "v_exp_f32 v115, v167\n"
      "v_mfma_f32_16x16x32_bf16 v[56:59], a[100:103], v[216:219], v[56:59]\n"
      "s_add_u32 m0, s77, 17408\n"
      "ds_read_b128 a[76:79], v243 offset:4096\n"
      "v_cvt_pk_bf16_f32 v211, v114, v115\n"
      "v_mfma_f32_16x16x32_bf16 a[136:139], a[104:107], v[216:219], a[136:139]\n"
      "buffer_load_dwordx4 v121, s[24:27], s61 offen nt lds\n"
      "v_exp_f32 v112, v168\n"
      "v_mfma_f32_16x16x32_bf16 a[140:143], a[104:107], v[220:223], a[140:143]\n"
      "s_add_u32 m0, s77, 24576\n"
      "ds_read_b128 a[80:83], v242 offset:1024\n"
      "v_or3_b32 v116, v116, v210, v211\n"
      "v_mfma_f32_16x16x32_bf16 a[156:159], a[108:111], v[220:223], a[156:159]\n"
      "buffer_load_dwordx4 v122, s[24:27], s61 offen nt lds\n"
      "v_exp_f32 v113, v169\n"
      "v_mfma_f32_16x16x32_bf16 a[152:155], a[108:111], v[216:219], a[152:155]\n"
      "s_add_u32 m0, s77, 25600\n"
      "ds_read_b128 a[84:87], v242 offset:5120\n"
      "v_cvt_pk_bf16_f32 v212, v112, v113\n"
      "v_mfma_f32_16x16x32_bf16 v[72:75], a[112:115], v[216:219], v[72:75]\n"
      "s_add_u32 s77, s77, 0x8000\n"
      "buffer_load_dwordx4 v123, s[24:27], s61 offen nt lds\n"
      "v_exp_f32 v114, v170\n"
      "v_mfma_f32_16x16x32_bf16 v[76:79], a[112:115], v[220:223], v[76:79]\n"
      "s_and_b32 s77, s77, 0x1ffff\n"
      "ds_read_b128 a[88:91], v243 offset:1024\n"
      "ds_read_b128 a[92:95], v243 offset:5120\n"
      "v_mfma_f32_16x16x32_bf16 v[92:95], a[116:119], v[220:223], v[92:95]\n"
      "s_add_u32 s61, s61, %[inc]\n"
      "v_exp_f32 v115, v171\n"
      "v_mfma_f32_16x16x32_bf16 v[88:91], a[116:119], v[216:219], v[88:91]\n"
      "v_cvt_pk_bf16_f32 v213, v114, v115\n"
      "v_mfma_f32_16x16x32_bf16 a[168:171], a[120:123], v[216:219], a[168:171]\n"
      "v_exp_f32 v112, v172\n"
      "v_mfma_f32_16x16x32_bf16 a[172:175], a[120:123], v[220:223], a[172:175]\n"
      "v_or3_b32 v116, v116, v212, v213\n"
      "v_mfma_f32_16x16x32_bf16 a[188:191], a[124:127], v[220:223], a[188:191]\n"
      "v_exp_f32 v113, v173\n"
      "v_mfma_f32_16x16x32_bf16 a[184:187], a[124:127], v[216:219], a[184:187]\n"
      "s_waitcnt lgkmcnt(0)\n"
      "v_cvt_pk_bf16_f32 v214, v112, v113\n"
      "ENTRY_%=:\n"
      "v_mfma_f32_16x16x32_bf16 v[128:131], a[64:67], a[0:3], v[224:227]\n"
      "s_sub_u32 s67, s68, s66\n"
      "v_exp_f32 v114, v174\n"
      "v_mfma_f32_16x16x32_bf16 v[128:131], a[72:75], a[16:19], v[128:131]\n"
      "s_cmp_gt_i32 s67, s70\n"
      "ds_read_b64_tr_b16 a[96:97], v244 offset:0\n"
      "ds_read_b64_tr_b16 a[98:99], v244 offset:4096\n"
      "v_mfma_f32_16x16x32_bf16 v[128:131], a[80:83], a[32:35], v[128:131]\n"
      "s_cselect_b32 s69, s86, 0\n"
      "v_exp_f32 v115, v175\n"
      "v_mfma_f32_16x16x32_bf16 v[128:131], a[88:91], a[48:51], v[128:131]\n"
      "s_add_u32 s66, s66, s69\n"
      "ds_read_b64_tr_b16 a[100:101], v245 offset:0\n"
      "v_cvt_pk_bf16_f32 v215, v114, v115\n"
      "v_mfma_f32_16x16x32_bf16 v[132:135], a[92:95], a[48:51], v[224:227]\n"
      "s_add_u32 s67, s67, s101\n"
      "v_exp_f32 v112, v176\n"
      "v_mfma_f32_16x16x32_bf16 v[132:135], a[84:87], a[32:35], v[132:135]\n"
      "s_min_i32 s70, s70, s67\n"
      "ds_read_b64_tr_b16 a[102:103], v245 offset:4096\n"
      "v_or3_b32 v116, v116, v214, v215\n"
      "v_mfma_f32_16x16x32_bf16 v[132:135], a[76:79], a[16:19], v[132:135]\n"
      "s_max_i32 s70, s70, 85\n"
      "v_exp_f32 v113, v177\n"
      "v_mfma_f32_16x16x32_bf16 v[132:135], a[68:71], a[0:3], v[132:135]\n"
      "s_add_u32 s66, s66, s86\n"
      "ds_read_b64_tr_b16 a[104:105], v246 offset:0\n"
      "v_cvt_pk_bf16_f32 v216, v112, v113\n"
      "v_mfma_f32_16x16x32_bf16 v[140:143], a[68:71], a[4:7], v[228:231]\n"
      "s_and_b32 s76, s80, 31\n"
      "v_exp_f32 v114, v178\n"
      "v_mfma_f32_16x16x32_bf16 v[140:143], a[76:79], a[20:23], v[140:143]\n"
      "s_lshr_b64 s[80:81], s[80:81], 5\n"
      "ds_read_b64_tr_b16 a[106:107], v246 offset:4096\n"
      "ds_read_b64_tr_b16 a[108:109], v247 offset:0\n"
      "v_mfma_f32_16x16x32_bf16 v[140:143], a[84:87], a[36:39], v[140:143]\n"
      "s_add_u32 s66, s66, s76\n"
      "v_exp_f32 v115, v179\n"
      "v_mfma_f32_16x16x32_bf16 v[140:143], a[92:95], a[52:55], v[140:143]\n"
      "s_sub_u32 s67, s68, s82\n"
      "ds_read_b64_tr_b16 a[110:111], v247 offset:4096\n"
      "v_cvt_pk_bf16_f32 v217, v114, v115\n"
      "v_mfma_f32_16x16x32_bf16 v[136:139], a[88:91], a[52:55], v[228:231]\n"
      "s_min_i32 s76, s67, s83\n"
      "v_exp_f32 v112, v180\n"
      "v_mfma_f32_16x16x32_bf16 v[136:139], a[80:83], a[36:39], v[136:139]\n"
      "s_mov_b32 s83, s67\n"
      "ds_read_b64_tr_b16 a[112:113], v244 offset:1024\n"
      "v_or3_b32 v116, v116, v216, v217\n"
      "v_mfma_f32_16x16x32_bf16 v[136:139], a[72:75], a[20:23], v[136:139]\n"
      "s_sub_u32 s76, s76, 36\n"
      "v_exp_f32 v113, v181\n"
      "v_mfma_f32_16x16x32_bf16 v[136:139], a[64:67], a[4:7], v[136:139]\n"
      "s_min_i32 s76, s76, 148\n"
      "ds_read_b64_tr_b16 a[114:115], v244 offset:5120\n"
      "v_cvt_pk_bf16_f32 v218, v112, v113\n"
      "v_mfma_f32_16x16x32_bf16 v[144:147], a[64:67], a[8:11], v[232:235]\n"
      "s_max_i32 s76, s76, 70\n"
      "v_exp_f32 v114, v182\n"
      "v_mfma_f32_16x16x32_bf16 v[144:147], a[72:75], a[24:27], v[144:147]\n"
      "s_mov_b32 s82, s68\n"
      "ds_read_b64_tr_b16 a[116:117], v245 offset:1024\n"
      "ds_read_b64_tr_b16 a[118:119], v245 offset:5120\n"
      "v_mfma_f32_16x16x32_bf16 v[144:147], a[80:83], a[40:43], v[144:147]\n"
      "s_add_u32 s65, s82, s76\n"
      "v_exp_f32 v115, v183\n"
      "v_mfma_f32_16x16x32_bf16 v[144:147], a[88:91], a[56:59], v[144:147]\n"
      "s_sub_u32 s93, s93, 1\n"
      "ds_read_b64_tr_b16 a[120:121], v246 offset:1024\n"
      "v_cvt_pk_bf16_f32 v219, v114, v115\n"
      "v_mfma_f32_16x16x32_bf16 v[148:151], a[92:95], a[56:59], v[232:235]\n"
      "s_cmp_le_i32 s93, 14\n"
      "v_exp_f32 v112, v184\n"
      "v_mfma_f32_16x16x32_bf16 v[148:151], a[84:87], a[40:43], v[148:151]\n"
      "s_cselect_b32 s86, 107, s86\n"
      "ds_read_b64_tr_b16 a[122:123], v246 offset:5120\n"
      "v_or3_b32 v116, v116, v218, v219\n"
      "v_mfma_f32_16x16x32_bf16 v[148:151], a[76:79], a[24:27], v[148:151]\n"
      "s_sub_u32 s67, s65, s66\n"
      "v_exp_f32 v113, v185\n"
      "v_mfma_f32_16x16x32_bf16 v[148:151], a[68:71], a[8:11], v[148:151]\n"
      "s_cmp_lt_i32 s67, 0\n"
      "ds_read_b64_tr_b16 a[124:125], v247 offset:1024\n"
      "v_cvt_pk_bf16_f32 v220, v112, v113\n"
      "v_mfma_f32_16x16x32_bf16 v[156:159], a[68:71], a[12:15], v[236:239]\n"
      "s_cselect_b32 s88, s66, s65\n"
      "v_exp_f32 v114, v186\n"
      "v_mfma_f32_16x16x32_bf16 v[156:159], a[76:79], a[28:31], v[156:159]\n"
      "s_add_u32 s90, s88, s91\n"
      "ds_read_b64_tr_b16 a[126:127], v247 offset:5120\n"
      "v_mfma_f32_16x16x32_bf16 v[156:159], a[84:87], a[44:47], v[156:159]\n"
      "v_exp_f32 v115, v187\n"
      "v_mfma_f32_16x16x32_bf16 v[156:159], a[92:95], a[60:63], v[156:159]\n"
      "v_cvt_pk_bf16_f32 v221, v114, v115\n"
      "v_mfma_f32_16x16x32_bf16 v[152:155], a[88:91], a[60:63], v[236:239]\n"
      "v_exp_f32 v112, v188\n"
      "v_mfma_f32_16x16x32_bf16 v[152:155], a[80:83], a[44:47], v[152:155]\n"
      "v_or3_b32 v116, v116, v220, v221\n"
      "v_mfma_f32_16x16x32_bf16 v[152:155], a[72:75], a[28:31], v[152:155]\n"
      "v_exp_f32 v113, v189\n"
      "v_mfma_f32_16x16x32_bf16 v[152:155], a[64:67], a[12:15], v[152:155]\n"
      "s_waitcnt lgkmcnt(10)\n"
      "v_cvt_pk_bf16_f32 v222, v112, v113\n"
      "v_mfma_f32_16x16x32_bf16 v[32:35], a[96:99], v[192:195], v[32:35]\n"
      "v_exp_f32 v114, v190\n"
      "v_mfma_f32_16x16x32_bf16 v[48:51], a[100:103], v[192:195], v[48:51]\n"
      "v_mfma_f32_16x16x32_bf16 a[128:131], a[104:107], v[192:195], a[128:131]\n"
      "s_waitcnt lgkmcnt(6)\n"
      "v_exp_f32 v115, v191\n"
      "v_mfma_f32_16x16x32_bf16 a[144:147], a[108:111], v[192:195], a[144:147]\n"
      "v_cvt_pk_bf16_f32 v223, v114, v115\n"
      "v_mfma_f32_16x16x32_bf16 v[64:67], a[112:115], v[192:195], v[64:67]\n"
      "s_waitcnt lgkmcnt(2)\n"
      "v_mfma_f32_16x16x32_bf16 v[80:83], a[116:119], v[192:195], v[80:83]\n"
      "v_or3_b32 v116, v116, v222, v223\n"
      "v_mfma_f32_16x16x32_bf16 a[160:163], a[120:123], v[192:195], a[160:163]\n"
      "s_mov_b32 s64, 0\n"
      "s_waitcnt lgkmcnt(0)\n"
      "v_and_b32 v116, 0x40004000, v116\n"
      "v_mfma_f32_16x16x32_bf16 a[176:179], a[124:127], v[192:195], a[176:179]\n"
      "v_cmp_ne_u32 vcc, 0, v116\n"
      "s_cbranch_vccnz FB1_%=\n"
      "RET1_0_%=:\n"
      "v_mfma_f32_16x16x32_bf16 a[180:183], a[124:127], v[196:199], a[180:183]\n"
      "v_exp_f32 v112, v128\n"
      "s_barrier\n"
      "v_mfma_f32_16x16x32_bf16 a[164:167], a[120:123], v[196:199], a[164:167]\n"
      "v_mfma_f32_16x16x32_bf16 a[192:195], a[196:199], v[208:211], a[192:195]\n"
      "s_add_u32 m0, s62, 0\n"
      "v_exp_f32 v113, v129\n"
      "v_mfma_f32_16x16x32_bf16 v[84:87], a[116:119], v[196:199], v[84:87]\n"
      "buffer_load_dwordx4 v248, s[20:23], s60 offen nt lds\n"
      "v_cvt_pk_bf16_f32 v192, v112, v113\n"
      "v_mfma_f32_16x16x32_bf16 v[68:71], a[112:115], v[196:199], v[68:71]\n"
      "s_add_u32 m0, s62, 896\n"
      "v_exp_f32 v114, v130\n"
      "v_mfma_f32_16x16x32_bf16 a[192:195], a[200:203], v[212:215], a[192:195]\n"
      "buffer_load_dwordx4 v248, s[20:23], s60 offen offset:128 nt lds\n"
      "v_mfma_f32_16x16x32_bf16 a[148:151], a[108:111], v[196:199], a[148:151]\n"
      "s_add_u32 m0, s62, 8192\n"
      "v_exp_f32 v115, v131\n"
      "v_mfma_f32_16x16x32_bf16 a[132:135], a[104:107], v[196:199], a[132:135]\n"
      "s_memrealtime s[68:69]\n"
      "buffer_load_dwordx4 v120, s[20:23], s60 offen nt lds\n"
      "v_cvt_pk_bf16_f32 v193, v114, v115\n"
      "v_mfma_f32_16x16x32_bf16 a[192:195], a[204:207], v[216:219], a[192:195]\n"
      "s_add_u32 m0, s62, 9088\n"
      "v_exp_f32 v112, v132\n"
      "v_mfma_f32_16x16x32_bf16 v[52:55], a[100:103], v[196:199], v[52:55]\n"
      "s_add_u32 s62, s62, 0x8000\n"
      "buffer_load_dwordx4 v120, s[20:23], s60 offen offset:128 nt lds\n"
      "v_or_b32 v116, v192, v193\n"
      "v_mfma_f32_16x16x32_bf16 v[36:39], a[96:99], v[196:199], v[36:39]\n"
      "s_and_b32 s62, s62, 0x1ffff\n"
      "v_exp_f32 v113, v133\n"
      "v_mfma_f32_16x16x32_bf16 a[192:195], a[208:211], v[220:223], a[192:195]\n"
      "s_add_u32 s60, s60, s78\n"
      "v_cvt_pk_bf16_f32 v194, v112, v113\n"
      "s_waitcnt vmcnt(12) lgkmcnt(0)\n"
      "s_sub_u32 s67, s68, s90\n"
      "s_cmp_lt_i32 s67, 0\n"
      "s_cbranch_scc1 GW1_%=\n"
      "GX1_%=:\n"
      "s_barrier\n"
      "v_mfma_f32_16x16x32_bf16 v[40:43], a[96:99], v[200:203], v[40:43]\n"
      "s_memrealtime s[68:69]\n"
      "ds_read_b128 a[64:67], v242 offset:32768\n"
      "v_exp_f32 v114, v134\n"
      "v_mfma_f32_16x16x32_bf16 v[44:47], a[96:99], v[204:207], v[44:47]\n"
      "s_add_u32 m0, s77, 16384\n"
      "ds_read_b128 a[68:71], v242 offset:36864\n"
      "ds_read_b128 a[72:75], v243 offset:32768\n"
      "v_mfma_f32_16x16x32_bf16 v[60:63], a[100:103], v[204:207], v[60:63]\n"
      "buffer_load_dwordx4 v249, s[24:27], s61 offen nt lds\n"
      "v_exp_f32 v115, v135\n"
      "v_mfma_f32_16x16x32_bf16 v[56:59], a[100:103], v[200:203], v[56:59]\n"
      "s_add_u32 m0, s77, 17408\n"
      "ds_read_b128 a[76:79], v243 offset:36864\n"
      "v_cvt_pk_bf16_f32 v195, v114, v115\n"
      "v_mfma_f32_16x16x32_bf16 a[136:139], a[104:107], v[200:203], a[136:139]\n"
      "buffer_load_dwordx4 v121, s[24:27], s61 offen nt lds\n"
      "v_exp_f32 v112, v136\n"
      "v_mfma_f32_16x16x32_bf16 a[140:143], a[104:107], v[204:207], a[140:143]\n"
      "s_add_u32 m0, s77, 24576\n"
      "ds_read_b128 a[80:83], v242 offset:33792\n"
      "v_or3_b32 v116, v116, v194, v195\n"
      "v_mfma_f32_16x16x32_bf16 a[156:159], a[108:111], v[204:207], a[156:159]\n"
      "buffer_load_dwordx4 v122, s[24:27], s61 offen nt lds\n"
      "v_exp_f32 v113, v137\n"
      "v_mfma_f32_16x16x32_bf16 a[152:155], a[108:111], v[200:203], a[152:155]\n"
      "s_add_u32 m0, s77, 25600\n"
      "ds_read_b128 a[84:87], v242 offset:37888\n"
      "v_cvt_pk_bf16_f32 v196, v112, v113\n"
      "v_mfma_f32_16x16x32_bf16 v[72:75], a[112:115], v[200:203], v[72:75]\n"
      "s_add_u32 s77, s77, 0x8000\n"
      "buffer_load_dwordx4 v123, s[24:27], s61 offen nt lds\n"
      "v_exp_f32 v114, v138\n"
      "v_mfma_f32_16x16x32_bf16 v[76:79], a[112:115], v[204:207], v[76:79]\n"
      "s_and_b32 s77, s77, 0x1ffff\n"
      "ds_read_b128 a[88:91], v243 offset:33792\n"
      "ds_read_b128 a[92:95], v243 offset:37888\n"
      "v_mfma_f32_16x16x32_bf16 v[92:95], a[116:119], v[204:207], v[92:95]\n"
      "s_add_u32 s61, s61, s78\n"
      "v_exp_f32 v115, v139\n"
      "v_mfma_f32_16x16x32_bf16 v[88:91], a[116:119], v[200:203], v[88:91]\n"
      "v_cvt_pk_bf16_f32 v197, v114, v115\n"
      "v_mfma_f32_16x16x32_bf16 a[168:171], a[120:123], v[200:203], a[168:171]\n"
      "v_exp_f32 v112, v140\n"
      "v_mfma_f32_16x16x32_bf16 a[172:175], a[120:123], v[204:207], a[172:175]\n"
      "v_or3_b32 v116, v116, v196, v197\n"
      "v_mfma_f32_16x16x32_bf16 a[188:191], a[124:127], v[204:207], a[188:191]\n"
      "v_exp_f32 v113, v141\n"
      "v_mfma_f32_16x16x32_bf16 a[184:187], a[124:127], v[200:203], a[184:187]\n"
      "s_waitcnt lgkmcnt(0)\n"
      "v_cvt_pk_bf16_f32 v198, v112, v113\n"
      "v_mfma_f32_16x16x32_bf16 v[160:163], a[64:67], a[0:3], v[224:227]\n"
      "s_sub_u32 s67, s68, s66\n"
      "v_exp_f32 v114, v142\n"
      "v_mfma_f32_16x16x32_bf16 v[160:163], a[72:75], a[16:19], v[160:163]\n"
      "s_cmp_gt_i32 s67, s70\n"
      "ds_read_b64_tr_b16 a[96:97], v244 offset:32768\n"
      "ds_read_b64_tr_b16 a[98:99], v244 offset:36864\n"
      "v_mfma_f32_16x16x32_bf16 v[160:163], a[80:83], a[32:35], v[160:163]\n"
      "s_cselect_b32 s69, s86, 0\n"
      "v_exp_f32 v115, v143\n"
      "v_mfma_f32_16x16x32_bf16 v[160:163], a[88:91], a[48:51], v[160:163]\n"
      "s_add_u32 s66, s66, s69\n"
      "ds_read_b64_tr_b16 a[100:101], v245 offset:32768\n"
      "v_cvt_pk_bf16_f32 v199, v114, v115\n"
      "v_mfma_f32_16x16x32_bf16 v[164:167], a[92:95], a[48:51], v[224:227]\n"
      "s_add_u32 s67, s67, s101\n"
      "v_exp_f32 v112, v144\n"
      "v_mfma_f32_16x16x32_bf16 v[164:167], a[84:87], a[32:35], v[164:167]\n"
      "s_min_i32 s70, s70, s67\n"
      "ds_read_b64_tr_b16 a[102:103], v245 offset:36864\n"
      "v_or3_b32 v116, v116, v198, v199\n"
      "v_mfma_f32_16x16x32_bf16 v[164:167], a[76:79], a[16:19], v[164:167]\n"
      "s_max_i32 s70, s70, 85\n"
      "v_exp_f32 v113, v145\n"
      "v_mfma_f32_16x16x32_bf16 v[164:167], a[68:71], a[0:3], v[164:167]\n"
      "s_add_u32 s66, s66, s86\n"
      "ds_read_b64_tr_b16 a[104:105], v246 offset:32768\n"
      "v_cvt_pk_bf16_f32 v200, v112, v113\n"
      "v_mfma_f32_16x16x32_bf16 v[172:175], a[68:71], a[4:7], v[228:231]\n"
      "s_and_b32 s76, s80, 31\n"
      "v_exp_f32 v114, v146\n"
      "v_mfma_f32_16x16x32_bf16 v[172:175], a[76:79], a[20:23], v[172:175]\n"
      "s_lshr_b64 s[80:81], s[80:81], 5\n"
      "ds_read_b64_tr_b16 a[106:107], v246 offset:36864\n"
      "ds_read_b64_tr_b16 a[108:109], v247 offset:32768\n"
      "v_mfma_f32_16x16x32_bf16 v[172:175], a[84:87], a[36:39], v[172:175]\n"
      "s_add_u32 s66, s66, s76\n"
      "v_exp_f32 v115, v147\n"
      "v_mfma_f32_16x16x32_bf16 v[172:175], a[92:95], a[52:55], v[172:175]\n"
      "s_sub_u32 s67, s68, s82\n"
      "ds_read_b64_tr_b16 a[110:111], v247 offset:36864\n"
      "v_cvt_pk_bf16_f32 v201, v114, v115\n"
      "v_mfma_f32_16x16x32_bf16 v[168:171], a[88:91], a[52:55], v[228:231]\n"
      "s_min_i32 s76, s67, s83\n"
      "v_exp_f32 v112, v148\n"
      "v_mfma_f32_16x16x32_bf16 v[168:171], a[80:83], a[36:39], v[168:171]\n"
      "s_mov_b32 s83, s67\n"
      "ds_read_b64_tr_b16 a[112:113], v244 offset:33792\n"
      "v_or3_b32 v116, v116, v200, v201\n"
      "v_mfma_f32_16x16x32_bf16 v[168:171], a[72:75], a[20:23], v[168:171]\n"
      "s_sub_u32 s76, s76, 36\n"
      "v_exp_f32 v113, v149\n"
      "v_mfma_f32_16x16x32_bf16 v[168:171], a[64:67], a[4:7], v[168:171]\n"
      "s_min_i32 s76, s76, 148\n"
      "ds_read_b64_tr_b16 a[114:115], v244 offset:37888\n"
      "v_cvt_pk_bf16_f32 v202, v112, v113\n"
      "v_mfma_f32_16x16x32_bf16 v[176:179], a[64:67], a[8:11], v[232:235]\n"
      "s_max_i32 s76, s76, 70\n"
      "v_exp_f32 v114, v150\n"
      "v_mfma_f32_16x16x32_bf16 v[176:179], a[72:75], a[24:27], v[176:179]\n"
      "s_mov_b32 s82, s68\n"
      "ds_read_b64_tr_b16 a[116:117], v245 offset:33792\n"
      "ds_read_b64_tr_b16 a[118:119], v245 offset:37888\n"
      "v_mfma_f32_16x16x32_bf16 v[176:179], a[80:83], a[40:43], v[176:179]\n"
      "s_add_u32 s65, s82, s76\n"
      "v_exp_f32 v115, v151\n"
      "v_mfma_f32_16x16x32_bf16 v[176:179], a[88:91], a[56:59], v[176:179]\n"
      "s_sub_u32 s93, s93, 1\n"
      "ds_read_b64_tr_b16 a[120:121], v246 offset:33792\n"
      "v_cvt_pk_bf16_f32 v203, v114, v115\n"
      "v_mfma_f32_16x16x32_bf16 v[180:183], a[92:95], a[56:59], v[232:235]\n"
      "s_cmp_le_i32 s93, 14\n"
      "v_exp_f32 v112, v152\n"
      "v_mfma_f32_16x16x32_bf16 v[180:183], a[84:87], a[40:43], v[180:183]\n"
      "s_cselect_b32 s86, 107, s86\n"
      "ds_read_b64_tr_b16 a[122:123], v246 offset:37888\n"
      "v_or3_b32 v116, v116, v202, v203\n"
      "v_mfma_f32_16x16x32_bf16 v[180:183], a[76:79], a[24:27], v[180:183]\n"
      "s_sub_u32 s67, s65, s66\n"
      "v_exp_f32 v113, v153\n"
      "v_mfma_f32_16x16x32_bf16 v[180:183], a[68:71], a[8:11], v[180:183]\n"
      "s_cmp_lt_i32 s67, 0\n"
      "ds_read_b64_tr_b16 a[124:125], v247 offset:33792\n"
      "v_cvt_pk_bf16_f32 v204, v112, v113\n"
      "v_mfma_f32_16x16x32_bf16 v[188:191], a[68:71], a[12:15], v[236:239]\n"
      "s_cselect_b32 s88, s66, s65\n"
      "v_exp_f32 v114, v154\n"
      "v_mfma_f32_16x16x32_bf16 v[188:191], a[76:79], a[28:31], v[188:191]\n"
      "s_add_u32 s90, s88, s91\n"
      "ds_read_b64_tr_b16 a[126:127], v247 offset:37888\n"
      "v_mfma_f32_16x16x32_bf16 v[188:191], a[84:87], a[44:47], v[188:191]\n"
      "v_exp_f32 v115, v155\n"
      "v_mfma_f32_16x16x32_bf16 v[188:191], a[92:95], a[60:63], v[188:191]\n"
      "v_cvt_pk_bf16_f32 v205, v114, v115\n"
      "v_mfma_f32_16x16x32_bf16 v[184:187], a[88:91], a[60:63], v[236:239]\n"
      "v_exp_f32 v112, v156\n"
      "v_mfma_f32_16x16x32_bf16 v[184:187], a[80:83], a[44:47], v[184:187]\n"
      "v_or3_b32 v116, v116, v204, v205\n"
      "v_mfma_f32_16x16x32_bf16 v[184:187], a[72:75], a[28:31], v[184:187]\n"
      "v_exp_f32 v113, v157\n"
      "v_mfma_f32_16x16x32_bf16 v[184:187], a[64:67], a[12:15], v[184:187]\n"
      "s_waitcnt lgkmcnt(10)\n"
      "v_cvt_pk_bf16_f32 v206, v112, v113\n"
      "v_mfma_f32_16x16x32_bf16 v[32:35], a[96:99], v[208:211], v[32:35]\n"
      "v_exp_f32 v114, v158\n"
      "v_mfma_f32_16x16x32_bf16 v[48:51], a[100:103], v[208:211], v[48:51]\n"
      "v_mfma_f32_16x16x32_bf16 a[128:131], a[104:107], v[208:211], a[128:131]\n"
      "s_waitcnt lgkmcnt(6)\n"
      "v_exp_f32 v115, v159\n"
      "v_mfma_f32_16x16x32_bf16 a[144:147], a[108:111], v[208:211], a[144:147]\n"
      "v_cvt_pk_bf16_f32 v207, v114, v115\n"
      "v_mfma_f32_16x16x32_bf16 v[64:67], a[112:115], v[208:211], v[64:67]\n"
      "s_waitcnt lgkmcnt(2)\n"
      "v_mfma_f32_16x16x32_bf16 v[80:83], a[116:119], v[208:211], v[80:83]\n"
      "v_or3_b32 v116, v116, v206, v207\n"
      "v_mfma_f32_16x16x32_bf16 a[160:163], a[120:123], v[208:211], a[160:163]\n"
      "s_mov_b32 s64, 0\n"
      "s_waitcnt lgkmcnt(0)\n"
      "v_and_b32 v116, 0x40004000, v116\n"
      "v_mfma_f32_16x16x32_bf16 a[176:179], a[124:127], v[208:211], a[176:179]\n"
      "v_cmp_ne_u32 vcc, 0, v116\n"
      "s_cbranch_vccnz FB0_%=\n"
      "RET0_0_%=:\n"
      "v_mfma_f32_16x16x32_bf16 a[180:183], a[124:127], v[212:215], a[180:183]\n"
      "v_exp_f32 v112, v160\n"
      "s_barrier\n"
      "v_mfma_f32_16x16x32_bf16 a[164:167], a[120:123], v[212:215], a[164:167]\n"
      "v_xor_b32 v242, 0x10000, v242\n"
      "v_mfma_f32_16x16x32_bf16 a[192:195], a[196:199], v[192:195], a[192:195]\n"
      "s_add_u32 m0, s62, 0\n"
      "v_exp_f32 v113, v161\n"
      "v_mfma_f32_16x16x32_bf16 v[84:87], a[116:119], v[212:215], v[84:87]\n"
      "buffer_load_dwordx4 v248, s[20:23], s60 offen nt lds\n"
      "v_cvt_pk_bf16_f32 v208, v112, v113\n"
      "v_xor_b32 v243, 0x10000, v243\n"
      "v_mfma_f32_16x16x32_bf16 v[68:71], a[112:115], v[212:215], v[68:71]\n"
      "s_add_u32 m0, s62, 896\n"
      "v_exp_f32 v114, v162\n"
      "v_mfma_f32_16x16x32_bf16 a[192:195], a[200:203], v[196:199], a[192:195]\n"
      "buffer_load_dwordx4 v248, s[20:23], s60 offen offset:128 nt lds\n"
      "v_xor_b32 v244, 0x10000, v244\n"
      "v_mfma_f32_16x16x32_bf16 a[148:151], a[108:111], v[212:215], a[148:151]\n"
      "s_add_u32 m0, s62, 8192\n"
      "v_exp_f32 v115, v163\n"
      "v_mfma_f32_16x16x32_bf16 a[132:135], a[104:107], v[212:215], a[132:135]\n"
      "s_memrealtime s[68:69]\n"
      "buffer_load_dwordx4 v120, s[20:23], s60 offen nt lds\n"
      "v_cvt_pk_bf16_f32 v209, v114, v115\n"
      "v_xor_b32 v245, 0x10000, v245\n"
      "v_mfma_f32_16x16x32_bf16 a[192:195], a[204:207], v[200:203], a[192:195]\n"
      "s_add_u32 m0, s62, 9088\n"
      "v_exp_f32 v112, v164\n"
      "v_mfma_f32_16x16x32_bf16 v[52:55], a[100:103], v[212:215], v[52:55]\n"
      "s_add_u32 s62, s62, 0x8000\n"
      "buffer_load_dwordx4 v120, s[20:23], s60 offen offset:128 nt lds\n"
      "v_or_b32 v116, v208, v209\n"
      "v_xor_b32 v246, 0x10000, v246\n"
      "v_mfma_f32_16x16x32_bf16 v[36:39], a[96:99], v[212:215], v[36:39]\n"
      "s_and_b32 s62, s62, 0x1ffff\n"
      "s_add_u32 s60, s60, %[inc]\n"
      "v_exp_f32 v113, v165\n"
      "v_mfma_f32_16x16x32_bf16 a[192:195], a[208:211], v[204:207], a[192:195]\n"
      "s_sub_u32 s63, s63, 1\n"
      "v_cvt_pk_bf16_f32 v210, v112, v113\n"
      "v_xor_b32 v247, 0x10000, v247\n"
      "s_cbranch_scc0 LOOP1_%=\n"
      "s_waitcnt vmcnt(12) lgkmcnt(0)\n"
      "s_sub_u32 s67, s68, s90\n"
      "s_cmp_lt_i32 s67, 0\n"
      "s_cbranch_scc1 GW2_%=\n"
      "GX2_%=:\n"
      "s_barrier\n"
      "v_mfma_f32_16x16x32_bf16 v[40:43], a[96:99], v[216:219], v[40:43]\n"
      "s_memrealtime s[68:69]\n"
      "buffer_load_dword v250, v251, s[28:31], 0 offen sc1\n"
      "ds_read_b128 a[64:67], v242 offset:0\n"
      "v_exp_f32 v114, v166\n"
      "v_mfma_f32_16x16x32_bf16 v[44:47], a[96:99], v[220:223], v[44:47]\n"
      "s_add_u32 m0, s77, 16384\n"
      "ds_read_b128 a[68:71], v242 offset:4096\n"
      "ds_read_b128 a[72:75], v243 offset:0\n"
      "v_mfma_f32_16x16x32_bf16 v[60:63], a[100:103], v[220:223], v[60:63]\n"
      "buffer_load_dwordx4 v249, s[24:27], s61 offen nt lds\n"
      "v_exp_f32 v115, v167\n"
      "v_mfma_f32_16x16x32_bf16 v[56:59], a[100:103], v[216:219], v[56:59]\n"
      "s_add_u32 m0, s77, 17408\n"
      "ds_read_b128 a[76:79], v243 offset:4096\n"
      "v_cvt_pk_bf16_f32 v211, v114, v115\n"
      "v_mfma_f32_16x16x32_bf16 a[136:139], a[104:107], v[216:219], a[136:139]\n"
      "buffer_load_dwordx4 v121, s[24:27], s61 offen nt lds\n"
      "v_exp_f32 v112, v168\n"
      "v_mfma_f32_16x16x32_bf16 a[140:143], a[104:107], v[220:223], a[140:143]\n"
      "s_add_u32 m0, s77, 24576\n"
      "ds_read_b128 a[80:83], v242 offset:1024\n"
      "v_or3_b32 v116, v116, v210, v211\n"
      "v_mfma_f32_16x16x32_bf16 a[156:159], a[108:111], v[220:223], a[156:159]\n"
      "buffer_load_dwordx4 v122, s[24:27], s61 offen nt lds\n"
      "v_exp_f32 v113, v169\n"
      "v_mfma_f32_16x16x32_bf16 a[152:155], a[108:111], v[216:219], a[152:155]\n"
      "s_add_u32 m0, s77, 25600\n"
      "ds_read_b128 a[84:87], v242 offset:5120\n"
      "v_cvt_pk_bf16_f32 v212, v112, v113\n"
      "v_mfma_f32_16x16x32_bf16 v[72:75], a[112:115], v[216:219], v[72:75]\n"
      "s_add_u32 s77, s77, 0x8000\n"
      "buffer_load_dwordx4 v123, s[24:27], s61 offen nt lds\n"
      "v_exp_f32 v114, v170\n"
      "v_mfma_f32_16x16x32_bf16 v[76:79], a[112:115], v[220:223], v[76:79]\n"
      "s_and_b32 s77, s77, 0x1ffff\n"
      "ds_read_b128 a[88:91], v243 offset:1024\n"
      "ds_read_b128 a[92:95], v243 offset:5120\n"
      "v_mfma_f32_16x16x32_bf16 v[92:95], a[116:119], v[220:223], v[92:95]\n"
      "s_add_u32 s61, s61, %[inc]\n"
      "v_exp_f32 v115, v171\n"
      "v_mfma_f32_16x16x32_bf16 v[88:91], a[116:119], v[216:219], v[88:91]\n"
      "v_cvt_pk_bf16_f32 v213, v114, v115\n"
      "v_mfma_f32_16x16x32_bf16 a[168:171], a[120:123], v[216:219], a[168:171]\n"
      "v_exp_f32 v112, v172\n"
      "v_mfma_f32_16x16x32_bf16 a[172:175], a[120:123], v[220:223], a[172:175]\n"
      "v_or3_b32 v116, v116, v212, v213\n"
      "v_mfma_f32_16x16x32_bf16 a[188:191], a[124:127], v[220:223], a[188:191]\n"
      "v_exp_f32 v113, v173\n"
      "v_mfma_f32_16x16x32_bf16 a[184:187], a[124:127], v[216:219], a[184:187]\n"
      "s_waitcnt lgkmcnt(0)\n"
      "v_cvt_pk_bf16_f32 v214, v112, v113\n"
      "v_mfma_f32_16x16x32_bf16 v[128:131], a[64:67], a[0:3], v[224:227]\n"
      "s_sub_u32 s67, s68, s66\n"
      "v_exp_f32 v114, v174\n"
      "v_mfma_f32_16x16x32_bf16 v[128:131], a[72:75], a[16:19], v[128:131]\n"
      "s_cmp_gt_i32 s67, s70\n"
      "ds_read_b64_tr_b16 a[96:97], v244 offset:0\n"
      "ds_read_b64_tr_b16 a[98:99], v244 offset:4096\n"
      "v_mfma_f32_16x16x32_bf16 v[128:131], a[80:83], a[32:35], v[128:131]\n"
      "s_cselect_b32 s69, s86, 0\n"
      "v_exp_f32 v115, v175\n"
      "v_mfma_f32_16x16x32_bf16 v[128:131], a[88:91], a[48:51], v[128:131]\n"
      "s_add_u32 s66, s66, s69\n"
      "ds_read_b64_tr_b16 a[100:101], v245 offset:0\n"
      "v_cvt_pk_bf16_f32 v215, v114, v115\n"
      "v_mfma_f32_16x16x32_bf16 v[132:135], a[92:95], a[48:51], v[224:227]\n"
      "s_add_u32 s67, s67, s101\n"
      "v_exp_f32 v112, v176\n"
      "v_mfma_f32_16x16x32_bf16 v[132:135], a[84:87], a[32:35], v[132:135]\n"
      "s_min_i32 s70, s70, s67\n"
      "ds_read_b64_tr_b16 a[102:103], v245 offset:4096\n"
      "v_or3_b32 v116, v116, v214, v215\n"
      "v_mfma_f32_16x16x32_bf16 v[132:135], a[76:79], a[16:19], v[132:135]\n"
      "s_max_i32 s70, s70, 85\n"
      "v_exp_f32 v113, v177\n"
      "v_mfma_f32_16x16x32_bf16 v[132:135], a[68:71], a[0:3], v[132:135]\n"
      "s_add_u32 s66, s66, s86\n"
      "ds_read_b64_tr_b16 a[104:105], v246 offset:0\n"
      "v_cvt_pk_bf16_f32 v216, v112, v113\n"
      "v_mfma_f32_16x16x32_bf16 v[140:143], a[68:71], a[4:7], v[228:231]\n"
      "s_and_b32 s76, s80, 31\n"
      "v_exp_f32 v114, v178\n"
      "v_mfma_f32_16x16x32_bf16 v[140:143], a[76:79], a[20:23], v[140:143]\n"
      "s_lshr_b64 s[80:81], s[80:81], 5\n"
      "ds_read_b64_tr_b16 a[106:107], v246 offset:4096\n"
      "ds_read_b64_tr_b16 a[108:109], v247 offset:0\n"
      "v_mfma_f32_16x16x32_bf16 v[140:143], a[84:87], a[36:39], v[140:143]\n"
      "s_add_u32 s66, s66, s76\n"
      "v_exp_f32 v115, v179\n"
      "v_mfma_f32_16x16x32_bf16 v[140:143], a[92:95], a[52:55], v[140:143]\n"
      "s_sub_u32 s67, s68, s82\n"
      "ds_read_b64_tr_b16 a[110:111], v247 offset:4096\n"
      "v_cvt_pk_bf16_f32 v217, v114, v115\n"
      "v_mfma_f32_16x16x32_bf16 v[136:139], a[88:91], a[52:55], v[228:231]\n"
      "s_min_i32 s76, s67, s83\n"
      "v_exp_f32 v112, v180\n"
      "v_mfma_f32_16x16x32_bf16 v[136:139], a[80:83], a[36:39], v[136:139]\n"
      "s_mov_b32 s83, s67\n"
      "ds_read_b64_tr_b16 a[112:113], v244 offset:1024\n"
      "v_or3_b32 v116, v116, v216, v217\n"
      "v_mfma_f32_16x16x32_bf16 v[136:139], a[72:75], a[20:23], v[136:139]\n"
      "s_sub_u32 s76, s76, 36\n"
      "v_exp_f32 v113, v181\n"
      "v_mfma_f32_16x16x32_bf16 v[136:139], a[64:67], a[4:7], v[136:139]\n"
      "s_min_i32 s76, s76, 148\n"
      "ds_read_b64_tr_b16 a[114:115], v244 offset:5120\n"
      "v_cvt_pk_bf16_f32 v218, v112, v113\n"
      "v_mfma_f32_16x16x32_bf16 v[144:147], a[64:67], a[8:11], v[232:235]\n"
      "s_max_i32 s76, s76, 70\n"
      "v_exp_f32 v114, v182\n"
      "v_mfma_f32_16x16x32_bf16 v[144:147], a[72:75], a[24:27], v[144:147]\n"
      "s_mov_b32 s82, s68\n"
      "ds_read_b64_tr_b16 a[116:117], v245 offset:1024\n"
      "ds_read_b64_tr_b16 a[118:119], v245 offset:5120\n"
      "v_mfma_f32_16x16x32_bf16 v[144:147], a[80:83], a[40:43], v[144:147]\n"
      "s_lshr_b32 s69, s86, 1\n"
      "v_exp_f32 v115, v183\n"
      "v_mfma_f32_16x16x32_bf16 v[144:147], a[88:91], a[56:59], v[144:147]\n"
      "s_add_u32 s66, s66, s69\n"
      "ds_read_b64_tr_b16 a[120:121], v246 offset:1024\n"
      "v_cvt_pk_bf16_f32 v219, v114, v115\n"
      "v_mfma_f32_16x16x32_bf16 v[148:151], a[92:95], a[56:59], v[232:235]\n"
      "s_add_u32 s82, s82, s69\n"
      "v_exp_f32 v112, v184\n"
      "v_mfma_f32_16x16x32_bf16 v[148:151], a[84:87], a[40:43], v[148:151]\n"
      "s_add_u32 s65, s82, s76\n"
      "ds_read_b64_tr_b16 a[122:123], v246 offset:5120\n"
      "v_or3_b32 v116, v116, v218, v219\n"
      "v_mfma_f32_16x16x32_bf16 v[148:151], a[76:79], a[24:27], v[148:151]\n"
      "s_sub_u32 s93, s93, 1\n"
      "v_exp_f32 v113, v185\n"
      "v_mfma_f32_16x16x32_bf16 v[148:151], a[68:71], a[8:11], v[148:151]\n"
      "s_cmp_le_i32 s93, 14\n"
      "ds_read_b64_tr_b16 a[124:125], v247 offset:1024\n"
      "v_cvt_pk_bf16_f32 v220, v112, v113\n"
      "v_mfma_f32_16x16x32_bf16 v[156:159], a[68:71], a[12:15], v[236:239]\n"
      "s_cselect_b32 s86, 107, s86\n"
      "v_exp_f32 v114, v186\n"
      "v_mfma_f32_16x16x32_bf16 v[156:159], a[76:79], a[28:31], v[156:159]\n"
      "s_sub_u32 s67, s65, s66\n"
      "ds_read_b64_tr_b16 a[126:127], v247 offset:5120\n"
      "v_mfma_f32_16x16x32_bf16 v[156:159], a[84:87], a[44:47], v[156:159]\n"
      "s_cmp_lt_i32 s67, 0\n"
      "v_exp_f32 v115, v187\n"
      "v_mfma_f32_16x16x32_bf16 v[156:159], a[92:95], a[60:63], v[156:159]\n"
      "s_cselect_b32 s88, s66, s65\n"
      "v_cvt_pk_bf16_f32 v221, v114, v115\n"
      "v_mfma_f32_16x16x32_bf16 v[152:155], a[88:91], a[60:63], v[236:239]\n"
      "s_add_u32 s90, s88, s91\n"
      "v_exp_f32 v112, v188\n"
      "v_mfma_f32_16x16x32_bf16 v[152:155], a[80:83], a[44:47], v[152:155]\n"
      "v_or3_b32 v116, v116, v220, v221\n"
      "v_mfma_f32_16x16x32_bf16 v[152:155], a[72:75], a[28:31], v[152:155]\n"
      "v_exp_f32 v113, v189\n"
      "v_mfma_f32_16x16x32_bf16 v[152:155], a[64:67], a[12:15], v[152:155]\n"
      "s_waitcnt lgkmcnt(10)\n"
      "v_cvt_pk_bf16_f32 v222, v112, v113\n"
      "v_mfma_f32_16x16x32_bf16 v[32:35], a[96:99], v[192:195], v[32:35]\n"
      "v_exp_f32 v114, v190\n"
      "v_mfma_f32_16x16x32_bf16 v[48:51], a[100:103], v[192:195], v[48:51]\n"
      "v_mfma_f32_16x16x32_bf16 a[128:131], a[104:107], v[192:195], a[128:131]\n"
      "s_waitcnt lgkmcnt(6)\n"
      "v_exp_f32 v115, v191\n"
      "v_mfma_f32_16x16x32_bf16 a[144:147], a[108:111], v[192:195], a[144:147]\n"
      "v_cvt_pk_bf16_f32 v223, v114, v115\n"
      "v_mfma_f32_16x16x32_bf16 v[64:67], a[112:115], v[192:195], v[64:67]\n"
      "s_waitcnt lgkmcnt(2)\n"
      "v_mfma_f32_16x16x32_bf16 v[80:83], a[116:119], v[192:195], v[80:83]\n"
      "v_or3_b32 v116, v116, v222, v223\n"
      "v_mfma_f32_16x16x32_bf16 a[160:163], a[120:123], v[192:195], a[160:163]\n"
      "s_mov_b32 s64, 1\n"
      "s_waitcnt lgkmcnt(0)\n"
      "v_and_b32 v116, 0x40004000, v116\n"
      "v_mfma_f32_16x16x32_bf16 a[176:179], a[124:127], v[192:195], a[176:179]\n"
      "v_cmp_ne_u32 vcc, 0, v116\n"
      "s_cbranch_vccnz FB1_%=\n"
      "RET1_1_%=:\n"
      "v_mfma_f32_16x16x32_bf16 a[180:183], a[124:127], v[196:199], a[180:183]\n"
      "v_exp_f32 v112, v128\n"
      "s_barrier\n"
      "v_mfma_f32_16x16x32_bf16 a[164:167], a[120:123], v[196:199], a[164:167]\n"
      "v_mfma_f32_16x16x32_bf16 a[192:195], a[196:199], v[208:211], a[192:195]\n"
      "s_add_u32 m0, s62, 0\n"
      "v_exp_f32 v113, v129\n"
      "v_mfma_f32_16x16x32_bf16 v[84:87], a[116:119], v[196:199], v[84:87]\n"
      "buffer_load_dwordx4 v248, s[20:23], s60 offen nt lds\n"
      "v_cvt_pk_bf16_f32 v192, v112, v113\n"
      "v_mfma_f32_16x16x32_bf16 v[68:71], a[112:115], v[196:199], v[68:71]\n"
      "s_add_u32 m0, s62, 896\n"
      "v_exp_f32 v114, v130\n"
      "v_mfma_f32_16x16x32_bf16 a[192:195], a[200:203], v[212:215], a[192:195]\n"
      "buffer_load_dwordx4 v248, s[20:23], s60 offen offset:128 nt lds\n"
      "v_mfma_f32_16x16x32_bf16 a[148:151], a[108:111], v[196:199], a[148:151]\n"
      "s_add_u32 m0, s62, 8192\n"
      "v_exp_f32 v115, v131\n"
      "v_mfma_f32_16x16x32_bf16 a[132:135], a[104:107], v[196:199], a[132:135]\n"
      "s_memrealtime s[68:69]\n"
      "buffer_load_dwordx4 v120, s[20:23], s60 offen nt lds\n"
      "v_cvt_pk_bf16_f32 v193, v114, v115\n"
      "v_mfma_f32_16x16x32_bf16 a[192:195], a[204:207], v[216:219], a[192:195]\n"
      "s_add_u32 m0, s62, 9088\n"
      "v_exp_f32 v112, v132\n"
      "v_mfma_f32_16x16x32_bf16 v[52:55], a[100:103], v[196:199], v[52:55]\n"
      "s_add_u32 s62, s62, 0x8000\n"
      "buffer_load_dwordx4 v120, s[20:23], s60 offen offset:128 nt lds\n"
      "v_or_b32 v116, v192, v193\n"
      "v_mfma_f32_16x16x32_bf16 v[36:39], a[96:99], v[196:199], v[36:39]\n"
      "s_and_b32 s62, s62, 0x1ffff\n"
      "v_exp_f32 v113, v133\n"
      "v_mfma_f32_16x16x32_bf16 a[192:195], a[208:211], v[220:223], a[192:195]\n"
      "s_add_u32 s60, s60, s78\n"
      "v_cvt_pk_bf16_f32 v194, v112, v113\n"
      "s_waitcnt vmcnt(12) lgkmcnt(0)\n"
      "s_barrier\n"
      "v_mfma_f32_16x16x32_bf16 v[40:43], a[96:99], v[200:203], v[40:43]\n"
      "ds_read_b128 a[64:67], v242 offset:32768\n"
      "v_exp_f32 v114, v134\n"
      "v_mfma_f32_16x16x32_bf16 v[44:47], a[96:99], v[204:207], v[44:47]\n"
      "ds_read_b128 a[68:71], v242 offset:36864\n"
      "ds_read_b128 a[72:75], v243 offset:32768\n"
      "v_mfma_f32_16x16x32_bf16 v[60:63], a[100:103], v[204:207], v[60:63]\n"
      "v_exp_f32 v115, v135\n"
      "v_mfma_f32_16x16x32_bf16 v[56:59], a[100:103], v[200:203], v[56:59]\n"
      "ds_read_b128 a[76:79], v243 offset:36864\n"
      "v_cvt_pk_bf16_f32 v195, v114, v115\n"
      "v_mfma_f32_16x16x32_bf16 a[136:139], a[104:107], v[200:203], a[136:139]\n"
      "v_exp_f32 v112, v136\n"
      "v_mfma_f32_16x16x32_bf16 a[140:143], a[104:107], v[204:207], a[140:143]\n"
      "ds_read_b128 a[80:83], v242 offset:33792\n"
      "v_or3_b32 v116, v116, v194, v195\n"
      "v_mfma_f32_16x16x32_bf16 a[156:159], a[108:111], v[204:207], a[156:159]\n"
      "v_exp_f32 v113, v137\n"
      "v_mfma_f32_16x16x32_bf16 a[152:155], a[108:111], v[200:203], a[152:155]\n"
      "ds_read_b128 a[84:87], v242 offset:37888\n"
      "v_cvt_pk_bf16_f32 v196, v112, v113\n"
      "v_mfma_f32_16x16x32_bf16 v[72:75], a[112:115], v[200:203], v[72:75]\n"
      "v_exp_f32 v114, v138\n"
      "v_mfma_f32_16x16x32_bf16 v[76:79], a[112:115], v[204:207], v[76:79]\n"
      "ds_read_b128 a[88:91], v243 offset:33792\n"
      "ds_read_b128 a[92:95], v243 offset:37888\n"
      "v_mfma_f32_16x16x32_bf16 v[92:95], a[116:119], v[204:207], v[92:95]\n"
      "v_exp_f32 v115, v139\n"
      "v_mfma_f32_16x16x32_bf16 v[88:91], a[116:119], v[200:203], v[88:91]\n"
      "v_cvt_pk_bf16_f32 v197, v114, v115\n"
      "v_mfma_f32_16x16x32_bf16 a[168:171], a[120:123], v[200:203], a[168:171]\n"
      "v_exp_f32 v112, v140\n"
      "v_mfma_f32_16x16x32_bf16 a[172:175], a[120:123], v[204:207], a[172:175]\n"
      "v_or3_b32 v116, v116, v196, v197\n"
      "v_mfma_f32_16x16x32_bf16 a[188:191], a[124:127], v[204:207], a[188:191]\n"
      "v_exp_f32 v113, v141\n"
      "v_mfma_f32_16x16x32_bf16 a[184:187], a[124:127], v[200:203], a[184:187]\n"
      "s_waitcnt lgkmcnt(0)\n"
      "v_cvt_pk_bf16_f32 v198, v112, v113\n"
      "v_mfma_f32_16x16x32_bf16 v[160:163], a[64:67], a[0:3], v[224:227]\n"
      "v_exp_f32 v114, v142\n"
      "v_mfma_f32_16x16x32_bf16 v[160:163], a[72:75], a[16:19], v[160:163]\n"
      "ds_read_b64_tr_b16 a[96:97], v244 offset:32768\n"
      "ds_read_b64_tr_b16 a[98:99], v244 offset:36864\n"
      "v_mfma_f32_16x16x32_bf16 v[160:163], a[80:83], a[32:35], v[160:163]\n"
      "v_exp_f32 v115, v143\n"
      "v_mfma_f32_16x16x32_bf16 v[160:163], a[88:91], a[48:51], v[160:163]\n"
      "ds_read_b64_tr_b16 a[100:101], v245 offset:32768\n"
      "v_cvt_pk_bf16_f32 v199, v114, v115\n"
      "v_mfma_f32_16x16x32_bf16 v[164:167], a[92:95], a[48:51], v[224:227]\n"
      "v_exp_f32 v112, v144\n"
      "v_mfma_f32_16x16x32_bf16 v[164:167], a[84:87], a[32:35], v[164:167]\n"
      "ds_read_b64_tr_b16 a[102:103], v245 offset:36864\n"
      "v_or3_b32 v116, v116, v198, v199\n"
      "v_mfma_f32_16x16x32_bf16 v[164:167], a[76:79], a[16:19], v[164:167]\n"
      "v_exp_f32 v113, v145\n"
      "v_mfma_f32_16x16x32_bf16 v[164:167], a[68:71], a[0:3], v[164:167]\n"
      "ds_read_b64_tr_b16 a[104:105], v246 offset:32768\n"
      "v_cvt_pk_bf16_f32 v200, v112, v113\n"
      "v_mfma_f32_16x16x32_bf16 v[172:175], a[68:71], a[4:7], v[228:231]\n"
      "v_exp_f32 v114, v146\n"
      "v_mfma_f32_16x16x32_bf16 v[172:175], a[76:79], a[20:23], v[172:175]\n"
      "ds_read_b64_tr_b16 a[106:107], v246 offset:36864\n"
      "ds_read_b64_tr_b16 a[108:109], v247 offset:32768\n"
      "v_mfma_f32_16x16x32_bf16 v[172:175], a[84:87], a[36:39], v[172:175]\n"
      "v_exp_f32 v115, v147\n"
      "v_mfma_f32_16x16x32_bf16 v[172:175], a[92:95], a[52:55], v[172:175]\n"
      "ds_read_b64_tr_b16 a[110:111], v247 offset:36864\n"
      "v_cvt_pk_bf16_f32 v201, v114, v115\n"
      "v_mfma_f32_16x16x32_bf16 v[168:171], a[88:91], a[52:55], v[228:231]\n"
      "v_exp_f32 v112, v148\n"
      "v_mfma_f32_16x16x32_bf16 v[168:171], a[80:83], a[36:39], v[168:171]\n"
      "ds_read_b64_tr_b16 a[112:113], v244 offset:33792\n"
      "v_or3_b32 v116, v116, v200, v201\n"
      "v_mfma_f32_16x16x32_bf16 v[168:171], a[72:75], a[20:23], v[168:171]\n"
      "v_exp_f32 v113, v149\n"
      "v_mfma_f32_16x16x32_bf16 v[168:171], a[64:67], a[4:7], v[168:171]\n"
      "ds_read_b64_tr_b16 a[114:115], v244 offset:37888\n"
      "v_cvt_pk_bf16_f32 v202, v112, v113\n"
      "v_mfma_f32_16x16x32_bf16 v[176:179], a[64:67], a[8:11], v[232:235]\n"
      "v_exp_f32 v114, v150\n"
      "v_mfma_f32_16x16x32_bf16 v[176:179], a[72:75], a[24:27], v[176:179]\n"
      "ds_read_b64_tr_b16 a[116:117], v245 offset:33792\n"
      "ds_read_b64_tr_b16 a[118:119], v245 offset:37888\n"
      "v_mfma_f32_16x16x32_bf16 v[176:179], a[80:83], a[40:43], v[176:179]\n"
      "v_exp_f32 v115, v151\n"
      "v_mfma_f32_16x16x32_bf16 v[176:179], a[88:91], a[56:59], v[176:179]\n"
      "ds_read_b64_tr_b16 a[120:121], v246 offset:33792\n"
      "v_cvt_pk_bf16_f32 v203, v114, v115\n"
      "v_mfma_f32_16x16x32_bf16 v[180:183], a[92:95], a[56:59], v[232:235]\n"
      "v_exp_f32 v112, v152\n"
      "v_mfma_f32_16x16x32_bf16 v[180:183], a[84:87], a[40:43], v[180:183]\n"
      "ds_read_b64_tr_b16 a[122:123], v246 offset:37888\n"
      "v_or3_b32 v116, v116, v202, v203\n"
      "v_mfma_f32_16x16x32_bf16 v[180:183], a[76:79], a[24:27], v[180:183]\n"
      "v_exp_f32 v113, v153\n"
      "v_mfma_f32_16x16x32_bf16 v[180:183], a[68:71], a[8:11], v[180:183]\n"
      "ds_read_b64_tr_b16 a[124:125], v247 offset:33792\n"
      "v_cvt_pk_bf16_f32 v204, v112, v113\n"
      "v_mfma_f32_16x16x32_bf16 v[188:191], a[68:71], a[12:15], v[236:239]\n"
      "v_exp_f32 v114, v154\n"
      "v_mfma_f32_16x16x32_bf16 v[188:191], a[76:79], a[28:31], v[188:191]\n"
      "ds_read_b64_tr_b16 a[126:127], v247 offset:37888\n"
      "v_mfma_f32_16x16x32_bf16 v[188:191], a[84:87], a[44:47], v[188:191]\n"
      "v_exp_f32 v115, v155\n"
      "v_mfma_f32_16x16x32_bf16 v[188:191], a[92:95], a[60:63], v[188:191]\n"
      "v_cvt_pk_bf16_f32 v205, v114, v115\n"
      "v_mfma_f32_16x16x32_bf16 v[184:187], a[88:91], a[60:63], v[236:239]\n"
      "v_exp_f32 v112, v156\n"
      "v_mfma_f32_16x16x32_bf16 v[184:187], a[80:83], a[44:47], v[184:187]\n"
      "v_or3_b32 v116, v116, v204, v205\n"
      "v_mfma_f32_16x16x32_bf16 v[184:187], a[72:75], a[28:31], v[184:187]\n"
      "v_exp_f32 v113, v157\n"
      "v_mfma_f32_16x16x32_bf16 v[184:187], a[64:67], a[12:15], v[184:187]\n"
      "s_waitcnt lgkmcnt(10)\n"
      "v_cvt_pk_bf16_f32 v206, v112, v113\n"
      "v_mfma_f32_16x16x32_bf16 v[32:35], a[96:99], v[208:211], v[32:35]\n"
      "v_exp_f32 v114, v158\n"
      "v_mfma_f32_16x16x32_bf16 v[48:51], a[100:103], v[208:211], v[48:51]\n"
      "v_mfma_f32_16x16x32_bf16 a[128:131], a[104:107], v[208:211], a[128:131]\n"
      "s_waitcnt lgkmcnt(6)\n"
      "v_exp_f32 v115, v159\n"
      "v_mfma_f32_16x16x32_bf16 a[144:147], a[108:111], v[208:211], a[144:147]\n"
      "v_cvt_pk_bf16_f32 v207, v114, v115\n"
      "v_mfma_f32_16x16x32_bf16 v[64:67], a[112:115], v[208:211], v[64:67]\n"
      "s_waitcnt lgkmcnt(2)\n"
      "v_mfma_f32_16x16x32_bf16 v[80:83], a[116:119], v[208:211], v[80:83]\n"
      "v_or3_b32 v116, v116, v206, v207\n"
      "v_mfma_f32_16x16x32_bf16 a[160:163], a[120:123], v[208:211], a[160:163]\n"
      "s_mov_b32 s64, 1\n"
      "s_waitcnt lgkmcnt(0)\n"
      "v_and_b32 v116, 0x40004000, v116\n"
      "v_mfma_f32_16x16x32_bf16 a[176:179], a[124:127], v[208:211], a[176:179]\n"
      "v_cmp_ne_u32 vcc, 0, v116\n"
      "s_cbranch_vccnz FB0_%=\n"
      "RET0_1_%=:\n"
      "v_mfma_f32_16x16x32_bf16 a[180:183], a[124:127], v[212:215], a[180:183]\n"
      "v_exp_f32 v112, v160\n"
      "v_mfma_f32_16x16x32_bf16 a[164:167], a[120:123], v[212:215], a[164:167]\n"
      "v_xor_b32 v242, 0x10000, v242\n"
      "v_mfma_f32_16x16x32_bf16 a[192:195], a[196:199], v[192:195], a[192:195]\n"
      "s_add_u32 m0, s77, 16384\n"
      "v_exp_f32 v113, v161\n"
      "v_mfma_f32_16x16x32_bf16 v[84:87], a[116:119], v[212:215], v[84:87]\n"
      "buffer_load_dwordx4 v249, s[24:27], s61 offen nt lds\n"
      "v_cvt_pk_bf16_f32 v208, v112, v113\n"
      "v_xor_b32 v243, 0x10000, v243\n"
      "v_mfma_f32_16x16x32_bf16 v[68:71], a[112:115], v[212:215], v[68:71]\n"
      "s_add_u32 m0, s77, 17408\n"
      "v_exp_f32 v114, v162\n"
      "v_mfma_f32_16x16x32_bf16 a[192:195], a[200:203], v[196:199], a[192:195]\n"
      "buffer_load_dwordx4 v121, s[24:27], s61 offen nt lds\n"
      "v_xor_b32 v244, 0x10000, v244\n"
      "v_mfma_f32_16x16x32_bf16 a[148:151], a[108:111], v[212:215], a[148:151]\n"
      "s_add_u32 m0, s77, 24576\n"
      "v_exp_f32 v115, v163\n"
      "v_mfma_f32_16x16x32_bf16 a[132:135], a[104:107], v[212:215], a[132:135]\n"
      "s_memrealtime s[68:69]\n"
      "buffer_load_dwordx4 v122, s[24:27], s61 offen nt lds\n"
      "v_cvt_pk_bf16_f32 v209, v114, v115\n"
      "v_xor_b32 v245, 0x10000, v245\n"
      "v_mfma_f32_16x16x32_bf16 a[192:195], a[204:207], v[200:203], a[192:195]\n"
      "s_add_u32 m0, s77, 25600\n"
      "v_exp_f32 v112, v164\n"
      "v_mfma_f32_16x16x32_bf16 v[52:55], a[100:103], v[212:215], v[52:55]\n"
      "s_add_u32 s77, s77, 0x8000\n"
      "buffer_load_dwordx4 v123, s[24:27], s61 offen nt lds\n"
      "v_or_b32 v116, v208, v209\n"
      "v_xor_b32 v246, 0x10000, v246\n"
      "v_mfma_f32_16x16x32_bf16 v[36:39], a[96:99], v[212:215], v[36:39]\n"
      "s_and_b32 s77, s77, 0x1ffff\n"
      "v_exp_f32 v113, v165\n"
      "v_mfma_f32_16x16x32_bf16 a[192:195], a[208:211], v[204:207], a[192:195]\n"
      "s_add_u32 s61, s61, s78\n"
      "v_cvt_pk_bf16_f32 v210, v112, v113\n"
      "v_xor_b32 v247, 0x10000, v247\n"
      "s_waitcnt vmcnt(8) lgkmcnt(0)\n"
      "s_sub_u32 s67, s68, s90\n"
      "s_cmp_lt_i32 s67, 0\n"
      "s_cbranch_scc1 GW3_%=\n"
      "GX3_%=:\n"
      "s_barrier\n"
      "v_mfma_f32_16x16x32_bf16 v[40:43], a[96:99], v[216:219], v[40:43]\n"
      "s_memrealtime s[68:69]\n"
      "buffer_load_dword v250, v251, s[28:31], 0 offen sc1\n"
      "ds_read_b128 a[64:67], v242 offset:0\n"
      "v_exp_f32 v114, v166\n"
      "v_mfma_f32_16x16x32_bf16 v[44:47], a[96:99], v[220:223], v[44:47]\n"
      "s_add_u32 m0, s62, 0\n"
      "ds_read_b128 a[68:71], v242 offset:4096\n"
      "ds_read_b128 a[72:75], v243 offset:0\n"
      "v_mfma_f32_16x16x32_bf16 v[60:63], a[100:103], v[220:223], v[60:63]\n"
      "buffer_load_dwordx4 v248, s[20:23], s60 offen nt lds\n"
      "v_exp_f32 v115, v167\n"
      "v_mfma_f32_16x16x32_bf16 v[56:59], a[100:103], v[216:219], v[56:59]\n"
      "s_add_u32 m0, s62, 896\n"
      "ds_read_b128 a[76:79], v243 offset:4096\n"
      "v_cvt_pk_bf16_f32 v211, v114, v115\n"
      "v_mfma_f32_16x16x32_bf16 a[136:139], a[104:107], v[216:219], a[136:139]\n"
      "buffer_load_dwordx4 v248, s[20:23], s60 offen offset:128 nt lds\n"
      "v_exp_f32 v112, v168\n"
      "v_mfma_f32_16x16x32_bf16 a[140:143], a[104:107], v[220:223], a[140:143]\n"
      "s_add_u32 m0, s62, 8192\n"
      "ds_read_b128 a[80:83], v242 offset:1024\n"
      "v_or3_b32 v116, v116, v210, v211\n"
      "v_mfma_f32_16x16x32_bf16 a[156:159], a[108:111], v[220:223], a[156:159]\n"
      "buffer_load_dwordx4 v120, s[20:23], s60 offen nt lds\n"
      "v_exp_f32 v113, v169\n"
      "v_mfma_f32_16x16x32_bf16 a[152:155], a[108:111], v[216:219], a[152:155]\n"
      "s_add_u32 m0, s62, 9088\n"
      "ds_read_b128 a[84:87], v242 offset:5120\n"
      "v_cvt_pk_bf16_f32 v212, v112, v113\n"
      "v_mfma_f32_16x16x32_bf16 v[72:75], a[112:115], v[216:219], v[72:75]\n"
      "s_add_u32 s62, s62, 0x8000\n"
      "buffer_load_dwordx4 v120, s[20:23], s60 offen offset:128 nt lds\n"
      "v_exp_f32 v114, v170\n"
      "v_mfma_f32_16x16x32_bf16 v[76:79], a[112:115], v[220:223], v[76:79]\n"
      "s_and_b32 s62, s62, 0x1ffff\n"
      "ds_read_b128 a[88:91], v243 offset:1024\n"
      "ds_read_b128 a[92:95], v243 offset:5120\n"
      "v_mfma_f32_16x16x32_bf16 v[92:95], a[116:119], v[220:223], v[92:95]\n"
      "s_add_u32 s60, s60, %[inc]\n"
      "v_exp_f32 v115, v171\n"
      "v_mfma_f32_16x16x32_bf16 v[88:91], a[116:119], v[216:219], v[88:91]\n"
      "v_cvt_pk_bf16_f32 v213, v114, v115\n"
      "v_mfma_f32_16x16x32_bf16 a[168:171], a[120:123], v[216:219], a[168:171]\n"
      "v_exp_f32 v112, v172\n"
      "v_mfma_f32_16x16x32_bf16 a[172:175], a[120:123], v[220:223], a[172:175]\n"
      "v_or3_b32 v116, v116, v212, v213\n"
      "v_mfma_f32_16x16x32_bf16 a[188:191], a[124:127], v[220:223], a[188:191]\n"
      "v_exp_f32 v113, v173\n"
      "v_mfma_f32_16x16x32_bf16 a[184:187], a[124:127], v[216:219], a[184:187]\n"
      "s_waitcnt lgkmcnt(0)\n"
      "v_cvt_pk_bf16_f32 v214, v112, v113\n"
      "v_mfma_f32_16x16x32_bf16 v[128:131], a[64:67], a[0:3], v[224:227]\n"
      "s_sub_u32 s67, s68, s66\n"
      "v_exp_f32 v114, v174\n"
      "v_mfma_f32_16x16x32_bf16 v[128:131], a[72:75], a[16:19], v[128:131]\n"
      "s_cmp_gt_i32 s67, s70\n"
      "ds_read_b64_tr_b16 a[96:97], v244 offset:0\n"
      "ds_read_b64_tr_b16 a[98:99], v244 offset:4096\n"
      "v_mfma_f32_16x16x32_bf16 v[128:131], a[80:83], a[32:35], v[128:131]\n"
      "s_cselect_b32 s69, s86, 0\n"
      "v_exp_f32 v115, v175\n"
      "v_mfma_f32_16x16x32_bf16 v[128:131], a[88:91], a[48:51], v[128:131]\n"
      "s_add_u32 s66, s66, s69\n"
      "ds_read_b64_tr_b16 a[100:101], v245 offset:0\n"
      "v_cvt_pk_bf16_f32 v215, v114, v115\n"
      "v_mfma_f32_16x16x32_bf16 v[132:135], a[92:95], a[48:51], v[224:227]\n"
      "s_add_u32 s67, s67, s101\n"
      "v_exp_f32 v112, v176\n"
      "v_mfma_f32_16x16x32_bf16 v[132:135], a[84:87], a[32:35], v[132:135]\n"
      "s_min_i32 s70, s70, s67\n"
      "ds_read_b64_tr_b16 a[102:103], v245 offset:4096\n"
      "v_or3_b32 v116, v116, v214, v215\n"
      "v_mfma_f32_16x16x32_bf16 v[132:135], a[76:79], a[16:19], v[132:135]\n"
      "s_max_i32 s70, s70, 85\n"
      "v_exp_f32 v113, v177\n"
      "v_mfma_f32_16x16x32_bf16 v[132:135], a[68:71], a[0:3], v[132:135]\n"
      "s_add_u32 s66, s66, s86\n"
      "ds_read_b64_tr_b16 a[104:105], v246 offset:0\n"
      "v_cvt_pk_bf16_f32 v216, v112, v113\n"
      "v_mfma_f32_16x16x32_bf16 v[140:143], a[68:71], a[4:7], v[228:231]\n"
      "s_and_b32 s76, s80, 31\n"
      "v_exp_f32 v114, v178\n"
      "v_mfma_f32_16x16x32_bf16 v[140:143], a[76:79], a[20:23], v[140:143]\n"
      "s_lshr_b64 s[80:81], s[80:81], 5\n"
      "ds_read_b64_tr_b16 a[106:107], v246 offset:4096\n"
      "ds_read_b64_tr_b16 a[108:109], v247 offset:0\n"
      "v_mfma_f32_16x16x32_bf16 v[140:143], a[84:87], a[36:39], v[140:143]\n"
      "s_add_u32 s66, s66, s76\n"
      "v_exp_f32 v115, v179\n"
      "v_mfma_f32_16x16x32_bf16 v[140:143], a[92:95], a[52:55], v[140:143]\n"
      "s_sub_u32 s67, s68, s82\n"
      "ds_read_b64_tr_b16 a[110:111], v247 offset:4096\n"
      "v_cvt_pk_bf16_f32 v217, v114, v115\n"
      "v_mfma_f32_16x16x32_bf16 v[136:139], a[88:91], a[52:55], v[228:231]\n"
      "s_min_i32 s76, s67, s83\n"
      "v_exp_f32 v112, v180\n"
      "v_mfma_f32_16x16x32_bf16 v[136:139], a[80:83], a[36:39], v[136:139]\n"
      "s_mov_b32 s83, s67\n"
      "ds_read_b64_tr_b16 a[112:113], v244 offset:1024\n"
      "v_or3_b32 v116, v116, v216, v217\n"
      "v_mfma_f32_16x16x32_bf16 v[136:139], a[72:75], a[20:23], v[136:139]\n"
      "s_sub_u32 s76, s76, 36\n"
      "v_exp_f32 v113, v181\n"
      "v_mfma_f32_16x16x32_bf16 v[136:139], a[64:67], a[4:7], v[136:139]\n"
      "s_min_i32 s76, s76, 148\n"
      "ds_read_b64_tr_b16 a[114:115], v244 offset:5120\n"
      "v_cvt_pk_bf16_f32 v218, v112, v113\n"
      "v_mfma_f32_16x16x32_bf16 v[144:147], a[64:67], a[8:11], v[232:235]\n"
      "s_max_i32 s76, s76, 70\n"
      "v_exp_f32 v114, v182\n"
      "v_mfma_f32_16x16x32_bf16 v[144:147], a[72:75], a[24:27], v[144:147]\n"
      "s_mov_b32 s82, s68\n"
      "ds_read_b64_tr_b16 a[116:117], v245 offset:1024\n"
      "ds_read_b64_tr_b16 a[118:119], v245 offset:5120\n"
      "v_mfma_f32_16x16x32_bf16 v[144:147], a[80:83], a[40:43], v[144:147]\n"
      "s_lshr_b32 s69, s86, 1\n"
      "v_exp_f32 v115, v183\n"
      "v_mfma_f32_16x16x32_bf16 v[144:147], a[88:91], a[56:59], v[144:147]\n"
      "s_add_u32 s66, s66, s69\n"
      "ds_read_b64_tr_b16 a[120:121], v246 offset:1024\n"
      "v_cvt_pk_bf16_f32 v219, v114, v115\n"
      "v_mfma_f32_16x16x32_bf16 v[148:151], a[92:95], a[56:59], v[232:235]\n"
      "s_add_u32 s82, s82, s69\n"
      "v_exp_f32 v112, v184\n"
      "v_mfma_f32_16x16x32_bf16 v[148:151], a[84:87], a[40:43], v[148:151]\n"
      "s_add_u32 s65, s82, s76\n"
      "ds_read_b64_tr_b16 a[122:123], v246 offset:5120\n"
      "v_or3_b32 v116, v116, v218, v219\n"
      "v_mfma_f32_16x16x32_bf16 v[148:151], a[76:79], a[24:27], v[148:151]\n"
      "s_sub_u32 s93, s93, 1\n"
      "v_exp_f32 v113, v185\n"
      "v_mfma_f32_16x16x32_bf16 v[148:151], a[68:71], a[8:11], v[148:151]\n"
      "s_cmp_le_i32 s93, 14\n"
      "ds_read_b64_tr_b16 a[124:125], v247 offset:1024\n"
      "v_cvt_pk_bf16_f32 v220, v112, v113\n"
      "v_mfma_f32_16x16x32_bf16 v[156:159], a[68:71], a[12:15], v[236:239]\n"
      "s_cselect_b32 s86, 107, s86\n"
      "v_exp_f32 v114, v186\n"
      "v_mfma_f32_16x16x32_bf16 v[156:159], a[76:79], a[28:31], v[156:159]\n"
      "s_sub_u32 s67, s65, s66\n"
      "ds_read_b64_tr_b16 a[126:127], v247 offset:5120\n"
      "v_mfma_f32_16x16x32_bf16 v[156:159], a[84:87], a[44:47], v[156:159]\n"
      "s_cmp_lt_i32 s67, 0\n"
      "v_exp_f32 v115, v187\n"
      "v_mfma_f32_16x16x32_bf16 v[156:159], a[92:95], a[60:63], v[156:159]\n"
      "s_cselect_b32 s88, s66, s65\n"
      "v_cvt_pk_bf16_f32 v221, v114, v115\n"
      "v_mfma_f32_16x16x32_bf16 v[152:155], a[88:91], a[60:63], v[236:239]\n"
      "s_add_u32 s90, s88, s91\n"
      "v_exp_f32 v112, v188\n"
      "v_mfma_f32_16x16x32_bf16 v[152:155], a[80:83], a[44:47], v[152:155]\n"
      "v_or3_b32 v116, v116, v220, v221\n"
      "v_mfma_f32_16x16x32_bf16 v[152:155], a[72:75], a[28:31], v[152:155]\n"
      "v_exp_f32 v113, v189\n"
      "v_mfma_f32_16x16x32_bf16 v[152:155], a[64:67], a[12:15], v[152:155]\n"
      "s_waitcnt lgkmcnt(10)\n"
      "v_cvt_pk_bf16_f32 v222, v112, v113\n"
      "v_mfma_f32_16x16x32_bf16 v[32:35], a[96:99], v[192:195], v[32:35]\n"
      "v_exp_f32 v114, v190\n"
      "v_mfma_f32_16x16x32_bf16 v[48:51], a[100:103], v[192:195], v[48:51]\n"
      "v_mfma_f32_16x16x32_bf16 a[128:131], a[104:107], v[192:195], a[128:131]\n"
      "s_waitcnt lgkmcnt(6)\n"
      "v_exp_f32 v115, v191\n"
      "v_mfma_f32_16x16x32_bf16 a[144:147], a[108:111], v[192:195], a[144:147]\n"
      "v_cvt_pk_bf16_f32 v223, v114, v115\n"
      "v_mfma_f32_16x16x32_bf16 v[64:67], a[112:115], v[192:195], v[64:67]\n"
      "s_waitcnt lgkmcnt(2)\n"
      "v_mfma_f32_16x16x32_bf16 v[80:83], a[116:119], v[192:195], v[80:83]\n"
      "v_or3_b32 v116, v116, v222, v223\n"
      "v_mfma_f32_16x16x32_bf16 a[160:163], a[120:123], v[192:195], a[160:163]\n"
      "s_mov_b32 s64, 2\n"
      "s_waitcnt lgkmcnt(0)\n"
      "v_and_b32 v116, 0x40004000, v116\n"
      "v_mfma_f32_16x16x32_bf16 a[176:179], a[124:127], v[192:195], a[176:179]\n"
      "v_cmp_ne_u32 vcc, 0, v116\n"
      "s_cbranch_vccnz FB1_%=\n"
      "RET1_2_%=:\n"
      "v_mfma_f32_16x16x32_bf16 a[180:183], a[124:127], v[196:199], a[180:183]\n"
      "v_exp_f32 v112, v128\n"
      "v_mfma_f32_16x16x32_bf16 a[164:167], a[120:123], v[196:199], a[164:167]\n"
      "v_mfma_f32_16x16x32_bf16 a[192:195], a[196:199], v[208:211], a[192:195]\n"
      "s_add_u32 m0, s77, 16384\n"
      "v_exp_f32 v113, v129\n"
      "v_mfma_f32_16x16x32_bf16 v[84:87], a[116:119], v[196:199], v[84:87]\n"
      "buffer_load_dwordx4 v249, s[24:27], s61 offen nt lds\n"
      "v_cvt_pk_bf16_f32 v192, v112, v113\n"
      "v_mfma_f32_16x16x32_bf16 v[68:71], a[112:115], v[196:199], v[68:71]\n"
      "s_add_u32 m0, s77, 17408\n"
      "v_exp_f32 v114, v130\n"
      "v_mfma_f32_16x16x32_bf16 a[192:195], a[200:203], v[212:215], a[192:195]\n"
      "buffer_load_dwordx4 v121, s[24:27], s61 offen nt lds\n"
      "v_mfma_f32_16x16x32_bf16 a[148:151], a[108:111], v[196:199], a[148:151]\n"
      "s_add_u32 m0, s77, 24576\n"
      "v_exp_f32 v115, v131\n"
      "v_mfma_f32_16x16x32_bf16 a[132:135], a[104:107], v[196:199], a[132:135]\n"
      "s_memrealtime s[68:69]\n"
      "buffer_load_dwordx4 v122, s[24:27], s61 offen nt lds\n"
      "v_cvt_pk_bf16_f32 v193, v114, v115\n"
      "v_mfma_f32_16x16x32_bf16 a[192:195], a[204:207], v[216:219], a[192:195]\n"
      "s_add_u32 m0, s77, 25600\n"
      "v_exp_f32 v112, v132\n"
      "v_mfma_f32_16x16x32_bf16 v[52:55], a[100:103], v[196:199], v[52:55]\n"
      "s_add_u32 s77, s77, 0x8000\n"
      "buffer_load_dwordx4 v123, s[24:27], s61 offen nt lds\n"
      "v_or_b32 v116, v192, v193\n"
      "v_mfma_f32_16x16x32_bf16 v[36:39], a[96:99], v[196:199], v[36:39]\n"
      "s_and_b32 s77, s77, 0x1ffff\n"
      "v_exp_f32 v113, v133\n"
      "v_mfma_f32_16x16x32_bf16 a[192:195], a[208:211], v[220:223], a[192:195]\n"
      "s_add_u32 s61, s61, %[inc]\n"
      "v_cvt_pk_bf16_f32 v194, v112, v113\n"
      "s_waitcnt vmcnt(9) lgkmcnt(0)\n"
      "s_barrier\n"
      "v_mfma_f32_16x16x32_bf16 v[40:43], a[96:99], v[200:203], v[40:43]\n"
      "ds_read_b128 a[64:67], v242 offset:32768\n"
      "v_exp_f32 v114, v134\n"
      "v_mfma_f32_16x16x32_bf16 v[44:47], a[96:99], v[204:207], v[44:47]\n"
      "ds_read_b128 a[68:71], v242 offset:36864\n"
      "ds_read_b128 a[72:75], v243 offset:32768\n"
      "v_mfma_f32_16x16x32_bf16 v[60:63], a[100:103], v[204:207], v[60:63]\n"
      "v_exp_f32 v115, v135\n"
      "v_mfma_f32_16x16x32_bf16 v[56:59], a[100:103], v[200:203], v[56:59]\n"
      "ds_read_b128 a[76:79], v243 offset:36864\n"
      "v_cvt_pk_bf16_f32 v195, v114, v115\n"
      "v_mfma_f32_16x16x32_bf16 a[136:139], a[104:107], v[200:203], a[136:139]\n"
      "v_exp_f32 v112, v136\n"
      "v_mfma_f32_16x16x32_bf16 a[140:143], a[104:107], v[204:207], a[140:143]\n"
      "ds_read_b128 a[80:83], v242 offset:33792\n"
      "v_or3_b32 v116, v116, v194, v195\n"
      "v_mfma_f32_16x16x32_bf16 a[156:159], a[108:111], v[204:207], a[156:159]\n"
      "v_exp_f32 v113, v137\n"
      "v_mfma_f32_16x16x32_bf16 a[152:155], a[108:111], v[200:203], a[152:155]\n"
      "ds_read_b128 a[84:87], v242 offset:37888\n"
      "v_cvt_pk_bf16_f32 v196, v112, v113\n"
      "v_mfma_f32_16x16x32_bf16 v[72:75], a[112:115], v[200:203], v[72:75]\n"
      "v_exp_f32 v114, v138\n"
      "v_mfma_f32_16x16x32_bf16 v[76:79], a[112:115], v[204:207], v[76:79]\n"
      "ds_read_b128 a[88:91], v243 offset:33792\n"
      "ds_read_b128 a[92:95], v243 offset:37888\n"
      "v_mfma_f32_16x16x32_bf16 v[92:95], a[116:119], v[204:207], v[92:95]\n"
      "v_exp_f32 v115, v139\n"
      "v_mfma_f32_16x16x32_bf16 v[88:91], a[116:119], v[200:203], v[88:91]\n"
      "v_cvt_pk_bf16_f32 v197, v114, v115\n"
      "v_mfma_f32_16x16x32_bf16 a[168:171], a[120:123], v[200:203], a[168:171]\n"
      "v_exp_f32 v112, v140\n"
      "v_mfma_f32_16x16x32_bf16 a[172:175], a[120:123], v[204:207], a[172:175]\n"
      "v_or3_b32 v116, v116, v196, v197\n"
      "v_mfma_f32_16x16x32_bf16 a[188:191], a[124:127], v[204:207], a[188:191]\n"
      "v_exp_f32 v113, v141\n"
      "v_mfma_f32_16x16x32_bf16 a[184:187], a[124:127], v[200:203], a[184:187]\n"
      "s_waitcnt lgkmcnt(0)\n"
      "v_cvt_pk_bf16_f32 v198, v112, v113\n"
      "v_mfma_f32_16x16x32_bf16 v[160:163], a[64:67], a[0:3], v[224:227]\n"
      "v_exp_f32 v114, v142\n"
      "v_mfma_f32_16x16x32_bf16 v[160:163], a[72:75], a[16:19], v[160:163]\n"
      "ds_read_b64_tr_b16 a[96:97], v244 offset:32768\n"
      "ds_read_b64_tr_b16 a[98:99], v244 offset:36864\n"
      "v_mfma_f32_16x16x32_bf16 v[160:163], a[80:83], a[32:35], v[160:163]\n"
      "v_exp_f32 v115, v143\n"
      "v_mfma_f32_16x16x32_bf16 v[160:163], a[88:91], a[48:51], v[160:163]\n"
      "ds_read_b64_tr_b16 a[100:101], v245 offset:32768\n"
      "v_cvt_pk_bf16_f32 v199, v114, v115\n"
      "v_mfma_f32_16x16x32_bf16 v[164:167], a[92:95], a[48:51], v[224:227]\n"
      "v_exp_f32 v112, v144\n"
      "v_mfma_f32_16x16x32_bf16 v[164:167], a[84:87], a[32:35], v[164:167]\n"
      "ds_read_b64_tr_b16 a[102:103], v245 offset:36864\n"
      "v_or3_b32 v116, v116, v198, v199\n"
      "v_mfma_f32_16x16x32_bf16 v[164:167], a[76:79], a[16:19], v[164:167]\n"
      "v_exp_f32 v113, v145\n"
      "v_mfma_f32_16x16x32_bf16 v[164:167], a[68:71], a[0:3], v[164:167]\n"
      "ds_read_b64_tr_b16 a[104:105], v246 offset:32768\n"
      "v_cvt_pk_bf16_f32 v200, v112, v113\n"
      "v_mfma_f32_16x16x32_bf16 v[172:175], a[68:71], a[4:7], v[228:231]\n"
      "v_exp_f32 v114, v146\n"
      "v_mfma_f32_16x16x32_bf16 v[172:175], a[76:79], a[20:23], v[172:175]\n"
      "ds_read_b64_tr_b16 a[106:107], v246 offset:36864\n"
      "ds_read_b64_tr_b16 a[108:109], v247 offset:32768\n"
      "v_mfma_f32_16x16x32_bf16 v[172:175], a[84:87], a[36:39], v[172:175]\n"
      "v_exp_f32 v115, v147\n"
      "v_mfma_f32_16x16x32_bf16 v[172:175], a[92:95], a[52:55], v[172:175]\n"
      "ds_read_b64_tr_b16 a[110:111], v247 offset:36864\n"
      "v_cvt_pk_bf16_f32 v201, v114, v115\n"
      "v_mfma_f32_16x16x32_bf16 v[168:171], a[88:91], a[52:55], v[228:231]\n"
      "v_exp_f32 v112, v148\n"
      "v_mfma_f32_16x16x32_bf16 v[168:171], a[80:83], a[36:39], v[168:171]\n"
      "ds_read_b64_tr_b16 a[112:113], v244 offset:33792\n"
      "v_or3_b32 v116, v116, v200, v201\n"
      "v_mfma_f32_16x16x32_bf16 v[168:171], a[72:75], a[20:23], v[168:171]\n"
      "v_exp_f32 v113, v149\n"
      "v_mfma_f32_16x16x32_bf16 v[168:171], a[64:67], a[4:7], v[168:171]\n"
      "ds_read_b64_tr_b16 a[114:115], v244 offset:37888\n"
      "v_cvt_pk_bf16_f32 v202, v112, v113\n"
      "v_mfma_f32_16x16x32_bf16 v[176:179], a[64:67], a[8:11], v[232:235]\n"
      "v_exp_f32 v114, v150\n"
      "v_mfma_f32_16x16x32_bf16 v[176:179], a[72:75], a[24:27], v[176:179]\n"
      "ds_read_b64_tr_b16 a[116:117], v245 offset:33792\n"
      "ds_read_b64_tr_b16 a[118:119], v245 offset:37888\n"
      "v_mfma_f32_16x16x32_bf16 v[176:179], a[80:83], a[40:43], v[176:179]\n"
      "v_exp_f32 v115, v151\n"
      "v_mfma_f32_16x16x32_bf16 v[176:179], a[88:91], a[56:59], v[176:179]\n"
      "ds_read_b64_tr_b16 a[120:121], v246 offset:33792\n"
      "v_cvt_pk_bf16_f32 v203, v114, v115\n"
      "v_mfma_f32_16x16x32_bf16 v[180:183], a[92:95], a[56:59], v[232:235]\n"
      "v_exp_f32 v112, v152\n"
      "v_mfma_f32_16x16x32_bf16 v[180:183], a[84:87], a[40:43], v[180:183]\n"
      "ds_read_b64_tr_b16 a[122:123], v246 offset:37888\n"
      "v_or3_b32 v116, v116, v202, v203\n"
      "v_mfma_f32_16x16x32_bf16 v[180:183], a[76:79], a[24:27], v[180:183]\n"
      "v_exp_f32 v113, v153\n"
      "v_mfma_f32_16x16x32_bf16 v[180:183], a[68:71], a[8:11], v[180:183]\n"
      "ds_read_b64_tr_b16 a[124:125], v247 offset:33792\n"
      "v_cvt_pk_bf16_f32 v204, v112, v113\n"
      "v_mfma_f32_16x16x32_bf16 v[188:191], a[68:71], a[12:15], v[236:239]\n"
      "v_exp_f32 v114, v154\n"
      "v_mfma_f32_16x16x32_bf16 v[188:191], a[76:79], a[28:31], v[188:191]\n"
      "ds_read_b64_tr_b16 a[126:127], v247 offset:37888\n"
      "v_mfma_f32_16x16x32_bf16 v[188:191], a[84:87], a[44:47], v[188:191]\n"
      "v_exp_f32 v115, v155\n"
      "v_mfma_f32_16x16x32_bf16 v[188:191], a[92:95], a[60:63], v[188:191]\n"
      "v_cvt_pk_bf16_f32 v205, v114, v115\n"
      "v_mfma_f32_16x16x32_bf16 v[184:187], a[88:91], a[60:63], v[236:239]\n"
      "v_exp_f32 v112, v156\n"
      "v_mfma_f32_16x16x32_bf16 v[184:187], a[80:83], a[44:47], v[184:187]\n"
      "v_or3_b32 v116, v116, v204, v205\n"
      "v_mfma_f32_16x16x32_bf16 v[184:187], a[72:75], a[28:31], v[184:187]\n"
      "v_exp_f32 v113, v157\n"
      "v_mfma_f32_16x16x32_bf16 v[184:187], a[64:67], a[12:15], v[184:187]\n"
      "s_waitcnt lgkmcnt(10)\n"
      "v_cvt_pk_bf16_f32 v206, v112, v113\n"
      "v_mfma_f32_16x16x32_bf16 v[32:35], a[96:99], v[208:211], v[32:35]\n"
      "v_exp_f32 v114, v158\n"
      "v_mfma_f32_16x16x32_bf16 v[48:51], a[100:103], v[208:211], v[48:51]\n"
      "v_mfma_f32_16x16x32_bf16 a[128:131], a[104:107], v[208:211], a[128:131]\n"
      "s_waitcnt lgkmcnt(6)\n"
      "v_exp_f32 v115, v159\n"
      "v_mfma_f32_16x16x32_bf16 a[144:147], a[108:111], v[208:211], a[144:147]\n"
      "v_cvt_pk_bf16_f32 v207, v114, v115\n"
      "v_mfma_f32_16x16x32_bf16 v[64:67], a[112:115], v[208:211], v[64:67]\n"
      "s_waitcnt lgkmcnt(2)\n"
      "v_mfma_f32_16x16x32_bf16 v[80:83], a[116:119], v[208:211], v[80:83]\n"
      "v_or3_b32 v116, v116, v206, v207\n"
      "v_mfma_f32_16x16x32_bf16 a[160:163], a[120:123], v[208:211], a[160:163]\n"
      "s_mov_b32 s64, 2\n"
      "s_waitcnt lgkmcnt(0)\n"
      "v_and_b32 v116, 0x40004000, v116\n"
      "v_mfma_f32_16x16x32_bf16 a[176:179], a[124:127], v[208:211], a[176:179]\n"
      "v_cmp_ne_u32 vcc, 0, v116\n"
      "s_cbranch_vccnz FB0_%=\n"
      "RET0_2_%=:\n"
      "v_mfma_f32_16x16x32_bf16 a[180:183], a[124:127], v[212:215], a[180:183]\n"
      "v_exp_f32 v112, v160\n"
      "v_mfma_f32_16x16x32_bf16 a[164:167], a[120:123], v[212:215], a[164:167]\n"
      "v_xor_b32 v242, 0x10000, v242\n"
      "v_mfma_f32_16x16x32_bf16 a[192:195], a[196:199], v[192:195], a[192:195]\n"
      "s_add_u32 m0, s62, 0\n"
      "v_exp_f32 v113, v161\n"
      "v_mfma_f32_16x16x32_bf16 v[84:87], a[116:119], v[212:215], v[84:87]\n"
      "buffer_load_dwordx4 v248, s[20:23], s60 offen nt lds\n"
      "v_cvt_pk_bf16_f32 v208, v112, v113\n"
      "v_xor_b32 v243, 0x10000, v243\n"
      "v_mfma_f32_16x16x32_bf16 v[68:71], a[112:115], v[212:215], v[68:71]\n"
      "s_add_u32 m0, s62, 896\n"
      "v_exp_f32 v114, v162\n"
      "v_mfma_f32_16x16x32_bf16 a[192:195], a[200:203], v[196:199], a[192:195]\n"
      "buffer_load_dwordx4 v248, s[20:23], s60 offen offset:128 nt lds\n"
      "v_xor_b32 v244, 0x10000, v244\n"
      "v_mfma_f32_16x16x32_bf16 a[148:151], a[108:111], v[212:215], a[148:151]\n"
      "s_add_u32 m0, s62, 8192\n"
      "v_exp_f32 v115, v163\n"
      "v_mfma_f32_16x16x32_bf16 a[132:135], a[104:107], v[212:215], a[132:135]\n"
      "s_memrealtime s[68:69]\n"
      "buffer_load_dwordx4 v120, s[20:23], s60 offen nt lds\n"
      "v_cvt_pk_bf16_f32 v209, v114, v115\n"
      "v_xor_b32 v245, 0x10000, v245\n"
      "v_mfma_f32_16x16x32_bf16 a[192:195], a[204:207], v[200:203], a[192:195]\n"
      "s_add_u32 m0, s62, 9088\n"
      "v_exp_f32 v112, v164\n"
      "v_mfma_f32_16x16x32_bf16 v[52:55], a[100:103], v[212:215], v[52:55]\n"
      "s_add_u32 s62, s62, 0x8000\n"
      "buffer_load_dwordx4 v120, s[20:23], s60 offen offset:128 nt lds\n"
      "v_or_b32 v116, v208, v209\n"
      "v_xor_b32 v246, 0x10000, v246\n"
      "v_mfma_f32_16x16x32_bf16 v[36:39], a[96:99], v[212:215], v[36:39]\n"
      "s_and_b32 s62, s62, 0x1ffff\n"
      "v_exp_f32 v113, v165\n"
      "v_mfma_f32_16x16x32_bf16 a[192:195], a[208:211], v[204:207], a[192:195]\n"
      "s_add_u32 s60, s60, s78\n"
      "v_cvt_pk_bf16_f32 v210, v112, v113\n"
      "v_xor_b32 v247, 0x10000, v247\n"
      "s_waitcnt vmcnt(8)\n"
      "s_sub_u32 s63, 11, %[n1]\n"
      "LOOP2_%=:\n"
      "s_waitcnt vmcnt(9) lgkmcnt(0)\n"
      "s_sub_u32 s67, s68, s90\n"
      "s_cmp_lt_i32 s67, 0\n"
      "s_cbranch_scc1 GW4_%=\n"
      "GX4_%=:\n"
      "s_barrier\n"
      "v_mfma_f32_16x16x32_bf16 v[40:43], a[96:99], v[216:219], v[40:43]\n"
      "s_memrealtime s[68:69]\n"
      "ds_read_b128 a[64:67], v242 offset:0\n"
      "v_exp_f32 v114, v166\n"
      "v_mfma_f32_16x16x32_bf16 v[44:47], a[96:99], v[220:223], v[44:47]\n"
      "s_add_u32 m0, s77, 16384\n"
      "ds_read_b128 a[68:71], v242 offset:4096\n"
      "ds_read_b128 a[72:75], v243 offset:0\n"
      "v_mfma_f32_16x16x32_bf16 v[60:63], a[100:103], v[220:223], v[60:63]\n"
      "buffer_load_dwordx4 v249, s[24:27], s61 offen nt lds\n"
      "v_exp_f32 v115, v167\n"
      "v_mfma_f32_16x16x32_bf16 v[56:59], a[100:103], v[216:219], v[56:59]\n"
      "s_add_u32 m0, s77, 17408\n"
      "ds_read_b128 a[76:79], v243 offset:4096\n"
      "v_cvt_pk_bf16_f32 v211, v114, v115\n"
      "v_mfma_f32_16x16x32_bf16 a[136:139], a[104:107], v[216:219], a[136:139]\n"
      "buffer_load_dwordx4 v121, s[24:27], s61 offen nt lds\n"
      "v_exp_f32 v112, v168\n"
      "v_mfma_f32_16x16x32_bf16 a[140:143], a[104:107], v[220:223], a[140:143]\n"
      "s_add_u32 m0, s77, 24576\n"
      "ds_read_b128 a[80:83], v242 offset:1024\n"
      "v_or3_b32 v116, v116, v210, v211\n"
      "v_mfma_f32_16x16x32_bf16 a[156:159], a[108:111], v[220:223], a[156:159]\n"
      "buffer_load_dwordx4 v122, s[24:27], s61 offen nt lds\n"
      "v_exp_f32 v113, v169\n"
      "v_mfma_f32_16x16x32_bf16 a[152:155], a[108:111], v[216:219], a[152:155]\n"
      "s_add_u32 m0, s77, 25600\n"
      "ds_read_b128 a[84:87], v242 offset:5120\n"
      "v_cvt_pk_bf16_f32 v212, v112, v113\n"
      "v_mfma_f32_16x16x32_bf16 v[72:75], a[112:115], v[216:219], v[72:75]\n"
      "s_add_u32 s77, s77, 0x8000\n"
      "buffer_load_dwordx4 v123, s[24:27], s61 offen nt lds\n"
      "v_exp_f32 v114, v170\n"
      "v_mfma_f32_16x16x32_bf16 v[76:79], a[112:115], v[220:223], v[76:79]\n"
      "s_and_b32 s77, s77, 0x1ffff\n"
      "ds_read_b128 a[88:91], v243 offset:1024\n"
      "ds_read_b128 a[92:95], v243 offset:5120\n"
      "v_mfma_f32_16x16x32_bf16 v[92:95], a[116:119], v[220:223], v[92:95]\n"
      "s_add_u32 s61, s61, s78\n"
      "v_exp_f32 v115, v171\n"
      "v_mfma_f32_16x16x32_bf16 v[88:91], a[116:119], v[216:219], v[88:91]\n"
      "v_cvt_pk_bf16_f32 v213, v114, v115\n"
      "v_mfma_f32_16x16x32_bf16 a[168:171], a[120:123], v[216:219], a[168:171]\n"
      "v_exp_f32 v112, v172\n"
      "v_mfma_f32_16x16x32_bf16 a[172:175], a[120:123], v[220:223], a[172:175]\n"
      "v_or3_b32 v116, v116, v212, v213\n"
      "v_mfma_f32_16x16x32_bf16 a[188:191], a[124:127], v[220:223], a[188:191]\n"
      "v_exp_f32 v113, v173\n"
      "v_mfma_f32_16x16x32_bf16 a[184:187], a[124:127], v[216:219], a[184:187]\n"
      "s_waitcnt lgkmcnt(0)\n"
      "v_cvt_pk_bf16_f32 v214, v112, v113\n"
      "v_mfma_f32_16x16x32_bf16 v[128:131], a[64:67], a[0:3], v[224:227]\n"
      "s_sub_u32 s67, s68, s66\n"
      "v_exp_f32 v114, v174\n"
      "v_mfma_f32_16x16x32_bf16 v[128:131], a[72:75], a[16:19], v[128:131]\n"
      "s_cmp_gt_i32 s67, s70\n"
      "v_mfma_f32_16x16x32_bf16 v[128:131], a[80:83], a[32:35], v[128:131]\n"
      "s_cselect_b32 s69, s86, 0\n"
      "v_exp_f32 v115, v175\n"
      "v_mfma_f32_16x16x32_bf16 v[128:131], a[88:91], a[48:51], v[128:131]\n"
      "s_add_u32 s66, s66, s69\n"
      "v_cvt_pk_bf16_f32 v215, v114, v115\n"
      "v_mfma_f32_16x16x32_bf16 v[132:135], a[92:95], a[48:51], v[224:227]\n"
      "s_add_u32 s67, s67, s101\n"
      "v_exp_f32 v112, v176\n"
      "v_mfma_f32_16x16x32_bf16 v[132:135], a[84:87], a[32:35], v[132:135]\n"
      "s_min_i32 s70, s70, s67\n"
      "v_or3_b32 v116, v116, v214, v215\n"
      "v_mfma_f32_16x16x32_bf16 v[132:135], a[76:79], a[16:19], v[132:135]\n"
      "s_max_i32 s70, s70, 85\n"
      "v_exp_f32 v113, v177\n"
      "v_mfma_f32_16x16x32_bf16 v[132:135], a[68:71], a[0:3], v[132:135]\n"
      "s_add_u32 s66, s66, s86\n"
      "v_cvt_pk_bf16_f32 v216, v112, v113\n"
      "v_mfma_f32_16x16x32_bf16 v[140:143], a[68:71], a[4:7], v[228:231]\n"
      "s_and_b32 s76, s80, 31\n"
      "v_exp_f32 v114, v178\n"
      "v_mfma_f32_16x16x32_bf16 v[140:143], a[76:79], a[20:23], v[140:143]\n"
      "s_lshr_b64 s[80:81], s[80:81], 5\n"
      "v_mfma_f32_16x16x32_bf16 v[140:143], a[84:87], a[36:39], v[140:143]\n"
      "s_add_u32 s66, s66, s76\n"
      "v_exp_f32 v115, v179\n"
      "v_mfma_f32_16x16x32_bf16 v[140:143], a[92:95], a[52:55], v[140:143]\n"
      "s_sub_u32 s67, s68, s82\n"
      "v_cvt_pk_bf16_f32 v217, v114, v115\n"
      "v_mfma_f32_16x16x32_bf16 v[136:139], a[88:91], a[52:55], v[228:231]\n"
      "s_min_i32 s76, s67, s83\n"
      "v_exp_f32 v112, v180\n"
      "s_waitcnt vmcnt(8)\n"
      "s_barrier\n"
      "v_mfma_f32_16x16x32_bf16 v[136:139], a[80:83], a[36:39], v[136:139]\n"
      "s_mov_b32 s83, s67\n"
      "ds_read_b64_tr_b16 a[96:97], v244 offset:0\n"
      "v_or3_b32 v116, v116, v216, v217\n"
      "v_mfma_f32_16x16x32_bf16 v[136:139], a[72:75], a[20:23], v[136:139]\n"
      "s_sub_u32 s76, s76, 36\n"
      "v_exp_f32 v113, v181\n"
      "v_mfma_f32_16x16x32_bf16 v[136:139], a[64:67], a[4:7], v[136:139]\n"
      "s_min_i32 s76, s76, 148\n"
      "ds_read_b64_tr_b16 a[98:99], v244 offset:4096\n"
      "v_cvt_pk_bf16_f32 v218, v112, v113\n"
      "v_mfma_f32_16x16x32_bf16 v[144:147], a[64:67], a[8:11], v[232:235]\n"
      "s_max_i32 s76, s76, 70\n"
      "v_exp_f32 v114, v182\n"
      "v_mfma_f32_16x16x32_bf16 v[144:147], a[72:75], a[24:27], v[144:147]\n"
      "s_mov_b32 s82, s68\n"
      "ds_read_b64_tr_b16 a[100:101], v245 offset:0\n"
      "ds_read_b64_tr_b16 a[102:103], v245 offset:4096\n"
      "v_mfma_f32_16x16x32_bf16 v[144:147], a[80:83], a[40:43], v[144:147]\n"
      "s_add_u32 s65, s82, s76\n"
      "v_exp_f32 v115, v183\n"
      "v_mfma_f32_16x16x32_bf16 v[144:147], a[88:91], a[56:59], v[144:147]\n"
      "s_sub_u32 s93, s93, 1\n"
      "ds_read_b64_tr_b16 a[104:105], v246 offset:0\n"
      "v_cvt_pk_bf16_f32 v219, v114, v115\n"
      "v_mfma_f32_16x16x32_bf16 v[148:151], a[92:95], a[56:59], v[232:235]\n"
      "s_cmp_le_i32 s93, 14\n"
      "v_exp_f32 v112, v184\n"
      "v_mfma_f32_16x16x32_bf16 v[148:151], a[84:87], a[40:43], v[148:151]\n"
      "s_cselect_b32 s86, 107, s86\n"
      "ds_read_b64_tr_b16 a[106:107], v246 offset:4096\n"
      "v_or3_b32 v116, v116, v218, v219\n"
      "v_mfma_f32_16x16x32_bf16 v[148:151], a[76:79], a[24:27], v[148:151]\n"
      "s_sub_u32 s67, s65, s66\n"
      "v_exp_f32 v113, v185\n"
      "v_mfma_f32_16x16x32_bf16 v[148:151], a[68:71], a[8:11], v[148:151]\n"
      "s_cmp_lt_i32 s67, 0\n"
      "ds_read_b64_tr_b16 a[108:109], v247 offset:0\n"
      "v_cvt_pk_bf16_f32 v220, v112, v113\n"
      "v_mfma_f32_16x16x32_bf16 v[156:159], a[68:71], a[12:15], v[236:239]\n"
      "s_cselect_b32 s88, s66, s65\n"
      "v_exp_f32 v114, v186\n"
      "v_mfma_f32_16x16x32_bf16 v[156:159], a[76:79], a[28:31], v[156:159]\n"
      "s_add_u32 s90, s88, s91\n"
      "ds_read_b64_tr_b16 a[110:111], v247 offset:4096\n"
      "ds_read_b64_tr_b16 a[112:113], v244 offset:1024\n"
      "v_mfma_f32_16x16x32_bf16 v[156:159], a[84:87], a[44:47], v[156:159]\n"
      "v_exp_f32 v115, v187\n"
      "v_mfma_f32_16x16x32_bf16 v[156:159], a[92:95], a[60:63], v[156:159]\n"
      "ds_read_b64_tr_b16 a[114:115], v244 offset:5120\n"
      "v_cvt_pk_bf16_f32 v221, v114, v115\n"
      "v_mfma_f32_16x16x32_bf16 v[152:155], a[88:91], a[60:63], v[236:239]\n"
      "v_exp_f32 v112, v188\n"
      "v_mfma_f32_16x16x32_bf16 v[152:155], a[80:83], a[44:47], v[152:155]\n"
      "ds_read_b64_tr_b16 a[116:117], v245 offset:1024\n"
      "v_or3_b32 v116, v116, v220, v221\n"
      "v_mfma_f32_16x16x32_bf16 v[152:155], a[72:75], a[28:31], v[152:155]\n"
      "v_exp_f32 v113, v189\n"
      "v_mfma_f32_16x16x32_bf16 v[152:155], a[64:67], a[12:15], v[152:155]\n"
      "ds_read_b64_tr_b16 a[118:119], v245 offset:5120\n"
      "v_cvt_pk_bf16_f32 v222, v112, v113\n"
      "s_waitcnt lgkmcnt(6)\n"
      "v_mfma_f32_16x16x32_bf16 v[32:35], a[96:99], v[192:195], v[32:35]\n"
      "v_exp_f32 v114, v190\n"
      "v_mfma_f32_16x16x32_bf16 v[48:51], a[100:103], v[192:195], v[48:51]\n"
      "ds_read_b64_tr_b16 a[120:121], v246 offset:1024\n"
      "ds_read_b64_tr_b16 a[122:123], v246 offset:5120\n"
      "v_mfma_f32_16x16x32_bf16 a[128:131], a[104:107], v[192:195], a[128:131]\n"
      "s_waitcnt lgkmcnt(4)\n"
      "v_exp_f32 v115, v191\n"
      "v_mfma_f32_16x16x32_bf16 a[144:147], a[108:111], v[192:195], a[144:147]\n"
      "ds_read_b64_tr_b16 a[124:125], v247 offset:1024\n"
      "v_cvt_pk_bf16_f32 v223, v114, v115\n"
      "v_mfma_f32_16x16x32_bf16 v[64:67], a[112:115], v[192:195], v[64:67]\n"
      "ds_read_b64_tr_b16 a[126:127], v247 offset:5120\n"
      "s_waitcnt lgkmcnt(2)\n"
      "v_mfma_f32_16x16x32_bf16 v[80:83], a[116:119], v[192:195], v[80:83]\n"
      "v_or3_b32 v116, v116, v222, v223\n"
      "v_mfma_f32_16x16x32_bf16 a[160:163], a[120:123], v[192:195], a[160:163]\n"
      "s_mov_b32 s64, 3\n"
      "s_waitcnt lgkmcnt(0)\n"
      "v_and_b32 v116, 0x40004000, v116\n"
      "v_mfma_f32_16x16x32_bf16 a[176:179], a[124:127], v[192:195], a[176:179]\n"
      "v_cmp_ne_u32 vcc, 0, v116\n"
      "s_cbranch_vccnz FB1_%=\n"
      "RET1_3_%=:\n"
      "v_mfma_f32_16x16x32_bf16 a[180:183], a[124:127], v[196:199], a[180:183]\n"
      "v_exp_f32 v112, v128\n"
      "v_mfma_f32_16x16x32_bf16 a[164:167], a[120:123], v[196:199], a[164:167]\n"
      "v_mfma_f32_16x16x32_bf16 a[192:195], a[196:199], v[208:211], a[192:195]\n"
      "s_add_u32 m0, s62, 0\n"
      "v_exp_f32 v113, v129\n"
      "v_mfma_f32_16x16x32_bf16 v[84:87], a[116:119], v[196:199], v[84:87]\n"
      "buffer_load_dwordx4 v248, s[20:23], s60 offen nt lds\n"
      "v_cvt_pk_bf16_f32 v192, v112, v113\n"
      "v_mfma_f32_16x16x32_bf16 v[68:71], a[112:115], v[196:199], v[68:71]\n"
      "s_add_u32 m0, s62, 896\n"
      "v_exp_f32 v114, v130\n"
      "v_mfma_f32_16x16x32_bf16 a[192:195], a[200:203], v[212:215], a[192:195]\n"
      "buffer_load_dwordx4 v248, s[20:23], s60 offen offset:128 nt lds\n"
      "v_mfma_f32_16x16x32_bf16 a[148:151], a[108:111], v[196:199], a[148:151]\n"
      "s_add_u32 m0, s62, 8192\n"
      "v_exp_f32 v115, v131\n"
      "v_mfma_f32_16x16x32_bf16 a[132:135], a[104:107], v[196:199], a[132:135]\n"
      "s_memrealtime s[68:69]\n"
      "buffer_load_dwordx4 v120, s[20:23], s60 offen nt lds\n"
      "v_cvt_pk_bf16_f32 v193, v114, v115\n"
      "v_mfma_f32_16x16x32_bf16 a[192:195], a[204:207], v[216:219], a[192:195]\n"
      "s_add_u32 m0, s62, 9088\n"
      "v_exp_f32 v112, v132\n"
      "v_mfma_f32_16x16x32_bf16 v[52:55], a[100:103], v[196:199], v[52:55]\n"
      "s_add_u32 s62, s62, 0x8000\n"
      "buffer_load_dwordx4 v120, s[20:23], s60 offen offset:128 nt lds\n"
      "v_or_b32 v116, v192, v193\n"
      "v_mfma_f32_16x16x32_bf16 v[36:39], a[96:99], v[196:199], v[36:39]\n"
      "s_and_b32 s62, s62, 0x1ffff\n"
      "v_exp_f32 v113, v133\n"
      "v_mfma_f32_16x16x32_bf16 a[192:195], a[208:211], v[220:223], a[192:195]\n"
      "s_add_u32 s60, s60, %[inc]\n"
      "v_cvt_pk_bf16_f32 v194, v112, v113\n"
      "s_waitcnt vmcnt(8) lgkmcnt(0)\n"
      "s_sub_u32 s67, s68, s90\n"
      "s_cmp_lt_i32 s67, 0\n"
      "s_cbranch_scc1 GW5_%=\n"
      "GX5_%=:\n"
      "s_barrier\n"
      "v_mfma_f32_16x16x32_bf16 v[40:43], a[96:99], v[200:203], v[40:43]\n"
      "s_memrealtime s[68:69]\n"
      "buffer_load_dword v250, v251, s[28:31], 0 offen sc1\n"
      "ds_read_b128 a[64:67], v242 offset:32768\n"
      "v_exp_f32 v114, v134\n"
      "v_mfma_f32_16x16x32_bf16 v[44:47], a[96:99], v[204:207], v[44:47]\n"
      "s_add_u32 m0, s77, 16384\n"
      "ds_read_b128 a[68:71], v242 offset:36864\n"
      "ds_read_b128 a[72:75], v243 offset:32768\n"
      "v_mfma_f32_16x16x32_bf16 v[60:63], a[100:103], v[204:207], v[60:63]\n"
      "buffer_load_dwordx4 v249, s[24:27], s61 offen nt lds\n"
      "v_exp_f32 v115, v135\n"
      "v_mfma_f32_16x16x32_bf16 v[56:59], a[100:103], v[200:203], v[56:59]\n"
      "s_add_u32 m0, s77, 17408\n"
      "ds_read_b128 a[76:79], v243 offset:36864\n"
      "v_cvt_pk_bf16_f32 v195, v114, v115\n"
      "v_mfma_f32_16x16x32_bf16 a[136:139], a[104:107], v[200:203], a[136:139]\n"
      "buffer_load_dwordx4 v121, s[24:27], s61 offen nt lds\n"
      "v_exp_f32 v112, v136\n"
      "v_mfma_f32_16x16x32_bf16 a[140:143], a[104:107], v[204:207], a[140:143]\n"
      "s_add_u32 m0, s77, 24576\n"
      "ds_read_b128 a[80:83], v242 offset:33792\n"
      "v_or3_b32 v116, v116, v194, v195\n"
      "v_mfma_f32_16x16x32_bf16 a[156:159], a[108:111], v[204:207], a[156:159]\n"
      "buffer_load_dwordx4 v122, s[24:27], s61 offen nt lds\n"
      "v_exp_f32 v113, v137\n"
      "v_mfma_f32_16x16x32_bf16 a[152:155], a[108:111], v[200:203], a[152:155]\n"
      "s_add_u32 m0, s77, 25600\n"
      "ds_read_b128 a[84:87], v242 offset:37888\n"
      "v_cvt_pk_bf16_f32 v196, v112, v113\n"
      "v_mfma_f32_16x16x32_bf16 v[72:75], a[112:115], v[200:203], v[72:75]\n"
      "s_add_u32 s77, s77, 0x8000\n"
      "buffer_load_dwordx4 v123, s[24:27], s61 offen nt lds\n"
      "v_exp_f32 v114, v138\n"
      "v_mfma_f32_16x16x32_bf16 v[76:79], a[112:115], v[204:207], v[76:79]\n"
      "s_and_b32 s77, s77, 0x1ffff\n"
      "ds_read_b128 a[88:91], v243 offset:33792\n"
      "ds_read_b128 a[92:95], v243 offset:37888\n"
      "v_mfma_f32_16x16x32_bf16 v[92:95], a[116:119], v[204:207], v[92:95]\n"
      "s_add_u32 s61, s61, %[inc]\n"
      "v_exp_f32 v115, v139\n"
      "v_mfma_f32_16x16x32_bf16 v[88:91], a[116:119], v[200:203], v[88:91]\n"
      "v_cvt_pk_bf16_f32 v197, v114, v115\n"
      "v_mfma_f32_16x16x32_bf16 a[168:171], a[120:123], v[200:203], a[168:171]\n"
      "v_exp_f32 v112, v140\n"
      "v_mfma_f32_16x16x32_bf16 a[172:175], a[120:123], v[204:207], a[172:175]\n"
      "v_or3_b32 v116, v116, v196, v197\n"
      "v_mfma_f32_16x16x32_bf16 a[188:191], a[124:127], v[204:207], a[188:191]\n"
      "v_exp_f32 v113, v141\n"
      "v_mfma_f32_16x16x32_bf16 a[184:187], a[124:127], v[200:203], a[184:187]\n"
      "s_waitcnt lgkmcnt(0)\n"
      "v_cvt_pk_bf16_f32 v198, v112, v113\n"
      "v_mfma_f32_16x16x32_bf16 v[160:163], a[64:67], a[0:3], v[224:227]\n"
      "s_sub_u32 s67, s68, s66\n"
      "v_exp_f32 v114, v142\n"
      "v_mfma_f32_16x16x32_bf16 v[160:163], a[72:75], a[16:19], v[160:163]\n"
      "s_cmp_gt_i32 s67, s70\n"
      "v_mfma_f32_16x16x32_bf16 v[160:163], a[80:83], a[32:35], v[160:163]\n"
      "s_cselect_b32 s69, s86, 0\n"
      "v_exp_f32 v115, v143\n"
      "v_mfma_f32_16x16x32_bf16 v[160:163], a[88:91], a[48:51], v[160:163]\n"
      "s_add_u32 s66, s66, s69\n"
      "v_cvt_pk_bf16_f32 v199, v114, v115\n"
      "v_mfma_f32_16x16x32_bf16 v[164:167], a[92:95], a[48:51], v[224:227]\n"
      "s_add_u32 s67, s67, s101\n"
      "v_exp_f32 v112, v144\n"
      "v_mfma_f32_16x16x32_bf16 v[164:167], a[84:87], a[32:35], v[164:167]\n"
      "s_min_i32 s70, s70, s67\n"
      "v_or3_b32 v116, v116, v198, v199\n"
      "v_mfma_f32_16x16x32_bf16 v[164:167], a[76:79], a[16:19], v[164:167]\n"
      "s_max_i32 s70, s70, 85\n"
      "v_exp_f32 v113, v145\n"
      "v_mfma_f32_16x16x32_bf16 v[164:167], a[68:71], a[0:3], v[164:167]\n"
      "s_add_u32 s66, s66, s86\n"
      "v_cvt_pk_bf16_f32 v200, v112, v113\n"
      "v_mfma_f32_16x16x32_bf16 v[172:175], a[68:71], a[4:7], v[228:231]\n"
      "s_and_b32 s76, s80, 31\n"
      "v_exp_f32 v114, v146\n"
      "v_mfma_f32_16x16x32_bf16 v[172:175], a[76:79], a[20:23], v[172:175]\n"
      "s_lshr_b64 s[80:81], s[80:81], 5\n"
      "v_mfma_f32_16x16x32_bf16 v[172:175], a[84:87], a[36:39], v[172:175]\n"
      "s_add_u32 s66, s66, s76\n"
      "v_exp_f32 v115, v147\n"
      "v_mfma_f32_16x16x32_bf16 v[172:175], a[92:95], a[52:55], v[172:175]\n"
      "s_sub_u32 s67, s68, s82\n"
      "v_cvt_pk_bf16_f32 v201, v114, v115\n"
      "v_mfma_f32_16x16x32_bf16 v[168:171], a[88:91], a[52:55], v[228:231]\n"
      "s_min_i32 s76, s67, s83\n"
      "v_exp_f32 v112, v148\n"
      "s_waitcnt vmcnt(9)\n"
      "s_barrier\n"
      "v_mfma_f32_16x16x32_bf16 v[168:171], a[80:83], a[36:39], v[168:171]\n"
      "s_mov_b32 s83, s67\n"
      "ds_read_b64_tr_b16 a[96:97], v244 offset:32768\n"
      "v_or3_b32 v116, v116, v200, v201\n"
      "v_mfma_f32_16x16x32_bf16 v[168:171], a[72:75], a[20:23], v[168:171]\n"
      "s_sub_u32 s76, s76, 36\n"
      "v_exp_f32 v113, v149\n"
      "v_mfma_f32_16x16x32_bf16 v[168:171], a[64:67], a[4:7], v[168:171]\n"
      "s_min_i32 s76, s76, 148\n"
      "ds_read_b64_tr_b16 a[98:99], v244 offset:36864\n"
      "v_cvt_pk_bf16_f32 v202, v112, v113\n"
      "v_mfma_f32_16x16x32_bf16 v[176:179], a[64:67], a[8:11], v[232:235]\n"
      "s_max_i32 s76, s76, 70\n"
      "v_exp_f32 v114, v150\n"
      "v_mfma_f32_16x16x32_bf16 v[176:179], a[72:75], a[24:27], v[176:179]\n"
      "s_mov_b32 s82, s68\n"
      "ds_read_b64_tr_b16 a[100:101], v245 offset:32768\n"
      "ds_read_b64_tr_b16 a[102:103], v245 offset:36864\n"
      "v_mfma_f32_16x16x32_bf16 v[176:179], a[80:83], a[40:43], v[176:179]\n"
      "s_add_u32 s65, s82, s76\n"
      "v_exp_f32 v115, v151\n"
      "v_mfma_f32_16x16x32_bf16 v[176:179], a[88:91], a[56:59], v[176:179]\n"
      "s_sub_u32 s93, s93, 1\n"
      "ds_read_b64_tr_b16 a[104:105], v246 offset:32768\n"
      "v_cvt_pk_bf16_f32 v203, v114, v115\n"
      "v_mfma_f32_16x16x32_bf16 v[180:183], a[92:95], a[56:59], v[232:235]\n"
      "s_cmp_le_i32 s93, 14\n"
      "v_exp_f32 v112, v152\n"
      "v_mfma_f32_16x16x32_bf16 v[180:183], a[84:87], a[40:43], v[180:183]\n"
      "s_cselect_b32 s86, 107, s86\n"
      "ds_read_b64_tr_b16 a[106:107], v246 offset:36864\n"
      "v_or3_b32 v116, v116, v202, v203\n"
      "v_mfma_f32_16x16x32_bf16 v[180:183], a[76:79], a[24:27], v[180:183]\n"
      "s_sub_u32 s67, s65, s66\n"
      "v_exp_f32 v113, v153\n"
      "v_mfma_f32_16x16x32_bf16 v[180:183], a[68:71], a[8:11], v[180:183]\n"
      "s_cmp_lt_i32 s67, 0\n"
      "ds_read_b64_tr_b16 a[108:109], v247 offset:32768\n"
      "v_cvt_pk_bf16_f32 v204, v112, v113\n"
      "v_mfma_f32_16x16x32_bf16 v[188:191], a[68:71], a[12:15], v[236:239]\n"
      "s_cselect_b32 s88, s66, s65\n"
      "v_exp_f32 v114, v154\n"
      "v_mfma_f32_16x16x32_bf16 v[188:191], a[76:79], a[28:31], v[188:191]\n"
      "s_add_u32 s90, s88, s91\n"
      "ds_read_b64_tr_b16 a[110:111], v247 offset:36864\n"
      "ds_read_b64_tr_b16 a[112:113], v244 offset:33792\n"
      "v_mfma_f32_16x16x32_bf16 v[188:191], a[84:87], a[44:47], v[188:191]\n"
      "v_exp_f32 v115, v155\n"
      "v_mfma_f32_16x16x32_bf16 v[188:191], a[92:95], a[60:63], v[188:191]\n"
      "ds_read_b64_tr_b16 a[114:115], v244 offset:37888\n"
      "v_cvt_pk_bf16_f32 v205, v114, v115\n"
      "v_mfma_f32_16x16x32_bf16 v[184:187], a[88:91], a[60:63], v[236:239]\n"
      "v_exp_f32 v112, v156\n"
      "v_mfma_f32_16x16x32_bf16 v[184:187], a[80:83], a[44:47], v[184:187]\n"
      "ds_read_b64_tr_b16 a[116:117], v245 offset:33792\n"
      "v_or3_b32 v116, v116, v204, v205\n"
      "v_mfma_f32_16x16x32_bf16 v[184:187], a[72:75], a[28:31], v[184:187]\n"
      "v_exp_f32 v113, v157\n"
      "v_mfma_f32_16x16x32_bf16 v[184:187], a[64:67], a[12:15], v[184:187]\n"
      "ds_read_b64_tr_b16 a[118:119], v245 offset:37888\n"
      "v_cvt_pk_bf16_f32 v206, v112, v113\n"
      "s_waitcnt lgkmcnt(6)\n"
      "v_mfma_f32_16x16x32_bf16 v[32:35], a[96:99], v[208:211], v[32:35]\n"
      "v_exp_f32 v114, v158\n"
      "v_mfma_f32_16x16x32_bf16 v[48:51], a[100:103], v[208:211], v[48:51]\n"
      "ds_read_b64_tr_b16 a[120:121], v246 offset:33792\n"
      "ds_read_b64_tr_b16 a[122:123], v246 offset:37888\n"
      "v_mfma_f32_16x16x32_bf16 a[128:131], a[104:107], v[208:211], a[128:131]\n"
      "s_waitcnt lgkmcnt(4)\n"
      "v_exp_f32 v115, v159\n"
      "v_mfma_f32_16x16x32_bf16 a[144:147], a[108:111], v[208:211], a[144:147]\n"
      "ds_read_b64_tr_b16 a[124:125], v247 offset:33792\n"
      "v_cvt_pk_bf16_f32 v207, v114, v115\n"
      "v_mfma_f32_16x16x32_bf16 v[64:67], a[112:115], v[208:211], v[64:67]\n"
      "ds_read_b64_tr_b16 a[126:127], v247 offset:37888\n"
      "s_waitcnt lgkmcnt(2)\n"
      "v_mfma_f32_16x16x32_bf16 v[80:83], a[116:119], v[208:211], v[80:83]\n"
      "v_or3_b32 v116, v116, v206, v207\n"
      "v_mfma_f32_16x16x32_bf16 a[160:163], a[120:123], v[208:211], a[160:163]\n"
      "s_mov_b32 s64, 3\n"
      "s_waitcnt lgkmcnt(0)\n"
      "v_and_b32 v116, 0x40004000, v116\n"
      "v_mfma_f32_16x16x32_bf16 a[176:179], a[124:127], v[208:211], a[176:179]\n"
      "v_cmp_ne_u32 vcc, 0, v116\n"
      "s_cbranch_vccnz FB0_%=\n"
      "RET0_3_%=:\n"
      "v_mfma_f32_16x16x32_bf16 a[180:183], a[124:127], v[212:215], a[180:183]\n"
      "v_exp_f32 v112, v160\n"
      "v_mfma_f32_16x16x32_bf16 a[164:167], a[120:123], v[212:215], a[164:167]\n"
      "v_xor_b32 v242, 0x10000, v242\n"
      "v_mfma_f32_16x16x32_bf16 a[192:195], a[196:199], v[192:195], a[192:195]\n"
      "s_add_u32 m0, s62, 0\n"
      "v_exp_f32 v113, v161\n"
      "v_mfma_f32_16x16x32_bf16 v[84:87], a[116:119], v[212:215], v[84:87]\n"
      "buffer_load_dwordx4 v248, s[20:23], s60 offen nt lds\n"
      "v_cvt_pk_bf16_f32 v208, v112, v113\n"
      "v_xor_b32 v243, 0x10000, v243\n"
      "v_mfma_f32_16x16x32_bf16 v[68:71], a[112:115], v[212:215], v[68:71]\n"
      "s_add_u32 m0, s62, 896\n"
      "v_exp_f32 v114, v162\n"
      "v_mfma_f32_16x16x32_bf16 a[192:195], a[200:203], v[196:199], a[192:195]\n"
      "buffer_load_dwordx4 v248, s[20:23], s60 offen offset:128 nt lds\n"
      "v_xor_b32 v244, 0x10000, v244\n"
      "v_mfma_f32_16x16x32_bf16 a[148:151], a[108:111], v[212:215], a[148:151]\n"
      "s_add_u32 m0, s62, 8192\n"
      "v_exp_f32 v115, v163\n"
      "v_mfma_f32_16x16x32_bf16 a[132:135], a[104:107], v[212:215], a[132:135]\n"
      "s_memrealtime s[68:69]\n"
      "buffer_load_dwordx4 v120, s[20:23], s60 offen nt lds\n"
      "v_cvt_pk_bf16_f32 v209, v114, v115\n"
      "v_xor_b32 v245, 0x10000, v245\n"
      "v_mfma_f32_16x16x32_bf16 a[192:195], a[204:207], v[200:203], a[192:195]\n"
      "s_add_u32 m0, s62, 9088\n"
      "v_exp_f32 v112, v164\n"
      "v_mfma_f32_16x16x32_bf16 v[52:55], a[100:103], v[212:215], v[52:55]\n"
      "s_add_u32 s62, s62, 0x8000\n"
      "buffer_load_dwordx4 v120, s[20:23], s60 offen offset:128 nt lds\n"
      "v_or_b32 v116, v208, v209\n"
      "v_xor_b32 v246, 0x10000, v246\n"
      "v_mfma_f32_16x16x32_bf16 v[36:39], a[96:99], v[212:215], v[36:39]\n"
      "s_and_b32 s62, s62, 0x1ffff\n"
      "s_add_u32 s60, s60, s78\n"
      "v_exp_f32 v113, v165\n"
      "v_mfma_f32_16x16x32_bf16 a[192:195], a[208:211], v[204:207], a[192:195]\n"
      "s_sub_u32 s63, s63, 1\n"
      "v_cvt_pk_bf16_f32 v210, v112, v113\n"
      "v_xor_b32 v247, 0x10000, v247\n"
      "s_cbranch_scc0 LOOP2_%=\n"
      "s_waitcnt vmcnt(9) lgkmcnt(0)\n"
      "s_sub_u32 s67, s68, s90\n"
      "s_cmp_lt_i32 s67, 0\n"
      "s_cbranch_scc1 GW6_%=\n"
      "GX6_%=:\n"
      "s_barrier\n"
      "v_mfma_f32_16x16x32_bf16 v[40:43], a[96:99], v[216:219], v[40:43]\n"
      "s_memrealtime s[68:69]\n"
      "ds_read_b128 a[64:67], v242 offset:0\n"
      "v_exp_f32 v114, v166\n"
      "v_mfma_f32_16x16x32_bf16 v[44:47], a[96:99], v[220:223], v[44:47]\n"
      "s_add_u32 m0, %[wb], 50176\n"
      "ds_read_b128 a[68:71], v242 offset:4096\n"
      "ds_read_b128 a[72:75], v243 offset:0\n"
      "v_mfma_f32_16x16x32_bf16 v[60:63], a[100:103], v[220:223], v[60:63]\n"
      "s_add_u32 s94, s61, s78\n"
      "buffer_load_dwordx4 v121, s[24:27], s61 offen nt lds\n"
      "v_exp_f32 v115, v167\n"
      "v_mfma_f32_16x16x32_bf16 v[56:59], a[100:103], v[216:219], v[56:59]\n"
      "s_add_u32 m0, %[wb], 58368\n"
      "ds_read_b128 a[76:79], v243 offset:4096\n"
      "v_cvt_pk_bf16_f32 v211, v114, v115\n"
      "v_mfma_f32_16x16x32_bf16 a[136:139], a[104:107], v[216:219], a[136:139]\n"
      "s_add_u32 s95, s61, 0x10000\n"
      "buffer_load_dwordx4 v123, s[24:27], s61 offen nt lds\n"
      "v_exp_f32 v112, v168\n"
      "v_mfma_f32_16x16x32_bf16 a[140:143], a[104:107], v[220:223], a[140:143]\n"
      "s_add_u32 s77, s94, 0x10000\n"
      "ds_read_b128 a[80:83], v242 offset:1024\n"
      "v_or3_b32 v116, v116, v210, v211\n"
      "v_mfma_f32_16x16x32_bf16 a[156:159], a[108:111], v[220:223], a[156:159]\n"
      "s_add_u32 s78, s61, 0x20000\n"
      "v_exp_f32 v113, v169\n"
      "v_mfma_f32_16x16x32_bf16 a[152:155], a[108:111], v[216:219], a[152:155]\n"
      "ds_read_b128 a[84:87], v242 offset:5120\n"
      "v_cvt_pk_bf16_f32 v212, v112, v113\n"
      "v_mfma_f32_16x16x32_bf16 v[72:75], a[112:115], v[216:219], v[72:75]\n"
      "v_exp_f32 v114, v170\n"
      "v_mfma_f32_16x16x32_bf16 v[76:79], a[112:115], v[220:223], v[76:79]\n"
      "ds_read_b128 a[88:91], v243 offset:1024\n"
      "ds_read_b128 a[92:95], v243 offset:5120\n"
      "v_mfma_f32_16x16x32_bf16 v[92:95], a[116:119], v[220:223], v[92:95]\n"
      "v_exp_f32 v115, v171\n"
      "v_mfma_f32_16x16x32_bf16 v[88:91], a[116:119], v[216:219], v[88:91]\n"
      "v_cvt_pk_bf16_f32 v213, v114, v115\n"
      "v_mfma_f32_16x16x32_bf16 a[168:171], a[120:123], v[216:219], a[168:171]\n"
      "v_exp_f32 v112, v172\n"
      "v_mfma_f32_16x16x32_bf16 a[172:175], a[120:123], v[220:223], a[172:175]\n"
      "v_or3_b32 v116, v116, v212, v213\n"
      "v_mfma_f32_16x16x32_bf16 a[188:191], a[124:127], v[220:223], a[188:191]\n"
      "v_exp_f32 v113, v173\n"
      "v_mfma_f32_16x16x32_bf16 a[184:187], a[124:127], v[216:219], a[184:187]\n"
      "s_waitcnt lgkmcnt(0)\n"
      "v_cvt_pk_bf16_f32 v214, v112, v113\n"
      "v_mfma_f32_16x16x32_bf16 v[128:131], a[64:67], a[0:3], v[224:227]\n"
      "s_sub_u32 s67, s68, s66\n"
      "v_exp_f32 v114, v174\n"
      "v_mfma_f32_16x16x32_bf16 v[128:131], a[72:75], a[16:19], v[128:131]\n"
      "s_cmp_gt_i32 s67, s70\n"
      "v_mfma_f32_16x16x32_bf16 v[128:131], a[80:83], a[32:35], v[128:131]\n"
      "s_cselect_b32 s69, s86, 0\n"
      "v_exp_f32 v115, v175\n"
      "v_mfma_f32_16x16x32_bf16 v[128:131], a[88:91], a[48:51], v[128:131]\n"
      "s_add_u32 s66, s66, s69\n"
      "v_cvt_pk_bf16_f32 v215, v114, v115\n"
      "v_mfma_f32_16x16x32_bf16 v[132:135], a[92:95], a[48:51], v[224:227]\n"
      "s_add_u32 s67, s67, s101\n"
      "v_exp_f32 v112, v176\n"
      "v_mfma_f32_16x16x32_bf16 v[132:135], a[84:87], a[32:35], v[132:135]\n"
      "s_min_i32 s70, s70, s67\n"
      "v_or3_b32 v116, v116, v214, v215\n"
      "v_mfma_f32_16x16x32_bf16 v[132:135], a[76:79], a[16:19], v[132:135]\n"
      "s_max_i32 s70, s70, 85\n"
      "v_exp_f32 v113, v177\n"
      "v_mfma_f32_16x16x32_bf16 v[132:135], a[68:71], a[0:3], v[132:135]\n"
      "s_add_u32 s66, s66, s86\n"
      "v_cvt_pk_bf16_f32 v216, v112, v113\n"
      "v_mfma_f32_16x16x32_bf16 v[140:143], a[68:71], a[4:7], v[228:231]\n"
      "s_and_b32 s76, s80, 31\n"
      "v_exp_f32 v114, v178\n"
      "v_mfma_f32_16x16x32_bf16 v[140:143], a[76:79], a[20:23], v[140:143]\n"
      "s_lshr_b64 s[80:81], s[80:81], 5\n"
      "v_mfma_f32_16x16x32_bf16 v[140:143], a[84:87], a[36:39], v[140:143]\n"
      "s_add_u32 s66, s66, s76\n"
      "v_exp_f32 v115, v179\n"
      "v_mfma_f32_16x16x32_bf16 v[140:143], a[92:95], a[52:55], v[140:143]\n"
      "s_sub_u32 s67, s68, s82\n"
      "v_cvt_pk_bf16_f32 v217, v114, v115\n"
      "v_mfma_f32_16x16x32_bf16 v[136:139], a[88:91], a[52:55], v[228:231]\n"
      "s_min_i32 s76, s67, s83\n"
      "v_exp_f32 v112, v180\n"
      "s_waitcnt vmcnt(6)\n"
      "s_barrier\n"
      "v_mfma_f32_16x16x32_bf16 v[136:139], a[80:83], a[36:39], v[136:139]\n"
      "s_mov_b32 s83, s67\n"
      "ds_read_b64_tr_b16 a[96:97], v244 offset:0\n"
      "v_or3_b32 v116, v116, v216, v217\n"
      "v_mfma_f32_16x16x32_bf16 v[136:139], a[72:75], a[20:23], v[136:139]\n"
      "s_add_u32 m0, s62, 0\n"
      "v_exp_f32 v113, v181\n"
      "v_mfma_f32_16x16x32_bf16 v[136:139], a[64:67], a[4:7], v[136:139]\n"
      "s_sub_u32 s76, s76, 36\n"
      "ds_read_b64_tr_b16 a[98:99], v244 offset:4096\n"
      "buffer_load_dwordx4 v248, s[20:23], s60 offen nt lds\n"
      "v_cvt_pk_bf16_f32 v218, v112, v113\n"
      "v_mfma_f32_16x16x32_bf16 v[144:147], a[64:67], a[8:11], v[232:235]\n"
      "s_add_u32 m0, s62, 896\n"
      "v_exp_f32 v114, v182\n"
      "v_mfma_f32_16x16x32_bf16 v[144:147], a[72:75], a[24:27], v[144:147]\n"
      "s_min_i32 s76, s76, 148\n"
      "ds_read_b64_tr_b16 a[100:101], v245 offset:0\n"
      "ds_read_b64_tr_b16 a[102:103], v245 offset:4096\n"
      "buffer_load_dwordx4 v248, s[20:23], s60 offen offset:128 nt lds\n"
      "v_mfma_f32_16x16x32_bf16 v[144:147], a[80:83], a[40:43], v[144:147]\n"
      "s_add_u32 m0, s62, 8192\n"
      "v_exp_f32 v115, v183\n"
      "v_mfma_f32_16x16x32_bf16 v[144:147], a[88:91], a[56:59], v[144:147]\n"
      "s_max_i32 s76, s76, 70\n"
      "ds_read_b64_tr_b16 a[104:105], v246 offset:0\n"
      "buffer_load_dwordx4 v120, s[20:23], s60 offen nt lds\n"
      "v_cvt_pk_bf16_f32 v219, v114, v115\n"
      "v_mfma_f32_16x16x32_bf16 v[148:151], a[92:95], a[56:59], v[232:235]\n"
      "s_add_u32 m0, s62, 9088\n"
      "v_exp_f32 v112, v184\n"
      "v_mfma_f32_16x16x32_bf16 v[148:151], a[84:87], a[40:43], v[148:151]\n"
      "s_add_u32 s62, s62, 0x8000\n"
      "ds_read_b64_tr_b16 a[106:107], v246 offset:4096\n"
      "buffer_load_dwordx4 v120, s[20:23], s60 offen offset:128 nt lds\n"
      "v_or3_b32 v116, v116, v218, v219\n"
      "v_mfma_f32_16x16x32_bf16 v[148:151], a[76:79], a[24:27], v[148:151]\n"
      "s_and_b32 s62, s62, 0x1ffff\n"
      "v_exp_f32 v113, v185\n"
      "v_mfma_f32_16x16x32_bf16 v[148:151], a[68:71], a[8:11], v[148:151]\n"
      "s_add_u32 s60, s60, %[inc]\n"
      "ds_read_b64_tr_b16 a[108:109], v247 offset:0\n"
      "v_cvt_pk_bf16_f32 v220, v112, v113\n"
      "v_mfma_f32_16x16x32_bf16 v[156:159], a[68:71], a[12:15], v[236:239]\n"
      "s_mov_b32 s82, s68\n"
      "v_exp_f32 v114, v186\n"
      "v_mfma_f32_16x16x32_bf16 v[156:159], a[76:79], a[28:31], v[156:159]\n"
      "s_sub_u32 s66, s66, s99\n"
      "ds_read_b64_tr_b16 a[110:111], v247 offset:4096\n"
      "ds_read_b64_tr_b16 a[112:113], v244 offset:1024\n"
      "v_mfma_f32_16x16x32_bf16 v[156:159], a[84:87], a[44:47], v[156:159]\n"
      "s_sub_u32 s82, s82, s99\n"
      "v_exp_f32 v115, v187\n"
      "v_mfma_f32_16x16x32_bf16 v[156:159], a[92:95], a[60:63], v[156:159]\n"
      "s_add_u32 s65, s82, s76\n"
      "ds_read_b64_tr_b16 a[114:115], v244 offset:5120\n"
      "v_cvt_pk_bf16_f32 v221, v114, v115\n"
      "v_mfma_f32_16x16x32_bf16 v[152:155], a[88:91], a[60:63], v[236:239]\n"
      "s_sub_u32 s93, s93, 1\n"
      "v_exp_f32 v112, v188\n"
      "v_mfma_f32_16x16x32_bf16 v[152:155], a[80:83], a[44:47], v[152:155]\n"
      "s_cmp_le_i32 s93, 14\n"
      "ds_read_b64_tr_b16 a[116:117], v245 offset:1024\n"
      "v_or3_b32 v116, v116, v220, v221\n"
      "v_mfma_f32_16x16x32_bf16 v[152:155], a[72:75], a[28:31], v[152:155]\n"
      "s_cselect_b32 s86, 107, s86\n"
      "v_exp_f32 v113, v189\n"
      "v_mfma_f32_16x16x32_bf16 v[152:155], a[64:67], a[12:15], v[152:155]\n"
      "ds_read_b64_tr_b16 a[118:119], v245 offset:5120\n"
      "v_cvt_pk_bf16_f32 v222, v112, v113\n"
      "s_waitcnt lgkmcnt(6)\n"
      "v_mfma_f32_16x16x32_bf16 v[32:35], a[96:99], v[192:195], v[32:35]\n"
      "s_sub_u32 s67, s65, s66\n"
      "v_exp_f32 v114, v190\n"
      "v_mfma_f32_16x16x32_bf16 v[48:51], a[100:103], v[192:195], v[48:51]\n"
      "s_cmp_lt_i32 s67, 0\n"
      "s_cselect_b32 s88, s66, s65\n"
      "ds_read_b64_tr_b16 a[120:121], v246 offset:1024\n"
      "ds_read_b64_tr_b16 a[122:123], v246 offset:5120\n"
      "v_mfma_f32_16x16x32_bf16 a[128:131], a[104:107], v[192:195], a[128:131]\n"
      "s_waitcnt lgkmcnt(4)\n"
      "v_exp_f32 v115, v191\n"
      "v_mfma_f32_16x16x32_bf16 a[144:147], a[108:111], v[192:195], a[144:147]\n"
      "s_add_u32 s90, s88, s91\n"
      "ds_read_b64_tr_b16 a[124:125], v247 offset:1024\n"
      "v_cvt_pk_bf16_f32 v223, v114, v115\n"
      "v_mfma_f32_16x16x32_bf16 v[64:67], a[112:115], v[192:195], v[64:67]\n"
      "ds_read_b64_tr_b16 a[126:127], v247 offset:5120\n"
      "s_waitcnt lgkmcnt(2)\n"
      "v_mfma_f32_16x16x32_bf16 v[80:83], a[116:119], v[192:195], v[80:83]\n"
      "v_or3_b32 v116, v116, v222, v223\n"
      "v_mfma_f32_16x16x32_bf16 a[160:163], a[120:123], v[192:195], a[160:163]\n"
      "s_mov_b32 s64, 4\n"
      "s_waitcnt lgkmcnt(0)\n"
      "v_and_b32 v116, 0x40004000, v116\n"
      "v_mfma_f32_16x16x32_bf16 a[176:179], a[124:127], v[192:195], a[176:179]\n"
      "v_cmp_ne_u32 vcc, 0, v116\n"
      "s_cbranch_vccnz FB1_%=\n"
      "RET1_4_%=:\n"
      "v_mfma_f32_16x16x32_bf16 a[180:183], a[124:127], v[196:199], a[180:183]\n"
      "v_exp_f32 v112, v128\n"
      "v_mfma_f32_16x16x32_bf16 a[164:167], a[120:123], v[196:199], a[164:167]\n"
      "v_xor_b32 v240, 0x10000, v244\n"
      "v_mfma_f32_16x16x32_bf16 a[192:195], a[196:199], v[208:211], a[192:195]\n"
      "v_exp_f32 v113, v129\n"
      "v_mfma_f32_16x16x32_bf16 v[84:87], a[116:119], v[196:199], v[84:87]\n"
      "v_cvt_pk_bf16_f32 v192, v112, v113\n"
      "v_xor_b32 v241, 0x10000, v245\n"
      "v_mfma_f32_16x16x32_bf16 v[68:71], a[112:115], v[196:199], v[68:71]\n"
      "v_exp_f32 v114, v130\n"
      "v_mfma_f32_16x16x32_bf16 a[192:195], a[200:203], v[212:215], a[192:195]\n"
      "v_xor_b32 v250, 0x10000, v246\n"
      "v_mfma_f32_16x16x32_bf16 a[148:151], a[108:111], v[196:199], a[148:151]\n"
      "v_exp_f32 v115, v131\n"
      "v_mfma_f32_16x16x32_bf16 a[132:135], a[104:107], v[196:199], a[132:135]\n"
      "s_memrealtime s[68:69]\n"
      "v_cvt_pk_bf16_f32 v193, v114, v115\n"
      "v_xor_b32 v251, 0x10000, v247\n"
      "v_mfma_f32_16x16x32_bf16 a[192:195], a[204:207], v[216:219], a[192:195]\n"
      "v_exp_f32 v112, v132\n"
      "v_mfma_f32_16x16x32_bf16 v[52:55], a[100:103], v[196:199], v[52:55]\n"
      "v_or_b32 v116, v192, v193\n"
      "v_mfma_f32_16x16x32_bf16 v[36:39], a[96:99], v[196:199], v[36:39]\n"
      "v_exp_f32 v113, v133\n"
      "v_mfma_f32_16x16x32_bf16 a[192:195], a[208:211], v[220:223], a[192:195]\n"
      "v_cvt_pk_bf16_f32 v194, v112, v113\n"
      "s_waitcnt vmcnt(6) lgkmcnt(0)\n"
      "s_sub_u32 s67, s68, s90\n"
      "s_cmp_lt_i32 s67, 0\n"
      "s_cbranch_scc1 GW7_%=\n"
      "GX7_%=:\n"
      "s_barrier\n"
      "v_mfma_f32_16x16x32_bf16 v[40:43], a[96:99], v[200:203], v[40:43]\n"
      "s_memrealtime s[68:69]\n"
      "ds_read_b128 a[64:67], v242 offset:32768\n"
      "v_exp_f32 v114, v134\n"
      "v_mfma_f32_16x16x32_bf16 v[44:47], a[96:99], v[204:207], v[44:47]\n"
      "s_add_u32 m0, %[wb], 82944\n"
      "ds_read_b128 a[68:71], v242 offset:36864\n"
      "v_mfma_f32_16x16x32_bf16 v[60:63], a[100:103], v[204:207], v[60:63]\n"
      "ds_read_b128 a[72:75], v243 offset:32768\n"
      "buffer_load_dwordx4 v121, s[24:27], s94 offen nt lds\n"
      "v_mfma_f32_16x16x32_bf16 v[56:59], a[100:103], v[200:203], v[56:59]\n"
      "s_add_u32 m0, %[wb], 91136\n"
      "ds_read_b128 a[76:79], v243 offset:36864\n"
      "v_mfma_f32_16x16x32_bf16 a[136:139], a[104:107], v[200:203], a[136:139]\n"
      "ds_read_b128 a[80:83], v242 offset:33792\n"
      "buffer_load_dwordx4 v123, s[24:27], s94 offen nt lds\n"
      "v_mfma_f32_16x16x32_bf16 a[140:143], a[104:107], v[204:207], a[140:143]\n"
      "ds_read_b128 a[84:87], v242 offset:37888\n"
      "v_mfma_f32_16x16x32_bf16 a[156:159], a[108:111], v[204:207], a[156:159]\n"
      "ds_read_b128 a[88:91], v243 offset:33792\n"
      "v_mfma_f32_16x16x32_bf16 a[152:155], a[108:111], v[200:203], a[152:155]\n"
      "ds_read_b128 a[92:95], v243 offset:37888\n"
      "v_mfma_f32_16x16x32_bf16 v[72:75], a[112:115], v[200:203], v[72:75]\n"
      "v_exp_f32 v115, v135\n"
      "v_mfma_f32_16x16x32_bf16 v[76:79], a[112:115], v[204:207], v[76:79]\n"
      "v_exp_f32 v112, v136\n"
      "v_mfma_f32_16x16x32_bf16 v[92:95], a[116:119], v[204:207], v[92:95]\n"
      "v_cvt_pk_bf16_f32 v195, v114, v115\n"
      "v_exp_f32 v113, v137\n"
      "v_mfma_f32_16x16x32_bf16 v[88:91], a[116:119], v[200:203], v[88:91]\n"
      "v_or3_b32 v116, v116, v194, v195\n"
      "v_mfma_f32_16x16x32_bf16 a[168:171], a[120:123], v[200:203], a[168:171]\n"
      "v_exp_f32 v114, v138\n"
      "v_mfma_f32_16x16x32_bf16 a[172:175], a[120:123], v[204:207], a[172:175]\n"
      "v_cvt_pk_bf16_f32 v196, v112, v113\n"
      "v_exp_f32 v115, v139\n"
      "v_mfma_f32_16x16x32_bf16 a[188:191], a[124:127], v[204:207], a[188:191]\n"
      "v_exp_f32 v112, v140\n"
      "v_mfma_f32_16x16x32_bf16 a[184:187], a[124:127], v[200:203], a[184:187]\n"
      "s_waitcnt lgkmcnt(0)\n"
      "v_cvt_pk_bf16_f32 v197, v114, v115\n"
      "v_mfma_f32_16x16x32_bf16 v[160:163], a[64:67], a[0:3], v[224:227]\n"
      "s_sub_u32 s67, s68, s66\n"
      "v_exp_f32 v113, v141\n"
      "v_mfma_f32_16x16x32_bf16 v[168:171], a[64:67], a[4:7], v[228:231]\n"
      "s_cmp_gt_i32 s67, s70\n"
      "v_or3_b32 v116, v116, v196, v197\n"
      "v_exp_f32 v114, v142\n"
      "v_mfma_f32_16x16x32_bf16 v[176:179], a[64:67], a[8:11], v[232:235]\n"
      "s_cselect_b32 s69, s86, 0\n"
      "v_cvt_pk_bf16_f32 v198, v112, v113\n"
      "v_mfma_f32_16x16x32_bf16 v[184:187], a[64:67], a[12:15], v[236:239]\n"
      "s_add_u32 s66, s66, s69\n"
      "v_exp_f32 v115, v143\n"
      "v_mfma_f32_16x16x32_bf16 v[164:167], a[68:71], a[0:3], v[224:227]\n"
      "s_add_u32 s67, s67, s101\n"
      "v_exp_f32 v112, v144\n"
      "v_mfma_f32_16x16x32_bf16 v[172:175], a[68:71], a[4:7], v[228:231]\n"
      "s_min_i32 s70, s70, s67\n"
      "v_cvt_pk_bf16_f32 v199, v114, v115\n"
      "v_exp_f32 v113, v145\n"
      "v_mfma_f32_16x16x32_bf16 v[180:183], a[68:71], a[8:11], v[232:235]\n"
      "s_max_i32 s70, s70, 85\n"
      "v_or3_b32 v116, v116, v198, v199\n"
      "v_mfma_f32_16x16x32_bf16 v[188:191], a[68:71], a[12:15], v[236:239]\n"
      "s_waitcnt lgkmcnt(4)\n"
      "v_exp_f32 v114, v146\n"
      "v_mfma_f32_16x16x32_bf16 v[160:163], a[72:75], a[16:19], v[160:163]\n"
      "s_add_u32 s66, s66, s86\n"
      "v_cvt_pk_bf16_f32 v200, v112, v113\n"
      "v_exp_f32 v115, v147\n"
      "v_mfma_f32_16x16x32_bf16 v[168:171], a[72:75], a[20:23], v[168:171]\n"
      "s_and_b32 s76, s80, 31\n"
      "v_exp_f32 v112, v148\n"
      "v_mfma_f32_16x16x32_bf16 v[176:179], a[72:75], a[24:27], v[176:179]\n"
      "s_lshr_b64 s[80:81], s[80:81], 5\n"
      "v_cvt_pk_bf16_f32 v201, v114, v115\n"
      "v_mfma_f32_16x16x32_bf16 v[184:187], a[72:75], a[28:31], v[184:187]\n"
      "s_add_u32 s66, s66, s76\n"
      "v_exp_f32 v113, v149\n"
      "v_mfma_f32_16x16x32_bf16 v[164:167], a[76:79], a[16:19], v[164:167]\n"
      "s_sub_u32 s67, s68, s82\n"
      "v_or3_b32 v116, v116, v200, v201\n"
      "v_exp_f32 v114, v150\n"
      "v_mfma_f32_16x16x32_bf16 v[172:175], a[76:79], a[20:23], v[172:175]\n"
      "s_min_i32 s76, s67, s83\n"
      "v_cvt_pk_bf16_f32 v202, v112, v113\n"
      "v_mfma_f32_16x16x32_bf16 v[180:183], a[76:79], a[24:27], v[180:183]\n"
      "s_add_u32 m0, s62, 0\n"
      "v_exp_f32 v115, v151\n"
      "v_mfma_f32_16x16x32_bf16 v[188:191], a[76:79], a[28:31], v[188:191]\n"
      "buffer_load_dwordx4 v248, s[20:23], s60 offen nt lds\n"
      "s_waitcnt lgkmcnt(2)\n"
      "v_mfma_f32_16x16x32_bf16 v[160:163], a[80:83], a[32:35], v[160:163]\n"
      "s_add_u32 m0, s62, 896\n"
      "v_exp_f32 v112, v152\n"
      "v_mfma_f32_16x16x32_bf16 v[168:171], a[80:83], a[36:39], v[168:171]\n"
      "s_mov_b32 s83, s67\n"
      "buffer_load_dwordx4 v248, s[20:23], s60 offen offset:128 nt lds\n"
      "v_mfma_f32_16x16x32_bf16 v[176:179], a[80:83], a[40:43], v[176:179]\n"
      "s_add_u32 m0, s62, 8192\n"
      "v_mfma_f32_16x16x32_bf16 v[184:187], a[80:83], a[44:47], v[184:187]\n"
      "s_sub_u32 s76, s76, 36\n"
      "buffer_load_dwordx4 v120, s[20:23], s60 offen nt lds\n"
      "s_waitcnt vmcnt(9)\n"
      "s_barrier\n"
      "v_mfma_f32_16x16x32_bf16 v[164:167], a[84:87], a[32:35], v[164:167]\n"
      "s_add_u32 m0, s62, 9088\n"
      "ds_read_b64_tr_b16 a[96:97], v246 offset:33792\n"
      "v_mfma_f32_16x16x32_bf16 v[172:175], a[84:87], a[36:39], v[172:175]\n"
      "s_min_i32 s76, s76, 148\n"
      "ds_read_b64_tr_b16 a[98:99], v246 offset:37888\n"
      "buffer_load_dwordx4 v120, s[20:23], s60 offen offset:128 nt lds\n"
      "v_mfma_f32_16x16x32_bf16 v[180:183], a[84:87], a[40:43], v[180:183]\n"
      "s_max_i32 s76, s76, 70\n"
      "ds_read_b64_tr_b16 a[100:101], v247 offset:33792\n"
      "v_mfma_f32_16x16x32_bf16 v[188:191], a[84:87], a[44:47], v[188:191]\n"
      "ds_read_b64_tr_b16 a[102:103], v247 offset:37888\n"
      "s_waitcnt lgkmcnt(4)\n"
      "v_mfma_f32_16x16x32_bf16 v[160:163], a[88:91], a[48:51], v[160:163]\n"
      "s_mov_b32 s82, s68\n"
      "ds_read_b64_tr_b16 a[104:105], v244 offset:33792\n"
      "v_cvt_pk_bf16_f32 v203, v114, v115\n"
      "v_mfma_f32_16x16x32_bf16 v[168:171], a[88:91], a[52:55], v[168:171]\n"
      "s_sub_u32 s66, s66, s99\n"
      "ds_read_b64_tr_b16 a[106:107], v244 offset:37888\n"
      "v_exp_f32 v113, v153\n"
      "v_mfma_f32_16x16x32_bf16 v[176:179], a[88:91], a[56:59], v[176:179]\n"
      "s_sub_u32 s82, s82, s99\n"
      "ds_read_b64_tr_b16 a[108:109], v245 offset:33792\n"
      "v_mfma_f32_16x16x32_bf16 v[184:187], a[88:91], a[60:63], v[184:187]\n"
      "s_add_u32 s65, s82, s76\n"
      "ds_read_b64_tr_b16 a[110:111], v245 offset:37888\n"
      "v_or3_b32 v116, v116, v202, v203\n"
      "v_mfma_f32_16x16x32_bf16 v[164:167], a[92:95], a[48:51], v[164:167]\n"
      "s_sub_u32 s93, s93, 1\n"
      "v_exp_f32 v114, v154\n"
      "v_mfma_f32_16x16x32_bf16 v[172:175], a[92:95], a[52:55], v[172:175]\n"
      "s_cmp_le_i32 s93, 14\n"
      "v_cvt_pk_bf16_f32 v204, v112, v113\n"
      "v_exp_f32 v115, v155\n"
      "v_mfma_f32_16x16x32_bf16 v[180:183], a[92:95], a[56:59], v[180:183]\n"
      "s_cselect_b32 s86, 107, s86\n"
      "v_exp_f32 v112, v156\n"
      "v_mfma_f32_16x16x32_bf16 v[188:191], a[92:95], a[60:63], v[188:191]\n"
      "s_waitcnt lgkmcnt(4)\n"
      "v_cvt_pk_bf16_f32 v205, v114, v115\n"
      "v_mfma_f32_16x16x32_bf16 a[160:163], a[96:99], v[208:211], a[160:163]\n"
      "s_sub_u32 s67, s65, s66\n"
      "v_exp_f32 v113, v157\n"
      "v_mfma_f32_16x16x32_bf16 a[164:167], a[96:99], v[212:215], a[164:167]\n"
      "s_cmp_lt_i32 s67, 0\n"
      "v_or3_b32 v116, v116, v204, v205\n"
      "v_exp_f32 v114, v158\n"
      "v_mfma_f32_16x16x32_bf16 a[168:171], a[96:99], v[216:219], a[168:171]\n"
      "s_cselect_b32 s88, s66, s65\n"
      "v_cvt_pk_bf16_f32 v206, v112, v113\n"
      "v_mfma_f32_16x16x32_bf16 a[172:175], a[96:99], v[220:223], a[172:175]\n"
      "s_add_u32 s90, s88, s91\n"
      "v_exp_f32 v115, v159\n"
      "s_nop 0\n"
      "v_mfma_f32_16x16x32_bf16 a[176:179], a[100:103], v[208:211], a[176:179]\n"
      "s_mov_b32 s64, 4\n"
      "v_cvt_pk_bf16_f32 v207, v114, v115\n"
      "v_or3_b32 v116, v116, v206, v207\n"
      "v_mfma_f32_16x16x32_bf16 a[180:183], a[100:103], v[212:215], a[180:183]\n"
      "v_and_b32 v116, 0x40004000, v116\n"
      "v_cmp_ne_u32 vcc, 0, v116\n"
      "s_cbranch_vccnz FB0_%=\n"
      "RET0_4_%=:\n"
      "v_mfma_f32_16x16x32_bf16 a[184:187], a[100:103], v[216:219], a[184:187]\n"
      "v_exp_f32 v112, v160\n"
      "v_mfma_f32_16x16x32_bf16 a[188:191], a[100:103], v[220:223], a[188:191]\n"
      "s_memrealtime s[68:69]\n"
      "v_xor_b32 v242, 0x10000, v242\n"
      "v_mfma_f32_16x16x32_bf16 a[192:195], a[196:199], v[192:195], a[192:195]\n"
      "v_exp_f32 v113, v161\n"
      "v_mfma_f32_16x16x32_bf16 a[192:195], a[200:203], v[196:199], a[192:195]\n"
      "v_xor_b32 v243, 0x10000, v243\n"
      "v_exp_f32 v114, v162\n"
      "v_mfma_f32_16x16x32_bf16 a[192:195], a[204:207], v[200:203], a[192:195]\n"
      "v_cvt_pk_bf16_f32 v0, v112, v113\n"
      "v_mfma_f32_16x16x32_bf16 a[192:195], a[208:211], v[204:207], a[192:195]\n"
      "v_exp_f32 v115, v163\n"
      "s_waitcnt vmcnt(6) lgkmcnt(0)\n"
      "s_sub_u32 s67, s68, s90\n"
      "s_cmp_lt_i32 s67, 0\n"
      "s_cbranch_scc1 GW8_%=\n"
      "GX8_%=:\n"
      "s_barrier\n"
      "v_mfma_f32_16x16x32_bf16 v[64:67], a[104:107], v[208:211], v[64:67]\n"
      "ds_read_b128 a[64:67], v242 offset:0\n"
      "ds_read_b128 a[68:71], v242 offset:4096\n"
      "v_mfma_f32_16x16x32_bf16 v[68:71], a[104:107], v[212:215], v[68:71]\n"
      "s_add_u32 m0, %[wb], 115712\n"
      "ds_read_b128 a[72:75], v243 offset:0\n"
      "ds_read_b128 a[76:79], v243 offset:4096\n"
      "v_mfma_f32_16x16x32_bf16 v[72:75], a[104:107], v[216:219], v[72:75]\n"
      "ds_read_b128 a[80:83], v242 offset:1024\n"
      "ds_read_b128 a[84:87], v242 offset:5120\n"
      "buffer_load_dwordx4 v121, s[24:27], s95 offen nt lds\n"
      "v_mfma_f32_16x16x32_bf16 v[76:79], a[104:107], v[220:223], v[76:79]\n"
      "s_add_u32 m0, %[wb], 123904\n"
      "ds_read_b128 a[88:91], v243 offset:1024\n"
      "ds_read_b128 a[92:95], v243 offset:5120\n"
      "v_mfma_f32_16x16x32_bf16 v[80:83], a[108:111], v[208:211], v[80:83]\n"
      "buffer_load_dwordx4 v123, s[24:27], s95 offen nt lds\n"
      "v_mfma_f32_16x16x32_bf16 v[84:87], a[108:111], v[212:215], v[84:87]\n"
      "s_add_u32 m0, %[wb], 17408\n"
      "v_mfma_f32_16x16x32_bf16 v[88:91], a[108:111], v[216:219], v[88:91]\n"
      "buffer_load_dwordx4 v121, s[24:27], s77 offen nt lds\n"
      "v_mfma_f32_16x16x32_bf16 v[92:95], a[108:111], v[220:223], v[92:95]\n"
      "s_waitcnt lgkmcnt(6)\n"
      "s_add_u32 m0, %[wb], 25600\n"
      "v_exp_f32 v112, v164\n"
      "v_mfma_f32_16x16x32_bf16 v[128:131], a[64:67], a[0:3], v[224:227]\n"
      "buffer_load_dwordx4 v123, s[24:27], s77 offen nt lds\n"
      "v_mfma_f32_16x16x32_bf16 v[136:139], a[64:67], a[4:7], v[228:231]\n"
      "v_cvt_pk_bf16_f32 v1, v114, v115\n"
      "v_exp_f32 v113, v165\n"
      "v_mfma_f32_16x16x32_bf16 v[144:147], a[64:67], a[8:11], v[232:235]\n"
      "v_or_b32 v116, v0, v1\n"
      "v_mfma_f32_16x16x32_bf16 v[152:155], a[64:67], a[12:15], v[236:239]\n"
      "v_exp_f32 v114, v166\n"
      "v_cvt_pk_bf16_f32 v2, v112, v113\n"
      "v_mfma_f32_16x16x32_bf16 v[132:135], a[68:71], a[0:3], v[224:227]\n"
      "v_exp_f32 v115, v167\n"
      "v_mfma_f32_16x16x32_bf16 v[140:143], a[68:71], a[4:7], v[228:231]\n"
      "v_exp_f32 v112, v168\n"
      "v_cvt_pk_bf16_f32 v3, v114, v115\n"
      "v_mfma_f32_16x16x32_bf16 v[148:151], a[68:71], a[8:11], v[232:235]\n"
      "v_exp_f32 v113, v169\n"
      "v_or3_b32 v116, v116, v2, v3\n"
      "v_mfma_f32_16x16x32_bf16 v[156:159], a[68:71], a[12:15], v[236:239]\n"
      "s_waitcnt lgkmcnt(4)\n"
      "v_exp_f32 v114, v170\n"
      "v_mfma_f32_16x16x32_bf16 v[128:131], a[72:75], a[16:19], v[128:131]\n"
      "v_cvt_pk_bf16_f32 v4, v112, v113\n"
      "v_exp_f32 v115, v171\n"
      "v_mfma_f32_16x16x32_bf16 v[136:139], a[72:75], a[20:23], v[136:139]\n"
      "v_exp_f32 v112, v172\n"
      "v_mfma_f32_16x16x32_bf16 v[144:147], a[72:75], a[24:27], v[144:147]\n"
      "v_cvt_pk_bf16_f32 v5, v114, v115\n"
      "v_exp_f32 v113, v173\n"
      "v_mfma_f32_16x16x32_bf16 v[152:155], a[72:75], a[28:31], v[152:155]\n"
      "v_or3_b32 v116, v116, v4, v5\n"
      "v_mfma_f32_16x16x32_bf16 v[132:135], a[76:79], a[16:19], v[132:135]\n"
      "v_exp_f32 v114, v174\n"
      "v_cvt_pk_bf16_f32 v6, v112, v113\n"
      "v_mfma_f32_16x16x32_bf16 v[140:143], a[76:79], a[20:23], v[140:143]\n"
      "v_exp_f32 v115, v175\n"
      "v_mfma_f32_16x16x32_bf16 v[148:151], a[76:79], a[24:27], v[148:151]\n"
      "v_exp_f32 v112, v176\n"
      "v_cvt_pk_bf16_f32 v7, v114, v115\n"
      "v_mfma_f32_16x16x32_bf16 v[156:159], a[76:79], a[28:31], v[156:159]\n"
      "s_waitcnt lgkmcnt(2)\n"
      "v_exp_f32 v113, v177\n"
      "v_mfma_f32_16x16x32_bf16 v[128:131], a[80:83], a[32:35], v[128:131]\n"
      "v_or3_b32 v116, v116, v6, v7\n"
      "v_exp_f32 v114, v178\n"
      "v_mfma_f32_16x16x32_bf16 v[136:139], a[80:83], a[36:39], v[136:139]\n"
      "v_cvt_pk_bf16_f32 v8, v112, v113\n"
      "v_exp_f32 v115, v179\n"
      "v_mfma_f32_16x16x32_bf16 v[144:147], a[80:83], a[40:43], v[144:147]\n"
      "v_exp_f32 v112, v180\n"
      "v_mfma_f32_16x16x32_bf16 v[152:155], a[80:83], a[44:47], v[152:155]\n"
      "v_cvt_pk_bf16_f32 v9, v114, v115\n"
      "v_exp_f32 v113, v181\n"
      "s_waitcnt vmcnt(8)\n"
      "s_barrier\n"
      "v_mfma_f32_16x16x32_bf16 v[132:135], a[84:87], a[32:35], v[132:135]\n"
      "ds_read_b64_tr_b16 a[112:113], v250 offset:1024\n"
      "v_or3_b32 v116, v116, v8, v9\n"
      "v_mfma_f32_16x16x32_bf16 v[140:143], a[84:87], a[36:39], v[140:143]\n"
      "ds_read_b64_tr_b16 a[114:115], v250 offset:5120\n"
      "v_exp_f32 v114, v182\n"
      "v_mfma_f32_16x16x32_bf16 v[148:151], a[84:87], a[40:43], v[148:151]\n"
      "ds_read_b64_tr_b16 a[116:117], v251 offset:1024\n"
      "v_mfma_f32_16x16x32_bf16 v[156:159], a[84:87], a[44:47], v[156:159]\n"
      "ds_read_b64_tr_b16 a[118:119], v251 offset:5120\n"
      "v_cvt_pk_bf16_f32 v10, v112, v113\n"
      "s_waitcnt lgkmcnt(4)\n"
      "v_mfma_f32_16x16x32_bf16 v[128:131], a[88:91], a[48:51], v[128:131]\n"
      "ds_read_b64_tr_b16 a[120:121], v240 offset:1024\n"
      "v_exp_f32 v115, v183\n"
      "v_mfma_f32_16x16x32_bf16 v[136:139], a[88:91], a[52:55], v[136:139]\n"
      "ds_read_b64_tr_b16 a[122:123], v240 offset:5120\n"
      "v_exp_f32 v112, v184\n"
      "v_mfma_f32_16x16x32_bf16 v[144:147], a[88:91], a[56:59], v[144:147]\n"
      "ds_read_b64_tr_b16 a[124:125], v241 offset:1024\n"
      "v_cvt_pk_bf16_f32 v11, v114, v115\n"
      "v_mfma_f32_16x16x32_bf16 v[152:155], a[88:91], a[60:63], v[152:155]\n"
      "ds_read_b64_tr_b16 a[126:127], v241 offset:5120\n"
      "v_exp_f32 v113, v185\n"
      "v_mfma_f32_16x16x32_bf16 v[132:135], a[92:95], a[48:51], v[132:135]\n"
      "v_or3_b32 v116, v116, v10, v11\n"
      "v_exp_f32 v114, v186\n"
      "v_mfma_f32_16x16x32_bf16 v[140:143], a[92:95], a[52:55], v[140:143]\n"
      "v_cvt_pk_bf16_f32 v12, v112, v113\n"
      "v_mfma_f32_16x16x32_bf16 v[148:151], a[92:95], a[56:59], v[148:151]\n"
      "v_exp_f32 v115, v187\n"
      "v_mfma_f32_16x16x32_bf16 v[156:159], a[92:95], a[60:63], v[156:159]\n"
      "s_waitcnt lgkmcnt(4)\n"
      "v_exp_f32 v112, v188\n"
      "v_cvt_pk_bf16_f32 v13, v114, v115\n"
      "v_mfma_f32_16x16x32_bf16 a[160:163], a[112:115], v[192:195], a[160:163]\n"
      "v_exp_f32 v113, v189\n"
      "v_or3_b32 v116, v116, v12, v13\n"
      "v_mfma_f32_16x16x32_bf16 a[164:167], a[112:115], v[196:199], a[164:167]\n"
      "v_exp_f32 v114, v190\n"
      "v_mfma_f32_16x16x32_bf16 a[168:171], a[112:115], v[200:203], a[168:171]\n"
      "v_cvt_pk_bf16_f32 v14, v112, v113\n"
      "v_exp_f32 v115, v191\n"
      "v_mfma_f32_16x16x32_bf16 a[172:175], a[112:115], v[204:207], a[172:175]\n"
      "s_add_u32 m0, %[wb], 50176\n"
      "s_nop 0\n"
      "v_cvt_pk_bf16_f32 v15, v114, v115\n"
      "v_or3_b32 v116, v116, v14, v15\n"
      "v_mfma_f32_16x16x32_bf16 a[176:179], a[116:119], v[192:195], a[176:179]\n"
      "s_mov_b32 s64, 5\n"
      "buffer_load_dwordx4 v121, s[24:27], s78 offen nt lds\n"
      "v_mfma_f32_16x16x32_bf16 a[180:183], a[116:119], v[196:199], a[180:183]\n"
      "s_add_u32 m0, %[wb], 58368\n"
      "v_and_b32 v116, 0x40004000, v116\n"
      "v_cmp_ne_u32 vcc, 0, v116\n"
      "s_cbranch_vccnz FB1_%=\n"
      "RET1_5_%=:\n"
      "v_mfma_f32_16x16x32_bf16 a[184:187], a[116:119], v[200:203], a[184:187]\n"
      "buffer_load_dwordx4 v123, s[24:27], s78 offen nt lds\n"
      "v_mfma_f32_16x16x32_bf16 a[188:191], a[116:119], v[204:207], a[188:191]\n"
      "s_memrealtime s[68:69]\n"
      "v_exp_f32 v112, v128\n"
      "v_exp_f32 v113, v129\n"
      "v_mfma_f32_16x16x32_bf16 a[192:195], a[196:199], v[0:3], a[192:195]\n"
      "v_exp_f32 v114, v130\n"
      "v_mfma_f32_16x16x32_bf16 a[192:195], a[200:203], v[4:7], a[192:195]\n"
      "v_cvt_pk_bf16_f32 v16, v112, v113\n"
      "v_exp_f32 v115, v131\n"
      "v_mfma_f32_16x16x32_bf16 a[192:195], a[204:207], v[8:11], a[192:195]\n"
      "v_exp_f32 v112, v132\n"
      "v_cvt_pk_bf16_f32 v17, v114, v115\n"
      "v_mfma_f32_16x16x32_bf16 a[192:195], a[208:211], v[12:15], a[192:195]\n"
      "v_exp_f32 v113, v133\n"
      "v_or_b32 v116, v16, v17\n"
      "s_add_u32 s72, s72, 0x7f4a7c15\n"
      "s_addc_u32 s76, s79, 0x9e3779b9\n"
      "s_waitcnt vmcnt(6) lgkmcnt(0)\n"
      "s_barrier\n"
      "v_mfma_f32_16x16x32_bf16 v[64:67], a[120:123], v[192:195], v[64:67]\n"
      "s_xor_b32 s80, %[xo], 0x400000\n"
      "ds_read_b128 a[64:67], v242 offset:32768\n"
      "ds_read_b128 a[68:71], v242 offset:36864\n"
      "v_mfma_f32_16x16x32_bf16 v[68:71], a[120:123], v[196:199], v[68:71]\n"
      "s_add_u32 s79, s80, 0x1000\n"
      "ds_read_b128 a[72:75], v243 offset:32768\n"
      "ds_read_b128 a[76:79], v243 offset:36864\n"
      "v_exp_f32 v114, v134\n"
      "v_mfma_f32_16x16x32_bf16 v[72:75], a[120:123], v[200:203], v[72:75]\n"
      "s_add_u32 s81, %[xo], 0x1000\n"
      "ds_read_b128 a[80:83], v242 offset:33792\n"
      "ds_read_b128 a[84:87], v242 offset:37888\n"
      "v_cvt_pk_bf16_f32 v18, v112, v113\n"
      "v_mfma_f32_16x16x32_bf16 v[76:79], a[120:123], v[204:207], v[76:79]\n"
      "s_mov_b32 s63, 0x100000\n"
      "ds_read_b128 a[88:91], v243 offset:33792\n"
      "ds_read_b128 a[92:95], v243 offset:37888\n"
      "v_mfma_f32_16x16x32_bf16 v[80:83], a[124:127], v[192:195], v[80:83]\n"
      "s_mov_b32 s60, 0x928b2f\n"
      "v_exp_f32 v115, v135\n"
      "v_exp_f32 v112, v136\n"
      "v_mfma_f32_16x16x32_bf16 v[84:87], a[124:127], v[196:199], v[84:87]\n"
      "s_mov_b32 s62, 0x9818e9\n"
      "v_cvt_pk_bf16_f32 v19, v114, v115\n"
      "v_exp_f32 v113, v137\n"
      "v_mfma_f32_16x16x32_bf16 v[88:91], a[124:127], v[200:203], v[88:91]\n"
      "s_mov_b32 s70, 0xdd9dc9\n"
      "v_xor_b32 v248, 0x2000, v244\n"
      "v_or3_b32 v116, v116, v18, v19\n"
      "v_mfma_f32_16x16x32_bf16 v[92:95], a[124:127], v[204:207], v[92:95]\n"
      "s_waitcnt lgkmcnt(6)\n"
      "v_exp_f32 v114, v138\n"
      "v_cvt_pk_bf16_f32 v20, v112, v113\n"
      "v_mfma_f32_16x16x32_bf16 v[160:163], a[64:67], a[0:3], v[224:227]\n"
      "s_mov_b32 s71, 0x8ed905\n"
      "v_exp_f32 v115, v139\n"
      "v_mfma_f32_16x16x32_bf16 v[168:171], a[64:67], a[4:7], v[228:231]\n"
      "s_sub_u32 s88, 324, %[cph]\n"
      "v_xor_b32 v120, 0x2000, v245\n"
      "v_exp_f32 v112, v140\n"
      "v_cvt_pk_bf16_f32 v21, v114, v115\n"
      "v_mfma_f32_16x16x32_bf16 v[176:179], a[64:67], a[8:11], v[232:235]\n"
      "s_add_u32 s88, s88, s66\n"
      "v_exp_f32 v113, v141\n"
      "v_mfma_f32_16x16x32_bf16 v[184:187], a[64:67], a[12:15], v[236:239]\n"
      "s_add_u32 m0, %[wb], 49152\n"
      "v_or3_b32 v116, v116, v20, v21\n"
      "v_exp_f32 v114, v142\n"
      "v_mfma_f32_16x16x32_bf16 v[164:167], a[68:71], a[0:3], v[224:227]\n"
      "s_add_u32 s88, s88, s100\n"
      "buffer_load_dwordx4 v249, s[24:27], s61 offen nt lds\n"
      "v_mbcnt_lo_u32_b32 v127, -1, 0\n"
      "v_mfma_f32_16x16x32_bf16 v[172:175], a[68:71], a[4:7], v[228:231]\n"
      "s_add_u32 m0, %[wb], 57344\n"
      "v_cvt_pk_bf16_f32 v22, v112, v113\n"
      "v_mfma_f32_16x16x32_bf16 v[180:183], a[68:71], a[8:11], v[232:235]\n"
      "buffer_load_dwordx4 v122, s[24:27], s61 offen nt lds\n"
      "v_mfma_f32_16x16x32_bf16 v[188:191], a[68:71], a[12:15], v[236:239]\n"
      "s_waitcnt lgkmcnt(4)\n"
      "v_mbcnt_hi_u32_b32 v127, -1, v127\n"
      "v_exp_f32 v115, v143\n"
      "v_mfma_f32_16x16x32_bf16 v[160:163], a[72:75], a[16:19], v[160:163]\n"
      "v_exp_f32 v112, v144\n"
      "v_cvt_pk_bf16_f32 v23, v114, v115\n"
      "v_mfma_f32_16x16x32_bf16 v[168:171], a[72:75], a[20:23], v[168:171]\n"
      "v_exp_f32 v113, v145\n"
      "v_or3_b32 v116, v116, v22, v23\n"
      "v_mfma_f32_16x16x32_bf16 v[176:179], a[72:75], a[24:27], v[176:179]\n"
      "v_lshlrev_b32 v127, 4, v127\n"
      "v_exp_f32 v114, v146\n"
      "v_mfma_f32_16x16x32_bf16 v[184:187], a[72:75], a[28:31], v[184:187]\n"
      "v_cvt_pk_bf16_f32 v24, v112, v113\n"
      "v_exp_f32 v115, v147\n"
      "v_mfma_f32_16x16x32_bf16 v[164:167], a[76:79], a[16:19], v[164:167]\n"
      "v_exp_f32 v112, v148\n"
      "v_cvt_pk_bf16_f32 v25, v114, v115\n"
      "v_mfma_f32_16x16x32_bf16 v[172:175], a[76:79], a[20:23], v[172:175]\n"
      "v_add_u32 v125, %[xw], v127\n"
      "v_exp_f32 v113, v149\n"
      "v_mfma_f32_16x16x32_bf16 v[180:183], a[76:79], a[24:27], v[180:183]\n"
      "v_or3_b32 v116, v116, v24, v25\n"
      "v_exp_f32 v114, v150\n"
      "v_mfma_f32_16x16x32_bf16 v[188:191], a[76:79], a[28:31], v[188:191]\n"
      "s_waitcnt lgkmcnt(2)\n"
      "v_cvt_pk_bf16_f32 v26, v112, v113\n"
      "v_exp_f32 v115, v151\n"
      "v_mfma_f32_16x16x32_bf16 v[160:163], a[80:83], a[32:35], v[160:163]\n"
      "v_add_u32 v126, %[xr], v127\n"
      "v_exp_f32 v112, v152\n"
      "v_mfma_f32_16x16x32_bf16 v[168:171], a[80:83], a[36:39], v[168:171]\n"
      "s_add_u32 m0, %[wb], 81920\n"
      "v_cvt_pk_bf16_f32 v27, v114, v115\n"
      "v_exp_f32 v113, v153\n"
      "v_mfma_f32_16x16x32_bf16 v[176:179], a[80:83], a[40:43], v[176:179]\n"
      "buffer_load_dwordx4 v249, s[24:27], s94 offen nt lds\n"
      "v_mfma_f32_16x16x32_bf16 v[184:187], a[80:83], a[44:47], v[184:187]\n"
      "s_add_u32 m0, %[wb], 90112\n"
      "v_or3_b32 v116, v116, v26, v27\n"
      "v_mfma_f32_16x16x32_bf16 v[164:167], a[84:87], a[32:35], v[164:167]\n"
      "buffer_load_dwordx4 v122, s[24:27], s94 offen nt lds\n"
      "v_mfma_f32_16x16x32_bf16 v[172:175], a[84:87], a[36:39], v[172:175]\n"
      "v_exp_f32 v114, v154\n"
      "v_cvt_pk_bf16_f32 v28, v112, v113\n"
      "v_mfma_f32_16x16x32_bf16 v[180:183], a[84:87], a[40:43], v[180:183]\n"
      "v_exp_f32 v115, v155\n"
      "v_exp_f32 v112, v156\n"
      "v_mov_b32 v123, s88\n"
      "v_mfma_f32_16x16x32_bf16 v[188:191], a[84:87], a[44:47], v[188:191]\n"
      "s_waitcnt lgkmcnt(0)\n"
      "v_cvt_pk_bf16_f32 v29, v114, v115\n"
      "v_exp_f32 v113, v157\n"
      "s_waitcnt vmcnt(6)\n"
      "s_barrier\n"
      "v_mfma_f32_16x16x32_bf16 v[160:163], a[88:91], a[48:51], v[160:163]\n"
      "ds_read_b64_tr_b16 a[96:97], v250 offset:33792\n"
      "ds_read_b64_tr_b16 a[98:99], v250 offset:37888\n"
      "v_mfma_f32_16x16x32_bf16 v[168:171], a[88:91], a[52:55], v[168:171]\n"
      "ds_read_b64_tr_b16 a[100:101], v251 offset:33792\n"
      "ds_read_b64_tr_b16 a[102:103], v251 offset:37888\n"
      "v_or3_b32 v116, v116, v28, v29\n"
      "v_mfma_f32_16x16x32_bf16 v[176:179], a[88:91], a[56:59], v[176:179]\n"
      "ds_read_b64_tr_b16 a[112:113], v246 offset:1024\n"
      "ds_read_b64_tr_b16 a[114:115], v246 offset:5120\n"
      "v_mfma_f32_16x16x32_bf16 v[184:187], a[88:91], a[60:63], v[184:187]\n"
      "ds_read_b64_tr_b16 a[116:117], v247 offset:1024\n"
      "ds_read_b64_tr_b16 a[118:119], v247 offset:5120\n"
      "v_exp_f32 v114, v158\n"
      "v_mfma_f32_16x16x32_bf16 v[164:167], a[92:95], a[48:51], v[164:167]\n"
      "ds_read_b64_tr_b16 a[104:105], v240 offset:33792\n"
      "v_cvt_pk_bf16_f32 v30, v112, v113\n"
      "v_mfma_f32_16x16x32_bf16 v[172:175], a[92:95], a[52:55], v[172:175]\n"
      "s_mov_b32 s64, 5\n"
      "ds_read_b64_tr_b16 a[106:107], v240 offset:37888\n"
      "v_exp_f32 v115, v159\n"
      "s_nop 0\n"
      "v_mfma_f32_16x16x32_bf16 v[180:183], a[92:95], a[56:59], v[180:183]\n"
      "ds_read_b64_tr_b16 a[108:109], v241 offset:33792\n"
      "v_cvt_pk_bf16_f32 v31, v114, v115\n"
      "v_or3_b32 v116, v116, v30, v31\n"
      "v_mfma_f32_16x16x32_bf16 v[188:191], a[92:95], a[60:63], v[188:191]\n"
      "ds_read_b64_tr_b16 a[110:111], v241 offset:37888\n"
      "v_and_b32 v116, 0x40004000, v116\n"
      "v_cmp_ne_u32 vcc, 0, v116\n"
      "s_waitcnt lgkmcnt(4)\n"
      "s_cbranch_vccnz FB0_%=\n"
      "RET0_5_%=:\n"
      "v_mfma_f32_16x16x32_bf16 a[160:163], a[96:99], v[0:3], a[160:163]\n"
      "ds_read_b64_tr_b16 a[120:121], v244 offset:1024\n"
      "v_mov_b32 v117, v224\n"
      "v_xor_b32 v242, 0x10000, v242\n"
      "v_xor_b32 v243, 0x10000, v243\n"
      "v_mfma_f32_16x16x32_bf16 a[176:179], a[100:103], v[0:3], a[176:179]\n"
      "ds_read_b64_tr_b16 a[122:123], v244 offset:5120\n"
      "v_exp_f32 v112, v160\n"
      "v_exp_f32 v113, v161\n"
      "v_mfma_f32_16x16x32_bf16 a[160:163], a[112:115], v[16:19], a[160:163]\n"
      "s_mov_b32 s64, 6\n"
      "ds_read_b64_tr_b16 a[124:125], v245 offset:1024\n"
      "v_exp_f32 v114, v162\n"
      "v_mfma_f32_16x16x32_bf16 a[176:179], a[116:119], v[16:19], a[176:179]\n"
      "s_add_u32 m0, %[wb], 114688\n"
      "ds_read_b64_tr_b16 a[126:127], v245 offset:5120\n"
      "v_cvt_pk_bf16_f32 v96, v112, v113\n"
      "v_exp_f32 v115, v163\n"
      "v_mfma_f32_16x16x32_bf16 a[164:167], a[96:99], v[4:7], a[164:167]\n"
      "buffer_load_dwordx4 v249, s[24:27], s95 offen nt lds\n"
      "v_mov_b32_dpp v117, v228 quad_perm:[0,1,2,3] row_mask:0x2 bank_mask:0xf\n"
      "v_mfma_f32_16x16x32_bf16 a[180:183], a[100:103], v[4:7], a[180:183]\n"
      "s_add_u32 m0, %[wb], 122880\n"
      "v_exp_f32 v112, v164\n"
      "v_cvt_pk_bf16_f32 v97, v114, v115\n"
      "v_exp_f32 v113, v165\n"
      "v_mfma_f32_16x16x32_bf16 a[164:167], a[112:115], v[20:23], a[164:167]\n"
      "buffer_load_dwordx4 v122, s[24:27], s95 offen nt lds\n"
      "v_or_b32 v116, v96, v97\n"
      "v_mfma_f32_16x16x32_bf16 a[180:183], a[116:119], v[20:23], a[180:183]\n"
      "ds_write_b128 v125, a[160:163] offset:0\n"
      "v_mfma_f32_16x16x32_bf16 a[168:171], a[96:99], v[8:11], a[168:171]\n"
      "v_mov_b32_dpp v117, v232 quad_perm:[0,1,2,3] row_mask:0x4 bank_mask:0xf\n"
      "v_exp_f32 v114, v166\n"
      "v_cvt_pk_bf16_f32 v98, v112, v113\n"
      "v_mfma_f32_16x16x32_bf16 a[184:187], a[100:103], v[8:11], a[184:187]\n"
      "ds_write_b128 v125, a[176:179] offset:4096\n"
      "v_exp_f32 v115, v167\n"
      "v_mfma_f32_16x16x32_bf16 a[168:171], a[112:115], v[24:27], a[168:171]\n"
      "v_exp_f32 v112, v168\n"
      "v_cvt_pk_bf16_f32 v99, v114, v115\n"
      "v_exp_f32 v113, v169\n"
      "v_mfma_f32_16x16x32_bf16 a[184:187], a[116:119], v[24:27], a[184:187]\n"
      "v_or3_b32 v116, v116, v98, v99\n"
      "v_exp_f32 v114, v170\n"
      "v_mfma_f32_16x16x32_bf16 a[172:175], a[96:99], v[12:15], a[172:175]\n"
      "ds_write_b128 v125, v[96:99] offset:8192\n"
      "v_mov_b32_dpp v117, v236 quad_perm:[0,1,2,3] row_mask:0x8 bank_mask:0xf\n"
      "v_mfma_f32_16x16x32_bf16 a[188:191], a[100:103], v[12:15], a[188:191]\n"
      "v_cvt_pk_bf16_f32 v100, v112, v113\n"
      "v_exp_f32 v115, v171\n"
      "v_exp_f32 v112, v172\n"
      "v_mfma_f32_16x16x32_bf16 a[172:175], a[112:115], v[28:31], a[172:175]\n"
      "v_cvt_pk_bf16_f32 v101, v114, v115\n"
      "v_exp_f32 v113, v173\n"
      "v_mfma_f32_16x16x32_bf16 a[188:191], a[116:119], v[28:31], a[188:191]\n"
      "s_add_u32 m0, %[wb], 16384\n"
      "ds_write_b128 v125, a[164:167] offset:1024\n"
      "v_or3_b32 v116, v116, v100, v101\n"
      "v_mfma_f32_16x16x32_bf16 a[192:195], a[196:199], v[16:19], a[192:195]\n"
      "buffer_load_dwordx4 v249, s[24:27], s77 offen nt lds\n"
      "ds_write_b32 v125, v117 offset:12292\n"
      "v_mfma_f32_16x16x32_bf16 a[192:195], a[200:203], v[20:23], a[192:195]\n"
      "s_add_u32 m0, %[wb], 24576\n"
      "v_exp_f32 v114, v174\n"
      "v_cvt_pk_bf16_f32 v102, v112, v113\n"
      "v_exp_f32 v115, v175\n"
      "v_mfma_f32_16x16x32_bf16 a[192:195], a[204:207], v[24:27], a[192:195]\n"
      "buffer_load_dwordx4 v122, s[24:27], s77 offen nt lds\n"
      "ds_write_b128 v125, a[180:183] offset:5120\n"
      "ds_write_b32 v125, v123 offset:12296\n"
      "v_mfma_f32_16x16x32_bf16 a[192:195], a[208:211], v[28:31], a[192:195]\n"
      "s_waitcnt lgkmcnt(7)\n"
      "v_exp_f32 v112, v176\n"
      "v_mfma_f32_16x16x32_bf16 v[64:67], a[104:107], v[0:3], v[64:67]\n"
      "v_cvt_pk_bf16_f32 v103, v114, v115\n"
      "v_exp_f32 v113, v177\n"
      "v_or3_b32 v116, v116, v102, v103\n"
      "v_mfma_f32_16x16x32_bf16 v[68:71], a[104:107], v[4:7], v[68:71]\n"
      "ds_write_b128 v125, v[100:103] offset:9216\n"
      "v_exp_f32 v114, v178\n"
      "v_mfma_f32_16x16x32_bf16 v[72:75], a[104:107], v[8:11], v[72:75]\n"
      "v_cvt_pk_bf16_f32 v104, v112, v113\n"
      "v_exp_f32 v115, v179\n"
      "v_mfma_f32_16x16x32_bf16 v[76:79], a[104:107], v[12:15], v[76:79]\n"
      "ds_write_b128 v125, a[168:171] offset:2048\n"
      "v_exp_f32 v112, v180\n"
      "v_mfma_f32_16x16x32_bf16 a[228:231], a[196:199], v[96:99], a[192:195]\n"
      "v_cvt_pk_bf16_f32 v105, v114, v115\n"
      "v_exp_f32 v113, v181\n"
      "v_or3_b32 v116, v116, v104, v105\n"
      "v_mfma_f32_16x16x32_bf16 v[80:83], a[108:111], v[0:3], v[80:83]\n"
      "v_exp_f32 v114, v182\n"
      "v_cvt_pk_bf16_f32 v106, v112, v113\n"
      "s_waitcnt vmcnt(8)\n"
      "s_barrier\n"
      "v_mfma_f32_16x16x32_bf16 v[84:87], a[108:111], v[4:7], v[84:87]\n"
      "ds_write_b128 v125, a[184:187] offset:6144\n"
      "ds_read_b64_tr_b16 a[212:213], v244 offset:33792\n"
      "v_mfma_f32_16x16x32_bf16 v[88:91], a[108:111], v[8:11], v[88:91]\n"
      "ds_read_b64_tr_b16 a[214:215], v244 offset:37888\n"
      "v_exp_f32 v115, v183\n"
      "v_exp_f32 v112, v184\n"
      "v_mfma_f32_16x16x32_bf16 v[92:95], a[108:111], v[12:15], v[92:95]\n"
      "ds_read_b64_tr_b16 a[216:217], v245 offset:33792\n"
      "v_cvt_pk_bf16_f32 v107, v114, v115\n"
      "v_exp_f32 v113, v185\n"
      "v_mfma_f32_16x16x32_bf16 v[64:67], a[120:123], v[16:19], v[64:67]\n"
      "s_add_u32 m0, %[wb], 82944\n"
      "ds_write_b128 v125, v[104:107] offset:10240\n"
      "ds_read_b64_tr_b16 a[218:219], v245 offset:37888\n"
      "v_mfma_f32_16x16x32_bf16 v[68:71], a[120:123], v[20:23], v[68:71]\n"
      "buffer_load_dwordx4 v249, s[24:27], s78 offen nt lds\n"
      "ds_read_b64_tr_b16 a[220:221], v248 offset:33792\n"
      "v_mfma_f32_16x16x32_bf16 v[72:75], a[120:123], v[24:27], v[72:75]\n"
      "s_add_u32 m0, %[wb], 91136\n"
      "ds_read_b64_tr_b16 a[222:223], v248 offset:37888\n"
      "v_or3_b32 v116, v116, v106, v107\n"
      "v_exp_f32 v114, v186\n"
      "v_mfma_f32_16x16x32_bf16 v[76:79], a[120:123], v[28:31], v[76:79]\n"
      "buffer_load_dwordx4 v122, s[24:27], s78 offen nt lds\n"
      "ds_write_b128 v125, a[172:175] offset:3072\n"
      "ds_read_b64_tr_b16 a[224:225], v120 offset:33792\n"
      "v_mfma_f32_16x16x32_bf16 a[228:231], a[200:203], v[100:103], a[228:231]\n"
      "ds_read_b64_tr_b16 a[226:227], v120 offset:37888\n"
      "v_mfma_f32_16x16x32_bf16 v[80:83], a[124:127], v[16:19], v[80:83]\n"
      "v_cvt_pk_bf16_f32 v108, v112, v113\n"
      "v_exp_f32 v115, v187\n"
      "v_exp_f32 v112, v188\n"
      "v_mfma_f32_16x16x32_bf16 v[84:87], a[124:127], v[20:23], v[84:87]\n"
      "ds_write_b128 v125, a[188:191] offset:7168\n"
      "v_cvt_pk_bf16_f32 v109, v114, v115\n"
      "v_mfma_f32_16x16x32_bf16 v[88:91], a[124:127], v[24:27], v[88:91]\n"
      "v_exp_f32 v113, v189\n"
      "v_or3_b32 v116, v116, v108, v109\n"
      "v_mfma_f32_16x16x32_bf16 v[92:95], a[124:127], v[28:31], v[92:95]\n"
      "v_exp_f32 v114, v190\n"
      "v_cvt_pk_bf16_f32 v110, v112, v113\n"
      "v_exp_f32 v115, v191\n"
      "v_mfma_f32_16x16x32_bf16 a[228:231], a[204:207], v[104:107], a[228:231]\n"
      "s_nop 0\n"
      "v_cvt_pk_bf16_f32 v111, v114, v115\n"
      "v_or3_b32 v116, v116, v110, v111\n"
      "v_and_b32 v116, 0x40004000, v116\n"
      "v_cmp_ne_u32 vcc, 0, v116\n"
      "s_cbranch_vccnz FB1_%=\n"
      "RET1_6_%=:\n"
      "ds_write_b128 v125, v[108:111] offset:11264\n"
      "v_mfma_f32_16x16x32_bf16 a[228:231], a[208:211], v[108:111], a[228:231]\n"
      "s_waitcnt lgkmcnt(7)\n"
      "v_mfma_f32_16x16x32_bf16 v[64:67], a[212:215], v[96:99], v[64:67]\n"
      "v_mov_b32 v148, 1.0\n"
      "v_mfma_f32_16x16x32_bf16 v[80:83], a[216:219], v[96:99], v[80:83]\n"
      "v_mov_b32 v149, 1.0\n"
      "v_mfma_f32_16x16x32_bf16 v[68:71], a[212:215], v[100:103], v[68:71]\n"
      "v_mov_b32 v157, %[fo]\n"
      "v_mfma_f32_16x16x32_bf16 v[84:87], a[216:219], v[100:103], v[84:87]\n"
      "v_mov_b32 v150, s72\n"
      "v_mfma_f32_16x16x32_bf16 v[72:75], a[212:215], v[104:107], v[72:75]\n"
      "v_mov_b32 v151, s76\n"
      "ds_write_b32 v125, a228 offset:12288\n"
      "v_mfma_f32_16x16x32_bf16 v[88:91], a[216:219], v[104:107], v[88:91]\n"
      "v_xor_b32 v158, 8, v157\n"
      "v_mfma_f32_16x16x32_bf16 v[76:79], a[212:215], v[108:111], v[76:79]\n"
      "v_mfma_f32_16x16x32_bf16 v[92:95], a[216:219], v[108:111], v[92:95]\n"
      "s_waitcnt lgkmcnt(0)\n"
      "s_barrier\n"
      "s_memrealtime s[68:69]\n"
      "ds_read_b32 v237, v127 offset:12296\n"
      "ds_read_b64 v[152:153], v126 offset:12288\n"
      "ds_read_b128 v[160:163], v126 offset:0\n"
      "ds_read_b128 v[176:179], v126 offset:4096\n"
      "ds_read_b128 v[164:167], v126 offset:1024\n"
      "ds_read_b128 v[180:183], v126 offset:5120\n"
      "ds_read_b128 v[168:171], v126 offset:2048\n"
      "ds_read_b128 v[184:187], v126 offset:6144\n"
      "ds_read_b128 v[172:175], v126 offset:3072\n"
      "ds_read_b128 v[188:191], v126 offset:7168\n"
      "ds_read_b128 a[64:67], v126 offset:8192\n"
      "ds_read_b128 a[68:71], v126 offset:9216\n"
      "ds_read_b128 a[72:75], v126 offset:10240\n"
      "ds_read_b128 a[76:79], v126 offset:11264\n"
      "v_accvgpr_read_b32 v144, a228\n"
      "v_and_b32 v118, 0xf0, v127\n"
      "v_lshrrev_b32 v156, 3, v127\n"
      "v_and_b32 v156, 32, v156\n"
      "v_lshl_or_b32 v156, v118, 4, v156\n"
      "v_lshrrev_b32 v118, 5, v127\n"
      "v_and_or_b32 v156, v118, 16, v156\n"
      "v_add_u32 v156, %[oo], v156\n"
      "v_add_u32 v155, 0x2000, v156\n"
      "v_xor_b32 v242, 0x2000, v240\n"
      "v_xor_b32 v243, 0x2000, v241\n"
      "v_mov_b32 v236, v150\n"
      "s_waitcnt lgkmcnt(12)\n"
      "v_cmp_neq_f32 vcc, v153, v117\n"
      "v_readfirstlane_b32 s88, v237\n"
      "s_cbranch_vccnz MIS_%=\n"
      "MR_%=:\n"
      "v_mul_f32 v144, v144, v148\n"
      "v_mov_b32 v145, v117\n"
      "v_fmac_f32 v144, v152, v149\n"
      "s_waitcnt lgkmcnt(10)\n"
      "v_pk_add_f32 v[64:65], v[64:65], v[160:161]\n"
      "v_pk_add_f32 v[66:67], v[66:67], v[162:163]\n"
      "v_pk_add_f32 v[80:81], v[80:81], v[176:177]\n"
      "v_pk_add_f32 v[82:83], v[82:83], v[178:179]\n"
      "s_waitcnt lgkmcnt(8)\n"
      "v_pk_add_f32 v[68:69], v[68:69], v[164:165]\n"
      "v_pk_add_f32 v[70:71], v[70:71], v[166:167]\n"
      "v_pk_add_f32 v[84:85], v[84:85], v[180:181]\n"
      "v_pk_add_f32 v[86:87], v[86:87], v[182:183]\n"
      "s_waitcnt lgkmcnt(6)\n"
      "v_pk_add_f32 v[72:73], v[72:73], v[168:169]\n"
      "v_pk_add_f32 v[74:75], v[74:75], v[170:171]\n"
      "v_pk_add_f32 v[88:89], v[88:89], v[184:185]\n"
      "v_pk_add_f32 v[90:91], v[90:91], v[186:187]\n"
      "s_waitcnt lgkmcnt(4)\n"
      "v_pk_add_f32 v[76:77], v[76:77], v[172:173]\n"
      "v_pk_add_f32 v[78:79], v[78:79], v[174:175]\n"
      "v_pk_add_f32 v[92:93], v[92:93], v[188:189]\n"
      "v_pk_add_f32 v[94:95], v[94:95], v[190:191]\n"
      "s_waitcnt lgkmcnt(0)\n"
      "s_add_u32 s65, s68, 55\n"
      "s_mov_b32 s66, s88\n"
      "s_sub_u32 s67, s65, s66\n"
      "s_cmp_lt_i32 s67, 0\n"
      "s_cselect_b32 s88, s66, s65\n"
      "v_mov_b32 v237, s88\n"
      "s_mov_b32 s88, s66\n"
      "s_cmp_lg_u32 %[xw], 0\n"
      "s_cbranch_scc1 NH_%=\n"
      "s_mov_b64 exec, 1\n"
      "global_store_dwordx2 v157, v[236:237], s[18:19] sc1\n"
      "s_mov_b64 exec, -1\n"
      "NH_%=:\n"
      "v_mfma_f32_16x16x32_bf16 v[64:67], a[220:223], a[64:67], v[64:67]\n"
      "v_mfma_f32_16x16x32_bf16 v[80:83], a[224:227], a[64:67], v[80:83]\n"
      "v_mfma_f32_16x16x32_bf16 v[68:71], a[220:223], a[68:71], v[68:71]\n"
      "v_mfma_f32_16x16x32_bf16 v[84:87], a[224:227], a[68:71], v[84:87]\n"
      "v_mfma_f32_16x16x32_bf16 v[72:75], a[220:223], a[72:75], v[72:75]\n"
      "v_mfma_f32_16x16x32_bf16 v[88:91], a[224:227], a[72:75], v[88:91]\n"
      "v_mfma_f32_16x16x32_bf16 v[76:79], a[220:223], a[76:79], v[76:79]\n"
      "v_mfma_f32_16x16x32_bf16 v[92:95], a[224:227], a[76:79], v[92:95]\n"
      "s_nop 1\n"
      "s_memrealtime s[68:69]\n"
      "v_cvt_pk_bf16_f32 v128, v64, v65\n"
      "v_cvt_pk_bf16_f32 v129, v66, v67\n"
      "v_cvt_pk_bf16_f32 v130, v80, v81\n"
      "v_cvt_pk_bf16_f32 v131, v82, v83\n"
      "v_cvt_pk_bf16_f32 v132, v68, v69\n"
      "v_cvt_pk_bf16_f32 v133, v70, v71\n"
      "v_cvt_pk_bf16_f32 v134, v84, v85\n"
      "v_cvt_pk_bf16_f32 v135, v86, v87\n"
      "v_cvt_pk_bf16_f32 v136, v72, v73\n"
      "v_cvt_pk_bf16_f32 v137, v74, v75\n"
      "v_cvt_pk_bf16_f32 v138, v88, v89\n"
      "v_cvt_pk_bf16_f32 v139, v90, v91\n"
      "v_cvt_pk_bf16_f32 v140, v76, v77\n"
      "v_cvt_pk_bf16_f32 v141, v78, v79\n"
      "v_cvt_pk_bf16_f32 v142, v92, v93\n"
      "v_cvt_pk_bf16_f32 v143, v94, v95\n"
      "v_mad_u32_u24 v147, v144, s60, v151\n"
      "v_mad_u32_u24 v147, v145, s62, v147\n"
      "v_add3_u32 v146, v150, v144, v145\n"
      "v_mad_u32_u24 v147, v128, s60, v147\n"
      "v_mad_u32_u24 v147, v129, s62, v147\n"
      "v_add3_u32 v146, v146, v128, v129\n"
      "v_mad_u32_u24 v147, v130, s70, v147\n"
      "v_mad_u32_u24 v147, v131, s71, v147\n"
      "v_add3_u32 v146, v146, v130, v131\n"
      "v_mad_u32_u24 v147, v132, s60, v147\n"
      "v_mad_u32_u24 v147, v133, s62, v147\n"
      "v_add3_u32 v146, v146, v132, v133\n"
      "v_mad_u32_u24 v147, v134, s70, v147\n"
      "v_mad_u32_u24 v147, v135, s71, v147\n"
      "v_add3_u32 v146, v146, v134, v135\n"
      "v_mad_u32_u24 v147, v136, s60, v147\n"
      "v_mad_u32_u24 v147, v137, s62, v147\n"
      "v_add3_u32 v146, v146, v136, v137\n"
      "v_mad_u32_u24 v147, v138, s70, v147\n"
      "v_mad_u32_u24 v147, v139, s71, v147\n"
      "v_add3_u32 v146, v146, v138, v139\n"
      "v_mad_u32_u24 v147, v140, s60, v147\n"
      "v_mad_u32_u24 v147, v141, s62, v147\n"
      "v_add3_u32 v146, v146, v140, v141\n"
      "v_mad_u32_u24 v147, v142, s70, v147\n"
      "v_mad_u32_u24 v147, v143, s71, v147\n"
      "v_add3_u32 v146, v146, v142, v143\n"
      "s_waitcnt lgkmcnt(0)\n"
      "s_call_b64 s[84:85], SPIN_%=\n"
      "global_load_dwordx2 v[232:233], v158, s[18:19] sc1\n"
      "buffer_store_dwordx4 v[128:131], v127, s[28:31], %[xo] offen offset:0 sc1\n"
      "buffer_store_dwordx4 v[132:135], v127, s[28:31], %[xo] offen offset:1024 sc1\n"
      "buffer_store_dwordx4 v[136:139], v127, s[28:31], %[xo] offen offset:2048 sc1\n"
      "buffer_store_dwordx4 v[140:143], v127, s[28:31], %[xo] offen offset:3072 sc1\n"
      "buffer_store_dwordx4 v[144:147], v127, s[28:31], s81 offen sc1\n"
      "global_load_dwordx2 v[234:235], v158, s[18:19] sc1\n"
      "s_waitcnt vmcnt(7)\n"
      "s_barrier\n"
      "ds_read_b64_tr_b16 a[96:97], v246 offset:32768\n"
      "ds_read_b64_tr_b16 a[98:99], v246 offset:36864\n"
      "ds_read_b64_tr_b16 a[100:101], v247 offset:32768\n"
      "ds_read_b64_tr_b16 a[102:103], v247 offset:36864\n"
      "ds_read_b64_tr_b16 a[112:113], v250 offset:0\n"
      "ds_read_b64_tr_b16 a[114:115], v250 offset:4096\n"
      "ds_read_b64_tr_b16 a[116:117], v251 offset:0\n"
      "ds_read_b64_tr_b16 a[118:119], v251 offset:4096\n"
      "ds_read_b64_tr_b16 a[0:1], v250 offset:32768\n"
      "ds_read_b64_tr_b16 a[2:3], v250 offset:36864\n"
      "ds_read_b64_tr_b16 a[4:5], v251 offset:32768\n"
      "ds_read_b64_tr_b16 a[6:7], v251 offset:36864\n"
      "s_waitcnt lgkmcnt(8)\n"
      "v_mfma_f32_16x16x32_bf16 a[128:131], a[96:99], v[208:211], a[128:131]\n"
      "ds_read_b64_tr_b16 a[104:105], v244 offset:32768\n"
      "ds_read_b64_tr_b16 a[106:107], v244 offset:36864\n"
      "v_mfma_f32_16x16x32_bf16 a[144:147], a[100:103], v[208:211], a[144:147]\n"
      "ds_read_b64_tr_b16 a[108:109], v245 offset:32768\n"
      "ds_read_b64_tr_b16 a[110:111], v245 offset:36864\n"
      "s_waitcnt lgkmcnt(8)\n"
      "v_mfma_f32_16x16x32_bf16 a[128:131], a[112:115], v[192:195], a[128:131]\n"
      "ds_read_b64_tr_b16 a[120:121], v240 offset:0\n"
      "ds_read_b64_tr_b16 a[122:123], v240 offset:4096\n"
      "v_mfma_f32_16x16x32_bf16 a[144:147], a[116:119], v[192:195], a[144:147]\n"
      "ds_read_b64_tr_b16 a[124:125], v241 offset:0\n"
      "ds_read_b64_tr_b16 a[126:127], v241 offset:4096\n"
      "s_waitcnt lgkmcnt(8)\n"
      "v_mfma_f32_16x16x32_bf16 a[128:131], a[0:3], v[0:3], a[128:131]\n"
      "ds_read_b64_tr_b16 a[8:9], v240 offset:32768\n"
      "ds_read_b64_tr_b16 a[10:11], v240 offset:36864\n"
      "v_mfma_f32_16x16x32_bf16 a[144:147], a[4:7], v[0:3], a[144:147]\n"
      "ds_read_b64_tr_b16 a[12:13], v241 offset:32768\n"
      "ds_read_b64_tr_b16 a[14:15], v241 offset:36864\n"
      "v_mfma_f32_16x16x32_bf16 a[132:135], a[96:99], v[212:215], a[132:135]\n"
      "ds_write_b128 v125, v[16:19] offset:8192\n"
      "v_lshrrev_b32 v118, 2, v127\n"
      "v_mfma_f32_16x16x32_bf16 a[148:151], a[100:103], v[212:215], a[148:151]\n"
      "v_xor_b32 v118, v118, v127\n"
      "v_mfma_f32_16x16x32_bf16 a[132:135], a[112:115], v[196:199], a[132:135]\n"
      "v_and_b32 v118, 0xc0, v118\n"
      "v_mfma_f32_16x16x32_bf16 a[148:151], a[116:119], v[196:199], a[148:151]\n"
      "v_and_or_b32 v118, v127, 32, v118\n"
      "v_mfma_f32_16x16x32_bf16 a[132:135], a[0:3], v[4:7], a[132:135]\n"
      "ds_write_b128 v125, v[20:23] offset:9216\n"
      "v_and_b32 v119, 16, v127\n"
      "v_mfma_f32_16x16x32_bf16 a[148:151], a[4:7], v[4:7], a[148:151]\n"
      "v_mov_b32 v84, 0x3f80\n"
      "v_mfma_f32_16x16x32_bf16 a[136:139], a[96:99], v[216:219], a[136:139]\n"
      "v_lshlrev_b32 v119, v119, v84\n"
      "v_mfma_f32_16x16x32_bf16 a[152:155], a[100:103], v[216:219], a[152:155]\n"
      "v_cmp_eq_u32 vcc, 0, v118\n"
      "v_mfma_f32_16x16x32_bf16 a[136:139], a[112:115], v[200:203], a[136:139]\n"
      "ds_write_b128 v125, v[24:27] offset:10240\n"
      "v_mov_b32 v84, 0\n"
      "v_mfma_f32_16x16x32_bf16 a[152:155], a[116:119], v[200:203], a[152:155]\n"
      "v_cndmask_b32 v86, 0, v119, vcc\n"
      "v_mfma_f32_16x16x32_bf16 a[136:139], a[0:3], v[8:11], a[136:139]\n"
      "v_cmp_eq_u32 vcc, 32, v118\n"
      "v_mfma_f32_16x16x32_bf16 a[152:155], a[4:7], v[8:11], a[152:155]\n"
      "v_mov_b32 v85, 0\n"
      "v_mfma_f32_16x16x32_bf16 a[140:143], a[96:99], v[220:223], a[140:143]\n"
      "ds_write_b128 v125, v[28:31] offset:11264\n"
      "v_cndmask_b32 v87, 0, v119, vcc\n"
      "v_mfma_f32_16x16x32_bf16 a[156:159], a[100:103], v[220:223], a[156:159]\n"
      "v_mov_b32 v88, 0\n"
      "v_mfma_f32_16x16x32_bf16 a[140:143], a[112:115], v[204:207], a[140:143]\n"
      "v_mov_b32 v89, 0\n"
      "v_mfma_f32_16x16x32_bf16 a[156:159], a[116:119], v[204:207], a[156:159]\n"
      "v_mfma_f32_16x16x32_bf16 a[140:143], a[0:3], v[12:15], a[140:143]\n"
      "ds_write_b128 v125, a[128:131] offset:0\n"
      "v_mfma_f32_16x16x32_bf16 a[156:159], a[4:7], v[12:15], a[156:159]\n"
      "s_waitcnt lgkmcnt(13)\n"
      "v_mfma_f32_16x16x32_bf16 v[32:35], a[104:107], v[208:211], v[32:35]\n"
      "ds_read_b64_tr_b16 a[32:33], v240 offset:1024\n"
      "v_mfma_f32_16x16x32_bf16 v[36:39], a[104:107], v[212:215], v[36:39]\n"
      "ds_read_b64_tr_b16 a[34:35], v240 offset:5120\n"
      "v_mfma_f32_16x16x32_bf16 v[40:43], a[104:107], v[216:219], v[40:43]\n"
      "ds_write_b128 v125, a[144:147] offset:4096\n"
      "ds_read_b64_tr_b16 a[36:37], v241 offset:1024\n"
      "v_mfma_f32_16x16x32_bf16 v[44:47], a[104:107], v[220:223], v[44:47]\n"
      "ds_read_b64_tr_b16 a[38:39], v241 offset:5120\n"
      "v_mfma_f32_16x16x32_bf16 v[48:51], a[108:111], v[208:211], v[48:51]\n"
      "ds_read_b64_tr_b16 a[40:41], v242 offset:1024\n"
      "v_mfma_f32_16x16x32_bf16 v[52:55], a[108:111], v[212:215], v[52:55]\n"
      "ds_read_b64_tr_b16 a[42:43], v242 offset:5120\n"
      "v_mfma_f32_16x16x32_bf16 v[56:59], a[108:111], v[216:219], v[56:59]\n"
      "ds_write_b128 v125, a[132:135] offset:1024\n"
      "ds_read_b64_tr_b16 a[44:45], v243 offset:1024\n"
      "v_mfma_f32_16x16x32_bf16 v[60:63], a[108:111], v[220:223], v[60:63]\n"
      "ds_read_b64_tr_b16 a[46:47], v243 offset:5120\n"
      "s_waitcnt lgkmcnt(15)\n"
      "v_mfma_f32_16x16x32_bf16 v[32:35], a[120:123], v[192:195], v[32:35]\n"
      "ds_read_b64_tr_b16 a[16:17], v244 offset:0\n"
      "v_mfma_f32_16x16x32_bf16 v[36:39], a[120:123], v[196:199], v[36:39]\n"
      "ds_read_b64_tr_b16 a[18:19], v244 offset:4096\n"
      "v_mfma_f32_16x16x32_bf16 v[40:43], a[120:123], v[200:203], v[40:43]\n"
      "ds_write_b128 v125, a[148:151] offset:5120\n"
      "ds_read_b64_tr_b16 a[20:21], v245 offset:0\n"
      "v_mfma_f32_16x16x32_bf16 v[44:47], a[120:123], v[204:207], v[44:47]\n"
      "ds_read_b64_tr_b16 a[22:23], v245 offset:4096\n"
      "v_mfma_f32_16x16x32_bf16 v[48:51], a[124:127], v[192:195], v[48:51]\n"
      "ds_read_b64_tr_b16 a[24:25], v248 offset:0\n"
      "v_mfma_f32_16x16x32_bf16 v[52:55], a[124:127], v[196:199], v[52:55]\n"
      "ds_read_b64_tr_b16 a[26:27], v248 offset:4096\n"
      "v_mfma_f32_16x16x32_bf16 v[56:59], a[124:127], v[200:203], v[56:59]\n"
      "ds_write_b128 v125, a[136:139] offset:2048\n"
      "ds_read_b64_tr_b16 a[28:29], v120 offset:0\n"
      "v_mfma_f32_16x16x32_bf16 v[60:63], a[124:127], v[204:207], v[60:63]\n"
      "ds_read_b64_tr_b16 a[30:31], v120 offset:4096\n"
      "s_waitcnt lgkmcnt(15)\n"
      "v_mfma_f32_16x16x32_bf16 v[32:35], a[8:11], v[0:3], v[32:35]\n"
      "v_mfma_f32_16x16x32_bf16 v[36:39], a[8:11], v[4:7], v[36:39]\n"
      "v_mfma_f32_16x16x32_bf16 v[40:43], a[8:11], v[8:11], v[40:43]\n"
      "ds_write_b128 v125, a[152:155] offset:6144\n"
      "v_mfma_f32_16x16x32_bf16 v[44:47], a[8:11], v[12:15], v[44:47]\n"
      "s_waitcnt vmcnt(6)\n"
      "v_cmp_ne_u32 vcc, v232, v150\n"
      "s_memrealtime s[68:69]\n"
      "s_cbranch_vccnz POLL_%=\n"
      "GOT_%=:\n"
      "v_readfirstlane_b32 s88, v233\n"
      "s_waitcnt lgkmcnt(0)\n"
      "s_add_u32 s88, s88, 60\n"
      "s_sub_u32 s67, s88, s68\n"
      "s_cmp_gt_u32 s67, 1000\n"
      "s_cbranch_scc1 LD_%=\n"
      "s_call_b64 s[84:85], SPIN_%=\n"
      "LD_%=:\n"
      "buffer_load_dwordx4 v[80:83], v127, s[28:31], s79 offen sc1\n"
      "buffer_load_dwordx4 v[64:67], v127, s[28:31], s80 offen offset:0 sc1\n"
      "buffer_load_dwordx4 v[68:71], v127, s[28:31], s80 offen offset:1024 sc1\n"
      "buffer_load_dwordx4 v[72:75], v127, s[28:31], s80 offen offset:2048 sc1\n"
      "buffer_load_dwordx4 v[76:79], v127, s[28:31], s80 offen offset:3072 sc1\n"
      "v_mfma_f32_16x16x32_bf16 v[48:51], a[12:15], v[0:3], v[48:51]\n"
      "v_mfma_f32_16x16x32_bf16 v[52:55], a[12:15], v[4:7], v[52:55]\n"
      "v_mfma_f32_16x16x32_bf16 v[56:59], a[12:15], v[8:11], v[56:59]\n"
      "ds_write_b128 v125, a[140:143] offset:3072\n"
      "v_mfma_f32_16x16x32_bf16 v[60:63], a[12:15], v[12:15], v[60:63]\n"
      "s_waitcnt lgkmcnt(15)\n"
      "v_mfma_f32_16x16x32_bf16 v[32:35], a[32:35], v[96:99], v[32:35]\n"
      "v_mfma_f32_16x16x32_bf16 v[48:51], a[36:39], v[96:99], v[48:51]\n"
      "v_mfma_f32_16x16x32_bf16 v[36:39], a[32:35], v[100:103], v[36:39]\n"
      "ds_write_b128 v125, a[156:159] offset:7168\n"
      "v_mfma_f32_16x16x32_bf16 v[52:55], a[36:39], v[100:103], v[52:55]\n"
      "v_mfma_f32_16x16x32_bf16 v[40:43], a[32:35], v[104:107], v[40:43]\n"
      "v_mfma_f32_16x16x32_bf16 v[56:59], a[36:39], v[104:107], v[56:59]\n"
      "v_mfma_f32_16x16x32_bf16 v[44:47], a[32:35], v[108:111], v[44:47]\n"
      "v_mfma_f32_16x16x32_bf16 v[60:63], a[36:39], v[108:111], v[60:63]\n"
      "s_waitcnt lgkmcnt(13)\n"
      "v_mfma_f32_16x16x32_bf16 v[32:35], a[40:43], a[64:67], v[32:35]\n"
      "v_mfma_f32_16x16x32_bf16 v[48:51], a[44:47], a[64:67], v[48:51]\n"
      "v_mfma_f32_16x16x32_bf16 v[36:39], a[40:43], a[68:71], v[36:39]\n"
      "v_mfma_f32_16x16x32_bf16 v[52:55], a[44:47], a[68:71], v[52:55]\n"
      "v_mfma_f32_16x16x32_bf16 v[40:43], a[40:43], a[72:75], v[40:43]\n"
      "v_mfma_f32_16x16x32_bf16 v[56:59], a[44:47], a[72:75], v[56:59]\n"
      "v_mfma_f32_16x16x32_bf16 v[44:47], a[40:43], a[76:79], v[44:47]\n"
      "v_mfma_f32_16x16x32_bf16 v[60:63], a[44:47], a[76:79], v[60:63]\n"
      "s_waitcnt lgkmcnt(8)\n"
      "v_mfma_f32_16x16x32_bf16 v[32:35], a[16:19], v[16:19], v[32:35]\n"
      "v_mfma_f32_16x16x32_bf16 v[48:51], a[20:23], v[16:19], v[48:51]\n"
      "v_mfma_f32_16x16x32_bf16 v[36:39], a[16:19], v[20:23], v[36:39]\n"
      "v_mfma_f32_16x16x32_bf16 v[52:55], a[20:23], v[20:23], v[52:55]\n"
      "v_mfma_f32_16x16x32_bf16 v[40:43], a[16:19], v[24:27], v[40:43]\n"
      "v_mfma_f32_16x16x32_bf16 v[56:59], a[20:23], v[24:27], v[56:59]\n"
      "v_mfma_f32_16x16x32_bf16 v[44:47], a[16:19], v[28:31], v[44:47]\n"
      "v_mfma_f32_16x16x32_bf16 v[60:63], a[20:23], v[28:31], v[60:63]\n"
      "s_waitcnt lgkmcnt(0)\n"
      "s_barrier\n"
      "ds_read_b128 v[160:163], v126 offset:0\n"
      "ds_read_b128 v[176:179], v126 offset:4096\n"
      "ds_read_b128 v[164:167], v126 offset:1024\n"
      "ds_read_b128 v[180:183], v126 offset:5120\n"
      "ds_read_b128 v[168:171], v126 offset:2048\n"
      "ds_read_b128 v[184:187], v126 offset:6144\n"
      "ds_read_b128 v[172:175], v126 offset:3072\n"
      "ds_read_b128 v[188:191], v126 offset:7168\n"
      "ds_read_b128 a[80:83], v126 offset:8192\n"
      "ds_read_b128 a[84:87], v126 offset:9216\n"
      "ds_read_b128 a[88:91], v126 offset:10240\n"
      "ds_read_b128 a[92:95], v126 offset:11264\n"
      "s_waitcnt lgkmcnt(10)\n"
      "v_pk_add_f32 v[32:33], v[32:33], v[160:161]\n"
      "v_pk_add_f32 v[34:35], v[34:35], v[162:163]\n"
      "v_pk_add_f32 v[48:49], v[48:49], v[176:177]\n"
      "v_pk_add_f32 v[50:51], v[50:51], v[178:179]\n"
      "s_waitcnt lgkmcnt(8)\n"
      "v_pk_add_f32 v[36:37], v[36:37], v[164:165]\n"
      "v_pk_add_f32 v[38:39], v[38:39], v[166:167]\n"
      "v_pk_add_f32 v[52:53], v[52:53], v[180:181]\n"
      "v_pk_add_f32 v[54:55], v[54:55], v[182:183]\n"
      "s_waitcnt lgkmcnt(6)\n"
      "v_pk_add_f32 v[40:41], v[40:41], v[168:169]\n"
      "v_pk_add_f32 v[42:43], v[42:43], v[170:171]\n"
      "v_pk_add_f32 v[56:57], v[56:57], v[184:185]\n"
      "v_pk_add_f32 v[58:59], v[58:59], v[186:187]\n"
      "s_waitcnt lgkmcnt(4)\n"
      "v_pk_add_f32 v[44:45], v[44:45], v[172:173]\n"
      "v_pk_add_f32 v[46:47], v[46:47], v[174:175]\n"
      "v_pk_add_f32 v[60:61], v[60:61], v[188:189]\n"
      "v_pk_add_f32 v[62:63], v[62:63], v[190:191]\n"
      "s_waitcnt lgkmcnt(0)\n"
      "v_mfma_f32_16x16x32_bf16 v[32:35], a[24:27], a[80:83], v[32:35]\n"
      "v_mfma_f32_16x16x32_bf16 v[48:51], a[28:31], a[80:83], v[48:51]\n"
      "v_mfma_f32_16x16x32_bf16 v[36:39], a[24:27], a[84:87], v[36:39]\n"
      "v_mfma_f32_16x16x32_bf16 v[52:55], a[28:31], a[84:87], v[52:55]\n"
      "v_mfma_f32_16x16x32_bf16 v[40:43], a[24:27], a[88:91], v[40:43]\n"
      "v_mfma_f32_16x16x32_bf16 v[56:59], a[28:31], a[88:91], v[56:59]\n"
      "v_mfma_f32_16x16x32_bf16 v[44:47], a[24:27], a[92:95], v[44:47]\n"
      "v_mfma_f32_16x16x32_bf16 v[60:63], a[28:31], a[92:95], v[60:63]\n"
      "CHK_%=:\n"
      "s_waitcnt vmcnt(4)\n"
      "v_add_f32 v224, v144, v80\n"
      "v_rcp_f32 v224, v224\n"
      "v_mad_u32_u24 v119, v80, s60, v151\n"
      "v_mad_u32_u24 v119, v81, s62, v119\n"
      "v_add3_u32 v118, v150, v80, v81\n"
      "s_waitcnt vmcnt(3)\n"
      "v_mfma_f32_16x16x32_bf16 v[160:163], v[86:89], v[64:67], v[32:35]\n"
      "v_mad_u32_u24 v119, v64, s60, v119\n"
      "v_mad_u32_u24 v119, v65, s62, v119\n"
      "v_add3_u32 v118, v118, v64, v65\n"
      "v_mov_b32 v226, v224\n"
      "v_mfma_f32_16x16x32_bf16 v[176:179], v[84:87], v[64:67], v[48:51]\n"
      "v_mad_u32_u24 v119, v66, s70, v119\n"
      "v_mad_u32_u24 v119, v67, s71, v119\n"
      "v_add3_u32 v118, v118, v66, v67\n"
      "v_permlane32_swap_b32 v224, v226\n"
      "s_waitcnt vmcnt(2)\n"
      "v_mfma_f32_16x16x32_bf16 v[164:167], v[86:89], v[68:71], v[36:39]\n"
      "v_mad_u32_u24 v119, v68, s60, v119\n"
      "v_mad_u32_u24 v119, v69, s62, v119\n"
      "v_add3_u32 v118, v118, v68, v69\n"
      "v_mov_b32 v225, v224\n"
      "v_mov_b32 v227, v226\n"
      "v_mfma_f32_16x16x32_bf16 v[180:183], v[84:87], v[68:71], v[52:55]\n"
      "v_mad_u32_u24 v119, v70, s70, v119\n"
      "v_mad_u32_u24 v119, v71, s71, v119\n"
      "v_add3_u32 v118, v118, v70, v71\n"
      "v_permlane16_swap_b32 v224, v225\n"
      "s_waitcnt vmcnt(1)\n"
      "v_mfma_f32_16x16x32_bf16 v[168:171], v[86:89], v[72:75], v[40:43]\n"
      "v_mad_u32_u24 v119, v72, s60, v119\n"
      "v_mad_u32_u24 v119, v73, s62, v119\n"
      "v_add3_u32 v118, v118, v72, v73\n"
      "v_permlane16_swap_b32 v226, v227\n"
      "v_mfma_f32_16x16x32_bf16 v[184:187], v[84:87], v[72:75], v[56:59]\n"
      "v_mad_u32_u24 v119, v74, s70, v119\n"
      "v_mad_u32_u24 v119, v75, s71, v119\n"
      "v_add3_u32 v118, v118, v74, v75\n"
      "s_waitcnt vmcnt(0)\n"
      "v_mfma_f32_16x16x32_bf16 v[172:175], v[86:89], v[76:79], v[44:47]\n"
      "v_mad_u32_u24 v119, v76, s60, v119\n"
      "v_mad_u32_u24 v119, v77, s62, v119\n"
      "v_add3_u32 v118, v118, v76, v77\n"
      "v_mfma_f32_16x16x32_bf16 v[188:191], v[84:87], v[76:79], v[60:63]\n"
      "v_mad_u32_u24 v119, v78, s70, v119\n"
      "v_mad_u32_u24 v119, v79, s71, v119\n"
      "v_add3_u32 v118, v118, v78, v79\n"
      "v_cmp_ne_u64 vcc, v[118:119], v[82:83]\n"
      "s_cbranch_vccnz RETRY_%=\n"
      "OK_%=:\n"
      "v_cmp_neq_f32 vcc, v81, v117\n"
      "s_cbranch_vccnz FIX_%=\n"
      "FR_%=:\n"
      "v_pk_mul_f32 v[160:161], v[160:161], v[224:225] op_sel:[0,0] op_sel_hi:[1,0]\n"
      "v_pk_mul_f32 v[162:163], v[162:163], v[224:225] op_sel:[0,0] op_sel_hi:[1,0]\n"
      "v_pk_mul_f32 v[176:177], v[176:177], v[224:225] op_sel:[0,0] op_sel_hi:[1,0]\n"
      "v_pk_mul_f32 v[178:179], v[178:179], v[224:225] op_sel:[0,0] op_sel_hi:[1,0]\n"
      "v_cvt_pk_bf16_f32 v128, v160, v161\n"
      "v_cvt_pk_bf16_f32 v130, v176, v177\n"
      "v_cvt_pk_bf16_f32 v129, v162, v163\n"
      "v_cvt_pk_bf16_f32 v131, v178, v179\n"
      "v_pk_mul_f32 v[164:165], v[164:165], v[224:225] op_sel:[0,1] op_sel_hi:[1,1]\n"
      "v_permlane16_swap_b32 v128, v130\n"
      "v_permlane16_swap_b32 v129, v131\n"
      "global_store_dwordx4 v156, v[128:131], s[16:17] offset:-4096 sc1\n"
      "v_pk_mul_f32 v[166:167], v[166:167], v[224:225] op_sel:[0,1] op_sel_hi:[1,1]\n"
      "v_pk_mul_f32 v[180:181], v[180:181], v[224:225] op_sel:[0,1] op_sel_hi:[1,1]\n"
      "v_pk_mul_f32 v[182:183], v[182:183], v[224:225] op_sel:[0,1] op_sel_hi:[1,1]\n"
      "v_cvt_pk_bf16_f32 v132, v164, v165\n"
      "v_cvt_pk_bf16_f32 v134, v180, v181\n"
      "v_cvt_pk_bf16_f32 v133, v166, v167\n"
      "v_cvt_pk_bf16_f32 v135, v182, v183\n"
      "v_pk_mul_f32 v[168:169], v[168:169], v[226:227] op_sel:[0,0] op_sel_hi:[1,0]\n"
      "v_permlane16_swap_b32 v132, v134\n"
      "v_permlane16_swap_b32 v133, v135\n"
      "global_store_dwordx4 v156, v[132:135], s[16:17] offset:0 sc1\n"
      "v_pk_mul_f32 v[170:171], v[170:171], v[226:227] op_sel:[0,0] op_sel_hi:[1,0]\n"
      "v_pk_mul_f32 v[184:185], v[184:185], v[226:227] op_sel:[0,0] op_sel_hi:[1,0]\n"
      "v_pk_mul_f32 v[186:187], v[186:187], v[226:227] op_sel:[0,0] op_sel_hi:[1,0]\n"
      "v_cvt_pk_bf16_f32 v136, v168, v169\n"
      "v_cvt_pk_bf16_f32 v138, v184, v185\n"
      "v_cvt_pk_bf16_f32 v137, v170, v171\n"
      "v_cvt_pk_bf16_f32 v139, v186, v187\n"
      "v_pk_mul_f32 v[172:173], v[172:173], v[226:227] op_sel:[0,1] op_sel_hi:[1,1]\n"
      "v_permlane16_swap_b32 v136, v138\n"
      "v_permlane16_swap_b32 v137, v139\n"
      "global_store_dwordx4 v155, v[136:139], s[16:17] offset:-4096 sc1\n"
      "v_pk_mul_f32 v[174:175], v[174:175], v[226:227] op_sel:[0,1] op_sel_hi:[1,1]\n"
      "v_pk_mul_f32 v[188:189], v[188:189], v[226:227] op_sel:[0,1] op_sel_hi:[1,1]\n"
      "v_pk_mul_f32 v[190:191], v[190:191], v[226:227] op_sel:[0,1] op_sel_hi:[1,1]\n"
      "v_cvt_pk_bf16_f32 v140, v172, v173\n"
      "v_cvt_pk_bf16_f32 v142, v188, v189\n"
      "v_cvt_pk_bf16_f32 v141, v174, v175\n"
      "v_cvt_pk_bf16_f32 v143, v190, v191\n"
      "v_and_b32 v157, 0xffffff80, v157\n"
      "v_permlane16_swap_b32 v140, v142\n"
      "v_permlane16_swap_b32 v141, v143\n"
      "global_store_dwordx4 v155, v[140:143], s[16:17] offset:0 sc1\n"
      "s_and_b32 s67, %[fo], 8\n"
      "s_or_b32 s67, s67, %[xw]\n"
      "s_cbranch_scc1 END_%=\n"
      "s_mov_b64 exec, 1\n"
      "global_store_dwordx2 v157, v[150:151], s[18:19] sc1\n"
      "s_mov_b64 exec, -1\n"
      "END_%=:\n"
      ".subsection 1\n"
      "POLL_%=:\n"
      "s_waitcnt vmcnt(0) lgkmcnt(0)\n"
      "v_cmp_ne_u32 vcc, v234, v150\n"
      "v_mov_b32 v233, v235\n"
      "s_memrealtime s[68:69]\n"
      "s_sub_u32 s63, s63, 1\n"
      "s_cbranch_vccz GOT_%=\n"
      "global_load_dwordx2 v[234:235], v158, s[18:19] sc1\n"
      "s_cbranch_scc0 POLL_%=\n"
      "s_branch GOT_%=\n"
      "RETRY_%=:\n"
      "s_sub_u32 s63, s63, 1\n"
      "s_cbranch_scc1 OK_%=\n"
      "s_sleep 4\n"
      "buffer_load_dwordx4 v[80:83], v127, s[28:31], s79 offen sc1\n"
      "buffer_load_dwordx4 v[64:67], v127, s[28:31], s80 offen offset:0 sc1\n"
      "buffer_load_dwordx4 v[68:71], v127, s[28:31], s80 offen offset:1024 sc1\n"
      "buffer_load_dwordx4 v[72:75], v127, s[28:31], s80 offen offset:2048 sc1\n"
      "buffer_load_dwordx4 v[76:79], v127, s[28:31], s80 offen offset:3072 sc1\n"
      "s_branch CHK_%=\n"
      "GW0_%=:\n"
      "s_call_b64 s[84:85], WAIT_%=\n"
      "s_branch GX0_%=\n"
      "GW1_%=:\n"
      "s_call_b64 s[84:85], WAIT_%=\n"
      "s_branch GX1_%=\n"
      "GW2_%=:\n"
      "s_call_b64 s[84:85], WAIT_%=\n"
      "s_branch GX2_%=\n"
      "GW3_%=:\n"
      "s_call_b64 s[84:85], WAIT_%=\n"
      "s_add_u32 s87, s87, 1\n"
      "s_branch GX3_%=\n"
      "GW4_%=:\n"
      "s_call_b64 s[84:85], WAIT_%=\n"
      "s_branch GX4_%=\n"
      "GW5_%=:\n"
      "s_call_b64 s[84:85], WAIT_%=\n"
      "s_branch GX5_%=\n"
      "GW6_%=:\n"
      "s_call_b64 s[84:85], WAIT_%=\n"
      "s_branch GX6_%=\n"
      "GW7_%=:\n"
      "s_call_b64 s[84:85], WAIT_%=\n"
      "s_branch GX7_%=\n"
      "GW8_%=:\n"
      "s_call_b64 s[84:85], WAIT_%=\n"
      "s_branch GX8_%=\n"
      "WAIT_%=:\n"
      "s_add_u32 s87, s87, 1\n"
      "s_bitcmp1_b32 s89, s87\n"
      "s_cbranch_scc1 H1_%=\n"
      "SPIN_%=:\n"
      "s_sub_u32 s67, s68, s88\n"
      "s_cmp_ge_i32 s67, 0\n"
      "s_cbranch_scc1 GO_%=\n"
      "s_sleep 1\n"
      "s_memrealtime s[68:69]\n"
      "s_waitcnt lgkmcnt(0)\n"
      "s_branch SPIN_%=\n"
      "GO_%=:\n"
      "s_setpc_b64 s[84:85]\n"
      "H1_%=:\n"
      "s_cmp_gt_i32 s93, 50\n"
      "s_cbranch_scc1 HS_%=\n"
      "s_bitcmp1_b32 0x2a80, s87\n"
      "s_cbranch_scc0 H2_%=\n"
      "s_add_u32 s67, s66, 400\n"
      "s_waitcnt vmcnt(8)\n"
      "v_sub_u32 v250, s67, v250\n"
      "v_cmp_gt_u32 vcc, 900, v250\n"
      "v_add_u32 v250, 0xa00, v250\n"
      "v_xor_b32 v251, 0x100, v251\n"
      "s_sub_u32 s69, s66, s71\n"
      "s_cmp_ge_u32 s87, 12\n"
      "s_cselect_b32 s101, 50, s101\n"
      "s_bcnt1_i32_b32 s74, vcc_lo\n"
      "s_bcnt1_i32_b32 s75, vcc_hi\n"
      "v_cmp_gt_u32 vcc, 0x800, v250\n"
      "s_add_u32 s67, s74, s75\n"
      "s_cmp_le_u32 s67, 2\n"
      "s_cselect_b32 s86, 107, s86\n"
      "s_cmp_ge_u32 s67, 9\n"
      "s_cselect_b32 s86, 112, s86\n"
      "s_cmp_ge_u32 s67, 38\n"
      "s_cselect_b32 s76, 16, 33\n"
      "s_cmp_eq_u32 s87, 7\n"
      "s_cselect_b32 s76, s76, 15\n"
      "s_cbranch_vccnz H6_%=\n"
      "s_cmp_ge_u32 s74, s76\n"
      "s_cbranch_scc1 H3_%=\n"
      "s_cmp_eq_u32 s87, 13\n"
      "s_cbranch_scc1 H4_%=\n"
      "H8_%=:\n"
      "s_lshr_b32 s67, %[rep], 7\n"
      "s_cmp_eq_u32 s67, 1\n"
      "s_cbranch_scc0 SPIN_%=\n"
      "H5_%=:\n"
      "s_cmp_gt_i32 s93, 50\n"
      "s_cselect_b32 s75, -10, 0\n"
      "v_mov_b32 v127, s66\n"
      "s_add_u32 s74, s87, 2\n"
      "s_bfe_u32 s74, s74, 0x10001\n"
      "s_lshl_b32 s74, s74, 8\n"
      "s_add_u32 s74, s74, %[rep]\n"
      "RS_%=:\n"
      "s_sub_u32 s67, s68, s88\n"
      "s_cmp_ge_i32 s67, s75\n"
      "s_cbranch_scc1 RG_%=\n"
      "s_sleep 1\n"
      "s_memrealtime s[68:69]\n"
      "s_waitcnt lgkmcnt(0)\n"
      "s_branch RS_%=\n"
      "RG_%=:\n"
      "s_getreg_b32 s67, hwreg(HW_REG_IB_STS, 0, 4)\n"
      "s_cmp_le_u32 s67, 4\n"
      "s_cbranch_scc1 SPIN_%=\n"
      "buffer_store_dword v127, v124, s[28:31], s74 offen sc1\n"
      "s_branch SPIN_%=\n"
      "H6_%=:\n"
      "s_ff1_i32_b64 s67, vcc\n"
      "v_sub_u32 v250, s66, v250\n"
      "s_nop 3\n"
      "v_readlane_b32 s76, v250, s67\n"
      "s_add_u32 s76, s76, 2960\n"
      "s_and_b32 s76, s76, 31\n"
      "s_lshl_b32 s76, s76, 3\n"
      "s_lshl_b32 s89, 4, s87\n"
      "s_branch H7_%=\n"
      "H3_%=:\n"
      "s_mul_i32 s76, s69, 110\n"
      "s_ashr_i32 s76, s76, 10\n"
      "s_min_i32 s76, s76, 40\n"
      "s_add_u32 s76, s76, 84\n"
      "s_cmp_lt_u32 s75, 6\n"
      "s_cselect_b32 s76, 32, s76\n"
      "s_add_u32 s67, s66, 2000\n"
      "s_and_b32 s67, s67, -32\n"
      "s_lshr_b32 s75, s76, 3\n"
      "s_or_b32 s67, s67, s75\n"
      "v_mov_b32 v127, s67\n"
      "s_add_u32 s74, s87, 2\n"
      "s_bfe_u32 s74, s74, 0x10001\n"
      "s_lshl_b32 s74, s74, 8\n"
      "s_add_u32 s74, s74, %[rep]\n"
      "s_cmp_lt_u32 %[rep], 128\n"
      "s_cselect_b32 s89, 22, 20\n"
      "s_lshl_b32 s89, s89, s87\n"
      "s_sub_u32 s75, %[rep], 128\n"
      "s_cmp_lt_u32 s75, 32\n"
      "s_cbranch_scc0 H7_%=\n"
      "buffer_store_dword v127, v124, s[28:31], s74 offen sc1\n"
      "H7_%=:\n"
      "s_add_u32 s66, s66, s76\n"
      "s_mov_b32 s99, 13\n"
      "s_mov_b32 s100, 286\n"
      "s_add_u32 s93, s93, 99\n"
      "s_mov_b32 s86, 110\n"
      "s_mov_b32 s80, 0xc853212b\n"
      "s_mov_b32 s81, 0x842210\n"
      "s_sub_u32 s67, s65, s66\n"
      "s_cmp_lt_i32 s67, 0\n"
      "s_cselect_b32 s88, s66, s65\n"
      "s_mov_b32 s101, 200\n"
      "s_branch SPIN_%=\n"
      "H4_%=:\n"
      "s_mov_b32 s91, 0\n"
      "s_mov_b32 s89, 0\n"
      "v_mov_b32 v251, 0x4000000\n"
      "s_branch SPIN_%=\n"
      "HS_%=:\n"
      "s_bitcmp1_b32 s87, 0\n"
      "s_cbranch_scc0 H5_%=\n"
      "v_xor_b32 v251, 0x100, v251\n"
      "s_lshr_b32 s67, s89, s87\n"
      "s_cmp_gt_u32 s67, 1\n"
      "s_cbranch_scc1 SPIN_%=\n"
      "s_add_u32 s67, s66, 400\n"
      "s_waitcnt vmcnt(8)\n"
      "v_sub_u32 v250, s67, v250\n"
      "v_cmp_gt_u32 vcc, 900, v250\n"
      "s_nop 3\n"
      "s_bcnt1_i32_b32 s74, vcc_lo\n"
      "s_cmp_gt_u32 s74, 2\n"
      "s_cbranch_scc1 H4_%=\n"
      "s_mov_b32 s99, 21\n"
      "s_mov_b32 s100, 282\n"
      "s_mov_b32 s86, 107\n"
      "s_mov_b64 s[80:81], 0\n"
      "s_branch H4_%=\n"
      "H0_%=:\n"
      "buffer_load_dword v250, v251, s[28:31], 0 offen sc1\n"
      "s_branch SPIN_%=\n"
      "H9_%=:\n"
      "s_bitcmp1_b32 s87, 0\n"
      "s_cbranch_scc0 H5_%=\n"
      "v_xor_b32 v251, 0x100, v251\n"
      "s_cmp_eq_u32 s87, 5\n"
      "s_cbranch_scc1 H8_%=\n"
      "s_branch SPIN_%=\n"
      "H2_%=:\n"
      "s_cmp_gt_u32 s87, 2\n"
      "s_cbranch_scc1 H9_%=\n"
      "s_cmp_eq_u32 s87, 1\n"
      "s_cbranch_scc1 H0_%=\n"
      "s_waitcnt vmcnt(8)\n"
      "v_subrev_u32 v250, s71, v250\n"
      "v_min_u32 v250, 0x258, v250\n"
      "v_mbcnt_lo_u32_b32 v251, -1, 0\n"
      "v_mbcnt_hi_u32_b32 v251, -1, v251\n"
      "v_add_u32_dpp v250, v250, v250 row_shr:1 row_mask:0xf bank_mask:0xf bound_ctrl:0\n"
      "v_lshrrev_b32 v124, 1, v251\n"
      "v_lshl_or_b32 v124, v124, 14, v251\n"
      "v_add_u32_dpp v250, v250, v250 row_shr:2 row_mask:0xf bank_mask:0xf bound_ctrl:0\n"
      "v_lshlrev_b32 v251, 2, v251\n"
      "v_lshlrev_b32 v124, 12, v124\n"
      "v_add_u32 v124, 0x6000, v124\n"
      "v_add_u32_dpp v250, v250, v250 row_shr:4 row_mask:0xf bank_mask:0xf bound_ctrl:0\n"
      "v_add_u32 v251, %[vrd], v251\n"
      "v_readlane_b32 s66, v250, 7\n"
      "s_nop 0\n"
      "s_lshr_b32 s66, s66, 3\n"
      "s_add_u32 s66, s66, s71\n"
      "s_add_u32 s66, s66, %[cph]\n"
      "s_add_u32 s71, s66, 545\n"
      "s_sub_u32 s67, s65, s66\n"
      "s_cmp_lt_i32 s67, 0\n"
      "s_cselect_b32 s88, s66, s65\n"
      "s_branch SPIN_%=\n"
      "FB0_%=:\n"
      "s_nop 7\n"
      "s_nop 7\n"
      "v_xor_b32 v118, 0x0, v242\n"
      "v_xor_b32 v119, 0x0, v243\n"
      "ds_read_b128 a[64:67], v118 offset:0\n"
      "ds_read_b128 a[68:71], v118 offset:4096\n"
      "ds_read_b128 a[72:75], v119 offset:0\n"
      "ds_read_b128 a[76:79], v119 offset:4096\n"
      "ds_read_b128 a[80:83], v118 offset:1024\n"
      "ds_read_b128 a[84:87], v118 offset:5120\n"
      "ds_read_b128 a[88:91], v119 offset:1024\n"
      "ds_read_b128 a[92:95], v119 offset:5120\n"
      "s_waitcnt lgkmcnt(0)\n"
      "v_mfma_f32_16x16x32_bf16 v[128:131], a[64:67], a[0:3], 0\n"
      "v_mfma_f32_16x16x32_bf16 v[136:139], a[64:67], a[4:7], 0\n"
      "v_mfma_f32_16x16x32_bf16 v[144:147], a[64:67], a[8:11], 0\n"
      "v_mfma_f32_16x16x32_bf16 v[152:155], a[64:67], a[12:15], 0\n"
      "v_mfma_f32_16x16x32_bf16 v[132:135], a[68:71], a[0:3], 0\n"
      "v_mfma_f32_16x16x32_bf16 v[140:143], a[68:71], a[4:7], 0\n"
      "v_mfma_f32_16x16x32_bf16 v[148:151], a[68:71], a[8:11], 0\n"
      "v_mfma_f32_16x16x32_bf16 v[156:159], a[68:71], a[12:15], 0\n"
      "v_mfma_f32_16x16x32_bf16 v[128:131], a[72:75], a[16:19], v[128:131]\n"
      "v_mfma_f32_16x16x32_bf16 v[136:139], a[72:75], a[20:23], v[136:139]\n"
      "v_mfma_f32_16x16x32_bf16 v[144:147], a[72:75], a[24:27], v[144:147]\n"
      "v_mfma_f32_16x16x32_bf16 v[152:155], a[72:75], a[28:31], v[152:155]\n"
      "v_mfma_f32_16x16x32_bf16 v[132:135], a[76:79], a[16:19], v[132:135]\n"
      "v_mfma_f32_16x16x32_bf16 v[140:143], a[76:79], a[20:23], v[140:143]\n"
      "v_mfma_f32_16x16x32_bf16 v[148:151], a[76:79], a[24:27], v[148:151]\n"
      "v_mfma_f32_16x16x32_bf16 v[156:159], a[76:79], a[28:31], v[156:159]\n"
      "v_mfma_f32_16x16x32_bf16 v[128:131], a[80:83], a[32:35], v[128:131]\n"
      "v_mfma_f32_16x16x32_bf16 v[136:139], a[80:83], a[36:39], v[136:139]\n"
      "v_mfma_f32_16x16x32_bf16 v[144:147], a[80:83], a[40:43], v[144:147]\n"
      "v_mfma_f32_16x16x32_bf16 v[152:155], a[80:83], a[44:47], v[152:155]\n"
      "v_mfma_f32_16x16x32_bf16 v[132:135], a[84:87], a[32:35], v[132:135]\n"
      "v_mfma_f32_16x16x32_bf16 v[140:143], a[84:87], a[36:39], v[140:143]\n"
      "v_mfma_f32_16x16x32_bf16 v[148:151], a[84:87], a[40:43], v[148:151]\n"
      "v_mfma_f32_16x16x32_bf16 v[156:159], a[84:87], a[44:47], v[156:159]\n"
      "v_mfma_f32_16x16x32_bf16 v[128:131], a[88:91], a[48:51], v[128:131]\n"
      "v_mfma_f32_16x16x32_bf16 v[136:139], a[88:91], a[52:55], v[136:139]\n"
      "v_mfma_f32_16x16x32_bf16 v[144:147], a[88:91], a[56:59], v[144:147]\n"
      "v_mfma_f32_16x16x32_bf16 v[152:155], a[88:91], a[60:63], v[152:155]\n"
      "v_mfma_f32_16x16x32_bf16 v[132:135], a[92:95], a[48:51], v[132:135]\n"
      "v_mfma_f32_16x16x32_bf16 v[140:143], a[92:95], a[52:55], v[140:143]\n"
      "v_mfma_f32_16x16x32_bf16 v[148:151], a[92:95], a[56:59], v[148:151]\n"
      "v_mfma_f32_16x16x32_bf16 v[156:159], a[92:95], a[60:63], v[156:159]\n"
      "s_nop 7\n"
      "v_max3_f32 v112, v128, v129, v130\n"
      "v_max3_f32 v113, v136, v137, v138\n"
      "v_max3_f32 v114, v144, v145, v146\n"
      "v_max3_f32 v115, v152, v153, v154\n"
      "v_max3_f32 v112, v112, v131, v132\n"
      "v_max3_f32 v113, v113, v139, v140\n"
      "v_max3_f32 v114, v114, v147, v148\n"
      "v_max3_f32 v115, v115, v155, v156\n"
      "v_max3_f32 v112, v112, v133, v134\n"
      "v_max3_f32 v113, v113, v141, v142\n"
      "v_max3_f32 v114, v114, v149, v150\n"
      "v_max3_f32 v115, v115, v157, v158\n"
      "v_max_f32 v112, v112, v135\n"
      "v_max_f32 v113, v113, v143\n"
      "v_max_f32 v114, v114, v151\n"
      "v_max_f32 v115, v115, v159\n"
      "v_mov_b32 v160, v112\n"
      "v_mov_b32 v161, v113\n"
      "v_mov_b32 v162, v114\n"
      "v_mov_b32 v163, v115\n"
      "v_permlane16_swap_b32 v112, v160\n"
      "v_permlane16_swap_b32 v113, v161\n"
      "v_permlane16_swap_b32 v114, v162\n"
      "v_permlane16_swap_b32 v115, v163\n"
      "v_max_f32 v112, v112, v160\n"
      "v_max_f32 v113, v113, v161\n"
      "v_max_f32 v114, v114, v162\n"
      "v_max_f32 v115, v115, v163\n"
      "v_mov_b32 v160, v112\n"
      "v_mov_b32 v161, v113\n"
      "v_mov_b32 v162, v114\n"
      "v_mov_b32 v163, v115\n"
      "v_permlane32_swap_b32 v112, v160\n"
      "v_permlane32_swap_b32 v113, v161\n"
      "v_permlane32_swap_b32 v114, v162\n"
      "v_permlane32_swap_b32 v115, v163\n"
      "v_max_f32 v112, v112, v160\n"
      "v_max_f32 v113, v113, v161\n"
      "v_max_f32 v114, v114, v162\n"
      "v_max_f32 v115, v115, v163\n"
      "v_sub_f32 v112, 0xc1800000, v112\n"
      "v_sub_f32 v113, 0xc1800000, v113\n"
      "v_sub_f32 v114, 0xc1800000, v114\n"
      "v_sub_f32 v115, 0xc1800000, v115\n"
      "v_floor_f32 v112, v112\n"
      "v_floor_f32 v113, v113\n"
      "v_floor_f32 v114, v114\n"
      "v_floor_f32 v115, v115\n"
      "v_min_f32 v112, v112, v224\n"
      "v_min_f32 v113, v113, v228\n"
      "v_min_f32 v114, v114, v232\n"
      "v_min_f32 v115, v115, v236\n"
      "v_sub_f32 v164, v112, v224\n"
      "v_sub_f32 v165, v113, v228\n"
      "v_sub_f32 v166, v114, v232\n"
      "v_sub_f32 v167, v115, v236\n"
      "v_mov_b32 v224, v112\n"
      "v_mov_b32 v228, v113\n"
      "v_mov_b32 v232, v114\n"
      "v_mov_b32 v236, v115\n"
      "v_exp_f32 v112, v164\n"
      "v_exp_f32 v113, v165\n"
      "v_exp_f32 v114, v166\n"
      "v_exp_f32 v115, v167\n"
      "v_mov_b32 v225, v224\n"
      "v_mov_b32 v229, v228\n"
      "v_mov_b32 v233, v232\n"
      "v_mov_b32 v237, v236\n"
      "v_mov_b64 v[226:227], v[224:225]\n"
      "v_mov_b64 v[230:231], v[228:229]\n"
      "v_mov_b64 v[234:235], v[232:233]\n"
      "v_mov_b64 v[238:239], v[236:237]\n"
      "s_call_b64 s[84:85], SCALE_%=\n"
      "v_pk_add_f32 v[128:129], v[128:129], v[224:225]\n"
      "v_pk_add_f32 v[130:131], v[130:131], v[224:225]\n"
      "v_pk_add_f32 v[132:133], v[132:133], v[224:225]\n"
      "v_pk_add_f32 v[134:135], v[134:135], v[224:225]\n"
      "v_pk_add_f32 v[136:137], v[136:137], v[228:229]\n"
      "v_pk_add_f32 v[138:139], v[138:139], v[228:229]\n"
      "v_pk_add_f32 v[140:141], v[140:141], v[228:229]\n"
      "v_pk_add_f32 v[142:143], v[142:143], v[228:229]\n"
      "v_pk_add_f32 v[144:145], v[144:145], v[232:233]\n"
      "v_pk_add_f32 v[146:147], v[146:147], v[232:233]\n"
      "v_pk_add_f32 v[148:149], v[148:149], v[232:233]\n"
      "v_pk_add_f32 v[150:151], v[150:151], v[232:233]\n"
      "v_pk_add_f32 v[152:153], v[152:153], v[236:237]\n"
      "v_pk_add_f32 v[154:155], v[154:155], v[236:237]\n"
      "v_pk_add_f32 v[156:157], v[156:157], v[236:237]\n"
      "v_pk_add_f32 v[158:159], v[158:159], v[236:237]\n"
      "v_exp_f32 v112, v128\n"
      "v_exp_f32 v113, v129\n"
      "v_exp_f32 v114, v130\n"
      "v_cvt_pk_bf16_f32 v128, v112, v113\n"
      "v_exp_f32 v115, v131\n"
      "v_exp_f32 v112, v132\n"
      "v_cvt_pk_bf16_f32 v129, v114, v115\n"
      "v_exp_f32 v113, v133\n"
      "v_exp_f32 v114, v134\n"
      "v_cvt_pk_bf16_f32 v130, v112, v113\n"
      "v_exp_f32 v115, v135\n"
      "v_exp_f32 v112, v136\n"
      "v_cvt_pk_bf16_f32 v131, v114, v115\n"
      "v_exp_f32 v113, v137\n"
      "v_exp_f32 v114, v138\n"
      "v_cvt_pk_bf16_f32 v132, v112, v113\n"
      "v_exp_f32 v115, v139\n"
      "v_exp_f32 v112, v140\n"
      "v_cvt_pk_bf16_f32 v133, v114, v115\n"
      "v_exp_f32 v113, v141\n"
      "v_exp_f32 v114, v142\n"
      "v_cvt_pk_bf16_f32 v134, v112, v113\n"
      "v_exp_f32 v115, v143\n"
      "v_exp_f32 v112, v144\n"
      "v_cvt_pk_bf16_f32 v135, v114, v115\n"
      "v_exp_f32 v113, v145\n"
      "v_exp_f32 v114, v146\n"
      "v_cvt_pk_bf16_f32 v136, v112, v113\n"
      "v_exp_f32 v115, v147\n"
      "v_exp_f32 v112, v148\n"
      "v_cvt_pk_bf16_f32 v137, v114, v115\n"
      "v_exp_f32 v113, v149\n"
      "v_exp_f32 v114, v150\n"
      "v_cvt_pk_bf16_f32 v138, v112, v113\n"
      "v_exp_f32 v115, v151\n"
      "v_exp_f32 v112, v152\n"
      "v_cvt_pk_bf16_f32 v139, v114, v115\n"
      "v_exp_f32 v113, v153\n"
      "v_exp_f32 v114, v154\n"
      "v_cvt_pk_bf16_f32 v140, v112, v113\n"
      "v_exp_f32 v115, v155\n"
      "v_exp_f32 v112, v156\n"
      "v_cvt_pk_bf16_f32 v141, v114, v115\n"
      "v_exp_f32 v113, v157\n"
      "v_exp_f32 v114, v158\n"
      "v_cvt_pk_bf16_f32 v142, v112, v113\n"
      "v_exp_f32 v115, v159\n"
      "s_nop 0\n"
      "v_cvt_pk_bf16_f32 v143, v114, v115\n"
      "ds_read_b128 a[64:67], v242 offset:32768\n"
      "ds_read_b128 a[68:71], v242 offset:36864\n"
      "ds_read_b128 a[72:75], v243 offset:32768\n"
      "ds_read_b128 a[76:79], v243 offset:36864\n"
      "ds_read_b128 a[80:83], v242 offset:33792\n"
      "ds_read_b128 a[84:87], v242 offset:37888\n"
      "ds_read_b128 a[88:91], v243 offset:33792\n"
      "ds_read_b128 a[92:95], v243 offset:37888\n"
      "s_waitcnt lgkmcnt(0)\n"
      "v_mfma_f32_16x16x32_bf16 v[160:163], a[64:67], a[0:3], v[224:227]\n"
      "v_mfma_f32_16x16x32_bf16 v[168:171], a[64:67], a[4:7], v[228:231]\n"
      "v_mfma_f32_16x16x32_bf16 v[176:179], a[64:67], a[8:11], v[232:235]\n"
      "v_mfma_f32_16x16x32_bf16 v[184:187], a[64:67], a[12:15], v[236:239]\n"
      "v_mfma_f32_16x16x32_bf16 v[164:167], a[68:71], a[0:3], v[224:227]\n"
      "v_mfma_f32_16x16x32_bf16 v[172:175], a[68:71], a[4:7], v[228:231]\n"
      "v_mfma_f32_16x16x32_bf16 v[180:183], a[68:71], a[8:11], v[232:235]\n"
      "v_mfma_f32_16x16x32_bf16 v[188:191], a[68:71], a[12:15], v[236:239]\n"
      "v_mfma_f32_16x16x32_bf16 v[160:163], a[72:75], a[16:19], v[160:163]\n"
      "v_mfma_f32_16x16x32_bf16 v[168:171], a[72:75], a[20:23], v[168:171]\n"
      "v_mfma_f32_16x16x32_bf16 v[176:179], a[72:75], a[24:27], v[176:179]\n"
      "v_mfma_f32_16x16x32_bf16 v[184:187], a[72:75], a[28:31], v[184:187]\n"
      "v_mfma_f32_16x16x32_bf16 v[164:167], a[76:79], a[16:19], v[164:167]\n"
      "v_mfma_f32_16x16x32_bf16 v[172:175], a[76:79], a[20:23], v[172:175]\n"
      "v_mfma_f32_16x16x32_bf16 v[180:183], a[76:79], a[24:27], v[180:183]\n"
      "v_mfma_f32_16x16x32_bf16 v[188:191], a[76:79], a[28:31], v[188:191]\n"
      "v_mfma_f32_16x16x32_bf16 v[160:163], a[80:83], a[32:35], v[160:163]\n"
      "v_mfma_f32_16x16x32_bf16 v[168:171], a[80:83], a[36:39], v[168:171]\n"
      "v_mfma_f32_16x16x32_bf16 v[176:179], a[80:83], a[40:43], v[176:179]\n"
      "v_mfma_f32_16x16x32_bf16 v[184:187], a[80:83], a[44:47], v[184:187]\n"
      "v_mfma_f32_16x16x32_bf16 v[164:167], a[84:87], a[32:35], v[164:167]\n"
      "v_mfma_f32_16x16x32_bf16 v[172:175], a[84:87], a[36:39], v[172:175]\n"
      "v_mfma_f32_16x16x32_bf16 v[180:183], a[84:87], a[40:43], v[180:183]\n"
      "v_mfma_f32_16x16x32_bf16 v[188:191], a[84:87], a[44:47], v[188:191]\n"
      "v_mfma_f32_16x16x32_bf16 v[160:163], a[88:91], a[48:51], v[160:163]\n"
      "v_mfma_f32_16x16x32_bf16 v[168:171], a[88:91], a[52:55], v[168:171]\n"
      "v_mfma_f32_16x16x32_bf16 v[176:179], a[88:91], a[56:59], v[176:179]\n"
      "v_mfma_f32_16x16x32_bf16 v[184:187], a[88:91], a[60:63], v[184:187]\n"
      "v_mfma_f32_16x16x32_bf16 v[164:167], a[92:95], a[48:51], v[164:167]\n"
      "v_mfma_f32_16x16x32_bf16 v[172:175], a[92:95], a[52:55], v[172:175]\n"
      "v_mfma_f32_16x16x32_bf16 v[180:183], a[92:95], a[56:59], v[180:183]\n"
      "v_mfma_f32_16x16x32_bf16 v[188:191], a[92:95], a[60:63], v[188:191]\n"
      "s_cmp_eq_u32 s64, 0\n"
      "s_cbranch_scc0 NRET0_0_%=\n"
      "v_mov_b64 v[192:193], v[128:129]\n"
      "v_mov_b64 v[194:195], v[130:131]\n"
      "v_mov_b64 v[196:197], v[132:133]\n"
      "v_mov_b64 v[198:199], v[134:135]\n"
      "v_mov_b64 v[200:201], v[136:137]\n"
      "v_mov_b64 v[202:203], v[138:139]\n"
      "v_mov_b64 v[204:205], v[140:141]\n"
      "v_mov_b64 v[206:207], v[142:143]\n"
      "s_nop 1\n"
      "s_branch RET0_0_%=\n"
      "NRET0_0_%=:\n"
      "s_cmp_eq_u32 s64, 1\n"
      "s_cbranch_scc0 NRET0_1_%=\n"
      "v_mov_b64 v[192:193], v[128:129]\n"
      "v_mov_b64 v[194:195], v[130:131]\n"
      "v_mov_b64 v[196:197], v[132:133]\n"
      "v_mov_b64 v[198:199], v[134:135]\n"
      "v_mov_b64 v[200:201], v[136:137]\n"
      "v_mov_b64 v[202:203], v[138:139]\n"
      "v_mov_b64 v[204:205], v[140:141]\n"
      "v_mov_b64 v[206:207], v[142:143]\n"
      "s_nop 1\n"
      "s_branch RET0_1_%=\n"
      "NRET0_1_%=:\n"
      "s_cmp_eq_u32 s64, 2\n"
      "s_cbranch_scc0 NRET0_2_%=\n"
      "v_mov_b64 v[192:193], v[128:129]\n"
      "v_mov_b64 v[194:195], v[130:131]\n"
      "v_mov_b64 v[196:197], v[132:133]\n"
      "v_mov_b64 v[198:199], v[134:135]\n"
      "v_mov_b64 v[200:201], v[136:137]\n"
      "v_mov_b64 v[202:203], v[138:139]\n"
      "v_mov_b64 v[204:205], v[140:141]\n"
      "v_mov_b64 v[206:207], v[142:143]\n"
      "s_nop 1\n"
      "s_branch RET0_2_%=\n"
      "NRET0_2_%=:\n"
      "s_cmp_eq_u32 s64, 3\n"
      "s_cbranch_scc0 NRET0_3_%=\n"
      "v_mov_b64 v[192:193], v[128:129]\n"
      "v_mov_b64 v[194:195], v[130:131]\n"
      "v_mov_b64 v[196:197], v[132:133]\n"
      "v_mov_b64 v[198:199], v[134:135]\n"
      "v_mov_b64 v[200:201], v[136:137]\n"
      "v_mov_b64 v[202:203], v[138:139]\n"
      "v_mov_b64 v[204:205], v[140:141]\n"
      "v_mov_b64 v[206:207], v[142:143]\n"
      "s_nop 1\n"
      "s_branch RET0_3_%=\n"
      "NRET0_3_%=:\n"
      "s_cmp_eq_u32 s64, 4\n"
      "s_cbranch_scc0 NRET0_4_%=\n"
      "v_mov_b64 v[192:193], v[128:129]\n"
      "v_mov_b64 v[194:195], v[130:131]\n"
      "v_mov_b64 v[196:197], v[132:133]\n"
      "v_mov_b64 v[198:199], v[134:135]\n"
      "v_mov_b64 v[200:201], v[136:137]\n"
      "v_mov_b64 v[202:203], v[138:139]\n"
      "v_mov_b64 v[204:205], v[140:141]\n"
      "v_mov_b64 v[206:207], v[142:143]\n"
      "s_nop 1\n"
      "s_branch RET0_4_%=\n"
      "NRET0_4_%=:\n"
      "s_cmp_eq_u32 s64, 5\n"
      "s_cbranch_scc0 NRET0_5_%=\n"
      "v_mov_b64 v[16:17], v[128:129]\n"
      "v_mov_b64 v[18:19], v[130:131]\n"
      "v_mov_b64 v[20:21], v[132:133]\n"
      "v_mov_b64 v[22:23], v[134:135]\n"
      "v_mov_b64 v[24:25], v[136:137]\n"
      "v_mov_b64 v[26:27], v[138:139]\n"
      "v_mov_b64 v[28:29], v[140:141]\n"
      "v_mov_b64 v[30:31], v[142:143]\n"
      "s_nop 1\n"
      "s_branch RET0_5_%=\n"
      "NRET0_5_%=:\n"
      "FB1_%=:\n"
      "s_add_u32 s84, s63, 1\n"
      "s_sub_u32 s84, s84, %[n1]\n"
      "s_or_b32 s84, s84, s64\n"
      "s_cbranch_scc0 RET1_0_%=\n"
      "s_nop 7\n"
      "s_nop 7\n"
      "v_xor_b32 v118, 0x10000, v242\n"
      "v_xor_b32 v119, 0x10000, v243\n"
      "ds_read_b128 a[64:67], v118 offset:32768\n"
      "ds_read_b128 a[68:71], v118 offset:36864\n"
      "ds_read_b128 a[72:75], v119 offset:32768\n"
      "ds_read_b128 a[76:79], v119 offset:36864\n"
      "ds_read_b128 a[80:83], v118 offset:33792\n"
      "ds_read_b128 a[84:87], v118 offset:37888\n"
      "ds_read_b128 a[88:91], v119 offset:33792\n"
      "ds_read_b128 a[92:95], v119 offset:37888\n"
      "s_waitcnt lgkmcnt(0)\n"
      "v_mfma_f32_16x16x32_bf16 v[160:163], a[64:67], a[0:3], 0\n"
      "v_mfma_f32_16x16x32_bf16 v[168:171], a[64:67], a[4:7], 0\n"
      "v_mfma_f32_16x16x32_bf16 v[176:179], a[64:67], a[8:11], 0\n"
      "v_mfma_f32_16x16x32_bf16 v[184:187], a[64:67], a[12:15], 0\n"
      "v_mfma_f32_16x16x32_bf16 v[164:167], a[68:71], a[0:3], 0\n"
      "v_mfma_f32_16x16x32_bf16 v[172:175], a[68:71], a[4:7], 0\n"
      "v_mfma_f32_16x16x32_bf16 v[180:183], a[68:71], a[8:11], 0\n"
      "v_mfma_f32_16x16x32_bf16 v[188:191], a[68:71], a[12:15], 0\n"
      "v_mfma_f32_16x16x32_bf16 v[160:163], a[72:75], a[16:19], v[160:163]\n"
      "v_mfma_f32_16x16x32_bf16 v[168:171], a[72:75], a[20:23], v[168:171]\n"
      "v_mfma_f32_16x16x32_bf16 v[176:179], a[72:75], a[24:27], v[176:179]\n"
      "v_mfma_f32_16x16x32_bf16 v[184:187], a[72:75], a[28:31], v[184:187]\n"
      "v_mfma_f32_16x16x32_bf16 v[164:167], a[76:79], a[16:19], v[164:167]\n"
      "v_mfma_f32_16x16x32_bf16 v[172:175], a[76:79], a[20:23], v[172:175]\n"
      "v_mfma_f32_16x16x32_bf16 v[180:183], a[76:79], a[24:27], v[180:183]\n"
      "v_mfma_f32_16x16x32_bf16 v[188:191], a[76:79], a[28:31], v[188:191]\n"
      "v_mfma_f32_16x16x32_bf16 v[160:163], a[80:83], a[32:35], v[160:163]\n"
      "v_mfma_f32_16x16x32_bf16 v[168:171], a[80:83], a[36:39], v[168:171]\n"
      "v_mfma_f32_16x16x32_bf16 v[176:179], a[80:83], a[40:43], v[176:179]\n"
      "v_mfma_f32_16x16x32_bf16 v[184:187], a[80:83], a[44:47], v[184:187]\n"
      "v_mfma_f32_16x16x32_bf16 v[164:167], a[84:87], a[32:35], v[164:167]\n"
      "v_mfma_f32_16x16x32_bf16 v[172:175], a[84:87], a[36:39], v[172:175]\n"
      "v_mfma_f32_16x16x32_bf16 v[180:183], a[84:87], a[40:43], v[180:183]\n"
      "v_mfma_f32_16x16x32_bf16 v[188:191], a[84:87], a[44:47], v[188:191]\n"
      "v_mfma_f32_16x16x32_bf16 v[160:163], a[88:91], a[48:51], v[160:163]\n"
      "v_mfma_f32_16x16x32_bf16 v[168:171], a[88:91], a[52:55], v[168:171]\n"
      "v_mfma_f32_16x16x32_bf16 v[176:179], a[88:91], a[56:59], v[176:179]\n"
      "v_mfma_f32_16x16x32_bf16 v[184:187], a[88:91], a[60:63], v[184:187]\n"
      "v_mfma_f32_16x16x32_bf16 v[164:167], a[92:95], a[48:51], v[164:167]\n"
      "v_mfma_f32_16x16x32_bf16 v[172:175], a[92:95], a[52:55], v[172:175]\n"
      "v_mfma_f32_16x16x32_bf16 v[180:183], a[92:95], a[56:59], v[180:183]\n"
      "v_mfma_f32_16x16x32_bf16 v[188:191], a[92:95], a[60:63], v[188:191]\n"
      "s_nop 7\n"
      "v_max3_f32 v112, v160, v161, v162\n"
      "v_max3_f32 v113, v168, v169, v170\n"
      "v_max3_f32 v114, v176, v177, v178\n"
      "v_max3_f32 v115, v184, v185, v186\n"
      "v_max3_f32 v112, v112, v163, v164\n"
      "v_max3_f32 v113, v113, v171, v172\n"
      "v_max3_f32 v114, v114, v179, v180\n"
      "v_max3_f32 v115, v115, v187, v188\n"
      "v_max3_f32 v112, v112, v165, v166\n"
      "v_max3_f32 v113, v113, v173, v174\n"
      "v_max3_f32 v114, v114, v181, v182\n"
      "v_max3_f32 v115, v115, v189, v190\n"
      "v_max_f32 v112, v112, v167\n"
      "v_max_f32 v113, v113, v175\n"
      "v_max_f32 v114, v114, v183\n"
      "v_max_f32 v115, v115, v191\n"
      "v_mov_b32 v128, v112\n"
      "v_mov_b32 v129, v113\n"
      "v_mov_b32 v130, v114\n"
      "v_mov_b32 v131, v115\n"
      "v_permlane16_swap_b32 v112, v128\n"
      "v_permlane16_swap_b32 v113, v129\n"
      "v_permlane16_swap_b32 v114, v130\n"
      "v_permlane16_swap_b32 v115, v131\n"
      "v_max_f32 v112, v112, v128\n"
      "v_max_f32 v113, v113, v129\n"
      "v_max_f32 v114, v114, v130\n"
      "v_max_f32 v115, v115, v131\n"
      "v_mov_b32 v128, v112\n"
      "v_mov_b32 v129, v113\n"
      "v_mov_b32 v130, v114\n"
      "v_mov_b32 v131, v115\n"
      "v_permlane32_swap_b32 v112, v128\n"
      "v_permlane32_swap_b32 v113, v129\n"
      "v_permlane32_swap_b32 v114, v130\n"
      "v_permlane32_swap_b32 v115, v131\n"
      "v_max_f32 v112, v112, v128\n"
      "v_max_f32 v113, v113, v129\n"
      "v_max_f32 v114, v114, v130\n"
      "v_max_f32 v115, v115, v131\n"
      "v_sub_f32 v112, 0xc1800000, v112\n"
      "v_sub_f32 v113, 0xc1800000, v113\n"
      "v_sub_f32 v114, 0xc1800000, v114\n"
      "v_sub_f32 v115, 0xc1800000, v115\n"
      "v_floor_f32 v112, v112\n"
      "v_floor_f32 v113, v113\n"
      "v_floor_f32 v114, v114\n"
      "v_floor_f32 v115, v115\n"
      "v_min_f32 v112, v112, v224\n"
      "v_min_f32 v113, v113, v228\n"
      "v_min_f32 v114, v114, v232\n"
      "v_min_f32 v115, v115, v236\n"
      "v_sub_f32 v132, v112, v224\n"
      "v_sub_f32 v133, v113, v228\n"
      "v_sub_f32 v134, v114, v232\n"
      "v_sub_f32 v135, v115, v236\n"
      "v_mov_b32 v224, v112\n"
      "v_mov_b32 v228, v113\n"
      "v_mov_b32 v232, v114\n"
      "v_mov_b32 v236, v115\n"
      "v_exp_f32 v112, v132\n"
      "v_exp_f32 v113, v133\n"
      "v_exp_f32 v114, v134\n"
      "v_exp_f32 v115, v135\n"
      "v_mov_b32 v225, v224\n"
      "v_mov_b32 v229, v228\n"
      "v_mov_b32 v233, v232\n"
      "v_mov_b32 v237, v236\n"
      "v_mov_b64 v[226:227], v[224:225]\n"
      "v_mov_b64 v[230:231], v[228:229]\n"
      "v_mov_b64 v[234:235], v[232:233]\n"
      "v_mov_b64 v[238:239], v[236:237]\n"
      "s_call_b64 s[84:85], SCALE_%=\n"
      "v_pk_add_f32 v[160:161], v[160:161], v[224:225]\n"
      "v_pk_add_f32 v[162:163], v[162:163], v[224:225]\n"
      "v_pk_add_f32 v[164:165], v[164:165], v[224:225]\n"
      "v_pk_add_f32 v[166:167], v[166:167], v[224:225]\n"
      "v_pk_add_f32 v[168:169], v[168:169], v[228:229]\n"
      "v_pk_add_f32 v[170:171], v[170:171], v[228:229]\n"
      "v_pk_add_f32 v[172:173], v[172:173], v[228:229]\n"
      "v_pk_add_f32 v[174:175], v[174:175], v[228:229]\n"
      "v_pk_add_f32 v[176:177], v[176:177], v[232:233]\n"
      "v_pk_add_f32 v[178:179], v[178:179], v[232:233]\n"
      "v_pk_add_f32 v[180:181], v[180:181], v[232:233]\n"
      "v_pk_add_f32 v[182:183], v[182:183], v[232:233]\n"
      "v_pk_add_f32 v[184:185], v[184:185], v[236:237]\n"
      "v_pk_add_f32 v[186:187], v[186:187], v[236:237]\n"
      "v_pk_add_f32 v[188:189], v[188:189], v[236:237]\n"
      "v_pk_add_f32 v[190:191], v[190:191], v[236:237]\n"
      "v_exp_f32 v112, v160\n"
      "v_exp_f32 v113, v161\n"
      "v_exp_f32 v114, v162\n"
      "v_cvt_pk_bf16_f32 v160, v112, v113\n"
      "v_exp_f32 v115, v163\n"
      "v_exp_f32 v112, v164\n"
      "v_cvt_pk_bf16_f32 v161, v114, v115\n"
      "v_exp_f32 v113, v165\n"
      "v_exp_f32 v114, v166\n"
      "v_cvt_pk_bf16_f32 v162, v112, v113\n"
      "v_exp_f32 v115, v167\n"
      "v_exp_f32 v112, v168\n"
      "v_cvt_pk_bf16_f32 v163, v114, v115\n"
      "v_exp_f32 v113, v169\n"
      "v_exp_f32 v114, v170\n"
      "v_cvt_pk_bf16_f32 v164, v112, v113\n"
      "v_exp_f32 v115, v171\n"
      "v_exp_f32 v112, v172\n"
      "v_cvt_pk_bf16_f32 v165, v114, v115\n"
      "v_exp_f32 v113, v173\n"
      "v_exp_f32 v114, v174\n"
      "v_cvt_pk_bf16_f32 v166, v112, v113\n"
      "v_exp_f32 v115, v175\n"
      "v_exp_f32 v112, v176\n"
      "v_cvt_pk_bf16_f32 v167, v114, v115\n"
      "v_exp_f32 v113, v177\n"
      "v_exp_f32 v114, v178\n"
      "v_cvt_pk_bf16_f32 v168, v112, v113\n"
      "v_exp_f32 v115, v179\n"
      "v_exp_f32 v112, v180\n"
      "v_cvt_pk_bf16_f32 v169, v114, v115\n"
      "v_exp_f32 v113, v181\n"
      "v_exp_f32 v114, v182\n"
      "v_cvt_pk_bf16_f32 v170, v112, v113\n"
      "v_exp_f32 v115, v183\n"
      "v_exp_f32 v112, v184\n"
      "v_cvt_pk_bf16_f32 v171, v114, v115\n"
      "v_exp_f32 v113, v185\n"
      "v_exp_f32 v114, v186\n"
      "v_cvt_pk_bf16_f32 v172, v112, v113\n"
      "v_exp_f32 v115, v187\n"
      "v_exp_f32 v112, v188\n"
      "v_cvt_pk_bf16_f32 v173, v114, v115\n"
      "v_exp_f32 v113, v189\n"
      "v_exp_f32 v114, v190\n"
      "v_cvt_pk_bf16_f32 v174, v112, v113\n"
      "v_exp_f32 v115, v191\n"
      "s_nop 0\n"
      "v_cvt_pk_bf16_f32 v175, v114, v115\n"
      "ds_read_b128 a[64:67], v242 offset:0\n"
      "ds_read_b128 a[68:71], v242 offset:4096\n"
      "ds_read_b128 a[72:75], v243 offset:0\n"
      "ds_read_b128 a[76:79], v243 offset:4096\n"
      "ds_read_b128 a[80:83], v242 offset:1024\n"
      "ds_read_b128 a[84:87], v242 offset:5120\n"
      "ds_read_b128 a[88:91], v243 offset:1024\n"
      "ds_read_b128 a[92:95], v243 offset:5120\n"
      "s_waitcnt lgkmcnt(0)\n"
      "v_mfma_f32_16x16x32_bf16 v[128:131], a[64:67], a[0:3], v[224:227]\n"
      "v_mfma_f32_16x16x32_bf16 v[136:139], a[64:67], a[4:7], v[228:231]\n"
      "v_mfma_f32_16x16x32_bf16 v[144:147], a[64:67], a[8:11], v[232:235]\n"
      "v_mfma_f32_16x16x32_bf16 v[152:155], a[64:67], a[12:15], v[236:239]\n"
      "v_mfma_f32_16x16x32_bf16 v[132:135], a[68:71], a[0:3], v[224:227]\n"
      "v_mfma_f32_16x16x32_bf16 v[140:143], a[68:71], a[4:7], v[228:231]\n"
      "v_mfma_f32_16x16x32_bf16 v[148:151], a[68:71], a[8:11], v[232:235]\n"
      "v_mfma_f32_16x16x32_bf16 v[156:159], a[68:71], a[12:15], v[236:239]\n"
      "v_mfma_f32_16x16x32_bf16 v[128:131], a[72:75], a[16:19], v[128:131]\n"
      "v_mfma_f32_16x16x32_bf16 v[136:139], a[72:75], a[20:23], v[136:139]\n"
      "v_mfma_f32_16x16x32_bf16 v[144:147], a[72:75], a[24:27], v[144:147]\n"
      "v_mfma_f32_16x16x32_bf16 v[152:155], a[72:75], a[28:31], v[152:155]\n"
      "v_mfma_f32_16x16x32_bf16 v[132:135], a[76:79], a[16:19], v[132:135]\n"
      "v_mfma_f32_16x16x32_bf16 v[140:143], a[76:79], a[20:23], v[140:143]\n"
      "v_mfma_f32_16x16x32_bf16 v[148:151], a[76:79], a[24:27], v[148:151]\n"
      "v_mfma_f32_16x16x32_bf16 v[156:159], a[76:79], a[28:31], v[156:159]\n"
      "v_mfma_f32_16x16x32_bf16 v[128:131], a[80:83], a[32:35], v[128:131]\n"
      "v_mfma_f32_16x16x32_bf16 v[136:139], a[80:83], a[36:39], v[136:139]\n"
      "v_mfma_f32_16x16x32_bf16 v[144:147], a[80:83], a[40:43], v[144:147]\n"
      "v_mfma_f32_16x16x32_bf16 v[152:155], a[80:83], a[44:47], v[152:155]\n"
      "v_mfma_f32_16x16x32_bf16 v[132:135], a[84:87], a[32:35], v[132:135]\n"
      "v_mfma_f32_16x16x32_bf16 v[140:143], a[84:87], a[36:39], v[140:143]\n"
      "v_mfma_f32_16x16x32_bf16 v[148:151], a[84:87], a[40:43], v[148:151]\n"
      "v_mfma_f32_16x16x32_bf16 v[156:159], a[84:87], a[44:47], v[156:159]\n"
      "v_mfma_f32_16x16x32_bf16 v[128:131], a[88:91], a[48:51], v[128:131]\n"
      "v_mfma_f32_16x16x32_bf16 v[136:139], a[88:91], a[52:55], v[136:139]\n"
      "v_mfma_f32_16x16x32_bf16 v[144:147], a[88:91], a[56:59], v[144:147]\n"
      "v_mfma_f32_16x16x32_bf16 v[152:155], a[88:91], a[60:63], v[152:155]\n"
      "v_mfma_f32_16x16x32_bf16 v[132:135], a[92:95], a[48:51], v[132:135]\n"
      "v_mfma_f32_16x16x32_bf16 v[140:143], a[92:95], a[52:55], v[140:143]\n"
      "v_mfma_f32_16x16x32_bf16 v[148:151], a[92:95], a[56:59], v[148:151]\n"
      "v_mfma_f32_16x16x32_bf16 v[156:159], a[92:95], a[60:63], v[156:159]\n"
      "s_cmp_eq_u32 s64, 0\n"
      "s_cbranch_scc0 NRET1_0_%=\n"
      "v_mov_b64 v[208:209], v[160:161]\n"
      "v_mov_b64 v[210:211], v[162:163]\n"
      "v_mov_b64 v[212:213], v[164:165]\n"
      "v_mov_b64 v[214:215], v[166:167]\n"
      "v_mov_b64 v[216:217], v[168:169]\n"
      "v_mov_b64 v[218:219], v[170:171]\n"
      "v_mov_b64 v[220:221], v[172:173]\n"
      "v_mov_b64 v[222:223], v[174:175]\n"
      "s_nop 1\n"
      "s_branch RET1_0_%=\n"
      "NRET1_0_%=:\n"
      "s_cmp_eq_u32 s64, 1\n"
      "s_cbranch_scc0 NRET1_1_%=\n"
      "v_mov_b64 v[208:209], v[160:161]\n"
      "v_mov_b64 v[210:211], v[162:163]\n"
      "v_mov_b64 v[212:213], v[164:165]\n"
      "v_mov_b64 v[214:215], v[166:167]\n"
      "v_mov_b64 v[216:217], v[168:169]\n"
      "v_mov_b64 v[218:219], v[170:171]\n"
      "v_mov_b64 v[220:221], v[172:173]\n"
      "v_mov_b64 v[222:223], v[174:175]\n"
      "s_nop 1\n"
      "s_branch RET1_1_%=\n"
      "NRET1_1_%=:\n"
      "s_cmp_eq_u32 s64, 2\n"
      "s_cbranch_scc0 NRET1_2_%=\n"
      "v_mov_b64 v[208:209], v[160:161]\n"
      "v_mov_b64 v[210:211], v[162:163]\n"
      "v_mov_b64 v[212:213], v[164:165]\n"
      "v_mov_b64 v[214:215], v[166:167]\n"
      "v_mov_b64 v[216:217], v[168:169]\n"
      "v_mov_b64 v[218:219], v[170:171]\n"
      "v_mov_b64 v[220:221], v[172:173]\n"
      "v_mov_b64 v[222:223], v[174:175]\n"
      "s_nop 1\n"
      "s_branch RET1_2_%=\n"
      "NRET1_2_%=:\n"
      "s_cmp_eq_u32 s64, 3\n"
      "s_cbranch_scc0 NRET1_3_%=\n"
      "v_mov_b64 v[208:209], v[160:161]\n"
      "v_mov_b64 v[210:211], v[162:163]\n"
      "v_mov_b64 v[212:213], v[164:165]\n"
      "v_mov_b64 v[214:215], v[166:167]\n"
      "v_mov_b64 v[216:217], v[168:169]\n"
      "v_mov_b64 v[218:219], v[170:171]\n"
      "v_mov_b64 v[220:221], v[172:173]\n"
      "v_mov_b64 v[222:223], v[174:175]\n"
      "s_nop 1\n"
      "s_branch RET1_3_%=\n"
      "NRET1_3_%=:\n"
      "s_cmp_eq_u32 s64, 4\n"
      "s_cbranch_scc0 NRET1_4_%=\n"
      "v_mov_b64 v[208:209], v[160:161]\n"
      "v_mov_b64 v[210:211], v[162:163]\n"
      "v_mov_b64 v[212:213], v[164:165]\n"
      "v_mov_b64 v[214:215], v[166:167]\n"
      "v_mov_b64 v[216:217], v[168:169]\n"
      "v_mov_b64 v[218:219], v[170:171]\n"
      "v_mov_b64 v[220:221], v[172:173]\n"
      "v_mov_b64 v[222:223], v[174:175]\n"
      "s_nop 1\n"
      "s_branch RET1_4_%=\n"
      "NRET1_4_%=:\n"
      "s_cmp_eq_u32 s64, 5\n"
      "s_cbranch_scc0 NRET1_5_%=\n"
      "v_mov_b64 v[0:1], v[160:161]\n"
      "v_mov_b64 v[2:3], v[162:163]\n"
      "v_mov_b64 v[4:5], v[164:165]\n"
      "v_mov_b64 v[6:7], v[166:167]\n"
      "v_mov_b64 v[8:9], v[168:169]\n"
      "v_mov_b64 v[10:11], v[170:171]\n"
      "v_mov_b64 v[12:13], v[172:173]\n"
      "v_mov_b64 v[14:15], v[174:175]\n"
      "s_nop 1\n"
      "s_branch RET1_5_%=\n"
      "NRET1_5_%=:\n"
      "s_cmp_eq_u32 s64, 6\n"
      "s_cbranch_scc0 NRET1_6_%=\n"
      "v_mov_b64 v[96:97], v[160:161]\n"
      "v_mov_b64 v[98:99], v[162:163]\n"
      "v_mov_b64 v[100:101], v[164:165]\n"
      "v_mov_b64 v[102:103], v[166:167]\n"
      "v_mov_b64 v[104:105], v[168:169]\n"
      "v_mov_b64 v[106:107], v[170:171]\n"
      "v_mov_b64 v[108:109], v[172:173]\n"
      "v_mov_b64 v[110:111], v[174:175]\n"
      "v_mov_b32 v117, v224\n"
      "s_nop 1\n"
      "v_mov_b32_dpp v117, v228 quad_perm:[0,1,2,3] row_mask:0x2 bank_mask:0xf\n"
      "s_nop 1\n"
      "v_mov_b32_dpp v117, v232 quad_perm:[0,1,2,3] row_mask:0x4 bank_mask:0xf\n"
      "s_nop 1\n"
      "v_mov_b32_dpp v117, v236 quad_perm:[0,1,2,3] row_mask:0x8 bank_mask:0xf\n"
      "v_mfma_f32_16x16x32_bf16 a[228:231], a[196:199], v[96:99], a[192:195]\n"
      "v_mfma_f32_16x16x32_bf16 a[228:231], a[200:203], v[100:103], a[228:231]\n"
      "v_mfma_f32_16x16x32_bf16 a[228:231], a[204:207], v[104:107], a[228:231]\n"
      "ds_write_b128 v125, a[160:163] offset:0\n"
      "ds_write_b128 v125, a[164:167] offset:1024\n"
      "ds_write_b128 v125, a[168:171] offset:2048\n"
      "ds_write_b128 v125, a[172:175] offset:3072\n"
      "ds_write_b128 v125, a[176:179] offset:4096\n"
      "ds_write_b128 v125, a[180:183] offset:5120\n"
      "ds_write_b128 v125, a[184:187] offset:6144\n"
      "ds_write_b128 v125, a[188:191] offset:7168\n"
      "ds_write_b128 v125, v[96:99] offset:8192\n"
      "ds_write_b128 v125, v[100:103] offset:9216\n"
      "ds_write_b128 v125, v[104:107] offset:10240\n"
      "ds_write_b32 v125, v117 offset:12292\n"
      "s_nop 1\n"
      "s_branch RET1_6_%=\n"
      "NRET1_6_%=:\n"
      "SCALE_%=:\n"
      "v_pk_mul_f32 v[32:33], v[32:33], v[112:113] op_sel:[0,0] op_sel_hi:[1,0]\n"
      "v_pk_mul_f32 v[34:35], v[34:35], v[112:113] op_sel:[0,0] op_sel_hi:[1,0]\n"
      "v_pk_mul_f32 v[36:37], v[36:37], v[112:113] op_sel:[0,1] op_sel_hi:[1,1]\n"
      "v_pk_mul_f32 v[38:39], v[38:39], v[112:113] op_sel:[0,1] op_sel_hi:[1,1]\n"
      "v_pk_mul_f32 v[40:41], v[40:41], v[114:115] op_sel:[0,0] op_sel_hi:[1,0]\n"
      "v_pk_mul_f32 v[42:43], v[42:43], v[114:115] op_sel:[0,0] op_sel_hi:[1,0]\n"
      "v_pk_mul_f32 v[44:45], v[44:45], v[114:115] op_sel:[0,1] op_sel_hi:[1,1]\n"
      "v_pk_mul_f32 v[46:47], v[46:47], v[114:115] op_sel:[0,1] op_sel_hi:[1,1]\n"
      "v_pk_mul_f32 v[48:49], v[48:49], v[112:113] op_sel:[0,0] op_sel_hi:[1,0]\n"
      "v_pk_mul_f32 v[50:51], v[50:51], v[112:113] op_sel:[0,0] op_sel_hi:[1,0]\n"
      "v_pk_mul_f32 v[52:53], v[52:53], v[112:113] op_sel:[0,1] op_sel_hi:[1,1]\n"
      "v_pk_mul_f32 v[54:55], v[54:55], v[112:113] op_sel:[0,1] op_sel_hi:[1,1]\n"
      "v_pk_mul_f32 v[56:57], v[56:57], v[114:115] op_sel:[0,0] op_sel_hi:[1,0]\n"
      "v_pk_mul_f32 v[58:59], v[58:59], v[114:115] op_sel:[0,0] op_sel_hi:[1,0]\n"
      "v_pk_mul_f32 v[60:61], v[60:61], v[114:115] op_sel:[0,1] op_sel_hi:[1,1]\n"
      "v_pk_mul_f32 v[62:63], v[62:63], v[114:115] op_sel:[0,1] op_sel_hi:[1,1]\n"
      "v_pk_mul_f32 v[64:65], v[64:65], v[112:113] op_sel:[0,0] op_sel_hi:[1,0]\n"
      "v_pk_mul_f32 v[66:67], v[66:67], v[112:113] op_sel:[0,0] op_sel_hi:[1,0]\n"
      "v_pk_mul_f32 v[68:69], v[68:69], v[112:113] op_sel:[0,1] op_sel_hi:[1,1]\n"
      "v_pk_mul_f32 v[70:71], v[70:71], v[112:113] op_sel:[0,1] op_sel_hi:[1,1]\n"
      "v_pk_mul_f32 v[72:73], v[72:73], v[114:115] op_sel:[0,0] op_sel_hi:[1,0]\n"
      "v_pk_mul_f32 v[74:75], v[74:75], v[114:115] op_sel:[0,0] op_sel_hi:[1,0]\n"
      "v_pk_mul_f32 v[76:77], v[76:77], v[114:115] op_sel:[0,1] op_sel_hi:[1,1]\n"
      "v_pk_mul_f32 v[78:79], v[78:79], v[114:115] op_sel:[0,1] op_sel_hi:[1,1]\n"
      "v_pk_mul_f32 v[80:81], v[80:81], v[112:113] op_sel:[0,0] op_sel_hi:[1,0]\n"
      "v_pk_mul_f32 v[82:83], v[82:83], v[112:113] op_sel:[0,0] op_sel_hi:[1,0]\n"
      "v_pk_mul_f32 v[84:85], v[84:85], v[112:113] op_sel:[0,1] op_sel_hi:[1,1]\n"
      "v_pk_mul_f32 v[86:87], v[86:87], v[112:113] op_sel:[0,1] op_sel_hi:[1,1]\n"
      "v_pk_mul_f32 v[88:89], v[88:89], v[114:115] op_sel:[0,0] op_sel_hi:[1,0]\n"
      "v_pk_mul_f32 v[90:91], v[90:91], v[114:115] op_sel:[0,0] op_sel_hi:[1,0]\n"
      "v_pk_mul_f32 v[92:93], v[92:93], v[114:115] op_sel:[0,1] op_sel_hi:[1,1]\n"
      "v_pk_mul_f32 v[94:95], v[94:95], v[114:115] op_sel:[0,1] op_sel_hi:[1,1]\n"
      "v_accvgpr_read_b32 v118, a128\n"
      "v_accvgpr_read_b32 v119, a129\n"
      "v_pk_mul_f32 v[118:119], v[118:119], v[112:113] op_sel:[0,0] op_sel_hi:[1,0]\n"
      "v_accvgpr_write_b32 a128, v118\n"
      "v_accvgpr_write_b32 a129, v119\n"
      "v_accvgpr_read_b32 v118, a130\n"
      "v_accvgpr_read_b32 v119, a131\n"
      "v_pk_mul_f32 v[118:119], v[118:119], v[112:113] op_sel:[0,0] op_sel_hi:[1,0]\n"
      "v_accvgpr_write_b32 a130, v118\n"
      "v_accvgpr_write_b32 a131, v119\n"
      "v_accvgpr_read_b32 v118, a132\n"
      "v_accvgpr_read_b32 v119, a133\n"
      "v_pk_mul_f32 v[118:119], v[118:119], v[112:113] op_sel:[0,1] op_sel_hi:[1,1]\n"
      "v_accvgpr_write_b32 a132, v118\n"
      "v_accvgpr_write_b32 a133, v119\n"
      "v_accvgpr_read_b32 v118, a134\n"
      "v_accvgpr_read_b32 v119, a135\n"
      "v_pk_mul_f32 v[118:119], v[118:119], v[112:113] op_sel:[0,1] op_sel_hi:[1,1]\n"
      "v_accvgpr_write_b32 a134, v118\n"
      "v_accvgpr_write_b32 a135, v119\n"
      "v_accvgpr_read_b32 v118, a136\n"
      "v_accvgpr_read_b32 v119, a137\n"
      "v_pk_mul_f32 v[118:119], v[118:119], v[114:115] op_sel:[0,0] op_sel_hi:[1,0]\n"
      "v_accvgpr_write_b32 a136, v118\n"
      "v_accvgpr_write_b32 a137, v119\n"
      "v_accvgpr_read_b32 v118, a138\n"
      "v_accvgpr_read_b32 v119, a139\n"
      "v_pk_mul_f32 v[118:119], v[118:119], v[114:115] op_sel:[0,0] op_sel_hi:[1,0]\n"
      "v_accvgpr_write_b32 a138, v118\n"
      "v_accvgpr_write_b32 a139, v119\n"
      "v_accvgpr_read_b32 v118, a140\n"
      "v_accvgpr_read_b32 v119, a141\n"
      "v_pk_mul_f32 v[118:119], v[118:119], v[114:115] op_sel:[0,1] op_sel_hi:[1,1]\n"
      "v_accvgpr_write_b32 a140, v118\n"
      "v_accvgpr_write_b32 a141, v119\n"
      "v_accvgpr_read_b32 v118, a142\n"
      "v_accvgpr_read_b32 v119, a143\n"
      "v_pk_mul_f32 v[118:119], v[118:119], v[114:115] op_sel:[0,1] op_sel_hi:[1,1]\n"
      "v_accvgpr_write_b32 a142, v118\n"
      "v_accvgpr_write_b32 a143, v119\n"
      "v_accvgpr_read_b32 v118, a144\n"
      "v_accvgpr_read_b32 v119, a145\n"
      "v_pk_mul_f32 v[118:119], v[118:119], v[112:113] op_sel:[0,0] op_sel_hi:[1,0]\n"
      "v_accvgpr_write_b32 a144, v118\n"
      "v_accvgpr_write_b32 a145, v119\n"
      "v_accvgpr_read_b32 v118, a146\n"
      "v_accvgpr_read_b32 v119, a147\n"
      "v_pk_mul_f32 v[118:119], v[118:119], v[112:113] op_sel:[0,0] op_sel_hi:[1,0]\n"
      "v_accvgpr_write_b32 a146, v118\n"
      "v_accvgpr_write_b32 a147, v119\n"
      "v_accvgpr_read_b32 v118, a148\n"
      "v_accvgpr_read_b32 v119, a149\n"
      "v_pk_mul_f32 v[118:119], v[118:119], v[112:113] op_sel:[0,1] op_sel_hi:[1,1]\n"
      "v_accvgpr_write_b32 a148, v118\n"
      "v_accvgpr_write_b32 a149, v119\n"
      "v_accvgpr_read_b32 v118, a150\n"
      "v_accvgpr_read_b32 v119, a151\n"
      "v_pk_mul_f32 v[118:119], v[118:119], v[112:113] op_sel:[0,1] op_sel_hi:[1,1]\n"
      "v_accvgpr_write_b32 a150, v118\n"
      "v_accvgpr_write_b32 a151, v119\n"
      "v_accvgpr_read_b32 v118, a152\n"
      "v_accvgpr_read_b32 v119, a153\n"
      "v_pk_mul_f32 v[118:119], v[118:119], v[114:115] op_sel:[0,0] op_sel_hi:[1,0]\n"
      "v_accvgpr_write_b32 a152, v118\n"
      "v_accvgpr_write_b32 a153, v119\n"
      "v_accvgpr_read_b32 v118, a154\n"
      "v_accvgpr_read_b32 v119, a155\n"
      "v_pk_mul_f32 v[118:119], v[118:119], v[114:115] op_sel:[0,0] op_sel_hi:[1,0]\n"
      "v_accvgpr_write_b32 a154, v118\n"
      "v_accvgpr_write_b32 a155, v119\n"
      "v_accvgpr_read_b32 v118, a156\n"
      "v_accvgpr_read_b32 v119, a157\n"
      "v_pk_mul_f32 v[118:119], v[118:119], v[114:115] op_sel:[0,1] op_sel_hi:[1,1]\n"
      "v_accvgpr_write_b32 a156, v118\n"
      "v_accvgpr_write_b32 a157, v119\n"
      "v_accvgpr_read_b32 v118, a158\n"
      "v_accvgpr_read_b32 v119, a159\n"
      "v_pk_mul_f32 v[118:119], v[118:119], v[114:115] op_sel:[0,1] op_sel_hi:[1,1]\n"
      "v_accvgpr_write_b32 a158, v118\n"
      "v_accvgpr_write_b32 a159, v119\n"
      "v_accvgpr_read_b32 v118, a160\n"
      "v_accvgpr_read_b32 v119, a161\n"
      "v_pk_mul_f32 v[118:119], v[118:119], v[112:113] op_sel:[0,0] op_sel_hi:[1,0]\n"
      "v_accvgpr_write_b32 a160, v118\n"
      "v_accvgpr_write_b32 a161, v119\n"
      "v_accvgpr_read_b32 v118, a162\n"
      "v_accvgpr_read_b32 v119, a163\n"
      "v_pk_mul_f32 v[118:119], v[118:119], v[112:113] op_sel:[0,0] op_sel_hi:[1,0]\n"
      "v_accvgpr_write_b32 a162, v118\n"
      "v_accvgpr_write_b32 a163, v119\n"
      "v_accvgpr_read_b32 v118, a164\n"
      "v_accvgpr_read_b32 v119, a165\n"
      "v_pk_mul_f32 v[118:119], v[118:119], v[112:113] op_sel:[0,1] op_sel_hi:[1,1]\n"
      "v_accvgpr_write_b32 a164, v118\n"
      "v_accvgpr_write_b32 a165, v119\n"
      "v_accvgpr_read_b32 v118, a166\n"
      "v_accvgpr_read_b32 v119, a167\n"
      "v_pk_mul_f32 v[118:119], v[118:119], v[112:113] op_sel:[0,1] op_sel_hi:[1,1]\n"
      "v_accvgpr_write_b32 a166, v118\n"
      "v_accvgpr_write_b32 a167, v119\n"
      "v_accvgpr_read_b32 v118, a168\n"
      "v_accvgpr_read_b32 v119, a169\n"
      "v_pk_mul_f32 v[118:119], v[118:119], v[114:115] op_sel:[0,0] op_sel_hi:[1,0]\n"
      "v_accvgpr_write_b32 a168, v118\n"
      "v_accvgpr_write_b32 a169, v119\n"
      "v_accvgpr_read_b32 v118, a170\n"
      "v_accvgpr_read_b32 v119, a171\n"
      "v_pk_mul_f32 v[118:119], v[118:119], v[114:115] op_sel:[0,0] op_sel_hi:[1,0]\n"
      "v_accvgpr_write_b32 a170, v118\n"
      "v_accvgpr_write_b32 a171, v119\n"
      "v_accvgpr_read_b32 v118, a172\n"
      "v_accvgpr_read_b32 v119, a173\n"
      "v_pk_mul_f32 v[118:119], v[118:119], v[114:115] op_sel:[0,1] op_sel_hi:[1,1]\n"
      "v_accvgpr_write_b32 a172, v118\n"
      "v_accvgpr_write_b32 a173, v119\n"
      "v_accvgpr_read_b32 v118, a174\n"
      "v_accvgpr_read_b32 v119, a175\n"
      "v_pk_mul_f32 v[118:119], v[118:119], v[114:115] op_sel:[0,1] op_sel_hi:[1,1]\n"
      "v_accvgpr_write_b32 a174, v118\n"
      "v_accvgpr_write_b32 a175, v119\n"
      "v_accvgpr_read_b32 v118, a176\n"
      "v_accvgpr_read_b32 v119, a177\n"
      "v_pk_mul_f32 v[118:119], v[118:119], v[112:113] op_sel:[0,0] op_sel_hi:[1,0]\n"
      "v_accvgpr_write_b32 a176, v118\n"
      "v_accvgpr_write_b32 a177, v119\n"
      "v_accvgpr_read_b32 v118, a178\n"
      "v_accvgpr_read_b32 v119, a179\n"
      "v_pk_mul_f32 v[118:119], v[118:119], v[112:113] op_sel:[0,0] op_sel_hi:[1,0]\n"
      "v_accvgpr_write_b32 a178, v118\n"
      "v_accvgpr_write_b32 a179, v119\n"
      "v_accvgpr_read_b32 v118, a180\n"
      "v_accvgpr_read_b32 v119, a181\n"
      "v_pk_mul_f32 v[118:119], v[118:119], v[112:113] op_sel:[0,1] op_sel_hi:[1,1]\n"
      "v_accvgpr_write_b32 a180, v118\n"
      "v_accvgpr_write_b32 a181, v119\n"
      "v_accvgpr_read_b32 v118, a182\n"
      "v_accvgpr_read_b32 v119, a183\n"
      "v_pk_mul_f32 v[118:119], v[118:119], v[112:113] op_sel:[0,1] op_sel_hi:[1,1]\n"
      "v_accvgpr_write_b32 a182, v118\n"
      "v_accvgpr_write_b32 a183, v119\n"
      "v_accvgpr_read_b32 v118, a184\n"
      "v_accvgpr_read_b32 v119, a185\n"
      "v_pk_mul_f32 v[118:119], v[118:119], v[114:115] op_sel:[0,0] op_sel_hi:[1,0]\n"
      "v_accvgpr_write_b32 a184, v118\n"
      "v_accvgpr_write_b32 a185, v119\n"
      "v_accvgpr_read_b32 v118, a186\n"
      "v_accvgpr_read_b32 v119, a187\n"
      "v_pk_mul_f32 v[118:119], v[118:119], v[114:115] op_sel:[0,0] op_sel_hi:[1,0]\n"
      "v_accvgpr_write_b32 a186, v118\n"
      "v_accvgpr_write_b32 a187, v119\n"
      "v_accvgpr_read_b32 v118, a188\n"
      "v_accvgpr_read_b32 v119, a189\n"
      "v_pk_mul_f32 v[118:119], v[118:119], v[114:115] op_sel:[0,1] op_sel_hi:[1,1]\n"
      "v_accvgpr_write_b32 a188, v118\n"
      "v_accvgpr_write_b32 a189, v119\n"
      "v_accvgpr_read_b32 v118, a190\n"
      "v_accvgpr_read_b32 v119, a191\n"
      "v_pk_mul_f32 v[118:119], v[118:119], v[114:115] op_sel:[0,1] op_sel_hi:[1,1]\n"
      "v_accvgpr_write_b32 a190, v118\n"
      "v_accvgpr_write_b32 a191, v119\n"
      "v_mov_b32 v118, v112\n"
      "s_nop 1\n"
      "v_mov_b32_dpp v118, v113 quad_perm:[0,1,2,3] row_mask:0x2 bank_mask:0xf\n"
      "s_nop 1\n"
      "v_mov_b32_dpp v118, v114 quad_perm:[0,1,2,3] row_mask:0x4 bank_mask:0xf\n"
      "s_nop 1\n"
      "v_mov_b32_dpp v118, v115 quad_perm:[0,1,2,3] row_mask:0x8 bank_mask:0xf\n"
      "v_accvgpr_read_b32 v119, a192\n"
      "v_mul_f32 v118, v118, v119\n"
      "v_accvgpr_write_b32 a192, v118\n"
      "v_lshlrev_b32 v118, 16, v208\n"
      "v_and_b32 v119, 0xffff0000, v208\n"
      "v_pk_mul_f32 v[118:119], v[118:119], v[112:113] op_sel:[0,0] op_sel_hi:[1,0]\n"
      "v_cvt_pk_bf16_f32 v208, v118, v119\n"
      "v_lshlrev_b32 v118, 16, v209\n"
      "v_and_b32 v119, 0xffff0000, v209\n"
      "v_pk_mul_f32 v[118:119], v[118:119], v[112:113] op_sel:[0,0] op_sel_hi:[1,0]\n"
      "v_cvt_pk_bf16_f32 v209, v118, v119\n"
      "v_lshlrev_b32 v118, 16, v210\n"
      "v_and_b32 v119, 0xffff0000, v210\n"
      "v_pk_mul_f32 v[118:119], v[118:119], v[112:113] op_sel:[0,0] op_sel_hi:[1,0]\n"
      "v_cvt_pk_bf16_f32 v210, v118, v119\n"
      "v_lshlrev_b32 v118, 16, v211\n"
      "v_and_b32 v119, 0xffff0000, v211\n"
      "v_pk_mul_f32 v[118:119], v[118:119], v[112:113] op_sel:[0,0] op_sel_hi:[1,0]\n"
      "v_cvt_pk_bf16_f32 v211, v118, v119\n"
      "v_lshlrev_b32 v118, 16, v212\n"
      "v_and_b32 v119, 0xffff0000, v212\n"
      "v_pk_mul_f32 v[118:119], v[118:119], v[112:113] op_sel:[0,1] op_sel_hi:[1,1]\n"
      "v_cvt_pk_bf16_f32 v212, v118, v119\n"
      "v_lshlrev_b32 v118, 16, v213\n"
      "v_and_b32 v119, 0xffff0000, v213\n"
      "v_pk_mul_f32 v[118:119], v[118:119], v[112:113] op_sel:[0,1] op_sel_hi:[1,1]\n"
      "v_cvt_pk_bf16_f32 v213, v118, v119\n"
      "v_lshlrev_b32 v118, 16, v214\n"
      "v_and_b32 v119, 0xffff0000, v214\n"
      "v_pk_mul_f32 v[118:119], v[118:119], v[112:113] op_sel:[0,1] op_sel_hi:[1,1]\n"
      "v_cvt_pk_bf16_f32 v214, v118, v119\n"
      "v_lshlrev_b32 v118, 16, v215\n"
      "v_and_b32 v119, 0xffff0000, v215\n"
      "v_pk_mul_f32 v[118:119], v[118:119], v[112:113] op_sel:[0,1] op_sel_hi:[1,1]\n"
      "v_cvt_pk_bf16_f32 v215, v118, v119\n"
      "v_lshlrev_b32 v118, 16, v216\n"
      "v_and_b32 v119, 0xffff0000, v216\n"
      "v_pk_mul_f32 v[118:119], v[118:119], v[114:115] op_sel:[0,0] op_sel_hi:[1,0]\n"
      "v_cvt_pk_bf16_f32 v216, v118, v119\n"
      "v_lshlrev_b32 v118, 16, v217\n"
      "v_and_b32 v119, 0xffff0000, v217\n"
      "v_pk_mul_f32 v[118:119], v[118:119], v[114:115] op_sel:[0,0] op_sel_hi:[1,0]\n"
      "v_cvt_pk_bf16_f32 v217, v118, v119\n"
      "v_lshlrev_b32 v118, 16, v218\n"
      "v_and_b32 v119, 0xffff0000, v218\n"
      "v_pk_mul_f32 v[118:119], v[118:119], v[114:115] op_sel:[0,0] op_sel_hi:[1,0]\n"
      "v_cvt_pk_bf16_f32 v218, v118, v119\n"
      "v_lshlrev_b32 v118, 16, v219\n"
      "v_and_b32 v119, 0xffff0000, v219\n"
      "v_pk_mul_f32 v[118:119], v[118:119], v[114:115] op_sel:[0,0] op_sel_hi:[1,0]\n"
      "v_cvt_pk_bf16_f32 v219, v118, v119\n"
      "v_lshlrev_b32 v118, 16, v220\n"
      "v_and_b32 v119, 0xffff0000, v220\n"
      "v_pk_mul_f32 v[118:119], v[118:119], v[114:115] op_sel:[0,1] op_sel_hi:[1,1]\n"
      "v_cvt_pk_bf16_f32 v220, v118, v119\n"
      "v_lshlrev_b32 v118, 16, v221\n"
      "v_and_b32 v119, 0xffff0000, v221\n"
      "v_pk_mul_f32 v[118:119], v[118:119], v[114:115] op_sel:[0,1] op_sel_hi:[1,1]\n"
      "v_cvt_pk_bf16_f32 v221, v118, v119\n"
      "v_lshlrev_b32 v118, 16, v222\n"
      "v_and_b32 v119, 0xffff0000, v222\n"
      "v_pk_mul_f32 v[118:119], v[118:119], v[114:115] op_sel:[0,1] op_sel_hi:[1,1]\n"
      "v_cvt_pk_bf16_f32 v222, v118, v119\n"
      "v_lshlrev_b32 v118, 16, v223\n"
      "v_and_b32 v119, 0xffff0000, v223\n"
      "v_pk_mul_f32 v[118:119], v[118:119], v[114:115] op_sel:[0,1] op_sel_hi:[1,1]\n"
      "v_cvt_pk_bf16_f32 v223, v118, v119\n"
      "v_lshlrev_b32 v118, 16, v192\n"
      "v_and_b32 v119, 0xffff0000, v192\n"
      "v_pk_mul_f32 v[118:119], v[118:119], v[112:113] op_sel:[0,0] op_sel_hi:[1,0]\n"
      "v_cvt_pk_bf16_f32 v192, v118, v119\n"
      "v_lshlrev_b32 v118, 16, v193\n"
      "v_and_b32 v119, 0xffff0000, v193\n"
      "v_pk_mul_f32 v[118:119], v[118:119], v[112:113] op_sel:[0,0] op_sel_hi:[1,0]\n"
      "v_cvt_pk_bf16_f32 v193, v118, v119\n"
      "v_lshlrev_b32 v118, 16, v194\n"
      "v_and_b32 v119, 0xffff0000, v194\n"
      "v_pk_mul_f32 v[118:119], v[118:119], v[112:113] op_sel:[0,0] op_sel_hi:[1,0]\n"
      "v_cvt_pk_bf16_f32 v194, v118, v119\n"
      "v_lshlrev_b32 v118, 16, v195\n"
      "v_and_b32 v119, 0xffff0000, v195\n"
      "v_pk_mul_f32 v[118:119], v[118:119], v[112:113] op_sel:[0,0] op_sel_hi:[1,0]\n"
      "v_cvt_pk_bf16_f32 v195, v118, v119\n"
      "v_lshlrev_b32 v118, 16, v196\n"
      "v_and_b32 v119, 0xffff0000, v196\n"
      "v_pk_mul_f32 v[118:119], v[118:119], v[112:113] op_sel:[0,1] op_sel_hi:[1,1]\n"
      "v_cvt_pk_bf16_f32 v196, v118, v119\n"
      "v_lshlrev_b32 v118, 16, v197\n"
      "v_and_b32 v119, 0xffff0000, v197\n"
      "v_pk_mul_f32 v[118:119], v[118:119], v[112:113] op_sel:[0,1] op_sel_hi:[1,1]\n"
      "v_cvt_pk_bf16_f32 v197, v118, v119\n"
      "v_lshlrev_b32 v118, 16, v198\n"
      "v_and_b32 v119, 0xffff0000, v198\n"
      "v_pk_mul_f32 v[118:119], v[118:119], v[112:113] op_sel:[0,1] op_sel_hi:[1,1]\n"
      "v_cvt_pk_bf16_f32 v198, v118, v119\n"
      "v_lshlrev_b32 v118, 16, v199\n"
      "v_and_b32 v119, 0xffff0000, v199\n"
      "v_pk_mul_f32 v[118:119], v[118:119], v[112:113] op_sel:[0,1] op_sel_hi:[1,1]\n"
      "v_cvt_pk_bf16_f32 v199, v118, v119\n"
      "v_lshlrev_b32 v118, 16, v200\n"
      "v_and_b32 v119, 0xffff0000, v200\n"
      "v_pk_mul_f32 v[118:119], v[118:119], v[114:115] op_sel:[0,0] op_sel_hi:[1,0]\n"
      "v_cvt_pk_bf16_f32 v200, v118, v119\n"
      "v_lshlrev_b32 v118, 16, v201\n"
      "v_and_b32 v119, 0xffff0000, v201\n"
      "v_pk_mul_f32 v[118:119], v[118:119], v[114:115] op_sel:[0,0] op_sel_hi:[1,0]\n"
      "v_cvt_pk_bf16_f32 v201, v118, v119\n"
      "v_lshlrev_b32 v118, 16, v202\n"
      "v_and_b32 v119, 0xffff0000, v202\n"
      "v_pk_mul_f32 v[118:119], v[118:119], v[114:115] op_sel:[0,0] op_sel_hi:[1,0]\n"
      "v_cvt_pk_bf16_f32 v202, v118, v119\n"
      "v_lshlrev_b32 v118, 16, v203\n"
      "v_and_b32 v119, 0xffff0000, v203\n"
      "v_pk_mul_f32 v[118:119], v[118:119], v[114:115] op_sel:[0,0] op_sel_hi:[1,0]\n"
      "v_cvt_pk_bf16_f32 v203, v118, v119\n"
      "v_lshlrev_b32 v118, 16, v204\n"
      "v_and_b32 v119, 0xffff0000, v204\n"
      "v_pk_mul_f32 v[118:119], v[118:119], v[114:115] op_sel:[0,1] op_sel_hi:[1,1]\n"
      "v_cvt_pk_bf16_f32 v204, v118, v119\n"
      "v_lshlrev_b32 v118, 16, v205\n"
      "v_and_b32 v119, 0xffff0000, v205\n"
      "v_pk_mul_f32 v[118:119], v[118:119], v[114:115] op_sel:[0,1] op_sel_hi:[1,1]\n"
      "v_cvt_pk_bf16_f32 v205, v118, v119\n"
      "v_lshlrev_b32 v118, 16, v206\n"
      "v_and_b32 v119, 0xffff0000, v206\n"
      "v_pk_mul_f32 v[118:119], v[118:119], v[114:115] op_sel:[0,1] op_sel_hi:[1,1]\n"
      "v_cvt_pk_bf16_f32 v206, v118, v119\n"
      "v_lshlrev_b32 v118, 16, v207\n"
      "v_and_b32 v119, 0xffff0000, v207\n"
      "v_pk_mul_f32 v[118:119], v[118:119], v[114:115] op_sel:[0,1] op_sel_hi:[1,1]\n"
      "v_cvt_pk_bf16_f32 v207, v118, v119\n"
      "v_lshlrev_b32 v118, 16, v0\n"
      "v_and_b32 v119, 0xffff0000, v0\n"
      "v_pk_mul_f32 v[118:119], v[118:119], v[112:113] op_sel:[0,0] op_sel_hi:[1,0]\n"
      "v_cvt_pk_bf16_f32 v0, v118, v119\n"
      "v_lshlrev_b32 v118, 16, v1\n"
      "v_and_b32 v119, 0xffff0000, v1\n"
      "v_pk_mul_f32 v[118:119], v[118:119], v[112:113] op_sel:[0,0] op_sel_hi:[1,0]\n"
      "v_cvt_pk_bf16_f32 v1, v118, v119\n"
      "v_lshlrev_b32 v118, 16, v2\n"
      "v_and_b32 v119, 0xffff0000, v2\n"
      "v_pk_mul_f32 v[118:119], v[118:119], v[112:113] op_sel:[0,0] op_sel_hi:[1,0]\n"
      "v_cvt_pk_bf16_f32 v2, v118, v119\n"
      "v_lshlrev_b32 v118, 16, v3\n"
      "v_and_b32 v119, 0xffff0000, v3\n"
      "v_pk_mul_f32 v[118:119], v[118:119], v[112:113] op_sel:[0,0] op_sel_hi:[1,0]\n"
      "v_cvt_pk_bf16_f32 v3, v118, v119\n"
      "v_lshlrev_b32 v118, 16, v4\n"
      "v_and_b32 v119, 0xffff0000, v4\n"
      "v_pk_mul_f32 v[118:119], v[118:119], v[112:113] op_sel:[0,1] op_sel_hi:[1,1]\n"
      "v_cvt_pk_bf16_f32 v4, v118, v119\n"
      "v_lshlrev_b32 v118, 16, v5\n"
      "v_and_b32 v119, 0xffff0000, v5\n"
      "v_pk_mul_f32 v[118:119], v[118:119], v[112:113] op_sel:[0,1] op_sel_hi:[1,1]\n"
      "v_cvt_pk_bf16_f32 v5, v118, v119\n"
      "v_lshlrev_b32 v118, 16, v6\n"
      "v_and_b32 v119, 0xffff0000, v6\n"
      "v_pk_mul_f32 v[118:119], v[118:119], v[112:113] op_sel:[0,1] op_sel_hi:[1,1]\n"
      "v_cvt_pk_bf16_f32 v6, v118, v119\n"
      "v_lshlrev_b32 v118, 16, v7\n"
      "v_and_b32 v119, 0xffff0000, v7\n"
      "v_pk_mul_f32 v[118:119], v[118:119], v[112:113] op_sel:[0,1] op_sel_hi:[1,1]\n"
      "v_cvt_pk_bf16_f32 v7, v118, v119\n"
      "v_lshlrev_b32 v118, 16, v8\n"
      "v_and_b32 v119, 0xffff0000, v8\n"
      "v_pk_mul_f32 v[118:119], v[118:119], v[114:115] op_sel:[0,0] op_sel_hi:[1,0]\n"
      "v_cvt_pk_bf16_f32 v8, v118, v119\n"
      "v_lshlrev_b32 v118, 16, v9\n"
      "v_and_b32 v119, 0xffff0000, v9\n"
      "v_pk_mul_f32 v[118:119], v[118:119], v[114:115] op_sel:[0,0] op_sel_hi:[1,0]\n"
      "v_cvt_pk_bf16_f32 v9, v118, v119\n"
      "v_lshlrev_b32 v118, 16, v10\n"
      "v_and_b32 v119, 0xffff0000, v10\n"
      "v_pk_mul_f32 v[118:119], v[118:119], v[114:115] op_sel:[0,0] op_sel_hi:[1,0]\n"
      "v_cvt_pk_bf16_f32 v10, v118, v119\n"
      "v_lshlrev_b32 v118, 16, v11\n"
      "v_and_b32 v119, 0xffff0000, v11\n"
      "v_pk_mul_f32 v[118:119], v[118:119], v[114:115] op_sel:[0,0] op_sel_hi:[1,0]\n"
      "v_cvt_pk_bf16_f32 v11, v118, v119\n"
      "v_lshlrev_b32 v118, 16, v12\n"
      "v_and_b32 v119, 0xffff0000, v12\n"
      "v_pk_mul_f32 v[118:119], v[118:119], v[114:115] op_sel:[0,1] op_sel_hi:[1,1]\n"
      "v_cvt_pk_bf16_f32 v12, v118, v119\n"
      "v_lshlrev_b32 v118, 16, v13\n"
      "v_and_b32 v119, 0xffff0000, v13\n"
      "v_pk_mul_f32 v[118:119], v[118:119], v[114:115] op_sel:[0,1] op_sel_hi:[1,1]\n"
      "v_cvt_pk_bf16_f32 v13, v118, v119\n"
      "v_lshlrev_b32 v118, 16, v14\n"
      "v_and_b32 v119, 0xffff0000, v14\n"
      "v_pk_mul_f32 v[118:119], v[118:119], v[114:115] op_sel:[0,1] op_sel_hi:[1,1]\n"
      "v_cvt_pk_bf16_f32 v14, v118, v119\n"
      "v_lshlrev_b32 v118, 16, v15\n"
      "v_and_b32 v119, 0xffff0000, v15\n"
      "v_pk_mul_f32 v[118:119], v[118:119], v[114:115] op_sel:[0,1] op_sel_hi:[1,1]\n"
      "v_cvt_pk_bf16_f32 v15, v118, v119\n"
      "v_lshlrev_b32 v118, 16, v16\n"
      "v_and_b32 v119, 0xffff0000, v16\n"
      "v_pk_mul_f32 v[118:119], v[118:119], v[112:113] op_sel:[0,0] op_sel_hi:[1,0]\n"
      "v_cvt_pk_bf16_f32 v16, v118, v119\n"
      "v_lshlrev_b32 v118, 16, v17\n"
      "v_and_b32 v119, 0xffff0000, v17\n"
      "v_pk_mul_f32 v[118:119], v[118:119], v[112:113] op_sel:[0,0] op_sel_hi:[1,0]\n"
      "v_cvt_pk_bf16_f32 v17, v118, v119\n"
      "v_lshlrev_b32 v118, 16, v18\n"
      "v_and_b32 v119, 0xffff0000, v18\n"
      "v_pk_mul_f32 v[118:119], v[118:119], v[112:113] op_sel:[0,0] op_sel_hi:[1,0]\n"
      "v_cvt_pk_bf16_f32 v18, v118, v119\n"
      "v_lshlrev_b32 v118, 16, v19\n"
      "v_and_b32 v119, 0xffff0000, v19\n"
      "v_pk_mul_f32 v[118:119], v[118:119], v[112:113] op_sel:[0,0] op_sel_hi:[1,0]\n"
      "v_cvt_pk_bf16_f32 v19, v118, v119\n"
      "v_lshlrev_b32 v118, 16, v20\n"
      "v_and_b32 v119, 0xffff0000, v20\n"
      "v_pk_mul_f32 v[118:119], v[118:119], v[112:113] op_sel:[0,1] op_sel_hi:[1,1]\n"
      "v_cvt_pk_bf16_f32 v20, v118, v119\n"
      "v_lshlrev_b32 v118, 16, v21\n"
      "v_and_b32 v119, 0xffff0000, v21\n"
      "v_pk_mul_f32 v[118:119], v[118:119], v[112:113] op_sel:[0,1] op_sel_hi:[1,1]\n"
      "v_cvt_pk_bf16_f32 v21, v118, v119\n"
      "v_lshlrev_b32 v118, 16, v22\n"
      "v_and_b32 v119, 0xffff0000, v22\n"
      "v_pk_mul_f32 v[118:119], v[118:119], v[112:113] op_sel:[0,1] op_sel_hi:[1,1]\n"
      "v_cvt_pk_bf16_f32 v22, v118, v119\n"
      "v_lshlrev_b32 v118, 16, v23\n"
      "v_and_b32 v119, 0xffff0000, v23\n"
      "v_pk_mul_f32 v[118:119], v[118:119], v[112:113] op_sel:[0,1] op_sel_hi:[1,1]\n"
      "v_cvt_pk_bf16_f32 v23, v118, v119\n"
      "v_lshlrev_b32 v118, 16, v24\n"
      "v_and_b32 v119, 0xffff0000, v24\n"
      "v_pk_mul_f32 v[118:119], v[118:119], v[114:115] op_sel:[0,0] op_sel_hi:[1,0]\n"
      "v_cvt_pk_bf16_f32 v24, v118, v119\n"
      "v_lshlrev_b32 v118, 16, v25\n"
      "v_and_b32 v119, 0xffff0000, v25\n"
      "v_pk_mul_f32 v[118:119], v[118:119], v[114:115] op_sel:[0,0] op_sel_hi:[1,0]\n"
      "v_cvt_pk_bf16_f32 v25, v118, v119\n"
      "v_lshlrev_b32 v118, 16, v26\n"
      "v_and_b32 v119, 0xffff0000, v26\n"
      "v_pk_mul_f32 v[118:119], v[118:119], v[114:115] op_sel:[0,0] op_sel_hi:[1,0]\n"
      "v_cvt_pk_bf16_f32 v26, v118, v119\n"
      "v_lshlrev_b32 v118, 16, v27\n"
      "v_and_b32 v119, 0xffff0000, v27\n"
      "v_pk_mul_f32 v[118:119], v[118:119], v[114:115] op_sel:[0,0] op_sel_hi:[1,0]\n"
      "v_cvt_pk_bf16_f32 v27, v118, v119\n"
      "v_lshlrev_b32 v118, 16, v28\n"
      "v_and_b32 v119, 0xffff0000, v28\n"
      "v_pk_mul_f32 v[118:119], v[118:119], v[114:115] op_sel:[0,1] op_sel_hi:[1,1]\n"
      "v_cvt_pk_bf16_f32 v28, v118, v119\n"
      "v_lshlrev_b32 v118, 16, v29\n"
      "v_and_b32 v119, 0xffff0000, v29\n"
      "v_pk_mul_f32 v[118:119], v[118:119], v[114:115] op_sel:[0,1] op_sel_hi:[1,1]\n"
      "v_cvt_pk_bf16_f32 v29, v118, v119\n"
      "v_lshlrev_b32 v118, 16, v30\n"
      "v_and_b32 v119, 0xffff0000, v30\n"
      "v_pk_mul_f32 v[118:119], v[118:119], v[114:115] op_sel:[0,1] op_sel_hi:[1,1]\n"
      "v_cvt_pk_bf16_f32 v30, v118, v119\n"
      "v_lshlrev_b32 v118, 16, v31\n"
      "v_and_b32 v119, 0xffff0000, v31\n"
      "v_pk_mul_f32 v[118:119], v[118:119], v[114:115] op_sel:[0,1] op_sel_hi:[1,1]\n"
      "v_cvt_pk_bf16_f32 v31, v118, v119\n"
      "v_lshlrev_b32 v118, 16, v96\n"
      "v_and_b32 v119, 0xffff0000, v96\n"
      "v_pk_mul_f32 v[118:119], v[118:119], v[112:113] op_sel:[0,0] op_sel_hi:[1,0]\n"
      "v_cvt_pk_bf16_f32 v96, v118, v119\n"
      "v_lshlrev_b32 v118, 16, v97\n"
      "v_and_b32 v119, 0xffff0000, v97\n"
      "v_pk_mul_f32 v[118:119], v[118:119], v[112:113] op_sel:[0,0] op_sel_hi:[1,0]\n"
      "v_cvt_pk_bf16_f32 v97, v118, v119\n"
      "v_lshlrev_b32 v118, 16, v98\n"
      "v_and_b32 v119, 0xffff0000, v98\n"
      "v_pk_mul_f32 v[118:119], v[118:119], v[112:113] op_sel:[0,0] op_sel_hi:[1,0]\n"
      "v_cvt_pk_bf16_f32 v98, v118, v119\n"
      "v_lshlrev_b32 v118, 16, v99\n"
      "v_and_b32 v119, 0xffff0000, v99\n"
      "v_pk_mul_f32 v[118:119], v[118:119], v[112:113] op_sel:[0,0] op_sel_hi:[1,0]\n"
      "v_cvt_pk_bf16_f32 v99, v118, v119\n"
      "v_lshlrev_b32 v118, 16, v100\n"
      "v_and_b32 v119, 0xffff0000, v100\n"
      "v_pk_mul_f32 v[118:119], v[118:119], v[112:113] op_sel:[0,1] op_sel_hi:[1,1]\n"
      "v_cvt_pk_bf16_f32 v100, v118, v119\n"
      "v_lshlrev_b32 v118, 16, v101\n"
      "v_and_b32 v119, 0xffff0000, v101\n"
      "v_pk_mul_f32 v[118:119], v[118:119], v[112:113] op_sel:[0,1] op_sel_hi:[1,1]\n"
      "v_cvt_pk_bf16_f32 v101, v118, v119\n"
      "v_lshlrev_b32 v118, 16, v102\n"
      "v_and_b32 v119, 0xffff0000, v102\n"
      "v_pk_mul_f32 v[118:119], v[118:119], v[112:113] op_sel:[0,1] op_sel_hi:[1,1]\n"
      "v_cvt_pk_bf16_f32 v102, v118, v119\n"
      "v_lshlrev_b32 v118, 16, v103\n"
      "v_and_b32 v119, 0xffff0000, v103\n"
      "v_pk_mul_f32 v[118:119], v[118:119], v[112:113] op_sel:[0,1] op_sel_hi:[1,1]\n"
      "v_cvt_pk_bf16_f32 v103, v118, v119\n"
      "v_lshlrev_b32 v118, 16, v104\n"
      "v_and_b32 v119, 0xffff0000, v104\n"
      "v_pk_mul_f32 v[118:119], v[118:119], v[114:115] op_sel:[0,0] op_sel_hi:[1,0]\n"
      "v_cvt_pk_bf16_f32 v104, v118, v119\n"
      "v_lshlrev_b32 v118, 16, v105\n"
      "v_and_b32 v119, 0xffff0000, v105\n"
      "v_pk_mul_f32 v[118:119], v[118:119], v[114:115] op_sel:[0,0] op_sel_hi:[1,0]\n"
      "v_cvt_pk_bf16_f32 v105, v118, v119\n"
      "v_lshlrev_b32 v118, 16, v106\n"
      "v_and_b32 v119, 0xffff0000, v106\n"
      "v_pk_mul_f32 v[118:119], v[118:119], v[114:115] op_sel:[0,0] op_sel_hi:[1,0]\n"
      "v_cvt_pk_bf16_f32 v106, v118, v119\n"
      "v_lshlrev_b32 v118, 16, v107\n"
      "v_and_b32 v119, 0xffff0000, v107\n"
      "v_pk_mul_f32 v[118:119], v[118:119], v[114:115] op_sel:[0,0] op_sel_hi:[1,0]\n"
      "v_cvt_pk_bf16_f32 v107, v118, v119\n"
      "v_lshlrev_b32 v118, 16, v108\n"
      "v_and_b32 v119, 0xffff0000, v108\n"
      "v_pk_mul_f32 v[118:119], v[118:119], v[114:115] op_sel:[0,1] op_sel_hi:[1,1]\n"
      "v_cvt_pk_bf16_f32 v108, v118, v119\n"
      "v_lshlrev_b32 v118, 16, v109\n"
      "v_and_b32 v119, 0xffff0000, v109\n"
      "v_pk_mul_f32 v[118:119], v[118:119], v[114:115] op_sel:[0,1] op_sel_hi:[1,1]\n"
      "v_cvt_pk_bf16_f32 v109, v118, v119\n"
      "v_lshlrev_b32 v118, 16, v110\n"
      "v_and_b32 v119, 0xffff0000, v110\n"
      "v_pk_mul_f32 v[118:119], v[118:119], v[114:115] op_sel:[0,1] op_sel_hi:[1,1]\n"
      "v_cvt_pk_bf16_f32 v110, v118, v119\n"
      "v_lshlrev_b32 v118, 16, v111\n"
      "v_and_b32 v119, 0xffff0000, v111\n"
      "v_pk_mul_f32 v[118:119], v[118:119], v[114:115] op_sel:[0,1] op_sel_hi:[1,1]\n"
      "v_cvt_pk_bf16_f32 v111, v118, v119\n"
      "s_setpc_b64 s[84:85]\n"
      "MIS_%=:\n"
      "s_waitcnt lgkmcnt(0)\n"
      "v_sub_f32 v148, v153, v117\n"
      "v_min_f32 v149, 0, -v148\n"
      "v_min_f32 v148, 0, v148\n"
      "v_exp_f32 v149, v149\n"
      "v_exp_f32 v148, v148\n"
      "v_min_f32 v117, v117, v153\n"
      "v_mov_b32 v112, v148\n"
      "v_mov_b32 v228, v149\n"
      "v_mov_b32 v114, v112\n"
      "v_mov_b32 v230, v228\n"
      "s_nop 0\n"
      "v_permlane32_swap_b32 v112, v114\n"
      "v_permlane32_swap_b32 v228, v230\n"
      "v_mov_b32 v113, v112\n"
      "v_mov_b32 v229, v228\n"
      "v_mov_b32 v115, v114\n"
      "v_mov_b32 v231, v230\n"
      "v_permlane16_swap_b32 v112, v113\n"
      "v_permlane16_swap_b32 v228, v229\n"
      "v_permlane16_swap_b32 v114, v115\n"
      "v_permlane16_swap_b32 v230, v231\n"
      "s_call_b64 s[84:85], SCALE_%=\n"
      "v_pk_mul_f32 v[160:161], v[160:161], v[228:229] op_sel:[0,0] op_sel_hi:[1,0]\n"
      "v_pk_mul_f32 v[162:163], v[162:163], v[228:229] op_sel:[0,0] op_sel_hi:[1,0]\n"
      "v_pk_mul_f32 v[164:165], v[164:165], v[228:229] op_sel:[0,1] op_sel_hi:[1,1]\n"
      "v_pk_mul_f32 v[166:167], v[166:167], v[228:229] op_sel:[0,1] op_sel_hi:[1,1]\n"
      "v_pk_mul_f32 v[168:169], v[168:169], v[230:231] op_sel:[0,0] op_sel_hi:[1,0]\n"
      "v_pk_mul_f32 v[170:171], v[170:171], v[230:231] op_sel:[0,0] op_sel_hi:[1,0]\n"
      "v_pk_mul_f32 v[172:173], v[172:173], v[230:231] op_sel:[0,1] op_sel_hi:[1,1]\n"
      "v_pk_mul_f32 v[174:175], v[174:175], v[230:231] op_sel:[0,1] op_sel_hi:[1,1]\n"
      "v_pk_mul_f32 v[176:177], v[176:177], v[228:229] op_sel:[0,0] op_sel_hi:[1,0]\n"
      "v_pk_mul_f32 v[178:179], v[178:179], v[228:229] op_sel:[0,0] op_sel_hi:[1,0]\n"
      "v_pk_mul_f32 v[180:181], v[180:181], v[228:229] op_sel:[0,1] op_sel_hi:[1,1]\n"
      "v_pk_mul_f32 v[182:183], v[182:183], v[228:229] op_sel:[0,1] op_sel_hi:[1,1]\n"
      "v_pk_mul_f32 v[184:185], v[184:185], v[230:231] op_sel:[0,0] op_sel_hi:[1,0]\n"
      "v_pk_mul_f32 v[186:187], v[186:187], v[230:231] op_sel:[0,0] op_sel_hi:[1,0]\n"
      "v_pk_mul_f32 v[188:189], v[188:189], v[230:231] op_sel:[0,1] op_sel_hi:[1,1]\n"
      "v_pk_mul_f32 v[190:191], v[190:191], v[230:231] op_sel:[0,1] op_sel_hi:[1,1]\n"
      "v_mov_b32 v112, v228\n"
      "v_mov_b32 v113, v229\n"
      "v_mov_b32 v114, v230\n"
      "v_mov_b32 v115, v231\n"
      "v_accvgpr_read_b32 v128, a64\n"
      "v_accvgpr_read_b32 v129, a65\n"
      "v_accvgpr_read_b32 v130, a66\n"
      "v_accvgpr_read_b32 v131, a67\n"
      "v_accvgpr_read_b32 v132, a68\n"
      "v_accvgpr_read_b32 v133, a69\n"
      "v_accvgpr_read_b32 v134, a70\n"
      "v_accvgpr_read_b32 v135, a71\n"
      "v_accvgpr_read_b32 v136, a72\n"
      "v_accvgpr_read_b32 v137, a73\n"
      "v_accvgpr_read_b32 v138, a74\n"
      "v_accvgpr_read_b32 v139, a75\n"
      "v_accvgpr_read_b32 v140, a76\n"
      "v_accvgpr_read_b32 v141, a77\n"
      "v_accvgpr_read_b32 v142, a78\n"
      "v_accvgpr_read_b32 v143, a79\n"
      "v_lshlrev_b32 v118, 16, v128\n"
      "v_and_b32 v119, 0xffff0000, v128\n"
      "v_pk_mul_f32 v[118:119], v[118:119], v[112:113] op_sel:[0,0] op_sel_hi:[1,0]\n"
      "v_cvt_pk_bf16_f32 v128, v118, v119\n"
      "v_lshlrev_b32 v118, 16, v129\n"
      "v_and_b32 v119, 0xffff0000, v129\n"
      "v_pk_mul_f32 v[118:119], v[118:119], v[112:113] op_sel:[0,0] op_sel_hi:[1,0]\n"
      "v_cvt_pk_bf16_f32 v129, v118, v119\n"
      "v_lshlrev_b32 v118, 16, v130\n"
      "v_and_b32 v119, 0xffff0000, v130\n"
      "v_pk_mul_f32 v[118:119], v[118:119], v[112:113] op_sel:[0,0] op_sel_hi:[1,0]\n"
      "v_cvt_pk_bf16_f32 v130, v118, v119\n"
      "v_lshlrev_b32 v118, 16, v131\n"
      "v_and_b32 v119, 0xffff0000, v131\n"
      "v_pk_mul_f32 v[118:119], v[118:119], v[112:113] op_sel:[0,0] op_sel_hi:[1,0]\n"
      "v_cvt_pk_bf16_f32 v131, v118, v119\n"
      "v_lshlrev_b32 v118, 16, v132\n"
      "v_and_b32 v119, 0xffff0000, v132\n"
      "v_pk_mul_f32 v[118:119], v[118:119], v[112:113] op_sel:[0,1] op_sel_hi:[1,1]\n"
      "v_cvt_pk_bf16_f32 v132, v118, v119\n"
      "v_lshlrev_b32 v118, 16, v133\n"
      "v_and_b32 v119, 0xffff0000, v133\n"
      "v_pk_mul_f32 v[118:119], v[118:119], v[112:113] op_sel:[0,1] op_sel_hi:[1,1]\n"
      "v_cvt_pk_bf16_f32 v133, v118, v119\n"
      "v_lshlrev_b32 v118, 16, v134\n"
      "v_and_b32 v119, 0xffff0000, v134\n"
      "v_pk_mul_f32 v[118:119], v[118:119], v[112:113] op_sel:[0,1] op_sel_hi:[1,1]\n"
      "v_cvt_pk_bf16_f32 v134, v118, v119\n"
      "v_lshlrev_b32 v118, 16, v135\n"
      "v_and_b32 v119, 0xffff0000, v135\n"
      "v_pk_mul_f32 v[118:119], v[118:119], v[112:113] op_sel:[0,1] op_sel_hi:[1,1]\n"
      "v_cvt_pk_bf16_f32 v135, v118, v119\n"
      "v_lshlrev_b32 v118, 16, v136\n"
      "v_and_b32 v119, 0xffff0000, v136\n"
      "v_pk_mul_f32 v[118:119], v[118:119], v[114:115] op_sel:[0,0] op_sel_hi:[1,0]\n"
      "v_cvt_pk_bf16_f32 v136, v118, v119\n"
      "v_lshlrev_b32 v118, 16, v137\n"
      "v_and_b32 v119, 0xffff0000, v137\n"
      "v_pk_mul_f32 v[118:119], v[118:119], v[114:115] op_sel:[0,0] op_sel_hi:[1,0]\n"
      "v_cvt_pk_bf16_f32 v137, v118, v119\n"
      "v_lshlrev_b32 v118, 16, v138\n"
      "v_and_b32 v119, 0xffff0000, v138\n"
      "v_pk_mul_f32 v[118:119], v[118:119], v[114:115] op_sel:[0,0] op_sel_hi:[1,0]\n"
      "v_cvt_pk_bf16_f32 v138, v118, v119\n"
      "v_lshlrev_b32 v118, 16, v139\n"
      "v_and_b32 v119, 0xffff0000, v139\n"
      "v_pk_mul_f32 v[118:119], v[118:119], v[114:115] op_sel:[0,0] op_sel_hi:[1,0]\n"
      "v_cvt_pk_bf16_f32 v139, v118, v119\n"
      "v_lshlrev_b32 v118, 16, v140\n"
      "v_and_b32 v119, 0xffff0000, v140\n"
      "v_pk_mul_f32 v[118:119], v[118:119], v[114:115] op_sel:[0,1] op_sel_hi:[1,1]\n"
      "v_cvt_pk_bf16_f32 v140, v118, v119\n"
      "v_lshlrev_b32 v118, 16, v141\n"
      "v_and_b32 v119, 0xffff0000, v141\n"
      "v_pk_mul_f32 v[118:119], v[118:119], v[114:115] op_sel:[0,1] op_sel_hi:[1,1]\n"
      "v_cvt_pk_bf16_f32 v141, v118, v119\n"
      "v_lshlrev_b32 v118, 16, v142\n"
      "v_and_b32 v119, 0xffff0000, v142\n"
      "v_pk_mul_f32 v[118:119], v[118:119], v[114:115] op_sel:[0,1] op_sel_hi:[1,1]\n"
      "v_cvt_pk_bf16_f32 v142, v118, v119\n"
      "v_lshlrev_b32 v118, 16, v143\n"
      "v_and_b32 v119, 0xffff0000, v143\n"
      "v_pk_mul_f32 v[118:119], v[118:119], v[114:115] op_sel:[0,1] op_sel_hi:[1,1]\n"
      "v_cvt_pk_bf16_f32 v143, v118, v119\n"
      "v_accvgpr_write_b32 a64, v128\n"
      "v_accvgpr_write_b32 a65, v129\n"
      "v_accvgpr_write_b32 a66, v130\n"
      "v_accvgpr_write_b32 a67, v131\n"
      "v_accvgpr_write_b32 a68, v132\n"
      "v_accvgpr_write_b32 a69, v133\n"
      "v_accvgpr_write_b32 a70, v134\n"
      "v_accvgpr_write_b32 a71, v135\n"
      "v_accvgpr_write_b32 a72, v136\n"
      "v_accvgpr_write_b32 a73, v137\n"
      "v_accvgpr_write_b32 a74, v138\n"
      "v_accvgpr_write_b32 a75, v139\n"
      "v_accvgpr_write_b32 a76, v140\n"
      "v_accvgpr_write_b32 a77, v141\n"
      "v_accvgpr_write_b32 a78, v142\n"
      "v_accvgpr_write_b32 a79, v143\n"
      "s_branch MR_%=\n"
      "FIX_%=:\n"
      "s_nop 7\n"
      "v_sub_f32 v224, v81, v117\n"
      "v_min_f32 v112, 0, -v224\n"
      "v_min_f32 v224, 0, v224\n"
      "v_exp_f32 v112, v112\n"
      "v_exp_f32 v224, v224\n"
      "s_nop 0\n"
      "v_mul_f32 v144, v144, v224\n"
      "v_mul_f32 v80, v80, v112\n"
      "v_mov_b32 v226, v224\n"
      "v_mov_b32 v114, v112\n"
      "s_nop 0\n"
      "v_permlane32_swap_b32 v224, v226\n"
      "v_permlane32_swap_b32 v112, v114\n"
      "v_mov_b32 v225, v224\n"
      "v_mov_b32 v113, v112\n"
      "v_mov_b32 v227, v226\n"
      "v_mov_b32 v115, v114\n"
      "v_permlane16_swap_b32 v224, v225\n"
      "v_permlane16_swap_b32 v112, v113\n"
      "v_permlane16_swap_b32 v226, v227\n"
      "v_permlane16_swap_b32 v114, v115\n"
      "v_pk_mul_f32 v[32:33], v[32:33], v[224:225] op_sel:[0,0] op_sel_hi:[1,0]\n"
      "v_pk_mul_f32 v[34:35], v[34:35], v[224:225] op_sel:[0,0] op_sel_hi:[1,0]\n"
      "v_pk_mul_f32 v[36:37], v[36:37], v[224:225] op_sel:[0,1] op_sel_hi:[1,1]\n"
      "v_pk_mul_f32 v[38:39], v[38:39], v[224:225] op_sel:[0,1] op_sel_hi:[1,1]\n"
      "v_pk_mul_f32 v[40:41], v[40:41], v[226:227] op_sel:[0,0] op_sel_hi:[1,0]\n"
      "v_pk_mul_f32 v[42:43], v[42:43], v[226:227] op_sel:[0,0] op_sel_hi:[1,0]\n"
      "v_pk_mul_f32 v[44:45], v[44:45], v[226:227] op_sel:[0,1] op_sel_hi:[1,1]\n"
      "v_pk_mul_f32 v[46:47], v[46:47], v[226:227] op_sel:[0,1] op_sel_hi:[1,1]\n"
      "v_pk_mul_f32 v[48:49], v[48:49], v[224:225] op_sel:[0,0] op_sel_hi:[1,0]\n"
      "v_pk_mul_f32 v[50:51], v[50:51], v[224:225] op_sel:[0,0] op_sel_hi:[1,0]\n"
      "v_pk_mul_f32 v[52:53], v[52:53], v[224:225] op_sel:[0,1] op_sel_hi:[1,1]\n"
      "v_pk_mul_f32 v[54:55], v[54:55], v[224:225] op_sel:[0,1] op_sel_hi:[1,1]\n"
      "v_pk_mul_f32 v[56:57], v[56:57], v[226:227] op_sel:[0,0] op_sel_hi:[1,0]\n"
      "v_pk_mul_f32 v[58:59], v[58:59], v[226:227] op_sel:[0,0] op_sel_hi:[1,0]\n"
      "v_pk_mul_f32 v[60:61], v[60:61], v[226:227] op_sel:[0,1] op_sel_hi:[1,1]\n"
      "v_pk_mul_f32 v[62:63], v[62:63], v[226:227] op_sel:[0,1] op_sel_hi:[1,1]\n"
      "v_lshlrev_b32 v118, 16, v64\n"
      "v_and_b32 v119, 0xffff0000, v64\n"
      "v_pk_mul_f32 v[118:119], v[118:119], v[112:113] op_sel:[0,0] op_sel_hi:[1,0]\n"
      "v_cvt_pk_bf16_f32 v64, v118, v119\n"
      "v_lshlrev_b32 v118, 16, v65\n"
      "v_and_b32 v119, 0xffff0000, v65\n"
      "v_pk_mul_f32 v[118:119], v[118:119], v[112:113] op_sel:[0,0] op_sel_hi:[1,0]\n"
      "v_cvt_pk_bf16_f32 v65, v118, v119\n"
      "v_lshlrev_b32 v118, 16, v66\n"
      "v_and_b32 v119, 0xffff0000, v66\n"
      "v_pk_mul_f32 v[118:119], v[118:119], v[112:113] op_sel:[0,0] op_sel_hi:[1,0]\n"
      "v_cvt_pk_bf16_f32 v66, v118, v119\n"
      "v_lshlrev_b32 v118, 16, v67\n"
      "v_and_b32 v119, 0xffff0000, v67\n"
      "v_pk_mul_f32 v[118:119], v[118:119], v[112:113] op_sel:[0,0] op_sel_hi:[1,0]\n"
      "v_cvt_pk_bf16_f32 v67, v118, v119\n"
      "v_lshlrev_b32 v118, 16, v68\n"
      "v_and_b32 v119, 0xffff0000, v68\n"
      "v_pk_mul_f32 v[118:119], v[118:119], v[112:113] op_sel:[0,1] op_sel_hi:[1,1]\n"
      "v_cvt_pk_bf16_f32 v68, v118, v119\n"
      "v_lshlrev_b32 v118, 16, v69\n"
      "v_and_b32 v119, 0xffff0000, v69\n"
      "v_pk_mul_f32 v[118:119], v[118:119], v[112:113] op_sel:[0,1] op_sel_hi:[1,1]\n"
      "v_cvt_pk_bf16_f32 v69, v118, v119\n"
      "v_lshlrev_b32 v118, 16, v70\n"
      "v_and_b32 v119, 0xffff0000, v70\n"
      "v_pk_mul_f32 v[118:119], v[118:119], v[112:113] op_sel:[0,1] op_sel_hi:[1,1]\n"
      "v_cvt_pk_bf16_f32 v70, v118, v119\n"
      "v_lshlrev_b32 v118, 16, v71\n"
      "v_and_b32 v119, 0xffff0000, v71\n"
      "v_pk_mul_f32 v[118:119], v[118:119], v[112:113] op_sel:[0,1] op_sel_hi:[1,1]\n"
      "v_cvt_pk_bf16_f32 v71, v118, v119\n"
      "v_lshlrev_b32 v118, 16, v72\n"
      "v_and_b32 v119, 0xffff0000, v72\n"
      "v_pk_mul_f32 v[118:119], v[118:119], v[114:115] op_sel:[0,0] op_sel_hi:[1,0]\n"
      "v_cvt_pk_bf16_f32 v72, v118, v119\n"
      "v_lshlrev_b32 v118, 16, v73\n"
      "v_and_b32 v119, 0xffff0000, v73\n"
      "v_pk_mul_f32 v[118:119], v[118:119], v[114:115] op_sel:[0,0] op_sel_hi:[1,0]\n"
      "v_cvt_pk_bf16_f32 v73, v118, v119\n"
      "v_lshlrev_b32 v118, 16, v74\n"
      "v_and_b32 v119, 0xffff0000, v74\n"
      "v_pk_mul_f32 v[118:119], v[118:119], v[114:115] op_sel:[0,0] op_sel_hi:[1,0]\n"
      "v_cvt_pk_bf16_f32 v74, v118, v119\n"
      "v_lshlrev_b32 v118, 16, v75\n"
      "v_and_b32 v119, 0xffff0000, v75\n"
      "v_pk_mul_f32 v[118:119], v[118:119], v[114:115] op_sel:[0,0] op_sel_hi:[1,0]\n"
      "v_cvt_pk_bf16_f32 v75, v118, v119\n"
      "v_lshlrev_b32 v118, 16, v76\n"
      "v_and_b32 v119, 0xffff0000, v76\n"
      "v_pk_mul_f32 v[118:119], v[118:119], v[114:115] op_sel:[0,1] op_sel_hi:[1,1]\n"
      "v_cvt_pk_bf16_f32 v76, v118, v119\n"
      "v_lshlrev_b32 v118, 16, v77\n"
      "v_and_b32 v119, 0xffff0000, v77\n"
      "v_pk_mul_f32 v[118:119], v[118:119], v[114:115] op_sel:[0,1] op_sel_hi:[1,1]\n"
      "v_cvt_pk_bf16_f32 v77, v118, v119\n"
      "v_lshlrev_b32 v118, 16, v78\n"
      "v_and_b32 v119, 0xffff0000, v78\n"
      "v_pk_mul_f32 v[118:119], v[118:119], v[114:115] op_sel:[0,1] op_sel_hi:[1,1]\n"
      "v_cvt_pk_bf16_f32 v78, v118, v119\n"
      "v_lshlrev_b32 v118, 16, v79\n"
      "v_and_b32 v119, 0xffff0000, v79\n"
      "v_pk_mul_f32 v[118:119], v[118:119], v[114:115] op_sel:[0,1] op_sel_hi:[1,1]\n"
      "v_cvt_pk_bf16_f32 v79, v118, v119\n"
      "v_add_f32 v224, v144, v80\n"
      "v_rcp_f32 v224, v224\n"
      "v_mfma_f32_16x16x32_bf16 v[160:163], v[86:89], v[64:67], v[32:35]\n"
      "v_mfma_f32_16x16x32_bf16 v[176:179], v[84:87], v[64:67], v[48:51]\n"
      "v_mfma_f32_16x16x32_bf16 v[164:167], v[86:89], v[68:71], v[36:39]\n"
      "v_mfma_f32_16x16x32_bf16 v[180:183], v[84:87], v[68:71], v[52:55]\n"
      "v_mfma_f32_16x16x32_bf16 v[168:171], v[86:89], v[72:75], v[40:43]\n"
      "v_mfma_f32_16x16x32_bf16 v[184:187], v[84:87], v[72:75], v[56:59]\n"
      "v_mfma_f32_16x16x32_bf16 v[172:175], v[86:89], v[76:79], v[44:47]\n"
      "v_mfma_f32_16x16x32_bf16 v[188:191], v[84:87], v[76:79], v[60:63]\n"
      "v_mov_b32 v226, v224\n"
      "s_nop 1\n"
      "s_nop 0\n"
      "s_nop 1\n"
      "v_permlane32_swap_b32 v224, v226\n"
      "s_nop 1\n"
      "v_mov_b32 v225, v224\n"
      "s_nop 1\n"
      "v_mov_b32 v227, v226\n"
      "s_nop 1\n"
      "v_permlane16_swap_b32 v224, v225\n"
      "s_nop 1\n"
      "v_permlane16_swap_b32 v226, v227\n"
      "s_nop 1\n"
      "s_branch FR_%=\n"
      ".subsection 0\n"
      : "+{s[4:19]}"(ka), "+{v[128:131]}"(pre), "+{s92}"(tch), "+{v[242:249]}"(ad), "+{v[250:251]}"(ra)
      : [qo] "s"(bk * 128 * D_ * 2), [oo] "s"((bk * 128 + rh * 64) * (D_ * 2) + (2 * split + kh) * 64 + 4096),
        [fo] "s"((unsigned)WS_EPOCH + bk * 128 + 64 + split * 8), [xo] "s"((unsigned)(WS_X + split * WS_X1 + (bk * 4 + wid) * XW_BYTES)),
        [xw] "s"(xarea(wid)), [xr] "s"(xarea(wid ^ 2)),
        [so0] "s"(so0), [inc] "s"(inc), [wb] "s"(wbase), [tv] "s"(wbase + STAGE_BYTES + KIMG + split * 8192), [n1] "s"(n1),
        [qoff] "s"(Q_OFF + rh * 16384 - kh * (8192 - 1024)), [toff] "s"(TREE_OFF - kh * (8192 - 1024)),
        [neg] "s"((wid ^ (3 * split)) ? 0xff800000u : 0u),
        [lead] "s"(blk * 4 + wid * 32), [cph] "s"(C2 + (unsigned)((jj & ~3) * 8 + xg * 4) * WIN / 256u),
        [rep] "s"((jj >> 2) == 160 / 32 ? 128 + (blk & 31) * 4 : (blk - 224) * 4), [vrd] "s"((unsigned)WS_REF + 8192 + 256 + (jj & (2 - 1)) * (8192 / 2))
      : "v0", "v1", "v2", "v3", "v4", "v5", "v6", "v7", "v8", "v9", "v10", "v11", "v12", "v13", "v14", "v15", "v16", "v17", "v18", "v19", "v20", "v21", "v22", "v23", "v24", "v25", "v26", "v27", "v28", "v29", "v30", "v31", "v32", "v33", "v34", "v35", "v36", "v37", "v38", "v39", "v40", "v41", "v42", "v43", "v44", "v45", "v46", "v47", "v48", "v49", "v50", "v51", "v52", "v53", "v54", "v55", "v56", "v57", "v58", "v59", "v60", "v61", "v62", "v63", "v64", "v65", "v66", "v67", "v68", "v69", "v70", "v71", "v72", "v73", "v74", "v75", "v76", "v77", "v78", "v79", "v80", "v81", "v82", "v83", "v84", "v85", "v86", "v87", "v88", "v89", "v90", "v91", "v92", "v93", "v94", "v95", "v96", "v97", "v98", "v99", "v100", "v101", "v102", "v103", "v104", "v105", "v106", "v107", "v108", "v109", "v110", "v111", "v112", "v113", "v114", "v115", "v116", "v117", "v118", "v119", "v120", "v121", "v122", "v123", "v124", "v125", "v126", "v127", "v132", "v133", "v134", "v135", "v136", "v137", "v138", "v139", "v140", "v141", "v142", "v143", "v144", "v145", "v146", "v147", "v148", "v149", "v150", "v151", "v152", "v153", "v154", "v155", "v156", "v157", "v158", "v159", "v160", "v161", "v162", "v163", "v164", "v165", "v166", "v167", "v168", "v169", "v170", "v171", "v172", "v173", "v174", "v175", "v176", "v177", "v178", "v179", "v180", "v181", "v182", "v183", "v184", "v185", "v186", "v187", "v188", "v189", "v190", "v191", "v192", "v193", "v194", "v195", "v196", "v197", "v198", "v199", "v200", "v201", "v202", "v203", "v204", "v205", "v206", "v207", "v208", "v209", "v210", "v211", "v212", "v213", "v214", "v215", "v216", "v217", "v218", "v219", "v220", "v221", "v222", "v223", "v224", "v225", "v226", "v227", "v228", "v229", "v230", "v231", "v232", "v233", "v234", "v235", "v236", "v237", "v238", "v239", "v240", "v241", "a0", "a1", "a2", "a3", "a4", "a5", "a6", "a7", "a8", "a9", "a10", "a11", "a12", "a13", "a14", "a15", "a16", "a17", "a18", "a19", "a20", "a21", "a22", "a23", "a24", "a25", "a26", "a27", "a28", "a29", "a30", "a31", "a32", "a33", "a34", "a35", "a36", "a37", "a38", "a39", "a40", "a41", "a42", "a43", "a44", "a45", "a46", "a47", "a48", "a49", "a50", "a51", "a52", "a53", "a54", "a55", "a56", "a57", "a58", "a59", "a60", "a61", "a62", "a63", "a64", "a65", "a66", "a67", "a68", "a69", "a70", "a71", "a72", "a73", "a74", "a75", "a76", "a77", "a78", "a79", "a80", "a81", "a82", "a83", "a84", "a85", "a86", "a87", "a88", "a89", "a90", "a91", "a92", "a93", "a94", "a95", "a96", "a97", "a98", "a99", "a100", "a101", "a102", "a103", "a104", "a105", "a106", "a107", "a108", "a109", "a110", "a111", "a112", "a113", "a114", "a115", "a116", "a117", "a118", "a119", "a120", "a121", "a122", "a123", "a124", "a125", "a126", "a127", "a128", "a129", "a130", "a131", "a132", "a133", "a134", "a135", "a136", "a137", "a138", "a139", "a140", "a141", "a142", "a143", "a144", "a145", "a146", "a147", "a148", "a149", "a150", "a151", "a152", "a153", "a154", "a155", "a156", "a157", "a158", "a159", "a160", "a161", "a162", "a163", "a164", "a165", "a166", "a167", "a168", "a169", "a170", "a171", "a172", "a173", "a174", "a175", "a176", "a177", "a178", "a179", "a180", "a181", "a182", "a183", "a184", "a185", "a186", "a187", "a188", "a189", "a190", "a191", "a192", "a193", "a194", "a195", "a196", "a197", "a198", "a199", "a200", "a201", "a202", "a203", "a204", "a205", "a206", "a207", "a208", "a209", "a210", "a211", "a212", "a213", "a214", "a215", "a216", "a217", "a218", "a219", "a220", "a221", "a222", "a223", "a224", "a225", "a226", "a227", "a228", "a229", "a230", "a231", "s20", "s21", "s22", "s23", "s24", "s25", "s26", "s27", "s28", "s29", "s30", "s31", "s60", "s61", "s62", "s63", "s64", "s65", "s66", "s67", "s68", "s69", "s70", "s71", "s72", "s73", "s74", "s75", "s76", "s77", "s78", "s79", "s80", "s81", "s82", "s83", "s84", "s85", "s86", "s87", "s88", "s89", "s90", "s91", "s93", "s94", "s95", "s99", "s100", "s101", "vcc", "scc", "memory");
}

}

extern "C" void kernel_launch(void* const* d_in, const int* in_sizes, int n_in, void* d_out, int out_size,
                              void* d_ws, size_t ws_size, hipStream_t stream) {
  if (n_in != 6 || ws_size < WS_END) return;
  const __hip_bfloat16* q = (const __hip_bfloat16*)d_in[0];
  const __hip_bfloat16* kc = (const __hip_bfloat16*)d_in[1];
  const __hip_bfloat16* vc = (const __hip_bfloat16*)d_in[2];
  const __hip_bfloat16* kt = (const __hip_bfloat16*)d_in[3];
  const __hip_bfloat16* vt = (const __hip_bfloat16*)d_in[4];
  const __hip_bfloat16* tm = (const __hip_bfloat16*)d_in[5];
  hipLaunchKernelGGL(tree_attn_main, dim3(B_ * HKV_ * 2), dim3(256), 0, stream, q, kc, vc, kt, vt, tm,
                     (__hip_bfloat16*)d_out, (char*)d_ws);
}
```

```cpp
#include <hip/hip_runtime.h>
#include <hip/hip_bf16.h>
#include <stdint.h>

#pragma clang diagnostic ignored "-Wint-to-pointer-cast"
#pragma clang diagnostic ignored "-Winline-asm"

namespace {

constexpr int B_ = 16, HQ_ = 32, HKV_ = 8, T_ = 32, C_ = 4096, D_ = 128;
constexpr int TILE = 64;
constexpr int KIMG = TILE * D_ * 2;
constexpr int STAGE_BYTES = 2 * KIMG;
constexpr int TREE_OFF = 4 * STAGE_BYTES;
constexpr int Q_OFF = 3 * STAGE_BYTES;
constexpr int LDS_BYTES = TREE_OFF + 2 * 13312;
constexpr unsigned C2 = 324, WIN = 52;

constexpr size_t WS_EPOCH = 0;
constexpr size_t WS_REF = WS_EPOCH + 128 * 128;
constexpr size_t WS_X = WS_REF + 16384, WS_X1 = 4 << 20;
constexpr size_t XW_BYTES = 4096 + 1024;
constexpr size_t WS_END = 0x680000 + WS_X;
static_assert(WS_X1 + WS_X + 128 * 4 * XW_BYTES <= WS_END, "");

typedef float    f32x4  __attribute__((ext_vector_type(4)));
typedef unsigned long long u64x8 __attribute__((ext_vector_type(8)));
typedef unsigned u32x8  __attribute__((ext_vector_type(8)));
typedef unsigned u32x4  __attribute__((ext_vector_type(4)));
typedef unsigned u32x2  __attribute__((ext_vector_type(2)));
typedef __attribute__((address_space(3))) f32x4 lds_f32x4;
typedef __attribute__((address_space(1))) unsigned long long gu64;

__global__ __launch_bounds__(256, 1)
void tree_attn_main(const __hip_bfloat16*, const __hip_bfloat16*, const __hip_bfloat16*, const __hip_bfloat16*,
                    const __hip_bfloat16*, const __hip_bfloat16*, __hip_bfloat16*, char*) {
  __shared__ __attribute__((aligned(1024))) char smem[LDS_BYTES];
  unsigned blk = blockIdx.x;
  u64x8 ka; unsigned pre, tch, voffK; int tid = threadIdx.x;
  asm volatile(
      "s_load_dwordx16 s[4:19], %[kp], 0x0\n"
      "s_getpc_b64 s[84:85]\n"
      "v_lshlrev_b32 v112, 4, %[tid]\n"
      "s_lshl_b32 s69, %[blk], 7\n"
      "s_and_b32 s69, s69, 0x7c00\n"
      "v_lshl_add_u32 v120, %[tid], 2, s69\n"
      "global_load_dword v128, v120, s[84:85]\n"
      "v_readfirstlane_b32 s77, %[tid]\n"
      "v_lshrrev_b32 %[vk], 4, %[tid]\n"
      "s_and_b32 s78, %[blk], 7\n"
      "v_xor_b32 %[vk], %[vk], %[tid]\n"
      "s_lshr_b32 s74, %[blk], 4\n"
      "v_and_b32 %[vk], 7, %[vk]\n"
      "s_lshl4_add_u32 s78, s78, s74\n"
      "v_lshrrev_b32 v120, 3, %[tid]\n"
      "s_lshl_b32 s78, s78, 15\n"
      "v_lshlrev_b32 %[vk], 4, %[vk]\n"
      "s_lshl_b32 s77, s77, 5\n"
      "v_lshl_or_b32 %[vk], v120, 8, %[vk]\n"
      "s_bfm_b32 s66, 1, 22\n"
      "s_bfm_b32 s67, 1, 17\n"
      "s_add_u32 m0, s77, 0x18000\n"
      "s_waitcnt lgkmcnt(0)\n"
      "s_mov_b32 s64, s4\n"
      "s_and_b32 s65, s5, 0xffff\n"
      "buffer_load_dwordx4 %[vk], s[64:67], s78 offen nt lds\n"
      "s_add_u32 m0, s77, 99200\n"
      "v_add_u32 v120, 0x2000, %[vk]\n"
      "buffer_load_dwordx4 %[vk], s[64:67], s78 offen offset:128 nt lds\n"
      "s_add_u32 m0, s77, 106496\n"
      "s_add_u32 s74, s78, 0x4000\n"
      "s_lshl_b32 s75, s78, 5\n"
      "buffer_load_dwordx4 v120, s[64:67], s78 offen nt lds\n"
      "s_add_u32 m0, s77, 107392\n"
      "s_nop 0\n"
      "buffer_load_dwordx4 v120, s[64:67], s78 offen offset:128 nt lds\n"
      "s_add_u32 m0, s77, 114688\n"
      "s_load_dword s92, s[6:7], s75\n"
      "s_load_dword s92, s[8:9], s75\n"
      "buffer_load_dwordx4 %[vk], s[64:67], s74 offen nt lds\n"
      "s_add_u32 m0, s77, 115584\n"
      "s_mov_b32 s84, s10\n"
      "s_and_b32 s85, s11, 0xffff\n"
      "buffer_load_dwordx4 %[vk], s[64:67], s74 offen offset:128 nt lds\n"
      "s_add_u32 m0, s77, 122880\n"
      "s_mov_b32 s86, 0x100000\n"
      "s_mov_b32 s87, 0x20000\n"
      "s_lshr_b32 s75, s78, 2\n"
      "buffer_load_dwordx4 v120, s[64:67], s74 offen nt lds\n"
      "s_add_u32 m0, s77, 123776\n"
      "s_lshr_b32 s76, s78, 18\n"
      "s_lshl_b32 s76, s76, 11\n"
      "buffer_load_dwordx4 v120, s[64:67], s74 offen offset:128 nt lds\n"
      "s_add_u32 m0, s77, 131072\n"
      "s_lshr_b32 s69, s77, 1\n"
      "buffer_load_dwordx4 %[vk], s[84:87], s75 offen nt lds\n"
      "s_add_u32 m0, s77, 131968\n"
      "s_cmp_gt_u32 s77, 2048\n"
      "buffer_load_dwordx4 %[vk], s[84:87], s75 offen offset:128 nt lds\n"
      "s_cbranch_scc1 NOMASK_%=\n"
      "s_add_u32 m0, s69, 140288\n"
      "s_mov_b32 s64, s14\n"
      "s_and_b32 s65, s15, 0xffff\n"
      "s_mov_b32 s66, 0x8000\n"
      "buffer_load_dwordx4 v112, s[64:67], s76 offen lds\n"
      "NOMASK_%=:\n"
      : "=&{s[4:19]}"(ka), "=&{v128}"(pre), "=&{s92}"(tch), [vk] "=&v"(voffK), [tid] "+v"(tid)
      : [kp] "s"(__builtin_amdgcn_kernarg_segment_ptr()), [blk] "s"(blk)
      : "v112", "v120", "s64", "s65", "s66", "s67", "s69", "s70", "s71", "s74", "s75", "s76", "s77", "s78", "s84", "s85", "s86", "s87", "s96", "s97", "scc", "memory");
  asm volatile("" : "+s"(blk));
  const int lane = tid & 63;
  const int wid = __builtin_amdgcn_readfirstlane(tid >> 6);
  const unsigned wbase = wid * 2048;
  const int rh = wid & 1, kh = wid >> 1;
  const int xg = blk & 7, jj = blk >> 3, bk = xg * 16 + (jj >> 1), split = jj & 1;
  const int c16 = lane & 15, g = lane >> 4;
  const unsigned lds0 = (unsigned)(uintptr_t)smem;

  const unsigned voffV = (unsigned)((8 * wid + (lane >> 3)) * 256 + (((lane & 7) ^ (g << 1)) << 4));
  const unsigned so0 = (unsigned)(bk * C_ + split * 2 * TILE) * (D_ * 2) + (bk & 1) * KIMG, inc = KIMG - 2 * (bk & 1) * KIMG;
#pragma unroll
  for (int j = 0; j < 4; ++j) *(lds_f32x4*)(lds0 + KIMG + 4096 * j + tid * 16) = f32x4{0.f, 0.f, 0.f, 0.f};
#pragma unroll
  for (int j = 0; j < 2; ++j) *(lds_f32x4*)(lds0 + STAGE_BYTES + KIMG + (1 - split) * 8192 + 4096 * j + tid * 16) = f32x4{0.f, 0.f, 0.f, 0.f};
  u32x8 ad;
#pragma unroll
  for (int j = 0; j < 2; ++j)
    ad[j] = lds0 + (unsigned)(2048 * (c16 >> 3) + 128 * (c16 & 7) + (((4 * j + g) ^ (c16 >> 1)) << 4) + kh * 8192);
  {
    const int qq = c16 >> 2, p = lane & 3;
#pragma unroll
    for (int j = 0; j < 4; ++j)
      ad[2 + j] = lds0 + (unsigned)(2048 * (g >> 1) + 128 * (4 * (g & 1) + qq) + kh * 8192 + KIMG +
                                    (((2 * (j ^ (2 * kh)) + (p >> 1)) ^ ((2 * (g & 1) + (qq >> 1)) << 1)) << 4) + 8 * (p & 1));
  }
  ad[6] = voffK; ad[7] = voffV + 128 * split;
  u32x2 ra = {(unsigned)(lane < 4 ? WS_REF + lane * 512 : 1 << 26), (unsigned)(WS_REF + (jj & (4 - 1)) * 512 + (lane & 7) * 4 + (wid << 26))};
  auto xarea = [&](int w) { return lds0 + ((0x8d802000u >> (8 * w)) & 255u) * 1024; };
  const unsigned pg = jj >> 2, px = pg - (pg > 160 / 32) + xg * 5;
  const unsigned n1 = 4 + max(px - 6 * (px * 43 >> 8), ((0x70000000u | 6u << 160 / 8) >> 4 * pg) & 7);
  asm volatile(
      "s_memrealtime s[82:83]\n"
      "s_mov_b32 s60, %[so0]\n"
      "s_mov_b32 s62, %[wb]\n"
      "v_add_u32 v120, 0x2000, v248\n"
      "s_and_b32 s80, %[fo], 0xffffff80\n"
      "v_mov_b32 v240, s80\n"
      "s_mov_b32 s22, 0x8000000\n"
      "s_mov_b32 s23, 0x20000\n"
      "s_mov_b32 s20, s6\n"
      "s_and_b32 s21, s7, 0xffff\n"
      "s_add_u32 m0, s62, 0\n"
      "s_nop 0\n"
      "buffer_load_dwordx4 v248, s[20:23], s60 offen nt lds\n"
      "s_mov_b32 s86, 0x100000\n"
      "s_mov_b32 s87, 0x20000\n"
      "s_lshr_b32 s75, %[qo], 2\n"
      "s_mov_b32 s84, s12\n"
      "s_and_b32 s85, s13, 0xffff\n"
      "v_mov_b64 v[216:217], 0\n"
      "v_mov_b64 v[218:219], 0\n"
      "v_xor_b32 v121, 0x80, v249\n"
      "v_add_u32 v122, 0x2000, v249\n"
      "s_add_u32 m0, s62, 896\n"
      "s_nop 0\n"
      "buffer_load_dwordx4 v248, s[20:23], s60 offen offset:128 nt lds\n"
      "v_mfma_f32_32x32x16_bf16 v[32:47], v[216:219], v[216:219], 0\n"
      "v_add_u32 v123, 0x2000, v121\n"
      "s_mov_b32 s61, %[so0]\n"
      "s_sub_u32 s78, 0x10000, %[inc]\n"
      "s_add_u32 s77, %[wb], 0x10000\n"
      "v_mfma_f32_32x32x16_bf16 v[48:63], v[216:219], v[216:219], 0\n"
      "s_mov_b32 s70, 300\n"
      "s_mov_b32 s80, 0x443212a\n"
      "s_mov_b32 s81, 0x0\n"
      "s_add_u32 m0, s62, 8192\n"
      "s_nop 0\n"
      "buffer_load_dwordx4 v120, s[20:23], s60 offen nt lds\n"
      "v_mfma_f32_32x32x16_bf16 v[64:79], v[216:219], v[216:219], 0\n"
      "s_mov_b32 s101, 1000\n"
      "s_mov_b32 s98, 109\n"
      "s_mov_b32 s93, 28\n"
      "s_mov_b32 s99, 21\n"
      "s_mov_b32 s100, 282\n"
      "v_mfma_f32_32x32x16_bf16 v[80:95], v[216:219], v[216:219], 0\n"
      "s_lshl_b32 s91, %[wb], 18\n"
      "s_sub_u32 s91, 0x10000000, s91\n"
      "s_add_u32 m0, s62, 9088\n"
      "s_nop 0\n"
      "buffer_load_dwordx4 v120, s[20:23], s60 offen offset:128 nt lds\n"
      "s_add_u32 s62, s62, 0x8000\n"
      "s_and_b32 s62, s62, 0x1ffff\n"
      "s_add_u32 s60, s60, %[inc]\n"
      "s_mov_b32 s94, 0x3e0293ee\n"
      "s_mov_b32 s95, 0xffff0000\n"
      "v_mfma_f32_32x32x16_bf16 v[192:207], v[216:219], v[216:219], 0\n"
      "s_mov_b32 s76, 0xd0289660\n"
      "v_mbcnt_lo_u32_b32 v112, -1, 0\n"
      "v_mbcnt_hi_u32_b32 v112, -1, v112\n"
      "v_lshrrev_b32 v115, 4, v112\n"
      "v_and_b32 v112, 15, v112\n"
      "v_lshlrev_b32 v115, 3, v115\n"
      "s_add_u32 m0, s62, 0\n"
      "s_nop 0\n"
      "buffer_load_dwordx4 v248, s[20:23], s60 offen nt lds\n"
      "v_lshl_add_u32 v115, v112, 6, v115\n"
      "v_add_u32 v115, 140288, v115\n"
      "s_mov_b32 s24, s8\n"
      "s_and_b32 s25, s9, 0xffff\n"
      "s_mov_b32 s26, 0x8000000\n"
      "s_mov_b32 s27, 0x20000\n"
      "s_mov_b32 s30, 0x688000\n"
      "s_mov_b32 s31, 0x20000\n"
      "v_mov_b32 v113, 0x3f803f80\n"
      "s_add_u32 m0, s62, 896\n"
      "s_nop 0\n"
      "buffer_load_dwordx4 v248, s[20:23], s60 offen offset:128 nt lds\n"
      "v_accvgpr_write_b32 a192, 0\n"
      "v_add_u32 v224, %[qoff], v242\n"
      "v_add_u32 v228, %[toff], v242\n"
      "v_xor_b32 v226, 0x400, v224\n"
      "v_xor_b32 v230, 0x400, v228\n"
      "v_and_b32 v118, 0xfffffbff, v224\n"
      "v_add_u32 v225, %[qoff], v243\n"
      "v_add_u32 v229, %[toff], v243\n"
      "v_xor_b32 v227, 0x400, v225\n"
      "s_add_u32 m0, s62, 8192\n"
      "s_nop 0\n"
      "buffer_load_dwordx4 v120, s[20:23], s60 offen nt lds\n"
      "v_xor_b32 v231, 0x400, v229\n"
      "v_and_b32 v119, 0xfffffbff, v225\n"
      "v_cmp_eq_u32 vcc, 0, v112\n"
      "v_mfma_f32_32x32x16_bf16 a[128:143], v[216:219], v[216:219], 0\n"
      "s_nop 0\n"
      "v_cndmask_b32 v114, 0, v113, vcc\n"
      "v_accvgpr_write_b32 a196, v114\n"
      "v_accvgpr_write_b32 a197, v114\n"
      "v_accvgpr_write_b32 a198, v114\n"
      "s_add_u32 m0, s62, 9088\n"
      "s_nop 0\n"
      "buffer_load_dwordx4 v120, s[20:23], s60 offen offset:128 nt lds\n"
      "v_accvgpr_write_b32 a199, v114\n"
      "v_cmp_eq_u32 vcc, 4, v112\n"
      "v_mfma_f32_32x32x16_bf16 a[144:159], v[216:219], v[216:219], 0\n"
      "s_nop 0\n"
      "v_cndmask_b32 v114, 0, v113, vcc\n"
      "v_accvgpr_write_b32 a200, v114\n"
      "v_accvgpr_write_b32 a201, v114\n"
      "v_accvgpr_write_b32 a202, v114\n"
      "v_accvgpr_write_b32 a203, v114\n"
      "s_add_u32 m0, %[tv], 0\n"
      "s_nop 0\n"
      "buffer_load_dwordx4 v249, s[84:87], s75 offen nt lds\n"
      "v_cmp_eq_u32 vcc, 8, v112\n"
      "v_mfma_f32_32x32x16_bf16 a[160:175], v[216:219], v[216:219], 0\n"
      "s_nop 0\n"
      "v_cndmask_b32 v114, 0, v113, vcc\n"
      "v_accvgpr_write_b32 a204, v114\n"
      "v_accvgpr_write_b32 a205, v114\n"
      "v_accvgpr_write_b32 a206, v114\n"
      "v_accvgpr_write_b32 a207, v114\n"
      "v_cmp_eq_u32 vcc, 12, v112\n"
      "s_add_u32 m0, %[tv], 1024\n"
      "s_nop 0\n"
      "buffer_load_dwordx4 v121, s[84:87], s75 offen nt lds\n"
      "v_mfma_f32_32x32x16_bf16 a[176:191], v[216:219], v[216:219], 0\n"
      "s_nop 0\n"
      "v_cndmask_b32 v114, 0, v113, vcc\n"
      "v_accvgpr_write_b32 a208, v114\n"
      "v_accvgpr_write_b32 a209, v114\n"
      "v_accvgpr_write_b32 a210, v114\n"
      "v_accvgpr_write_b32 a211, v114\n"
      "s_add_u32 s62, s62, 0x8000\n"
      "s_and_b32 s62, s62, 0x1ffff\n"
      "s_add_u32 s60, s60, s78\n"
      "s_cmp_lt_u32 %[rep], 128\n"
      "s_cselect_b32 s89, 0x2ffe, 0\n"
      "s_or_b32 s89, s89, 0x2aae\n"
      "s_waitcnt vmcnt(10)\n"
      "s_barrier\n"
      "s_memrealtime s[68:69]\n"
      "ds_read_b64 v[208:209], v115 offset:0\n"
      "ds_read_b64 v[210:211], v115 offset:32\n"
      "ds_read_b64 v[212:213], v115 offset:1024\n"
      "ds_read_b64 v[214:215], v115 offset:1056\n"
      "ds_read_b128 a[64:67], v228 offset:0\n"
      "ds_read_b128 a[68:71], v228 offset:4096\n"
      "ds_read_b128 v[128:131], v224 offset:0\n"
      "ds_read_b128 v[132:135], v224 offset:4096\n"
      "ds_read_b128 v[136:139], v224 offset:8192\n"
      "ds_read_b128 v[140:143], v224 offset:12288\n"
      "ds_read_b128 a[72:75], v229 offset:0\n"
      "ds_read_b128 a[76:79], v229 offset:4096\n"
      "ds_read_b128 v[144:147], v225 offset:0\n"
      "ds_read_b128 v[148:151], v225 offset:4096\n"
      "s_waitcnt lgkmcnt(10)\n"
      "ds_read_b128 v[152:155], v225 offset:8192\n"
      "ds_read_b128 v[156:159], v225 offset:12288\n"
      "v_lshlrev_b32 v96, 16, v208\n"
      "v_and_b32 v97, 0xffff0000, v208\n"
      "v_lshlrev_b32 v98, 16, v209\n"
      "v_and_b32 v99, 0xffff0000, v209\n"
      "v_lshlrev_b32 v100, 16, v210\n"
      "v_and_b32 v101, 0xffff0000, v210\n"
      "v_lshlrev_b32 v102, 16, v211\n"
      "v_and_b32 v103, 0xffff0000, v211\n"
      "v_lshlrev_b32 v104, 16, v212\n"
      "v_and_b32 v105, 0xffff0000, v212\n"
      "v_lshlrev_b32 v106, 16, v213\n"
      "v_and_b32 v107, 0xffff0000, v213\n"
      "v_lshlrev_b32 v108, 16, v214\n"
      "v_and_b32 v109, 0xffff0000, v214\n"
      "v_lshlrev_b32 v110, 16, v215\n"
      "v_and_b32 v111, 0xffff0000, v215\n"
      "v_fma_f32 v96, -v96, s76, s76\n"
      "v_fma_f32 v97, -v97, s76, s76\n"
      "v_fma_f32 v98, -v98, s76, s76\n"
      "v_fma_f32 v99, -v99, s76, s76\n"
      "v_fma_f32 v100, -v100, s76, s76\n"
      "v_fma_f32 v101, -v101, s76, s76\n"
      "v_fma_f32 v102, -v102, s76, s76\n"
      "v_fma_f32 v103, -v103, s76, s76\n"
      "v_fma_f32 v104, -v104, s76, s76\n"
      "v_fma_f32 v105, -v105, s76, s76\n"
      "v_fma_f32 v106, -v106, s76, s76\n"
      "v_fma_f32 v107, -v107, s76, s76\n"
      "v_fma_f32 v108, -v108, s76, s76\n"
      "v_fma_f32 v109, -v109, s76, s76\n"
      "v_fma_f32 v110, -v110, s76, s76\n"
      "v_fma_f32 v111, -v111, s76, s76\n"
      "s_mov_b32 s86, 109\n"
      "s_mov_b32 s87, 0\n"
      "s_waitcnt lgkmcnt(0)\n"
      "ds_read_b128 a[80:83], v230 offset:0\n"
      "ds_read_b128 a[84:87], v230 offset:4096\n"
      "ds_read_b128 a[32:35], v226 offset:0\n"
      "ds_read_b128 a[36:39], v226 offset:4096\n"
      "ds_read_b128 a[40:43], v226 offset:8192\n"
      "ds_read_b128 a[44:47], v226 offset:12288\n"
      "ds_read_b128 a[88:91], v231 offset:0\n"
      "ds_read_b128 a[92:95], v231 offset:4096\n"
      "ds_read_b128 a[48:51], v227 offset:0\n"
      "ds_read_b128 a[52:55], v227 offset:4096\n"
      "ds_read_b128 a[56:59], v227 offset:8192\n"
      "ds_read_b128 a[60:63], v227 offset:12288\n"
      "global_load_dwordx2 v[240:241], v240, s[18:19] sc1\n"
      "s_mov_b32 s28, s18\n"
      "s_and_b32 s29, s19, 0xffff\n"
      "s_cmp_lt_u32 %[lead], 32\n"
      "s_cbranch_scc0 NOLEAD_%=\n"
      "v_mov_b32 v112, s68\n"
      "buffer_store_dword v112, v250, s[28:31], %[lead] offen sc1\n"
      "NOLEAD_%=:\n"
      "v_mfma_f32_16x16x32_bf16 v[160:163], a[64:67], v[128:131], v[96:99]\n"
      "s_mov_b32 s83, 0\n"
      "v_lshlrev_b32 v232, 16, v128\n"
      "v_and_b32 v233, s95, v128\n"
      "v_mfma_f32_16x16x32_bf16 v[168:171], a[64:67], v[132:135], v[104:107]\n"
      "s_sub_u32 s67, s68, s82\n"
      "v_lshlrev_b32 v234, 16, v129\n"
      "v_and_b32 v235, s95, v129\n"
      "v_mul_f32 v232, s94, v232\n"
      "v_mul_f32 v233, s94, v233\n"
      "v_mfma_f32_16x16x32_bf16 v[176:179], a[64:67], v[136:139], v[96:99]\n"
      "s_min_u32 s67, s67, 140\n"
      "v_lshlrev_b32 v236, 16, v130\n"
      "v_and_b32 v237, s95, v130\n"
      "v_mul_f32 v234, s94, v234\n"
      "v_mul_f32 v235, s94, v235\n"
      "v_cvt_pk_bf16_f32 v0, v232, v233\n"
      "v_mfma_f32_16x16x32_bf16 v[184:187], a[64:67], v[140:143], v[104:107]\n"
      "s_add_u32 s66, s82, s67\n"
      "v_lshlrev_b32 v238, 16, v131\n"
      "v_and_b32 v239, s95, v131\n"
      "v_mul_f32 v236, s94, v236\n"
      "v_mul_f32 v237, s94, v237\n"
      "v_cvt_pk_bf16_f32 v1, v234, v235\n"
      "v_mfma_f32_16x16x32_bf16 v[164:167], a[68:71], v[128:131], v[100:103]\n"
      "s_add_u32 s66, s66, 108\n"
      "v_lshlrev_b32 v232, 16, v132\n"
      "v_and_b32 v233, s95, v132\n"
      "v_mul_f32 v238, s94, v238\n"
      "v_mul_f32 v239, s94, v239\n"
      "v_cvt_pk_bf16_f32 v2, v236, v237\n"
      "v_mfma_f32_16x16x32_bf16 v[172:175], a[68:71], v[132:135], v[108:111]\n"
      "s_sub_u32 s71, s68, 400\n"
      "v_lshlrev_b32 v234, 16, v133\n"
      "v_and_b32 v235, s95, v133\n"
      "v_mul_f32 v232, s94, v232\n"
      "v_mul_f32 v233, s94, v233\n"
      "v_cvt_pk_bf16_f32 v3, v238, v239\n"
      "v_mfma_f32_16x16x32_bf16 v[180:183], a[68:71], v[136:139], v[100:103]\n"
      "s_mov_b32 s68, s66\n"
      "v_lshlrev_b32 v236, 16, v134\n"
      "v_and_b32 v237, s95, v134\n"
      "v_mul_f32 v234, s94, v234\n"
      "v_mul_f32 v235, s94, v235\n"
      "v_cvt_pk_bf16_f32 v4, v232, v233\n"
      "v_mfma_f32_16x16x32_bf16 v[188:191], a[68:71], v[140:143], v[108:111]\n"
      "s_mov_b32 s82, s66\n"
      "v_lshlrev_b32 v238, 16, v135\n"
      "v_and_b32 v239, s95, v135\n"
      "v_mul_f32 v236, s94, v236\n"
      "v_mul_f32 v237, s94, v237\n"
      "v_cvt_pk_bf16_f32 v5, v234, v235\n"
      "v_mfma_f32_16x16x32_bf16 v[160:163], a[72:75], v[144:147], v[160:163]\n"
      "v_lshlrev_b32 v232, 16, v136\n"
      "v_and_b32 v233, s95, v136\n"
      "v_mul_f32 v238, s94, v238\n"
      "v_mul_f32 v239, s94, v239\n"
      "v_cvt_pk_bf16_f32 v6, v236, v237\n"
      "v_mfma_f32_16x16x32_bf16 v[168:171], a[72:75], v[148:151], v[168:171]\n"
      "v_lshlrev_b32 v234, 16, v137\n"
      "v_and_b32 v235, s95, v137\n"
      "v_mul_f32 v232, s94, v232\n"
      "v_mul_f32 v233, s94, v233\n"
      "v_cvt_pk_bf16_f32 v7, v238, v239\n"
      "s_waitcnt lgkmcnt(0)\n"
      "s_barrier\n"
      "v_mfma_f32_16x16x32_bf16 v[176:179], a[72:75], v[152:155], v[176:179]\n"
      "v_lshlrev_b32 v236, 16, v138\n"
      "v_and_b32 v237, s95, v138\n"
      "v_mul_f32 v234, s94, v234\n"
      "v_mul_f32 v235, s94, v235\n"
      "v_cvt_pk_bf16_f32 v8, v232, v233\n"
      "ds_write_b128 v224, v[0:3] offset:0\n"
      "v_mfma_f32_16x16x32_bf16 v[184:187], a[72:75], v[156:159], v[184:187]\n"
      "s_add_u32 m0, s62, 0\n"
      "v_lshlrev_b32 v238, 16, v139\n"
      "v_and_b32 v239, s95, v139\n"
      "v_mul_f32 v236, s94, v236\n"
      "v_mul_f32 v237, s94, v237\n"
      "v_cvt_pk_bf16_f32 v9, v234, v235\n"
      "v_mfma_f32_16x16x32_bf16 v[164:167], a[76:79], v[144:147], v[164:167]\n"
      "v_lshlrev_b32 v232, 16, v140\n"
      "v_and_b32 v233, s95, v140\n"
      "v_mul_f32 v238, s94, v238\n"
      "v_mul_f32 v239, s94, v239\n"
      "v_cvt_pk_bf16_f32 v10, v236, v237\n"
      "buffer_load_dwordx4 v248, s[20:23], s60 offen nt lds\n"
      "ds_write_b128 v224, v[4:7] offset:4096\n"
      "v_mfma_f32_16x16x32_bf16 v[172:175], a[76:79], v[148:151], v[172:175]\n"
      "s_add_u32 m0, s62, 896\n"
      "v_lshlrev_b32 v234, 16, v141\n"
      "v_and_b32 v235, s95, v141\n"
      "v_mul_f32 v232, s94, v232\n"
      "v_mul_f32 v233, s94, v233\n"
      "v_cvt_pk_bf16_f32 v11, v238, v239\n"
      "v_mfma_f32_16x16x32_bf16 v[180:183], a[76:79], v[152:155], v[180:183]\n"
      "v_lshlrev_b32 v236, 16, v142\n"
      "v_and_b32 v237, s95, v142\n"
      "v_mul_f32 v234, s94, v234\n"
      "v_mul_f32 v235, s94, v235\n"
      "v_cvt_pk_bf16_f32 v12, v232, v233\n"
      "buffer_load_dwordx4 v248, s[20:23], s60 offen offset:128 nt lds\n"
      "ds_write_b128 v224, v[8:11] offset:8192\n"
      "v_mfma_f32_16x16x32_bf16 v[188:191], a[76:79], v[156:159], v[188:191]\n"
      "s_add_u32 m0, s62, 8192\n"
      "v_lshlrev_b32 v238, 16, v143\n"
      "v_and_b32 v239, s95, v143\n"
      "v_mul_f32 v236, s94, v236\n"
      "v_mul_f32 v237, s94, v237\n"
      "v_cvt_pk_bf16_f32 v13, v234, v235\n"
      "v_mfma_f32_16x16x32_bf16 v[160:163], a[80:83], a[32:35], v[160:163]\n"
      "v_lshlrev_b32 v232, 16, v144\n"
      "v_and_b32 v233, s95, v144\n"
      "v_mul_f32 v238, s94, v238\n"
      "v_mul_f32 v239, s94, v239\n"
      "v_cvt_pk_bf16_f32 v14, v236, v237\n"
      "buffer_load_dwordx4 v120, s[20:23], s60 offen nt lds\n"
      "v_mfma_f32_16x16x32_bf16 v[168:171], a[80:83], a[36:39], v[168:171]\n"
      "s_add_u32 m0, s62, 9088\n"
      "v_lshlrev_b32 v234, 16, v145\n"
      "v_and_b32 v235, s95, v145\n"
      "v_mul_f32 v232, s94, v232\n"
      "v_mul_f32 v233, s94, v233\n"
      "v_cvt_pk_bf16_f32 v15, v238, v239\n"
      "ds_write_b128 v224, v[12:15] offset:12288\n"
      "v_mfma_f32_16x16x32_bf16 v[176:179], a[80:83], a[40:43], v[176:179]\n"
      "s_add_u32 s62, s62, 0x8000\n"
      "v_lshlrev_b32 v236, 16, v146\n"
      "v_and_b32 v237, s95, v146\n"
      "v_mul_f32 v234, s94, v234\n"
      "v_mul_f32 v235, s94, v235\n"
      "v_cvt_pk_bf16_f32 v16, v232, v233\n"
      "buffer_load_dwordx4 v120, s[20:23], s60 offen offset:128 nt lds\n"
      "v_mfma_f32_16x16x32_bf16 v[184:187], a[80:83], a[44:47], v[184:187]\n"
      "s_and_b32 s62, s62, 0x1ffff\n"
      "v_lshlrev_b32 v238, 16, v147\n"
      "v_and_b32 v239, s95, v147\n"
      "v_mul_f32 v236, s94, v236\n"
      "v_mul_f32 v237, s94, v237\n"
      "v_cvt_pk_bf16_f32 v17, v234, v235\n"
      "v_mfma_f32_16x16x32_bf16 v[164:167], a[84:87], a[32:35], v[164:167]\n"
      "s_add_u32 s60, s60, %[inc]\n"
      "v_lshlrev_b32 v232, 16, v148\n"
      "v_and_b32 v233, s95, v148\n"
      "v_mul_f32 v238, s94, v238\n"
      "v_mul_f32 v239, s94, v239\n"
      "v_cvt_pk_bf16_f32 v18, v236, v237\n"
      "v_mfma_f32_16x16x32_bf16 v[172:175], a[84:87], a[36:39], v[172:175]\n"
      "v_lshlrev_b32 v234, 16, v149\n"
      "v_and_b32 v235, s95, v149\n"
      "v_mul_f32 v232, s94, v232\n"
      "v_mul_f32 v233, s94, v233\n"
      "v_cvt_pk_bf16_f32 v19, v238, v239\n"
      "ds_write_b128 v225, v[16:19] offset:0\n"
      "v_mfma_f32_16x16x32_bf16 v[180:183], a[84:87], a[40:43], v[180:183]\n"
      "v_lshlrev_b32 v236, 16, v150\n"
      "v_and_b32 v237, s95, v150\n"
      "v_mul_f32 v234, s94, v234\n"
      "v_mul_f32 v235, s94, v235\n"
      "v_cvt_pk_bf16_f32 v20, v232, v233\n"
      "v_mfma_f32_16x16x32_bf16 v[188:191], a[84:87], a[44:47], v[188:191]\n"
      "v_lshlrev_b32 v238, 16, v151\n"
      "v_and_b32 v239, s95, v151\n"
      "v_mul_f32 v236, s94, v236\n"
      "v_mul_f32 v237, s94, v237\n"
      "v_cvt_pk_bf16_f32 v21, v234, v235\n"
      "v_mfma_f32_16x16x32_bf16 v[160:163], a[88:91], a[48:51], v[160:163]\n"
      "v_lshlrev_b32 v232, 16, v152\n"
      "v_and_b32 v233, s95, v152\n"
      "v_mul_f32 v238, s94, v238\n"
      "v_mul_f32 v239, s94, v239\n"
      "v_cvt_pk_bf16_f32 v22, v236, v237\n"
      "v_mfma_f32_16x16x32_bf16 v[168:171], a[88:91], a[52:55], v[168:171]\n"
      "v_lshlrev_b32 v234, 16, v153\n"
      "v_and_b32 v235, s95, v153\n"
      "v_mul_f32 v232, s94, v232\n"
      "v_mul_f32 v233, s94, v233\n"
      "v_cvt_pk_bf16_f32 v23, v238, v239\n"
      "ds_write_b128 v225, v[20:23] offset:4096\n"
      "v_mfma_f32_16x16x32_bf16 v[176:179], a[88:91], a[56:59], v[176:179]\n"
      "v_lshlrev_b32 v236, 16, v154\n"
      "v_and_b32 v237, s95, v154\n"
      "v_mul_f32 v234, s94, v234\n"
      "v_mul_f32 v235, s94, v235\n"
      "v_cvt_pk_bf16_f32 v24, v232, v233\n"
      "v_mfma_f32_16x16x32_bf16 v[184:187], a[88:91], a[60:63], v[184:187]\n"
      "v_lshlrev_b32 v238, 16, v155\n"
      "v_and_b32 v239, s95, v155\n"
      "v_mul_f32 v236, s94, v236\n"
      "v_mul_f32 v237, s94, v237\n"
      "v_cvt_pk_bf16_f32 v25, v234, v235\n"
      "v_mfma_f32_16x16x32_bf16 v[164:167], a[92:95], a[48:51], v[164:167]\n"
      "v_lshlrev_b32 v232, 16, v156\n"
      "v_and_b32 v233, s95, v156\n"
      "v_mul_f32 v238, s94, v238\n"
      "v_mul_f32 v239, s94, v239\n"
      "v_cvt_pk_bf16_f32 v26, v236, v237\n"
      "v_mfma_f32_16x16x32_bf16 v[172:175], a[92:95], a[52:55], v[172:175]\n"
      "v_lshlrev_b32 v234, 16, v157\n"
      "v_and_b32 v235, s95, v157\n"
      "v_mul_f32 v232, s94, v232\n"
      "v_mul_f32 v233, s94, v233\n"
      "v_cvt_pk_bf16_f32 v27, v238, v239\n"
      "ds_write_b128 v225, v[24:27] offset:8192\n"
      "v_mfma_f32_16x16x32_bf16 v[180:183], a[92:95], a[56:59], v[180:183]\n"
      "v_lshlrev_b32 v236, 16, v158\n"
      "v_and_b32 v237, s95, v158\n"
      "v_mul_f32 v234, s94, v234\n"
      "v_mul_f32 v235, s94, v235\n"
      "v_cvt_pk_bf16_f32 v28, v232, v233\n"
      "v_mfma_f32_16x16x32_bf16 v[188:191], a[92:95], a[60:63], v[188:191]\n"
      "v_lshlrev_b32 v238, 16, v159\n"
      "v_and_b32 v239, s95, v159\n"
      "v_mul_f32 v236, s94, v236\n"
      "v_mul_f32 v237, s94, v237\n"
      "v_cvt_pk_bf16_f32 v29, v234, v235\n"
      "v_mul_f32 v238, s94, v238\n"
      "v_mul_f32 v239, s94, v239\n"
      "v_cvt_pk_bf16_f32 v30, v236, v237\n"
      "v_cvt_pk_bf16_f32 v31, v238, v239\n"
      "ds_write_b128 v225, v[28:31] offset:12288\n"
      "v_max3_f32 v112, v160, v161, v162\n"
      "v_max3_f32 v113, v168, v169, v170\n"
      "v_max3_f32 v114, v176, v177, v178\n"
      "v_max3_f32 v115, v184, v185, v186\n"
      "v_max3_f32 v112, v112, v163, v164\n"
      "v_max3_f32 v113, v113, v171, v172\n"
      "v_max3_f32 v114, v114, v179, v180\n"
      "v_max3_f32 v115, v115, v187, v188\n"
      "v_max3_f32 v112, v112, v165, v166\n"
      "v_max3_f32 v113, v113, v173, v174\n"
      "v_max3_f32 v114, v114, v181, v182\n"
      "v_max3_f32 v115, v115, v189, v190\n"
      "s_waitcnt lgkmcnt(0)\n"
      "s_barrier\n"
      "ds_read_b128 a[0:3], v118 offset:0\n"
      "ds_read_b128 a[4:7], v118 offset:4096\n"
      "ds_read_b128 a[8:11], v118 offset:8192\n"
      "ds_read_b128 a[12:15], v118 offset:12288\n"
      "ds_read_b128 a[16:19], v119 offset:0\n"
      "ds_read_b128 a[20:23], v119 offset:4096\n"
      "ds_read_b128 a[24:27], v119 offset:8192\n"
      "ds_read_b128 a[28:31], v119 offset:12288\n"
      "ds_read_b128 a[32:35], v118 offset:1024\n"
      "ds_read_b128 a[36:39], v118 offset:5120\n"
      "ds_read_b128 a[40:43], v118 offset:9216\n"
      "ds_read_b128 a[44:47], v118 offset:13312\n"
      "v_max_f32 v112, v112, v167\n"
      "v_max_f32 v113, v113, v175\n"
      "v_max_f32 v114, v114, v183\n"
      "v_max_f32 v115, v115, v191\n"
      "v_mov_b32 v208, v112\n"
      "v_mov_b32 v209, v113\n"
      "v_mov_b32 v210, v114\n"
      "v_mov_b32 v211, v115\n"
      "v_permlane16_swap_b32 v112, v208\n"
      "v_permlane16_swap_b32 v113, v209\n"
      "v_permlane16_swap_b32 v114, v210\n"
      "v_permlane16_swap_b32 v115, v211\n"
      "s_waitcnt lgkmcnt(8)\n"
      "ds_read_b128 a[48:51], v119 offset:1024\n"
      "ds_read_b128 a[52:55], v119 offset:5120\n"
      "ds_read_b128 a[56:59], v119 offset:9216\n"
      "ds_read_b128 a[60:63], v119 offset:13312\n"
      "v_max_f32 v112, v112, v208\n"
      "v_max_f32 v113, v113, v209\n"
      "v_max_f32 v114, v114, v210\n"
      "v_max_f32 v115, v115, v211\n"
      "v_mov_b32 v208, v112\n"
      "v_mov_b32 v209, v113\n"
      "v_mov_b32 v210, v114\n"
      "v_mov_b32 v211, v115\n"
      "v_permlane32_swap_b32 v112, v208\n"
      "v_permlane32_swap_b32 v113, v209\n"
      "v_permlane32_swap_b32 v114, v210\n"
      "v_permlane32_swap_b32 v115, v211\n"
      "v_max_f32 v112, v112, v208\n"
      "v_max_f32 v113, v113, v209\n"
      "v_max_f32 v114, v114, v210\n"
      "v_max_f32 v115, v115, v211\n"
      "v_mov_b32 v116, 0xc1800000\n"
      "v_fmamk_f32 v112, v112, 0xbe0293ee, v116\n"
      "v_fmamk_f32 v113, v113, 0xbe0293ee, v116\n"
      "v_fmamk_f32 v114, v114, 0xbe0293ee, v116\n"
      "v_fmamk_f32 v115, v115, 0xbe0293ee, v116\n"
      "v_floor_f32 v112, v112\n"
      "v_floor_f32 v113, v113\n"
      "v_floor_f32 v114, v114\n"
      "v_floor_f32 v115, v115\n"
      "s_waitcnt vmcnt(11) lgkmcnt(0)\n"
      "s_barrier\n"
      "ds_read_b128 a[64:67], v242 offset:0\n"
      "ds_read_b128 a[68:71], v242 offset:4096\n"
      "ds_read_b128 a[72:75], v243 offset:0\n"
      "ds_read_b128 a[76:79], v243 offset:4096\n"
      "ds_read_b128 a[80:83], v242 offset:1024\n"
      "ds_read_b128 a[84:87], v242 offset:5120\n"
      "ds_read_b128 a[88:91], v243 offset:1024\n"
      "ds_read_b128 a[92:95], v243 offset:5120\n"
      "v_min_f32 v224, 0x7f000000, v112\n"
      "v_min_f32 v228, 0x7f000000, v113\n"
      "v_min_f32 v232, 0x7f000000, v114\n"
      "v_min_f32 v236, 0x7f000000, v115\n"
      "v_add_f32 v112, %[neg], v224\n"
      "v_add_f32 v113, %[neg], v228\n"
      "v_add_f32 v114, %[neg], v232\n"
      "v_add_f32 v115, %[neg], v236\n"
      "v_mov_b32 v225, v224\n"
      "v_mov_b32 v229, v228\n"
      "v_mov_b32 v233, v232\n"
      "v_mov_b32 v237, v236\n"
      "v_mov_b64 v[226:227], v[224:225]\n"
      "v_mov_b64 v[230:231], v[228:229]\n"
      "v_mov_b64 v[234:235], v[232:233]\n"
      "v_mov_b64 v[238:239], v[236:237]\n"
      "s_mov_b32 s95, s94\n"
      "s_add_u32 m0, s77, 16384\n"
      "v_pk_fma_f32 v[160:161], v[160:161], s[94:95], v[112:113] op_sel:[0,0,0] op_sel_hi:[1,1,0]\n"
      "buffer_load_dwordx4 v249, s[24:27], s61 offen nt lds\n"
      "v_pk_fma_f32 v[162:163], v[162:163], s[94:95], v[112:113] op_sel:[0,0,0] op_sel_hi:[1,1,0]\n"
      "v_pk_fma_f32 v[164:165], v[164:165], s[94:95], v[112:113] op_sel:[0,0,0] op_sel_hi:[1,1,0]\n"
      "v_pk_fma_f32 v[166:167], v[166:167], s[94:95], v[112:113] op_sel:[0,0,0] op_sel_hi:[1,1,0]\n"
      "s_add_u32 m0, s77, 17408\n"
      "v_pk_fma_f32 v[168:169], v[168:169], s[94:95], v[112:113] op_sel:[0,0,1] op_sel_hi:[1,1,1]\n"
      "buffer_load_dwordx4 v121, s[24:27], s61 offen nt lds\n"
      "v_pk_fma_f32 v[170:171], v[170:171], s[94:95], v[112:113] op_sel:[0,0,1] op_sel_hi:[1,1,1]\n"
      "v_pk_fma_f32 v[172:173], v[172:173], s[94:95], v[112:113] op_sel:[0,0,1] op_sel_hi:[1,1,1]\n"
      "v_pk_fma_f32 v[174:175], v[174:175], s[94:95], v[112:113] op_sel:[0,0,1] op_sel_hi:[1,1,1]\n"
      "s_add_u32 m0, s77, 24576\n"
      "v_pk_fma_f32 v[176:177], v[176:177], s[94:95], v[114:115] op_sel:[0,0,0] op_sel_hi:[1,1,0]\n"
      "buffer_load_dwordx4 v122, s[24:27], s61 offen nt lds\n"
      "v_pk_fma_f32 v[178:179], v[178:179], s[94:95], v[114:115] op_sel:[0,0,0] op_sel_hi:[1,1,0]\n"
      "v_pk_fma_f32 v[180:181], v[180:181], s[94:95], v[114:115] op_sel:[0,0,0] op_sel_hi:[1,1,0]\n"
      "v_pk_fma_f32 v[182:183], v[182:183], s[94:95], v[114:115] op_sel:[0,0,0] op_sel_hi:[1,1,0]\n"
      "s_add_u32 m0, s77, 25600\n"
      "v_pk_fma_f32 v[184:185], v[184:185], s[94:95], v[114:115] op_sel:[0,0,1] op_sel_hi:[1,1,1]\n"
      "buffer_load_dwordx4 v123, s[24:27], s61 offen nt lds\n"
      "v_pk_fma_f32 v[186:187], v[186:187], s[94:95], v[114:115] op_sel:[0,0,1] op_sel_hi:[1,1,1]\n"
      "v_pk_fma_f32 v[188:189], v[188:189], s[94:95], v[114:115] op_sel:[0,0,1] op_sel_hi:[1,1,1]\n"
      "v_pk_fma_f32 v[190:191], v[190:191], s[94:95], v[114:115] op_sel:[0,0,1] op_sel_hi:[1,1,1]\n"
      "s_add_u32 s77, s77, 0x8000\n"
      "s_and_b32 s77, s77, 0x1ffff\n"
      "s_add_u32 s61, s61, %[inc]\n"
      "v_exp_f32 v112, v160\n"
      "v_exp_f32 v113, v161\n"
      "v_exp_f32 v114, v162\n"
      "v_cvt_pk_bf16_f32 v208, v112, v113\n"
      "v_exp_f32 v115, v163\n"
      "v_exp_f32 v112, v164\n"
      "v_cvt_pk_bf16_f32 v209, v114, v115\n"
      "v_exp_f32 v113, v165\n"
      "v_or_b32 v116, v208, v209\n"
      "v_exp_f32 v114, v166\n"
      "v_cvt_pk_bf16_f32 v210, v112, v113\n"
      "v_exp_f32 v115, v167\n"
      "v_exp_f32 v112, v168\n"
      "v_cvt_pk_bf16_f32 v211, v114, v115\n"
      "v_exp_f32 v113, v169\n"
      "v_or3_b32 v116, v116, v210, v211\n"
      "v_exp_f32 v114, v170\n"
      "v_cvt_pk_bf16_f32 v212, v112, v113\n"
      "v_exp_f32 v115, v171\n"
      "v_exp_f32 v112, v172\n"
      "v_cvt_pk_bf16_f32 v213, v114, v115\n"
      "v_exp_f32 v113, v173\n"
      "v_or3_b32 v116, v116, v212, v213\n"
      "s_waitcnt lgkmcnt(0)\n"
      "v_cvt_pk_bf16_f32 v214, v112, v113\n"
      "s_sub_u32 s63, %[n1], 1\n"
      "s_branch ENTRY_%=\n"
      "LOOP1_%=:\n"
      "s_waitcnt vmcnt(12) lgkmcnt(0)\n"
      "s_sub_u32 s67, s68, s90\n"
      "s_cmp_lt_i32 s67, 0\n"
      "s_cbranch_scc1 GW0_%=\n"
      "GX0_%=:\n"
      "s_barrier\n"
      "v_mfma_f32_16x16x32_bf16 v[40:43], a[96:99], v[216:219], v[40:43]\n"
      "s_memrealtime s[68:69]\n"
      "buffer_load_dword v250, v251, s[28:31], 0 offen sc1\n"
      "ds_read_b128 a[64:67], v242 offset:0\n"
      "v_exp_f32 v114, v166\n"
      "v_mfma_f32_16x16x32_bf16 v[44:47], a[96:99], v[220:223], v[44:47]\n"
      "s_add_u32 m0, s77, 16384\n"
      "ds_read_b128 a[68:71], v242 offset:4096\n"
      "ds_read_b128 a[72:75], v243 offset:0\n"
      "v_mfma_f32_16x16x32_bf16 v[60:63], a[100:103], v[220:223], v[60:63]\n"
      "buffer_load_dwordx4 v249, s[24:27], s61 offen nt lds\n"
      "v_exp_f32 v115, v167\n"
      "v_mfma_f32_16x16x32_bf16 v[56:59], a[100:103], v[216:219], v[56:59]\n"
      "s_add_u32 m0, s77, 17408\n"
      "ds_read_b128 a[76:79], v243 offset:4096\n"
      "v_cvt_pk_bf16_f32 v211, v114, v115\n"
      "v_mfma_f32_16x16x32_bf16 a[136:139], a[104:107], v[216:219], a[136:139]\n"
      "buffer_load_dwordx4 v121, s[24:27], s61 offen nt lds\n"
      "v_exp_f32 v112, v168\n"
      "v_mfma_f32_16x16x32_bf16 a[140:143], a[104:107], v[220:223], a[140:143]\n"
      "s_add_u32 m0, s77, 24576\n"
      "ds_read_b128 a[80:83], v242 offset:1024\n"
      "v_or3_b32 v116, v116, v210, v211\n"
      "v_mfma_f32_16x16x32_bf16 a[156:159], a[108:111], v[220:223], a[156:159]\n"
      "buffer_load_dwordx4 v122, s[24:27], s61 offen nt lds\n"
      "v_exp_f32 v113, v169\n"
      "v_mfma_f32_16x16x32_bf16 a[152:155], a[108:111], v[216:219], a[152:155]\n"
      "s_add_u32 m0, s77, 25600\n"
      "ds_read_b128 a[84:87], v242 offset:5120\n"
      "v_cvt_pk_bf16_f32 v212, v112, v113\n"
      "v_mfma_f32_16x16x32_bf16 v[72:75], a[112:115], v[216:219], v[72:75]\n"
      "s_add_u32 s77, s77, 0x8000\n"
      "buffer_load_dwordx4 v123, s[24:27], s61 offen nt lds\n"
      "v_exp_f32 v114, v170\n"
      "v_mfma_f32_16x16x32_bf16 v[76:79], a[112:115], v[220:223], v[76:79]\n"
      "s_and_b32 s77, s77, 0x1ffff\n"
      "ds_read_b128 a[88:91], v243 offset:1024\n"
      "ds_read_b128 a[92:95], v243 offset:5120\n"
      "v_mfma_f32_16x16x32_bf16 v[92:95], a[116:119], v[220:223], v[92:95]\n"
      "s_add_u32 s61, s61, %[inc]\n"
      "v_exp_f32 v115, v171\n"
      "v_mfma_f32_16x16x32_bf16 v[88:91], a[116:119], v[216:219], v[88:91]\n"
      "v_cvt_pk_bf16_f32 v213, v114, v115\n"
      "v_mfma_f32_16x16x32_bf16 a[168:171], a[120:123], v[216:219], a[168:171]\n"
      "v_exp_f32 v112, v172\n"
      "v_mfma_f32_16x16x32_bf16 a[172:175], a[120:123], v[220:223], a[172:175]\n"
      "v_or3_b32 v116, v116, v212, v213\n"
      "v_mfma_f32_16x16x32_bf16 a[188:191], a[124:127], v[220:223], a[188:191]\n"
      "v_exp_f32 v113, v173\n"
      "v_mfma_f32_16x16x32_bf16 a[184:187], a[124:127], v[216:219], a[184:187]\n"
      "s_waitcnt lgkmcnt(0)\n"
      "v_cvt_pk_bf16_f32 v214, v112, v113\n"
      "ENTRY_%=:\n"
      "v_mfma_f32_16x16x32_bf16 v[128:131], a[64:67], a[0:3], v[224:227]\n"
      "s_sub_u32 s67, s68, s66\n"
      "v_exp_f32 v114, v174\n"
      "v_mfma_f32_16x16x32_bf16 v[128:131], a[72:75], a[16:19], v[128:131]\n"
      "s_cmp_gt_i32 s67, s70\n"
      "ds_read_b64_tr_b16 a[96:97], v244 offset:0\n"
      "ds_read_b64_tr_b16 a[98:99], v244 offset:4096\n"
      "v_mfma_f32_16x16x32_bf16 v[128:131], a[80:83], a[32:35], v[128:131]\n"
      "s_cselect_b32 s69, s86, 0\n"
      "v_exp_f32 v115, v175\n"
      "v_mfma_f32_16x16x32_bf16 v[128:131], a[88:91], a[48:51], v[128:131]\n"
      "s_add_u32 s66, s66, s69\n"
      "ds_read_b64_tr_b16 a[100:101], v245 offset:0\n"
      "v_cvt_pk_bf16_f32 v215, v114, v115\n"
      "v_mfma_f32_16x16x32_bf16 v[132:135], a[92:95], a[48:51], v[224:227]\n"
      "s_add_u32 s67, s67, s101\n"
      "v_exp_f32 v112, v176\n"
      "v_mfma_f32_16x16x32_bf16 v[132:135], a[84:87], a[32:35], v[132:135]\n"
      "s_min_i32 s70, s70, s67\n"
      "ds_read_b64_tr_b16 a[102:103], v245 offset:4096\n"
      "v_or3_b32 v116, v116, v214, v215\n"
      "v_mfma_f32_16x16x32_bf16 v[132:135], a[76:79], a[16:19], v[132:135]\n"
      "s_max_i32 s70, s70, 85\n"
      "v_exp_f32 v113, v177\n"
      "v_mfma_f32_16x16x32_bf16 v[132:135], a[68:71], a[0:3], v[132:135]\n"
      "s_add_u32 s66, s66, s86\n"
      "ds_read_b64_tr_b16 a[104:105], v246 offset:0\n"
      "v_cvt_pk_bf16_f32 v216, v112, v113\n"
      "v_mfma_f32_16x16x32_bf16 v[140:143], a[68:71], a[4:7], v[228:231]\n"
      "s_and_b32 s76, s80, 31\n"
      "v_exp_f32 v114, v178\n"
      "v_mfma_f32_16x16x32_bf16 v[140:143], a[76:79], a[20:23], v[140:143]\n"
      "s_lshr_b64 s[80:81], s[80:81], 5\n"
      "ds_read_b64_tr_b16 a[106:107], v246 offset:4096\n"
      "ds_read_b64_tr_b16 a[108:109], v247 offset:0\n"
      "v_mfma_f32_16x16x32_bf16 v[140:143], a[84:87], a[36:39], v[140:143]\n"
      "s_add_u32 s66, s66, s76\n"
      "v_exp_f32 v115, v179\n"
      "v_mfma_f32_16x16x32_bf16 v[140:143], a[92:95], a[52:55], v[140:143]\n"
      "s_sub_u32 s67, s68, s82\n"
      "ds_read_b64_tr_b16 a[110:111], v247 offset:4096\n"
      "v_cvt_pk_bf16_f32 v217, v114, v115\n"
      "v_mfma_f32_16x16x32_bf16 v[136:139], a[88:91], a[52:55], v[228:231]\n"
      "s_min_i32 s76, s67, s83\n"
      "v_exp_f32 v112, v180\n"
      "v_mfma_f32_16x16x32_bf16 v[136:139], a[80:83], a[36:39], v[136:139]\n"
      "s_mov_b32 s83, s67\n"
      "ds_read_b64_tr_b16 a[112:113], v244 offset:1024\n"
      "v_or3_b32 v116, v116, v216, v217\n"
      "v_mfma_f32_16x16x32_bf16 v[136:139], a[72:75], a[20:23], v[136:139]\n"
      "s_sub_u32 s76, s76, 36\n"
      "v_exp_f32 v113, v181\n"
      "v_mfma_f32_16x16x32_bf16 v[136:139], a[64:67], a[4:7], v[136:139]\n"
      "s_min_i32 s76, s76, 148\n"
      "ds_read_b64_tr_b16 a[114:115], v244 offset:5120\n"
      "v_cvt_pk_bf16_f32 v218, v112, v113\n"
      "v_mfma_f32_16x16x32_bf16 v[144:147], a[64:67], a[8:11], v[232:235]\n"
      "s_max_i32 s76, s76, 70\n"
      "v_exp_f32 v114, v182\n"
      "v_mfma_f32_16x16x32_bf16 v[144:147], a[72:75], a[24:27], v[144:147]\n"
      "s_mov_b32 s82, s68\n"
      "ds_read_b64_tr_b16 a[116:117], v245 offset:1024\n"
      "ds_read_b64_tr_b16 a[118:119], v245 offset:5120\n"
      "v_mfma_f32_16x16x32_bf16 v[144:147], a[80:83], a[40:43], v[144:147]\n"
      "s_add_u32 s65, s82, s76\n"
      "v_exp_f32 v115, v183\n"
      "v_mfma_f32_16x16x32_bf16 v[144:147], a[88:91], a[56:59], v[144:147]\n"
      "s_sub_u32 s93, s93, 1\n"
      "ds_read_b64_tr_b16 a[120:121], v246 offset:1024\n"
      "v_cvt_pk_bf16_f32 v219, v114, v115\n"
      "v_mfma_f32_16x16x32_bf16 v[148:151], a[92:95], a[56:59], v[232:235]\n"
      "s_cmp_le_i32 s93, 14\n"
      "v_exp_f32 v112, v184\n"
      "v_mfma_f32_16x16x32_bf16 v[148:151], a[84:87], a[40:43], v[148:151]\n"
      "s_cselect_b32 s86, 107, s86\n"
      "ds_read_b64_tr_b16 a[122:123], v246 offset:5120\n"
      "v_or3_b32 v116, v116, v218, v219\n"
      "v_mfma_f32_16x16x32_bf16 v[148:151], a[76:79], a[24:27], v[148:151]\n"
      "s_sub_u32 s67, s65, s66\n"
      "v_exp_f32 v113, v185\n"
      "v_mfma_f32_16x16x32_bf16 v[148:151], a[68:71], a[8:11], v[148:151]\n"
      "s_cmp_lt_i32 s67, 0\n"
      "ds_read_b64_tr_b16 a[124:125], v247 offset:1024\n"
      "v_cvt_pk_bf16_f32 v220, v112, v113\n"
      "v_mfma_f32_16x16x32_bf16 v[156:159], a[68:71], a[12:15], v[236:239]\n"
      "s_cselect_b32 s88, s66, s65\n"
      "v_exp_f32 v114, v186\n"
      "v_mfma_f32_16x16x32_bf16 v[156:159], a[76:79], a[28:31], v[156:159]\n"
      "s_add_u32 s90, s88, s91\n"
      "ds_read_b64_tr_b16 a[126:127], v247 offset:5120\n"
      "v_mfma_f32_16x16x32_bf16 v[156:159], a[84:87], a[44:47], v[156:159]\n"
      "v_exp_f32 v115, v187\n"
      "v_mfma_f32_16x16x32_bf16 v[156:159], a[92:95], a[60:63], v[156:159]\n"
      "v_cvt_pk_bf16_f32 v221, v114, v115\n"
      "v_mfma_f32_16x16x32_bf16 v[152:155], a[88:91], a[60:63], v[236:239]\n"
      "v_exp_f32 v112, v188\n"
      "v_mfma_f32_16x16x32_bf16 v[152:155], a[80:83], a[44:47], v[152:155]\n"
      "v_or3_b32 v116, v116, v220, v221\n"
      "v_mfma_f32_16x16x32_bf16 v[152:155], a[72:75], a[28:31], v[152:155]\n"
      "v_exp_f32 v113, v189\n"
      "v_mfma_f32_16x16x32_bf16 v[152:155], a[64:67], a[12:15], v[152:155]\n"
      "s_waitcnt lgkmcnt(10)\n"
      "v_cvt_pk_bf16_f32 v222, v112, v113\n"
      "v_mfma_f32_16x16x32_bf16 v[32:35], a[96:99], v[192:195], v[32:35]\n"
      "v_exp_f32 v114, v190\n"
      "v_mfma_f32_16x16x32_bf16 v[48:51], a[100:103], v[192:195], v[48:51]\n"
      "v_mfma_f32_16x16x32_bf16 a[128:131], a[104:107], v[192:195], a[128:131]\n"
      "s_waitcnt lgkmcnt(6)\n"
      "v_exp_f32 v115, v191\n"
      "v_mfma_f32_16x16x32_bf16 a[144:147], a[108:111], v[192:195], a[144:147]\n"
      "v_cvt_pk_bf16_f32 v223, v114, v115\n"
      "v_mfma_f32_16x16x32_bf16 v[64:67], a[112:115], v[192:195], v[64:67]\n"
      "s_waitcnt lgkmcnt(2)\n"
      "v_mfma_f32_16x16x32_bf16 v[80:83], a[116:119], v[192:195], v[80:83]\n"
      "v_or3_b32 v116, v116, v222, v223\n"
      "v_mfma_f32_16x16x32_bf16 a[160:163], a[120:123], v[192:195], a[160:163]\n"
      "s_mov_b32 s64, 0\n"
      "s_waitcnt lgkmcnt(0)\n"
      "v_and_b32 v116, 0x40004000, v116\n"
      "v_mfma_f32_16x16x32_bf16 a[176:179], a[124:127], v[192:195], a[176:179]\n"
      "v_cmp_ne_u32 vcc, 0, v116\n"
      "s_cbranch_vccnz FB1_%=\n"
      "RET1_0_%=:\n"
      "v_mfma_f32_16x16x32_bf16 a[180:183], a[124:127], v[196:199], a[180:183]\n"
      "v_exp_f32 v112, v128\n"
      "s_barrier\n"
      "v_mfma_f32_16x16x32_bf16 a[164:167], a[120:123], v[196:199], a[164:167]\n"
      "v_mfma_f32_16x16x32_bf16 a[192:195], a[196:199], v[208:211], a[192:195]\n"
      "s_add_u32 m0, s62, 0\n"
      "v_exp_f32 v113, v129\n"
      "v_mfma_f32_16x16x32_bf16 v[84:87], a[116:119], v[196:199], v[84:87]\n"
      "buffer_load_dwordx4 v248, s[20:23], s60 offen nt lds\n"
      "v_cvt_pk_bf16_f32 v192, v112, v113\n"
      "v_mfma_f32_16x16x32_bf16 v[68:71], a[112:115], v[196:199], v[68:71]\n"
      "s_add_u32 m0, s62, 896\n"
      "v_exp_f32 v114, v130\n"
      "v_mfma_f32_16x16x32_bf16 a[192:195], a[200:203], v[212:215], a[192:195]\n"
      "buffer_load_dwordx4 v248, s[20:23], s60 offen offset:128 nt lds\n"
      "v_mfma_f32_16x16x32_bf16 a[148:151], a[108:111], v[196:199], a[148:151]\n"
      "s_add_u32 m0, s62, 8192\n"
      "v_exp_f32 v115, v131\n"
      "v_mfma_f32_16x16x32_bf16 a[132:135], a[104:107], v[196:199], a[132:135]\n"
      "s_memrealtime s[68:69]\n"
      "buffer_load_dwordx4 v120, s[20:23], s60 offen nt lds\n"
      "v_cvt_pk_bf16_f32 v193, v114, v115\n"
      "v_mfma_f32_16x16x32_bf16 a[192:195], a[204:207], v[216:219], a[192:195]\n"
      "s_add_u32 m0, s62, 9088\n"
      "v_exp_f32 v112, v132\n"
      "v_mfma_f32_16x16x32_bf16 v[52:55], a[100:103], v[196:199], v[52:55]\n"
      "s_add_u32 s62, s62, 0x8000\n"
      "buffer_load_dwordx4 v120, s[20:23], s60 offen offset:128 nt lds\n"
      "v_or_b32 v116, v192, v193\n"
      "v_mfma_f32_16x16x32_bf16 v[36:39], a[96:99], v[196:199], v[36:39]\n"
      "s_and_b32 s62, s62, 0x1ffff\n"
      "v_exp_f32 v113, v133\n"
      "v_mfma_f32_16x16x32_bf16 a[192:195], a[208:211], v[220:223], a[192:195]\n"
      "s_add_u32 s60, s60, s78\n"
      "v_cvt_pk_bf16_f32 v194, v112, v113\n"
      "s_waitcnt vmcnt(12) lgkmcnt(0)\n"
      "s_sub_u32 s67, s68, s90\n"
      "s_cmp_lt_i32 s67, 0\n"
      "s_cbranch_scc1 GW1_%=\n"
      "GX1_%=:\n"
      "s_barrier\n"
      "v_mfma_f32_16x16x32_bf16 v[40:43], a[96:99], v[200:203], v[40:43]\n"
      "s_memrealtime s[68:69]\n"
      "ds_read_b128 a[64:67], v242 offset:32768\n"
      "v_exp_f32 v114, v134\n"
      "v_mfma_f32_16x16x32_bf16 v[44:47], a[96:99], v[204:207], v[44:47]\n"
      "s_add_u32 m0, s77, 16384\n"
      "ds_read_b128 a[68:71], v242 offset:36864\n"
      "ds_read_b128 a[72:75], v243 offset:32768\n"
      "v_mfma_f32_16x16x32_bf16 v[60:63], a[100:103], v[204:207], v[60:63]\n"
      "buffer_load_dwordx4 v249, s[24:27], s61 offen nt lds\n"
      "v_exp_f32 v115, v135\n"
      "v_mfma_f32_16x16x32_bf16 v[56:59], a[100:103], v[200:203], v[56:59]\n"
      "s_add_u32 m0, s77, 17408\n"
      "ds_read_b128 a[76:79], v243 offset:36864\n"
      "v_cvt_pk_bf16_f32 v195, v114, v115\n"
      "v_mfma_f32_16x16x32_bf16 a[136:139], a[104:107], v[200:203], a[136:139]\n"
      "buffer_load_dwordx4 v121, s[24:27], s61 offen nt lds\n"
      "v_exp_f32 v112, v136\n"
      "v_mfma_f32_16x16x32_bf16 a[140:143], a[104:107], v[204:207], a[140:143]\n"
      "s_add_u32 m0, s77, 24576\n"
      "ds_read_b128 a[80:83], v242 offset:33792\n"
      "v_or3_b32 v116, v116, v194, v195\n"
      "v_mfma_f32_16x16x32_bf16 a[156:159], a[108:111], v[204:207], a[156:159]\n"
      "buffer_load_dwordx4 v122, s[24:27], s61 offen nt lds\n"
      "v_exp_f32 v113, v137\n"
      "v_mfma_f32_16x16x32_bf16 a[152:155], a[108:111], v[200:203], a[152:155]\n"
      "s_add_u32 m0, s77, 25600\n"
      "ds_read_b128 a[84:87], v242 offset:37888\n"
      "v_cvt_pk_bf16_f32 v196, v112, v113\n"
      "v_mfma_f32_16x16x32_bf16 v[72:75], a[112:115], v[200:203], v[72:75]\n"
      "s_add_u32 s77, s77, 0x8000\n"
      "buffer_load_dwordx4 v123, s[24:27], s61 offen nt lds\n"
      "v_exp_f32 v114, v138\n"
      "v_mfma_f32_16x16x32_bf16 v[76:79], a[112:115], v[204:207], v[76:79]\n"
      "s_and_b32 s77, s77, 0x1ffff\n"
      "ds_read_b128 a[88:91], v243 offset:33792\n"
      "ds_read_b128 a[92:95], v243 offset:37888\n"
      "v_mfma_f32_16x16x32_bf16 v[92:95], a[116:119], v[204:207], v[92:95]\n"
      "s_add_u32 s61, s61, s78\n"
      "v_exp_f32 v115, v139\n"
      "v_mfma_f32_16x16x32_bf16 v[88:91], a[116:119], v[200:203], v[88:91]\n"
      "v_cvt_pk_bf16_f32 v197, v114, v115\n"
      "v_mfma_f32_16x16x32_bf16 a[168:171], a[120:123], v[200:203], a[168:171]\n"
      "v_exp_f32 v112, v140\n"
      "v_mfma_f32_16x16x32_bf16 a[172:175], a[120:123], v[204:207], a[172:175]\n"
      "v_or3_b32 v116, v116, v196, v197\n"
      "v_mfma_f32_16x16x32_bf16 a[188:191], a[124:127], v[204:207], a[188:191]\n"
      "v_exp_f32 v113, v141\n"
      "v_mfma_f32_16x16x32_bf16 a[184:187], a[124:127], v[200:203], a[184:187]\n"
      "s_waitcnt lgkmcnt(0)\n"
      "v_cvt_pk_bf16_f32 v198, v112, v113\n"
      "v_mfma_f32_16x16x32_bf16 v[160:163], a[64:67], a[0:3], v[224:227]\n"
      "s_sub_u32 s67, s68, s66\n"
      "v_exp_f32 v114, v142\n"
      "v_mfma_f32_16x16x32_bf16 v[160:163], a[72:75], a[16:19], v[160:163]\n"
      "s_cmp_gt_i32 s67, s70\n"
      "ds_read_b64_tr_b16 a[96:97], v244 offset:32768\n"
      "ds_read_b64_tr_b16 a[98:99], v244 offset:36864\n"
      "v_mfma_f32_16x16x32_bf16 v[160:163], a[80:83], a[32:35], v[160:163]\n"
      "s_cselect_b32 s69, s86, 0\n"
      "v_exp_f32 v115, v143\n"
      "v_mfma_f32_16x16x32_bf16 v[160:163], a[88:91], a[48:51], v[160:163]\n"
      "s_add_u32 s66, s66, s69\n"
      "ds_read_b64_tr_b16 a[100:101], v245 offset:32768\n"
      "v_cvt_pk_bf16_f32 v199, v114, v115\n"
      "v_mfma_f32_16x16x32_bf16 v[164:167], a[92:95], a[48:51], v[224:227]\n"
      "s_add_u32 s67, s67, s101\n"
      "v_exp_f32 v112, v144\n"
      "v_mfma_f32_16x16x32_bf16 v[164:167], a[84:87], a[32:35], v[164:167]\n"
      "s_min_i32 s70, s70, s67\n"
      "ds_read_b64_tr_b16 a[102:103], v245 offset:36864\n"
      "v_or3_b32 v116, v116, v198, v199\n"
      "v_mfma_f32_16x16x32_bf16 v[164:167], a[76:79], a[16:19], v[164:167]\n"
      "s_max_i32 s70, s70, 85\n"
      "v_exp_f32 v113, v145\n"
      "v_mfma_f32_16x16x32_bf16 v[164:167], a[68:71], a[0:3], v[164:167]\n"
      "s_add_u32 s66, s66, s86\n"
      "ds_read_b64_tr_b16 a[104:105], v246 offset:32768\n"
      "v_cvt_pk_bf16_f32 v200, v112, v113\n"
      "v_mfma_f32_16x16x32_bf16 v[172:175], a[68:71], a[4:7], v[228:231]\n"
      "s_and_b32 s76, s80, 31\n"
      "v_exp_f32 v114, v146\n"
      "v_mfma_f32_16x16x32_bf16 v[172:175], a[76:79], a[20:23], v[172:175]\n"
      "s_lshr_b64 s[80:81], s[80:81], 5\n"
      "ds_read_b64_tr_b16 a[106:107], v246 offset:36864\n"
      "ds_read_b64_tr_b16 a[108:109], v247 offset:32768\n"
      "v_mfma_f32_16x16x32_bf16 v[172:175], a[84:87], a[36:39], v[172:175]\n"
      "s_add_u32 s66, s66, s76\n"
      "v_exp_f32 v115, v147\n"
      "v_mfma_f32_16x16x32_bf16 v[172:175], a[92:95], a[52:55], v[172:175]\n"
      "s_sub_u32 s67, s68, s82\n"
      "ds_read_b64_tr_b16 a[110:111], v247 offset:36864\n"
      "v_cvt_pk_bf16_f32 v201, v114, v115\n"
      "v_mfma_f32_16x16x32_bf16 v[168:171], a[88:91], a[52:55], v[228:231]\n"
      "s_min_i32 s76, s67, s83\n"
      "v_exp_f32 v112, v148\n"
      "v_mfma_f32_16x16x32_bf16 v[168:171], a[80:83], a[36:39], v[168:171]\n"
      "s_mov_b32 s83, s67\n"
      "ds_read_b64_tr_b16 a[112:113], v244 offset:33792\n"
      "v_or3_b32 v116, v116, v200, v201\n"
      "v_mfma_f32_16x16x32_bf16 v[168:171], a[72:75], a[20:23], v[168:171]\n"
      "s_sub_u32 s76, s76, 36\n"
      "v_exp_f32 v113, v149\n"
      "v_mfma_f32_16x16x32_bf16 v[168:171], a[64:67], a[4:7], v[168:171]\n"
      "s_min_i32 s76, s76, 148\n"
      "ds_read_b64_tr_b16 a[114:115], v244 offset:37888\n"
      "v_cvt_pk_bf16_f32 v202, v112, v113\n"
      "v_mfma_f32_16x16x32_bf16 v[176:179], a[64:67], a[8:11], v[232:235]\n"
      "s_max_i32 s76, s76, 70\n"
      "v_exp_f32 v114, v150\n"
      "v_mfma_f32_16x16x32_bf16 v[176:179], a[72:75], a[24:27], v[176:179]\n"
      "s_mov_b32 s82, s68\n"
      "ds_read_b64_tr_b16 a[116:117], v245 offset:33792\n"
      "ds_read_b64_tr_b16 a[118:119], v245 offset:37888\n"
      "v_mfma_f32_16x16x32_bf16 v[176:179], a[80:83], a[40:43], v[176:179]\n"
      "s_add_u32 s65, s82, s76\n"
      "v_exp_f32 v115, v151\n"
      "v_mfma_f32_16x16x32_bf16 v[176:179], a[88:91], a[56:59], v[176:179]\n"
      "s_sub_u32 s93, s93, 1\n"
      "ds_read_b64_tr_b16 a[120:121], v246 offset:33792\n"
      "v_cvt_pk_bf16_f32 v203, v114, v115\n"
      "v_mfma_f32_16x16x32_bf16 v[180:183], a[92:95], a[56:59], v[232:235]\n"
      "s_cmp_le_i32 s93, 14\n"
      "v_exp_f32 v112, v152\n"
      "v_mfma_f32_16x16x32_bf16 v[180:183], a[84:87], a[40:43], v[180:183]\n"
      "s_cselect_b32 s86, 107, s86\n"
      "ds_read_b64_tr_b16 a[122:123], v246 offset:37888\n"
      "v_or3_b32 v116, v116, v202, v203\n"
      "v_mfma_f32_16x16x32_bf16 v[180:183], a[76:79], a[24:27], v[180:183]\n"
      "s_sub_u32 s67, s65, s66\n"
      "v_exp_f32 v113, v153\n"
      "v_mfma_f32_16x16x32_bf16 v[180:183], a[68:71], a[8:11], v[180:183]\n"
      "s_cmp_lt_i32 s67, 0\n"
      "ds_read_b64_tr_b16 a[124:125], v247 offset:33792\n"
      "v_cvt_pk_bf16_f32 v204, v112, v113\n"
      "v_mfma_f32_16x16x32_bf16 v[188:191], a[68:71], a[12:15], v[236:239]\n"
      "s_cselect_b32 s88, s66, s65\n"
      "v_exp_f32 v114, v154\n"
      "v_mfma_f32_16x16x32_bf16 v[188:191], a[76:79], a[28:31], v[188:191]\n"
      "s_add_u32 s90, s88, s91\n"
      "ds_read_b64_tr_b16 a[126:127], v247 offset:37888\n"
      "v_mfma_f32_16x16x32_bf16 v[188:191], a[84:87], a[44:47], v[188:191]\n"
      "v_exp_f32 v115, v155\n"
      "v_mfma_f32_16x16x32_bf16 v[188:191], a[92:95], a[60:63], v[188:191]\n"
      "v_cvt_pk_bf16_f32 v205, v114, v115\n"
      "v_mfma_f32_16x16x32_bf16 v[184:187], a[88:91], a[60:63], v[236:239]\n"
      "v_exp_f32 v112, v156\n"
      "v_mfma_f32_16x16x32_bf16 v[184:187], a[80:83], a[44:47], v[184:187]\n"
      "v_or3_b32 v116, v116, v204, v205\n"
      "v_mfma_f32_16x16x32_bf16 v[184:187], a[72:75], a[28:31], v[184:187]\n"
      "v_exp_f32 v113, v157\n"
      "v_mfma_f32_16x16x32_bf16 v[184:187], a[64:67], a[12:15], v[184:187]\n"
      "s_waitcnt lgkmcnt(10)\n"
      "v_cvt_pk_bf16_f32 v206, v112, v113\n"
      "v_mfma_f32_16x16x32_bf16 v[32:35], a[96:99], v[208:211], v[32:35]\n"
      "v_exp_f32 v114, v158\n"
      "v_mfma_f32_16x16x32_bf16 v[48:51], a[100:103], v[208:211], v[48:51]\n"
      "v_mfma_f32_16x16x32_bf16 a[128:131], a[104:107], v[208:211], a[128:131]\n"
      "s_waitcnt lgkmcnt(6)\n"
      "v_exp_f32 v115, v159\n"
      "v_mfma_f32_16x16x32_bf16 a[144:147], a[108:111], v[208:211], a[144:147]\n"
      "v_cvt_pk_bf16_f32 v207, v114, v115\n"
      "v_mfma_f32_16x16x32_bf16 v[64:67], a[112:115], v[208:211], v[64:67]\n"
      "s_waitcnt lgkmcnt(2)\n"
      "v_mfma_f32_16x16x32_bf16 v[80:83], a[116:119], v[208:211], v[80:83]\n"
      "v_or3_b32 v116, v116, v206, v207\n"
      "v_mfma_f32_16x16x32_bf16 a[160:163], a[120:123], v[208:211], a[160:163]\n"
      "s_mov_b32 s64, 0\n"
      "s_waitcnt lgkmcnt(0)\n"
      "v_and_b32 v116, 0x40004000, v116\n"
      "v_mfma_f32_16x16x32_bf16 a[176:179], a[124:127], v[208:211], a[176:179]\n"
      "v_cmp_ne_u32 vcc, 0, v116\n"
      "s_cbranch_vccnz FB0_%=\n"
      "RET0_0_%=:\n"
      "v_mfma_f32_16x16x32_bf16 a[180:183], a[124:127], v[212:215], a[180:183]\n"
      "v_exp_f32 v112, v160\n"
      "s_barrier\n"
      "v_mfma_f32_16x16x32_bf16 a[164:167], a[120:123], v[212:215], a[164:167]\n"
      "v_xor_b32 v242, 0x10000, v242\n"
      "v_mfma_f32_16x16x32_bf16 a[192:195], a[196:199], v[192:195], a[192:195]\n"
      "s_add_u32 m0, s62, 0\n"
      "v_exp_f32 v113, v161\n"
      "v_mfma_f32_16x16x32_bf16 v[84:87], a[116:119], v[212:215], v[84:87]\n"
      "buffer_load_dwordx4 v248, s[20:23], s60 offen nt lds\n"
      "v_cvt_pk_bf16_f32 v208, v112, v113\n"
      "v_xor_b32 v243, 0x10000, v243\n"
      "v_mfma_f32_16x16x32_bf16 v[68:71], a[112:115], v[212:215], v[68:71]\n"
      "s_add_u32 m0, s62, 896\n"
      "v_exp_f32 v114, v162\n"
      "v_mfma_f32_16x16x32_bf16 a[192:195], a[200:203], v[196:199], a[192:195]\n"
      "buffer_load_dwordx4 v248, s[20:23], s60 offen offset:128 nt lds\n"
      "v_xor_b32 v244, 0x10000, v244\n"
      "v_mfma_f32_16x16x32_bf16 a[148:151], a[108:111], v[212:215], a[148:151]\n"
      "s_add_u32 m0, s62, 8192\n"
      "v_exp_f32 v115, v163\n"
      "v_mfma_f32_16x16x32_bf16 a[132:135], a[104:107], v[212:215], a[132:135]\n"
      "s_memrealtime s[68:69]\n"
      "buffer_load_dwordx4 v120, s[20:23], s60 offen nt lds\n"
      "v_cvt_pk_bf16_f32 v209, v114, v115\n"
      "v_xor_b32 v245, 0x10000, v245\n"
      "v_mfma_f32_16x16x32_bf16 a[192:195], a[204:207], v[200:203], a[192:195]\n"
      "s_add_u32 m0, s62, 9088\n"
      "v_exp_f32 v112, v164\n"
      "v_mfma_f32_16x16x32_bf16 v[52:55], a[100:103], v[212:215], v[52:55]\n"
      "s_add_u32 s62, s62, 0x8000\n"
      "buffer_load_dwordx4 v120, s[20:23], s60 offen offset:128 nt lds\n"
      "v_or_b32 v116, v208, v209\n"
      "v_xor_b32 v246, 0x10000, v246\n"
      "v_mfma_f32_16x16x32_bf16 v[36:39], a[96:99], v[212:215], v[36:39]\n"
      "s_and_b32 s62, s62, 0x1ffff\n"
      "s_add_u32 s60, s60, %[inc]\n"
      "v_exp_f32 v113, v165\n"
      "v_mfma_f32_16x16x32_bf16 a[192:195], a[208:211], v[204:207], a[192:195]\n"
      "s_sub_u32 s63, s63, 1\n"
      "v_cvt_pk_bf16_f32 v210, v112, v113\n"
      "v_xor_b32 v247, 0x10000, v247\n"
      "s_cbranch_scc0 LOOP1_%=\n"
      "v_readfirstlane_b32 s72, v240\n"
      "v_readfirstlane_b32 s79, v241\n"
      "s_waitcnt vmcnt(12) lgkmcnt(0)\n"
      "s_sub_u32 s67, s68, s90\n"
      "s_cmp_lt_i32 s67, 0\n"
      "s_cbranch_scc1 GW2_%=\n"
      "GX2_%=:\n"
      "s_barrier\n"
      "v_mfma_f32_16x16x32_bf16 v[40:43], a[96:99], v[216:219], v[40:43]\n"
      "s_memrealtime s[68:69]\n"
      "buffer_load_dword v250, v251, s[28:31], 0 offen sc1\n"
      "ds_read_b128 a[64:67], v242 offset:0\n"
      "v_exp_f32 v114, v166\n"
      "v_mfma_f32_16x16x32_bf16 v[44:47], a[96:99], v[220:223], v[44:47]\n"
      "s_add_u32 m0, s77, 16384\n"
      "ds_read_b128 a[68:71], v242 offset:4096\n"
      "ds_read_b128 a[72:75], v243 offset:0\n"
      "v_mfma_f32_16x16x32_bf16 v[60:63], a[100:103], v[220:223], v[60:63]\n"
      "buffer_load_dwordx4 v249, s[24:27], s61 offen nt lds\n"
      "v_exp_f32 v115, v167\n"
      "v_mfma_f32_16x16x32_bf16 v[56:59], a[100:103], v[216:219], v[56:59]\n"
      "s_add_u32 m0, s77, 17408\n"
      "ds_read_b128 a[76:79], v243 offset:4096\n"
      "v_cvt_pk_bf16_f32 v211, v114, v115\n"
      "v_mfma_f32_16x16x32_bf16 a[136:139], a[104:107], v[216:219], a[136:139]\n"
      "buffer_load_dwordx4 v121, s[24:27], s61 offen nt lds\n"
      "v_exp_f32 v112, v168\n"
      "v_mfma_f32_16x16x32_bf16 a[140:143], a[104:107], v[220:223], a[140:143]\n"
      "s_add_u32 m0, s77, 24576\n"
      "ds_read_b128 a[80:83], v242 offset:1024\n"
      "v_or3_b32 v116, v116, v210, v211\n"
      "v_mfma_f32_16x16x32_bf16 a[156:159], a[108:111], v[220:223], a[156:159]\n"
      "buffer_load_dwordx4 v122, s[24:27], s61 offen nt lds\n"
      "v_exp_f32 v113, v169\n"
      "v_mfma_f32_16x16x32_bf16 a[152:155], a[108:111], v[216:219], a[152:155]\n"
      "s_add_u32 m0, s77, 25600\n"
      "ds_read_b128 a[84:87], v242 offset:5120\n"
      "v_cvt_pk_bf16_f32 v212, v112, v113\n"
      "v_mfma_f32_16x16x32_bf16 v[72:75], a[112:115], v[216:219], v[72:75]\n"
      "s_add_u32 s77, s77, 0x8000\n"
      "buffer_load_dwordx4 v123, s[24:27], s61 offen nt lds\n"
      "v_exp_f32 v114, v170\n"
      "v_mfma_f32_16x16x32_bf16 v[76:79], a[112:115], v[220:223], v[76:79]\n"
      "s_and_b32 s77, s77, 0x1ffff\n"
      "ds_read_b128 a[88:91], v243 offset:1024\n"
      "ds_read_b128 a[92:95], v243 offset:5120\n"
      "v_mfma_f32_16x16x32_bf16 v[92:95], a[116:119], v[220:223], v[92:95]\n"
      "s_add_u32 s61, s61, %[inc]\n"
      "v_exp_f32 v115, v171\n"
      "v_mfma_f32_16x16x32_bf16 v[88:91], a[116:119], v[216:219], v[88:91]\n"
      "v_cvt_pk_bf16_f32 v213, v114, v115\n"
      "v_mfma_f32_16x16x32_bf16 a[168:171], a[120:123], v[216:219], a[168:171]\n"
      "v_exp_f32 v112, v172\n"
      "v_mfma_f32_16x16x32_bf16 a[172:175], a[120:123], v[220:223], a[172:175]\n"
      "v_or3_b32 v116, v116, v212, v213\n"
      "v_mfma_f32_16x16x32_bf16 a[188:191], a[124:127], v[220:223], a[188:191]\n"
      "v_exp_f32 v113, v173\n"
      "v_mfma_f32_16x16x32_bf16 a[184:187], a[124:127], v[216:219], a[184:187]\n"
      "s_waitcnt lgkmcnt(0)\n"
      "v_cvt_pk_bf16_f32 v214, v112, v113\n"
      "v_mfma_f32_16x16x32_bf16 v[128:131], a[64:67], a[0:3], v[224:227]\n"
      "s_sub_u32 s67, s68, s66\n"
      "v_exp_f32 v114, v174\n"
      "v_mfma_f32_16x16x32_bf16 v[128:131], a[72:75], a[16:19], v[128:131]\n"
      "s_cmp_gt_i32 s67, s70\n"
      "ds_read_b64_tr_b16 a[96:97], v244 offset:0\n"
      "ds_read_b64_tr_b16 a[98:99], v244 offset:4096\n"
      "v_mfma_f32_16x16x32_bf16 v[128:131], a[80:83], a[32:35], v[128:131]\n"
      "s_cselect_b32 s69, s86, 0\n"
      "v_exp_f32 v115, v175\n"
      "v_mfma_f32_16x16x32_bf16 v[128:131], a[88:91], a[48:51], v[128:131]\n"
      "s_add_u32 s66, s66, s69\n"
      "ds_read_b64_tr_b16 a[100:101], v245 offset:0\n"
      "v_cvt_pk_bf16_f32 v215, v114, v115\n"
      "v_mfma_f32_16x16x32_bf16 v[132:135], a[92:95], a[48:51], v[224:227]\n"
      "s_add_u32 s67, s67, s101\n"
      "v_exp_f32 v112, v176\n"
      "v_mfma_f32_16x16x32_bf16 v[132:135], a[84:87], a[32:35], v[132:135]\n"
      "s_min_i32 s70, s70, s67\n"
      "ds_read_b64_tr_b16 a[102:103], v245 offset:4096\n"
      "v_or3_b32 v116, v116, v214, v215\n"
      "v_mfma_f32_16x16x32_bf16 v[132:135], a[76:79], a[16:19], v[132:135]\n"
      "s_max_i32 s70, s70, 85\n"
      "v_exp_f32 v113, v177\n"
      "v_mfma_f32_16x16x32_bf16 v[132:135], a[68:71], a[0:3], v[132:135]\n"
      "s_add_u32 s66, s66, s86\n"
      "ds_read_b64_tr_b16 a[104:105], v246 offset:0\n"
      "v_cvt_pk_bf16_f32 v216, v112, v113\n"
      "v_mfma_f32_16x16x32_bf16 v[140:143], a[68:71], a[4:7], v[228:231]\n"
      "s_and_b32 s76, s80, 31\n"
      "v_exp_f32 v114, v178\n"
      "v_mfma_f32_16x16x32_bf16 v[140:143], a[76:79], a[20:23], v[140:143]\n"
      "s_lshr_b64 s[80:81], s[80:81], 5\n"
      "ds_read_b64_tr_b16 a[106:107], v246 offset:4096\n"
      "ds_read_b64_tr_b16 a[108:109], v247 offset:0\n"
      "v_mfma_f32_16x16x32_bf16 v[140:143], a[84:87], a[36:39], v[140:143]\n"
      "s_add_u32 s66, s66, s76\n"
      "v_exp_f32 v115, v179\n"
      "v_mfma_f32_16x16x32_bf16 v[140:143], a[92:95], a[52:55], v[140:143]\n"
      "s_sub_u32 s67, s68, s82\n"
      "ds_read_b64_tr_b16 a[110:111], v247 offset:4096\n"
      "v_cvt_pk_bf16_f32 v217, v114, v115\n"
      "v_mfma_f32_16x16x32_bf16 v[136:139], a[88:91], a[52:55], v[228:231]\n"
      "s_min_i32 s76, s67, s83\n"
      "v_exp_f32 v112, v180\n"
      "v_mfma_f32_16x16x32_bf16 v[136:139], a[80:83], a[36:39], v[136:139]\n"
      "s_mov_b32 s83, s67\n"
      "ds_read_b64_tr_b16 a[112:113], v244 offset:1024\n"
      "v_or3_b32 v116, v116, v216, v217\n"
      "v_mfma_f32_16x16x32_bf16 v[136:139], a[72:75], a[20:23], v[136:139]\n"
      "s_sub_u32 s76, s76, 36\n"
      "v_exp_f32 v113, v181\n"
      "v_mfma_f32_16x16x32_bf16 v[136:139], a[64:67], a[4:7], v[136:139]\n"
      "s_min_i32 s76, s76, 148\n"
      "ds_read_b64_tr_b16 a[114:115], v244 offset:5120\n"
      "v_cvt_pk_bf16_f32 v218, v112, v113\n"
      "v_mfma_f32_16x16x32_bf16 v[144:147], a[64:67], a[8:11], v[232:235]\n"
      "s_max_i32 s76, s76, 70\n"
      "v_exp_f32 v114, v182\n"
      "v_mfma_f32_16x16x32_bf16 v[144:147], a[72:75], a[24:27], v[144:147]\n"
      "s_mov_b32 s82, s68\n"
      "ds_read_b64_tr_b16 a[116:117], v245 offset:1024\n"
      "ds_read_b64_tr_b16 a[118:119], v245 offset:5120\n"
      "v_mfma_f32_16x16x32_bf16 v[144:147], a[80:83], a[40:43], v[144:147]\n"
      "s_lshr_b32 s69, s86, 1\n"
      "v_exp_f32 v115, v183\n"
      "v_mfma_f32_16x16x32_bf16 v[144:147], a[88:91], a[56:59], v[144:147]\n"
      "s_add_u32 s66, s66, s69\n"
      "ds_read_b64_tr_b16 a[120:121], v246 offset:1024\n"
      "v_cvt_pk_bf16_f32 v219, v114, v115\n"
      "v_mfma_f32_16x16x32_bf16 v[148:151], a[92:95], a[56:59], v[232:235]\n"
      "s_add_u32 s82, s82, s69\n"
      "v_exp_f32 v112, v184\n"
      "v_mfma_f32_16x16x32_bf16 v[148:151], a[84:87], a[40:43], v[148:151]\n"
      "s_add_u32 s65, s82, s76\n"
      "ds_read_b64_tr_b16 a[122:123], v246 offset:5120\n"
      "v_or3_b32 v116, v116, v218, v219\n"
      "v_mfma_f32_16x16x32_bf16 v[148:151], a[76:79], a[24:27], v[148:151]\n"
      "s_sub_u32 s93, s93, 1\n"
      "v_exp_f32 v113, v185\n"
      "v_mfma_f32_16x16x32_bf16 v[148:151], a[68:71], a[8:11], v[148:151]\n"
      "s_cmp_le_i32 s93, 14\n"
      "ds_read_b64_tr_b16 a[124:125], v247 offset:1024\n"
      "v_cvt_pk_bf16_f32 v220, v112, v113\n"
      "v_mfma_f32_16x16x32_bf16 v[156:159], a[68:71], a[12:15], v[236:239]\n"
      "s_cselect_b32 s86, 107, s86\n"
      "v_exp_f32 v114, v186\n"
      "v_mfma_f32_16x16x32_bf16 v[156:159], a[76:79], a[28:31], v[156:159]\n"
      "s_sub_u32 s67, s65, s66\n"
      "ds_read_b64_tr_b16 a[126:127], v247 offset:5120\n"
      "v_mfma_f32_16x16x32_bf16 v[156:159], a[84:87], a[44:47], v[156:159]\n"
      "s_cmp_lt_i32 s67, 0\n"
      "v_exp_f32 v115, v187\n"
      "v_mfma_f32_16x16x32_bf16 v[156:159], a[92:95], a[60:63], v[156:159]\n"
      "s_cselect_b32 s88, s66, s65\n"
      "v_cvt_pk_bf16_f32 v221, v114, v115\n"
      "v_mfma_f32_16x16x32_bf16 v[152:155], a[88:91], a[60:63], v[236:239]\n"
      "s_add_u32 s90, s88, s91\n"
      "v_exp_f32 v112, v188\n"
      "v_mfma_f32_16x16x32_bf16 v[152:155], a[80:83], a[44:47], v[152:155]\n"
      "v_or3_b32 v116, v116, v220, v221\n"
      "v_mfma_f32_16x16x32_bf16 v[152:155], a[72:75], a[28:31], v[152:155]\n"
      "v_exp_f32 v113, v189\n"
      "v_mfma_f32_16x16x32_bf16 v[152:155], a[64:67], a[12:15], v[152:155]\n"
      "s_waitcnt lgkmcnt(10)\n"
      "v_cvt_pk_bf16_f32 v222, v112, v113\n"
      "v_mfma_f32_16x16x32_bf16 v[32:35], a[96:99], v[192:195], v[32:35]\n"
      "v_exp_f32 v114, v190\n"
      "v_mfma_f32_16x16x32_bf16 v[48:51], a[100:103], v[192:195], v[48:51]\n"
      "v_mfma_f32_16x16x32_bf16 a[128:131], a[104:107], v[192:195], a[128:131]\n"
      "s_waitcnt lgkmcnt(6)\n"
      "v_exp_f32 v115, v191\n"
      "v_mfma_f32_16x16x32_bf16 a[144:147], a[108:111], v[192:195], a[144:147]\n"
      "v_cvt_pk_bf16_f32 v223, v114, v115\n"
      "v_mfma_f32_16x16x32_bf16 v[64:67], a[112:115], v[192:195], v[64:67]\n"
      "s_waitcnt lgkmcnt(2)\n"
      "v_mfma_f32_16x16x32_bf16 v[80:83], a[116:119], v[192:195], v[80:83]\n"
      "v_or3_b32 v116, v116, v222, v223\n"
      "v_mfma_f32_16x16x32_bf16 a[160:163], a[120:123], v[192:195], a[160:163]\n"
      "s_mov_b32 s64, 1\n"
      "s_waitcnt lgkmcnt(0)\n"
      "v_and_b32 v116, 0x40004000, v116\n"
      "v_mfma_f32_16x16x32_bf16 a[176:179], a[124:127], v[192:195], a[176:179]\n"
      "v_cmp_ne_u32 vcc, 0, v116\n"
      "s_cbranch_vccnz FB1_%=\n"
      "RET1_1_%=:\n"
      "v_mfma_f32_16x16x32_bf16 a[180:183], a[124:127], v[196:199], a[180:183]\n"
      "v_exp_f32 v112, v128\n"
      "s_barrier\n"
      "v_mfma_f32_16x16x32_bf16 a[164:167], a[120:123], v[196:199], a[164:167]\n"
      "v_mfma_f32_16x16x32_bf16 a[192:195], a[196:199], v[208:211], a[192:195]\n"
      "s_add_u32 m0, s62, 0\n"
      "v_exp_f32 v113, v129\n"
      "v_mfma_f32_16x16x32_bf16 v[84:87], a[116:119], v[196:199], v[84:87]\n"
      "buffer_load_dwordx4 v248, s[20:23], s60 offen nt lds\n"
      "v_cvt_pk_bf16_f32 v192, v112, v113\n"
      "v_mfma_f32_16x16x32_bf16 v[68:71], a[112:115], v[196:199], v[68:71]\n"
      "s_add_u32 m0, s62, 896\n"
      "v_exp_f32 v114, v130\n"
      "v_mfma_f32_16x16x32_bf16 a[192:195], a[200:203], v[212:215], a[192:195]\n"
      "buffer_load_dwordx4 v248, s[20:23], s60 offen offset:128 nt lds\n"
      "v_mfma_f32_16x16x32_bf16 a[148:151], a[108:111], v[196:199], a[148:151]\n"
      "s_add_u32 m0, s62, 8192\n"
      "v_exp_f32 v115, v131\n"
      "v_mfma_f32_16x16x32_bf16 a[132:135], a[104:107], v[196:199], a[132:135]\n"
      "s_memrealtime s[68:69]\n"
      "buffer_load_dwordx4 v120, s[20:23], s60 offen nt lds\n"
      "v_cvt_pk_bf16_f32 v193, v114, v115\n"
      "v_mfma_f32_16x16x32_bf16 a[192:195], a[204:207], v[216:219], a[192:195]\n"
      "s_add_u32 m0, s62, 9088\n"
      "v_exp_f32 v112, v132\n"
      "v_mfma_f32_16x16x32_bf16 v[52:55], a[100:103], v[196:199], v[52:55]\n"
      "s_add_u32 s62, s62, 0x8000\n"
      "buffer_load_dwordx4 v120, s[20:23], s60 offen offset:128 nt lds\n"
      "v_or_b32 v116, v192, v193\n"
      "v_mfma_f32_16x16x32_bf16 v[36:39], a[96:99], v[196:199], v[36:39]\n"
      "s_and_b32 s62, s62, 0x1ffff\n"
      "v_exp_f32 v113, v133\n"
      "v_mfma_f32_16x16x32_bf16 a[192:195], a[208:211], v[220:223], a[192:195]\n"
      "s_add_u32 s60, s60, s78\n"
      "v_cvt_pk_bf16_f32 v194, v112, v113\n"
      "s_waitcnt vmcnt(12) lgkmcnt(0)\n"
      "s_barrier\n"
      "v_mfma_f32_16x16x32_bf16 v[40:43], a[96:99], v[200:203], v[40:43]\n"
      "ds_read_b128 a[64:67], v242 offset:32768\n"
      "v_exp_f32 v114, v134\n"
      "v_mfma_f32_16x16x32_bf16 v[44:47], a[96:99], v[204:207], v[44:47]\n"
      "ds_read_b128 a[68:71], v242 offset:36864\n"
      "ds_read_b128 a[72:75], v243 offset:32768\n"
      "v_mfma_f32_16x16x32_bf16 v[60:63], a[100:103], v[204:207], v[60:63]\n"
      "v_exp_f32 v115, v135\n"
      "v_mfma_f32_16x16x32_bf16 v[56:59], a[100:103], v[200:203], v[56:59]\n"
      "ds_read_b128 a[76:79], v243 offset:36864\n"
      "v_cvt_pk_bf16_f32 v195, v114, v115\n"
      "v_mfma_f32_16x16x32_bf16 a[136:139], a[104:107], v[200:203], a[136:139]\n"
      "v_exp_f32 v112, v136\n"
      "v_mfma_f32_16x16x32_bf16 a[140:143], a[104:107], v[204:207], a[140:143]\n"
      "ds_read_b128 a[80:83], v242 offset:33792\n"
      "v_or3_b32 v116, v116, v194, v195\n"
      "v_mfma_f32_16x16x32_bf16 a[156:159], a[108:111], v[204:207], a[156:159]\n"
      "v_exp_f32 v113, v137\n"
      "v_mfma_f32_16x16x32_bf16 a[152:155], a[108:111], v[200:203], a[152:155]\n"
      "ds_read_b128 a[84:87], v242 offset:37888\n"
      "v_cvt_pk_bf16_f32 v196, v112, v113\n"
      "v_mfma_f32_16x16x32_bf16 v[72:75], a[112:115], v[200:203], v[72:75]\n"
      "v_exp_f32 v114, v138\n"
      "v_mfma_f32_16x16x32_bf16 v[76:79], a[112:115], v[204:207], v[76:79]\n"
      "ds_read_b128 a[88:91], v243 offset:33792\n"
      "ds_read_b128 a[92:95], v243 offset:37888\n"
      "v_mfma_f32_16x16x32_bf16 v[92:95], a[116:119], v[204:207], v[92:95]\n"
      "v_exp_f32 v115, v139\n"
      "v_mfma_f32_16x16x32_bf16 v[88:91], a[116:119], v[200:203], v[88:91]\n"
      "v_cvt_pk_bf16_f32 v197, v114, v115\n"
      "v_mfma_f32_16x16x32_bf16 a[168:171], a[120:123], v[200:203], a[168:171]\n"
      "v_exp_f32 v112, v140\n"
      "v_mfma_f32_16x16x32_bf16 a[172:175], a[120:123], v[204:207], a[172:175]\n"
      "v_or3_b32 v116, v116, v196, v197\n"
      "v_mfma_f32_16x16x32_bf16 a[188:191], a[124:127], v[204:207], a[188:191]\n"
      "v_exp_f32 v113, v141\n"
      "v_mfma_f32_16x16x32_bf16 a[184:187], a[124:127], v[200:203], a[184:187]\n"
      "s_waitcnt lgkmcnt(0)\n"
      "v_cvt_pk_bf16_f32 v198, v112, v113\n"
      "v_mfma_f32_16x16x32_bf16 v[160:163], a[64:67], a[0:3], v[224:227]\n"
      "v_exp_f32 v114, v142\n"
      "v_mfma_f32_16x16x32_bf16 v[160:163], a[72:75], a[16:19], v[160:163]\n"
      "ds_read_b64_tr_b16 a[96:97], v244 offset:32768\n"
      "ds_read_b64_tr_b16 a[98:99], v244 offset:36864\n"
      "v_mfma_f32_16x16x32_bf16 v[160:163], a[80:83], a[32:35], v[160:163]\n"
      "v_exp_f32 v115, v143\n"
      "v_mfma_f32_16x16x32_bf16 v[160:163], a[88:91], a[48:51], v[160:163]\n"
      "ds_read_b64_tr_b16 a[100:101], v245 offset:32768\n"
      "v_cvt_pk_bf16_f32 v199, v114, v115\n"
      "v_mfma_f32_16x16x32_bf16 v[164:167], a[92:95], a[48:51], v[224:227]\n"
      "v_exp_f32 v112, v144\n"
      "v_mfma_f32_16x16x32_bf16 v[164:167], a[84:87], a[32:35], v[164:167]\n"
      "ds_read_b64_tr_b16 a[102:103], v245 offset:36864\n"
      "v_or3_b32 v116, v116, v198, v199\n"
      "v_mfma_f32_16x16x32_bf16 v[164:167], a[76:79], a[16:19], v[164:167]\n"
      "v_exp_f32 v113, v145\n"
      "v_mfma_f32_16x16x32_bf16 v[164:167], a[68:71], a[0:3], v[164:167]\n"
      "ds_read_b64_tr_b16 a[104:105], v246 offset:32768\n"
      "v_cvt_pk_bf16_f32 v200, v112, v113\n"
      "v_mfma_f32_16x16x32_bf16 v[172:175], a[68:71], a[4:7], v[228:231]\n"
      "v_exp_f32 v114, v146\n"
      "v_mfma_f32_16x16x32_bf16 v[172:175], a[76:79], a[20:23], v[172:175]\n"
      "ds_read_b64_tr_b16 a[106:107], v246 offset:36864\n"
      "ds_read_b64_tr_b16 a[108:109], v247 offset:32768\n"
      "v_mfma_f32_16x16x32_bf16 v[172:175], a[84:87], a[36:39], v[172:175]\n"
      "v_exp_f32 v115, v147\n"
      "v_mfma_f32_16x16x32_bf16 v[172:175], a[92:95], a[52:55], v[172:175]\n"
      "ds_read_b64_tr_b16 a[110:111], v247 offset:36864\n"
      "v_cvt_pk_bf16_f32 v201, v114, v115\n"
      "v_mfma_f32_16x16x32_bf16 v[168:171], a[88:91], a[52:55], v[228:231]\n"
      "v_exp_f32 v112, v148\n"
      "v_mfma_f32_16x16x32_bf16 v[168:171], a[80:83], a[36:39], v[168:171]\n"
      "ds_read_b64_tr_b16 a[112:113], v244 offset:33792\n"
      "v_or3_b32 v116, v116, v200, v201\n"
      "v_mfma_f32_16x16x32_bf16 v[168:171], a[72:75], a[20:23], v[168:171]\n"
      "v_exp_f32 v113, v149\n"
      "v_mfma_f32_16x16x32_bf16 v[168:171], a[64:67], a[4:7], v[168:171]\n"
      "ds_read_b64_tr_b16 a[114:115], v244 offset:37888\n"
      "v_cvt_pk_bf16_f32 v202, v112, v113\n"
      "v_mfma_f32_16x16x32_bf16 v[176:179], a[64:67], a[8:11], v[232:235]\n"
      "v_exp_f32 v114, v150\n"
      "v_mfma_f32_16x16x32_bf16 v[176:179], a[72:75], a[24:27], v[176:179]\n"
      "ds_read_b64_tr_b16 a[116:117], v245 offset:33792\n"
      "ds_read_b64_tr_b16 a[118:119], v245 offset:37888\n"
      "v_mfma_f32_16x16x32_bf16 v[176:179], a[80:83], a[40:43], v[176:179]\n"
      "v_exp_f32 v115, v151\n"
      "v_mfma_f32_16x16x32_bf16 v[176:179], a[88:91], a[56:59], v[176:179]\n"
      "ds_read_b64_tr_b16 a[120:121], v246 offset:33792\n"
      "v_cvt_pk_bf16_f32 v203, v114, v115\n"
      "v_mfma_f32_16x16x32_bf16 v[180:183], a[92:95], a[56:59], v[232:235]\n"
      "v_exp_f32 v112, v152\n"
      "v_mfma_f32_16x16x32_bf16 v[180:183], a[84:87], a[40:43], v[180:183]\n"
      "ds_read_b64_tr_b16 a[122:123], v246 offset:37888\n"
      "v_or3_b32 v116, v116, v202, v203\n"
      "v_mfma_f32_16x16x32_bf16 v[180:183], a[76:79], a[24:27], v[180:183]\n"
      "v_exp_f32 v113, v153\n"
      "v_mfma_f32_16x16x32_bf16 v[180:183], a[68:71], a[8:11], v[180:183]\n"
      "ds_read_b64_tr_b16 a[124:125], v247 offset:33792\n"
      "v_cvt_pk_bf16_f32 v204, v112, v113\n"
      "v_mfma_f32_16x16x32_bf16 v[188:191], a[68:71], a[12:15], v[236:239]\n"
      "v_exp_f32 v114, v154\n"
      "v_mfma_f32_16x16x32_bf16 v[188:191], a[76:79], a[28:31], v[188:191]\n"
      "ds_read_b64_tr_b16 a[126:127], v247 offset:37888\n"
      "v_mfma_f32_16x16x32_bf16 v[188:191], a[84:87], a[44:47], v[188:191]\n"
      "v_exp_f32 v115, v155\n"
      "v_mfma_f32_16x16x32_bf16 v[188:191], a[92:95], a[60:63], v[188:191]\n"
      "v_cvt_pk_bf16_f32 v205, v114, v115\n"
      "v_mfma_f32_16x16x32_bf16 v[184:187], a[88:91], a[60:63], v[236:239]\n"
      "v_exp_f32 v112, v156\n"
      "v_mfma_f32_16x16x32_bf16 v[184:187], a[80:83], a[44:47], v[184:187]\n"
      "v_or3_b32 v116, v116, v204, v205\n"
      "v_mfma_f32_16x16x32_bf16 v[184:187], a[72:75], a[28:31], v[184:187]\n"
      "v_exp_f32 v113, v157\n"
      "v_mfma_f32_16x16x32_bf16 v[184:187], a[64:67], a[12:15], v[184:187]\n"
      "s_waitcnt lgkmcnt(10)\n"
      "v_cvt_pk_bf16_f32 v206, v112, v113\n"
      "v_mfma_f32_16x16x32_bf16 v[32:35], a[96:99], v[208:211], v[32:35]\n"
      "v_exp_f32 v114, v158\n"
      "v_mfma_f32_16x16x32_bf16 v[48:51], a[100:103], v[208:211], v[48:51]\n"
      "v_mfma_f32_16x16x32_bf16 a[128:131], a[104:107], v[208:211], a[128:131]\n"
      "s_waitcnt lgkmcnt(6)\n"
      "v_exp_f32 v115, v159\n"
      "v_mfma_f32_16x16x32_bf16 a[144:147], a[108:111], v[208:211], a[144:147]\n"
      "v_cvt_pk_bf16_f32 v207, v114, v115\n"
      "v_mfma_f32_16x16x32_bf16 v[64:67], a[112:115], v[208:211], v[64:67]\n"
      "s_waitcnt lgkmcnt(2)\n"
      "v_mfma_f32_16x16x32_bf16 v[80:83], a[116:119], v[208:211], v[80:83]\n"
      "v_or3_b32 v116, v116, v206, v207\n"
      "v_mfma_f32_16x16x32_bf16 a[160:163], a[120:123], v[208:211], a[160:163]\n"
      "s_mov_b32 s64, 1\n"
      "s_waitcnt lgkmcnt(0)\n"
      "v_and_b32 v116, 0x40004000, v116\n"
      "v_mfma_f32_16x16x32_bf16 a[176:179], a[124:127], v[208:211], a[176:179]\n"
      "v_cmp_ne_u32 vcc, 0, v116\n"
      "s_cbranch_vccnz FB0_%=\n"
      "RET0_1_%=:\n"
      "v_mfma_f32_16x16x32_bf16 a[180:183], a[124:127], v[212:215], a[180:183]\n"
      "v_exp_f32 v112, v160\n"
      "v_mfma_f32_16x16x32_bf16 a[164:167], a[120:123], v[212:215], a[164:167]\n"
      "v_xor_b32 v242, 0x10000, v242\n"
      "v_mfma_f32_16x16x32_bf16 a[192:195], a[196:199], v[192:195], a[192:195]\n"
      "s_add_u32 m0, s77, 16384\n"
      "v_exp_f32 v113, v161\n"
      "v_mfma_f32_16x16x32_bf16 v[84:87], a[116:119], v[212:215], v[84:87]\n"
      "buffer_load_dwordx4 v249, s[24:27], s61 offen nt lds\n"
      "v_cvt_pk_bf16_f32 v208, v112, v113\n"
      "v_xor_b32 v243, 0x10000, v243\n"
      "v_mfma_f32_16x16x32_bf16 v[68:71], a[112:115], v[212:215], v[68:71]\n"
      "s_add_u32 m0, s77, 17408\n"
      "v_exp_f32 v114, v162\n"
      "v_mfma_f32_16x16x32_bf16 a[192:195], a[200:203], v[196:199], a[192:195]\n"
      "buffer_load_dwordx4 v121, s[24:27], s61 offen nt lds\n"
      "v_xor_b32 v244, 0x10000, v244\n"
      "v_mfma_f32_16x16x32_bf16 a[148:151], a[108:111], v[212:215], a[148:151]\n"
      "s_add_u32 m0, s77, 24576\n"
      "v_exp_f32 v115, v163\n"
      "v_mfma_f32_16x16x32_bf16 a[132:135], a[104:107], v[212:215], a[132:135]\n"
      "s_memrealtime s[68:69]\n"
      "buffer_load_dwordx4 v122, s[24:27], s61 offen nt lds\n"
      "v_cvt_pk_bf16_f32 v209, v114, v115\n"
      "v_xor_b32 v245, 0x10000, v245\n"
      "v_mfma_f32_16x16x32_bf16 a[192:195], a[204:207], v[200:203], a[192:195]\n"
      "s_add_u32 m0, s77, 25600\n"
      "v_exp_f32 v112, v164\n"
      "v_mfma_f32_16x16x32_bf16 v[52:55], a[100:103], v[212:215], v[52:55]\n"
      "s_add_u32 s77, s77, 0x8000\n"
      "buffer_load_dwordx4 v123, s[24:27], s61 offen nt lds\n"
      "v_or_b32 v116, v208, v209\n"
      "v_xor_b32 v246, 0x10000, v246\n"
      "v_mfma_f32_16x16x32_bf16 v[36:39], a[96:99], v[212:215], v[36:39]\n"
      "s_and_b32 s77, s77, 0x1ffff\n"
      "v_exp_f32 v113, v165\n"
      "v_mfma_f32_16x16x32_bf16 a[192:195], a[208:211], v[204:207], a[192:195]\n"
      "s_add_u32 s61, s61, s78\n"
      "v_cvt_pk_bf16_f32 v210, v112, v113\n"
      "v_xor_b32 v247, 0x10000, v247\n"
      "s_waitcnt vmcnt(8) lgkmcnt(0)\n"
      "s_sub_u32 s67, s68, s90\n"
      "s_cmp_lt_i32 s67, 0\n"
      "s_cbranch_scc1 GW3_%=\n"
      "GX3_%=:\n"
      "s_barrier\n"
      "v_mfma_f32_16x16x32_bf16 v[40:43], a[96:99], v[216:219], v[40:43]\n"
      "s_memrealtime s[68:69]\n"
      "buffer_load_dword v250, v251, s[28:31], 0 offen sc1\n"
      "ds_read_b128 a[64:67], v242 offset:0\n"
      "v_exp_f32 v114, v166\n"
      "v_mfma_f32_16x16x32_bf16 v[44:47], a[96:99], v[220:223], v[44:47]\n"
      "s_add_u32 m0, s62, 0\n"
      "ds_read_b128 a[68:71], v242 offset:4096\n"
      "ds_read_b128 a[72:75], v243 offset:0\n"
      "v_mfma_f32_16x16x32_bf16 v[60:63], a[100:103], v[220:223], v[60:63]\n"
      "buffer_load_dwordx4 v248, s[20:23], s60 offen nt lds\n"
      "v_exp_f32 v115, v167\n"
      "v_mfma_f32_16x16x32_bf16 v[56:59], a[100:103], v[216:219], v[56:59]\n"
      "s_add_u32 m0, s62, 896\n"
      "ds_read_b128 a[76:79], v243 offset:4096\n"
      "v_cvt_pk_bf16_f32 v211, v114, v115\n"
      "v_mfma_f32_16x16x32_bf16 a[136:139], a[104:107], v[216:219], a[136:139]\n"
      "buffer_load_dwordx4 v248, s[20:23], s60 offen offset:128 nt lds\n"
      "v_exp_f32 v112, v168\n"
      "v_mfma_f32_16x16x32_bf16 a[140:143], a[104:107], v[220:223], a[140:143]\n"
      "s_add_u32 m0, s62, 8192\n"
      "ds_read_b128 a[80:83], v242 offset:1024\n"
      "v_or3_b32 v116, v116, v210, v211\n"
      "v_mfma_f32_16x16x32_bf16 a[156:159], a[108:111], v[220:223], a[156:159]\n"
      "buffer_load_dwordx4 v120, s[20:23], s60 offen nt lds\n"
      "v_exp_f32 v113, v169\n"
      "v_mfma_f32_16x16x32_bf16 a[152:155], a[108:111], v[216:219], a[152:155]\n"
      "s_add_u32 m0, s62, 9088\n"
      "ds_read_b128 a[84:87], v242 offset:5120\n"
      "v_cvt_pk_bf16_f32 v212, v112, v113\n"
      "v_mfma_f32_16x16x32_bf16 v[72:75], a[112:115], v[216:219], v[72:75]\n"
      "s_add_u32 s62, s62, 0x8000\n"
      "buffer_load_dwordx4 v120, s[20:23], s60 offen offset:128 nt lds\n"
      "v_exp_f32 v114, v170\n"
      "v_mfma_f32_16x16x32_bf16 v[76:79], a[112:115], v[220:223], v[76:79]\n"
      "s_and_b32 s62, s62, 0x1ffff\n"
      "ds_read_b128 a[88:91], v243 offset:1024\n"
      "ds_read_b128 a[92:95], v243 offset:5120\n"
      "v_mfma_f32_16x16x32_bf16 v[92:95], a[116:119], v[220:223], v[92:95]\n"
      "s_add_u32 s60, s60, %[inc]\n"
      "v_exp_f32 v115, v171\n"
      "v_mfma_f32_16x16x32_bf16 v[88:91], a[116:119], v[216:219], v[88:91]\n"
      "v_cvt_pk_bf16_f32 v213, v114, v115\n"
      "v_mfma_f32_16x16x32_bf16 a[168:171], a[120:123], v[216:219], a[168:171]\n"
      "v_exp_f32 v112, v172\n"
      "v_mfma_f32_16x16x32_bf16 a[172:175], a[120:123], v[220:223], a[172:175]\n"
      "v_or3_b32 v116, v116, v212, v213\n"
      "v_mfma_f32_16x16x32_bf16 a[188:191], a[124:127], v[220:223], a[188:191]\n"
      "v_exp_f32 v113, v173\n"
      "v_mfma_f32_16x16x32_bf16 a[184:187], a[124:127], v[216:219], a[184:187]\n"
      "s_waitcnt lgkmcnt(0)\n"
      "v_cvt_pk_bf16_f32 v214, v112, v113\n"
      "v_mfma_f32_16x16x32_bf16 v[128:131], a[64:67], a[0:3], v[224:227]\n"
      "s_sub_u32 s67, s68, s66\n"
      "v_exp_f32 v114, v174\n"
      "v_mfma_f32_16x16x32_bf16 v[128:131], a[72:75], a[16:19], v[128:131]\n"
      "s_cmp_gt_i32 s67, s70\n"
      "ds_read_b64_tr_b16 a[96:97], v244 offset:0\n"
      "ds_read_b64_tr_b16 a[98:99], v244 offset:4096\n"
      "v_mfma_f32_16x16x32_bf16 v[128:131], a[80:83], a[32:35], v[128:131]\n"
      "s_cselect_b32 s69, s86, 0\n"
      "v_exp_f32 v115, v175\n"
      "v_mfma_f32_16x16x32_bf16 v[128:131], a[88:91], a[48:51], v[128:131]\n"
      "s_add_u32 s66, s66, s69\n"
      "ds_read_b64_tr_b16 a[100:101], v245 offset:0\n"
      "v_cvt_pk_bf16_f32 v215, v114, v115\n"
      "v_mfma_f32_16x16x32_bf16 v[132:135], a[92:95], a[48:51], v[224:227]\n"
      "s_add_u32 s67, s67, s101\n"
      "v_exp_f32 v112, v176\n"
      "v_mfma_f32_16x16x32_bf16 v[132:135], a[84:87], a[32:35], v[132:135]\n"
      "s_min_i32 s70, s70, s67\n"
      "ds_read_b64_tr_b16 a[102:103], v245 offset:4096\n"
      "v_or3_b32 v116, v116, v214, v215\n"
      "v_mfma_f32_16x16x32_bf16 v[132:135], a[76:79], a[16:19], v[132:135]\n"
      "s_max_i32 s70, s70, 85\n"
      "v_exp_f32 v113, v177\n"
      "v_mfma_f32_16x16x32_bf16 v[132:135], a[68:71], a[0:3], v[132:135]\n"
      "s_add_u32 s66, s66, s86\n"
      "ds_read_b64_tr_b16 a[104:105], v246 offset:0\n"
      "v_cvt_pk_bf16_f32 v216, v112, v113\n"
      "v_mfma_f32_16x16x32_bf16 v[140:143], a[68:71], a[4:7], v[228:231]\n"
      "s_and_b32 s76, s80, 31\n"
      "v_exp_f32 v114, v178\n"
      "v_mfma_f32_16x16x32_bf16 v[140:143], a[76:79], a[20:23], v[140:143]\n"
      "s_lshr_b64 s[80:81], s[80:81], 5\n"
      "ds_read_b64_tr_b16 a[106:107], v246 offset:4096\n"
      "ds_read_b64_tr_b16 a[108:109], v247 offset:0\n"
      "v_mfma_f32_16x16x32_bf16 v[140:143], a[84:87], a[36:39], v[140:143]\n"
      "s_add_u32 s66, s66, s76\n"
      "v_exp_f32 v115, v179\n"
      "v_mfma_f32_16x16x32_bf16 v[140:143], a[92:95], a[52:55], v[140:143]\n"
      "s_sub_u32 s67, s68, s82\n"
      "ds_read_b64_tr_b16 a[110:111], v247 offset:4096\n"
      "v_cvt_pk_bf16_f32 v217, v114, v115\n"
      "v_mfma_f32_16x16x32_bf16 v[136:139], a[88:91], a[52:55], v[228:231]\n"
      "s_min_i32 s76, s67, s83\n"
      "v_exp_f32 v112, v180\n"
      "v_mfma_f32_16x16x32_bf16 v[136:139], a[80:83], a[36:39], v[136:139]\n"
      "s_mov_b32 s83, s67\n"
      "ds_read_b64_tr_b16 a[112:113], v244 offset:1024\n"
      "v_or3_b32 v116, v116, v216, v217\n"
      "v_mfma_f32_16x16x32_bf16 v[136:139], a[72:75], a[20:23], v[136:139]\n"
      "s_sub_u32 s76, s76, 36\n"
      "v_exp_f32 v113, v181\n"
      "v_mfma_f32_16x16x32_bf16 v[136:139], a[64:67], a[4:7], v[136:139]\n"
      "s_min_i32 s76, s76, 148\n"
      "ds_read_b64_tr_b16 a[114:115], v244 offset:5120\n"
      "v_cvt_pk_bf16_f32 v218, v112, v113\n"
      "v_mfma_f32_16x16x32_bf16 v[144:147], a[64:67], a[8:11], v[232:235]\n"
      "s_max_i32 s76, s76, 70\n"
      "v_exp_f32 v114, v182\n"
      "v_mfma_f32_16x16x32_bf16 v[144:147], a[72:75], a[24:27], v[144:147]\n"
      "s_mov_b32 s82, s68\n"
      "ds_read_b64_tr_b16 a[116:117], v245 offset:1024\n"
      "ds_read_b64_tr_b16 a[118:119], v245 offset:5120\n"
      "v_mfma_f32_16x16x32_bf16 v[144:147], a[80:83], a[40:43], v[144:147]\n"
      "s_lshr_b32 s69, s86, 1\n"
      "v_exp_f32 v115, v183\n"
      "v_mfma_f32_16x16x32_bf16 v[144:147], a[88:91], a[56:59], v[144:147]\n"
      "s_add_u32 s66, s66, s69\n"
      "ds_read_b64_tr_b16 a[120:121], v246 offset:1024\n"
      "v_cvt_pk_bf16_f32 v219, v114, v115\n"
      "v_mfma_f32_16x16x32_bf16 v[148:151], a[92:95], a[56:59], v[232:235]\n"
      "s_add_u32 s82, s82, s69\n"
      "v_exp_f32 v112, v184\n"
      "v_mfma_f32_16x16x32_bf16 v[148:151], a[84:87], a[40:43], v[148:151]\n"
      "s_add_u32 s65, s82, s76\n"
      "ds_read_b64_tr_b16 a[122:123], v246 offset:5120\n"
      "v_or3_b32 v116, v116, v218, v219\n"
      "v_mfma_f32_16x16x32_bf16 v[148:151], a[76:79], a[24:27], v[148:151]\n"
      "s_sub_u32 s93, s93, 1\n"
      "v_exp_f32 v113, v185\n"
      "v_mfma_f32_16x16x32_bf16 v[148:151], a[68:71], a[8:11], v[148:151]\n"
      "s_cmp_le_i32 s93, 14\n"
      "ds_read_b64_tr_b16 a[124:125], v247 offset:1024\n"
      "v_cvt_pk_bf16_f32 v220, v112, v113\n"
      "v_mfma_f32_16x16x32_bf16 v[156:159], a[68:71], a[12:15], v[236:239]\n"
      "s_cselect_b32 s86, 107, s86\n"
      "v_exp_f32 v114, v186\n"
      "v_mfma_f32_16x16x32_bf16 v[156:159], a[76:79], a[28:31], v[156:159]\n"
      "s_sub_u32 s67, s65, s66\n"
      "ds_read_b64_tr_b16 a[126:127], v247 offset:5120\n"
      "v_mfma_f32_16x16x32_bf16 v[156:159], a[84:87], a[44:47], v[156:159]\n"
      "s_cmp_lt_i32 s67, 0\n"
      "v_exp_f32 v115, v187\n"
      "v_mfma_f32_16x16x32_bf16 v[156:159], a[92:95], a[60:63], v[156:159]\n"
      "s_cselect_b32 s88, s66, s65\n"
      "v_cvt_pk_bf16_f32 v221, v114, v115\n"
      "v_mfma_f32_16x16x32_bf16 v[152:155], a[88:91], a[60:63], v[236:239]\n"
      "s_add_u32 s90, s88, s91\n"
      "v_exp_f32 v112, v188\n"
      "v_mfma_f32_16x16x32_bf16 v[152:155], a[80:83], a[44:47], v[152:155]\n"
      "v_or3_b32 v116, v116, v220, v221\n"
      "v_mfma_f32_16x16x32_bf16 v[152:155], a[72:75], a[28:31], v[152:155]\n"
      "v_exp_f32 v113, v189\n"
      "v_mfma_f32_16x16x32_bf16 v[152:155], a[64:67], a[12:15], v[152:155]\n"
      "s_waitcnt lgkmcnt(10)\n"
      "v_cvt_pk_bf16_f32 v222, v112, v113\n"
      "v_mfma_f32_16x16x32_bf16 v[32:35], a[96:99], v[192:195], v[32:35]\n"
      "v_exp_f32 v114, v190\n"
      "v_mfma_f32_16x16x32_bf16 v[48:51], a[100:103], v[192:195], v[48:51]\n"
      "v_mfma_f32_16x16x32_bf16 a[128:131], a[104:107], v[192:195], a[128:131]\n"
      "s_waitcnt lgkmcnt(6)\n"
      "v_exp_f32 v115, v191\n"
      "v_mfma_f32_16x16x32_bf16 a[144:147], a[108:111], v[192:195], a[144:147]\n"
      "v_cvt_pk_bf16_f32 v223, v114, v115\n"
      "v_mfma_f32_16x16x32_bf16 v[64:67], a[112:115], v[192:195], v[64:67]\n"
      "s_waitcnt lgkmcnt(2)\n"
      "v_mfma_f32_16x16x32_bf16 v[80:83], a[116:119], v[192:195], v[80:83]\n"
      "v_or3_b32 v116, v116, v222, v223\n"
      "v_mfma_f32_16x16x32_bf16 a[160:163], a[120:123], v[192:195], a[160:163]\n"
      "s_mov_b32 s64, 2\n"
      "s_waitcnt lgkmcnt(0)\n"
      "v_and_b32 v116, 0x40004000, v116\n"
      "v_mfma_f32_16x16x32_bf16 a[176:179], a[124:127], v[192:195], a[176:179]\n"
      "v_cmp_ne_u32 vcc, 0, v116\n"
      "s_cbranch_vccnz FB1_%=\n"
      "RET1_2_%=:\n"
      "v_mfma_f32_16x16x32_bf16 a[180:183], a[124:127], v[196:199], a[180:183]\n"
      "v_exp_f32 v112, v128\n"
      "v_mfma_f32_16x16x32_bf16 a[164:167], a[120:123], v[196:199], a[164:167]\n"
      "v_mfma_f32_16x16x32_bf16 a[192:195], a[196:199], v[208:211], a[192:195]\n"
      "s_add_u32 m0, s77, 16384\n"
      "v_exp_f32 v113, v129\n"
      "v_mfma_f32_16x16x32_bf16 v[84:87], a[116:119], v[196:199], v[84:87]\n"
      "buffer_load_dwordx4 v249, s[24:27], s61 offen nt lds\n"
      "v_cvt_pk_bf16_f32 v192, v112, v113\n"
      "v_mfma_f32_16x16x32_bf16 v[68:71], a[112:115], v[196:199], v[68:71]\n"
      "s_add_u32 m0, s77, 17408\n"
      "v_exp_f32 v114, v130\n"
      "v_mfma_f32_16x16x32_bf16 a[192:195], a[200:203], v[212:215], a[192:195]\n"
      "buffer_load_dwordx4 v121, s[24:27], s61 offen nt lds\n"
      "v_mfma_f32_16x16x32_bf16 a[148:151], a[108:111], v[196:199], a[148:151]\n"
      "s_add_u32 m0, s77, 24576\n"
      "v_exp_f32 v115, v131\n"
      "v_mfma_f32_16x16x32_bf16 a[132:135], a[104:107], v[196:199], a[132:135]\n"
      "s_memrealtime s[68:69]\n"
      "buffer_load_dwordx4 v122, s[24:27], s61 offen nt lds\n"
      "v_cvt_pk_bf16_f32 v193, v114, v115\n"
      "v_mfma_f32_16x16x32_bf16 a[192:195], a[204:207], v[216:219], a[192:195]\n"
      "s_add_u32 m0, s77, 25600\n"
      "v_exp_f32 v112, v132\n"
      "v_mfma_f32_16x16x32_bf16 v[52:55], a[100:103], v[196:199], v[52:55]\n"
      "s_add_u32 s77, s77, 0x8000\n"
      "buffer_load_dwordx4 v123, s[24:27], s61 offen nt lds\n"
      "v_or_b32 v116, v192, v193\n"
      "v_mfma_f32_16x16x32_bf16 v[36:39], a[96:99], v[196:199], v[36:39]\n"
      "s_and_b32 s77, s77, 0x1ffff\n"
      "v_exp_f32 v113, v133\n"
      "v_mfma_f32_16x16x32_bf16 a[192:195], a[208:211], v[220:223], a[192:195]\n"
      "s_add_u32 s61, s61, %[inc]\n"
      "v_cvt_pk_bf16_f32 v194, v112, v113\n"
      "s_waitcnt vmcnt(9) lgkmcnt(0)\n"
      "s_barrier\n"
      "v_mfma_f32_16x16x32_bf16 v[40:43], a[96:99], v[200:203], v[40:43]\n"
      "ds_read_b128 a[64:67], v242 offset:32768\n"
      "v_exp_f32 v114, v134\n"
      "v_mfma_f32_16x16x32_bf16 v[44:47], a[96:99], v[204:207], v[44:47]\n"
      "ds_read_b128 a[68:71], v242 offset:36864\n"
      "ds_read_b128 a[72:75], v243 offset:32768\n"
      "v_mfma_f32_16x16x32_bf16 v[60:63], a[100:103], v[204:207], v[60:63]\n"
      "v_exp_f32 v115, v135\n"
      "v_mfma_f32_16x16x32_bf16 v[56:59], a[100:103], v[200:203], v[56:59]\n"
      "ds_read_b128 a[76:79], v243 offset:36864\n"
      "v_cvt_pk_bf16_f32 v195, v114, v115\n"
      "v_mfma_f32_16x16x32_bf16 a[136:139], a[104:107], v[200:203], a[136:139]\n"
      "v_exp_f32 v112, v136\n"
      "v_mfma_f32_16x16x32_bf16 a[140:143], a[104:107], v[204:207], a[140:143]\n"
      "ds_read_b128 a[80:83], v242 offset:33792\n"
      "v_or3_b32 v116, v116, v194, v195\n"
      "v_mfma_f32_16x16x32_bf16 a[156:159], a[108:111], v[204:207], a[156:159]\n"
      "v_exp_f32 v113, v137\n"
      "v_mfma_f32_16x16x32_bf16 a[152:155], a[108:111], v[200:203], a[152:155]\n"
      "ds_read_b128 a[84:87], v242 offset:37888\n"
      "v_cvt_pk_bf16_f32 v196, v112, v113\n"
      "v_mfma_f32_16x16x32_bf16 v[72:75], a[112:115], v[200:203], v[72:75]\n"
      "v_exp_f32 v114, v138\n"
      "v_mfma_f32_16x16x32_bf16 v[76:79], a[112:115], v[204:207], v[76:79]\n"
      "ds_read_b128 a[88:91], v243 offset:33792\n"
      "ds_read_b128 a[92:95], v243 offset:37888\n"
      "v_mfma_f32_16x16x32_bf16 v[92:95], a[116:119], v[204:207], v[92:95]\n"
      "v_exp_f32 v115, v139\n"
      "v_mfma_f32_16x16x32_bf16 v[88:91], a[116:119], v[200:203], v[88:91]\n"
      "v_cvt_pk_bf16_f32 v197, v114, v115\n"
      "v_mfma_f32_16x16x32_bf16 a[168:171], a[120:123], v[200:203], a[168:171]\n"
      "v_exp_f32 v112, v140\n"
      "v_mfma_f32_16x16x32_bf16 a[172:175], a[120:123], v[204:207], a[172:175]\n"
      "v_or3_b32 v116, v116, v196, v197\n"
      "v_mfma_f32_16x16x32_bf16 a[188:191], a[124:127], v[204:207], a[188:191]\n"
      "v_exp_f32 v113, v141\n"
      "v_mfma_f32_16x16x32_bf16 a[184:187], a[124:127], v[200:203], a[184:187]\n"
      "s_waitcnt lgkmcnt(0)\n"
      "v_cvt_pk_bf16_f32 v198, v112, v113\n"
      "v_mfma_f32_16x16x32_bf16 v[160:163], a[64:67], a[0:3], v[224:227]\n"
      "v_exp_f32 v114, v142\n"
      "v_mfma_f32_16x16x32_bf16 v[160:163], a[72:75], a[16:19], v[160:163]\n"
      "ds_read_b64_tr_b16 a[96:97], v244 offset:32768\n"
      "ds_read_b64_tr_b16 a[98:99], v244 offset:36864\n"
      "v_mfma_f32_16x16x32_bf16 v[160:163], a[80:83], a[32:35], v[160:163]\n"
      "v_exp_f32 v115, v143\n"
      "v_mfma_f32_16x16x32_bf16 v[160:163], a[88:91], a[48:51], v[160:163]\n"
      "ds_read_b64_tr_b16 a[100:101], v245 offset:32768\n"
      "v_cvt_pk_bf16_f32 v199, v114, v115\n"
      "v_mfma_f32_16x16x32_bf16 v[164:167], a[92:95], a[48:51], v[224:227]\n"
      "v_exp_f32 v112, v144\n"
      "v_mfma_f32_16x16x32_bf16 v[164:167], a[84:87], a[32:35], v[164:167]\n"
      "ds_read_b64_tr_b16 a[102:103], v245 offset:36864\n"
      "v_or3_b32 v116, v116, v198, v199\n"
      "v_mfma_f32_16x16x32_bf16 v[164:167], a[76:79], a[16:19], v[164:167]\n"
      "v_exp_f32 v113, v145\n"
      "v_mfma_f32_16x16x32_bf16 v[164:167], a[68:71], a[0:3], v[164:167]\n"
      "ds_read_b64_tr_b16 a[104:105], v246 offset:32768\n"
      "v_cvt_pk_bf16_f32 v200, v112, v113\n"
      "v_mfma_f32_16x16x32_bf16 v[172:175], a[68:71], a[4:7], v[228:231]\n"
      "v_exp_f32 v114, v146\n"
      "v_mfma_f32_16x16x32_bf16 v[172:175], a[76:79], a[20:23], v[172:175]\n"
      "ds_read_b64_tr_b16 a[106:107], v246 offset:36864\n"
      "ds_read_b64_tr_b16 a[108:109], v247 offset:32768\n"
      "v_mfma_f32_16x16x32_bf16 v[172:175], a[84:87], a[36:39], v[172:175]\n"
      "v_exp_f32 v115, v147\n"
      "v_mfma_f32_16x16x32_bf16 v[172:175], a[92:95], a[52:55], v[172:175]\n"
      "ds_read_b64_tr_b16 a[110:111], v247 offset:36864\n"
      "v_cvt_pk_bf16_f32 v201, v114, v115\n"
      "v_mfma_f32_16x16x32_bf16 v[168:171], a[88:91], a[52:55], v[228:231]\n"
      "v_exp_f32 v112, v148\n"
      "v_mfma_f32_16x16x32_bf16 v[168:171], a[80:83], a[36:39], v[168:171]\n"
      "ds_read_b64_tr_b16 a[112:113], v244 offset:33792\n"
      "v_or3_b32 v116, v116, v200, v201\n"
      "v_mfma_f32_16x16x32_bf16 v[168:171], a[72:75], a[20:23], v[168:171]\n"
      "v_exp_f32 v113, v149\n"
      "v_mfma_f32_16x16x32_bf16 v[168:171], a[64:67], a[4:7], v[168:171]\n"
      "ds_read_b64_tr_b16 a[114:115], v244 offset:37888\n"
      "v_cvt_pk_bf16_f32 v202, v112, v113\n"
      "v_mfma_f32_16x16x32_bf16 v[176:179], a[64:67], a[8:11], v[232:235]\n"
      "v_exp_f32 v114, v150\n"
      "v_mfma_f32_16x16x32_bf16 v[176:179], a[72:75], a[24:27], v[176:179]\n"
      "ds_read_b64_tr_b16 a[116:117], v245 offset:33792\n"
      "ds_read_b64_tr_b16 a[118:119], v245 offset:37888\n"
      "v_mfma_f32_16x16x32_bf16 v[176:179], a[80:83], a[40:43], v[176:179]\n"
      "v_exp_f32 v115, v151\n"
      "v_mfma_f32_16x16x32_bf16 v[176:179], a[88:91], a[56:59], v[176:179]\n"
      "ds_read_b64_tr_b16 a[120:121], v246 offset:33792\n"
      "v_cvt_pk_bf16_f32 v203, v114, v115\n"
      "v_mfma_f32_16x16x32_bf16 v[180:183], a[92:95], a[56:59], v[232:235]\n"
      "v_exp_f32 v112, v152\n"
      "v_mfma_f32_16x16x32_bf16 v[180:183], a[84:87], a[40:43], v[180:183]\n"
      "ds_read_b64_tr_b16 a[122:123], v246 offset:37888\n"
      "v_or3_b32 v116, v116, v202, v203\n"
      "v_mfma_f32_16x16x32_bf16 v[180:183], a[76:79], a[24:27], v[180:183]\n"
      "v_exp_f32 v113, v153\n"
      "v_mfma_f32_16x16x32_bf16 v[180:183], a[68:71], a[8:11], v[180:183]\n"
      "ds_read_b64_tr_b16 a[124:125], v247 offset:33792\n"
      "v_cvt_pk_bf16_f32 v204, v112, v113\n"
      "v_mfma_f32_16x16x32_bf16 v[188:191], a[68:71], a[12:15], v[236:239]\n"
      "v_exp_f32 v114, v154\n"
      "v_mfma_f32_16x16x32_bf16 v[188:191], a[76:79], a[28:31], v[188:191]\n"
      "ds_read_b64_tr_b16 a[126:127], v247 offset:37888\n"
      "v_mfma_f32_16x16x32_bf16 v[188:191], a[84:87], a[44:47], v[188:191]\n"
      "v_exp_f32 v115, v155\n"
      "v_mfma_f32_16x16x32_bf16 v[188:191], a[92:95], a[60:63], v[188:191]\n"
      "v_cvt_pk_bf16_f32 v205, v114, v115\n"
      "v_mfma_f32_16x16x32_bf16 v[184:187], a[88:91], a[60:63], v[236:239]\n"
      "v_exp_f32 v112, v156\n"
      "v_mfma_f32_16x16x32_bf16 v[184:187], a[80:83], a[44:47], v[184:187]\n"
      "v_or3_b32 v116, v116, v204, v205\n"
      "v_mfma_f32_16x16x32_bf16 v[184:187], a[72:75], a[28:31], v[184:187]\n"
      "v_exp_f32 v113, v157\n"
      "v_mfma_f32_16x16x32_bf16 v[184:187], a[64:67], a[12:15], v[184:187]\n"
      "s_waitcnt lgkmcnt(10)\n"
      "v_cvt_pk_bf16_f32 v206, v112, v113\n"
      "v_mfma_f32_16x16x32_bf16 v[32:35], a[96:99], v[208:211], v[32:35]\n"
      "v_exp_f32 v114, v158\n"
      "v_mfma_f32_16x16x32_bf16 v[48:51], a[100:103], v[208:211], v[48:51]\n"
      "v_mfma_f32_16x16x32_bf16 a[128:131], a[104:107], v[208:211], a[128:131]\n"
      "s_waitcnt lgkmcnt(6)\n"
      "v_exp_f32 v115, v159\n"
      "v_mfma_f32_16x16x32_bf16 a[144:147], a[108:111], v[208:211], a[144:147]\n"
      "v_cvt_pk_bf16_f32 v207, v114, v115\n"
      "v_mfma_f32_16x16x32_bf16 v[64:67], a[112:115], v[208:211], v[64:67]\n"
      "s_waitcnt lgkmcnt(2)\n"
      "v_mfma_f32_16x16x32_bf16 v[80:83], a[116:119], v[208:211], v[80:83]\n"
      "v_or3_b32 v116, v116, v206, v207\n"
      "v_mfma_f32_16x16x32_bf16 a[160:163], a[120:123], v[208:211], a[160:163]\n"
      "s_mov_b32 s64, 2\n"
      "s_waitcnt lgkmcnt(0)\n"
      "v_and_b32 v116, 0x40004000, v116\n"
      "v_mfma_f32_16x16x32_bf16 a[176:179], a[124:127], v[208:211], a[176:179]\n"
      "v_cmp_ne_u32 vcc, 0, v116\n"
      "s_cbranch_vccnz FB0_%=\n"
      "RET0_2_%=:\n"
      "v_mfma_f32_16x16x32_bf16 a[180:183], a[124:127], v[212:215], a[180:183]\n"
      "v_exp_f32 v112, v160\n"
      "v_mfma_f32_16x16x32_bf16 a[164:167], a[120:123], v[212:215], a[164:167]\n"
      "v_xor_b32 v242, 0x10000, v242\n"
      "v_mfma_f32_16x16x32_bf16 a[192:195], a[196:199], v[192:195], a[192:195]\n"
      "s_add_u32 m0, s62, 0\n"
      "v_exp_f32 v113, v161\n"
      "v_mfma_f32_16x16x32_bf16 v[84:87], a[116:119], v[212:215], v[84:87]\n"
      "buffer_load_dwordx4 v248, s[20:23], s60 offen nt lds\n"
      "v_cvt_pk_bf16_f32 v208, v112, v113\n"
      "v_xor_b32 v243, 0x10000, v243\n"
      "v_mfma_f32_16x16x32_bf16 v[68:71], a[112:115], v[212:215], v[68:71]\n"
      "s_add_u32 m0, s62, 896\n"
      "v_exp_f32 v114, v162\n"
      "v_mfma_f32_16x16x32_bf16 a[192:195], a[200:203], v[196:199], a[192:195]\n"
      "buffer_load_dwordx4 v248, s[20:23], s60 offen offset:128 nt lds\n"
      "v_xor_b32 v244, 0x10000, v244\n"
      "v_mfma_f32_16x16x32_bf16 a[148:151], a[108:111], v[212:215], a[148:151]\n"
      "s_add_u32 m0, s62, 8192\n"
      "v_exp_f32 v115, v163\n"
      "v_mfma_f32_16x16x32_bf16 a[132:135], a[104:107], v[212:215], a[132:135]\n"
      "s_memrealtime s[68:69]\n"
      "buffer_load_dwordx4 v120, s[20:23], s60 offen nt lds\n"
      "v_cvt_pk_bf16_f32 v209, v114, v115\n"
      "v_xor_b32 v245, 0x10000, v245\n"
      "v_mfma_f32_16x16x32_bf16 a[192:195], a[204:207], v[200:203], a[192:195]\n"
      "s_add_u32 m0, s62, 9088\n"
      "v_exp_f32 v112, v164\n"
      "v_mfma_f32_16x16x32_bf16 v[52:55], a[100:103], v[212:215], v[52:55]\n"
      "s_add_u32 s62, s62, 0x8000\n"
      "buffer_load_dwordx4 v120, s[20:23], s60 offen offset:128 nt lds\n"
      "v_or_b32 v116, v208, v209\n"
      "v_xor_b32 v246, 0x10000, v246\n"
      "v_mfma_f32_16x16x32_bf16 v[36:39], a[96:99], v[212:215], v[36:39]\n"
      "s_and_b32 s62, s62, 0x1ffff\n"
      "v_exp_f32 v113, v165\n"
      "v_mfma_f32_16x16x32_bf16 a[192:195], a[208:211], v[204:207], a[192:195]\n"
      "s_add_u32 s60, s60, s78\n"
      "v_cvt_pk_bf16_f32 v210, v112, v113\n"
      "v_xor_b32 v247, 0x10000, v247\n"
      "s_waitcnt vmcnt(8)\n"
      "s_sub_u32 s63, 11, %[n1]\n"
      "LOOP2_%=:\n"
      "s_waitcnt vmcnt(9) lgkmcnt(0)\n"
      "s_sub_u32 s67, s68, s90\n"
      "s_cmp_lt_i32 s67, 0\n"
      "s_cbranch_scc1 GW4_%=\n"
      "GX4_%=:\n"
      "s_barrier\n"
      "v_mfma_f32_16x16x32_bf16 v[40:43], a[96:99], v[216:219], v[40:43]\n"
      "s_memrealtime s[68:69]\n"
      "ds_read_b128 a[64:67], v242 offset:0\n"
      "v_exp_f32 v114, v166\n"
      "v_mfma_f32_16x16x32_bf16 v[44:47], a[96:99], v[220:223], v[44:47]\n"
      "s_add_u32 m0, s77, 16384\n"
      "ds_read_b128 a[68:71], v242 offset:4096\n"
      "ds_read_b128 a[72:75], v243 offset:0\n"
      "v_mfma_f32_16x16x32_bf16 v[60:63], a[100:103], v[220:223], v[60:63]\n"
      "buffer_load_dwordx4 v249, s[24:27], s61 offen nt lds\n"
      "v_exp_f32 v115, v167\n"
      "v_mfma_f32_16x16x32_bf16 v[56:59], a[100:103], v[216:219], v[56:59]\n"
      "s_add_u32 m0, s77, 17408\n"
      "ds_read_b128 a[76:79], v243 offset:4096\n"
      "v_cvt_pk_bf16_f32 v211, v114, v115\n"
      "v_mfma_f32_16x16x32_bf16 a[136:139], a[104:107], v[216:219], a[136:139]\n"
      "buffer_load_dwordx4 v121, s[24:27], s61 offen nt lds\n"
      "v_exp_f32 v112, v168\n"
      "v_mfma_f32_16x16x32_bf16 a[140:143], a[104:107], v[220:223], a[140:143]\n"
      "s_add_u32 m0, s77, 24576\n"
      "ds_read_b128 a[80:83], v242 offset:1024\n"
      "v_or3_b32 v116, v116, v210, v211\n"
      "v_mfma_f32_16x16x32_bf16 a[156:159], a[108:111], v[220:223], a[156:159]\n"
      "buffer_load_dwordx4 v122, s[24:27], s61 offen nt lds\n"
      "v_exp_f32 v113, v169\n"
      "v_mfma_f32_16x16x32_bf16 a[152:155], a[108:111], v[216:219], a[152:155]\n"
      "s_add_u32 m0, s77, 25600\n"
      "ds_read_b128 a[84:87], v242 offset:5120\n"
      "v_cvt_pk_bf16_f32 v212, v112, v113\n"
      "v_mfma_f32_16x16x32_bf16 v[72:75], a[112:115], v[216:219], v[72:75]\n"
      "s_add_u32 s77, s77, 0x8000\n"
      "buffer_load_dwordx4 v123, s[24:27], s61 offen nt lds\n"
      "v_exp_f32 v114, v170\n"
      "v_mfma_f32_16x16x32_bf16 v[76:79], a[112:115], v[220:223], v[76:79]\n"
      "s_and_b32 s77, s77, 0x1ffff\n"
      "ds_read_b128 a[88:91], v243 offset:1024\n"
      "ds_read_b128 a[92:95], v243 offset:5120\n"
      "v_mfma_f32_16x16x32_bf16 v[92:95], a[116:119], v[220:223], v[92:95]\n"
      "s_add_u32 s61, s61, s78\n"
      "v_exp_f32 v115, v171\n"
      "v_mfma_f32_16x16x32_bf16 v[88:91], a[116:119], v[216:219], v[88:91]\n"
      "v_cvt_pk_bf16_f32 v213, v114, v115\n"
      "v_mfma_f32_16x16x32_bf16 a[168:171], a[120:123], v[216:219], a[168:171]\n"
      "v_exp_f32 v112, v172\n"
      "v_mfma_f32_16x16x32_bf16 a[172:175], a[120:123], v[220:223], a[172:175]\n"
      "v_or3_b32 v116, v116, v212, v213\n"
      "v_mfma_f32_16x16x32_bf16 a[188:191], a[124:127], v[220:223], a[188:191]\n"
      "v_exp_f32 v113, v173\n"
      "v_mfma_f32_16x16x32_bf16 a[184:187], a[124:127], v[216:219], a[184:187]\n"
      "s_waitcnt lgkmcnt(0)\n"
      "v_cvt_pk_bf16_f32 v214, v112, v113\n"
      "v_mfma_f32_16x16x32_bf16 v[128:131], a[64:67], a[0:3], v[224:227]\n"
      "s_sub_u32 s67, s68, s66\n"
      "v_exp_f32 v114, v174\n"
      "v_mfma_f32_16x16x32_bf16 v[128:131], a[72:75], a[16:19], v[128:131]\n"
      "s_cmp_gt_i32 s67, s70\n"
      "v_mfma_f32_16x16x32_bf16 v[128:131], a[80:83], a[32:35], v[128:131]\n"
      "s_cselect_b32 s69, s86, 0\n"
      "v_exp_f32 v115, v175\n"
      "v_mfma_f32_16x16x32_bf16 v[128:131], a[88:91], a[48:51], v[128:131]\n"
      "s_add_u32 s66, s66, s69\n"
      "v_cvt_pk_bf16_f32 v215, v114, v115\n"
      "v_mfma_f32_16x16x32_bf16 v[132:135], a[92:95], a[48:51], v[224:227]\n"
      "s_add_u32 s67, s67, s101\n"
      "v_exp_f32 v112, v176\n"
      "v_mfma_f32_16x16x32_bf16 v[132:135], a[84:87], a[32:35], v[132:135]\n"
      "s_min_i32 s70, s70, s67\n"
      "v_or3_b32 v116, v116, v214, v215\n"
      "v_mfma_f32_16x16x32_bf16 v[132:135], a[76:79], a[16:19], v[132:135]\n"
      "s_max_i32 s70, s70, 85\n"
      "v_exp_f32 v113, v177\n"
      "v_mfma_f32_16x16x32_bf16 v[132:135], a[68:71], a[0:3], v[132:135]\n"
      "s_add_u32 s66, s66, s86\n"
      "v_cvt_pk_bf16_f32 v216, v112, v113\n"
      "v_mfma_f32_16x16x32_bf16 v[140:143], a[68:71], a[4:7], v[228:231]\n"
      "s_and_b32 s76, s80, 31\n"
      "v_exp_f32 v114, v178\n"
      "v_mfma_f32_16x16x32_bf16 v[140:143], a[76:79], a[20:23], v[140:143]\n"
      "s_lshr_b64 s[80:81], s[80:81], 5\n"
      "v_mfma_f32_16x16x32_bf16 v[140:143], a[84:87], a[36:39], v[140:143]\n"
      "s_add_u32 s66, s66, s76\n"
      "v_exp_f32 v115, v179\n"
      "v_mfma_f32_16x16x32_bf16 v[140:143], a[92:95], a[52:55], v[140:143]\n"
      "s_sub_u32 s67, s68, s82\n"
      "v_cvt_pk_bf16_f32 v217, v114, v115\n"
      "v_mfma_f32_16x16x32_bf16 v[136:139], a[88:91], a[52:55], v[228:231]\n"
      "s_min_i32 s76, s67, s83\n"
      "v_exp_f32 v112, v180\n"
      "s_waitcnt vmcnt(8)\n"
      "s_barrier\n"
      "v_mfma_f32_16x16x32_bf16 v[136:139], a[80:83], a[36:39], v[136:139]\n"
      "s_mov_b32 s83, s67\n"
      "ds_read_b64_tr_b16 a[96:97], v244 offset:0\n"
      "v_or3_b32 v116, v116, v216, v217\n"
      "v_mfma_f32_16x16x32_bf16 v[136:139], a[72:75], a[20:23], v[136:139]\n"
      "s_sub_u32 s76, s76, 36\n"
      "v_exp_f32 v113, v181\n"
      "v_mfma_f32_16x16x32_bf16 v[136:139], a[64:67], a[4:7], v[136:139]\n"
      "s_min_i32 s76, s76, 148\n"
      "ds_read_b64_tr_b16 a[98:99], v244 offset:4096\n"
      "v_cvt_pk_bf16_f32 v218, v112, v113\n"
      "v_mfma_f32_16x16x32_bf16 v[144:147], a[64:67], a[8:11], v[232:235]\n"
      "s_max_i32 s76, s76, 70\n"
      "v_exp_f32 v114, v182\n"
      "v_mfma_f32_16x16x32_bf16 v[144:147], a[72:75], a[24:27], v[144:147]\n"
      "s_mov_b32 s82, s68\n"
      "ds_read_b64_tr_b16 a[100:101], v245 offset:0\n"
      "ds_read_b64_tr_b16 a[102:103], v245 offset:4096\n"
      "v_mfma_f32_16x16x32_bf16 v[144:147], a[80:83], a[40:43], v[144:147]\n"
      "s_add_u32 s65, s82, s76\n"
      "v_exp_f32 v115, v183\n"
      "v_mfma_f32_16x16x32_bf16 v[144:147], a[88:91], a[56:59], v[144:147]\n"
      "s_sub_u32 s93, s93, 1\n"
      "ds_read_b64_tr_b16 a[104:105], v246 offset:0\n"
      "v_cvt_pk_bf16_f32 v219, v114, v115\n"
      "v_mfma_f32_16x16x32_bf16 v[148:151], a[92:95], a[56:59], v[232:235]\n"
      "s_cmp_le_i32 s93, 14\n"
      "v_exp_f32 v112, v184\n"
      "v_mfma_f32_16x16x32_bf16 v[148:151], a[84:87], a[40:43], v[148:151]\n"
      "s_cselect_b32 s86, 107, s86\n"
      "ds_read_b64_tr_b16 a[106:107], v246 offset:4096\n"
      "v_or3_b32 v116, v116, v218, v219\n"
      "v_mfma_f32_16x16x32_bf16 v[148:151], a[76:79], a[24:27], v[148:151]\n"
      "s_sub_u32 s67, s65, s66\n"
      "v_exp_f32 v113, v185\n"
      "v_mfma_f32_16x16x32_bf16 v[148:151], a[68:71], a[8:11], v[148:151]\n"
      "s_cmp_lt_i32 s67, 0\n"
      "ds_read_b64_tr_b16 a[108:109], v247 offset:0\n"
      "v_cvt_pk_bf16_f32 v220, v112, v113\n"
      "v_mfma_f32_16x16x32_bf16 v[156:159], a[68:71], a[12:15], v[236:239]\n"
      "s_cselect_b32 s88, s66, s65\n"
      "v_exp_f32 v114, v186\n"
      "v_mfma_f32_16x16x32_bf16 v[156:159], a[76:79], a[28:31], v[156:159]\n"
      "s_add_u32 s90, s88, s91\n"
      "ds_read_b64_tr_b16 a[110:111], v247 offset:4096\n"
      "ds_read_b64_tr_b16 a[112:113], v244 offset:1024\n"
      "v_mfma_f32_16x16x32_bf16 v[156:159], a[84:87], a[44:47], v[156:159]\n"
      "v_exp_f32 v115, v187\n"
      "v_mfma_f32_16x16x32_bf16 v[156:159], a[92:95], a[60:63], v[156:159]\n"
      "ds_read_b64_tr_b16 a[114:115], v244 offset:5120\n"
      "v_cvt_pk_bf16_f32 v221, v114, v115\n"
      "v_mfma_f32_16x16x32_bf16 v[152:155], a[88:91], a[60:63], v[236:239]\n"
      "v_exp_f32 v112, v188\n"
      "v_mfma_f32_16x16x32_bf16 v[152:155], a[80:83], a[44:47], v[152:155]\n"
      "ds_read_b64_tr_b16 a[116:117], v245 offset:1024\n"
      "v_or3_b32 v116, v116, v220, v221\n"
      "v_mfma_f32_16x16x32_bf16 v[152:155], a[72:75], a[28:31], v[152:155]\n"
      "v_exp_f32 v113, v189\n"
      "v_mfma_f32_16x16x32_bf16 v[152:155], a[64:67], a[12:15], v[152:155]\n"
      "ds_read_b64_tr_b16 a[118:119], v245 offset:5120\n"
      "v_cvt_pk_bf16_f32 v222, v112, v113\n"
      "s_waitcnt lgkmcnt(6)\n"
      "v_mfma_f32_16x16x32_bf16 v[32:35], a[96:99], v[192:195], v[32:35]\n"
      "v_exp_f32 v114, v190\n"
      "v_mfma_f32_16x16x32_bf16 v[48:51], a[100:103], v[192:195], v[48:51]\n"
      "ds_read_b64_tr_b16 a[120:121], v246 offset:1024\n"
      "ds_read_b64_tr_b16 a[122:123], v246 offset:5120\n"
      "v_mfma_f32_16x16x32_bf16 a[128:131], a[104:107], v[192:195], a[128:131]\n"
      "s_waitcnt lgkmcnt(4)\n"
      "v_exp_f32 v115, v191\n"
      "v_mfma_f32_16x16x32_bf16 a[144:147], a[108:111], v[192:195], a[144:147]\n"
      "ds_read_b64_tr_b16 a[124:125], v247 offset:1024\n"
      "v_cvt_pk_bf16_f32 v223, v114, v115\n"
      "v_mfma_f32_16x16x32_bf16 v[64:67], a[112:115], v[192:195], v[64:67]\n"
      "ds_read_b64_tr_b16 a[126:127], v247 offset:5120\n"
      "s_waitcnt lgkmcnt(2)\n"
      "v_mfma_f32_16x16x32_bf16 v[80:83], a[116:119], v[192:195], v[80:83]\n"
      "v_or3_b32 v116, v116, v222, v223\n"
      "v_mfma_f32_16x16x32_bf16 a[160:163], a[120:123], v[192:195], a[160:163]\n"
      "s_mov_b32 s64, 3\n"
      "s_waitcnt lgkmcnt(0)\n"
      "v_and_b32 v116, 0x40004000, v116\n"
      "v_mfma_f32_16x16x32_bf16 a[176:179], a[124:127], v[192:195], a[176:179]\n"
      "v_cmp_ne_u32 vcc, 0, v116\n"
      "s_cbranch_vccnz FB1_%=\n"
      "RET1_3_%=:\n"
      "v_mfma_f32_16x16x32_bf16 a[180:183], a[124:127], v[196:199], a[180:183]\n"
      "v_exp_f32 v112, v128\n"
      "v_mfma_f32_16x16x32_bf16 a[164:167], a[120:123], v[196:199], a[164:167]\n"
      "v_mfma_f32_16x16x32_bf16 a[192:195], a[196:199], v[208:211], a[192:195]\n"
      "s_add_u32 m0, s62, 0\n"
      "v_exp_f32 v113, v129\n"
      "v_mfma_f32_16x16x32_bf16 v[84:87], a[116:119], v[196:199], v[84:87]\n"
      "buffer_load_dwordx4 v248, s[20:23], s60 offen nt lds\n"
      "v_cvt_pk_bf16_f32 v192, v112, v113\n"
      "v_mfma_f32_16x16x32_bf16 v[68:71], a[112:115], v[196:199], v[68:71]\n"
      "s_add_u32 m0, s62, 896\n"
      "v_exp_f32 v114, v130\n"
      "v_mfma_f32_16x16x32_bf16 a[192:195], a[200:203], v[212:215], a[192:195]\n"
      "buffer_load_dwordx4 v248, s[20:23], s60 offen offset:128 nt lds\n"
      "v_mfma_f32_16x16x32_bf16 a[148:151], a[108:111], v[196:199], a[148:151]\n"
      "s_add_u32 m0, s62, 8192\n"
      "v_exp_f32 v115, v131\n"
      "v_mfma_f32_16x16x32_bf16 a[132:135], a[104:107], v[196:199], a[132:135]\n"
      "s_memrealtime s[68:69]\n"
      "buffer_load_dwordx4 v120, s[20:23], s60 offen nt lds\n"
      "v_cvt_pk_bf16_f32 v193, v114, v115\n"
      "v_mfma_f32_16x16x32_bf16 a[192:195], a[204:207], v[216:219], a[192:195]\n"
      "s_add_u32 m0, s62, 9088\n"
      "v_exp_f32 v112, v132\n"
      "v_mfma_f32_16x16x32_bf16 v[52:55], a[100:103], v[196:199], v[52:55]\n"
      "s_add_u32 s62, s62, 0x8000\n"
      "buffer_load_dwordx4 v120, s[20:23], s60 offen offset:128 nt lds\n"
      "v_or_b32 v116, v192, v193\n"
      "v_mfma_f32_16x16x32_bf16 v[36:39], a[96:99], v[196:199], v[36:39]\n"
      "s_and_b32 s62, s62, 0x1ffff\n"
      "v_exp_f32 v113, v133\n"
      "v_mfma_f32_16x16x32_bf16 a[192:195], a[208:211], v[220:223], a[192:195]\n"
      "s_add_u32 s60, s60, %[inc]\n"
      "v_cvt_pk_bf16_f32 v194, v112, v113\n"
      "s_waitcnt vmcnt(8) lgkmcnt(0)\n"
      "s_sub_u32 s67, s68, s90\n"
      "s_cmp_lt_i32 s67, 0\n"
      "s_cbranch_scc1 GW5_%=\n"
      "GX5_%=:\n"
      "s_barrier\n"
      "v_mfma_f32_16x16x32_bf16 v[40:43], a[96:99], v[200:203], v[40:43]\n"
      "s_memrealtime s[68:69]\n"
      "buffer_load_dword v250, v251, s[28:31], 0 offen sc1\n"
      "ds_read_b128 a[64:67], v242 offset:32768\n"
      "v_exp_f32 v114, v134\n"
      "v_mfma_f32_16x16x32_bf16 v[44:47], a[96:99], v[204:207], v[44:47]\n"
      "s_add_u32 m0, s77, 16384\n"
      "ds_read_b128 a[68:71], v242 offset:36864\n"
      "ds_read_b128 a[72:75], v243 offset:32768\n"
      "v_mfma_f32_16x16x32_bf16 v[60:63], a[100:103], v[204:207], v[60:63]\n"
      "buffer_load_dwordx4 v249, s[24:27], s61 offen nt lds\n"
      "v_exp_f32 v115, v135\n"
      "v_mfma_f32_16x16x32_bf16 v[56:59], a[100:103], v[200:203], v[56:59]\n"
      "s_add_u32 m0, s77, 17408\n"
      "ds_read_b128 a[76:79], v243 offset:36864\n"
      "v_cvt_pk_bf16_f32 v195, v114, v115\n"
      "v_mfma_f32_16x16x32_bf16 a[136:139], a[104:107], v[200:203], a[136:139]\n"
      "buffer_load_dwordx4 v121, s[24:27], s61 offen nt lds\n"
      "v_exp_f32 v112, v136\n"
      "v_mfma_f32_16x16x32_bf16 a[140:143], a[104:107], v[204:207], a[140:143]\n"
      "s_add_u32 m0, s77, 24576\n"
      "ds_read_b128 a[80:83], v242 offset:33792\n"
      "v_or3_b32 v116, v116, v194, v195\n"
      "v_mfma_f32_16x16x32_bf16 a[156:159], a[108:111], v[204:207], a[156:159]\n"
      "buffer_load_dwordx4 v122, s[24:27], s61 offen nt lds\n"
      "v_exp_f32 v113, v137\n"
      "v_mfma_f32_16x16x32_bf16 a[152:155], a[108:111], v[200:203], a[152:155]\n"
      "s_add_u32 m0, s77, 25600\n"
      "ds_read_b128 a[84:87], v242 offset:37888\n"
      "v_cvt_pk_bf16_f32 v196, v112, v113\n"
      "v_mfma_f32_16x16x32_bf16 v[72:75], a[112:115], v[200:203], v[72:75]\n"
      "s_add_u32 s77, s77, 0x8000\n"
      "buffer_load_dwordx4 v123, s[24:27], s61 offen nt lds\n"
      "v_exp_f32 v114, v138\n"
      "v_mfma_f32_16x16x32_bf16 v[76:79], a[112:115], v[204:207], v[76:79]\n"
      "s_and_b32 s77, s77, 0x1ffff\n"
      "ds_read_b128 a[88:91], v243 offset:33792\n"
      "ds_read_b128 a[92:95], v243 offset:37888\n"
      "v_mfma_f32_16x16x32_bf16 v[92:95], a[116:119], v[204:207], v[92:95]\n"
      "s_add_u32 s61, s61, %[inc]\n"
      "v_exp_f32 v115, v139\n"
      "v_mfma_f32_16x16x32_bf16 v[88:91], a[116:119], v[200:203], v[88:91]\n"
      "v_cvt_pk_bf16_f32 v197, v114, v115\n"
      "v_mfma_f32_16x16x32_bf16 a[168:171], a[120:123], v[200:203], a[168:171]\n"
      "v_exp_f32 v112, v140\n"
      "v_mfma_f32_16x16x32_bf16 a[172:175], a[120:123], v[204:207], a[172:175]\n"
      "v_or3_b32 v116, v116, v196, v197\n"
      "v_mfma_f32_16x16x32_bf16 a[188:191], a[124:127], v[204:207], a[188:191]\n"
      "v_exp_f32 v113, v141\n"
      "v_mfma_f32_16x16x32_bf16 a[184:187], a[124:127], v[200:203], a[184:187]\n"
      "s_waitcnt lgkmcnt(0)\n"
      "v_cvt_pk_bf16_f32 v198, v112, v113\n"
      "v_mfma_f32_16x16x32_bf16 v[160:163], a[64:67], a[0:3], v[224:227]\n"
      "s_sub_u32 s67, s68, s66\n"
      "v_exp_f32 v114, v142\n"
      "v_mfma_f32_16x16x32_bf16 v[160:163], a[72:75], a[16:19], v[160:163]\n"
      "s_cmp_gt_i32 s67, s70\n"
      "v_mfma_f32_16x16x32_bf16 v[160:163], a[80:83], a[32:35], v[160:163]\n"
      "s_cselect_b32 s69, s86, 0\n"
      "v_exp_f32 v115, v143\n"
      "v_mfma_f32_16x16x32_bf16 v[160:163], a[88:91], a[48:51], v[160:163]\n"
      "s_add_u32 s66, s66, s69\n"
      "v_cvt_pk_bf16_f32 v199, v114, v115\n"
      "v_mfma_f32_16x16x32_bf16 v[164:167], a[92:95], a[48:51], v[224:227]\n"
      "s_add_u32 s67, s67, s101\n"
      "v_exp_f32 v112, v144\n"
      "v_mfma_f32_16x16x32_bf16 v[164:167], a[84:87], a[32:35], v[164:167]\n"
      "s_min_i32 s70, s70, s67\n"
      "v_or3_b32 v116, v116, v198, v199\n"
      "v_mfma_f32_16x16x32_bf16 v[164:167], a[76:79], a[16:19], v[164:167]\n"
      "s_max_i32 s70, s70, 85\n"
      "v_exp_f32 v113, v145\n"
      "v_mfma_f32_16x16x32_bf16 v[164:167], a[68:71], a[0:3], v[164:167]\n"
      "s_add_u32 s66, s66, s86\n"
      "v_cvt_pk_bf16_f32 v200, v112, v113\n"
      "v_mfma_f32_16x16x32_bf16 v[172:175], a[68:71], a[4:7], v[228:231]\n"
      "s_and_b32 s76, s80, 31\n"
      "v_exp_f32 v114, v146\n"
      "v_mfma_f32_16x16x32_bf16 v[172:175], a[76:79], a[20:23], v[172:175]\n"
      "s_lshr_b64 s[80:81], s[80:81], 5\n"
      "v_mfma_f32_16x16x32_bf16 v[172:175], a[84:87], a[36:39], v[172:175]\n"
      "s_add_u32 s66, s66, s76\n"
      "v_exp_f32 v115, v147\n"
      "v_mfma_f32_16x16x32_bf16 v[172:175], a[92:95], a[52:55], v[172:175]\n"
      "s_sub_u32 s67, s68, s82\n"
      "v_cvt_pk_bf16_f32 v201, v114, v115\n"
      "v_mfma_f32_16x16x32_bf16 v[168:171], a[88:91], a[52:55], v[228:231]\n"
      "s_min_i32 s76, s67, s83\n"
      "v_exp_f32 v112, v148\n"
      "s_waitcnt vmcnt(9)\n"
      "s_barrier\n"
      "v_mfma_f32_16x16x32_bf16 v[168:171], a[80:83], a[36:39], v[168:171]\n"
      "s_mov_b32 s83, s67\n"
      "ds_read_b64_tr_b16 a[96:97], v244 offset:32768\n"
      "v_or3_b32 v116, v116, v200, v201\n"
      "v_mfma_f32_16x16x32_bf16 v[168:171], a[72:75], a[20:23], v[168:171]\n"
      "s_sub_u32 s76, s76, 36\n"
      "v_exp_f32 v113, v149\n"
      "v_mfma_f32_16x16x32_bf16 v[168:171], a[64:67], a[4:7], v[168:171]\n"
      "s_min_i32 s76, s76, 148\n"
      "ds_read_b64_tr_b16 a[98:99], v244 offset:36864\n"
      "v_cvt_pk_bf16_f32 v202, v112, v113\n"
      "v_mfma_f32_16x16x32_bf16 v[176:179], a[64:67], a[8:11], v[232:235]\n"
      "s_max_i32 s76, s76, 70\n"
      "v_exp_f32 v114, v150\n"
      "v_mfma_f32_16x16x32_bf16 v[176:179], a[72:75], a[24:27], v[176:179]\n"
      "s_mov_b32 s82, s68\n"
      "ds_read_b64_tr_b16 a[100:101], v245 offset:32768\n"
      "ds_read_b64_tr_b16 a[102:103], v245 offset:36864\n"
      "v_mfma_f32_16x16x32_bf16 v[176:179], a[80:83], a[40:43], v[176:179]\n"
      "s_add_u32 s65, s82, s76\n"
      "v_exp_f32 v115, v151\n"
      "v_mfma_f32_16x16x32_bf16 v[176:179], a[88:91], a[56:59], v[176:179]\n"
      "s_sub_u32 s93, s93, 1\n"
      "ds_read_b64_tr_b16 a[104:105], v246 offset:32768\n"
      "v_cvt_pk_bf16_f32 v203, v114, v115\n"
      "v_mfma_f32_16x16x32_bf16 v[180:183], a[92:95], a[56:59], v[232:235]\n"
      "s_cmp_le_i32 s93, 14\n"
      "v_exp_f32 v112, v152\n"
      "v_mfma_f32_16x16x32_bf16 v[180:183], a[84:87], a[40:43], v[180:183]\n"
      "s_cselect_b32 s86, 107, s86\n"
      "ds_read_b64_tr_b16 a[106:107], v246 offset:36864\n"
      "v_or3_b32 v116, v116, v202, v203\n"
      "v_mfma_f32_16x16x32_bf16 v[180:183], a[76:79], a[24:27], v[180:183]\n"
      "s_sub_u32 s67, s65, s66\n"
      "v_exp_f32 v113, v153\n"
      "v_mfma_f32_16x16x32_bf16 v[180:183], a[68:71], a[8:11], v[180:183]\n"
      "s_cmp_lt_i32 s67, 0\n"
      "ds_read_b64_tr_b16 a[108:109], v247 offset:32768\n"
      "v_cvt_pk_bf16_f32 v204, v112, v113\n"
      "v_mfma_f32_16x16x32_bf16 v[188:191], a[68:71], a[12:15], v[236:239]\n"
      "s_cselect_b32 s88, s66, s65\n"
      "v_exp_f32 v114, v154\n"
      "v_mfma_f32_16x16x32_bf16 v[188:191], a[76:79], a[28:31], v[188:191]\n"
      "s_add_u32 s90, s88, s91\n"
      "ds_read_b64_tr_b16 a[110:111], v247 offset:36864\n"
      "ds_read_b64_tr_b16 a[112:113], v244 offset:33792\n"
      "v_mfma_f32_16x16x32_bf16 v[188:191], a[84:87], a[44:47], v[188:191]\n"
      "v_exp_f32 v115, v155\n"
      "v_mfma_f32_16x16x32_bf16 v[188:191], a[92:95], a[60:63], v[188:191]\n"
      "ds_read_b64_tr_b16 a[114:115], v244 offset:37888\n"
      "v_cvt_pk_bf16_f32 v205, v114, v115\n"
      "v_mfma_f32_16x16x32_bf16 v[184:187], a[88:91], a[60:63], v[236:239]\n"
      "v_exp_f32 v112, v156\n"
      "v_mfma_f32_16x16x32_bf16 v[184:187], a[80:83], a[44:47], v[184:187]\n"
      "ds_read_b64_tr_b16 a[116:117], v245 offset:33792\n"
      "v_or3_b32 v116, v116, v204, v205\n"
      "v_mfma_f32_16x16x32_bf16 v[184:187], a[72:75], a[28:31], v[184:187]\n"
      "v_exp_f32 v113, v157\n"
      "v_mfma_f32_16x16x32_bf16 v[184:187], a[64:67], a[12:15], v[184:187]\n"
      "ds_read_b64_tr_b16 a[118:119], v245 offset:37888\n"
      "v_cvt_pk_bf16_f32 v206, v112, v113\n"
      "s_waitcnt lgkmcnt(6)\n"
      "v_mfma_f32_16x16x32_bf16 v[32:35], a[96:99], v[208:211], v[32:35]\n"
      "v_exp_f32 v114, v158\n"
      "v_mfma_f32_16x16x32_bf16 v[48:51], a[100:103], v[208:211], v[48:51]\n"
      "ds_read_b64_tr_b16 a[120:121], v246 offset:33792\n"
      "ds_read_b64_tr_b16 a[122:123], v246 offset:37888\n"
      "v_mfma_f32_16x16x32_bf16 a[128:131], a[104:107], v[208:211], a[128:131]\n"
      "s_waitcnt lgkmcnt(4)\n"
      "v_exp_f32 v115, v159\n"
      "v_mfma_f32_16x16x32_bf16 a[144:147], a[108:111], v[208:211], a[144:147]\n"
      "ds_read_b64_tr_b16 a[124:125], v247 offset:33792\n"
      "v_cvt_pk_bf16_f32 v207, v114, v115\n"
      "v_mfma_f32_16x16x32_bf16 v[64:67], a[112:115], v[208:211], v[64:67]\n"
      "ds_read_b64_tr_b16 a[126:127], v247 offset:37888\n"
      "s_waitcnt lgkmcnt(2)\n"
      "v_mfma_f32_16x16x32_bf16 v[80:83], a[116:119], v[208:211], v[80:83]\n"
      "v_or3_b32 v116, v116, v206, v207\n"
      "v_mfma_f32_16x16x32_bf16 a[160:163], a[120:123], v[208:211], a[160:163]\n"
      "s_mov_b32 s64, 3\n"
      "s_waitcnt lgkmcnt(0)\n"
      "v_and_b32 v116, 0x40004000, v116\n"
      "v_mfma_f32_16x16x32_bf16 a[176:179], a[124:127], v[208:211], a[176:179]\n"
      "v_cmp_ne_u32 vcc, 0, v116\n"
      "s_cbranch_vccnz FB0_%=\n"
      "RET0_3_%=:\n"
      "v_mfma_f32_16x16x32_bf16 a[180:183], a[124:127], v[212:215], a[180:183]\n"
      "v_exp_f32 v112, v160\n"
      "v_mfma_f32_16x16x32_bf16 a[164:167], a[120:123], v[212:215], a[164:167]\n"
      "v_xor_b32 v242, 0x10000, v242\n"
      "v_mfma_f32_16x16x32_bf16 a[192:195], a[196:199], v[192:195], a[192:195]\n"
      "s_add_u32 m0, s62, 0\n"
      "v_exp_f32 v113, v161\n"
      "v_mfma_f32_16x16x32_bf16 v[84:87], a[116:119], v[212:215], v[84:87]\n"
      "buffer_load_dwordx4 v248, s[20:23], s60 offen nt lds\n"
      "v_cvt_pk_bf16_f32 v208, v112, v113\n"
      "v_xor_b32 v243, 0x10000, v243\n"
      "v_mfma_f32_16x16x32_bf16 v[68:71], a[112:115], v[212:215], v[68:71]\n"
      "s_add_u32 m0, s62, 896\n"
      "v_exp_f32 v114, v162\n"
      "v_mfma_f32_16x16x32_bf16 a[192:195], a[200:203], v[196:199], a[192:195]\n"
      "buffer_load_dwordx4 v248, s[20:23], s60 offen offset:128 nt lds\n"
      "v_xor_b32 v244, 0x10000, v244\n"
      "v_mfma_f32_16x16x32_bf16 a[148:151], a[108:111], v[212:215], a[148:151]\n"
      "s_add_u32 m0, s62, 8192\n"
      "v_exp_f32 v115, v163\n"
      "v_mfma_f32_16x16x32_bf16 a[132:135], a[104:107], v[212:215], a[132:135]\n"
      "s_memrealtime s[68:69]\n"
      "buffer_load_dwordx4 v120, s[20:23], s60 offen nt lds\n"
      "v_cvt_pk_bf16_f32 v209, v114, v115\n"
      "v_xor_b32 v245, 0x10000, v245\n"
      "v_mfma_f32_16x16x32_bf16 a[192:195], a[204:207], v[200:203], a[192:195]\n"
      "s_add_u32 m0, s62, 9088\n"
      "v_exp_f32 v112, v164\n"
      "v_mfma_f32_16x16x32_bf16 v[52:55], a[100:103], v[212:215], v[52:55]\n"
      "s_add_u32 s62, s62, 0x8000\n"
      "buffer_load_dwordx4 v120, s[20:23], s60 offen offset:128 nt lds\n"
      "v_or_b32 v116, v208, v209\n"
      "v_xor_b32 v246, 0x10000, v246\n"
      "v_mfma_f32_16x16x32_bf16 v[36:39], a[96:99], v[212:215], v[36:39]\n"
      "s_and_b32 s62, s62, 0x1ffff\n"
      "s_add_u32 s60, s60, s78\n"
      "v_exp_f32 v113, v165\n"
      "v_mfma_f32_16x16x32_bf16 a[192:195], a[208:211], v[204:207], a[192:195]\n"
      "s_sub_u32 s63, s63, 1\n"
      "v_cvt_pk_bf16_f32 v210, v112, v113\n"
      "v_xor_b32 v247, 0x10000, v247\n"
      "s_cbranch_scc0 LOOP2_%=\n"
      "s_waitcnt vmcnt(9) lgkmcnt(0)\n"
      "s_sub_u32 s67, s68, s90\n"
      "s_cmp_lt_i32 s67, 0\n"
      "s_cbranch_scc1 GW6_%=\n"
      "GX6_%=:\n"
      "s_barrier\n"
      "v_mfma_f32_16x16x32_bf16 v[40:43], a[96:99], v[216:219], v[40:43]\n"
      "s_memrealtime s[68:69]\n"
      "ds_read_b128 a[64:67], v242 offset:0\n"
      "v_exp_f32 v114, v166\n"
      "v_mfma_f32_16x16x32_bf16 v[44:47], a[96:99], v[220:223], v[44:47]\n"
      "s_add_u32 m0, %[wb], 50176\n"
      "ds_read_b128 a[68:71], v242 offset:4096\n"
      "ds_read_b128 a[72:75], v243 offset:0\n"
      "v_mfma_f32_16x16x32_bf16 v[60:63], a[100:103], v[220:223], v[60:63]\n"
      "s_add_u32 s94, s61, s78\n"
      "buffer_load_dwordx4 v121, s[24:27], s61 offen nt lds\n"
      "v_exp_f32 v115, v167\n"
      "v_mfma_f32_16x16x32_bf16 v[56:59], a[100:103], v[216:219], v[56:59]\n"
      "s_add_u32 m0, %[wb], 58368\n"
      "ds_read_b128 a[76:79], v243 offset:4096\n"
      "v_cvt_pk_bf16_f32 v211, v114, v115\n"
      "v_mfma_f32_16x16x32_bf16 a[136:139], a[104:107], v[216:219], a[136:139]\n"
      "s_add_u32 s95, s61, 0x10000\n"
      "buffer_load_dwordx4 v123, s[24:27], s61 offen nt lds\n"
      "v_exp_f32 v112, v168\n"
      "v_mfma_f32_16x16x32_bf16 a[140:143], a[104:107], v[220:223], a[140:143]\n"
      "s_add_u32 s77, s94, 0x10000\n"
      "ds_read_b128 a[80:83], v242 offset:1024\n"
      "v_or3_b32 v116, v116, v210, v211\n"
      "v_mfma_f32_16x16x32_bf16 a[156:159], a[108:111], v[220:223], a[156:159]\n"
      "s_add_u32 s78, s61, 0x20000\n"
      "v_exp_f32 v113, v169\n"
      "v_mfma_f32_16x16x32_bf16 a[152:155], a[108:111], v[216:219], a[152:155]\n"
      "ds_read_b128 a[84:87], v242 offset:5120\n"
      "v_cvt_pk_bf16_f32 v212, v112, v113\n"
      "v_mfma_f32_16x16x32_bf16 v[72:75], a[112:115], v[216:219], v[72:75]\n"
      "v_exp_f32 v114, v170\n"
      "v_mfma_f32_16x16x32_bf16 v[76:79], a[112:115], v[220:223], v[76:79]\n"
      "ds_read_b128 a[88:91], v243 offset:1024\n"
      "ds_read_b128 a[92:95], v243 offset:5120\n"
      "v_mfma_f32_16x16x32_bf16 v[92:95], a[116:119], v[220:223], v[92:95]\n"
      "v_exp_f32 v115, v171\n"
      "v_mfma_f32_16x16x32_bf16 v[88:91], a[116:119], v[216:219], v[88:91]\n"
      "v_cvt_pk_bf16_f32 v213, v114, v115\n"
      "v_mfma_f32_16x16x32_bf16 a[168:171], a[120:123], v[216:219], a[168:171]\n"
      "v_exp_f32 v112, v172\n"
      "v_mfma_f32_16x16x32_bf16 a[172:175], a[120:123], v[220:223], a[172:175]\n"
      "v_or3_b32 v116, v116, v212, v213\n"
      "v_mfma_f32_16x16x32_bf16 a[188:191], a[124:127], v[220:223], a[188:191]\n"
      "v_exp_f32 v113, v173\n"
      "v_mfma_f32_16x16x32_bf16 a[184:187], a[124:127], v[216:219], a[184:187]\n"
      "s_waitcnt lgkmcnt(0)\n"
      "v_cvt_pk_bf16_f32 v214, v112, v113\n"
      "v_mfma_f32_16x16x32_bf16 v[128:131], a[64:67], a[0:3], v[224:227]\n"
      "s_sub_u32 s67, s68, s66\n"
      "v_exp_f32 v114, v174\n"
      "v_mfma_f32_16x16x32_bf16 v[128:131], a[72:75], a[16:19], v[128:131]\n"
      "s_cmp_gt_i32 s67, s70\n"
      "v_mfma_f32_16x16x32_bf16 v[128:131], a[80:83], a[32:35], v[128:131]\n"
      "s_cselect_b32 s69, s86, 0\n"
      "v_exp_f32 v115, v175\n"
      "v_mfma_f32_16x16x32_bf16 v[128:131], a[88:91], a[48:51], v[128:131]\n"
      "s_add_u32 s66, s66, s69\n"
      "v_cvt_pk_bf16_f32 v215, v114, v115\n"
      "v_mfma_f32_16x16x32_bf16 v[132:135], a[92:95], a[48:51], v[224:227]\n"
      "s_add_u32 s67, s67, s101\n"
      "v_exp_f32 v112, v176\n"
      "v_mfma_f32_16x16x32_bf16 v[132:135], a[84:87], a[32:35], v[132:135]\n"
      "s_min_i32 s70, s70, s67\n"
      "v_or3_b32 v116, v116, v214, v215\n"
      "v_mfma_f32_16x16x32_bf16 v[132:135], a[76:79], a[16:19], v[132:135]\n"
      "s_max_i32 s70, s70, 85\n"
      "v_exp_f32 v113, v177\n"
      "v_mfma_f32_16x16x32_bf16 v[132:135], a[68:71], a[0:3], v[132:135]\n"
      "s_add_u32 s66, s66, s86\n"
      "v_cvt_pk_bf16_f32 v216, v112, v113\n"
      "v_mfma_f32_16x16x32_bf16 v[140:143], a[68:71], a[4:7], v[228:231]\n"
      "s_and_b32 s76, s80, 31\n"
      "v_exp_f32 v114, v178\n"
      "v_mfma_f32_16x16x32_bf16 v[140:143], a[76:79], a[20:23], v[140:143]\n"
      "s_lshr_b64 s[80:81], s[80:81], 5\n"
      "v_mfma_f32_16x16x32_bf16 v[140:143], a[84:87], a[36:39], v[140:143]\n"
      "s_add_u32 s66, s66, s76\n"
      "v_exp_f32 v115, v179\n"
      "v_mfma_f32_16x16x32_bf16 v[140:143], a[92:95], a[52:55], v[140:143]\n"
      "s_sub_u32 s67, s68, s82\n"
      "v_cvt_pk_bf16_f32 v217, v114, v115\n"
      "v_mfma_f32_16x16x32_bf16 v[136:139], a[88:91], a[52:55], v[228:231]\n"
      "s_min_i32 s76, s67, s83\n"
      "v_exp_f32 v112, v180\n"
      "s_waitcnt vmcnt(6)\n"
      "s_barrier\n"
      "v_mfma_f32_16x16x32_bf16 v[136:139], a[80:83], a[36:39], v[136:139]\n"
      "s_mov_b32 s83, s67\n"
      "ds_read_b64_tr_b16 a[96:97], v244 offset:0\n"
      "v_or3_b32 v116, v116, v216, v217\n"
      "v_mfma_f32_16x16x32_bf16 v[136:139], a[72:75], a[20:23], v[136:139]\n"
      "s_add_u32 m0, s62, 0\n"
      "v_exp_f32 v113, v181\n"
      "v_mfma_f32_16x16x32_bf16 v[136:139], a[64:67], a[4:7], v[136:139]\n"
      "s_sub_u32 s76, s76, 36\n"
      "ds_read_b64_tr_b16 a[98:99], v244 offset:4096\n"
      "buffer_load_dwordx4 v248, s[20:23], s60 offen nt lds\n"
      "v_cvt_pk_bf16_f32 v218, v112, v113\n"
      "v_mfma_f32_16x16x32_bf16 v[144:147], a[64:67], a[8:11], v[232:235]\n"
      "s_add_u32 m0, s62, 896\n"
      "v_exp_f32 v114, v182\n"
      "v_mfma_f32_16x16x32_bf16 v[144:147], a[72:75], a[24:27], v[144:147]\n"
      "s_min_i32 s76, s76, 148\n"
      "ds_read_b64_tr_b16 a[100:101], v245 offset:0\n"
      "ds_read_b64_tr_b16 a[102:103], v245 offset:4096\n"
      "buffer_load_dwordx4 v248, s[20:23], s60 offen offset:128 nt lds\n"
      "v_mfma_f32_16x16x32_bf16 v[144:147], a[80:83], a[40:43], v[144:147]\n"
      "s_add_u32 m0, s62, 8192\n"
      "v_exp_f32 v115, v183\n"
      "v_mfma_f32_16x16x32_bf16 v[144:147], a[88:91], a[56:59], v[144:147]\n"
      "s_max_i32 s76, s76, 70\n"
      "ds_read_b64_tr_b16 a[104:105], v246 offset:0\n"
      "buffer_load_dwordx4 v120, s[20:23], s60 offen nt lds\n"
      "v_cvt_pk_bf16_f32 v219, v114, v115\n"
      "v_mfma_f32_16x16x32_bf16 v[148:151], a[92:95], a[56:59], v[232:235]\n"
      "s_add_u32 m0, s62, 9088\n"
      "v_exp_f32 v112, v184\n"
      "v_mfma_f32_16x16x32_bf16 v[148:151], a[84:87], a[40:43], v[148:151]\n"
      "s_add_u32 s62, s62, 0x8000\n"
      "ds_read_b64_tr_b16 a[106:107], v246 offset:4096\n"
      "buffer_load_dwordx4 v120, s[20:23], s60 offen offset:128 nt lds\n"
      "v_or3_b32 v116, v116, v218, v219\n"
      "v_mfma_f32_16x16x32_bf16 v[148:151], a[76:79], a[24:27], v[148:151]\n"
      "s_and_b32 s62, s62, 0x1ffff\n"
      "v_exp_f32 v113, v185\n"
      "v_mfma_f32_16x16x32_bf16 v[148:151], a[68:71], a[8:11], v[148:151]\n"
      "s_add_u32 s60, s60, %[inc]\n"
      "ds_read_b64_tr_b16 a[108:109], v247 offset:0\n"
      "v_cvt_pk_bf16_f32 v220, v112, v113\n"
      "v_mfma_f32_16x16x32_bf16 v[156:159], a[68:71], a[12:15], v[236:239]\n"
      "s_mov_b32 s82, s68\n"
      "v_exp_f32 v114, v186\n"
      "v_mfma_f32_16x16x32_bf16 v[156:159], a[76:79], a[28:31], v[156:159]\n"
      "s_sub_u32 s66, s66, s99\n"
      "ds_read_b64_tr_b16 a[110:111], v247 offset:4096\n"
      "ds_read_b64_tr_b16 a[112:113], v244 offset:1024\n"
      "v_mfma_f32_16x16x32_bf16 v[156:159], a[84:87], a[44:47], v[156:159]\n"
      "s_sub_u32 s82, s82, s99\n"
      "v_exp_f32 v115, v187\n"
      "v_mfma_f32_16x16x32_bf16 v[156:159], a[92:95], a[60:63], v[156:159]\n"
      "s_add_u32 s65, s82, s76\n"
      "ds_read_b64_tr_b16 a[114:115], v244 offset:5120\n"
      "v_cvt_pk_bf16_f32 v221, v114, v115\n"
      "v_mfma_f32_16x16x32_bf16 v[152:155], a[88:91], a[60:63], v[236:239]\n"
      "s_sub_u32 s93, s93, 1\n"
      "v_exp_f32 v112, v188\n"
      "v_mfma_f32_16x16x32_bf16 v[152:155], a[80:83], a[44:47], v[152:155]\n"
      "s_cmp_le_i32 s93, 14\n"
      "ds_read_b64_tr_b16 a[116:117], v245 offset:1024\n"
      "v_or3_b32 v116, v116, v220, v221\n"
      "v_mfma_f32_16x16x32_bf16 v[152:155], a[72:75], a[28:31], v[152:155]\n"
      "s_cselect_b32 s86, 107, s86\n"
      "v_exp_f32 v113, v189\n"
      "v_mfma_f32_16x16x32_bf16 v[152:155], a[64:67], a[12:15], v[152:155]\n"
      "ds_read_b64_tr_b16 a[118:119], v245 offset:5120\n"
      "v_cvt_pk_bf16_f32 v222, v112, v113\n"
      "s_waitcnt lgkmcnt(6)\n"
      "v_mfma_f32_16x16x32_bf16 v[32:35], a[96:99], v[192:195], v[32:35]\n"
      "s_sub_u32 s67, s65, s66\n"
      "v_exp_f32 v114, v190\n"
      "v_mfma_f32_16x16x32_bf16 v[48:51], a[100:103], v[192:195], v[48:51]\n"
      "s_cmp_lt_i32 s67, 0\n"
      "s_cselect_b32 s88, s66, s65\n"
      "ds_read_b64_tr_b16 a[120:121], v246 offset:1024\n"
      "ds_read_b64_tr_b16 a[122:123], v246 offset:5120\n"
      "v_mfma_f32_16x16x32_bf16 a[128:131], a[104:107], v[192:195], a[128:131]\n"
      "s_waitcnt lgkmcnt(4)\n"
      "v_exp_f32 v115, v191\n"
      "v_mfma_f32_16x16x32_bf16 a[144:147], a[108:111], v[192:195], a[144:147]\n"
      "s_add_u32 s90, s88, s91\n"
      "ds_read_b64_tr_b16 a[124:125], v247 offset:1024\n"
      "v_cvt_pk_bf16_f32 v223, v114, v115\n"
      "v_mfma_f32_16x16x32_bf16 v[64:67], a[112:115], v[192:195], v[64:67]\n"
      "ds_read_b64_tr_b16 a[126:127], v247 offset:5120\n"
      "s_waitcnt lgkmcnt(2)\n"
      "v_mfma_f32_16x16x32_bf16 v[80:83], a[116:119], v[192:195], v[80:83]\n"
      "v_or3_b32 v116, v116, v222, v223\n"
      "v_mfma_f32_16x16x32_bf16 a[160:163], a[120:123], v[192:195], a[160:163]\n"
      "s_mov_b32 s64, 4\n"
      "s_waitcnt lgkmcnt(0)\n"
      "v_and_b32 v116, 0x40004000, v116\n"
      "v_mfma_f32_16x16x32_bf16 a[176:179], a[124:127], v[192:195], a[176:179]\n"
      "v_cmp_ne_u32 vcc, 0, v116\n"
      "s_cbranch_vccnz FB1_%=\n"
      "RET1_4_%=:\n"
      "v_mfma_f32_16x16x32_bf16 a[180:183], a[124:127], v[196:199], a[180:183]\n"
      "v_exp_f32 v112, v128\n"
      "v_mfma_f32_16x16x32_bf16 a[164:167], a[120:123], v[196:199], a[164:167]\n"
      "v_xor_b32 v240, 0x10000, v244\n"
      "v_mfma_f32_16x16x32_bf16 a[192:195], a[196:199], v[208:211], a[192:195]\n"
      "v_exp_f32 v113, v129\n"
      "v_mfma_f32_16x16x32_bf16 v[84:87], a[116:119], v[196:199], v[84:87]\n"
      "v_cvt_pk_bf16_f32 v192, v112, v113\n"
      "v_xor_b32 v241, 0x10000, v245\n"
      "v_mfma_f32_16x16x32_bf16 v[68:71], a[112:115], v[196:199], v[68:71]\n"
      "v_exp_f32 v114, v130\n"
      "v_mfma_f32_16x16x32_bf16 a[192:195], a[200:203], v[212:215], a[192:195]\n"
      "v_xor_b32 v250, 0x10000, v246\n"
      "v_mfma_f32_16x16x32_bf16 a[148:151], a[108:111], v[196:199], a[148:151]\n"
      "v_exp_f32 v115, v131\n"
      "v_mfma_f32_16x16x32_bf16 a[132:135], a[104:107], v[196:199], a[132:135]\n"
      "s_memrealtime s[68:69]\n"
      "v_cvt_pk_bf16_f32 v193, v114, v115\n"
      "v_xor_b32 v251, 0x10000, v247\n"
      "v_mfma_f32_16x16x32_bf16 a[192:195], a[204:207], v[216:219], a[192:195]\n"
      "v_exp_f32 v112, v132\n"
      "v_mfma_f32_16x16x32_bf16 v[52:55], a[100:103], v[196:199], v[52:55]\n"
      "v_or_b32 v116, v192, v193\n"
      "v_mfma_f32_16x16x32_bf16 v[36:39], a[96:99], v[196:199], v[36:39]\n"
      "v_exp_f32 v113, v133\n"
      "v_mfma_f32_16x16x32_bf16 a[192:195], a[208:211], v[220:223], a[192:195]\n"
      "v_cvt_pk_bf16_f32 v194, v112, v113\n"
      "s_waitcnt vmcnt(6) lgkmcnt(0)\n"
      "s_sub_u32 s67, s68, s90\n"
      "s_cmp_lt_i32 s67, 0\n"
      "s_cbranch_scc1 GW7_%=\n"
      "GX7_%=:\n"
      "s_barrier\n"
      "v_mfma_f32_16x16x32_bf16 v[40:43], a[96:99], v[200:203], v[40:43]\n"
      "s_memrealtime s[68:69]\n"
      "ds_read_b128 a[64:67], v242 offset:32768\n"
      "v_exp_f32 v114, v134\n"
      "v_mfma_f32_16x16x32_bf16 v[44:47], a[96:99], v[204:207], v[44:47]\n"
      "s_add_u32 m0, %[wb], 82944\n"
      "ds_read_b128 a[68:71], v242 offset:36864\n"
      "v_mfma_f32_16x16x32_bf16 v[60:63], a[100:103], v[204:207], v[60:63]\n"
      "ds_read_b128 a[72:75], v243 offset:32768\n"
      "buffer_load_dwordx4 v121, s[24:27], s94 offen nt lds\n"
      "v_mfma_f32_16x16x32_bf16 v[56:59], a[100:103], v[200:203], v[56:59]\n"
      "s_add_u32 m0, %[wb], 91136\n"
      "ds_read_b128 a[76:79], v243 offset:36864\n"
      "v_mfma_f32_16x16x32_bf16 a[136:139], a[104:107], v[200:203], a[136:139]\n"
      "ds_read_b128 a[80:83], v242 offset:33792\n"
      "buffer_load_dwordx4 v123, s[24:27], s94 offen nt lds\n"
      "v_mfma_f32_16x16x32_bf16 a[140:143], a[104:107], v[204:207], a[140:143]\n"
      "ds_read_b128 a[84:87], v242 offset:37888\n"
      "v_mfma_f32_16x16x32_bf16 a[156:159], a[108:111], v[204:207], a[156:159]\n"
      "ds_read_b128 a[88:91], v243 offset:33792\n"
      "v_mfma_f32_16x16x32_bf16 a[152:155], a[108:111], v[200:203], a[152:155]\n"
      "ds_read_b128 a[92:95], v243 offset:37888\n"
      "v_mfma_f32_16x16x32_bf16 v[72:75], a[112:115], v[200:203], v[72:75]\n"
      "v_exp_f32 v115, v135\n"
      "v_mfma_f32_16x16x32_bf16 v[76:79], a[112:115], v[204:207], v[76:79]\n"
      "v_exp_f32 v112, v136\n"
      "v_mfma_f32_16x16x32_bf16 v[92:95], a[116:119], v[204:207], v[92:95]\n"
      "v_cvt_pk_bf16_f32 v195, v114, v115\n"
      "v_exp_f32 v113, v137\n"
      "v_mfma_f32_16x16x32_bf16 v[88:91], a[116:119], v[200:203], v[88:91]\n"
      "v_or3_b32 v116, v116, v194, v195\n"
      "v_mfma_f32_16x16x32_bf16 a[168:171], a[120:123], v[200:203], a[168:171]\n"
      "v_exp_f32 v114, v138\n"
      "v_mfma_f32_16x16x32_bf16 a[172:175], a[120:123], v[204:207], a[172:175]\n"
      "v_cvt_pk_bf16_f32 v196, v112, v113\n"
      "v_exp_f32 v115, v139\n"
      "v_mfma_f32_16x16x32_bf16 a[188:191], a[124:127], v[204:207], a[188:191]\n"
      "v_exp_f32 v112, v140\n"
      "v_mfma_f32_16x16x32_bf16 a[184:187], a[124:127], v[200:203], a[184:187]\n"
      "s_waitcnt lgkmcnt(0)\n"
      "v_cvt_pk_bf16_f32 v197, v114, v115\n"
      "v_mfma_f32_16x16x32_bf16 v[160:163], a[64:67], a[0:3], v[224:227]\n"
      "s_sub_u32 s67, s68, s66\n"
      "v_exp_f32 v113, v141\n"
      "v_mfma_f32_16x16x32_bf16 v[168:171], a[64:67], a[4:7], v[228:231]\n"
      "s_cmp_gt_i32 s67, s70\n"
      "v_or3_b32 v116, v116, v196, v197\n"
      "v_exp_f32 v114, v142\n"
      "v_mfma_f32_16x16x32_bf16 v[176:179], a[64:67], a[8:11], v[232:235]\n"
      "s_cselect_b32 s69, s86, 0\n"
      "v_cvt_pk_bf16_f32 v198, v112, v113\n"
      "v_mfma_f32_16x16x32_bf16 v[184:187], a[64:67], a[12:15], v[236:239]\n"
      "s_add_u32 s66, s66, s69\n"
      "v_exp_f32 v115, v143\n"
      "v_mfma_f32_16x16x32_bf16 v[164:167], a[68:71], a[0:3], v[224:227]\n"
      "s_add_u32 s67, s67, s101\n"
      "v_exp_f32 v112, v144\n"
      "v_mfma_f32_16x16x32_bf16 v[172:175], a[68:71], a[4:7], v[228:231]\n"
      "s_min_i32 s70, s70, s67\n"
      "v_cvt_pk_bf16_f32 v199, v114, v115\n"
      "v_exp_f32 v113, v145\n"
      "v_mfma_f32_16x16x32_bf16 v[180:183], a[68:71], a[8:11], v[232:235]\n"
      "s_max_i32 s70, s70, 85\n"
      "v_or3_b32 v116, v116, v198, v199\n"
      "v_mfma_f32_16x16x32_bf16 v[188:191], a[68:71], a[12:15], v[236:239]\n"
      "s_waitcnt lgkmcnt(4)\n"
      "v_exp_f32 v114, v146\n"
      "v_mfma_f32_16x16x32_bf16 v[160:163], a[72:75], a[16:19], v[160:163]\n"
      "s_add_u32 s66, s66, s86\n"
      "v_cvt_pk_bf16_f32 v200, v112, v113\n"
      "v_exp_f32 v115, v147\n"
      "v_mfma_f32_16x16x32_bf16 v[168:171], a[72:75], a[20:23], v[168:171]\n"
      "s_and_b32 s76, s80, 31\n"
      "v_exp_f32 v112, v148\n"
      "v_mfma_f32_16x16x32_bf16 v[176:179], a[72:75], a[24:27], v[176:179]\n"
      "s_lshr_b64 s[80:81], s[80:81], 5\n"
      "v_cvt_pk_bf16_f32 v201, v114, v115\n"
      "v_mfma_f32_16x16x32_bf16 v[184:187], a[72:75], a[28:31], v[184:187]\n"
      "s_add_u32 s66, s66, s76\n"
      "v_exp_f32 v113, v149\n"
      "v_mfma_f32_16x16x32_bf16 v[164:167], a[76:79], a[16:19], v[164:167]\n"
      "s_sub_u32 s67, s68, s82\n"
      "v_or3_b32 v116, v116, v200, v201\n"
      "v_exp_f32 v114, v150\n"
      "v_mfma_f32_16x16x32_bf16 v[172:175], a[76:79], a[20:23], v[172:175]\n"
      "s_min_i32 s76, s67, s83\n"
      "v_cvt_pk_bf16_f32 v202, v112, v113\n"
      "v_mfma_f32_16x16x32_bf16 v[180:183], a[76:79], a[24:27], v[180:183]\n"
      "s_add_u32 m0, s62, 0\n"
      "v_exp_f32 v115, v151\n"
      "v_mfma_f32_16x16x32_bf16 v[188:191], a[76:79], a[28:31], v[188:191]\n"
      "buffer_load_dwordx4 v248, s[20:23], s60 offen nt lds\n"
      "s_waitcnt lgkmcnt(2)\n"
      "v_mfma_f32_16x16x32_bf16 v[160:163], a[80:83], a[32:35], v[160:163]\n"
      "s_add_u32 m0, s62, 896\n"
      "v_exp_f32 v112, v152\n"
      "v_mfma_f32_16x16x32_bf16 v[168:171], a[80:83], a[36:39], v[168:171]\n"
      "s_mov_b32 s83, s67\n"
      "buffer_load_dwordx4 v248, s[20:23], s60 offen offset:128 nt lds\n"
      "v_mfma_f32_16x16x32_bf16 v[176:179], a[80:83], a[40:43], v[176:179]\n"
      "s_add_u32 m0, s62, 8192\n"
      "v_mfma_f32_16x16x32_bf16 v[184:187], a[80:83], a[44:47], v[184:187]\n"
      "s_sub_u32 s76, s76, 36\n"
      "buffer_load_dwordx4 v120, s[20:23], s60 offen nt lds\n"
      "s_waitcnt vmcnt(9)\n"
      "s_barrier\n"
      "v_mfma_f32_16x16x32_bf16 v[164:167], a[84:87], a[32:35], v[164:167]\n"
      "s_add_u32 m0, s62, 9088\n"
      "ds_read_b64_tr_b16 a[96:97], v246 offset:33792\n"
      "v_mfma_f32_16x16x32_bf16 v[172:175], a[84:87], a[36:39], v[172:175]\n"
      "s_min_i32 s76, s76, 148\n"
      "ds_read_b64_tr_b16 a[98:99], v246 offset:37888\n"
      "buffer_load_dwordx4 v120, s[20:23], s60 offen offset:128 nt lds\n"
      "v_mfma_f32_16x16x32_bf16 v[180:183], a[84:87], a[40:43], v[180:183]\n"
      "s_max_i32 s76, s76, 70\n"
      "ds_read_b64_tr_b16 a[100:101], v247 offset:33792\n"
      "v_mfma_f32_16x16x32_bf16 v[188:191], a[84:87], a[44:47], v[188:191]\n"
      "ds_read_b64_tr_b16 a[102:103], v247 offset:37888\n"
      "s_waitcnt lgkmcnt(4)\n"
      "v_mfma_f32_16x16x32_bf16 v[160:163], a[88:91], a[48:51], v[160:163]\n"
      "s_mov_b32 s82, s68\n"
      "ds_read_b64_tr_b16 a[104:105], v244 offset:33792\n"
      "v_cvt_pk_bf16_f32 v203, v114, v115\n"
      "v_mfma_f32_16x16x32_bf16 v[168:171], a[88:91], a[52:55], v[168:171]\n"
      "s_sub_u32 s66, s66, s99\n"
      "ds_read_b64_tr_b16 a[106:107], v244 offset:37888\n"
      "v_exp_f32 v113, v153\n"
      "v_mfma_f32_16x16x32_bf16 v[176:179], a[88:91], a[56:59], v[176:179]\n"
      "s_sub_u32 s82, s82, s99\n"
      "ds_read_b64_tr_b16 a[108:109], v245 offset:33792\n"
      "v_mfma_f32_16x16x32_bf16 v[184:187], a[88:91], a[60:63], v[184:187]\n"
      "s_add_u32 s65, s82, s76\n"
      "ds_read_b64_tr_b16 a[110:111], v245 offset:37888\n"
      "v_or3_b32 v116, v116, v202, v203\n"
      "v_mfma_f32_16x16x32_bf16 v[164:167], a[92:95], a[48:51], v[164:167]\n"
      "s_sub_u32 s93, s93, 1\n"
      "v_exp_f32 v114, v154\n"
      "v_mfma_f32_16x16x32_bf16 v[172:175], a[92:95], a[52:55], v[172:175]\n"
      "s_cmp_le_i32 s93, 14\n"
      "v_cvt_pk_bf16_f32 v204, v112, v113\n"
      "v_exp_f32 v115, v155\n"
      "v_mfma_f32_16x16x32_bf16 v[180:183], a[92:95], a[56:59], v[180:183]\n"
      "s_cselect_b32 s86, 107, s86\n"
      "v_exp_f32 v112, v156\n"
      "v_mfma_f32_16x16x32_bf16 v[188:191], a[92:95], a[60:63], v[188:191]\n"
      "s_waitcnt lgkmcnt(4)\n"
      "v_cvt_pk_bf16_f32 v205, v114, v115\n"
      "v_mfma_f32_16x16x32_bf16 a[160:163], a[96:99], v[208:211], a[160:163]\n"
      "s_sub_u32 s67, s65, s66\n"
      "v_exp_f32 v113, v157\n"
      "v_mfma_f32_16x16x32_bf16 a[164:167], a[96:99], v[212:215], a[164:167]\n"
      "s_cmp_lt_i32 s67, 0\n"
      "v_or3_b32 v116, v116, v204, v205\n"
      "v_exp_f32 v114, v158\n"
      "v_mfma_f32_16x16x32_bf16 a[168:171], a[96:99], v[216:219], a[168:171]\n"
      "s_cselect_b32 s88, s66, s65\n"
      "v_cvt_pk_bf16_f32 v206, v112, v113\n"
      "v_mfma_f32_16x16x32_bf16 a[172:175], a[96:99], v[220:223], a[172:175]\n"
      "s_add_u32 s90, s88, s91\n"
      "v_exp_f32 v115, v159\n"
      "s_nop 0\n"
      "v_mfma_f32_16x16x32_bf16 a[176:179], a[100:103], v[208:211], a[176:179]\n"
      "s_mov_b32 s64, 4\n"
      "v_cvt_pk_bf16_f32 v207, v114, v115\n"
      "v_or3_b32 v116, v116, v206, v207\n"
      "v_mfma_f32_16x16x32_bf16 a[180:183], a[100:103], v[212:215], a[180:183]\n"
      "v_and_b32 v116, 0x40004000, v116\n"
      "v_cmp_ne_u32 vcc, 0, v116\n"
      "s_cbranch_vccnz FB0_%=\n"
      "RET0_4_%=:\n"
      "v_mfma_f32_16x16x32_bf16 a[184:187], a[100:103], v[216:219], a[184:187]\n"
      "v_exp_f32 v112, v160\n"
      "v_mfma_f32_16x16x32_bf16 a[188:191], a[100:103], v[220:223], a[188:191]\n"
      "s_memrealtime s[68:69]\n"
      "v_xor_b32 v242, 0x10000, v242\n"
      "v_mfma_f32_16x16x32_bf16 a[192:195], a[196:199], v[192:195], a[192:195]\n"
      "v_exp_f32 v113, v161\n"
      "v_mfma_f32_16x16x32_bf16 a[192:195], a[200:203], v[196:199], a[192:195]\n"
      "v_xor_b32 v243, 0x10000, v243\n"
      "v_exp_f32 v114, v162\n"
      "v_mfma_f32_16x16x32_bf16 a[192:195], a[204:207], v[200:203], a[192:195]\n"
      "v_cvt_pk_bf16_f32 v0, v112, v113\n"
      "v_mfma_f32_16x16x32_bf16 a[192:195], a[208:211], v[204:207], a[192:195]\n"
      "v_exp_f32 v115, v163\n"
      "s_waitcnt vmcnt(6) lgkmcnt(0)\n"
      "s_sub_u32 s67, s68, s90\n"
      "s_cmp_lt_i32 s67, 0\n"
      "s_cbranch_scc1 GW8_%=\n"
      "GX8_%=:\n"
      "s_barrier\n"
      "v_mfma_f32_16x16x32_bf16 v[64:67], a[104:107], v[208:211], v[64:67]\n"
      "ds_read_b128 a[64:67], v242 offset:0\n"
      "ds_read_b128 a[68:71], v242 offset:4096\n"
      "v_mfma_f32_16x16x32_bf16 v[68:71], a[104:107], v[212:215], v[68:71]\n"
      "s_add_u32 m0, %[wb], 115712\n"
      "ds_read_b128 a[72:75], v243 offset:0\n"
      "ds_read_b128 a[76:79], v243 offset:4096\n"
      "v_mfma_f32_16x16x32_bf16 v[72:75], a[104:107], v[216:219], v[72:75]\n"
      "ds_read_b128 a[80:83], v242 offset:1024\n"
      "ds_read_b128 a[84:87], v242 offset:5120\n"
      "buffer_load_dwordx4 v121, s[24:27], s95 offen nt lds\n"
      "v_mfma_f32_16x16x32_bf16 v[76:79], a[104:107], v[220:223], v[76:79]\n"
      "s_add_u32 m0, %[wb], 123904\n"
      "ds_read_b128 a[88:91], v243 offset:1024\n"
      "ds_read_b128 a[92:95], v243 offset:5120\n"
      "v_mfma_f32_16x16x32_bf16 v[80:83], a[108:111], v[208:211], v[80:83]\n"
      "buffer_load_dwordx4 v123, s[24:27], s95 offen nt lds\n"
      "v_mfma_f32_16x16x32_bf16 v[84:87], a[108:111], v[212:215], v[84:87]\n"
      "s_add_u32 m0, %[wb], 17408\n"
      "v_mfma_f32_16x16x32_bf16 v[88:91], a[108:111], v[216:219], v[88:91]\n"
      "buffer_load_dwordx4 v121, s[24:27], s77 offen nt lds\n"
      "v_mfma_f32_16x16x32_bf16 v[92:95], a[108:111], v[220:223], v[92:95]\n"
      "s_waitcnt lgkmcnt(6)\n"
      "s_add_u32 m0, %[wb], 25600\n"
      "v_exp_f32 v112, v164\n"
      "v_mfma_f32_16x16x32_bf16 v[128:131], a[64:67], a[0:3], v[224:227]\n"
      "buffer_load_dwordx4 v123, s[24:27], s77 offen nt lds\n"
      "v_mfma_f32_16x16x32_bf16 v[136:139], a[64:67], a[4:7], v[228:231]\n"
      "v_cvt_pk_bf16_f32 v1, v114, v115\n"
      "v_exp_f32 v113, v165\n"
      "v_mfma_f32_16x16x32_bf16 v[144:147], a[64:67], a[8:11], v[232:235]\n"
      "v_or_b32 v116, v0, v1\n"
      "v_mfma_f32_16x16x32_bf16 v[152:155], a[64:67], a[12:15], v[236:239]\n"
      "v_exp_f32 v114, v166\n"
      "v_cvt_pk_bf16_f32 v2, v112, v113\n"
      "v_mfma_f32_16x16x32_bf16 v[132:135], a[68:71], a[0:3], v[224:227]\n"
      "v_exp_f32 v115, v167\n"
      "v_mfma_f32_16x16x32_bf16 v[140:143], a[68:71], a[4:7], v[228:231]\n"
      "v_exp_f32 v112, v168\n"
      "v_cvt_pk_bf16_f32 v3, v114, v115\n"
      "v_mfma_f32_16x16x32_bf16 v[148:151], a[68:71], a[8:11], v[232:235]\n"
      "v_exp_f32 v113, v169\n"
      "v_or3_b32 v116, v116, v2, v3\n"
      "v_mfma_f32_16x16x32_bf16 v[156:159], a[68:71], a[12:15], v[236:239]\n"
      "s_waitcnt lgkmcnt(4)\n"
      "v_exp_f32 v114, v170\n"
      "v_mfma_f32_16x16x32_bf16 v[128:131], a[72:75], a[16:19], v[128:131]\n"
      "v_cvt_pk_bf16_f32 v4, v112, v113\n"
      "v_exp_f32 v115, v171\n"
      "v_mfma_f32_16x16x32_bf16 v[136:139], a[72:75], a[20:23], v[136:139]\n"
      "v_exp_f32 v112, v172\n"
      "v_mfma_f32_16x16x32_bf16 v[144:147], a[72:75], a[24:27], v[144:147]\n"
      "v_cvt_pk_bf16_f32 v5, v114, v115\n"
      "v_exp_f32 v113, v173\n"
      "v_mfma_f32_16x16x32_bf16 v[152:155], a[72:75], a[28:31], v[152:155]\n"
      "v_or3_b32 v116, v116, v4, v5\n"
      "v_mfma_f32_16x16x32_bf16 v[132:135], a[76:79], a[16:19], v[132:135]\n"
      "v_exp_f32 v114, v174\n"
      "v_cvt_pk_bf16_f32 v6, v112, v113\n"
      "v_mfma_f32_16x16x32_bf16 v[140:143], a[76:79], a[20:23], v[140:143]\n"
      "v_exp_f32 v115, v175\n"
      "v_mfma_f32_16x16x32_bf16 v[148:151], a[76:79], a[24:27], v[148:151]\n"
      "v_exp_f32 v112, v176\n"
      "v_cvt_pk_bf16_f32 v7, v114, v115\n"
      "v_mfma_f32_16x16x32_bf16 v[156:159], a[76:79], a[28:31], v[156:159]\n"
      "s_waitcnt lgkmcnt(2)\n"
      "v_exp_f32 v113, v177\n"
      "v_mfma_f32_16x16x32_bf16 v[128:131], a[80:83], a[32:35], v[128:131]\n"
      "v_or3_b32 v116, v116, v6, v7\n"
      "v_exp_f32 v114, v178\n"
      "v_mfma_f32_16x16x32_bf16 v[136:139], a[80:83], a[36:39], v[136:139]\n"
      "v_cvt_pk_bf16_f32 v8, v112, v113\n"
      "v_exp_f32 v115, v179\n"
      "v_mfma_f32_16x16x32_bf16 v[144:147], a[80:83], a[40:43], v[144:147]\n"
      "v_exp_f32 v112, v180\n"
      "v_mfma_f32_16x16x32_bf16 v[152:155], a[80:83], a[44:47], v[152:155]\n"
      "v_cvt_pk_bf16_f32 v9, v114, v115\n"
      "v_exp_f32 v113, v181\n"
      "s_waitcnt vmcnt(8)\n"
      "s_barrier\n"
      "v_mfma_f32_16x16x32_bf16 v[132:135], a[84:87], a[32:35], v[132:135]\n"
      "ds_read_b64_tr_b16 a[112:113], v250 offset:1024\n"
      "v_or3_b32 v116, v116, v8, v9\n"
      "v_mfma_f32_16x16x32_bf16 v[140:143], a[84:87], a[36:39], v[140:143]\n"
      "ds_read_b64_tr_b16 a[114:115], v250 offset:5120\n"
      "v_exp_f32 v114, v182\n"
      "v_mfma_f32_16x16x32_bf16 v[148:151], a[84:87], a[40:43], v[148:151]\n"
      "ds_read_b64_tr_b16 a[116:117], v251 offset:1024\n"
      "v_mfma_f32_16x16x32_bf16 v[156:159], a[84:87], a[44:47], v[156:159]\n"
      "ds_read_b64_tr_b16 a[118:119], v251 offset:5120\n"
      "v_cvt_pk_bf16_f32 v10, v112, v113\n"
      "s_waitcnt lgkmcnt(4)\n"
      "v_mfma_f32_16x16x32_bf16 v[128:131], a[88:91], a[48:51], v[128:131]\n"
      "ds_read_b64_tr_b16 a[120:121], v240 offset:1024\n"
      "v_exp_f32 v115, v183\n"
      "v_mfma_f32_16x16x32_bf16 v[136:139], a[88:91], a[52:55], v[136:139]\n"
      "ds_read_b64_tr_b16 a[122:123], v240 offset:5120\n"
      "v_exp_f32 v112, v184\n"
      "v_mfma_f32_16x16x32_bf16 v[144:147], a[88:91], a[56:59], v[144:147]\n"
      "ds_read_b64_tr_b16 a[124:125], v241 offset:1024\n"
      "v_cvt_pk_bf16_f32 v11, v114, v115\n"
      "v_mfma_f32_16x16x32_bf16 v[152:155], a[88:91], a[60:63], v[152:155]\n"
      "ds_read_b64_tr_b16 a[126:127], v241 offset:5120\n"
      "v_exp_f32 v113, v185\n"
      "v_mfma_f32_16x16x32_bf16 v[132:135], a[92:95], a[48:51], v[132:135]\n"
      "v_or3_b32 v116, v116, v10, v11\n"
      "v_exp_f32 v114, v186\n"
      "v_mfma_f32_16x16x32_bf16 v[140:143], a[92:95], a[52:55], v[140:143]\n"
      "v_cvt_pk_bf16_f32 v12, v112, v113\n"
      "v_mfma_f32_16x16x32_bf16 v[148:151], a[92:95], a[56:59], v[148:151]\n"
      "v_exp_f32 v115, v187\n"
      "v_mfma_f32_16x16x32_bf16 v[156:159], a[92:95], a[60:63], v[156:159]\n"
      "s_waitcnt lgkmcnt(4)\n"
      "v_exp_f32 v112, v188\n"
      "v_cvt_pk_bf16_f32 v13, v114, v115\n"
      "v_mfma_f32_16x16x32_bf16 a[160:163], a[112:115], v[192:195], a[160:163]\n"
      "v_exp_f32 v113, v189\n"
      "v_or3_b32 v116, v116, v12, v13\n"
      "v_mfma_f32_16x16x32_bf16 a[164:167], a[112:115], v[196:199], a[164:167]\n"
      "v_exp_f32 v114, v190\n"
      "v_mfma_f32_16x16x32_bf16 a[168:171], a[112:115], v[200:203], a[168:171]\n"
      "v_cvt_pk_bf16_f32 v14, v112, v113\n"
      "v_exp_f32 v115, v191\n"
      "v_mfma_f32_16x16x32_bf16 a[172:175], a[112:115], v[204:207], a[172:175]\n"
      "s_add_u32 m0, %[wb], 50176\n"
      "s_nop 0\n"
      "v_cvt_pk_bf16_f32 v15, v114, v115\n"
      "v_or3_b32 v116, v116, v14, v15\n"
      "v_mfma_f32_16x16x32_bf16 a[176:179], a[116:119], v[192:195], a[176:179]\n"
      "s_mov_b32 s64, 5\n"
      "buffer_load_dwordx4 v121, s[24:27], s78 offen nt lds\n"
      "v_mfma_f32_16x16x32_bf16 a[180:183], a[116:119], v[196:199], a[180:183]\n"
      "s_add_u32 m0, %[wb], 58368\n"
      "v_and_b32 v116, 0x40004000, v116\n"
      "v_cmp_ne_u32 vcc, 0, v116\n"
      "s_cbranch_vccnz FB1_%=\n"
      "RET1_5_%=:\n"
      "v_mfma_f32_16x16x32_bf16 a[184:187], a[116:119], v[200:203], a[184:187]\n"
      "buffer_load_dwordx4 v123, s[24:27], s78 offen nt lds\n"
      "v_mfma_f32_16x16x32_bf16 a[188:191], a[116:119], v[204:207], a[188:191]\n"
      "s_memrealtime s[68:69]\n"
      "v_exp_f32 v112, v128\n"
      "v_exp_f32 v113, v129\n"
      "v_mfma_f32_16x16x32_bf16 a[192:195], a[196:199], v[0:3], a[192:195]\n"
      "v_exp_f32 v114, v130\n"
      "v_mfma_f32_16x16x32_bf16 a[192:195], a[200:203], v[4:7], a[192:195]\n"
      "v_cvt_pk_bf16_f32 v16, v112, v113\n"
      "v_exp_f32 v115, v131\n"
      "v_mfma_f32_16x16x32_bf16 a[192:195], a[204:207], v[8:11], a[192:195]\n"
      "v_exp_f32 v112, v132\n"
      "v_cvt_pk_bf16_f32 v17, v114, v115\n"
      "v_mfma_f32_16x16x32_bf16 a[192:195], a[208:211], v[12:15], a[192:195]\n"
      "v_exp_f32 v113, v133\n"
      "v_or_b32 v116, v16, v17\n"
      "s_add_u32 s72, s72, 0x7f4a7c15\n"
      "s_addc_u32 s76, s79, 0x9e3779b9\n"
      "s_waitcnt vmcnt(6) lgkmcnt(0)\n"
      "s_barrier\n"
      "v_mfma_f32_16x16x32_bf16 v[64:67], a[120:123], v[192:195], v[64:67]\n"
      "s_xor_b32 s80, %[xo], 0x400000\n"
      "ds_read_b128 a[64:67], v242 offset:32768\n"
      "ds_read_b128 a[68:71], v242 offset:36864\n"
      "v_mfma_f32_16x16x32_bf16 v[68:71], a[120:123], v[196:199], v[68:71]\n"
      "s_add_u32 s79, s80, 0x1000\n"
      "ds_read_b128 a[72:75], v243 offset:32768\n"
      "ds_read_b128 a[76:79], v243 offset:36864\n"
      "v_exp_f32 v114, v134\n"
      "v_mfma_f32_16x16x32_bf16 v[72:75], a[120:123], v[200:203], v[72:75]\n"
      "s_add_u32 s81, %[xo], 0x1000\n"
      "ds_read_b128 a[80:83], v242 offset:33792\n"
      "ds_read_b128 a[84:87], v242 offset:37888\n"
      "v_cvt_pk_bf16_f32 v18, v112, v113\n"
      "v_mfma_f32_16x16x32_bf16 v[76:79], a[120:123], v[204:207], v[76:79]\n"
      "s_mov_b32 s63, 0x100000\n"
      "ds_read_b128 a[88:91], v243 offset:33792\n"
      "ds_read_b128 a[92:95], v243 offset:37888\n"
      "v_mfma_f32_16x16x32_bf16 v[80:83], a[124:127], v[192:195], v[80:83]\n"
      "s_mov_b32 s60, 0x928b2f\n"
      "v_exp_f32 v115, v135\n"
      "v_exp_f32 v112, v136\n"
      "v_mfma_f32_16x16x32_bf16 v[84:87], a[124:127], v[196:199], v[84:87]\n"
      "s_mov_b32 s62, 0x9818e9\n"
      "v_cvt_pk_bf16_f32 v19, v114, v115\n"
      "v_exp_f32 v113, v137\n"
      "v_mfma_f32_16x16x32_bf16 v[88:91], a[124:127], v[200:203], v[88:91]\n"
      "s_mov_b32 s70, 0xdd9dc9\n"
      "v_xor_b32 v248, 0x2000, v244\n"
      "v_or3_b32 v116, v116, v18, v19\n"
      "v_mfma_f32_16x16x32_bf16 v[92:95], a[124:127], v[204:207], v[92:95]\n"
      "s_waitcnt lgkmcnt(6)\n"
      "v_exp_f32 v114, v138\n"
      "v_cvt_pk_bf16_f32 v20, v112, v113\n"
      "v_mfma_f32_16x16x32_bf16 v[160:163], a[64:67], a[0:3], v[224:227]\n"
      "s_mov_b32 s71, 0x8ed905\n"
      "v_exp_f32 v115, v139\n"
      "v_mfma_f32_16x16x32_bf16 v[168:171], a[64:67], a[4:7], v[228:231]\n"
      "s_sub_u32 s88, 324, %[cph]\n"
      "v_xor_b32 v120, 0x2000, v245\n"
      "v_exp_f32 v112, v140\n"
      "v_cvt_pk_bf16_f32 v21, v114, v115\n"
      "v_mfma_f32_16x16x32_bf16 v[176:179], a[64:67], a[8:11], v[232:235]\n"
      "s_add_u32 s88, s88, s66\n"
      "v_exp_f32 v113, v141\n"
      "v_mfma_f32_16x16x32_bf16 v[184:187], a[64:67], a[12:15], v[236:239]\n"
      "s_add_u32 m0, %[wb], 49152\n"
      "v_or3_b32 v116, v116, v20, v21\n"
      "v_exp_f32 v114, v142\n"
      "v_mfma_f32_16x16x32_bf16 v[164:167], a[68:71], a[0:3], v[224:227]\n"
      "s_add_u32 s88, s88, s100\n"
      "buffer_load_dwordx4 v249, s[24:27], s61 offen nt lds\n"
      "v_mbcnt_lo_u32_b32 v127, -1, 0\n"
      "v_mfma_f32_16x16x32_bf16 v[172:175], a[68:71], a[4:7], v[228:231]\n"
      "s_add_u32 m0, %[wb], 57344\n"
      "v_cvt_pk_bf16_f32 v22, v112, v113\n"
      "v_mfma_f32_16x16x32_bf16 v[180:183], a[68:71], a[8:11], v[232:235]\n"
      "buffer_load_dwordx4 v122, s[24:27], s61 offen nt lds\n"
      "v_mfma_f32_16x16x32_bf16 v[188:191], a[68:71], a[12:15], v[236:239]\n"
      "s_waitcnt lgkmcnt(4)\n"
      "v_mbcnt_hi_u32_b32 v127, -1, v127\n"
      "v_exp_f32 v115, v143\n"
      "v_mfma_f32_16x16x32_bf16 v[160:163], a[72:75], a[16:19], v[160:163]\n"
      "v_exp_f32 v112, v144\n"
      "v_cvt_pk_bf16_f32 v23, v114, v115\n"
      "v_mfma_f32_16x16x32_bf16 v[168:171], a[72:75], a[20:23], v[168:171]\n"
      "v_exp_f32 v113, v145\n"
      "v_or3_b32 v116, v116, v22, v23\n"
      "v_mfma_f32_16x16x32_bf16 v[176:179], a[72:75], a[24:27], v[176:179]\n"
      "v_lshlrev_b32 v127, 4, v127\n"
      "v_exp_f32 v114, v146\n"
      "v_mfma_f32_16x16x32_bf16 v[184:187], a[72:75], a[28:31], v[184:187]\n"
      "v_cvt_pk_bf16_f32 v24, v112, v113\n"
      "v_exp_f32 v115, v147\n"
      "v_mfma_f32_16x16x32_bf16 v[164:167], a[76:79], a[16:19], v[164:167]\n"
      "v_exp_f32 v112, v148\n"
      "v_cvt_pk_bf16_f32 v25, v114, v115\n"
      "v_mfma_f32_16x16x32_bf16 v[172:175], a[76:79], a[20:23], v[172:175]\n"
      "v_add_u32 v125, %[xw], v127\n"
      "v_exp_f32 v113, v149\n"
      "v_mfma_f32_16x16x32_bf16 v[180:183], a[76:79], a[24:27], v[180:183]\n"
      "v_or3_b32 v116, v116, v24, v25\n"
      "v_exp_f32 v114, v150\n"
      "v_mfma_f32_16x16x32_bf16 v[188:191], a[76:79], a[28:31], v[188:191]\n"
      "s_waitcnt lgkmcnt(2)\n"
      "v_cvt_pk_bf16_f32 v26, v112, v113\n"
      "v_exp_f32 v115, v151\n"
      "v_mfma_f32_16x16x32_bf16 v[160:163], a[80:83], a[32:35], v[160:163]\n"
      "v_add_u32 v126, %[xr], v127\n"
      "v_exp_f32 v112, v152\n"
      "v_mfma_f32_16x16x32_bf16 v[168:171], a[80:83], a[36:39], v[168:171]\n"
      "s_add_u32 m0, %[wb], 81920\n"
      "v_cvt_pk_bf16_f32 v27, v114, v115\n"
      "v_exp_f32 v113, v153\n"
      "v_mfma_f32_16x16x32_bf16 v[176:179], a[80:83], a[40:43], v[176:179]\n"
      "buffer_load_dwordx4 v249, s[24:27], s94 offen nt lds\n"
      "v_mfma_f32_16x16x32_bf16 v[184:187], a[80:83], a[44:47], v[184:187]\n"
      "s_add_u32 m0, %[wb], 90112\n"
      "v_or3_b32 v116, v116, v26, v27\n"
      "v_mfma_f32_16x16x32_bf16 v[164:167], a[84:87], a[32:35], v[164:167]\n"
      "buffer_load_dwordx4 v122, s[24:27], s94 offen nt lds\n"
      "v_mfma_f32_16x16x32_bf16 v[172:175], a[84:87], a[36:39], v[172:175]\n"
      "v_exp_f32 v114, v154\n"
      "v_cvt_pk_bf16_f32 v28, v112, v113\n"
      "v_mfma_f32_16x16x32_bf16 v[180:183], a[84:87], a[40:43], v[180:183]\n"
      "v_exp_f32 v115, v155\n"
      "v_exp_f32 v112, v156\n"
      "v_mov_b32 v123, s88\n"
      "v_mfma_f32_16x16x32_bf16 v[188:191], a[84:87], a[44:47], v[188:191]\n"
      "s_waitcnt lgkmcnt(0)\n"
      "v_cvt_pk_bf16_f32 v29, v114, v115\n"
      "v_exp_f32 v113, v157\n"
      "s_waitcnt vmcnt(6)\n"
      "s_barrier\n"
      "v_mfma_f32_16x16x32_bf16 v[160:163], a[88:91], a[48:51], v[160:163]\n"
      "ds_read_b64_tr_b16 a[96:97], v250 offset:33792\n"
      "ds_read_b64_tr_b16 a[98:99], v250 offset:37888\n"
      "v_mfma_f32_16x16x32_bf16 v[168:171], a[88:91], a[52:55], v[168:171]\n"
      "ds_read_b64_tr_b16 a[100:101], v251 offset:33792\n"
      "ds_read_b64_tr_b16 a[102:103], v251 offset:37888\n"
      "v_or3_b32 v116, v116, v28, v29\n"
      "v_mfma_f32_16x16x32_bf16 v[176:179], a[88:91], a[56:59], v[176:179]\n"
      "ds_read_b64_tr_b16 a[112:113], v246 offset:1024\n"
      "ds_read_b64_tr_b16 a[114:115], v246 offset:5120\n"
      "v_mfma_f32_16x16x32_bf16 v[184:187], a[88:91], a[60:63], v[184:187]\n"
      "ds_read_b64_tr_b16 a[116:117], v247 offset:1024\n"
      "ds_read_b64_tr_b16 a[118:119], v247 offset:5120\n"
      "v_exp_f32 v114, v158\n"
      "v_mfma_f32_16x16x32_bf16 v[164:167], a[92:95], a[48:51], v[164:167]\n"
      "ds_read_b64_tr_b16 a[104:105], v240 offset:33792\n"
      "v_cvt_pk_bf16_f32 v30, v112, v113\n"
      "v_mfma_f32_16x16x32_bf16 v[172:175], a[92:95], a[52:55], v[172:175]\n"
      "s_mov_b32 s64, 5\n"
      "ds_read_b64_tr_b16 a[106:107], v240 offset:37888\n"
      "v_exp_f32 v115, v159\n"
      "s_nop 0\n"
      "v_mfma_f32_16x16x32_bf16 v[180:183], a[92:95], a[56:59], v[180:183]\n"
      "ds_read_b64_tr_b16 a[108:109], v241 offset:33792\n"
      "v_cvt_pk_bf16_f32 v31, v114, v115\n"
      "v_or3_b32 v116, v116, v30, v31\n"
      "v_mfma_f32_16x16x32_bf16 v[188:191], a[92:95], a[60:63], v[188:191]\n"
      "ds_read_b64_tr_b16 a[110:111], v241 offset:37888\n"
      "v_and_b32 v116, 0x40004000, v116\n"
      "v_cmp_ne_u32 vcc, 0, v116\n"
      "s_waitcnt lgkmcnt(4)\n"
      "s_cbranch_vccnz FB0_%=\n"
      "RET0_5_%=:\n"
      "v_mfma_f32_16x16x32_bf16 a[160:163], a[96:99], v[0:3], a[160:163]\n"
      "ds_read_b64_tr_b16 a[120:121], v244 offset:1024\n"
      "v_mov_b32 v117, v224\n"
      "v_xor_b32 v242, 0x10000, v242\n"
      "v_xor_b32 v243, 0x10000, v243\n"
      "v_mfma_f32_16x16x32_bf16 a[176:179], a[100:103], v[0:3], a[176:179]\n"
      "ds_read_b64_tr_b16 a[122:123], v244 offset:5120\n"
      "v_exp_f32 v112, v160\n"
      "v_mfma_f32_16x16x32_bf16 a[160:163], a[112:115], v[16:19], a[160:163]\n"
      "s_mov_b32 s64, 6\n"
      "ds_read_b64_tr_b16 a[124:125], v245 offset:1024\n"
      "v_exp_f32 v113, v161\n"
      "v_exp_f32 v114, v162\n"
      "v_mfma_f32_16x16x32_bf16 a[176:179], a[116:119], v[16:19], a[176:179]\n"
      "s_add_u32 m0, %[wb], 114688\n"
      "ds_read_b64_tr_b16 a[126:127], v245 offset:5120\n"
      "v_cvt_pk_bf16_f32 v96, v112, v113\n"
      "v_exp_f32 v115, v163\n"
      "v_mfma_f32_16x16x32_bf16 a[164:167], a[96:99], v[4:7], a[164:167]\n"
      "buffer_load_dwordx4 v249, s[24:27], s95 offen nt lds\n"
      "v_mov_b32_dpp v117, v228 quad_perm:[0,1,2,3] row_mask:0x2 bank_mask:0xf\n"
      "v_mfma_f32_16x16x32_bf16 a[180:183], a[100:103], v[4:7], a[180:183]\n"
      "s_add_u32 m0, %[wb], 122880\n"
      "v_exp_f32 v112, v164\n"
      "v_cvt_pk_bf16_f32 v97, v114, v115\n"
      "v_mfma_f32_16x16x32_bf16 a[164:167], a[112:115], v[20:23], a[164:167]\n"
      "buffer_load_dwordx4 v122, s[24:27], s95 offen nt lds\n"
      "v_mfma_f32_16x16x32_bf16 a[180:183], a[116:119], v[20:23], a[180:183]\n"
      "ds_write_b128 v125, a[160:163] offset:0\n"
      "v_exp_f32 v113, v165\n"
      "v_or_b32 v116, v96, v97\n"
      "v_mfma_f32_16x16x32_bf16 a[168:171], a[96:99], v[8:11], a[168:171]\n"
      "v_mov_b32_dpp v117, v232 quad_perm:[0,1,2,3] row_mask:0x4 bank_mask:0xf\n"
      "v_exp_f32 v114, v166\n"
      "v_cvt_pk_bf16_f32 v98, v112, v113\n"
      "v_mfma_f32_16x16x32_bf16 a[184:187], a[100:103], v[8:11], a[184:187]\n"
      "ds_write_b128 v125, a[176:179] offset:4096\n"
      "v_exp_f32 v115, v167\n"
      "v_mfma_f32_16x16x32_bf16 a[168:171], a[112:115], v[24:27], a[168:171]\n"
      "v_exp_f32 v112, v168\n"
      "v_cvt_pk_bf16_f32 v99, v114, v115\n"
      "v_mfma_f32_16x16x32_bf16 a[184:187], a[116:119], v[24:27], a[184:187]\n"
      "v_exp_f32 v113, v169\n"
      "v_or3_b32 v116, v116, v98, v99\n"
      "v_mfma_f32_16x16x32_bf16 a[172:175], a[96:99], v[12:15], a[172:175]\n"
      "ds_write_b128 v125, v[96:99] offset:8192\n"
      "v_mov_b32_dpp v117, v236 quad_perm:[0,1,2,3] row_mask:0x8 bank_mask:0xf\n"
      "v_exp_f32 v114, v170\n"
      "v_mfma_f32_16x16x32_bf16 a[188:191], a[100:103], v[12:15], a[188:191]\n"
      "v_cvt_pk_bf16_f32 v100, v112, v113\n"
      "v_exp_f32 v115, v171\n"
      "v_mfma_f32_16x16x32_bf16 a[172:175], a[112:115], v[28:31], a[172:175]\n"
      "v_exp_f32 v112, v172\n"
      "v_cvt_pk_bf16_f32 v101, v114, v115\n"
      "v_exp_f32 v113, v173\n"
      "v_mfma_f32_16x16x32_bf16 a[188:191], a[116:119], v[28:31], a[188:191]\n"
      "s_add_u32 m0, %[wb], 16384\n"
      "ds_write_b128 v125, a[164:167] offset:1024\n"
      "v_or3_b32 v116, v116, v100, v101\n"
      "v_mfma_f32_16x16x32_bf16 a[192:195], a[196:199], v[16:19], a[192:195]\n"
      "buffer_load_dwordx4 v249, s[24:27], s77 offen nt lds\n"
      "ds_write_b32 v125, v117 offset:12292\n"
      "v_mfma_f32_16x16x32_bf16 a[192:195], a[200:203], v[20:23], a[192:195]\n"
      "s_add_u32 m0, %[wb], 24576\n"
      "v_exp_f32 v114, v174\n"
      "v_cvt_pk_bf16_f32 v102, v112, v113\n"
      "v_mfma_f32_16x16x32_bf16 a[192:195], a[204:207], v[24:27], a[192:195]\n"
      "buffer_load_dwordx4 v122, s[24:27], s77 offen nt lds\n"
      "ds_write_b128 v125, a[180:183] offset:5120\n"
      "ds_write_b32 v125, v123 offset:12296\n"
      "v_mfma_f32_16x16x32_bf16 a[192:195], a[208:211], v[28:31], a[192:195]\n"
      "s_waitcnt lgkmcnt(7)\n"
      "v_exp_f32 v115, v175\n"
      "v_exp_f32 v112, v176\n"
      "v_mfma_f32_16x16x32_bf16 v[64:67], a[104:107], v[0:3], v[64:67]\n"
      "v_cvt_pk_bf16_f32 v103, v114, v115\n"
      "v_exp_f32 v113, v177\n"
      "v_mfma_f32_16x16x32_bf16 v[68:71], a[104:107], v[4:7], v[68:71]\n"
      "ds_write_b128 v125, v[100:103] offset:9216\n"
      "v_or3_b32 v116, v116, v102, v103\n"
      "v_exp_f32 v114, v178\n"
      "s_waitcnt vmcnt(8)\n"
      "s_barrier\n"
      "v_mfma_f32_16x16x32_bf16 v[72:75], a[104:107], v[8:11], v[72:75]\n"
      "ds_read_b64_tr_b16 a[212:213], v244 offset:33792\n"
      "v_cvt_pk_bf16_f32 v104, v112, v113\n"
      "v_mfma_f32_16x16x32_bf16 v[76:79], a[104:107], v[12:15], v[76:79]\n"
      "ds_write_b128 v125, a[168:171] offset:2048\n"
      "ds_read_b64_tr_b16 a[214:215], v244 offset:37888\n"
      "v_exp_f32 v115, v179\n"
      "v_mfma_f32_16x16x32_bf16 a[228:231], a[196:199], v[96:99], a[192:195]\n"
      "ds_read_b64_tr_b16 a[216:217], v245 offset:33792\n"
      "v_exp_f32 v112, v180\n"
      "v_cvt_pk_bf16_f32 v105, v114, v115\n"
      "v_mfma_f32_16x16x32_bf16 v[80:83], a[108:111], v[0:3], v[80:83]\n"
      "ds_read_b64_tr_b16 a[218:219], v245 offset:37888\n"
      "v_exp_f32 v113, v181\n"
      "v_mfma_f32_16x16x32_bf16 v[84:87], a[108:111], v[4:7], v[84:87]\n"
      "ds_write_b128 v125, a[184:187] offset:6144\n"
      "ds_read_b64_tr_b16 a[220:221], v248 offset:33792\n"
      "v_or3_b32 v116, v116, v104, v105\n"
      "v_mfma_f32_16x16x32_bf16 v[88:91], a[108:111], v[8:11], v[88:91]\n"
      "ds_read_b64_tr_b16 a[222:223], v248 offset:37888\n"
      "v_exp_f32 v114, v182\n"
      "v_cvt_pk_bf16_f32 v106, v112, v113\n"
      "v_mfma_f32_16x16x32_bf16 v[92:95], a[108:111], v[12:15], v[92:95]\n"
      "ds_read_b64_tr_b16 a[224:225], v120 offset:33792\n"
      "v_exp_f32 v115, v183\n"
      "v_mfma_f32_16x16x32_bf16 v[64:67], a[120:123], v[16:19], v[64:67]\n"
      "s_add_u32 m0, %[wb], 82944\n"
      "ds_write_b128 v125, a[172:175] offset:3072\n"
      "ds_read_b64_tr_b16 a[226:227], v120 offset:37888\n"
      "v_exp_f32 v112, v184\n"
      "v_mfma_f32_16x16x32_bf16 v[68:71], a[120:123], v[20:23], v[68:71]\n"
      "buffer_load_dwordx4 v249, s[24:27], s78 offen nt lds\n"
      "v_mfma_f32_16x16x32_bf16 v[72:75], a[120:123], v[24:27], v[72:75]\n"
      "s_add_u32 m0, %[wb], 91136\n"
      "v_cvt_pk_bf16_f32 v107, v114, v115\n"
      "v_exp_f32 v113, v185\n"
      "v_or3_b32 v116, v116, v106, v107\n"
      "v_mfma_f32_16x16x32_bf16 v[76:79], a[120:123], v[28:31], v[76:79]\n"
      "buffer_load_dwordx4 v122, s[24:27], s78 offen nt lds\n"
      "ds_write_b128 v125, v[104:107] offset:10240\n"
      "v_mfma_f32_16x16x32_bf16 a[228:231], a[200:203], v[100:103], a[228:231]\n"
      "v_exp_f32 v114, v186\n"
      "v_cvt_pk_bf16_f32 v108, v112, v113\n"
      "v_mfma_f32_16x16x32_bf16 v[80:83], a[124:127], v[16:19], v[80:83]\n"
      "v_exp_f32 v115, v187\n"
      "v_exp_f32 v112, v188\n"
      "v_mfma_f32_16x16x32_bf16 v[84:87], a[124:127], v[20:23], v[84:87]\n"
      "ds_write_b128 v125, a[188:191] offset:7168\n"
      "v_cvt_pk_bf16_f32 v109, v114, v115\n"
      "v_mfma_f32_16x16x32_bf16 v[88:91], a[124:127], v[24:27], v[88:91]\n"
      "v_exp_f32 v113, v189\n"
      "v_or3_b32 v116, v116, v108, v109\n"
      "v_exp_f32 v114, v190\n"
      "v_mfma_f32_16x16x32_bf16 v[92:95], a[124:127], v[28:31], v[92:95]\n"
      "v_cvt_pk_bf16_f32 v110, v112, v113\n"
      "v_exp_f32 v115, v191\n"
      "s_nop 0\n"
      "v_mfma_f32_16x16x32_bf16 a[228:231], a[204:207], v[104:107], a[228:231]\n"
      "v_cvt_pk_bf16_f32 v111, v114, v115\n"
      "v_or3_b32 v116, v116, v110, v111\n"
      "v_and_b32 v116, 0x40004000, v116\n"
      "v_cmp_ne_u32 vcc, 0, v116\n"
      "s_cbranch_vccnz FB1_%=\n"
      "RET1_6_%=:\n"
      "ds_write_b128 v125, v[108:111] offset:11264\n"
      "v_mfma_f32_16x16x32_bf16 a[228:231], a[208:211], v[108:111], a[228:231]\n"
      "s_waitcnt lgkmcnt(9)\n"
      "v_mfma_f32_16x16x32_bf16 v[64:67], a[212:215], v[96:99], v[64:67]\n"
      "v_mov_b32 v148, 1.0\n"
      "v_mfma_f32_16x16x32_bf16 v[80:83], a[216:219], v[96:99], v[80:83]\n"
      "v_mov_b32 v149, 1.0\n"
      "v_mfma_f32_16x16x32_bf16 v[68:71], a[212:215], v[100:103], v[68:71]\n"
      "v_mov_b32 v157, %[fo]\n"
      "v_mfma_f32_16x16x32_bf16 v[84:87], a[216:219], v[100:103], v[84:87]\n"
      "v_mov_b32 v150, s72\n"
      "v_mfma_f32_16x16x32_bf16 v[72:75], a[212:215], v[104:107], v[72:75]\n"
      "v_mov_b32 v151, s76\n"
      "ds_write_b32 v125, a228 offset:12288\n"
      "v_mfma_f32_16x16x32_bf16 v[88:91], a[216:219], v[104:107], v[88:91]\n"
      "v_xor_b32 v158, 8, v157\n"
      "v_mfma_f32_16x16x32_bf16 v[76:79], a[212:215], v[108:111], v[76:79]\n"
      "v_mfma_f32_16x16x32_bf16 v[92:95], a[216:219], v[108:111], v[92:95]\n"
      "s_waitcnt lgkmcnt(0)\n"
      "s_barrier\n"
      "s_memrealtime s[68:69]\n"
      "ds_read_b32 v237, v127 offset:12296\n"
      "ds_read_b64 v[152:153], v126 offset:12288\n"
      "ds_read_b128 v[160:163], v126 offset:0\n"
      "ds_read_b128 v[176:179], v126 offset:4096\n"
      "ds_read_b128 v[164:167], v126 offset:1024\n"
      "ds_read_b128 v[180:183], v126 offset:5120\n"
      "ds_read_b128 v[168:171], v126 offset:2048\n"
      "ds_read_b128 v[184:187], v126 offset:6144\n"
      "ds_read_b128 v[172:175], v126 offset:3072\n"
      "ds_read_b128 v[188:191], v126 offset:7168\n"
      "ds_read_b128 a[64:67], v126 offset:8192\n"
      "ds_read_b128 a[68:71], v126 offset:9216\n"
      "ds_read_b128 a[72:75], v126 offset:10240\n"
      "ds_read_b128 a[76:79], v126 offset:11264\n"
      "v_accvgpr_read_b32 v144, a228\n"
      "v_and_b32 v118, 0xf0, v127\n"
      "v_lshrrev_b32 v156, 3, v127\n"
      "v_and_b32 v156, 32, v156\n"
      "v_lshl_or_b32 v156, v118, 4, v156\n"
      "v_lshrrev_b32 v118, 5, v127\n"
      "v_and_or_b32 v156, v118, 16, v156\n"
      "v_add_u32 v156, %[oo], v156\n"
      "v_add_u32 v155, 0x2000, v156\n"
      "v_xor_b32 v242, 0x2000, v240\n"
      "v_xor_b32 v243, 0x2000, v241\n"
      "v_mov_b32 v236, v150\n"
      "s_waitcnt lgkmcnt(12)\n"
      "v_cmp_neq_f32 vcc, v153, v117\n"
      "v_readfirstlane_b32 s88, v237\n"
      "s_cbranch_vccnz MIS_%=\n"
      "MR_%=:\n"
      "v_mul_f32 v144, v144, v148\n"
      "v_mov_b32 v145, v117\n"
      "v_fmac_f32 v144, v152, v149\n"
      "s_waitcnt lgkmcnt(10)\n"
      "v_pk_add_f32 v[64:65], v[64:65], v[160:161]\n"
      "v_pk_add_f32 v[66:67], v[66:67], v[162:163]\n"
      "v_pk_add_f32 v[80:81], v[80:81], v[176:177]\n"
      "v_pk_add_f32 v[82:83], v[82:83], v[178:179]\n"
      "s_waitcnt lgkmcnt(8)\n"
      "v_pk_add_f32 v[68:69], v[68:69], v[164:165]\n"
      "v_pk_add_f32 v[70:71], v[70:71], v[166:167]\n"
      "v_pk_add_f32 v[84:85], v[84:85], v[180:181]\n"
      "v_pk_add_f32 v[86:87], v[86:87], v[182:183]\n"
      "s_waitcnt lgkmcnt(6)\n"
      "v_pk_add_f32 v[72:73], v[72:73], v[168:169]\n"
      "v_pk_add_f32 v[74:75], v[74:75], v[170:171]\n"
      "v_pk_add_f32 v[88:89], v[88:89], v[184:185]\n"
      "v_pk_add_f32 v[90:91], v[90:91], v[186:187]\n"
      "s_waitcnt lgkmcnt(4)\n"
      "v_pk_add_f32 v[76:77], v[76:77], v[172:173]\n"
      "v_pk_add_f32 v[78:79], v[78:79], v[174:175]\n"
      "v_pk_add_f32 v[92:93], v[92:93], v[188:189]\n"
      "v_pk_add_f32 v[94:95], v[94:95], v[190:191]\n"
      "s_waitcnt lgkmcnt(0)\n"
      "s_add_u32 s65, s68, 55\n"
      "s_mov_b32 s66, s88\n"
      "s_sub_u32 s67, s65, s66\n"
      "s_cmp_lt_i32 s67, 0\n"
      "s_cselect_b32 s88, s66, s65\n"
      "v_mov_b32 v237, s88\n"
      "s_mov_b32 s88, s66\n"
      "s_cmp_lg_u32 %[xw], 0\n"
      "s_cbranch_scc1 NH_%=\n"
      "s_mov_b64 exec, 1\n"
      "global_store_dwordx2 v157, v[236:237], s[18:19] sc1\n"
      "s_mov_b64 exec, -1\n"
      "NH_%=:\n"
      "v_mfma_f32_16x16x32_bf16 v[64:67], a[220:223], a[64:67], v[64:67]\n"
      "v_mfma_f32_16x16x32_bf16 v[80:83], a[224:227], a[64:67], v[80:83]\n"
      "v_mfma_f32_16x16x32_bf16 v[68:71], a[220:223], a[68:71], v[68:71]\n"
      "v_mfma_f32_16x16x32_bf16 v[84:87], a[224:227], a[68:71], v[84:87]\n"
      "v_mfma_f32_16x16x32_bf16 v[72:75], a[220:223], a[72:75], v[72:75]\n"
      "v_mfma_f32_16x16x32_bf16 v[88:91], a[224:227], a[72:75], v[88:91]\n"
      "v_mfma_f32_16x16x32_bf16 v[76:79], a[220:223], a[76:79], v[76:79]\n"
      "v_mfma_f32_16x16x32_bf16 v[92:95], a[224:227], a[76:79], v[92:95]\n"
      "s_nop 1\n"
      "s_memrealtime s[68:69]\n"
      "v_cvt_pk_bf16_f32 v128, v64, v65\n"
      "v_cvt_pk_bf16_f32 v129, v66, v67\n"
      "v_cvt_pk_bf16_f32 v130, v80, v81\n"
      "v_cvt_pk_bf16_f32 v131, v82, v83\n"
      "v_cvt_pk_bf16_f32 v132, v68, v69\n"
      "v_cvt_pk_bf16_f32 v133, v70, v71\n"
      "v_cvt_pk_bf16_f32 v134, v84, v85\n"
      "v_cvt_pk_bf16_f32 v135, v86, v87\n"
      "v_cvt_pk_bf16_f32 v136, v72, v73\n"
      "v_cvt_pk_bf16_f32 v137, v74, v75\n"
      "v_cvt_pk_bf16_f32 v138, v88, v89\n"
      "v_cvt_pk_bf16_f32 v139, v90, v91\n"
      "v_cvt_pk_bf16_f32 v140, v76, v77\n"
      "v_cvt_pk_bf16_f32 v141, v78, v79\n"
      "v_cvt_pk_bf16_f32 v142, v92, v93\n"
      "v_cvt_pk_bf16_f32 v143, v94, v95\n"
      "v_mad_u32_u24 v147, v144, s60, v151\n"
      "v_mad_u32_u24 v147, v145, s62, v147\n"
      "v_add3_u32 v146, v150, v144, v145\n"
      "v_mad_u32_u24 v147, v128, s60, v147\n"
      "v_mad_u32_u24 v147, v129, s62, v147\n"
      "v_add3_u32 v146, v146, v128, v129\n"
      "v_mad_u32_u24 v147, v130, s70, v147\n"
      "v_mad_u32_u24 v147, v131, s71, v147\n"
      "v_add3_u32 v146, v146, v130, v131\n"
      "v_mad_u32_u24 v147, v132, s60, v147\n"
      "v_mad_u32_u24 v147, v133, s62, v147\n"
      "v_add3_u32 v146, v146, v132, v133\n"
      "v_mad_u32_u24 v147, v134, s70, v147\n"
      "v_mad_u32_u24 v147, v135, s71, v147\n"
      "v_add3_u32 v146, v146, v134, v135\n"
      "v_mad_u32_u24 v147, v136, s60, v147\n"
      "v_mad_u32_u24 v147, v137, s62, v147\n"
      "v_add3_u32 v146, v146, v136, v137\n"
      "v_mad_u32_u24 v147, v138, s70, v147\n"
      "v_mad_u32_u24 v147, v139, s71, v147\n"
      "v_add3_u32 v146, v146, v138, v139\n"
      "v_mad_u32_u24 v147, v140, s60, v147\n"
      "v_mad_u32_u24 v147, v141, s62, v147\n"
      "v_add3_u32 v146, v146, v140, v141\n"
      "v_mad_u32_u24 v147, v142, s70, v147\n"
      "v_mad_u32_u24 v147, v143, s71, v147\n"
      "v_add3_u32 v146, v146, v142, v143\n"
      "s_waitcnt lgkmcnt(0)\n"
      "s_call_b64 s[84:85], SPIN_%=\n"
      "global_load_dwordx2 v[232:233], v158, s[18:19] sc1\n"
      "buffer_store_dwordx4 v[128:131], v127, s[28:31], %[xo] offen offset:0 sc1\n"
      "buffer_store_dwordx4 v[132:135], v127, s[28:31], %[xo] offen offset:1024 sc1\n"
      "buffer_store_dwordx4 v[136:139], v127, s[28:31], %[xo] offen offset:2048 sc1\n"
      "buffer_store_dwordx4 v[140:143], v127, s[28:31], %[xo] offen offset:3072 sc1\n"
      "buffer_store_dwordx4 v[144:147], v127, s[28:31], s81 offen sc1\n"
      "global_load_dwordx2 v[234:235], v158, s[18:19] sc1\n"
      "s_waitcnt vmcnt(7)\n"
      "s_barrier\n"
      "ds_read_b64_tr_b16 a[96:97], v246 offset:32768\n"
      "ds_read_b64_tr_b16 a[98:99], v246 offset:36864\n"
      "ds_read_b64_tr_b16 a[100:101], v247 offset:32768\n"
      "ds_read_b64_tr_b16 a[102:103], v247 offset:36864\n"
      "ds_read_b64_tr_b16 a[112:113], v250 offset:0\n"
      "ds_read_b64_tr_b16 a[114:115], v250 offset:4096\n"
      "ds_read_b64_tr_b16 a[116:117], v251 offset:0\n"
      "ds_read_b64_tr_b16 a[118:119], v251 offset:4096\n"
      "ds_read_b64_tr_b16 a[0:1], v250 offset:32768\n"
      "ds_read_b64_tr_b16 a[2:3], v250 offset:36864\n"
      "ds_read_b64_tr_b16 a[4:5], v251 offset:32768\n"
      "ds_read_b64_tr_b16 a[6:7], v251 offset:36864\n"
      "s_waitcnt lgkmcnt(8)\n"
      "v_mfma_f32_16x16x32_bf16 a[128:131], a[96:99], v[208:211], a[128:131]\n"
      "ds_read_b64_tr_b16 a[104:105], v244 offset:32768\n"
      "ds_read_b64_tr_b16 a[106:107], v244 offset:36864\n"
      "v_mfma_f32_16x16x32_bf16 a[144:147], a[100:103], v[208:211], a[144:147]\n"
      "ds_read_b64_tr_b16 a[108:109], v245 offset:32768\n"
      "ds_read_b64_tr_b16 a[110:111], v245 offset:36864\n"
      "s_waitcnt lgkmcnt(8)\n"
      "v_mfma_f32_16x16x32_bf16 a[128:131], a[112:115], v[192:195], a[128:131]\n"
      "ds_read_b64_tr_b16 a[120:121], v240 offset:0\n"
      "ds_read_b64_tr_b16 a[122:123], v240 offset:4096\n"
      "v_mfma_f32_16x16x32_bf16 a[144:147], a[116:119], v[192:195], a[144:147]\n"
      "ds_read_b64_tr_b16 a[124:125], v241 offset:0\n"
      "ds_read_b64_tr_b16 a[126:127], v241 offset:4096\n"
      "s_waitcnt lgkmcnt(8)\n"
      "v_mfma_f32_16x16x32_bf16 a[128:131], a[0:3], v[0:3], a[128:131]\n"
      "ds_read_b64_tr_b16 a[8:9], v240 offset:32768\n"
      "ds_read_b64_tr_b16 a[10:11], v240 offset:36864\n"
      "v_mfma_f32_16x16x32_bf16 a[144:147], a[4:7], v[0:3], a[144:147]\n"
      "ds_read_b64_tr_b16 a[12:13], v241 offset:32768\n"
      "ds_read_b64_tr_b16 a[14:15], v241 offset:36864\n"
      "v_mfma_f32_16x16x32_bf16 a[132:135], a[96:99], v[212:215], a[132:135]\n"
      "ds_write_b128 v125, v[16:19] offset:8192\n"
      "v_lshrrev_b32 v118, 2, v127\n"
      "v_mfma_f32_16x16x32_bf16 a[148:151], a[100:103], v[212:215], a[148:151]\n"
      "v_xor_b32 v118, v118, v127\n"
      "v_mfma_f32_16x16x32_bf16 a[132:135], a[112:115], v[196:199], a[132:135]\n"
      "v_and_b32 v118, 0xc0, v118\n"
      "v_mfma_f32_16x16x32_bf16 a[148:151], a[116:119], v[196:199], a[148:151]\n"
      "v_and_or_b32 v118, v127, 32, v118\n"
      "v_mfma_f32_16x16x32_bf16 a[132:135], a[0:3], v[4:7], a[132:135]\n"
      "ds_write_b128 v125, v[20:23] offset:9216\n"
      "v_and_b32 v119, 16, v127\n"
      "v_mfma_f32_16x16x32_bf16 a[148:151], a[4:7], v[4:7], a[148:151]\n"
      "v_mov_b32 v84, 0x3f80\n"
      "v_mfma_f32_16x16x32_bf16 a[136:139], a[96:99], v[216:219], a[136:139]\n"
      "v_lshlrev_b32 v119, v119, v84\n"
      "v_mfma_f32_16x16x32_bf16 a[152:155], a[100:103], v[216:219], a[152:155]\n"
      "v_cmp_eq_u32 vcc, 0, v118\n"
      "v_mfma_f32_16x16x32_bf16 a[136:139], a[112:115], v[200:203], a[136:139]\n"
      "ds_write_b128 v125, v[24:27] offset:10240\n"
      "v_mov_b32 v84, 0\n"
      "v_mfma_f32_16x16x32_bf16 a[152:155], a[116:119], v[200:203], a[152:155]\n"
      "v_cndmask_b32 v86, 0, v119, vcc\n"
      "v_mfma_f32_16x16x32_bf16 a[136:139], a[0:3], v[8:11], a[136:139]\n"
      "v_cmp_eq_u32 vcc, 32, v118\n"
      "v_mfma_f32_16x16x32_bf16 a[152:155], a[4:7], v[8:11], a[152:155]\n"
      "v_mov_b32 v85, 0\n"
      "v_mfma_f32_16x16x32_bf16 a[140:143], a[96:99], v[220:223], a[140:143]\n"
      "ds_write_b128 v125, v[28:31] offset:11264\n"
      "v_cndmask_b32 v87, 0, v119, vcc\n"
      "v_mfma_f32_16x16x32_bf16 a[156:159], a[100:103], v[220:223], a[156:159]\n"
      "v_mov_b32 v88, 0\n"
      "v_mfma_f32_16x16x32_bf16 a[140:143], a[112:115], v[204:207], a[140:143]\n"
      "v_mov_b32 v89, 0\n"
      "v_mfma_f32_16x16x32_bf16 a[156:159], a[116:119], v[204:207], a[156:159]\n"
      "v_mfma_f32_16x16x32_bf16 a[140:143], a[0:3], v[12:15], a[140:143]\n"
      "ds_write_b128 v125, a[128:131] offset:0\n"
      "v_mfma_f32_16x16x32_bf16 a[156:159], a[4:7], v[12:15], a[156:159]\n"
      "s_waitcnt lgkmcnt(13)\n"
      "v_mfma_f32_16x16x32_bf16 v[32:35], a[104:107], v[208:211], v[32:35]\n"
      "ds_read_b64_tr_b16 a[32:33], v240 offset:1024\n"
      "v_mfma_f32_16x16x32_bf16 v[36:39], a[104:107], v[212:215], v[36:39]\n"
      "ds_read_b64_tr_b16 a[34:35], v240 offset:5120\n"
      "v_mfma_f32_16x16x32_bf16 v[40:43], a[104:107], v[216:219], v[40:43]\n"
      "ds_write_b128 v125, a[144:147] offset:4096\n"
      "ds_read_b64_tr_b16 a[36:37], v241 offset:1024\n"
      "v_mfma_f32_16x16x32_bf16 v[44:47], a[104:107], v[220:223], v[44:47]\n"
      "ds_read_b64_tr_b16 a[38:39], v241 offset:5120\n"
      "v_mfma_f32_16x16x32_bf16 v[48:51], a[108:111], v[208:211], v[48:51]\n"
      "ds_read_b64_tr_b16 a[40:41], v242 offset:1024\n"
      "v_mfma_f32_16x16x32_bf16 v[52:55], a[108:111], v[212:215], v[52:55]\n"
      "ds_read_b64_tr_b16 a[42:43], v242 offset:5120\n"
      "v_mfma_f32_16x16x32_bf16 v[56:59], a[108:111], v[216:219], v[56:59]\n"
      "ds_write_b128 v125, a[132:135] offset:1024\n"
      "ds_read_b64_tr_b16 a[44:45], v243 offset:1024\n"
      "v_mfma_f32_16x16x32_bf16 v[60:63], a[108:111], v[220:223], v[60:63]\n"
      "ds_read_b64_tr_b16 a[46:47], v243 offset:5120\n"
      "s_waitcnt lgkmcnt(15)\n"
      "v_mfma_f32_16x16x32_bf16 v[32:35], a[120:123], v[192:195], v[32:35]\n"
      "ds_read_b64_tr_b16 a[16:17], v244 offset:0\n"
      "v_mfma_f32_16x16x32_bf16 v[36:39], a[120:123], v[196:199], v[36:39]\n"
      "ds_read_b64_tr_b16 a[18:19], v244 offset:4096\n"
      "v_mfma_f32_16x16x32_bf16 v[40:43], a[120:123], v[200:203], v[40:43]\n"
      "ds_write_b128 v125, a[148:151] offset:5120\n"
      "ds_read_b64_tr_b16 a[20:21], v245 offset:0\n"
      "v_mfma_f32_16x16x32_bf16 v[44:47], a[120:123], v[204:207], v[44:47]\n"
      "ds_read_b64_tr_b16 a[22:23], v245 offset:4096\n"
      "v_mfma_f32_16x16x32_bf16 v[48:51], a[124:127], v[192:195], v[48:51]\n"
      "ds_read_b64_tr_b16 a[24:25], v248 offset:0\n"
      "v_mfma_f32_16x16x32_bf16 v[52:55], a[124:127], v[196:199], v[52:55]\n"
      "ds_read_b64_tr_b16 a[26:27], v248 offset:4096\n"
      "v_mfma_f32_16x16x32_bf16 v[56:59], a[124:127], v[200:203], v[56:59]\n"
      "ds_write_b128 v125, a[136:139] offset:2048\n"
      "ds_read_b64_tr_b16 a[28:29], v120 offset:0\n"
      "v_mfma_f32_16x16x32_bf16 v[60:63], a[124:127], v[204:207], v[60:63]\n"
      "ds_read_b64_tr_b16 a[30:31], v120 offset:4096\n"
      "s_waitcnt lgkmcnt(15)\n"
      "v_mfma_f32_16x16x32_bf16 v[32:35], a[8:11], v[0:3], v[32:35]\n"
      "v_mfma_f32_16x16x32_bf16 v[36:39], a[8:11], v[4:7], v[36:39]\n"
      "v_mfma_f32_16x16x32_bf16 v[40:43], a[8:11], v[8:11], v[40:43]\n"
      "ds_write_b128 v125, a[152:155] offset:6144\n"
      "v_mfma_f32_16x16x32_bf16 v[44:47], a[8:11], v[12:15], v[44:47]\n"
      "s_waitcnt vmcnt(6)\n"
      "v_cmp_ne_u32 vcc, v232, v150\n"
      "s_memrealtime s[68:69]\n"
      "s_cbranch_vccnz POLL_%=\n"
      "GOT_%=:\n"
      "v_readfirstlane_b32 s88, v233\n"
      "s_waitcnt lgkmcnt(0)\n"
      "s_add_u32 s88, s88, 60\n"
      "s_sub_u32 s67, s88, s68\n"
      "s_cmp_gt_u32 s67, 1000\n"
      "s_cbranch_scc1 LD_%=\n"
      "s_call_b64 s[84:85], SPIN_%=\n"
      "LD_%=:\n"
      "buffer_load_dwordx4 v[80:83], v127, s[28:31], s79 offen sc1\n"
      "buffer_load_dwordx4 v[64:67], v127, s[28:31], s80 offen offset:0 sc1\n"
      "buffer_load_dwordx4 v[68:71], v127, s[28:31], s80 offen offset:1024 sc1\n"
      "buffer_load_dwordx4 v[72:75], v127, s[28:31], s80 offen offset:2048 sc1\n"
      "buffer_load_dwordx4 v[76:79], v127, s[28:31], s80 offen offset:3072 sc1\n"
      "v_mfma_f32_16x16x32_bf16 v[48:51], a[12:15], v[0:3], v[48:51]\n"
      "v_mfma_f32_16x16x32_bf16 v[52:55], a[12:15], v[4:7], v[52:55]\n"
      "v_mfma_f32_16x16x32_bf16 v[56:59], a[12:15], v[8:11], v[56:59]\n"
      "ds_write_b128 v125, a[140:143] offset:3072\n"
      "v_mfma_f32_16x16x32_bf16 v[60:63], a[12:15], v[12:15], v[60:63]\n"
      "s_waitcnt lgkmcnt(15)\n"
      "v_mfma_f32_16x16x32_bf16 v[32:35], a[32:35], v[96:99], v[32:35]\n"
      "v_mfma_f32_16x16x32_bf16 v[48:51], a[36:39], v[96:99], v[48:51]\n"
      "v_mfma_f32_16x16x32_bf16 v[36:39], a[32:35], v[100:103], v[36:39]\n"
      "ds_write_b128 v125, a[156:159] offset:7168\n"
      "v_mfma_f32_16x16x32_bf16 v[52:55], a[36:39], v[100:103], v[52:55]\n"
      "v_mfma_f32_16x16x32_bf16 v[40:43], a[32:35], v[104:107], v[40:43]\n"
      "v_mfma_f32_16x16x32_bf16 v[56:59], a[36:39], v[104:107], v[56:59]\n"
      "v_mfma_f32_16x16x32_bf16 v[44:47], a[32:35], v[108:111], v[44:47]\n"
      "v_mfma_f32_16x16x32_bf16 v[60:63], a[36:39], v[108:111], v[60:63]\n"
      "s_waitcnt lgkmcnt(13)\n"
      "v_mfma_f32_16x16x32_bf16 v[32:35], a[40:43], a[64:67], v[32:35]\n"
      "v_mfma_f32_16x16x32_bf16 v[48:51], a[44:47], a[64:67], v[48:51]\n"
      "v_mfma_f32_16x16x32_bf16 v[36:39], a[40:43], a[68:71], v[36:39]\n"
      "v_mfma_f32_16x16x32_bf16 v[52:55], a[44:47], a[68:71], v[52:55]\n"
      "v_mfma_f32_16x16x32_bf16 v[40:43], a[40:43], a[72:75], v[40:43]\n"
      "v_mfma_f32_16x16x32_bf16 v[56:59], a[44:47], a[72:75], v[56:59]\n"
      "v_mfma_f32_16x16x32_bf16 v[44:47], a[40:43], a[76:79], v[44:47]\n"
      "v_mfma_f32_16x16x32_bf16 v[60:63], a[44:47], a[76:79], v[60:63]\n"
      "s_waitcnt lgkmcnt(8)\n"
      "v_mfma_f32_16x16x32_bf16 v[32:35], a[16:19], v[16:19], v[32:35]\n"
      "v_mfma_f32_16x16x32_bf16 v[48:51], a[20:23], v[16:19], v[48:51]\n"
      "v_mfma_f32_16x16x32_bf16 v[36:39], a[16:19], v[20:23], v[36:39]\n"
      "v_mfma_f32_16x16x32_bf16 v[52:55], a[20:23], v[20:23], v[52:55]\n"
      "v_mfma_f32_16x16x32_bf16 v[40:43], a[16:19], v[24:27], v[40:43]\n"
      "v_mfma_f32_16x16x32_bf16 v[56:59], a[20:23], v[24:27], v[56:59]\n"
      "v_mfma_f32_16x16x32_bf16 v[44:47], a[16:19], v[28:31], v[44:47]\n"
      "v_mfma_f32_16x16x32_bf16 v[60:63], a[20:23], v[28:31], v[60:63]\n"
      "s_waitcnt lgkmcnt(0)\n"
      "s_barrier\n"
      "ds_read_b128 v[160:163], v126 offset:0\n"
      "ds_read_b128 v[176:179], v126 offset:4096\n"
      "ds_read_b128 v[164:167], v126 offset:1024\n"
      "ds_read_b128 v[180:183], v126 offset:5120\n"
      "ds_read_b128 v[168:171], v126 offset:2048\n"
      "ds_read_b128 v[184:187], v126 offset:6144\n"
      "ds_read_b128 v[172:175], v126 offset:3072\n"
      "ds_read_b128 v[188:191], v126 offset:7168\n"
      "ds_read_b128 a[80:83], v126 offset:8192\n"
      "ds_read_b128 a[84:87], v126 offset:9216\n"
      "ds_read_b128 a[88:91], v126 offset:10240\n"
      "ds_read_b128 a[92:95], v126 offset:11264\n"
      "s_waitcnt lgkmcnt(10)\n"
      "v_pk_add_f32 v[32:33], v[32:33], v[160:161]\n"
      "v_pk_add_f32 v[34:35], v[34:35], v[162:163]\n"
      "v_pk_add_f32 v[48:49], v[48:49], v[176:177]\n"
      "v_pk_add_f32 v[50:51], v[50:51], v[178:179]\n"
      "s_waitcnt lgkmcnt(8)\n"
      "v_pk_add_f32 v[36:37], v[36:37], v[164:165]\n"
      "v_pk_add_f32 v[38:39], v[38:39], v[166:167]\n"
      "v_pk_add_f32 v[52:53], v[52:53], v[180:181]\n"
      "v_pk_add_f32 v[54:55], v[54:55], v[182:183]\n"
      "s_waitcnt lgkmcnt(6)\n"
      "v_pk_add_f32 v[40:41], v[40:41], v[168:169]\n"
      "v_pk_add_f32 v[42:43], v[42:43], v[170:171]\n"
      "v_pk_add_f32 v[56:57], v[56:57], v[184:185]\n"
      "v_pk_add_f32 v[58:59], v[58:59], v[186:187]\n"
      "s_waitcnt lgkmcnt(4)\n"
      "v_pk_add_f32 v[44:45], v[44:45], v[172:173]\n"
      "v_pk_add_f32 v[46:47], v[46:47], v[174:175]\n"
      "v_pk_add_f32 v[60:61], v[60:61], v[188:189]\n"
      "v_pk_add_f32 v[62:63], v[62:63], v[190:191]\n"
      "s_waitcnt lgkmcnt(0)\n"
      "v_mfma_f32_16x16x32_bf16 v[32:35], a[24:27], a[80:83], v[32:35]\n"
      "v_mfma_f32_16x16x32_bf16 v[48:51], a[28:31], a[80:83], v[48:51]\n"
      "v_mfma_f32_16x16x32_bf16 v[36:39], a[24:27], a[84:87], v[36:39]\n"
      "v_mfma_f32_16x16x32_bf16 v[52:55], a[28:31], a[84:87], v[52:55]\n"
      "v_mfma_f32_16x16x32_bf16 v[40:43], a[24:27], a[88:91], v[40:43]\n"
      "v_mfma_f32_16x16x32_bf16 v[56:59], a[28:31], a[88:91], v[56:59]\n"
      "v_mfma_f32_16x16x32_bf16 v[44:47], a[24:27], a[92:95], v[44:47]\n"
      "v_mfma_f32_16x16x32_bf16 v[60:63], a[28:31], a[92:95], v[60:63]\n"
      "CHK_%=:\n"
      "s_waitcnt vmcnt(4)\n"
      "v_add_f32 v224, v144, v80\n"
      "v_rcp_f32 v224, v224\n"
      "v_mad_u32_u24 v119, v80, s60, v151\n"
      "v_mad_u32_u24 v119, v81, s62, v119\n"
      "v_add3_u32 v118, v150, v80, v81\n"
      "s_waitcnt vmcnt(3)\n"
      "v_mfma_f32_16x16x32_bf16 v[160:163], v[86:89], v[64:67], v[32:35]\n"
      "v_mad_u32_u24 v119, v64, s60, v119\n"
      "v_mad_u32_u24 v119, v65, s62, v119\n"
      "v_add3_u32 v118, v118, v64, v65\n"
      "v_mov_b32 v226, v224\n"
      "v_mfma_f32_16x16x32_bf16 v[176:179], v[84:87], v[64:67], v[48:51]\n"
      "v_mad_u32_u24 v119, v66, s70, v119\n"
      "v_mad_u32_u24 v119, v67, s71, v119\n"
      "v_add3_u32 v118, v118, v66, v67\n"
      "v_permlane32_swap_b32 v224, v226\n"
      "s_waitcnt vmcnt(2)\n"
      "v_mfma_f32_16x16x32_bf16 v[164:167], v[86:89], v[68:71], v[36:39]\n"
      "v_mad_u32_u24 v119, v68, s60, v119\n"
      "v_mad_u32_u24 v119, v69, s62, v119\n"
      "v_add3_u32 v118, v118, v68, v69\n"
      "v_mov_b32 v225, v224\n"
      "v_mov_b32 v227, v226\n"
      "v_mfma_f32_16x16x32_bf16 v[180:183], v[84:87], v[68:71], v[52:55]\n"
      "v_mad_u32_u24 v119, v70, s70, v119\n"
      "v_mad_u32_u24 v119, v71, s71, v119\n"
      "v_add3_u32 v118, v118, v70, v71\n"
      "v_permlane16_swap_b32 v224, v225\n"
      "s_waitcnt vmcnt(1)\n"
      "v_mfma_f32_16x16x32_bf16 v[168:171], v[86:89], v[72:75], v[40:43]\n"
      "v_mad_u32_u24 v119, v72, s60, v119\n"
      "v_mad_u32_u24 v119, v73, s62, v119\n"
      "v_add3_u32 v118, v118, v72, v73\n"
      "v_permlane16_swap_b32 v226, v227\n"
      "v_mfma_f32_16x16x32_bf16 v[184:187], v[84:87], v[72:75], v[56:59]\n"
      "v_mad_u32_u24 v119, v74, s70, v119\n"
      "v_mad_u32_u24 v119, v75, s71, v119\n"
      "v_add3_u32 v118, v118, v74, v75\n"
      "s_waitcnt vmcnt(0)\n"
      "v_mfma_f32_16x16x32_bf16 v[172:175], v[86:89], v[76:79], v[44:47]\n"
      "v_mad_u32_u24 v119, v76, s60, v119\n"
      "v_mad_u32_u24 v119, v77, s62, v119\n"
      "v_add3_u32 v118, v118, v76, v77\n"
      "v_mfma_f32_16x16x32_bf16 v[188:191], v[84:87], v[76:79], v[60:63]\n"
      "v_mad_u32_u24 v119, v78, s70, v119\n"
      "v_mad_u32_u24 v119, v79, s71, v119\n"
      "v_add3_u32 v118, v118, v78, v79\n"
      "v_cmp_ne_u64 vcc, v[118:119], v[82:83]\n"
      "s_cbranch_vccnz RETRY_%=\n"
      "OK_%=:\n"
      "v_cmp_neq_f32 vcc, v81, v117\n"
      "s_cbranch_vccnz FIX_%=\n"
      "FR_%=:\n"
      "v_pk_mul_f32 v[160:161], v[160:161], v[224:225] op_sel:[0,0] op_sel_hi:[1,0]\n"
      "v_pk_mul_f32 v[162:163], v[162:163], v[224:225] op_sel:[0,0] op_sel_hi:[1,0]\n"
      "v_pk_mul_f32 v[176:177], v[176:177], v[224:225] op_sel:[0,0] op_sel_hi:[1,0]\n"
      "v_pk_mul_f32 v[178:179], v[178:179], v[224:225] op_sel:[0,0] op_sel_hi:[1,0]\n"
      "v_cvt_pk_bf16_f32 v128, v160, v161\n"
      "v_cvt_pk_bf16_f32 v130, v176, v177\n"
      "v_cvt_pk_bf16_f32 v129, v162, v163\n"
      "v_cvt_pk_bf16_f32 v131, v178, v179\n"
      "v_pk_mul_f32 v[164:165], v[164:165], v[224:225] op_sel:[0,1] op_sel_hi:[1,1]\n"
      "v_permlane16_swap_b32 v128, v130\n"
      "v_permlane16_swap_b32 v129, v131\n"
      "global_store_dwordx4 v156, v[128:131], s[16:17] offset:-4096 sc1\n"
      "v_pk_mul_f32 v[166:167], v[166:167], v[224:225] op_sel:[0,1] op_sel_hi:[1,1]\n"
      "v_pk_mul_f32 v[180:181], v[180:181], v[224:225] op_sel:[0,1] op_sel_hi:[1,1]\n"
      "v_pk_mul_f32 v[182:183], v[182:183], v[224:225] op_sel:[0,1] op_sel_hi:[1,1]\n"
      "v_cvt_pk_bf16_f32 v132, v164, v165\n"
      "v_cvt_pk_bf16_f32 v134, v180, v181\n"
      "v_cvt_pk_bf16_f32 v133, v166, v167\n"
      "v_cvt_pk_bf16_f32 v135, v182, v183\n"
      "v_pk_mul_f32 v[168:169], v[168:169], v[226:227] op_sel:[0,0] op_sel_hi:[1,0]\n"
      "v_permlane16_swap_b32 v132, v134\n"
      "v_permlane16_swap_b32 v133, v135\n"
      "global_store_dwordx4 v156, v[132:135], s[16:17] offset:0 sc1\n"
      "v_pk_mul_f32 v[170:171], v[170:171], v[226:227] op_sel:[0,0] op_sel_hi:[1,0]\n"
      "v_pk_mul_f32 v[184:185], v[184:185], v[226:227] op_sel:[0,0] op_sel_hi:[1,0]\n"
      "v_pk_mul_f32 v[186:187], v[186:187], v[226:227] op_sel:[0,0] op_sel_hi:[1,0]\n"
      "v_cvt_pk_bf16_f32 v136, v168, v169\n"
      "v_cvt_pk_bf16_f32 v138, v184, v185\n"
      "v_cvt_pk_bf16_f32 v137, v170, v171\n"
      "v_cvt_pk_bf16_f32 v139, v186, v187\n"
      "v_pk_mul_f32 v[172:173], v[172:173], v[226:227] op_sel:[0,1] op_sel_hi:[1,1]\n"
      "v_permlane16_swap_b32 v136, v138\n"
      "v_permlane16_swap_b32 v137, v139\n"
      "global_store_dwordx4 v155, v[136:139], s[16:17] offset:-4096 sc1\n"
      "v_pk_mul_f32 v[174:175], v[174:175], v[226:227] op_sel:[0,1] op_sel_hi:[1,1]\n"
      "v_pk_mul_f32 v[188:189], v[188:189], v[226:227] op_sel:[0,1] op_sel_hi:[1,1]\n"
      "v_pk_mul_f32 v[190:191], v[190:191], v[226:227] op_sel:[0,1] op_sel_hi:[1,1]\n"
      "v_cvt_pk_bf16_f32 v140, v172, v173\n"
      "v_cvt_pk_bf16_f32 v142, v188, v189\n"
      "v_cvt_pk_bf16_f32 v141, v174, v175\n"
      "v_cvt_pk_bf16_f32 v143, v190, v191\n"
      "v_and_b32 v157, 0xffffff80, v157\n"
      "v_permlane16_swap_b32 v140, v142\n"
      "v_permlane16_swap_b32 v141, v143\n"
      "global_store_dwordx4 v155, v[140:143], s[16:17] offset:0 sc1\n"
      "s_and_b32 s67, %[fo], 8\n"
      "s_or_b32 s67, s67, %[xw]\n"
      "s_cbranch_scc1 END_%=\n"
      "s_mov_b64 exec, 1\n"
      "global_store_dwordx2 v157, v[150:151], s[18:19] sc1\n"
      "s_mov_b64 exec, -1\n"
      "END_%=:\n"
      ".subsection 1\n"
      "POLL_%=:\n"
      "s_waitcnt vmcnt(0) lgkmcnt(0)\n"
      "v_cmp_ne_u32 vcc, v234, v150\n"
      "v_mov_b32 v233, v235\n"
      "s_memrealtime s[68:69]\n"
      "s_sub_u32 s63, s63, 1\n"
      "s_cbranch_vccz GOT_%=\n"
      "global_load_dwordx2 v[234:235], v158, s[18:19] sc1\n"
      "s_cbranch_scc0 POLL_%=\n"
      "s_branch GOT_%=\n"
      "RETRY_%=:\n"
      "s_sub_u32 s63, s63, 1\n"
      "s_cbranch_scc1 OK_%=\n"
      "s_sleep 4\n"
      "buffer_load_dwordx4 v[80:83], v127, s[28:31], s79 offen sc1\n"
      "buffer_load_dwordx4 v[64:67], v127, s[28:31], s80 offen offset:0 sc1\n"
      "buffer_load_dwordx4 v[68:71], v127, s[28:31], s80 offen offset:1024 sc1\n"
      "buffer_load_dwordx4 v[72:75], v127, s[28:31], s80 offen offset:2048 sc1\n"
      "buffer_load_dwordx4 v[76:79], v127, s[28:31], s80 offen offset:3072 sc1\n"
      "s_branch CHK_%=\n"
      "GW0_%=:\n"
      "s_call_b64 s[84:85], WAIT_%=\n"
      "s_branch GX0_%=\n"
      "GW1_%=:\n"
      "s_call_b64 s[84:85], WAIT_%=\n"
      "s_branch GX1_%=\n"
      "GW2_%=:\n"
      "s_call_b64 s[84:85], WAIT_%=\n"
      "s_branch GX2_%=\n"
      "GW3_%=:\n"
      "s_call_b64 s[84:85], WAIT_%=\n"
      "s_add_u32 s87, s87, 1\n"
      "s_branch GX3_%=\n"
      "GW4_%=:\n"
      "s_call_b64 s[84:85], WAIT_%=\n"
      "s_branch GX4_%=\n"
      "GW5_%=:\n"
      "s_call_b64 s[84:85], WAIT_%=\n"
      "s_branch GX5_%=\n"
      "GW6_%=:\n"
      "s_call_b64 s[84:85], WAIT_%=\n"
      "s_branch GX6_%=\n"
      "GW7_%=:\n"
      "s_call_b64 s[84:85], WAIT_%=\n"
      "s_branch GX7_%=\n"
      "GW8_%=:\n"
      "s_call_b64 s[84:85], WAIT_%=\n"
      "s_branch GX8_%=\n"
      "WAIT_%=:\n"
      "s_add_u32 s87, s87, 1\n"
      "s_bitcmp1_b32 s89, s87\n"
      "s_cbranch_scc1 H1_%=\n"
      "SPIN_%=:\n"
      "s_sub_u32 s67, s68, s88\n"
      "s_cmp_ge_i32 s67, 0\n"
      "s_cbranch_scc1 GO_%=\n"
      "s_sleep 1\n"
      "s_memrealtime s[68:69]\n"
      "s_waitcnt lgkmcnt(0)\n"
      "s_branch SPIN_%=\n"
      "GO_%=:\n"
      "s_setpc_b64 s[84:85]\n"
      "H1_%=:\n"
      "s_cmp_gt_i32 s93, 50\n"
      "s_cbranch_scc1 HS_%=\n"
      "s_bitcmp1_b32 0x2a80, s87\n"
      "s_cbranch_scc0 H2_%=\n"
      "s_add_u32 s67, s66, 400\n"
      "s_waitcnt vmcnt(8)\n"
      "v_sub_u32 v250, s67, v250\n"
      "v_cmp_gt_u32 vcc, 900, v250\n"
      "v_add_u32 v250, 0xa00, v250\n"
      "v_xor_b32 v251, 0x100, v251\n"
      "s_sub_u32 s69, s66, s71\n"
      "s_cmp_ge_u32 s87, 12\n"
      "s_cselect_b32 s101, 50, s101\n"
      "s_bcnt1_i32_b32 s74, vcc_lo\n"
      "s_bcnt1_i32_b32 s75, vcc_hi\n"
      "v_cmp_gt_u32 vcc, 0x800, v250\n"
      "s_add_u32 s67, s74, s75\n"
      "s_cmp_eq_u32 s87, 7\n"
      "s_cselect_b32 s76, 1, 0\n"
      "s_lshr_b32 s76, s67, s76\n"
      "s_cmp_le_u32 s76, 2\n"
      "s_cselect_b32 s86, 107, s98\n"
      "s_cmp_ge_u32 s76, 9\n"
      "s_cselect_b32 s86, 112, s86\n"
      "s_cmp_ge_u32 s67, 38\n"
      "s_cselect_b32 s76, 16, 33\n"
      "s_cmp_eq_u32 s87, 7\n"
      "s_cselect_b32 s76, s76, 15\n"
      "s_cbranch_vccnz H6_%=\n"
      "s_cmp_ge_u32 s74, s76\n"
      "s_cbranch_scc1 H3_%=\n"
      "s_cmp_eq_u32 s87, 13\n"
      "s_cbranch_scc1 H4_%=\n"
      "H8_%=:\n"
      "s_lshr_b32 s67, %[rep], 7\n"
      "s_cmp_eq_u32 s67, 1\n"
      "s_cbranch_scc0 SPIN_%=\n"
      "H5_%=:\n"
      "s_cmp_gt_i32 s93, 50\n"
      "s_cselect_b32 s75, -10, 0\n"
      "v_mov_b32 v127, s66\n"
      "s_add_u32 s74, s87, 2\n"
      "s_bfe_u32 s74, s74, 0x10001\n"
      "s_lshl_b32 s74, s74, 8\n"
      "s_add_u32 s74, s74, %[rep]\n"
      "RS_%=:\n"
      "s_sub_u32 s67, s68, s88\n"
      "s_cmp_ge_i32 s67, s75\n"
      "s_cbranch_scc1 RG_%=\n"
      "s_sleep 1\n"
      "s_memrealtime s[68:69]\n"
      "s_waitcnt lgkmcnt(0)\n"
      "s_branch RS_%=\n"
      "RG_%=:\n"
      "s_getreg_b32 s67, hwreg(HW_REG_IB_STS, 0, 4)\n"
      "s_cmp_le_u32 s67, 4\n"
      "s_cbranch_scc1 SPIN_%=\n"
      "buffer_store_dword v127, v124, s[28:31], s74 offen sc1\n"
      "s_branch SPIN_%=\n"
      "H6_%=:\n"
      "s_ff1_i32_b64 s67, vcc\n"
      "v_sub_u32 v250, s66, v250\n"
      "s_nop 3\n"
      "v_readlane_b32 s76, v250, s67\n"
      "s_add_u32 s76, s76, 2960\n"
      "s_and_b32 s76, s76, 31\n"
      "s_lshl_b32 s76, s76, 3\n"
      "s_lshl_b32 s89, 4, s87\n"
      "s_branch H7_%=\n"
      "H3_%=:\n"
      "s_mul_i32 s76, s69, 110\n"
      "s_ashr_i32 s76, s76, 10\n"
      "s_min_i32 s76, s76, 40\n"
      "s_add_u32 s76, s76, 84\n"
      "s_cmp_lt_u32 s75, 6\n"
      "s_cselect_b32 s76, 32, s76\n"
      "s_add_u32 s67, s66, 2000\n"
      "s_and_b32 s67, s67, -32\n"
      "s_lshr_b32 s75, s76, 3\n"
      "s_or_b32 s67, s67, s75\n"
      "v_mov_b32 v127, s67\n"
      "s_add_u32 s74, s87, 2\n"
      "s_bfe_u32 s74, s74, 0x10001\n"
      "s_lshl_b32 s74, s74, 8\n"
      "s_add_u32 s74, s74, %[rep]\n"
      "s_cmp_lt_u32 %[rep], 128\n"
      "s_cselect_b32 s89, 22, 20\n"
      "s_lshl_b32 s89, s89, s87\n"
      "s_sub_u32 s75, %[rep], 128\n"
      "s_cmp_lt_u32 s75, 32\n"
      "s_cbranch_scc0 H7_%=\n"
      "buffer_store_dword v127, v124, s[28:31], s74 offen sc1\n"
      "H7_%=:\n"
      "s_add_u32 s66, s66, s76\n"
      "s_mov_b32 s99, 13\n"
      "s_mov_b32 s100, 286\n"
      "s_add_u32 s93, s93, 99\n"
      "s_mov_b32 s86, 110\n"
      "s_mov_b32 s80, 0xc853212b\n"
      "s_mov_b32 s81, 0x842210\n"
      "s_sub_u32 s67, s65, s66\n"
      "s_cmp_lt_i32 s67, 0\n"
      "s_cselect_b32 s88, s66, s65\n"
      "s_mov_b32 s101, 200\n"
      "s_branch SPIN_%=\n"
      "H4_%=:\n"
      "s_mov_b32 s91, 0\n"
      "s_mov_b32 s89, 0\n"
      "v_mov_b32 v251, 0x4000000\n"
      "s_branch SPIN_%=\n"
      "HS_%=:\n"
      "s_bitcmp1_b32 s87, 0\n"
      "s_cbranch_scc0 H5_%=\n"
      "v_xor_b32 v251, 0x100, v251\n"
      "s_lshr_b32 s67, s89, s87\n"
      "s_cmp_gt_u32 s67, 1\n"
      "s_cbranch_scc1 SPIN_%=\n"
      "s_add_u32 s67, s66, 400\n"
      "s_waitcnt vmcnt(8)\n"
      "v_sub_u32 v250, s67, v250\n"
      "v_cmp_gt_u32 vcc, 900, v250\n"
      "s_nop 3\n"
      "s_bcnt1_i32_b32 s74, vcc_lo\n"
      "s_cmp_gt_u32 s74, 2\n"
      "s_cbranch_scc1 H4_%=\n"
      "s_mov_b32 s99, 21\n"
      "s_mov_b32 s100, 282\n"
      "s_mov_b32 s86, 107\n"
      "s_mov_b64 s[80:81], 0\n"
      "s_branch H4_%=\n"
      "H0_%=:\n"
      "buffer_load_dword v250, v251, s[28:31], 0 offen sc1\n"
      "s_branch SPIN_%=\n"
      "H9_%=:\n"
      "s_bitcmp1_b32 s87, 0\n"
      "s_cbranch_scc0 H5_%=\n"
      "v_xor_b32 v251, 0x100, v251\n"
      "s_cmp_eq_u32 s87, 5\n"
      "s_cbranch_scc1 H8_%=\n"
      "s_branch SPIN_%=\n"
      "H2_%=:\n"
      "s_cmp_gt_u32 s87, 2\n"
      "s_cbranch_scc1 H9_%=\n"
      "s_cmp_eq_u32 s87, 1\n"
      "s_cbranch_scc1 H0_%=\n"
      "s_waitcnt vmcnt(8)\n"
      "v_subrev_u32 v250, s71, v250\n"
      "v_min_u32 v250, 0x258, v250\n"
      "v_mbcnt_lo_u32_b32 v251, -1, 0\n"
      "v_mbcnt_hi_u32_b32 v251, -1, v251\n"
      "v_add_u32_dpp v250, v250, v250 row_shr:1 row_mask:0xf bank_mask:0xf bound_ctrl:0\n"
      "v_lshlrev_b32 v124, 23, v251\n"
      "s_nop 0\n"
      "v_add_u32_dpp v250, v250, v250 row_shr:2 row_mask:0xf bank_mask:0xf bound_ctrl:0\n"
      "v_lshlrev_b32 v251, 2, v251\n"
      "v_add_u32 v124, 0x6000, v124\n"
      "v_add_u32_dpp v250, v250, v250 row_shr:4 row_mask:0xf bank_mask:0xf bound_ctrl:0\n"
      "v_add_u32 v251, 0x6100, v251\n"
      "v_readlane_b32 s66, v250, 7\n"
      "s_nop 0\n"
      "s_lshr_b32 s66, s66, 3\n"
      "s_add_u32 s66, s66, s71\n"
      "s_add_u32 s66, s66, %[cph]\n"
      "s_add_u32 s71, s66, 545\n"
      "s_sub_u32 s67, s65, s66\n"
      "s_cmp_lt_i32 s67, 0\n"
      "s_cselect_b32 s88, s66, s65\n"
      "s_branch SPIN_%=\n"
      "FB0_%=:\n"
      "s_nop 7\n"
      "s_nop 7\n"
      "v_xor_b32 v118, 0x0, v242\n"
      "v_xor_b32 v119, 0x0, v243\n"
      "ds_read_b128 a[64:67], v118 offset:0\n"
      "ds_read_b128 a[68:71], v118 offset:4096\n"
      "ds_read_b128 a[72:75], v119 offset:0\n"
      "ds_read_b128 a[76:79], v119 offset:4096\n"
      "ds_read_b128 a[80:83], v118 offset:1024\n"
      "ds_read_b128 a[84:87], v118 offset:5120\n"
      "ds_read_b128 a[88:91], v119 offset:1024\n"
      "ds_read_b128 a[92:95], v119 offset:5120\n"
      "s_waitcnt lgkmcnt(0)\n"
      "v_mfma_f32_16x16x32_bf16 v[128:131], a[64:67], a[0:3], 0\n"
      "v_mfma_f32_16x16x32_bf16 v[136:139], a[64:67], a[4:7], 0\n"
      "v_mfma_f32_16x16x32_bf16 v[144:147], a[64:67], a[8:11], 0\n"
      "v_mfma_f32_16x16x32_bf16 v[152:155], a[64:67], a[12:15], 0\n"
      "v_mfma_f32_16x16x32_bf16 v[132:135], a[68:71], a[0:3], 0\n"
      "v_mfma_f32_16x16x32_bf16 v[140:143], a[68:71], a[4:7], 0\n"
      "v_mfma_f32_16x16x32_bf16 v[148:151], a[68:71], a[8:11], 0\n"
      "v_mfma_f32_16x16x32_bf16 v[156:159], a[68:71], a[12:15], 0\n"
      "v_mfma_f32_16x16x32_bf16 v[128:131], a[72:75], a[16:19], v[128:131]\n"
      "v_mfma_f32_16x16x32_bf16 v[136:139], a[72:75], a[20:23], v[136:139]\n"
      "v_mfma_f32_16x16x32_bf16 v[144:147], a[72:75], a[24:27], v[144:147]\n"
      "v_mfma_f32_16x16x32_bf16 v[152:155], a[72:75], a[28:31], v[152:155]\n"
      "v_mfma_f32_16x16x32_bf16 v[132:135], a[76:79], a[16:19], v[132:135]\n"
      "v_mfma_f32_16x16x32_bf16 v[140:143], a[76:79], a[20:23], v[140:143]\n"
      "v_mfma_f32_16x16x32_bf16 v[148:151], a[76:79], a[24:27], v[148:151]\n"
      "v_mfma_f32_16x16x32_bf16 v[156:159], a[76:79], a[28:31], v[156:159]\n"
      "v_mfma_f32_16x16x32_bf16 v[128:131], a[80:83], a[32:35], v[128:131]\n"
      "v_mfma_f32_16x16x32_bf16 v[136:139], a[80:83], a[36:39], v[136:139]\n"
      "v_mfma_f32_16x16x32_bf16 v[144:147], a[80:83], a[40:43], v[144:147]\n"
      "v_mfma_f32_16x16x32_bf16 v[152:155], a[80:83], a[44:47], v[152:155]\n"
      "v_mfma_f32_16x16x32_bf16 v[132:135], a[84:87], a[32:35], v[132:135]\n"
      "v_mfma_f32_16x16x32_bf16 v[140:143], a[84:87], a[36:39], v[140:143]\n"
      "v_mfma_f32_16x16x32_bf16 v[148:151], a[84:87], a[40:43], v[148:151]\n"
      "v_mfma_f32_16x16x32_bf16 v[156:159], a[84:87], a[44:47], v[156:159]\n"
      "v_mfma_f32_16x16x32_bf16 v[128:131], a[88:91], a[48:51], v[128:131]\n"
      "v_mfma_f32_16x16x32_bf16 v[136:139], a[88:91], a[52:55], v[136:139]\n"
      "v_mfma_f32_16x16x32_bf16 v[144:147], a[88:91], a[56:59], v[144:147]\n"
      "v_mfma_f32_16x16x32_bf16 v[152:155], a[88:91], a[60:63], v[152:155]\n"
      "v_mfma_f32_16x16x32_bf16 v[132:135], a[92:95], a[48:51], v[132:135]\n"
      "v_mfma_f32_16x16x32_bf16 v[140:143], a[92:95], a[52:55], v[140:143]\n"
      "v_mfma_f32_16x16x32_bf16 v[148:151], a[92:95], a[56:59], v[148:151]\n"
      "v_mfma_f32_16x16x32_bf16 v[156:159], a[92:95], a[60:63], v[156:159]\n"
      "s_nop 7\n"
      "v_max3_f32 v112, v128, v129, v130\n"
      "v_max3_f32 v113, v136, v137, v138\n"
      "v_max3_f32 v114, v144, v145, v146\n"
      "v_max3_f32 v115, v152, v153, v154\n"
      "v_max3_f32 v112, v112, v131, v132\n"
      "v_max3_f32 v113, v113, v139, v140\n"
      "v_max3_f32 v114, v114, v147, v148\n"
      "v_max3_f32 v115, v115, v155, v156\n"
      "v_max3_f32 v112, v112, v133, v134\n"
      "v_max3_f32 v113, v113, v141, v142\n"
      "v_max3_f32 v114, v114, v149, v150\n"
      "v_max3_f32 v115, v115, v157, v158\n"
      "v_max_f32 v112, v112, v135\n"
      "v_max_f32 v113, v113, v143\n"
      "v_max_f32 v114, v114, v151\n"
      "v_max_f32 v115, v115, v159\n"
      "v_mov_b32 v160, v112\n"
      "v_mov_b32 v161, v113\n"
      "v_mov_b32 v162, v114\n"
      "v_mov_b32 v163, v115\n"
      "v_permlane16_swap_b32 v112, v160\n"
      "v_permlane16_swap_b32 v113, v161\n"
      "v_permlane16_swap_b32 v114, v162\n"
      "v_permlane16_swap_b32 v115, v163\n"
      "v_max_f32 v112, v112, v160\n"
      "v_max_f32 v113, v113, v161\n"
      "v_max_f32 v114, v114, v162\n"
      "v_max_f32 v115, v115, v163\n"
      "v_mov_b32 v160, v112\n"
      "v_mov_b32 v161, v113\n"
      "v_mov_b32 v162, v114\n"
      "v_mov_b32 v163, v115\n"
      "v_permlane32_swap_b32 v112, v160\n"
      "v_permlane32_swap_b32 v113, v161\n"
      "v_permlane32_swap_b32 v114, v162\n"
      "v_permlane32_swap_b32 v115, v163\n"
      "v_max_f32 v112, v112, v160\n"
      "v_max_f32 v113, v113, v161\n"
      "v_max_f32 v114, v114, v162\n"
      "v_max_f32 v115, v115, v163\n"
      "v_sub_f32 v112, 0xc1800000, v112\n"
      "v_sub_f32 v113, 0xc1800000, v113\n"
      "v_sub_f32 v114, 0xc1800000, v114\n"
      "v_sub_f32 v115, 0xc1800000, v115\n"
      "v_floor_f32 v112, v112\n"
      "v_floor_f32 v113, v113\n"
      "v_floor_f32 v114, v114\n"
      "v_floor_f32 v115, v115\n"
      "v_min_f32 v112, v112, v224\n"
      "v_min_f32 v113, v113, v228\n"
      "v_min_f32 v114, v114, v232\n"
      "v_min_f32 v115, v115, v236\n"
      "v_sub_f32 v164, v112, v224\n"
      "v_sub_f32 v165, v113, v228\n"
      "v_sub_f32 v166, v114, v232\n"
      "v_sub_f32 v167, v115, v236\n"
      "v_mov_b32 v224, v112\n"
      "v_mov_b32 v228, v113\n"
      "v_mov_b32 v232, v114\n"
      "v_mov_b32 v236, v115\n"
      "v_exp_f32 v112, v164\n"
      "v_exp_f32 v113, v165\n"
      "v_exp_f32 v114, v166\n"
      "v_exp_f32 v115, v167\n"
      "v_mov_b32 v225, v224\n"
      "v_mov_b32 v229, v228\n"
      "v_mov_b32 v233, v232\n"
      "v_mov_b32 v237, v236\n"
      "v_mov_b64 v[226:227], v[224:225]\n"
      "v_mov_b64 v[230:231], v[228:229]\n"
      "v_mov_b64 v[234:235], v[232:233]\n"
      "v_mov_b64 v[238:239], v[236:237]\n"
      "s_call_b64 s[84:85], SCALE_%=\n"
      "v_pk_add_f32 v[128:129], v[128:129], v[224:225]\n"
      "v_pk_add_f32 v[130:131], v[130:131], v[224:225]\n"
      "v_pk_add_f32 v[132:133], v[132:133], v[224:225]\n"
      "v_pk_add_f32 v[134:135], v[134:135], v[224:225]\n"
      "v_pk_add_f32 v[136:137], v[136:137], v[228:229]\n"
      "v_pk_add_f32 v[138:139], v[138:139], v[228:229]\n"
      "v_pk_add_f32 v[140:141], v[140:141], v[228:229]\n"
      "v_pk_add_f32 v[142:143], v[142:143], v[228:229]\n"
      "v_pk_add_f32 v[144:145], v[144:145], v[232:233]\n"
      "v_pk_add_f32 v[146:147], v[146:147], v[232:233]\n"
      "v_pk_add_f32 v[148:149], v[148:149], v[232:233]\n"
      "v_pk_add_f32 v[150:151], v[150:151], v[232:233]\n"
      "v_pk_add_f32 v[152:153], v[152:153], v[236:237]\n"
      "v_pk_add_f32 v[154:155], v[154:155], v[236:237]\n"
      "v_pk_add_f32 v[156:157], v[156:157], v[236:237]\n"
      "v_pk_add_f32 v[158:159], v[158:159], v[236:237]\n"
      "v_exp_f32 v112, v128\n"
      "v_exp_f32 v113, v129\n"
      "v_exp_f32 v114, v130\n"
      "v_cvt_pk_bf16_f32 v128, v112, v113\n"
      "v_exp_f32 v115, v131\n"
      "v_exp_f32 v112, v132\n"
      "v_cvt_pk_bf16_f32 v129, v114, v115\n"
      "v_exp_f32 v113, v133\n"
      "v_exp_f32 v114, v134\n"
      "v_cvt_pk_bf16_f32 v130, v112, v113\n"
      "v_exp_f32 v115, v135\n"
      "v_exp_f32 v112, v136\n"
      "v_cvt_pk_bf16_f32 v131, v114, v115\n"
      "v_exp_f32 v113, v137\n"
      "v_exp_f32 v114, v138\n"
      "v_cvt_pk_bf16_f32 v132, v112, v113\n"
      "v_exp_f32 v115, v139\n"
      "v_exp_f32 v112, v140\n"
      "v_cvt_pk_bf16_f32 v133, v114, v115\n"
      "v_exp_f32 v113, v141\n"
      "v_exp_f32 v114, v142\n"
      "v_cvt_pk_bf16_f32 v134, v112, v113\n"
      "v_exp_f32 v115, v143\n"
      "v_exp_f32 v112, v144\n"
      "v_cvt_pk_bf16_f32 v135, v114, v115\n"
      "v_exp_f32 v113, v145\n"
      "v_exp_f32 v114, v146\n"
      "v_cvt_pk_bf16_f32 v136, v112, v113\n"
      "v_exp_f32 v115, v147\n"
      "v_exp_f32 v112, v148\n"
      "v_cvt_pk_bf16_f32 v137, v114, v115\n"
      "v_exp_f32 v113, v149\n"
      "v_exp_f32 v114, v150\n"
      "v_cvt_pk_bf16_f32 v138, v112, v113\n"
      "v_exp_f32 v115, v151\n"
      "v_exp_f32 v112, v152\n"
      "v_cvt_pk_bf16_f32 v139, v114, v115\n"
      "v_exp_f32 v113, v153\n"
      "v_exp_f32 v114, v154\n"
      "v_cvt_pk_bf16_f32 v140, v112, v113\n"
      "v_exp_f32 v115, v155\n"
      "v_exp_f32 v112, v156\n"
      "v_cvt_pk_bf16_f32 v141, v114, v115\n"
      "v_exp_f32 v113, v157\n"
      "v_exp_f32 v114, v158\n"
      "v_cvt_pk_bf16_f32 v142, v112, v113\n"
      "v_exp_f32 v115, v159\n"
      "s_nop 0\n"
      "v_cvt_pk_bf16_f32 v143, v114, v115\n"
      "ds_read_b128 a[64:67], v242 offset:32768\n"
      "ds_read_b128 a[68:71], v242 offset:36864\n"
      "ds_read_b128 a[72:75], v243 offset:32768\n"
      "ds_read_b128 a[76:79], v243 offset:36864\n"
      "ds_read_b128 a[80:83], v242 offset:33792\n"
      "ds_read_b128 a[84:87], v242 offset:37888\n"
      "ds_read_b128 a[88:91], v243 offset:33792\n"
      "ds_read_b128 a[92:95], v243 offset:37888\n"
      "s_waitcnt lgkmcnt(0)\n"
      "v_mfma_f32_16x16x32_bf16 v[160:163], a[64:67], a[0:3], v[224:227]\n"
      "v_mfma_f32_16x16x32_bf16 v[168:171], a[64:67], a[4:7], v[228:231]\n"
      "v_mfma_f32_16x16x32_bf16 v[176:179], a[64:67], a[8:11], v[232:235]\n"
      "v_mfma_f32_16x16x32_bf16 v[184:187], a[64:67], a[12:15], v[236:239]\n"
      "v_mfma_f32_16x16x32_bf16 v[164:167], a[68:71], a[0:3], v[224:227]\n"
      "v_mfma_f32_16x16x32_bf16 v[172:175], a[68:71], a[4:7], v[228:231]\n"
      "v_mfma_f32_16x16x32_bf16 v[180:183], a[68:71], a[8:11], v[232:235]\n"
      "v_mfma_f32_16x16x32_bf16 v[188:191], a[68:71], a[12:15], v[236:239]\n"
      "v_mfma_f32_16x16x32_bf16 v[160:163], a[72:75], a[16:19], v[160:163]\n"
      "v_mfma_f32_16x16x32_bf16 v[168:171], a[72:75], a[20:23], v[168:171]\n"
      "v_mfma_f32_16x16x32_bf16 v[176:179], a[72:75], a[24:27], v[176:179]\n"
      "v_mfma_f32_16x16x32_bf16 v[184:187], a[72:75], a[28:31], v[184:187]\n"
      "v_mfma_f32_16x16x32_bf16 v[164:167], a[76:79], a[16:19], v[164:167]\n"
      "v_mfma_f32_16x16x32_bf16 v[172:175], a[76:79], a[20:23], v[172:175]\n"
      "v_mfma_f32_16x16x32_bf16 v[180:183], a[76:79], a[24:27], v[180:183]\n"
      "v_mfma_f32_16x16x32_bf16 v[188:191], a[76:79], a[28:31], v[188:191]\n"
      "v_mfma_f32_16x16x32_bf16 v[160:163], a[80:83], a[32:35], v[160:163]\n"
      "v_mfma_f32_16x16x32_bf16 v[168:171], a[80:83], a[36:39], v[168:171]\n"
      "v_mfma_f32_16x16x32_bf16 v[176:179], a[80:83], a[40:43], v[176:179]\n"
      "v_mfma_f32_16x16x32_bf16 v[184:187], a[80:83], a[44:47], v[184:187]\n"
      "v_mfma_f32_16x16x32_bf16 v[164:167], a[84:87], a[32:35], v[164:167]\n"
      "v_mfma_f32_16x16x32_bf16 v[172:175], a[84:87], a[36:39], v[172:175]\n"
      "v_mfma_f32_16x16x32_bf16 v[180:183], a[84:87], a[40:43], v[180:183]\n"
      "v_mfma_f32_16x16x32_bf16 v[188:191], a[84:87], a[44:47], v[188:191]\n"
      "v_mfma_f32_16x16x32_bf16 v[160:163], a[88:91], a[48:51], v[160:163]\n"
      "v_mfma_f32_16x16x32_bf16 v[168:171], a[88:91], a[52:55], v[168:171]\n"
      "v_mfma_f32_16x16x32_bf16 v[176:179], a[88:91], a[56:59], v[176:179]\n"
      "v_mfma_f32_16x16x32_bf16 v[184:187], a[88:91], a[60:63], v[184:187]\n"
      "v_mfma_f32_16x16x32_bf16 v[164:167], a[92:95], a[48:51], v[164:167]\n"
      "v_mfma_f32_16x16x32_bf16 v[172:175], a[92:95], a[52:55], v[172:175]\n"
      "v_mfma_f32_16x16x32_bf16 v[180:183], a[92:95], a[56:59], v[180:183]\n"
      "v_mfma_f32_16x16x32_bf16 v[188:191], a[92:95], a[60:63], v[188:191]\n"
      "s_cmp_eq_u32 s64, 0\n"
      "s_cbranch_scc0 NRET0_0_%=\n"
      "v_mov_b64 v[192:193], v[128:129]\n"
      "v_mov_b64 v[194:195], v[130:131]\n"
      "v_mov_b64 v[196:197], v[132:133]\n"
      "v_mov_b64 v[198:199], v[134:135]\n"
      "v_mov_b64 v[200:201], v[136:137]\n"
      "v_mov_b64 v[202:203], v[138:139]\n"
      "v_mov_b64 v[204:205], v[140:141]\n"
      "v_mov_b64 v[206:207], v[142:143]\n"
      "s_nop 1\n"
      "s_branch RET0_0_%=\n"
      "NRET0_0_%=:\n"
      "s_cmp_eq_u32 s64, 1\n"
      "s_cbranch_scc0 NRET0_1_%=\n"
      "v_mov_b64 v[192:193], v[128:129]\n"
      "v_mov_b64 v[194:195], v[130:131]\n"
      "v_mov_b64 v[196:197], v[132:133]\n"
      "v_mov_b64 v[198:199], v[134:135]\n"
      "v_mov_b64 v[200:201], v[136:137]\n"
      "v_mov_b64 v[202:203], v[138:139]\n"
      "v_mov_b64 v[204:205], v[140:141]\n"
      "v_mov_b64 v[206:207], v[142:143]\n"
      "s_nop 1\n"
      "s_branch RET0_1_%=\n"
      "NRET0_1_%=:\n"
      "s_cmp_eq_u32 s64, 2\n"
      "s_cbranch_scc0 NRET0_2_%=\n"
      "v_mov_b64 v[192:193], v[128:129]\n"
      "v_mov_b64 v[194:195], v[130:131]\n"
      "v_mov_b64 v[196:197], v[132:133]\n"
      "v_mov_b64 v[198:199], v[134:135]\n"
      "v_mov_b64 v[200:201], v[136:137]\n"
      "v_mov_b64 v[202:203], v[138:139]\n"
      "v_mov_b64 v[204:205], v[140:141]\n"
      "v_mov_b64 v[206:207], v[142:143]\n"
      "s_nop 1\n"
      "s_branch RET0_2_%=\n"
      "NRET0_2_%=:\n"
      "s_cmp_eq_u32 s64, 3\n"
      "s_cbranch_scc0 NRET0_3_%=\n"
      "v_mov_b64 v[192:193], v[128:129]\n"
      "v_mov_b64 v[194:195], v[130:131]\n"
      "v_mov_b64 v[196:197], v[132:133]\n"
      "v_mov_b64 v[198:199], v[134:135]\n"
      "v_mov_b64 v[200:201], v[136:137]\n"
      "v_mov_b64 v[202:203], v[138:139]\n"
      "v_mov_b64 v[204:205], v[140:141]\n"
      "v_mov_b64 v[206:207], v[142:143]\n"
      "s_nop 1\n"
      "s_branch RET0_3_%=\n"
      "NRET0_3_%=:\n"
      "s_cmp_eq_u32 s64, 4\n"
      "s_cbranch_scc0 NRET0_4_%=\n"
      "v_mov_b64 v[192:193], v[128:129]\n"
      "v_mov_b64 v[194:195], v[130:131]\n"
      "v_mov_b64 v[196:197], v[132:133]\n"
      "v_mov_b64 v[198:199], v[134:135]\n"
      "v_mov_b64 v[200:201], v[136:137]\n"
      "v_mov_b64 v[202:203], v[138:139]\n"
      "v_mov_b64 v[204:205], v[140:141]\n"
      "v_mov_b64 v[206:207], v[142:143]\n"
      "s_nop 1\n"
      "s_branch RET0_4_%=\n"
      "NRET0_4_%=:\n"
      "s_cmp_eq_u32 s64, 5\n"
      "s_cbranch_scc0 NRET0_5_%=\n"
      "v_mov_b64 v[16:17], v[128:129]\n"
      "v_mov_b64 v[18:19], v[130:131]\n"
      "v_mov_b64 v[20:21], v[132:133]\n"
      "v_mov_b64 v[22:23], v[134:135]\n"
      "v_mov_b64 v[24:25], v[136:137]\n"
      "v_mov_b64 v[26:27], v[138:139]\n"
      "v_mov_b64 v[28:29], v[140:141]\n"
      "v_mov_b64 v[30:31], v[142:143]\n"
      "s_nop 1\n"
      "s_branch RET0_5_%=\n"
      "NRET0_5_%=:\n"
      "FB1_%=:\n"
      "s_add_u32 s84, s63, 1\n"
      "s_sub_u32 s84, s84, %[n1]\n"
      "s_or_b32 s84, s84, s64\n"
      "s_cbranch_scc0 RET1_0_%=\n"
      "s_nop 7\n"
      "s_nop 7\n"
      "v_xor_b32 v118, 0x10000, v242\n"
      "v_xor_b32 v119, 0x10000, v243\n"
      "ds_read_b128 a[64:67], v118 offset:32768\n"
      "ds_read_b128 a[68:71], v118 offset:36864\n"
      "ds_read_b128 a[72:75], v119 offset:32768\n"
      "ds_read_b128 a[76:79], v119 offset:36864\n"
      "ds_read_b128 a[80:83], v118 offset:33792\n"
      "ds_read_b128 a[84:87], v118 offset:37888\n"
      "ds_read_b128 a[88:91], v119 offset:33792\n"
      "ds_read_b128 a[92:95], v119 offset:37888\n"
      "s_waitcnt lgkmcnt(0)\n"
      "v_mfma_f32_16x16x32_bf16 v[160:163], a[64:67], a[0:3], 0\n"
      "v_mfma_f32_16x16x32_bf16 v[168:171], a[64:67], a[4:7], 0\n"
      "v_mfma_f32_16x16x32_bf16 v[176:179], a[64:67], a[8:11], 0\n"
      "v_mfma_f32_16x16x32_bf16 v[184:187], a[64:67], a[12:15], 0\n"
      "v_mfma_f32_16x16x32_bf16 v[164:167], a[68:71], a[0:3], 0\n"
      "v_mfma_f32_16x16x32_bf16 v[172:175], a[68:71], a[4:7], 0\n"
      "v_mfma_f32_16x16x32_bf16 v[180:183], a[68:71], a[8:11], 0\n"
      "v_mfma_f32_16x16x32_bf16 v[188:191], a[68:71], a[12:15], 0\n"
      "v_mfma_f32_16x16x32_bf16 v[160:163], a[72:75], a[16:19], v[160:163]\n"
      "v_mfma_f32_16x16x32_bf16 v[168:171], a[72:75], a[20:23], v[168:171]\n"
      "v_mfma_f32_16x16x32_bf16 v[176:179], a[72:75], a[24:27], v[176:179]\n"
      "v_mfma_f32_16x16x32_bf16 v[184:187], a[72:75], a[28:31], v[184:187]\n"
      "v_mfma_f32_16x16x32_bf16 v[164:167], a[76:79], a[16:19], v[164:167]\n"
      "v_mfma_f32_16x16x32_bf16 v[172:175], a[76:79], a[20:23], v[172:175]\n"
      "v_mfma_f32_16x16x32_bf16 v[180:183], a[76:79], a[24:27], v[180:183]\n"
      "v_mfma_f32_16x16x32_bf16 v[188:191], a[76:79], a[28:31], v[188:191]\n"
      "v_mfma_f32_16x16x32_bf16 v[160:163], a[80:83], a[32:35], v[160:163]\n"
      "v_mfma_f32_16x16x32_bf16 v[168:171], a[80:83], a[36:39], v[168:171]\n"
      "v_mfma_f32_16x16x32_bf16 v[176:179], a[80:83], a[40:43], v[176:179]\n"
      "v_mfma_f32_16x16x32_bf16 v[184:187], a[80:83], a[44:47], v[184:187]\n"
      "v_mfma_f32_16x16x32_bf16 v[164:167], a[84:87], a[32:35], v[164:167]\n"
      "v_mfma_f32_16x16x32_bf16 v[172:175], a[84:87], a[36:39], v[172:175]\n"
      "v_mfma_f32_16x16x32_bf16 v[180:183], a[84:87], a[40:43], v[180:183]\n"
      "v_mfma_f32_16x16x32_bf16 v[188:191], a[84:87], a[44:47], v[188:191]\n"
      "v_mfma_f32_16x16x32_bf16 v[160:163], a[88:91], a[48:51], v[160:163]\n"
      "v_mfma_f32_16x16x32_bf16 v[168:171], a[88:91], a[52:55], v[168:171]\n"
      "v_mfma_f32_16x16x32_bf16 v[176:179], a[88:91], a[56:59], v[176:179]\n"
      "v_mfma_f32_16x16x32_bf16 v[184:187], a[88:91], a[60:63], v[184:187]\n"
      "v_mfma_f32_16x16x32_bf16 v[164:167], a[92:95], a[48:51], v[164:167]\n"
      "v_mfma_f32_16x16x32_bf16 v[172:175], a[92:95], a[52:55], v[172:175]\n"
      "v_mfma_f32_16x16x32_bf16 v[180:183], a[92:95], a[56:59], v[180:183]\n"
      "v_mfma_f32_16x16x32_bf16 v[188:191], a[92:95], a[60:63], v[188:191]\n"
      "s_nop 7\n"
      "v_max3_f32 v112, v160, v161, v162\n"
      "v_max3_f32 v113, v168, v169, v170\n"
      "v_max3_f32 v114, v176, v177, v178\n"
      "v_max3_f32 v115, v184, v185, v186\n"
      "v_max3_f32 v112, v112, v163, v164\n"
      "v_max3_f32 v113, v113, v171, v172\n"
      "v_max3_f32 v114, v114, v179, v180\n"
      "v_max3_f32 v115, v115, v187, v188\n"
      "v_max3_f32 v112, v112, v165, v166\n"
      "v_max3_f32 v113, v113, v173, v174\n"
      "v_max3_f32 v114, v114, v181, v182\n"
      "v_max3_f32 v115, v115, v189, v190\n"
      "v_max_f32 v112, v112, v167\n"
      "v_max_f32 v113, v113, v175\n"
      "v_max_f32 v114, v114, v183\n"
      "v_max_f32 v115, v115, v191\n"
      "v_mov_b32 v128, v112\n"
      "v_mov_b32 v129, v113\n"
      "v_mov_b32 v130, v114\n"
      "v_mov_b32 v131, v115\n"
      "v_permlane16_swap_b32 v112, v128\n"
      "v_permlane16_swap_b32 v113, v129\n"
      "v_permlane16_swap_b32 v114, v130\n"
      "v_permlane16_swap_b32 v115, v131\n"
      "v_max_f32 v112, v112, v128\n"
      "v_max_f32 v113, v113, v129\n"
      "v_max_f32 v114, v114, v130\n"
      "v_max_f32 v115, v115, v131\n"
      "v_mov_b32 v128, v112\n"
      "v_mov_b32 v129, v113\n"
      "v_mov_b32 v130, v114\n"
      "v_mov_b32 v131, v115\n"
      "v_permlane32_swap_b32 v112, v128\n"
      "v_permlane32_swap_b32 v113, v129\n"
      "v_permlane32_swap_b32 v114, v130\n"
      "v_permlane32_swap_b32 v115, v131\n"
      "v_max_f32 v112, v112, v128\n"
      "v_max_f32 v113, v113, v129\n"
      "v_max_f32 v114, v114, v130\n"
      "v_max_f32 v115, v115, v131\n"
      "v_sub_f32 v112, 0xc1800000, v112\n"
      "v_sub_f32 v113, 0xc1800000, v113\n"
      "v_sub_f32 v114, 0xc1800000, v114\n"
      "v_sub_f32 v115, 0xc1800000, v115\n"
      "v_floor_f32 v112, v112\n"
      "v_floor_f32 v113, v113\n"
      "v_floor_f32 v114, v114\n"
      "v_floor_f32 v115, v115\n"
      "v_min_f32 v112, v112, v224\n"
      "v_min_f32 v113, v113, v228\n"
      "v_min_f32 v114, v114, v232\n"
      "v_min_f32 v115, v115, v236\n"
      "v_sub_f32 v132, v112, v224\n"
      "v_sub_f32 v133, v113, v228\n"
      "v_sub_f32 v134, v114, v232\n"
      "v_sub_f32 v135, v115, v236\n"
      "v_mov_b32 v224, v112\n"
      "v_mov_b32 v228, v113\n"
      "v_mov_b32 v232, v114\n"
      "v_mov_b32 v236, v115\n"
      "v_exp_f32 v112, v132\n"
      "v_exp_f32 v113, v133\n"
      "v_exp_f32 v114, v134\n"
      "v_exp_f32 v115, v135\n"
      "v_mov_b32 v225, v224\n"
      "v_mov_b32 v229, v228\n"
      "v_mov_b32 v233, v232\n"
      "v_mov_b32 v237, v236\n"
      "v_mov_b64 v[226:227], v[224:225]\n"
      "v_mov_b64 v[230:231], v[228:229]\n"
      "v_mov_b64 v[234:235], v[232:233]\n"
      "v_mov_b64 v[238:239], v[236:237]\n"
      "s_call_b64 s[84:85], SCALE_%=\n"
      "v_pk_add_f32 v[160:161], v[160:161], v[224:225]\n"
      "v_pk_add_f32 v[162:163], v[162:163], v[224:225]\n"
      "v_pk_add_f32 v[164:165], v[164:165], v[224:225]\n"
      "v_pk_add_f32 v[166:167], v[166:167], v[224:225]\n"
      "v_pk_add_f32 v[168:169], v[168:169], v[228:229]\n"
      "v_pk_add_f32 v[170:171], v[170:171], v[228:229]\n"
      "v_pk_add_f32 v[172:173], v[172:173], v[228:229]\n"
      "v_pk_add_f32 v[174:175], v[174:175], v[228:229]\n"
      "v_pk_add_f32 v[176:177], v[176:177], v[232:233]\n"
      "v_pk_add_f32 v[178:179], v[178:179], v[232:233]\n"
      "v_pk_add_f32 v[180:181], v[180:181], v[232:233]\n"
      "v_pk_add_f32 v[182:183], v[182:183], v[232:233]\n"
      "v_pk_add_f32 v[184:185], v[184:185], v[236:237]\n"
      "v_pk_add_f32 v[186:187], v[186:187], v[236:237]\n"
      "v_pk_add_f32 v[188:189], v[188:189], v[236:237]\n"
      "v_pk_add_f32 v[190:191], v[190:191], v[236:237]\n"
      "v_exp_f32 v112, v160\n"
      "v_exp_f32 v113, v161\n"
      "v_exp_f32 v114, v162\n"
      "v_cvt_pk_bf16_f32 v160, v112, v113\n"
      "v_exp_f32 v115, v163\n"
      "v_exp_f32 v112, v164\n"
      "v_cvt_pk_bf16_f32 v161, v114, v115\n"
      "v_exp_f32 v113, v165\n"
      "v_exp_f32 v114, v166\n"
      "v_cvt_pk_bf16_f32 v162, v112, v113\n"
      "v_exp_f32 v115, v167\n"
      "v_exp_f32 v112, v168\n"
      "v_cvt_pk_bf16_f32 v163, v114, v115\n"
      "v_exp_f32 v113, v169\n"
      "v_exp_f32 v114, v170\n"
      "v_cvt_pk_bf16_f32 v164, v112, v113\n"
      "v_exp_f32 v115, v171\n"
      "v_exp_f32 v112, v172\n"
      "v_cvt_pk_bf16_f32 v165, v114, v115\n"
      "v_exp_f32 v113, v173\n"
      "v_exp_f32 v114, v174\n"
      "v_cvt_pk_bf16_f32 v166, v112, v113\n"
      "v_exp_f32 v115, v175\n"
      "v_exp_f32 v112, v176\n"
      "v_cvt_pk_bf16_f32 v167, v114, v115\n"
      "v_exp_f32 v113, v177\n"
      "v_exp_f32 v114, v178\n"
      "v_cvt_pk_bf16_f32 v168, v112, v113\n"
      "v_exp_f32 v115, v179\n"
      "v_exp_f32 v112, v180\n"
      "v_cvt_pk_bf16_f32 v169, v114, v115\n"
      "v_exp_f32 v113, v181\n"
      "v_exp_f32 v114, v182\n"
      "v_cvt_pk_bf16_f32 v170, v112, v113\n"
      "v_exp_f32 v115, v183\n"
      "v_exp_f32 v112, v184\n"
      "v_cvt_pk_bf16_f32 v171, v114, v115\n"
      "v_exp_f32 v113, v185\n"
      "v_exp_f32 v114, v186\n"
      "v_cvt_pk_bf16_f32 v172, v112, v113\n"
      "v_exp_f32 v115, v187\n"
      "v_exp_f32 v112, v188\n"
      "v_cvt_pk_bf16_f32 v173, v114, v115\n"
      "v_exp_f32 v113, v189\n"
      "v_exp_f32 v114, v190\n"
      "v_cvt_pk_bf16_f32 v174, v112, v113\n"
      "v_exp_f32 v115, v191\n"
      "s_nop 0\n"
      "v_cvt_pk_bf16_f32 v175, v114, v115\n"
      "ds_read_b128 a[64:67], v242 offset:0\n"
      "ds_read_b128 a[68:71], v242 offset:4096\n"
      "ds_read_b128 a[72:75], v243 offset:0\n"
      "ds_read_b128 a[76:79], v243 offset:4096\n"
      "ds_read_b128 a[80:83], v242 offset:1024\n"
      "ds_read_b128 a[84:87], v242 offset:5120\n"
      "ds_read_b128 a[88:91], v243 offset:1024\n"
      "ds_read_b128 a[92:95], v243 offset:5120\n"
      "s_waitcnt lgkmcnt(0)\n"
      "v_mfma_f32_16x16x32_bf16 v[128:131], a[64:67], a[0:3], v[224:227]\n"
      "v_mfma_f32_16x16x32_bf16 v[136:139], a[64:67], a[4:7], v[228:231]\n"
      "v_mfma_f32_16x16x32_bf16 v[144:147], a[64:67], a[8:11], v[232:235]\n"
      "v_mfma_f32_16x16x32_bf16 v[152:155], a[64:67], a[12:15], v[236:239]\n"
      "v_mfma_f32_16x16x32_bf16 v[132:135], a[68:71], a[0:3], v[224:227]\n"
      "v_mfma_f32_16x16x32_bf16 v[140:143], a[68:71], a[4:7], v[228:231]\n"
      "v_mfma_f32_16x16x32_bf16 v[148:151], a[68:71], a[8:11], v[232:235]\n"
      "v_mfma_f32_16x16x32_bf16 v[156:159], a[68:71], a[12:15], v[236:239]\n"
      "v_mfma_f32_16x16x32_bf16 v[128:131], a[72:75], a[16:19], v[128:131]\n"
      "v_mfma_f32_16x16x32_bf16 v[136:139], a[72:75], a[20:23], v[136:139]\n"
      "v_mfma_f32_16x16x32_bf16 v[144:147], a[72:75], a[24:27], v[144:147]\n"
      "v_mfma_f32_16x16x32_bf16 v[152:155], a[72:75], a[28:31], v[152:155]\n"
      "v_mfma_f32_16x16x32_bf16 v[132:135], a[76:79], a[16:19], v[132:135]\n"
      "v_mfma_f32_16x16x32_bf16 v[140:143], a[76:79], a[20:23], v[140:143]\n"
      "v_mfma_f32_16x16x32_bf16 v[148:151], a[76:79], a[24:27], v[148:151]\n"
      "v_mfma_f32_16x16x32_bf16 v[156:159], a[76:79], a[28:31], v[156:159]\n"
      "v_mfma_f32_16x16x32_bf16 v[128:131], a[80:83], a[32:35], v[128:131]\n"
      "v_mfma_f32_16x16x32_bf16 v[136:139], a[80:83], a[36:39], v[136:139]\n"
      "v_mfma_f32_16x16x32_bf16 v[144:147], a[80:83], a[40:43], v[144:147]\n"
      "v_mfma_f32_16x16x32_bf16 v[152:155], a[80:83], a[44:47], v[152:155]\n"
      "v_mfma_f32_16x16x32_bf16 v[132:135], a[84:87], a[32:35], v[132:135]\n"
      "v_mfma_f32_16x16x32_bf16 v[140:143], a[84:87], a[36:39], v[140:143]\n"
      "v_mfma_f32_16x16x32_bf16 v[148:151], a[84:87], a[40:43], v[148:151]\n"
      "v_mfma_f32_16x16x32_bf16 v[156:159], a[84:87], a[44:47], v[156:159]\n"
      "v_mfma_f32_16x16x32_bf16 v[128:131], a[88:91], a[48:51], v[128:131]\n"
      "v_mfma_f32_16x16x32_bf16 v[136:139], a[88:91], a[52:55], v[136:139]\n"
      "v_mfma_f32_16x16x32_bf16 v[144:147], a[88:91], a[56:59], v[144:147]\n"
      "v_mfma_f32_16x16x32_bf16 v[152:155], a[88:91], a[60:63], v[152:155]\n"
      "v_mfma_f32_16x16x32_bf16 v[132:135], a[92:95], a[48:51], v[132:135]\n"
      "v_mfma_f32_16x16x32_bf16 v[140:143], a[92:95], a[52:55], v[140:143]\n"
      "v_mfma_f32_16x16x32_bf16 v[148:151], a[92:95], a[56:59], v[148:151]\n"
      "v_mfma_f32_16x16x32_bf16 v[156:159], a[92:95], a[60:63], v[156:159]\n"
      "s_cmp_eq_u32 s64, 0\n"
      "s_cbranch_scc0 NRET1_0_%=\n"
      "v_mov_b64 v[208:209], v[160:161]\n"
      "v_mov_b64 v[210:211], v[162:163]\n"
      "v_mov_b64 v[212:213], v[164:165]\n"
      "v_mov_b64 v[214:215], v[166:167]\n"
      "v_mov_b64 v[216:217], v[168:169]\n"
      "v_mov_b64 v[218:219], v[170:171]\n"
      "v_mov_b64 v[220:221], v[172:173]\n"
      "v_mov_b64 v[222:223], v[174:175]\n"
      "s_nop 1\n"
      "s_branch RET1_0_%=\n"
      "NRET1_0_%=:\n"
      "s_cmp_eq_u32 s64, 1\n"
      "s_cbranch_scc0 NRET1_1_%=\n"
      "v_mov_b64 v[208:209], v[160:161]\n"
      "v_mov_b64 v[210:211], v[162:163]\n"
      "v_mov_b64 v[212:213], v[164:165]\n"
      "v_mov_b64 v[214:215], v[166:167]\n"
      "v_mov_b64 v[216:217], v[168:169]\n"
      "v_mov_b64 v[218:219], v[170:171]\n"
      "v_mov_b64 v[220:221], v[172:173]\n"
      "v_mov_b64 v[222:223], v[174:175]\n"
      "s_nop 1\n"
      "s_branch RET1_1_%=\n"
      "NRET1_1_%=:\n"
      "s_cmp_eq_u32 s64, 2\n"
      "s_cbranch_scc0 NRET1_2_%=\n"
      "v_mov_b64 v[208:209], v[160:161]\n"
      "v_mov_b64 v[210:211], v[162:163]\n"
      "v_mov_b64 v[212:213], v[164:165]\n"
      "v_mov_b64 v[214:215], v[166:167]\n"
      "v_mov_b64 v[216:217], v[168:169]\n"
      "v_mov_b64 v[218:219], v[170:171]\n"
      "v_mov_b64 v[220:221], v[172:173]\n"
      "v_mov_b64 v[222:223], v[174:175]\n"
      "s_nop 1\n"
      "s_branch RET1_2_%=\n"
      "NRET1_2_%=:\n"
      "s_cmp_eq_u32 s64, 3\n"
      "s_cbranch_scc0 NRET1_3_%=\n"
      "v_mov_b64 v[208:209], v[160:161]\n"
      "v_mov_b64 v[210:211], v[162:163]\n"
      "v_mov_b64 v[212:213], v[164:165]\n"
      "v_mov_b64 v[214:215], v[166:167]\n"
      "v_mov_b64 v[216:217], v[168:169]\n"
      "v_mov_b64 v[218:219], v[170:171]\n"
      "v_mov_b64 v[220:221], v[172:173]\n"
      "v_mov_b64 v[222:223], v[174:175]\n"
      "s_nop 1\n"
      "s_branch RET1_3_%=\n"
      "NRET1_3_%=:\n"
      "s_cmp_eq_u32 s64, 4\n"
      "s_cbranch_scc0 NRET1_4_%=\n"
      "v_mov_b64 v[208:209], v[160:161]\n"
      "v_mov_b64 v[210:211], v[162:163]\n"
      "v_mov_b64 v[212:213], v[164:165]\n"
      "v_mov_b64 v[214:215], v[166:167]\n"
      "v_mov_b64 v[216:217], v[168:169]\n"
      "v_mov_b64 v[218:219], v[170:171]\n"
      "v_mov_b64 v[220:221], v[172:173]\n"
      "v_mov_b64 v[222:223], v[174:175]\n"
      "s_nop 1\n"
      "s_branch RET1_4_%=\n"
      "NRET1_4_%=:\n"
      "s_cmp_eq_u32 s64, 5\n"
      "s_cbranch_scc0 NRET1_5_%=\n"
      "v_mov_b64 v[0:1], v[160:161]\n"
      "v_mov_b64 v[2:3], v[162:163]\n"
      "v_mov_b64 v[4:5], v[164:165]\n"
      "v_mov_b64 v[6:7], v[166:167]\n"
      "v_mov_b64 v[8:9], v[168:169]\n"
      "v_mov_b64 v[10:11], v[170:171]\n"
      "v_mov_b64 v[12:13], v[172:173]\n"
      "v_mov_b64 v[14:15], v[174:175]\n"
      "s_nop 1\n"
      "s_branch RET1_5_%=\n"
      "NRET1_5_%=:\n"
      "s_cmp_eq_u32 s64, 6\n"
      "s_cbranch_scc0 NRET1_6_%=\n"
      "v_mov_b64 v[96:97], v[160:161]\n"
      "v_mov_b64 v[98:99], v[162:163]\n"
      "v_mov_b64 v[100:101], v[164:165]\n"
      "v_mov_b64 v[102:103], v[166:167]\n"
      "v_mov_b64 v[104:105], v[168:169]\n"
      "v_mov_b64 v[106:107], v[170:171]\n"
      "v_mov_b64 v[108:109], v[172:173]\n"
      "v_mov_b64 v[110:111], v[174:175]\n"
      "v_mov_b32 v117, v224\n"
      "s_nop 1\n"
      "v_mov_b32_dpp v117, v228 quad_perm:[0,1,2,3] row_mask:0x2 bank_mask:0xf\n"
      "s_nop 1\n"
      "v_mov_b32_dpp v117, v232 quad_perm:[0,1,2,3] row_mask:0x4 bank_mask:0xf\n"
      "s_nop 1\n"
      "v_mov_b32_dpp v117, v236 quad_perm:[0,1,2,3] row_mask:0x8 bank_mask:0xf\n"
      "v_mfma_f32_16x16x32_bf16 a[228:231], a[196:199], v[96:99], a[192:195]\n"
      "v_mfma_f32_16x16x32_bf16 a[228:231], a[200:203], v[100:103], a[228:231]\n"
      "v_mfma_f32_16x16x32_bf16 a[228:231], a[204:207], v[104:107], a[228:231]\n"
      "ds_write_b128 v125, a[160:163] offset:0\n"
      "ds_write_b128 v125, a[164:167] offset:1024\n"
      "ds_write_b128 v125, a[168:171] offset:2048\n"
      "ds_write_b128 v125, a[172:175] offset:3072\n"
      "ds_write_b128 v125, a[176:179] offset:4096\n"
      "ds_write_b128 v125, a[180:183] offset:5120\n"
      "ds_write_b128 v125, a[184:187] offset:6144\n"
      "ds_write_b128 v125, a[188:191] offset:7168\n"
      "ds_write_b128 v125, v[96:99] offset:8192\n"
      "ds_write_b128 v125, v[100:103] offset:9216\n"
      "ds_write_b128 v125, v[104:107] offset:10240\n"
      "ds_write_b32 v125, v117 offset:12292\n"
      "s_nop 1\n"
      "s_branch RET1_6_%=\n"
      "NRET1_6_%=:\n"
      "SCALE_%=:\n"
      "v_pk_mul_f32 v[32:33], v[32:33], v[112:113] op_sel:[0,0] op_sel_hi:[1,0]\n"
      "v_pk_mul_f32 v[34:35], v[34:35], v[112:113] op_sel:[0,0] op_sel_hi:[1,0]\n"
      "v_pk_mul_f32 v[36:37], v[36:37], v[112:113] op_sel:[0,1] op_sel_hi:[1,1]\n"
      "v_pk_mul_f32 v[38:39], v[38:39], v[112:113] op_sel:[0,1] op_sel_hi:[1,1]\n"
      "v_pk_mul_f32 v[40:41], v[40:41], v[114:115] op_sel:[0,0] op_sel_hi:[1,0]\n"
      "v_pk_mul_f32 v[42:43], v[42:43], v[114:115] op_sel:[0,0] op_sel_hi:[1,0]\n"
      "v_pk_mul_f32 v[44:45], v[44:45], v[114:115] op_sel:[0,1] op_sel_hi:[1,1]\n"
      "v_pk_mul_f32 v[46:47], v[46:47], v[114:115] op_sel:[0,1] op_sel_hi:[1,1]\n"
      "v_pk_mul_f32 v[48:49], v[48:49], v[112:113] op_sel:[0,0] op_sel_hi:[1,0]\n"
      "v_pk_mul_f32 v[50:51], v[50:51], v[112:113] op_sel:[0,0] op_sel_hi:[1,0]\n"
      "v_pk_mul_f32 v[52:53], v[52:53], v[112:113] op_sel:[0,1] op_sel_hi:[1,1]\n"
      "v_pk_mul_f32 v[54:55], v[54:55], v[112:113] op_sel:[0,1] op_sel_hi:[1,1]\n"
      "v_pk_mul_f32 v[56:57], v[56:57], v[114:115] op_sel:[0,0] op_sel_hi:[1,0]\n"
      "v_pk_mul_f32 v[58:59], v[58:59], v[114:115] op_sel:[0,0] op_sel_hi:[1,0]\n"
      "v_pk_mul_f32 v[60:61], v[60:61], v[114:115] op_sel:[0,1] op_sel_hi:[1,1]\n"
      "v_pk_mul_f32 v[62:63], v[62:63], v[114:115] op_sel:[0,1] op_sel_hi:[1,1]\n"
      "v_pk_mul_f32 v[64:65], v[64:65], v[112:113] op_sel:[0,0] op_sel_hi:[1,0]\n"
      "v_pk_mul_f32 v[66:67], v[66:67], v[112:113] op_sel:[0,0] op_sel_hi:[1,0]\n"
      "v_pk_mul_f32 v[68:69], v[68:69], v[112:113] op_sel:[0,1] op_sel_hi:[1,1]\n"
      "v_pk_mul_f32 v[70:71], v[70:71], v[112:113] op_sel:[0,1] op_sel_hi:[1,1]\n"
      "v_pk_mul_f32 v[72:73], v[72:73], v[114:115] op_sel:[0,0] op_sel_hi:[1,0]\n"
      "v_pk_mul_f32 v[74:75], v[74:75], v[114:115] op_sel:[0,0] op_sel_hi:[1,0]\n"
      "v_pk_mul_f32 v[76:77], v[76:77], v[114:115] op_sel:[0,1] op_sel_hi:[1,1]\n"
      "v_pk_mul_f32 v[78:79], v[78:79], v[114:115] op_sel:[0,1] op_sel_hi:[1,1]\n"
      "v_pk_mul_f32 v[80:81], v[80:81], v[112:113] op_sel:[0,0] op_sel_hi:[1,0]\n"
      "v_pk_mul_f32 v[82:83], v[82:83], v[112:113] op_sel:[0,0] op_sel_hi:[1,0]\n"
      "v_pk_mul_f32 v[84:85], v[84:85], v[112:113] op_sel:[0,1] op_sel_hi:[1,1]\n"
      "v_pk_mul_f32 v[86:87], v[86:87], v[112:113] op_sel:[0,1] op_sel_hi:[1,1]\n"
      "v_pk_mul_f32 v[88:89], v[88:89], v[114:115] op_sel:[0,0] op_sel_hi:[1,0]\n"
      "v_pk_mul_f32 v[90:91], v[90:91], v[114:115] op_sel:[0,0] op_sel_hi:[1,0]\n"
      "v_pk_mul_f32 v[92:93], v[92:93], v[114:115] op_sel:[0,1] op_sel_hi:[1,1]\n"
      "v_pk_mul_f32 v[94:95], v[94:95], v[114:115] op_sel:[0,1] op_sel_hi:[1,1]\n"
      "v_accvgpr_read_b32 v118, a128\n"
      "v_accvgpr_read_b32 v119, a129\n"
      "v_pk_mul_f32 v[118:119], v[118:119], v[112:113] op_sel:[0,0] op_sel_hi:[1,0]\n"
      "v_accvgpr_write_b32 a128, v118\n"
      "v_accvgpr_write_b32 a129, v119\n"
      "v_accvgpr_read_b32 v118, a130\n"
      "v_accvgpr_read_b32 v119, a131\n"
      "v_pk_mul_f32 v[118:119], v[118:119], v[112:113] op_sel:[0,0] op_sel_hi:[1,0]\n"
      "v_accvgpr_write_b32 a130, v118\n"
      "v_accvgpr_write_b32 a131, v119\n"
      "v_accvgpr_read_b32 v118, a132\n"
      "v_accvgpr_read_b32 v119, a133\n"
      "v_pk_mul_f32 v[118:119], v[118:119], v[112:113] op_sel:[0,1] op_sel_hi:[1,1]\n"
      "v_accvgpr_write_b32 a132, v118\n"
      "v_accvgpr_write_b32 a133, v119\n"
      "v_accvgpr_read_b32 v118, a134\n"
      "v_accvgpr_read_b32 v119, a135\n"
      "v_pk_mul_f32 v[118:119], v[118:119], v[112:113] op_sel:[0,1] op_sel_hi:[1,1]\n"
      "v_accvgpr_write_b32 a134, v118\n"
      "v_accvgpr_write_b32 a135, v119\n"
      "v_accvgpr_read_b32 v118, a136\n"
      "v_accvgpr_read_b32 v119, a137\n"
      "v_pk_mul_f32 v[118:119], v[118:119], v[114:115] op_sel:[0,0] op_sel_hi:[1,0]\n"
      "v_accvgpr_write_b32 a136, v118\n"
      "v_accvgpr_write_b32 a137, v119\n"
      "v_accvgpr_read_b32 v118, a138\n"
      "v_accvgpr_read_b32 v119, a139\n"
      "v_pk_mul_f32 v[118:119], v[118:119], v[114:115] op_sel:[0,0] op_sel_hi:[1,0]\n"
      "v_accvgpr_write_b32 a138, v118\n"
      "v_accvgpr_write_b32 a139, v119\n"
      "v_accvgpr_read_b32 v118, a140\n"
      "v_accvgpr_read_b32 v119, a141\n"
      "v_pk_mul_f32 v[118:119], v[118:119], v[114:115] op_sel:[0,1] op_sel_hi:[1,1]\n"
      "v_accvgpr_write_b32 a140, v118\n"
      "v_accvgpr_write_b32 a141, v119\n"
      "v_accvgpr_read_b32 v118, a142\n"
      "v_accvgpr_read_b32 v119, a143\n"
      "v_pk_mul_f32 v[118:119], v[118:119], v[114:115] op_sel:[0,1] op_sel_hi:[1,1]\n"
      "v_accvgpr_write_b32 a142, v118\n"
      "v_accvgpr_write_b32 a143, v119\n"
      "v_accvgpr_read_b32 v118, a144\n"
      "v_accvgpr_read_b32 v119, a145\n"
      "v_pk_mul_f32 v[118:119], v[118:119], v[112:113] op_sel:[0,0] op_sel_hi:[1,0]\n"
      "v_accvgpr_write_b32 a144, v118\n"
      "v_accvgpr_write_b32 a145, v119\n"
      "v_accvgpr_read_b32 v118, a146\n"
      "v_accvgpr_read_b32 v119, a147\n"
      "v_pk_mul_f32 v[118:119], v[118:119], v[112:113] op_sel:[0,0] op_sel_hi:[1,0]\n"
      "v_accvgpr_write_b32 a146, v118\n"
      "v_accvgpr_write_b32 a147, v119\n"
      "v_accvgpr_read_b32 v118, a148\n"
      "v_accvgpr_read_b32 v119, a149\n"
      "v_pk_mul_f32 v[118:119], v[118:119], v[112:113] op_sel:[0,1] op_sel_hi:[1,1]\n"
      "v_accvgpr_write_b32 a148, v118\n"
      "v_accvgpr_write_b32 a149, v119\n"
      "v_accvgpr_read_b32 v118, a150\n"
      "v_accvgpr_read_b32 v119, a151\n"
      "v_pk_mul_f32 v[118:119], v[118:119], v[112:113] op_sel:[0,1] op_sel_hi:[1,1]\n"
      "v_accvgpr_write_b32 a150, v118\n"
      "v_accvgpr_write_b32 a151, v119\n"
      "v_accvgpr_read_b32 v118, a152\n"
      "v_accvgpr_read_b32 v119, a153\n"
      "v_pk_mul_f32 v[118:119], v[118:119], v[114:115] op_sel:[0,0] op_sel_hi:[1,0]\n"
      "v_accvgpr_write_b32 a152, v118\n"
      "v_accvgpr_write_b32 a153, v119\n"
      "v_accvgpr_read_b32 v118, a154\n"
      "v_accvgpr_read_b32 v119, a155\n"
      "v_pk_mul_f32 v[118:119], v[118:119], v[114:115] op_sel:[0,0] op_sel_hi:[1,0]\n"
      "v_accvgpr_write_b32 a154, v118\n"
      "v_accvgpr_write_b32 a155, v119\n"
      "v_accvgpr_read_b32 v118, a156\n"
      "v_accvgpr_read_b32 v119, a157\n"
      "v_pk_mul_f32 v[118:119], v[118:119], v[114:115] op_sel:[0,1] op_sel_hi:[1,1]\n"
      "v_accvgpr_write_b32 a156, v118\n"
      "v_accvgpr_write_b32 a157, v119\n"
      "v_accvgpr_read_b32 v118, a158\n"
      "v_accvgpr_read_b32 v119, a159\n"
      "v_pk_mul_f32 v[118:119], v[118:119], v[114:115] op_sel:[0,1] op_sel_hi:[1,1]\n"
      "v_accvgpr_write_b32 a158, v118\n"
      "v_accvgpr_write_b32 a159, v119\n"
      "v_accvgpr_read_b32 v118, a160\n"
      "v_accvgpr_read_b32 v119, a161\n"
      "v_pk_mul_f32 v[118:119], v[118:119], v[112:113] op_sel:[0,0] op_sel_hi:[1,0]\n"
      "v_accvgpr_write_b32 a160, v118\n"
      "v_accvgpr_write_b32 a161, v119\n"
      "v_accvgpr_read_b32 v118, a162\n"
      "v_accvgpr_read_b32 v119, a163\n"
      "v_pk_mul_f32 v[118:119], v[118:119], v[112:113] op_sel:[0,0] op_sel_hi:[1,0]\n"
      "v_accvgpr_write_b32 a162, v118\n"
      "v_accvgpr_write_b32 a163, v119\n"
      "v_accvgpr_read_b32 v118, a164\n"
      "v_accvgpr_read_b32 v119, a165\n"
      "v_pk_mul_f32 v[118:119], v[118:119], v[112:113] op_sel:[0,1] op_sel_hi:[1,1]\n"
      "v_accvgpr_write_b32 a164, v118\n"
      "v_accvgpr_write_b32 a165, v119\n"
      "v_accvgpr_read_b32 v118, a166\n"
      "v_accvgpr_read_b32 v119, a167\n"
      "v_pk_mul_f32 v[118:119], v[118:119], v[112:113] op_sel:[0,1] op_sel_hi:[1,1]\n"
      "v_accvgpr_write_b32 a166, v118\n"
      "v_accvgpr_write_b32 a167, v119\n"
      "v_accvgpr_read_b32 v118, a168\n"
      "v_accvgpr_read_b32 v119, a169\n"
      "v_pk_mul_f32 v[118:119], v[118:119], v[114:115] op_sel:[0,0] op_sel_hi:[1,0]\n"
      "v_accvgpr_write_b32 a168, v118\n"
      "v_accvgpr_write_b32 a169, v119\n"
      "v_accvgpr_read_b32 v118, a170\n"
      "v_accvgpr_read_b32 v119, a171\n"
      "v_pk_mul_f32 v[118:119], v[118:119], v[114:115] op_sel:[0,0] op_sel_hi:[1,0]\n"
      "v_accvgpr_write_b32 a170, v118\n"
      "v_accvgpr_write_b32 a171, v119\n"
      "v_accvgpr_read_b32 v118, a172\n"
      "v_accvgpr_read_b32 v119, a173\n"
      "v_pk_mul_f32 v[118:119], v[118:119], v[114:115] op_sel:[0,1] op_sel_hi:[1,1]\n"
      "v_accvgpr_write_b32 a172, v118\n"
      "v_accvgpr_write_b32 a173, v119\n"
      "v_accvgpr_read_b32 v118, a174\n"
      "v_accvgpr_read_b32 v119, a175\n"
      "v_pk_mul_f32 v[118:119], v[118:119], v[114:115] op_sel:[0,1] op_sel_hi:[1,1]\n"
      "v_accvgpr_write_b32 a174, v118\n"
      "v_accvgpr_write_b32 a175, v119\n"
      "v_accvgpr_read_b32 v118, a176\n"
      "v_accvgpr_read_b32 v119, a177\n"
      "v_pk_mul_f32 v[118:119], v[118:119], v[112:113] op_sel:[0,0] op_sel_hi:[1,0]\n"
      "v_accvgpr_write_b32 a176, v118\n"
      "v_accvgpr_write_b32 a177, v119\n"
      "v_accvgpr_read_b32 v118, a178\n"
      "v_accvgpr_read_b32 v119, a179\n"
      "v_pk_mul_f32 v[118:119], v[118:119], v[112:113] op_sel:[0,0] op_sel_hi:[1,0]\n"
      "v_accvgpr_write_b32 a178, v118\n"
      "v_accvgpr_write_b32 a179, v119\n"
      "v_accvgpr_read_b32 v118, a180\n"
      "v_accvgpr_read_b32 v119, a181\n"
      "v_pk_mul_f32 v[118:119], v[118:119], v[112:113] op_sel:[0,1] op_sel_hi:[1,1]\n"
      "v_accvgpr_write_b32 a180, v118\n"
      "v_accvgpr_write_b32 a181, v119\n"
      "v_accvgpr_read_b32 v118, a182\n"
      "v_accvgpr_read_b32 v119, a183\n"
      "v_pk_mul_f32 v[118:119], v[118:119], v[112:113] op_sel:[0,1] op_sel_hi:[1,1]\n"
      "v_accvgpr_write_b32 a182, v118\n"
      "v_accvgpr_write_b32 a183, v119\n"
      "v_accvgpr_read_b32 v118, a184\n"
      "v_accvgpr_read_b32 v119, a185\n"
      "v_pk_mul_f32 v[118:119], v[118:119], v[114:115] op_sel:[0,0] op_sel_hi:[1,0]\n"
      "v_accvgpr_write_b32 a184, v118\n"
      "v_accvgpr_write_b32 a185, v119\n"
      "v_accvgpr_read_b32 v118, a186\n"
      "v_accvgpr_read_b32 v119, a187\n"
      "v_pk_mul_f32 v[118:119], v[118:119], v[114:115] op_sel:[0,0] op_sel_hi:[1,0]\n"
      "v_accvgpr_write_b32 a186, v118\n"
      "v_accvgpr_write_b32 a187, v119\n"
      "v_accvgpr_read_b32 v118, a188\n"
      "v_accvgpr_read_b32 v119, a189\n"
      "v_pk_mul_f32 v[118:119], v[118:119], v[114:115] op_sel:[0,1] op_sel_hi:[1,1]\n"
      "v_accvgpr_write_b32 a188, v118\n"
      "v_accvgpr_write_b32 a189, v119\n"
      "v_accvgpr_read_b32 v118, a190\n"
      "v_accvgpr_read_b32 v119, a191\n"
      "v_pk_mul_f32 v[118:119], v[118:119], v[114:115] op_sel:[0,1] op_sel_hi:[1,1]\n"
      "v_accvgpr_write_b32 a190, v118\n"
      "v_accvgpr_write_b32 a191, v119\n"
      "v_mov_b32 v118, v112\n"
      "s_nop 1\n"
      "v_mov_b32_dpp v118, v113 quad_perm:[0,1,2,3] row_mask:0x2 bank_mask:0xf\n"
      "s_nop 1\n"
      "v_mov_b32_dpp v118, v114 quad_perm:[0,1,2,3] row_mask:0x4 bank_mask:0xf\n"
      "s_nop 1\n"
      "v_mov_b32_dpp v118, v115 quad_perm:[0,1,2,3] row_mask:0x8 bank_mask:0xf\n"
      "v_accvgpr_read_b32 v119, a192\n"
      "v_mul_f32 v118, v118, v119\n"
      "v_accvgpr_write_b32 a192, v118\n"
      "v_lshlrev_b32 v118, 16, v208\n"
      "v_and_b32 v119, 0xffff0000, v208\n"
      "v_pk_mul_f32 v[118:119], v[118:119], v[112:113] op_sel:[0,0] op_sel_hi:[1,0]\n"
      "v_cvt_pk_bf16_f32 v208, v118, v119\n"
      "v_lshlrev_b32 v118, 16, v209\n"
      "v_and_b32 v119, 0xffff0000, v209\n"
      "v_pk_mul_f32 v[118:119], v[118:119], v[112:113] op_sel:[0,0] op_sel_hi:[1,0]\n"
      "v_cvt_pk_bf16_f32 v209, v118, v119\n"
      "v_lshlrev_b32 v118, 16, v210\n"
      "v_and_b32 v119, 0xffff0000, v210\n"
      "v_pk_mul_f32 v[118:119], v[118:119], v[112:113] op_sel:[0,0] op_sel_hi:[1,0]\n"
      "v_cvt_pk_bf16_f32 v210, v118, v119\n"
      "v_lshlrev_b32 v118, 16, v211\n"
      "v_and_b32 v119, 0xffff0000, v211\n"
      "v_pk_mul_f32 v[118:119], v[118:119], v[112:113] op_sel:[0,0] op_sel_hi:[1,0]\n"
      "v_cvt_pk_bf16_f32 v211, v118, v119\n"
      "v_lshlrev_b32 v118, 16, v212\n"
      "v_and_b32 v119, 0xffff0000, v212\n"
      "v_pk_mul_f32 v[118:119], v[118:119], v[112:113] op_sel:[0,1] op_sel_hi:[1,1]\n"
      "v_cvt_pk_bf16_f32 v212, v118, v119\n"
      "v_lshlrev_b32 v118, 16, v213\n"
      "v_and_b32 v119, 0xffff0000, v213\n"
      "v_pk_mul_f32 v[118:119], v[118:119], v[112:113] op_sel:[0,1] op_sel_hi:[1,1]\n"
      "v_cvt_pk_bf16_f32 v213, v118, v119\n"
      "v_lshlrev_b32 v118, 16, v214\n"
      "v_and_b32 v119, 0xffff0000, v214\n"
      "v_pk_mul_f32 v[118:119], v[118:119], v[112:113] op_sel:[0,1] op_sel_hi:[1,1]\n"
      "v_cvt_pk_bf16_f32 v214, v118, v119\n"
      "v_lshlrev_b32 v118, 16, v215\n"
      "v_and_b32 v119, 0xffff0000, v215\n"
      "v_pk_mul_f32 v[118:119], v[118:119], v[112:113] op_sel:[0,1] op_sel_hi:[1,1]\n"
      "v_cvt_pk_bf16_f32 v215, v118, v119\n"
      "v_lshlrev_b32 v118, 16, v216\n"
      "v_and_b32 v119, 0xffff0000, v216\n"
      "v_pk_mul_f32 v[118:119], v[118:119], v[114:115] op_sel:[0,0] op_sel_hi:[1,0]\n"
      "v_cvt_pk_bf16_f32 v216, v118, v119\n"
      "v_lshlrev_b32 v118, 16, v217\n"
      "v_and_b32 v119, 0xffff0000, v217\n"
      "v_pk_mul_f32 v[118:119], v[118:119], v[114:115] op_sel:[0,0] op_sel_hi:[1,0]\n"
      "v_cvt_pk_bf16_f32 v217, v118, v119\n"
      "v_lshlrev_b32 v118, 16, v218\n"
      "v_and_b32 v119, 0xffff0000, v218\n"
      "v_pk_mul_f32 v[118:119], v[118:119], v[114:115] op_sel:[0,0] op_sel_hi:[1,0]\n"
      "v_cvt_pk_bf16_f32 v218, v118, v119\n"
      "v_lshlrev_b32 v118, 16, v219\n"
      "v_and_b32 v119, 0xffff0000, v219\n"
      "v_pk_mul_f32 v[118:119], v[118:119], v[114:115] op_sel:[0,0] op_sel_hi:[1,0]\n"
      "v_cvt_pk_bf16_f32 v219, v118, v119\n"
      "v_lshlrev_b32 v118, 16, v220\n"
      "v_and_b32 v119, 0xffff0000, v220\n"
      "v_pk_mul_f32 v[118:119], v[118:119], v[114:115] op_sel:[0,1] op_sel_hi:[1,1]\n"
      "v_cvt_pk_bf16_f32 v220, v118, v119\n"
      "v_lshlrev_b32 v118, 16, v221\n"
      "v_and_b32 v119, 0xffff0000, v221\n"
      "v_pk_mul_f32 v[118:119], v[118:119], v[114:115] op_sel:[0,1] op_sel_hi:[1,1]\n"
      "v_cvt_pk_bf16_f32 v221, v118, v119\n"
      "v_lshlrev_b32 v118, 16, v222\n"
      "v_and_b32 v119, 0xffff0000, v222\n"
      "v_pk_mul_f32 v[118:119], v[118:119], v[114:115] op_sel:[0,1] op_sel_hi:[1,1]\n"
      "v_cvt_pk_bf16_f32 v222, v118, v119\n"
      "v_lshlrev_b32 v118, 16, v223\n"
      "v_and_b32 v119, 0xffff0000, v223\n"
      "v_pk_mul_f32 v[118:119], v[118:119], v[114:115] op_sel:[0,1] op_sel_hi:[1,1]\n"
      "v_cvt_pk_bf16_f32 v223, v118, v119\n"
      "v_lshlrev_b32 v118, 16, v192\n"
      "v_and_b32 v119, 0xffff0000, v192\n"
      "v_pk_mul_f32 v[118:119], v[118:119], v[112:113] op_sel:[0,0] op_sel_hi:[1,0]\n"
      "v_cvt_pk_bf16_f32 v192, v118, v119\n"
      "v_lshlrev_b32 v118, 16, v193\n"
      "v_and_b32 v119, 0xffff0000, v193\n"
      "v_pk_mul_f32 v[118:119], v[118:119], v[112:113] op_sel:[0,0] op_sel_hi:[1,0]\n"
      "v_cvt_pk_bf16_f32 v193, v118, v119\n"
      "v_lshlrev_b32 v118, 16, v194\n"
      "v_and_b32 v119, 0xffff0000, v194\n"
      "v_pk_mul_f32 v[118:119], v[118:119], v[112:113] op_sel:[0,0] op_sel_hi:[1,0]\n"
      "v_cvt_pk_bf16_f32 v194, v118, v119\n"
      "v_lshlrev_b32 v118, 16, v195\n"
      "v_and_b32 v119, 0xffff0000, v195\n"
      "v_pk_mul_f32 v[118:119], v[118:119], v[112:113] op_sel:[0,0] op_sel_hi:[1,0]\n"
      "v_cvt_pk_bf16_f32 v195, v118, v119\n"
      "v_lshlrev_b32 v118, 16, v196\n"
      "v_and_b32 v119, 0xffff0000, v196\n"
      "v_pk_mul_f32 v[118:119], v[118:119], v[112:113] op_sel:[0,1] op_sel_hi:[1,1]\n"
      "v_cvt_pk_bf16_f32 v196, v118, v119\n"
      "v_lshlrev_b32 v118, 16, v197\n"
      "v_and_b32 v119, 0xffff0000, v197\n"
      "v_pk_mul_f32 v[118:119], v[118:119], v[112:113] op_sel:[0,1] op_sel_hi:[1,1]\n"
      "v_cvt_pk_bf16_f32 v197, v118, v119\n"
      "v_lshlrev_b32 v118, 16, v198\n"
      "v_and_b32 v119, 0xffff0000, v198\n"
      "v_pk_mul_f32 v[118:119], v[118:119], v[112:113] op_sel:[0,1] op_sel_hi:[1,1]\n"
      "v_cvt_pk_bf16_f32 v198, v118, v119\n"
      "v_lshlrev_b32 v118, 16, v199\n"
      "v_and_b32 v119, 0xffff0000, v199\n"
      "v_pk_mul_f32 v[118:119], v[118:119], v[112:113] op_sel:[0,1] op_sel_hi:[1,1]\n"
      "v_cvt_pk_bf16_f32 v199, v118, v119\n"
      "v_lshlrev_b32 v118, 16, v200\n"
      "v_and_b32 v119, 0xffff0000, v200\n"
      "v_pk_mul_f32 v[118:119], v[118:119], v[114:115] op_sel:[0,0] op_sel_hi:[1,0]\n"
      "v_cvt_pk_bf16_f32 v200, v118, v119\n"
      "v_lshlrev_b32 v118, 16, v201\n"
      "v_and_b32 v119, 0xffff0000, v201\n"
      "v_pk_mul_f32 v[118:119], v[118:119], v[114:115] op_sel:[0,0] op_sel_hi:[1,0]\n"
      "v_cvt_pk_bf16_f32 v201, v118, v119\n"
      "v_lshlrev_b32 v118, 16, v202\n"
      "v_and_b32 v119, 0xffff0000, v202\n"
      "v_pk_mul_f32 v[118:119], v[118:119], v[114:115] op_sel:[0,0] op_sel_hi:[1,0]\n"
      "v_cvt_pk_bf16_f32 v202, v118, v119\n"
      "v_lshlrev_b32 v118, 16, v203\n"
      "v_and_b32 v119, 0xffff0000, v203\n"
      "v_pk_mul_f32 v[118:119], v[118:119], v[114:115] op_sel:[0,0] op_sel_hi:[1,0]\n"
      "v_cvt_pk_bf16_f32 v203, v118, v119\n"
      "v_lshlrev_b32 v118, 16, v204\n"
      "v_and_b32 v119, 0xffff0000, v204\n"
      "v_pk_mul_f32 v[118:119], v[118:119], v[114:115] op_sel:[0,1] op_sel_hi:[1,1]\n"
      "v_cvt_pk_bf16_f32 v204, v118, v119\n"
      "v_lshlrev_b32 v118, 16, v205\n"
      "v_and_b32 v119, 0xffff0000, v205\n"
      "v_pk_mul_f32 v[118:119], v[118:119], v[114:115] op_sel:[0,1] op_sel_hi:[1,1]\n"
      "v_cvt_pk_bf16_f32 v205, v118, v119\n"
      "v_lshlrev_b32 v118, 16, v206\n"
      "v_and_b32 v119, 0xffff0000, v206\n"
      "v_pk_mul_f32 v[118:119], v[118:119], v[114:115] op_sel:[0,1] op_sel_hi:[1,1]\n"
      "v_cvt_pk_bf16_f32 v206, v118, v119\n"
      "v_lshlrev_b32 v118, 16, v207\n"
      "v_and_b32 v119, 0xffff0000, v207\n"
      "v_pk_mul_f32 v[118:119], v[118:119], v[114:115] op_sel:[0,1] op_sel_hi:[1,1]\n"
      "v_cvt_pk_bf16_f32 v207, v118, v119\n"
      "v_lshlrev_b32 v118, 16, v0\n"
      "v_and_b32 v119, 0xffff0000, v0\n"
      "v_pk_mul_f32 v[118:119], v[118:119], v[112:113] op_sel:[0,0] op_sel_hi:[1,0]\n"
      "v_cvt_pk_bf16_f32 v0, v118, v119\n"
      "v_lshlrev_b32 v118, 16, v1\n"
      "v_and_b32 v119, 0xffff0000, v1\n"
      "v_pk_mul_f32 v[118:119], v[118:119], v[112:113] op_sel:[0,0] op_sel_hi:[1,0]\n"
      "v_cvt_pk_bf16_f32 v1, v118, v119\n"
      "v_lshlrev_b32 v118, 16, v2\n"
      "v_and_b32 v119, 0xffff0000, v2\n"
      "v_pk_mul_f32 v[118:119], v[118:119], v[112:113] op_sel:[0,0] op_sel_hi:[1,0]\n"
      "v_cvt_pk_bf16_f32 v2, v118, v119\n"
      "v_lshlrev_b32 v118, 16, v3\n"
      "v_and_b32 v119, 0xffff0000, v3\n"
      "v_pk_mul_f32 v[118:119], v[118:119], v[112:113] op_sel:[0,0] op_sel_hi:[1,0]\n"
      "v_cvt_pk_bf16_f32 v3, v118, v119\n"
      "v_lshlrev_b32 v118, 16, v4\n"
      "v_and_b32 v119, 0xffff0000, v4\n"
      "v_pk_mul_f32 v[118:119], v[118:119], v[112:113] op_sel:[0,1] op_sel_hi:[1,1]\n"
      "v_cvt_pk_bf16_f32 v4, v118, v119\n"
      "v_lshlrev_b32 v118, 16, v5\n"
      "v_and_b32 v119, 0xffff0000, v5\n"
      "v_pk_mul_f32 v[118:119], v[118:119], v[112:113] op_sel:[0,1] op_sel_hi:[1,1]\n"
      "v_cvt_pk_bf16_f32 v5, v118, v119\n"
      "v_lshlrev_b32 v118, 16, v6\n"
      "v_and_b32 v119, 0xffff0000, v6\n"
      "v_pk_mul_f32 v[118:119], v[118:119], v[112:113] op_sel:[0,1] op_sel_hi:[1,1]\n"
      "v_cvt_pk_bf16_f32 v6, v118, v119\n"
      "v_lshlrev_b32 v118, 16, v7\n"
      "v_and_b32 v119, 0xffff0000, v7\n"
      "v_pk_mul_f32 v[118:119], v[118:119], v[112:113] op_sel:[0,1] op_sel_hi:[1,1]\n"
      "v_cvt_pk_bf16_f32 v7, v118, v119\n"
      "v_lshlrev_b32 v118, 16, v8\n"
      "v_and_b32 v119, 0xffff0000, v8\n"
      "v_pk_mul_f32 v[118:119], v[118:119], v[114:115] op_sel:[0,0] op_sel_hi:[1,0]\n"
      "v_cvt_pk_bf16_f32 v8, v118, v119\n"
      "v_lshlrev_b32 v118, 16, v9\n"
      "v_and_b32 v119, 0xffff0000, v9\n"
      "v_pk_mul_f32 v[118:119], v[118:119], v[114:115] op_sel:[0,0] op_sel_hi:[1,0]\n"
      "v_cvt_pk_bf16_f32 v9, v118, v119\n"
      "v_lshlrev_b32 v118, 16, v10\n"
      "v_and_b32 v119, 0xffff0000, v10\n"
      "v_pk_mul_f32 v[118:119], v[118:119], v[114:115] op_sel:[0,0] op_sel_hi:[1,0]\n"
      "v_cvt_pk_bf16_f32 v10, v118, v119\n"
      "v_lshlrev_b32 v118, 16, v11\n"
      "v_and_b32 v119, 0xffff0000, v11\n"
      "v_pk_mul_f32 v[118:119], v[118:119], v[114:115] op_sel:[0,0] op_sel_hi:[1,0]\n"
      "v_cvt_pk_bf16_f32 v11, v118, v119\n"
      "v_lshlrev_b32 v118, 16, v12\n"
      "v_and_b32 v119, 0xffff0000, v12\n"
      "v_pk_mul_f32 v[118:119], v[118:119], v[114:115] op_sel:[0,1] op_sel_hi:[1,1]\n"
      "v_cvt_pk_bf16_f32 v12, v118, v119\n"
      "v_lshlrev_b32 v118, 16, v13\n"
      "v_and_b32 v119, 0xffff0000, v13\n"
      "v_pk_mul_f32 v[118:119], v[118:119], v[114:115] op_sel:[0,1] op_sel_hi:[1,1]\n"
      "v_cvt_pk_bf16_f32 v13, v118, v119\n"
      "v_lshlrev_b32 v118, 16, v14\n"
      "v_and_b32 v119, 0xffff0000, v14\n"
      "v_pk_mul_f32 v[118:119], v[118:119], v[114:115] op_sel:[0,1] op_sel_hi:[1,1]\n"
      "v_cvt_pk_bf16_f32 v14, v118, v119\n"
      "v_lshlrev_b32 v118, 16, v15\n"
      "v_and_b32 v119, 0xffff0000, v15\n"
      "v_pk_mul_f32 v[118:119], v[118:119], v[114:115] op_sel:[0,1] op_sel_hi:[1,1]\n"
      "v_cvt_pk_bf16_f32 v15, v118, v119\n"
      "v_lshlrev_b32 v118, 16, v16\n"
      "v_and_b32 v119, 0xffff0000, v16\n"
      "v_pk_mul_f32 v[118:119], v[118:119], v[112:113] op_sel:[0,0] op_sel_hi:[1,0]\n"
      "v_cvt_pk_bf16_f32 v16, v118, v119\n"
      "v_lshlrev_b32 v118, 16, v17\n"
      "v_and_b32 v119, 0xffff0000, v17\n"
      "v_pk_mul_f32 v[118:119], v[118:119], v[112:113] op_sel:[0,0] op_sel_hi:[1,0]\n"
      "v_cvt_pk_bf16_f32 v17, v118, v119\n"
      "v_lshlrev_b32 v118, 16, v18\n"
      "v_and_b32 v119, 0xffff0000, v18\n"
      "v_pk_mul_f32 v[118:119], v[118:119], v[112:113] op_sel:[0,0] op_sel_hi:[1,0]\n"
      "v_cvt_pk_bf16_f32 v18, v118, v119\n"
      "v_lshlrev_b32 v118, 16, v19\n"
      "v_and_b32 v119, 0xffff0000, v19\n"
      "v_pk_mul_f32 v[118:119], v[118:119], v[112:113] op_sel:[0,0] op_sel_hi:[1,0]\n"
      "v_cvt_pk_bf16_f32 v19, v118, v119\n"
      "v_lshlrev_b32 v118, 16, v20\n"
      "v_and_b32 v119, 0xffff0000, v20\n"
      "v_pk_mul_f32 v[118:119], v[118:119], v[112:113] op_sel:[0,1] op_sel_hi:[1,1]\n"
      "v_cvt_pk_bf16_f32 v20, v118, v119\n"
      "v_lshlrev_b32 v118, 16, v21\n"
      "v_and_b32 v119, 0xffff0000, v21\n"
      "v_pk_mul_f32 v[118:119], v[118:119], v[112:113] op_sel:[0,1] op_sel_hi:[1,1]\n"
      "v_cvt_pk_bf16_f32 v21, v118, v119\n"
      "v_lshlrev_b32 v118, 16, v22\n"
      "v_and_b32 v119, 0xffff0000, v22\n"
      "v_pk_mul_f32 v[118:119], v[118:119], v[112:113] op_sel:[0,1] op_sel_hi:[1,1]\n"
      "v_cvt_pk_bf16_f32 v22, v118, v119\n"
      "v_lshlrev_b32 v118, 16, v23\n"
      "v_and_b32 v119, 0xffff0000, v23\n"
      "v_pk_mul_f32 v[118:119], v[118:119], v[112:113] op_sel:[0,1] op_sel_hi:[1,1]\n"
      "v_cvt_pk_bf16_f32 v23, v118, v119\n"
      "v_lshlrev_b32 v118, 16, v24\n"
      "v_and_b32 v119, 0xffff0000, v24\n"
      "v_pk_mul_f32 v[118:119], v[118:119], v[114:115] op_sel:[0,0] op_sel_hi:[1,0]\n"
      "v_cvt_pk_bf16_f32 v24, v118, v119\n"
      "v_lshlrev_b32 v118, 16, v25\n"
      "v_and_b32 v119, 0xffff0000, v25\n"
      "v_pk_mul_f32 v[118:119], v[118:119], v[114:115] op_sel:[0,0] op_sel_hi:[1,0]\n"
      "v_cvt_pk_bf16_f32 v25, v118, v119\n"
      "v_lshlrev_b32 v118, 16, v26\n"
      "v_and_b32 v119, 0xffff0000, v26\n"
      "v_pk_mul_f32 v[118:119], v[118:119], v[114:115] op_sel:[0,0] op_sel_hi:[1,0]\n"
      "v_cvt_pk_bf16_f32 v26, v118, v119\n"
      "v_lshlrev_b32 v118, 16, v27\n"
      "v_and_b32 v119, 0xffff0000, v27\n"
      "v_pk_mul_f32 v[118:119], v[118:119], v[114:115] op_sel:[0,0] op_sel_hi:[1,0]\n"
      "v_cvt_pk_bf16_f32 v27, v118, v119\n"
      "v_lshlrev_b32 v118, 16, v28\n"
      "v_and_b32 v119, 0xffff0000, v28\n"
      "v_pk_mul_f32 v[118:119], v[118:119], v[114:115] op_sel:[0,1] op_sel_hi:[1,1]\n"
      "v_cvt_pk_bf16_f32 v28, v118, v119\n"
      "v_lshlrev_b32 v118, 16, v29\n"
      "v_and_b32 v119, 0xffff0000, v29\n"
      "v_pk_mul_f32 v[118:119], v[118:119], v[114:115] op_sel:[0,1] op_sel_hi:[1,1]\n"
      "v_cvt_pk_bf16_f32 v29, v118, v119\n"
      "v_lshlrev_b32 v118, 16, v30\n"
      "v_and_b32 v119, 0xffff0000, v30\n"
      "v_pk_mul_f32 v[118:119], v[118:119], v[114:115] op_sel:[0,1] op_sel_hi:[1,1]\n"
      "v_cvt_pk_bf16_f32 v30, v118, v119\n"
      "v_lshlrev_b32 v118, 16, v31\n"
      "v_and_b32 v119, 0xffff0000, v31\n"
      "v_pk_mul_f32 v[118:119], v[118:119], v[114:115] op_sel:[0,1] op_sel_hi:[1,1]\n"
      "v_cvt_pk_bf16_f32 v31, v118, v119\n"
      "v_lshlrev_b32 v118, 16, v96\n"
      "v_and_b32 v119, 0xffff0000, v96\n"
      "v_pk_mul_f32 v[118:119], v[118:119], v[112:113] op_sel:[0,0] op_sel_hi:[1,0]\n"
      "v_cvt_pk_bf16_f32 v96, v118, v119\n"
      "v_lshlrev_b32 v118, 16, v97\n"
      "v_and_b32 v119, 0xffff0000, v97\n"
      "v_pk_mul_f32 v[118:119], v[118:119], v[112:113] op_sel:[0,0] op_sel_hi:[1,0]\n"
      "v_cvt_pk_bf16_f32 v97, v118, v119\n"
      "v_lshlrev_b32 v118, 16, v98\n"
      "v_and_b32 v119, 0xffff0000, v98\n"
      "v_pk_mul_f32 v[118:119], v[118:119], v[112:113] op_sel:[0,0] op_sel_hi:[1,0]\n"
      "v_cvt_pk_bf16_f32 v98, v118, v119\n"
      "v_lshlrev_b32 v118, 16, v99\n"
      "v_and_b32 v119, 0xffff0000, v99\n"
      "v_pk_mul_f32 v[118:119], v[118:119], v[112:113] op_sel:[0,0] op_sel_hi:[1,0]\n"
      "v_cvt_pk_bf16_f32 v99, v118, v119\n"
      "v_lshlrev_b32 v118, 16, v100\n"
      "v_and_b32 v119, 0xffff0000, v100\n"
      "v_pk_mul_f32 v[118:119], v[118:119], v[112:113] op_sel:[0,1] op_sel_hi:[1,1]\n"
      "v_cvt_pk_bf16_f32 v100, v118, v119\n"
      "v_lshlrev_b32 v118, 16, v101\n"
      "v_and_b32 v119, 0xffff0000, v101\n"
      "v_pk_mul_f32 v[118:119], v[118:119], v[112:113] op_sel:[0,1] op_sel_hi:[1,1]\n"
      "v_cvt_pk_bf16_f32 v101, v118, v119\n"
      "v_lshlrev_b32 v118, 16, v102\n"
      "v_and_b32 v119, 0xffff0000, v102\n"
      "v_pk_mul_f32 v[118:119], v[118:119], v[112:113] op_sel:[0,1] op_sel_hi:[1,1]\n"
      "v_cvt_pk_bf16_f32 v102, v118, v119\n"
      "v_lshlrev_b32 v118, 16, v103\n"
      "v_and_b32 v119, 0xffff0000, v103\n"
      "v_pk_mul_f32 v[118:119], v[118:119], v[112:113] op_sel:[0,1] op_sel_hi:[1,1]\n"
      "v_cvt_pk_bf16_f32 v103, v118, v119\n"
      "v_lshlrev_b32 v118, 16, v104\n"
      "v_and_b32 v119, 0xffff0000, v104\n"
      "v_pk_mul_f32 v[118:119], v[118:119], v[114:115] op_sel:[0,0] op_sel_hi:[1,0]\n"
      "v_cvt_pk_bf16_f32 v104, v118, v119\n"
      "v_lshlrev_b32 v118, 16, v105\n"
      "v_and_b32 v119, 0xffff0000, v105\n"
      "v_pk_mul_f32 v[118:119], v[118:119], v[114:115] op_sel:[0,0] op_sel_hi:[1,0]\n"
      "v_cvt_pk_bf16_f32 v105, v118, v119\n"
      "v_lshlrev_b32 v118, 16, v106\n"
      "v_and_b32 v119, 0xffff0000, v106\n"
      "v_pk_mul_f32 v[118:119], v[118:119], v[114:115] op_sel:[0,0] op_sel_hi:[1,0]\n"
      "v_cvt_pk_bf16_f32 v106, v118, v119\n"
      "v_lshlrev_b32 v118, 16, v107\n"
      "v_and_b32 v119, 0xffff0000, v107\n"
      "v_pk_mul_f32 v[118:119], v[118:119], v[114:115] op_sel:[0,0] op_sel_hi:[1,0]\n"
      "v_cvt_pk_bf16_f32 v107, v118, v119\n"
      "v_lshlrev_b32 v118, 16, v108\n"
      "v_and_b32 v119, 0xffff0000, v108\n"
      "v_pk_mul_f32 v[118:119], v[118:119], v[114:115] op_sel:[0,1] op_sel_hi:[1,1]\n"
      "v_cvt_pk_bf16_f32 v108, v118, v119\n"
      "v_lshlrev_b32 v118, 16, v109\n"
      "v_and_b32 v119, 0xffff0000, v109\n"
      "v_pk_mul_f32 v[118:119], v[118:119], v[114:115] op_sel:[0,1] op_sel_hi:[1,1]\n"
      "v_cvt_pk_bf16_f32 v109, v118, v119\n"
      "v_lshlrev_b32 v118, 16, v110\n"
      "v_and_b32 v119, 0xffff0000, v110\n"
      "v_pk_mul_f32 v[118:119], v[118:119], v[114:115] op_sel:[0,1] op_sel_hi:[1,1]\n"
      "v_cvt_pk_bf16_f32 v110, v118, v119\n"
      "v_lshlrev_b32 v118, 16, v111\n"
      "v_and_b32 v119, 0xffff0000, v111\n"
      "v_pk_mul_f32 v[118:119], v[118:119], v[114:115] op_sel:[0,1] op_sel_hi:[1,1]\n"
      "v_cvt_pk_bf16_f32 v111, v118, v119\n"
      "s_setpc_b64 s[84:85]\n"
      "MIS_%=:\n"
      "s_waitcnt lgkmcnt(0)\n"
      "v_sub_f32 v148, v153, v117\n"
      "v_min_f32 v149, 0, -v148\n"
      "v_min_f32 v148, 0, v148\n"
      "v_exp_f32 v149, v149\n"
      "v_exp_f32 v148, v148\n"
      "v_min_f32 v117, v117, v153\n"
      "v_mov_b32 v112, v148\n"
      "v_mov_b32 v228, v149\n"
      "v_mov_b32 v114, v112\n"
      "v_mov_b32 v230, v228\n"
      "s_nop 0\n"
      "v_permlane32_swap_b32 v112, v114\n"
      "v_permlane32_swap_b32 v228, v230\n"
      "v_mov_b32 v113, v112\n"
      "v_mov_b32 v229, v228\n"
      "v_mov_b32 v115, v114\n"
      "v_mov_b32 v231, v230\n"
      "v_permlane16_swap_b32 v112, v113\n"
      "v_permlane16_swap_b32 v228, v229\n"
      "v_permlane16_swap_b32 v114, v115\n"
      "v_permlane16_swap_b32 v230, v231\n"
      "s_call_b64 s[84:85], SCALE_%=\n"
      "v_pk_mul_f32 v[160:161], v[160:161], v[228:229] op_sel:[0,0] op_sel_hi:[1,0]\n"
      "v_pk_mul_f32 v[162:163], v[162:163], v[228:229] op_sel:[0,0] op_sel_hi:[1,0]\n"
      "v_pk_mul_f32 v[164:165], v[164:165], v[228:229] op_sel:[0,1] op_sel_hi:[1,1]\n"
      "v_pk_mul_f32 v[166:167], v[166:167], v[228:229] op_sel:[0,1] op_sel_hi:[1,1]\n"
      "v_pk_mul_f32 v[168:169], v[168:169], v[230:231] op_sel:[0,0] op_sel_hi:[1,0]\n"
      "v_pk_mul_f32 v[170:171], v[170:171], v[230:231] op_sel:[0,0] op_sel_hi:[1,0]\n"
      "v_pk_mul_f32 v[172:173], v[172:173], v[230:231] op_sel:[0,1] op_sel_hi:[1,1]\n"
      "v_pk_mul_f32 v[174:175], v[174:175], v[230:231] op_sel:[0,1] op_sel_hi:[1,1]\n"
      "v_pk_mul_f32 v[176:177], v[176:177], v[228:229] op_sel:[0,0] op_sel_hi:[1,0]\n"
      "v_pk_mul_f32 v[178:179], v[178:179], v[228:229] op_sel:[0,0] op_sel_hi:[1,0]\n"
      "v_pk_mul_f32 v[180:181], v[180:181], v[228:229] op_sel:[0,1] op_sel_hi:[1,1]\n"
      "v_pk_mul_f32 v[182:183], v[182:183], v[228:229] op_sel:[0,1] op_sel_hi:[1,1]\n"
      "v_pk_mul_f32 v[184:185], v[184:185], v[230:231] op_sel:[0,0] op_sel_hi:[1,0]\n"
      "v_pk_mul_f32 v[186:187], v[186:187], v[230:231] op_sel:[0,0] op_sel_hi:[1,0]\n"
      "v_pk_mul_f32 v[188:189], v[188:189], v[230:231] op_sel:[0,1] op_sel_hi:[1,1]\n"
      "v_pk_mul_f32 v[190:191], v[190:191], v[230:231] op_sel:[0,1] op_sel_hi:[1,1]\n"
      "v_mov_b32 v112, v228\n"
      "v_mov_b32 v113, v229\n"
      "v_mov_b32 v114, v230\n"
      "v_mov_b32 v115, v231\n"
      "v_accvgpr_read_b32 v128, a64\n"
      "v_accvgpr_read_b32 v129, a65\n"
      "v_accvgpr_read_b32 v130, a66\n"
      "v_accvgpr_read_b32 v131, a67\n"
      "v_accvgpr_read_b32 v132, a68\n"
      "v_accvgpr_read_b32 v133, a69\n"
      "v_accvgpr_read_b32 v134, a70\n"
      "v_accvgpr_read_b32 v135, a71\n"
      "v_accvgpr_read_b32 v136, a72\n"
      "v_accvgpr_read_b32 v137, a73\n"
      "v_accvgpr_read_b32 v138, a74\n"
      "v_accvgpr_read_b32 v139, a75\n"
      "v_accvgpr_read_b32 v140, a76\n"
      "v_accvgpr_read_b32 v141, a77\n"
      "v_accvgpr_read_b32 v142, a78\n"
      "v_accvgpr_read_b32 v143, a79\n"
      "v_lshlrev_b32 v118, 16, v128\n"
      "v_and_b32 v119, 0xffff0000, v128\n"
      "v_pk_mul_f32 v[118:119], v[118:119], v[112:113] op_sel:[0,0] op_sel_hi:[1,0]\n"
      "v_cvt_pk_bf16_f32 v128, v118, v119\n"
      "v_lshlrev_b32 v118, 16, v129\n"
      "v_and_b32 v119, 0xffff0000, v129\n"
      "v_pk_mul_f32 v[118:119], v[118:119], v[112:113] op_sel:[0,0] op_sel_hi:[1,0]\n"
      "v_cvt_pk_bf16_f32 v129, v118, v119\n"
      "v_lshlrev_b32 v118, 16, v130\n"
      "v_and_b32 v119, 0xffff0000, v130\n"
      "v_pk_mul_f32 v[118:119], v[118:119], v[112:113] op_sel:[0,0] op_sel_hi:[1,0]\n"
      "v_cvt_pk_bf16_f32 v130, v118, v119\n"
      "v_lshlrev_b32 v118, 16, v131\n"
      "v_and_b32 v119, 0xffff0000, v131\n"
      "v_pk_mul_f32 v[118:119], v[118:119], v[112:113] op_sel:[0,0] op_sel_hi:[1,0]\n"
      "v_cvt_pk_bf16_f32 v131, v118, v119\n"
      "v_lshlrev_b32 v118, 16, v132\n"
      "v_and_b32 v119, 0xffff0000, v132\n"
      "v_pk_mul_f32 v[118:119], v[118:119], v[112:113] op_sel:[0,1] op_sel_hi:[1,1]\n"
      "v_cvt_pk_bf16_f32 v132, v118, v119\n"
      "v_lshlrev_b32 v118, 16, v133\n"
      "v_and_b32 v119, 0xffff0000, v133\n"
      "v_pk_mul_f32 v[118:119], v[118:119], v[112:113] op_sel:[0,1] op_sel_hi:[1,1]\n"
      "v_cvt_pk_bf16_f32 v133, v118, v119\n"
      "v_lshlrev_b32 v118, 16, v134\n"
      "v_and_b32 v119, 0xffff0000, v134\n"
      "v_pk_mul_f32 v[118:119], v[118:119], v[112:113] op_sel:[0,1] op_sel_hi:[1,1]\n"
      "v_cvt_pk_bf16_f32 v134, v118, v119\n"
      "v_lshlrev_b32 v118, 16, v135\n"
      "v_and_b32 v119, 0xffff0000, v135\n"
      "v_pk_mul_f32 v[118:119], v[118:119], v[112:113] op_sel:[0,1] op_sel_hi:[1,1]\n"
      "v_cvt_pk_bf16_f32 v135, v118, v119\n"
      "v_lshlrev_b32 v118, 16, v136\n"
      "v_and_b32 v119, 0xffff0000, v136\n"
      "v_pk_mul_f32 v[118:119], v[118:119], v[114:115] op_sel:[0,0] op_sel_hi:[1,0]\n"
      "v_cvt_pk_bf16_f32 v136, v118, v119\n"
      "v_lshlrev_b32 v118, 16, v137\n"
      "v_and_b32 v119, 0xffff0000, v137\n"
      "v_pk_mul_f32 v[118:119], v[118:119], v[114:115] op_sel:[0,0] op_sel_hi:[1,0]\n"
      "v_cvt_pk_bf16_f32 v137, v118, v119\n"
      "v_lshlrev_b32 v118, 16, v138\n"
      "v_and_b32 v119, 0xffff0000, v138\n"
      "v_pk_mul_f32 v[118:119], v[118:119], v[114:115] op_sel:[0,0] op_sel_hi:[1,0]\n"
      "v_cvt_pk_bf16_f32 v138, v118, v119\n"
      "v_lshlrev_b32 v118, 16, v139\n"
      "v_and_b32 v119, 0xffff0000, v139\n"
      "v_pk_mul_f32 v[118:119], v[118:119], v[114:115] op_sel:[0,0] op_sel_hi:[1,0]\n"
      "v_cvt_pk_bf16_f32 v139, v118, v119\n"
      "v_lshlrev_b32 v118, 16, v140\n"
      "v_and_b32 v119, 0xffff0000, v140\n"
      "v_pk_mul_f32 v[118:119], v[118:119], v[114:115] op_sel:[0,1] op_sel_hi:[1,1]\n"
      "v_cvt_pk_bf16_f32 v140, v118, v119\n"
      "v_lshlrev_b32 v118, 16, v141\n"
      "v_and_b32 v119, 0xffff0000, v141\n"
      "v_pk_mul_f32 v[118:119], v[118:119], v[114:115] op_sel:[0,1] op_sel_hi:[1,1]\n"
      "v_cvt_pk_bf16_f32 v141, v118, v119\n"
      "v_lshlrev_b32 v118, 16, v142\n"
      "v_and_b32 v119, 0xffff0000, v142\n"
      "v_pk_mul_f32 v[118:119], v[118:119], v[114:115] op_sel:[0,1] op_sel_hi:[1,1]\n"
      "v_cvt_pk_bf16_f32 v142, v118, v119\n"
      "v_lshlrev_b32 v118, 16, v143\n"
      "v_and_b32 v119, 0xffff0000, v143\n"
      "v_pk_mul_f32 v[118:119], v[118:119], v[114:115] op_sel:[0,1] op_sel_hi:[1,1]\n"
      "v_cvt_pk_bf16_f32 v143, v118, v119\n"
      "v_accvgpr_write_b32 a64, v128\n"
      "v_accvgpr_write_b32 a65, v129\n"
      "v_accvgpr_write_b32 a66, v130\n"
      "v_accvgpr_write_b32 a67, v131\n"
      "v_accvgpr_write_b32 a68, v132\n"
      "v_accvgpr_write_b32 a69, v133\n"
      "v_accvgpr_write_b32 a70, v134\n"
      "v_accvgpr_write_b32 a71, v135\n"
      "v_accvgpr_write_b32 a72, v136\n"
      "v_accvgpr_write_b32 a73, v137\n"
      "v_accvgpr_write_b32 a74, v138\n"
      "v_accvgpr_write_b32 a75, v139\n"
      "v_accvgpr_write_b32 a76, v140\n"
      "v_accvgpr_write_b32 a77, v141\n"
      "v_accvgpr_write_b32 a78, v142\n"
      "v_accvgpr_write_b32 a79, v143\n"
      "s_branch MR_%=\n"
      "FIX_%=:\n"
      "s_nop 7\n"
      "v_sub_f32 v224, v81, v117\n"
      "v_min_f32 v112, 0, -v224\n"
      "v_min_f32 v224, 0, v224\n"
      "v_exp_f32 v112, v112\n"
      "v_exp_f32 v224, v224\n"
      "s_nop 0\n"
      "v_mul_f32 v144, v144, v224\n"
      "v_mul_f32 v80, v80, v112\n"
      "v_mov_b32 v226, v224\n"
      "v_mov_b32 v114, v112\n"
      "s_nop 0\n"
      "v_permlane32_swap_b32 v224, v226\n"
      "v_permlane32_swap_b32 v112, v114\n"
      "v_mov_b32 v225, v224\n"
      "v_mov_b32 v113, v112\n"
      "v_mov_b32 v227, v226\n"
      "v_mov_b32 v115, v114\n"
      "v_permlane16_swap_b32 v224, v225\n"
      "v_permlane16_swap_b32 v112, v113\n"
      "v_permlane16_swap_b32 v226, v227\n"
      "v_permlane16_swap_b32 v114, v115\n"
      "v_pk_mul_f32 v[32:33], v[32:33], v[224:225] op_sel:[0,0] op_sel_hi:[1,0]\n"
      "v_pk_mul_f32 v[34:35], v[34:35], v[224:225] op_sel:[0,0] op_sel_hi:[1,0]\n"
      "v_pk_mul_f32 v[36:37], v[36:37], v[224:225] op_sel:[0,1] op_sel_hi:[1,1]\n"
      "v_pk_mul_f32 v[38:39], v[38:39], v[224:225] op_sel:[0,1] op_sel_hi:[1,1]\n"
      "v_pk_mul_f32 v[40:41], v[40:41], v[226:227] op_sel:[0,0] op_sel_hi:[1,0]\n"
      "v_pk_mul_f32 v[42:43], v[42:43], v[226:227] op_sel:[0,0] op_sel_hi:[1,0]\n"
      "v_pk_mul_f32 v[44:45], v[44:45], v[226:227] op_sel:[0,1] op_sel_hi:[1,1]\n"
      "v_pk_mul_f32 v[46:47], v[46:47], v[226:227] op_sel:[0,1] op_sel_hi:[1,1]\n"
      "v_pk_mul_f32 v[48:49], v[48:49], v[224:225] op_sel:[0,0] op_sel_hi:[1,0]\n"
      "v_pk_mul_f32 v[50:51], v[50:51], v[224:225] op_sel:[0,0] op_sel_hi:[1,0]\n"
      "v_pk_mul_f32 v[52:53], v[52:53], v[224:225] op_sel:[0,1] op_sel_hi:[1,1]\n"
      "v_pk_mul_f32 v[54:55], v[54:55], v[224:225] op_sel:[0,1] op_sel_hi:[1,1]\n"
      "v_pk_mul_f32 v[56:57], v[56:57], v[226:227] op_sel:[0,0] op_sel_hi:[1,0]\n"
      "v_pk_mul_f32 v[58:59], v[58:59], v[226:227] op_sel:[0,0] op_sel_hi:[1,0]\n"
      "v_pk_mul_f32 v[60:61], v[60:61], v[226:227] op_sel:[0,1] op_sel_hi:[1,1]\n"
      "v_pk_mul_f32 v[62:63], v[62:63], v[226:227] op_sel:[0,1] op_sel_hi:[1,1]\n"
      "v_lshlrev_b32 v118, 16, v64\n"
      "v_and_b32 v119, 0xffff0000, v64\n"
      "v_pk_mul_f32 v[118:119], v[118:119], v[112:113] op_sel:[0,0] op_sel_hi:[1,0]\n"
      "v_cvt_pk_bf16_f32 v64, v118, v119\n"
      "v_lshlrev_b32 v118, 16, v65\n"
      "v_and_b32 v119, 0xffff0000, v65\n"
      "v_pk_mul_f32 v[118:119], v[118:119], v[112:113] op_sel:[0,0] op_sel_hi:[1,0]\n"
      "v_cvt_pk_bf16_f32 v65, v118, v119\n"
      "v_lshlrev_b32 v118, 16, v66\n"
      "v_and_b32 v119, 0xffff0000, v66\n"
      "v_pk_mul_f32 v[118:119], v[118:119], v[112:113] op_sel:[0,0] op_sel_hi:[1,0]\n"
      "v_cvt_pk_bf16_f32 v66, v118, v119\n"
      "v_lshlrev_b32 v118, 16, v67\n"
      "v_and_b32 v119, 0xffff0000, v67\n"
      "v_pk_mul_f32 v[118:119], v[118:119], v[112:113] op_sel:[0,0] op_sel_hi:[1,0]\n"
      "v_cvt_pk_bf16_f32 v67, v118, v119\n"
      "v_lshlrev_b32 v118, 16, v68\n"
      "v_and_b32 v119, 0xffff0000, v68\n"
      "v_pk_mul_f32 v[118:119], v[118:119], v[112:113] op_sel:[0,1] op_sel_hi:[1,1]\n"
      "v_cvt_pk_bf16_f32 v68, v118, v119\n"
      "v_lshlrev_b32 v118, 16, v69\n"
      "v_and_b32 v119, 0xffff0000, v69\n"
      "v_pk_mul_f32 v[118:119], v[118:119], v[112:113] op_sel:[0,1] op_sel_hi:[1,1]\n"
      "v_cvt_pk_bf16_f32 v69, v118, v119\n"
      "v_lshlrev_b32 v118, 16, v70\n"
      "v_and_b32 v119, 0xffff0000, v70\n"
      "v_pk_mul_f32 v[118:119], v[118:119], v[112:113] op_sel:[0,1] op_sel_hi:[1,1]\n"
      "v_cvt_pk_bf16_f32 v70, v118, v119\n"
      "v_lshlrev_b32 v118, 16, v71\n"
      "v_and_b32 v119, 0xffff0000, v71\n"
      "v_pk_mul_f32 v[118:119], v[118:119], v[112:113] op_sel:[0,1] op_sel_hi:[1,1]\n"
      "v_cvt_pk_bf16_f32 v71, v118, v119\n"
      "v_lshlrev_b32 v118, 16, v72\n"
      "v_and_b32 v119, 0xffff0000, v72\n"
      "v_pk_mul_f32 v[118:119], v[118:119], v[114:115] op_sel:[0,0] op_sel_hi:[1,0]\n"
      "v_cvt_pk_bf16_f32 v72, v118, v119\n"
      "v_lshlrev_b32 v118, 16, v73\n"
      "v_and_b32 v119, 0xffff0000, v73\n"
      "v_pk_mul_f32 v[118:119], v[118:119], v[114:115] op_sel:[0,0] op_sel_hi:[1,0]\n"
      "v_cvt_pk_bf16_f32 v73, v118, v119\n"
      "v_lshlrev_b32 v118, 16, v74\n"
      "v_and_b32 v119, 0xffff0000, v74\n"
      "v_pk_mul_f32 v[118:119], v[118:119], v[114:115] op_sel:[0,0] op_sel_hi:[1,0]\n"
      "v_cvt_pk_bf16_f32 v74, v118, v119\n"
      "v_lshlrev_b32 v118, 16, v75\n"
      "v_and_b32 v119, 0xffff0000, v75\n"
      "v_pk_mul_f32 v[118:119], v[118:119], v[114:115] op_sel:[0,0] op_sel_hi:[1,0]\n"
      "v_cvt_pk_bf16_f32 v75, v118, v119\n"
      "v_lshlrev_b32 v118, 16, v76\n"
      "v_and_b32 v119, 0xffff0000, v76\n"
      "v_pk_mul_f32 v[118:119], v[118:119], v[114:115] op_sel:[0,1] op_sel_hi:[1,1]\n"
      "v_cvt_pk_bf16_f32 v76, v118, v119\n"
      "v_lshlrev_b32 v118, 16, v77\n"
      "v_and_b32 v119, 0xffff0000, v77\n"
      "v_pk_mul_f32 v[118:119], v[118:119], v[114:115] op_sel:[0,1] op_sel_hi:[1,1]\n"
      "v_cvt_pk_bf16_f32 v77, v118, v119\n"
      "v_lshlrev_b32 v118, 16, v78\n"
      "v_and_b32 v119, 0xffff0000, v78\n"
      "v_pk_mul_f32 v[118:119], v[118:119], v[114:115] op_sel:[0,1] op_sel_hi:[1,1]\n"
      "v_cvt_pk_bf16_f32 v78, v118, v119\n"
      "v_lshlrev_b32 v118, 16, v79\n"
      "v_and_b32 v119, 0xffff0000, v79\n"
      "v_pk_mul_f32 v[118:119], v[118:119], v[114:115] op_sel:[0,1] op_sel_hi:[1,1]\n"
      "v_cvt_pk_bf16_f32 v79, v118, v119\n"
      "v_add_f32 v224, v144, v80\n"
      "v_rcp_f32 v224, v224\n"
      "v_mfma_f32_16x16x32_bf16 v[160:163], v[86:89], v[64:67], v[32:35]\n"
      "v_mfma_f32_16x16x32_bf16 v[176:179], v[84:87], v[64:67], v[48:51]\n"
      "v_mfma_f32_16x16x32_bf16 v[164:167], v[86:89], v[68:71], v[36:39]\n"
      "v_mfma_f32_16x16x32_bf16 v[180:183], v[84:87], v[68:71], v[52:55]\n"
      "v_mfma_f32_16x16x32_bf16 v[168:171], v[86:89], v[72:75], v[40:43]\n"
      "v_mfma_f32_16x16x32_bf16 v[184:187], v[84:87], v[72:75], v[56:59]\n"
      "v_mfma_f32_16x16x32_bf16 v[172:175], v[86:89], v[76:79], v[44:47]\n"
      "v_mfma_f32_16x16x32_bf16 v[188:191], v[84:87], v[76:79], v[60:63]\n"
      "v_mov_b32 v226, v224\n"
      "s_nop 1\n"
      "s_nop 0\n"
      "s_nop 1\n"
      "v_permlane32_swap_b32 v224, v226\n"
      "s_nop 1\n"
      "v_mov_b32 v225, v224\n"
      "s_nop 1\n"
      "v_mov_b32 v227, v226\n"
      "s_nop 1\n"
      "v_permlane16_swap_b32 v224, v225\n"
      "s_nop 1\n"
      "v_permlane16_swap_b32 v226, v227\n"
      "s_nop 1\n"
      "s_branch FR_%=\n"
      ".subsection 0\n"
      : "+{s[4:19]}"(ka), "+{v128}"(pre), "+{s92}"(tch), "+{v[242:249]}"(ad), "+{v[250:251]}"(ra)
      : [qo] "s"(bk * 128 * D_ * 2), [oo] "s"((bk * 128 + rh * 64) * (D_ * 2) + (2 * split + kh) * 64 + 4096),
        [fo] "s"((unsigned)WS_EPOCH + bk * 128 + 64 + split * 8), [xo] "s"((unsigned)(WS_X + split * WS_X1 + (bk * 4 + wid) * XW_BYTES)),
        [xw] "s"(xarea(wid)), [xr] "s"(xarea(wid ^ 2)),
        [so0] "s"(so0), [inc] "s"(inc), [wb] "s"(wbase), [tv] "s"(wbase + STAGE_BYTES + KIMG + split * 8192), [n1] "s"(n1),
        [qoff] "s"(Q_OFF + rh * 16384 - kh * (8192 - 1024)), [toff] "s"(TREE_OFF - kh * (8192 - 1024)),
        [neg] "s"((wid ^ (3 * split)) ? 0xff800000u : 0u),
        [lead] "s"(blk * 4 + wid * 32), [cph] "s"(C2 + (unsigned)((jj & ~3) * 8 + xg * 4) * WIN / 256u),
        [rep] "s"((jj >> 2) == 160 / 32 ? 128 + (blk & 31) * 4 : (blk - 224) * 4)
      : "v0", "v1", "v2", "v3", "v4", "v5", "v6", "v7", "v8", "v9", "v10", "v11", "v12", "v13", "v14", "v15", "v16", "v17", "v18", "v19", "v20", "v21", "v22", "v23", "v24", "v25", "v26", "v27", "v28", "v29", "v30", "v31", "v32", "v33", "v34", "v35", "v36", "v37", "v38", "v39", "v40", "v41", "v42", "v43", "v44", "v45", "v46", "v47", "v48", "v49", "v50", "v51", "v52", "v53", "v54", "v55", "v56", "v57", "v58", "v59", "v60", "v61", "v62", "v63", "v64", "v65", "v66", "v67", "v68", "v69", "v70", "v71", "v72", "v73", "v74", "v75", "v76", "v77", "v78", "v79", "v80", "v81", "v82", "v83", "v84", "v85", "v86", "v87", "v88", "v89", "v90", "v91", "v92", "v93", "v94", "v95", "v96", "v97", "v98", "v99", "v100", "v101", "v102", "v103", "v104", "v105", "v106", "v107", "v108", "v109", "v110", "v111", "v112", "v113", "v114", "v115", "v116", "v117", "v118", "v119", "v120", "v121", "v122", "v123", "v124", "v125", "v126", "v127", "v129", "v130", "v131", "v132", "v133", "v134", "v135", "v136", "v137", "v138", "v139", "v140", "v141", "v142", "v143", "v144", "v145", "v146", "v147", "v148", "v149", "v150", "v151", "v152", "v153", "v154", "v155", "v156", "v157", "v158", "v159", "v160", "v161", "v162", "v163", "v164", "v165", "v166", "v167", "v168", "v169", "v170", "v171", "v172", "v173", "v174", "v175", "v176", "v177", "v178", "v179", "v180", "v181", "v182", "v183", "v184", "v185", "v186", "v187", "v188", "v189", "v190", "v191", "v192", "v193", "v194", "v195", "v196", "v197", "v198", "v199", "v200", "v201", "v202", "v203", "v204", "v205", "v206", "v207", "v208", "v209", "v210", "v211", "v212", "v213", "v214", "v215", "v216", "v217", "v218", "v219", "v220", "v221", "v222", "v223", "v224", "v225", "v226", "v227", "v228", "v229", "v230", "v231", "v232", "v233", "v234", "v235", "v236", "v237", "v238", "v239", "v240", "v241", "a0", "a1", "a2", "a3", "a4", "a5", "a6", "a7", "a8", "a9", "a10", "a11", "a12", "a13", "a14", "a15", "a16", "a17", "a18", "a19", "a20", "a21", "a22", "a23", "a24", "a25", "a26", "a27", "a28", "a29", "a30", "a31", "a32", "a33", "a34", "a35", "a36", "a37", "a38", "a39", "a40", "a41", "a42", "a43", "a44", "a45", "a46", "a47", "a48", "a49", "a50", "a51", "a52", "a53", "a54", "a55", "a56", "a57", "a58", "a59", "a60", "a61", "a62", "a63", "a64", "a65", "a66", "a67", "a68", "a69", "a70", "a71", "a72", "a73", "a74", "a75", "a76", "a77", "a78", "a79", "a80", "a81", "a82", "a83", "a84", "a85", "a86", "a87", "a88", "a89", "a90", "a91", "a92", "a93", "a94", "a95", "a96", "a97", "a98", "a99", "a100", "a101", "a102", "a103", "a104", "a105", "a106", "a107", "a108", "a109", "a110", "a111", "a112", "a113", "a114", "a115", "a116", "a117", "a118", "a119", "a120", "a121", "a122", "a123", "a124", "a125", "a126", "a127", "a128", "a129", "a130", "a131", "a132", "a133", "a134", "a135", "a136", "a137", "a138", "a139", "a140", "a141", "a142", "a143", "a144", "a145", "a146", "a147", "a148", "a149", "a150", "a151", "a152", "a153", "a154", "a155", "a156", "a157", "a158", "a159", "a160", "a161", "a162", "a163", "a164", "a165", "a166", "a167", "a168", "a169", "a170", "a171", "a172", "a173", "a174", "a175", "a176", "a177", "a178", "a179", "a180", "a181", "a182", "a183", "a184", "a185", "a186", "a187", "a188", "a189", "a190", "a191", "a192", "a193", "a194", "a195", "a196", "a197", "a198", "a199", "a200", "a201", "a202", "a203", "a204", "a205", "a206", "a207", "a208", "a209", "a210", "a211", "a212", "a213", "a214", "a215", "a216", "a217", "a218", "a219", "a220", "a221", "a222", "a223", "a224", "a225", "a226", "a227", "a228", "a229", "a230", "a231", "s20", "s21", "s22", "s23", "s24", "s25", "s26", "s27", "s28", "s29", "s30", "s31", "s60", "s61", "s62", "s63", "s64", "s65", "s66", "s67", "s68", "s69", "s70", "s71", "s72", "s73", "s74", "s75", "s76", "s77", "s78", "s79", "s80", "s81", "s82", "s83", "s84", "s85", "s86", "s87", "s88", "s89", "s90", "s91", "s93", "s94", "s95", "s98", "s99", "s100", "s101", "vcc", "scc", "memory");
}

}

extern "C" void kernel_launch(void* const* d_in, const int* in_sizes, int n_in, void* d_out, int out_size,
                              void* d_ws, size_t ws_size, hipStream_t stream) {
  if (n_in != 6 || ws_size < WS_END) return;
  const __hip_bfloat16* q = (const __hip_bfloat16*)d_in[0];
  const __hip_bfloat16* kc = (const __hip_bfloat16*)d_in[1];
  const __hip_bfloat16* vc = (const __hip_bfloat16*)d_in[2];
  const __hip_bfloat16* kt = (const __hip_bfloat16*)d_in[3];
  const __hip_bfloat16* vt = (const __hip_bfloat16*)d_in[4];
  const __hip_bfloat16* tm = (const __hip_bfloat16*)d_in[5];
  hipLaunchKernelGGL(tree_attn_main, dim3(B_ * HKV_ * 2), dim3(256), 0, stream, q, kc, vc, kt, vt, tm,
                     (__hip_bfloat16*)d_out, (char*)d_ws);
}
```

```cpp
#include <hip/hip_runtime.h>
#include <hip/hip_bf16.h>
#include <stdint.h>

#pragma clang diagnostic ignored "-Wint-to-pointer-cast"
#pragma clang diagnostic ignored "-Winline-asm"

namespace {

constexpr int B_ = 16, HQ_ = 32, HKV_ = 8, T_ = 32, C_ = 4096, D_ = 128;
constexpr int TILE = 64;
constexpr int KIMG = TILE * D_ * 2;
constexpr int STAGE_BYTES = 2 * KIMG;
constexpr int TREE_OFF = 4 * STAGE_BYTES;
constexpr int Q_OFF = 3 * STAGE_BYTES;
constexpr int LDS_BYTES = TREE_OFF + 2 * 13312;
constexpr unsigned C2 = 364, WIN = 52;

constexpr size_t WS_EPOCH = 0;
constexpr size_t WS_REF = WS_EPOCH + 128 * 128;
constexpr size_t WS_X = WS_REF + 16384, WS_X1 = 4 << 20;
constexpr size_t XW_BYTES = 4096 + 1024;
constexpr size_t WS_END = 0x680000 + WS_X;
static_assert(WS_X1 + WS_X + 128 * 4 * XW_BYTES <= WS_END, "");

typedef float    f32x4  __attribute__((ext_vector_type(4)));
typedef unsigned long long u64x8 __attribute__((ext_vector_type(8)));
typedef unsigned u32x8  __attribute__((ext_vector_type(8)));
typedef unsigned u32x4  __attribute__((ext_vector_type(4)));
typedef unsigned u32x2  __attribute__((ext_vector_type(2)));
typedef __attribute__((address_space(3))) f32x4 lds_f32x4;
typedef __attribute__((address_space(1))) unsigned long long gu64;

__global__ __launch_bounds__(256, 1)
void tree_attn_main(const __hip_bfloat16*, const __hip_bfloat16*, const __hip_bfloat16*, const __hip_bfloat16*,
                    const __hip_bfloat16*, const __hip_bfloat16*, __hip_bfloat16*, char*) {
  __shared__ __attribute__((aligned(1024))) char smem[LDS_BYTES];
  unsigned blk = blockIdx.x;
  u64x8 ka; unsigned pre, tch, voffK; int tid = threadIdx.x;
  asm volatile(
      "s_load_dwordx16 s[4:19], %[kp], 0x0\n"
      "s_getpc_b64 s[84:85]\n"
      "v_lshlrev_b32 v112, 4, %[tid]\n"
      "s_lshl_b32 s69, %[blk], 7\n"
      "s_and_b32 s69, s69, 0x7c00\n"
      "v_lshl_add_u32 v120, %[tid], 2, s69\n"
      "global_load_dword v128, v120, s[84:85]\n"
      "v_readfirstlane_b32 s77, %[tid]\n"
      "v_lshrrev_b32 %[vk], 4, %[tid]\n"
      "s_and_b32 s78, %[blk], 7\n"
      "v_xor_b32 %[vk], %[vk], %[tid]\n"
      "s_lshr_b32 s74, %[blk], 4\n"
      "v_and_b32 %[vk], 7, %[vk]\n"
      "s_lshl4_add_u32 s78, s78, s74\n"
      "v_lshrrev_b32 v120, 3, %[tid]\n"
      "s_lshl_b32 s78, s78, 15\n"
      "v_lshlrev_b32 %[vk], 4, %[vk]\n"
      "s_lshl_b32 s77, s77, 5\n"
      "v_lshl_or_b32 %[vk], v120, 8, %[vk]\n"
      "s_bfm_b32 s66, 1, 22\n"
      "s_bfm_b32 s67, 1, 17\n"
      "s_add_u32 m0, s77, 0x18000\n"
      "s_waitcnt lgkmcnt(0)\n"
      "s_mov_b32 s64, s4\n"
      "s_and_b32 s65, s5, 0xffff\n"
      "buffer_load_dwordx4 %[vk], s[64:67], s78 offen nt lds\n"
      "s_add_u32 m0, s77, 99200\n"
      "v_add_u32 v120, 0x2000, %[vk]\n"
      "buffer_load_dwordx4 %[vk], s[64:67], s78 offen offset:128 nt lds\n"
      "s_add_u32 m0, s77, 106496\n"
      "s_add_u32 s74, s78, 0x4000\n"
      "s_lshl_b32 s75, s78, 5\n"
      "buffer_load_dwordx4 v120, s[64:67], s78 offen nt lds\n"
      "s_add_u32 m0, s77, 107392\n"
      "s_nop 0\n"
      "buffer_load_dwordx4 v120, s[64:67], s78 offen offset:128 nt lds\n"
      "s_add_u32 m0, s77, 114688\n"
      "s_load_dword s92, s[6:7], s75\n"
      "s_load_dword s92, s[8:9], s75\n"
      "buffer_load_dwordx4 %[vk], s[64:67], s74 offen nt lds\n"
      "s_add_u32 m0, s77, 115584\n"
      "s_mov_b32 s84, s10\n"
      "s_and_b32 s85, s11, 0xffff\n"
      "buffer_load_dwordx4 %[vk], s[64:67], s74 offen offset:128 nt lds\n"
      "s_add_u32 m0, s77, 122880\n"
      "s_mov_b32 s86, 0x100000\n"
      "s_mov_b32 s87, 0x20000\n"
      "s_lshr_b32 s75, s78, 2\n"
      "buffer_load_dwordx4 v120, s[64:67], s74 offen nt lds\n"
      "s_add_u32 m0, s77, 123776\n"
      "s_lshr_b32 s76, s78, 18\n"
      "s_lshl_b32 s76, s76, 11\n"
      "buffer_load_dwordx4 v120, s[64:67], s74 offen offset:128 nt lds\n"
      "s_add_u32 m0, s77, 131072\n"
      "s_lshr_b32 s69, s77, 1\n"
      "buffer_load_dwordx4 %[vk], s[84:87], s75 offen nt lds\n"
      "s_add_u32 m0, s77, 131968\n"
      "s_cmp_gt_u32 s77, 2048\n"
      "buffer_load_dwordx4 %[vk], s[84:87], s75 offen offset:128 nt lds\n"
      "s_cbranch_scc1 NOMASK_%=\n"
      "s_add_u32 m0, s69, 140288\n"
      "s_mov_b32 s64, s14\n"
      "s_and_b32 s65, s15, 0xffff\n"
      "s_mov_b32 s66, 0x8000\n"
      "buffer_load_dwordx4 v112, s[64:67], s76 offen lds\n"
      "NOMASK_%=:\n"
      : "=&{s[4:19]}"(ka), "=&{v128}"(pre), "=&{s92}"(tch), [vk] "=&v"(voffK), [tid] "+v"(tid)
      : [kp] "s"(__builtin_amdgcn_kernarg_segment_ptr()), [blk] "s"(blk)
      : "v112", "v120", "s64", "s65", "s66", "s67", "s69", "s70", "s71", "s74", "s75", "s76", "s77", "s78", "s84", "s85", "s86", "s87", "s96", "s97", "scc", "memory");
  asm volatile("" : "+s"(blk));
  const int lane = tid & 63;
  const int wid = __builtin_amdgcn_readfirstlane(tid >> 6);
  const unsigned wbase = wid * 2048;
  const int rh = wid & 1, kh = wid >> 1;
  const int xg = blk & 7, jj = blk >> 3, bk = xg * 16 + (jj >> 1), split = jj & 1;
  const int c16 = lane & 15, g = lane >> 4;
  const unsigned lds0 = (unsigned)(uintptr_t)smem;

  const unsigned voffV = (unsigned)((8 * wid + (lane >> 3)) * 256 + (((lane & 7) ^ (g << 1)) << 4));
  const unsigned so0 = (unsigned)(bk * C_ + split * 2 * TILE) * (D_ * 2) + (bk & 1) * KIMG, inc = KIMG - 2 * (bk & 1) * KIMG;
#pragma unroll
  for (int j = 0; j < 4; ++j) *(lds_f32x4*)(lds0 + KIMG + 4096 * j + tid * 16) = f32x4{0.f, 0.f, 0.f, 0.f};
#pragma unroll
  for (int j = 0; j < 2; ++j) *(lds_f32x4*)(lds0 + STAGE_BYTES + KIMG + (1 - split) * 8192 + 4096 * j + tid * 16) = f32x4{0.f, 0.f, 0.f, 0.f};
  u32x8 ad;
#pragma unroll
  for (int j = 0; j < 2; ++j)
    ad[j] = lds0 + (unsigned)(2048 * (c16 >> 3) + 128 * (c16 & 7) + (((4 * j + g) ^ (c16 >> 1)) << 4) + kh * 8192);
  {
    const int qq = c16 >> 2, p = lane & 3;
#pragma unroll
    for (int j = 0; j < 4; ++j)
      ad[2 + j] = lds0 + (unsigned)(2048 * (g >> 1) + 128 * (4 * (g & 1) + qq) + kh * 8192 + KIMG +
                                    (((2 * (j ^ (2 * kh)) + (p >> 1)) ^ ((2 * (g & 1) + (qq >> 1)) << 1)) << 4) + 8 * (p & 1));
  }
  ad[6] = voffK; ad[7] = voffV + 128 * split;
  u32x2 ra = {(unsigned)(lane < 4 ? WS_REF + lane * 512 : 1 << 26), (unsigned)(WS_REF + (jj & (4 - 1)) * 512 + (lane & 7) * 4 + (wid << 26))};
  auto xarea = [&](int w) { return lds0 + ((0x8d802000u >> (8 * w)) & 255u) * 1024; };
  const unsigned pg = jj >> 2, px = pg - (pg > 160 / 32) + xg * 5;
  const unsigned n1 = 4 + max(px - 6 * (px * 43 >> 8), ((0x70000000u | 6u << 160 / 8) >> 4 * pg) & 7);
  asm volatile(
      "s_memrealtime s[82:83]\n"
      "s_mov_b32 s60, %[so0]\n"
      "s_mov_b32 s62, %[wb]\n"
      "v_add_u32 v120, 0x2000, v248\n"
      "s_and_b32 s80, %[fo], 0xffffff80\n"
      "v_mov_b32 v240, s80\n"
      "s_mov_b32 s22, 0x8000000\n"
      "s_mov_b32 s23, 0x20000\n"
      "s_mov_b32 s20, s6\n"
      "s_and_b32 s21, s7, 0xffff\n"
      "s_add_u32 m0, s62, 0\n"
      "s_nop 0\n"
      "buffer_load_dwordx4 v248, s[20:23], s60 offen nt lds\n"
      "s_mov_b32 s86, 0x100000\n"
      "s_mov_b32 s87, 0x20000\n"
      "s_lshr_b32 s75, %[qo], 2\n"
      "s_mov_b32 s84, s12\n"
      "s_and_b32 s85, s13, 0xffff\n"
      "v_mov_b64 v[216:217], 0\n"
      "v_mov_b64 v[218:219], 0\n"
      "v_xor_b32 v121, 0x80, v249\n"
      "v_add_u32 v122, 0x2000, v249\n"
      "s_add_u32 m0, s62, 896\n"
      "s_nop 0\n"
      "buffer_load_dwordx4 v248, s[20:23], s60 offen offset:128 nt lds\n"
      "v_mfma_f32_32x32x16_bf16 v[32:47], v[216:219], v[216:219], 0\n"
      "v_add_u32 v123, 0x2000, v121\n"
      "s_mov_b32 s61, %[so0]\n"
      "s_sub_u32 s78, 0x10000, %[inc]\n"
      "s_add_u32 s77, %[wb], 0x10000\n"
      "v_mfma_f32_32x32x16_bf16 v[48:63], v[216:219], v[216:219], 0\n"
      "s_mov_b32 s70, 300\n"
      "s_mov_b32 s80, 0x12a\n"
      "s_mov_b32 s81, 0x0\n"
      "s_add_u32 m0, s62, 8192\n"
      "s_nop 0\n"
      "buffer_load_dwordx4 v120, s[20:23], s60 offen nt lds\n"
      "v_mfma_f32_32x32x16_bf16 v[64:79], v[216:219], v[216:219], 0\n"
      "s_mov_b32 s101, 1000\n"
      "s_mov_b32 s98, 110\n"
      "s_mov_b32 s93, 28\n"
      "s_mov_b32 s99, 21\n"
      "s_mov_b32 s100, 282\n"
      "v_mfma_f32_32x32x16_bf16 v[80:95], v[216:219], v[216:219], 0\n"
      "s_lshl_b32 s91, %[wb], 18\n"
      "s_sub_u32 s91, 0x10000000, s91\n"
      "s_add_u32 m0, s62, 9088\n"
      "s_nop 0\n"
      "buffer_load_dwordx4 v120, s[20:23], s60 offen offset:128 nt lds\n"
      "s_add_u32 s62, s62, 0x8000\n"
      "s_and_b32 s62, s62, 0x1ffff\n"
      "s_add_u32 s60, s60, %[inc]\n"
      "s_mov_b32 s94, 0x3e0293ee\n"
      "s_mov_b32 s95, 0xffff0000\n"
      "v_mfma_f32_32x32x16_bf16 v[192:207], v[216:219], v[216:219], 0\n"
      "s_mov_b32 s76, 0xd0289660\n"
      "v_mbcnt_lo_u32_b32 v112, -1, 0\n"
      "v_mbcnt_hi_u32_b32 v112, -1, v112\n"
      "v_lshrrev_b32 v115, 4, v112\n"
      "v_and_b32 v112, 15, v112\n"
      "v_lshlrev_b32 v115, 3, v115\n"
      "s_add_u32 m0, s62, 0\n"
      "s_nop 0\n"
      "buffer_load_dwordx4 v248, s[20:23], s60 offen nt lds\n"
      "v_lshl_add_u32 v115, v112, 6, v115\n"
      "v_add_u32 v115, 140288, v115\n"
      "s_mov_b32 s24, s8\n"
      "s_and_b32 s25, s9, 0xffff\n"
      "s_mov_b32 s26, 0x8000000\n"
      "s_mov_b32 s27, 0x20000\n"
      "s_mov_b32 s30, 0x688000\n"
      "s_mov_b32 s31, 0x20000\n"
      "v_mov_b32 v113, 0x3f803f80\n"
      "s_add_u32 m0, s62, 896\n"
      "s_nop 0\n"
      "buffer_load_dwordx4 v248, s[20:23], s60 offen offset:128 nt lds\n"
      "v_accvgpr_write_b32 a192, 0\n"
      "v_add_u32 v224, %[qoff], v242\n"
      "v_add_u32 v228, %[toff], v242\n"
      "v_xor_b32 v226, 0x400, v224\n"
      "v_xor_b32 v230, 0x400, v228\n"
      "v_and_b32 v118, 0xfffffbff, v224\n"
      "v_add_u32 v225, %[qoff], v243\n"
      "v_add_u32 v229, %[toff], v243\n"
      "v_xor_b32 v227, 0x400, v225\n"
      "s_add_u32 m0, s62, 8192\n"
      "s_nop 0\n"
      "buffer_load_dwordx4 v120, s[20:23], s60 offen nt lds\n"
      "v_xor_b32 v231, 0x400, v229\n"
      "v_and_b32 v119, 0xfffffbff, v225\n"
      "v_cmp_eq_u32 vcc, 0, v112\n"
      "v_mfma_f32_32x32x16_bf16 a[128:143], v[216:219], v[216:219], 0\n"
      "s_nop 0\n"
      "v_cndmask_b32 v114, 0, v113, vcc\n"
      "v_accvgpr_write_b32 a196, v114\n"
      "v_accvgpr_write_b32 a197, v114\n"
      "v_accvgpr_write_b32 a198, v114\n"
      "s_add_u32 m0, s62, 9088\n"
      "s_nop 0\n"
      "buffer_load_dwordx4 v120, s[20:23], s60 offen offset:128 nt lds\n"
      "v_accvgpr_write_b32 a199, v114\n"
      "v_cmp_eq_u32 vcc, 4, v112\n"
      "v_mfma_f32_32x32x16_bf16 a[144:159], v[216:219], v[216:219], 0\n"
      "s_nop 0\n"
      "v_cndmask_b32 v114, 0, v113, vcc\n"
      "v_accvgpr_write_b32 a200, v114\n"
      "v_accvgpr_write_b32 a201, v114\n"
      "v_accvgpr_write_b32 a202, v114\n"
      "v_accvgpr_write_b32 a203, v114\n"
      "s_add_u32 m0, %[tv], 0\n"
      "s_nop 0\n"
      "buffer_load_dwordx4 v249, s[84:87], s75 offen nt lds\n"
      "v_cmp_eq_u32 vcc, 8, v112\n"
      "v_mfma_f32_32x32x16_bf16 a[160:175], v[216:219], v[216:219], 0\n"
      "s_nop 0\n"
      "v_cndmask_b32 v114, 0, v113, vcc\n"
      "v_accvgpr_write_b32 a204, v114\n"
      "v_accvgpr_write_b32 a205, v114\n"
      "v_accvgpr_write_b32 a206, v114\n"
      "v_accvgpr_write_b32 a207, v114\n"
      "v_cmp_eq_u32 vcc, 12, v112\n"
      "s_add_u32 m0, %[tv], 1024\n"
      "s_nop 0\n"
      "buffer_load_dwordx4 v121, s[84:87], s75 offen nt lds\n"
      "v_mfma_f32_32x32x16_bf16 a[176:191], v[216:219], v[216:219], 0\n"
      "s_nop 0\n"
      "v_cndmask_b32 v114, 0, v113, vcc\n"
      "v_accvgpr_write_b32 a208, v114\n"
      "v_accvgpr_write_b32 a209, v114\n"
      "v_accvgpr_write_b32 a210, v114\n"
      "v_accvgpr_write_b32 a211, v114\n"
      "s_add_u32 s62, s62, 0x8000\n"
      "s_and_b32 s62, s62, 0x1ffff\n"
      "s_add_u32 s60, s60, s78\n"
      "s_cmp_lt_u32 %[rep], 128\n"
      "s_cselect_b32 s89, 0x2ffe, 0\n"
      "s_or_b32 s89, s89, 0x2aae\n"
      "s_waitcnt vmcnt(10)\n"
      "s_barrier\n"
      "s_memrealtime s[68:69]\n"
      "ds_read_b64 v[208:209], v115 offset:0\n"
      "ds_read_b64 v[210:211], v115 offset:32\n"
      "ds_read_b64 v[212:213], v115 offset:1024\n"
      "ds_read_b64 v[214:215], v115 offset:1056\n"
      "ds_read_b128 a[64:67], v228 offset:0\n"
      "ds_read_b128 a[68:71], v228 offset:4096\n"
      "ds_read_b128 v[128:131], v224 offset:0\n"
      "ds_read_b128 v[132:135], v224 offset:4096\n"
      "ds_read_b128 v[136:139], v224 offset:8192\n"
      "ds_read_b128 v[140:143], v224 offset:12288\n"
      "ds_read_b128 a[72:75], v229 offset:0\n"
      "ds_read_b128 a[76:79], v229 offset:4096\n"
      "ds_read_b128 v[144:147], v225 offset:0\n"
      "ds_read_b128 v[148:151], v225 offset:4096\n"
      "s_waitcnt lgkmcnt(10)\n"
      "ds_read_b128 v[152:155], v225 offset:8192\n"
      "ds_read_b128 v[156:159], v225 offset:12288\n"
      "v_lshlrev_b32 v96, 16, v208\n"
      "v_and_b32 v97, 0xffff0000, v208\n"
      "v_lshlrev_b32 v98, 16, v209\n"
      "v_and_b32 v99, 0xffff0000, v209\n"
      "v_lshlrev_b32 v100, 16, v210\n"
      "v_and_b32 v101, 0xffff0000, v210\n"
      "v_lshlrev_b32 v102, 16, v211\n"
      "v_and_b32 v103, 0xffff0000, v211\n"
      "v_lshlrev_b32 v104, 16, v212\n"
      "v_and_b32 v105, 0xffff0000, v212\n"
      "v_lshlrev_b32 v106, 16, v213\n"
      "v_and_b32 v107, 0xffff0000, v213\n"
      "v_lshlrev_b32 v108, 16, v214\n"
      "v_and_b32 v109, 0xffff0000, v214\n"
      "v_lshlrev_b32 v110, 16, v215\n"
      "v_and_b32 v111, 0xffff0000, v215\n"
      "v_fma_f32 v96, -v96, s76, s76\n"
      "v_fma_f32 v97, -v97, s76, s76\n"
      "v_fma_f32 v98, -v98, s76, s76\n"
      "v_fma_f32 v99, -v99, s76, s76\n"
      "v_fma_f32 v100, -v100, s76, s76\n"
      "v_fma_f32 v101, -v101, s76, s76\n"
      "v_fma_f32 v102, -v102, s76, s76\n"
      "v_fma_f32 v103, -v103, s76, s76\n"
      "v_fma_f32 v104, -v104, s76, s76\n"
      "v_fma_f32 v105, -v105, s76, s76\n"
      "v_fma_f32 v106, -v106, s76, s76\n"
      "v_fma_f32 v107, -v107, s76, s76\n"
      "v_fma_f32 v108, -v108, s76, s76\n"
      "v_fma_f32 v109, -v109, s76, s76\n"
      "v_fma_f32 v110, -v110, s76, s76\n"
      "v_fma_f32 v111, -v111, s76, s76\n"
      "s_mov_b32 s86, 110\n"
      "s_mov_b32 s87, 0\n"
      "s_waitcnt lgkmcnt(0)\n"
      "ds_read_b128 a[80:83], v230 offset:0\n"
      "ds_read_b128 a[84:87], v230 offset:4096\n"
      "ds_read_b128 a[32:35], v226 offset:0\n"
      "ds_read_b128 a[36:39], v226 offset:4096\n"
      "ds_read_b128 a[40:43], v226 offset:8192\n"
      "ds_read_b128 a[44:47], v226 offset:12288\n"
      "ds_read_b128 a[88:91], v231 offset:0\n"
      "ds_read_b128 a[92:95], v231 offset:4096\n"
      "ds_read_b128 a[48:51], v227 offset:0\n"
      "ds_read_b128 a[52:55], v227 offset:4096\n"
      "ds_read_b128 a[56:59], v227 offset:8192\n"
      "ds_read_b128 a[60:63], v227 offset:12288\n"
      "global_load_dwordx2 v[240:241], v240, s[18:19] sc1\n"
      "s_mov_b32 s28, s18\n"
      "s_and_b32 s29, s19, 0xffff\n"
      "s_cmp_lt_u32 %[lead], 32\n"
      "s_cbranch_scc0 NOLEAD_%=\n"
      "v_mov_b32 v112, s68\n"
      "buffer_store_dword v112, v250, s[28:31], %[lead] offen sc1\n"
      "NOLEAD_%=:\n"
      "v_mfma_f32_16x16x32_bf16 v[160:163], a[64:67], v[128:131], v[96:99]\n"
      "s_mov_b32 s83, 0\n"
      "v_lshlrev_b32 v232, 16, v128\n"
      "v_and_b32 v233, s95, v128\n"
      "v_mfma_f32_16x16x32_bf16 v[168:171], a[64:67], v[132:135], v[104:107]\n"
      "s_sub_u32 s67, s68, s82\n"
      "v_lshlrev_b32 v234, 16, v129\n"
      "v_and_b32 v235, s95, v129\n"
      "v_mul_f32 v232, s94, v232\n"
      "v_mul_f32 v233, s94, v233\n"
      "v_mfma_f32_16x16x32_bf16 v[176:179], a[64:67], v[136:139], v[96:99]\n"
      "s_min_u32 s67, s67, 140\n"
      "v_lshlrev_b32 v236, 16, v130\n"
      "v_and_b32 v237, s95, v130\n"
      "v_mul_f32 v234, s94, v234\n"
      "v_mul_f32 v235, s94, v235\n"
      "v_cvt_pk_bf16_f32 v0, v232, v233\n"
      "v_mfma_f32_16x16x32_bf16 v[184:187], a[64:67], v[140:143], v[104:107]\n"
      "s_add_u32 s66, s82, s67\n"
      "v_lshlrev_b32 v238, 16, v131\n"
      "v_and_b32 v239, s95, v131\n"
      "v_mul_f32 v236, s94, v236\n"
      "v_mul_f32 v237, s94, v237\n"
      "v_cvt_pk_bf16_f32 v1, v234, v235\n"
      "v_mfma_f32_16x16x32_bf16 v[164:167], a[68:71], v[128:131], v[100:103]\n"
      "s_add_u32 s66, s66, 130\n"
      "v_lshlrev_b32 v232, 16, v132\n"
      "v_and_b32 v233, s95, v132\n"
      "v_mul_f32 v238, s94, v238\n"
      "v_mul_f32 v239, s94, v239\n"
      "v_cvt_pk_bf16_f32 v2, v236, v237\n"
      "v_mfma_f32_16x16x32_bf16 v[172:175], a[68:71], v[132:135], v[108:111]\n"
      "s_sub_u32 s71, s68, 400\n"
      "v_lshlrev_b32 v234, 16, v133\n"
      "v_and_b32 v235, s95, v133\n"
      "v_mul_f32 v232, s94, v232\n"
      "v_mul_f32 v233, s94, v233\n"
      "v_cvt_pk_bf16_f32 v3, v238, v239\n"
      "v_mfma_f32_16x16x32_bf16 v[180:183], a[68:71], v[136:139], v[100:103]\n"
      "s_mov_b32 s68, s66\n"
      "v_lshlrev_b32 v236, 16, v134\n"
      "v_and_b32 v237, s95, v134\n"
      "v_mul_f32 v234, s94, v234\n"
      "v_mul_f32 v235, s94, v235\n"
      "v_cvt_pk_bf16_f32 v4, v232, v233\n"
      "v_mfma_f32_16x16x32_bf16 v[188:191], a[68:71], v[140:143], v[108:111]\n"
      "s_mov_b32 s82, s66\n"
      "v_lshlrev_b32 v238, 16, v135\n"
      "v_and_b32 v239, s95, v135\n"
      "v_mul_f32 v236, s94, v236\n"
      "v_mul_f32 v237, s94, v237\n"
      "v_cvt_pk_bf16_f32 v5, v234, v235\n"
      "v_mfma_f32_16x16x32_bf16 v[160:163], a[72:75], v[144:147], v[160:163]\n"
      "v_lshlrev_b32 v232, 16, v136\n"
      "v_and_b32 v233, s95, v136\n"
      "v_mul_f32 v238, s94, v238\n"
      "v_mul_f32 v239, s94, v239\n"
      "v_cvt_pk_bf16_f32 v6, v236, v237\n"
      "v_mfma_f32_16x16x32_bf16 v[168:171], a[72:75], v[148:151], v[168:171]\n"
      "v_lshlrev_b32 v234, 16, v137\n"
      "v_and_b32 v235, s95, v137\n"
      "v_mul_f32 v232, s94, v232\n"
      "v_mul_f32 v233, s94, v233\n"
      "v_cvt_pk_bf16_f32 v7, v238, v239\n"
      "s_waitcnt lgkmcnt(0)\n"
      "s_barrier\n"
      "v_mfma_f32_16x16x32_bf16 v[176:179], a[72:75], v[152:155], v[176:179]\n"
      "v_lshlrev_b32 v236, 16, v138\n"
      "v_and_b32 v237, s95, v138\n"
      "v_mul_f32 v234, s94, v234\n"
      "v_mul_f32 v235, s94, v235\n"
      "v_cvt_pk_bf16_f32 v8, v232, v233\n"
      "ds_write_b128 v224, v[0:3] offset:0\n"
      "v_mfma_f32_16x16x32_bf16 v[184:187], a[72:75], v[156:159], v[184:187]\n"
      "s_add_u32 m0, s62, 0\n"
      "v_lshlrev_b32 v238, 16, v139\n"
      "v_and_b32 v239, s95, v139\n"
      "v_mul_f32 v236, s94, v236\n"
      "v_mul_f32 v237, s94, v237\n"
      "v_cvt_pk_bf16_f32 v9, v234, v235\n"
      "v_mfma_f32_16x16x32_bf16 v[164:167], a[76:79], v[144:147], v[164:167]\n"
      "v_lshlrev_b32 v232, 16, v140\n"
      "v_and_b32 v233, s95, v140\n"
      "v_mul_f32 v238, s94, v238\n"
      "v_mul_f32 v239, s94, v239\n"
      "v_cvt_pk_bf16_f32 v10, v236, v237\n"
      "buffer_load_dwordx4 v248, s[20:23], s60 offen nt lds\n"
      "ds_write_b128 v224, v[4:7] offset:4096\n"
      "v_mfma_f32_16x16x32_bf16 v[172:175], a[76:79], v[148:151], v[172:175]\n"
      "s_add_u32 m0, s62, 896\n"
      "v_lshlrev_b32 v234, 16, v141\n"
      "v_and_b32 v235, s95, v141\n"
      "v_mul_f32 v232, s94, v232\n"
      "v_mul_f32 v233, s94, v233\n"
      "v_cvt_pk_bf16_f32 v11, v238, v239\n"
      "v_mfma_f32_16x16x32_bf16 v[180:183], a[76:79], v[152:155], v[180:183]\n"
      "v_lshlrev_b32 v236, 16, v142\n"
      "v_and_b32 v237, s95, v142\n"
      "v_mul_f32 v234, s94, v234\n"
      "v_mul_f32 v235, s94, v235\n"
      "v_cvt_pk_bf16_f32 v12, v232, v233\n"
      "buffer_load_dwordx4 v248, s[20:23], s60 offen offset:128 nt lds\n"
      "ds_write_b128 v224, v[8:11] offset:8192\n"
      "v_mfma_f32_16x16x32_bf16 v[188:191], a[76:79], v[156:159], v[188:191]\n"
      "s_add_u32 m0, s62, 8192\n"
      "v_lshlrev_b32 v238, 16, v143\n"
      "v_and_b32 v239, s95, v143\n"
      "v_mul_f32 v236, s94, v236\n"
      "v_mul_f32 v237, s94, v237\n"
      "v_cvt_pk_bf16_f32 v13, v234, v235\n"
      "v_mfma_f32_16x16x32_bf16 v[160:163], a[80:83], a[32:35], v[160:163]\n"
      "v_lshlrev_b32 v232, 16, v144\n"
      "v_and_b32 v233, s95, v144\n"
      "v_mul_f32 v238, s94, v238\n"
      "v_mul_f32 v239, s94, v239\n"
      "v_cvt_pk_bf16_f32 v14, v236, v237\n"
      "buffer_load_dwordx4 v120, s[20:23], s60 offen nt lds\n"
      "v_mfma_f32_16x16x32_bf16 v[168:171], a[80:83], a[36:39], v[168:171]\n"
      "s_add_u32 m0, s62, 9088\n"
      "v_lshlrev_b32 v234, 16, v145\n"
      "v_and_b32 v235, s95, v145\n"
      "v_mul_f32 v232, s94, v232\n"
      "v_mul_f32 v233, s94, v233\n"
      "v_cvt_pk_bf16_f32 v15, v238, v239\n"
      "ds_write_b128 v224, v[12:15] offset:12288\n"
      "v_mfma_f32_16x16x32_bf16 v[176:179], a[80:83], a[40:43], v[176:179]\n"
      "s_add_u32 s62, s62, 0x8000\n"
      "v_lshlrev_b32 v236, 16, v146\n"
      "v_and_b32 v237, s95, v146\n"
      "v_mul_f32 v234, s94, v234\n"
      "v_mul_f32 v235, s94, v235\n"
      "v_cvt_pk_bf16_f32 v16, v232, v233\n"
      "buffer_load_dwordx4 v120, s[20:23], s60 offen offset:128 nt lds\n"
      "v_mfma_f32_16x16x32_bf16 v[184:187], a[80:83], a[44:47], v[184:187]\n"
      "s_and_b32 s62, s62, 0x1ffff\n"
      "v_lshlrev_b32 v238, 16, v147\n"
      "v_and_b32 v239, s95, v147\n"
      "v_mul_f32 v236, s94, v236\n"
      "v_mul_f32 v237, s94, v237\n"
      "v_cvt_pk_bf16_f32 v17, v234, v235\n"
      "v_mfma_f32_16x16x32_bf16 v[164:167], a[84:87], a[32:35], v[164:167]\n"
      "s_add_u32 s60, s60, %[inc]\n"
      "v_lshlrev_b32 v232, 16, v148\n"
      "v_and_b32 v233, s95, v148\n"
      "v_mul_f32 v238, s94, v238\n"
      "v_mul_f32 v239, s94, v239\n"
      "v_cvt_pk_bf16_f32 v18, v236, v237\n"
      "v_mfma_f32_16x16x32_bf16 v[172:175], a[84:87], a[36:39], v[172:175]\n"
      "v_lshlrev_b32 v234, 16, v149\n"
      "v_and_b32 v235, s95, v149\n"
      "v_mul_f32 v232, s94, v232\n"
      "v_mul_f32 v233, s94, v233\n"
      "v_cvt_pk_bf16_f32 v19, v238, v239\n"
      "ds_write_b128 v225, v[16:19] offset:0\n"
      "v_mfma_f32_16x16x32_bf16 v[180:183], a[84:87], a[40:43], v[180:183]\n"
      "v_lshlrev_b32 v236, 16, v150\n"
      "v_and_b32 v237, s95, v150\n"
      "v_mul_f32 v234, s94, v234\n"
      "v_mul_f32 v235, s94, v235\n"
      "v_cvt_pk_bf16_f32 v20, v232, v233\n"
      "v_mfma_f32_16x16x32_bf16 v[188:191], a[84:87], a[44:47], v[188:191]\n"
      "v_lshlrev_b32 v238, 16, v151\n"
      "v_and_b32 v239, s95, v151\n"
      "v_mul_f32 v236, s94, v236\n"
      "v_mul_f32 v237, s94, v237\n"
      "v_cvt_pk_bf16_f32 v21, v234, v235\n"
      "v_mfma_f32_16x16x32_bf16 v[160:163], a[88:91], a[48:51], v[160:163]\n"
      "v_lshlrev_b32 v232, 16, v152\n"
      "v_and_b32 v233, s95, v152\n"
      "v_mul_f32 v238, s94, v238\n"
      "v_mul_f32 v239, s94, v239\n"
      "v_cvt_pk_bf16_f32 v22, v236, v237\n"
      "v_mfma_f32_16x16x32_bf16 v[168:171], a[88:91], a[52:55], v[168:171]\n"
      "v_lshlrev_b32 v234, 16, v153\n"
      "v_and_b32 v235, s95, v153\n"
      "v_mul_f32 v232, s94, v232\n"
      "v_mul_f32 v233, s94, v233\n"
      "v_cvt_pk_bf16_f32 v23, v238, v239\n"
      "ds_write_b128 v225, v[20:23] offset:4096\n"
      "v_mfma_f32_16x16x32_bf16 v[176:179], a[88:91], a[56:59], v[176:179]\n"
      "v_lshlrev_b32 v236, 16, v154\n"
      "v_and_b32 v237, s95, v154\n"
      "v_mul_f32 v234, s94, v234\n"
      "v_mul_f32 v235, s94, v235\n"
      "v_cvt_pk_bf16_f32 v24, v232, v233\n"
      "v_mfma_f32_16x16x32_bf16 v[184:187], a[88:91], a[60:63], v[184:187]\n"
      "v_lshlrev_b32 v238, 16, v155\n"
      "v_and_b32 v239, s95, v155\n"
      "v_mul_f32 v236, s94, v236\n"
      "v_mul_f32 v237, s94, v237\n"
      "v_cvt_pk_bf16_f32 v25, v234, v235\n"
      "v_mfma_f32_16x16x32_bf16 v[164:167], a[92:95], a[48:51], v[164:167]\n"
      "v_lshlrev_b32 v232, 16, v156\n"
      "v_and_b32 v233, s95, v156\n"
      "v_mul_f32 v238, s94, v238\n"
      "v_mul_f32 v239, s94, v239\n"
      "v_cvt_pk_bf16_f32 v26, v236, v237\n"
      "v_mfma_f32_16x16x32_bf16 v[172:175], a[92:95], a[52:55], v[172:175]\n"
      "v_lshlrev_b32 v234, 16, v157\n"
      "v_and_b32 v235, s95, v157\n"
      "v_mul_f32 v232, s94, v232\n"
      "v_mul_f32 v233, s94, v233\n"
      "v_cvt_pk_bf16_f32 v27, v238, v239\n"
      "ds_write_b128 v225, v[24:27] offset:8192\n"
      "v_mfma_f32_16x16x32_bf16 v[180:183], a[92:95], a[56:59], v[180:183]\n"
      "v_lshlrev_b32 v236, 16, v158\n"
      "v_and_b32 v237, s95, v158\n"
      "v_mul_f32 v234, s94, v234\n"
      "v_mul_f32 v235, s94, v235\n"
      "v_cvt_pk_bf16_f32 v28, v232, v233\n"
      "v_mfma_f32_16x16x32_bf16 v[188:191], a[92:95], a[60:63], v[188:191]\n"
      "v_lshlrev_b32 v238, 16, v159\n"
      "v_and_b32 v239, s95, v159\n"
      "v_mul_f32 v236, s94, v236\n"
      "v_mul_f32 v237, s94, v237\n"
      "v_cvt_pk_bf16_f32 v29, v234, v235\n"
      "v_mul_f32 v238, s94, v238\n"
      "v_mul_f32 v239, s94, v239\n"
      "v_cvt_pk_bf16_f32 v30, v236, v237\n"
      "v_cvt_pk_bf16_f32 v31, v238, v239\n"
      "ds_write_b128 v225, v[28:31] offset:12288\n"
      "v_max3_f32 v112, v160, v161, v162\n"
      "v_max3_f32 v113, v168, v169, v170\n"
      "v_max3_f32 v114, v176, v177, v178\n"
      "v_max3_f32 v115, v184, v185, v186\n"
      "v_max3_f32 v112, v112, v163, v164\n"
      "v_max3_f32 v113, v113, v171, v172\n"
      "v_max3_f32 v114, v114, v179, v180\n"
      "v_max3_f32 v115, v115, v187, v188\n"
      "v_max3_f32 v112, v112, v165, v166\n"
      "v_max3_f32 v113, v113, v173, v174\n"
      "v_max3_f32 v114, v114, v181, v182\n"
      "v_max3_f32 v115, v115, v189, v190\n"
      "s_waitcnt lgkmcnt(0)\n"
      "s_barrier\n"
      "ds_read_b128 a[0:3], v118 offset:0\n"
      "ds_read_b128 a[4:7], v118 offset:4096\n"
      "ds_read_b128 a[8:11], v118 offset:8192\n"
      "ds_read_b128 a[12:15], v118 offset:12288\n"
      "ds_read_b128 a[16:19], v119 offset:0\n"
      "ds_read_b128 a[20:23], v119 offset:4096\n"
      "ds_read_b128 a[24:27], v119 offset:8192\n"
      "ds_read_b128 a[28:31], v119 offset:12288\n"
      "ds_read_b128 a[32:35], v118 offset:1024\n"
      "ds_read_b128 a[36:39], v118 offset:5120\n"
      "ds_read_b128 a[40:43], v118 offset:9216\n"
      "ds_read_b128 a[44:47], v118 offset:13312\n"
      "v_max_f32 v112, v112, v167\n"
      "v_max_f32 v113, v113, v175\n"
      "v_max_f32 v114, v114, v183\n"
      "v_max_f32 v115, v115, v191\n"
      "v_mov_b32 v208, v112\n"
      "v_mov_b32 v209, v113\n"
      "v_mov_b32 v210, v114\n"
      "v_mov_b32 v211, v115\n"
      "v_permlane16_swap_b32 v112, v208\n"
      "v_permlane16_swap_b32 v113, v209\n"
      "v_permlane16_swap_b32 v114, v210\n"
      "v_permlane16_swap_b32 v115, v211\n"
      "s_waitcnt lgkmcnt(8)\n"
      "ds_read_b128 a[48:51], v119 offset:1024\n"
      "ds_read_b128 a[52:55], v119 offset:5120\n"
      "ds_read_b128 a[56:59], v119 offset:9216\n"
      "ds_read_b128 a[60:63], v119 offset:13312\n"
      "v_max_f32 v112, v112, v208\n"
      "v_max_f32 v113, v113, v209\n"
      "v_max_f32 v114, v114, v210\n"
      "v_max_f32 v115, v115, v211\n"
      "v_mov_b32 v208, v112\n"
      "v_mov_b32 v209, v113\n"
      "v_mov_b32 v210, v114\n"
      "v_mov_b32 v211, v115\n"
      "v_permlane32_swap_b32 v112, v208\n"
      "v_permlane32_swap_b32 v113, v209\n"
      "v_permlane32_swap_b32 v114, v210\n"
      "v_permlane32_swap_b32 v115, v211\n"
      "v_max_f32 v112, v112, v208\n"
      "v_max_f32 v113, v113, v209\n"
      "v_max_f32 v114, v114, v210\n"
      "v_max_f32 v115, v115, v211\n"
      "v_mov_b32 v116, 0xc1800000\n"
      "v_fmamk_f32 v112, v112, 0xbe0293ee, v116\n"
      "v_fmamk_f32 v113, v113, 0xbe0293ee, v116\n"
      "v_fmamk_f32 v114, v114, 0xbe0293ee, v116\n"
      "v_fmamk_f32 v115, v115, 0xbe0293ee, v116\n"
      "v_floor_f32 v112, v112\n"
      "v_floor_f32 v113, v113\n"
      "v_floor_f32 v114, v114\n"
      "v_floor_f32 v115, v115\n"
      "s_waitcnt vmcnt(11) lgkmcnt(0)\n"
      "s_barrier\n"
      "ds_read_b128 a[64:67], v242 offset:0\n"
      "ds_read_b128 a[68:71], v242 offset:4096\n"
      "ds_read_b128 a[72:75], v243 offset:0\n"
      "ds_read_b128 a[76:79], v243 offset:4096\n"
      "ds_read_b128 a[80:83], v242 offset:1024\n"
      "ds_read_b128 a[84:87], v242 offset:5120\n"
      "ds_read_b128 a[88:91], v243 offset:1024\n"
      "ds_read_b128 a[92:95], v243 offset:5120\n"
      "v_min_f32 v224, 0x7f000000, v112\n"
      "v_min_f32 v228, 0x7f000000, v113\n"
      "v_min_f32 v232, 0x7f000000, v114\n"
      "v_min_f32 v236, 0x7f000000, v115\n"
      "v_add_f32 v112, %[neg], v224\n"
      "v_add_f32 v113, %[neg], v228\n"
      "v_add_f32 v114, %[neg], v232\n"
      "v_add_f32 v115, %[neg], v236\n"
      "v_mov_b32 v225, v224\n"
      "v_mov_b32 v229, v228\n"
      "v_mov_b32 v233, v232\n"
      "v_mov_b32 v237, v236\n"
      "v_mov_b64 v[226:227], v[224:225]\n"
      "v_mov_b64 v[230:231], v[228:229]\n"
      "v_mov_b64 v[234:235], v[232:233]\n"
      "v_mov_b64 v[238:239], v[236:237]\n"
      "s_mov_b32 s95, s94\n"
      "s_add_u32 m0, s77, 16384\n"
      "v_pk_fma_f32 v[160:161], v[160:161], s[94:95], v[112:113] op_sel:[0,0,0] op_sel_hi:[1,1,0]\n"
      "buffer_load_dwordx4 v249, s[24:27], s61 offen nt lds\n"
      "v_pk_fma_f32 v[162:163], v[162:163], s[94:95], v[112:113] op_sel:[0,0,0] op_sel_hi:[1,1,0]\n"
      "v_pk_fma_f32 v[164:165], v[164:165], s[94:95], v[112:113] op_sel:[0,0,0] op_sel_hi:[1,1,0]\n"
      "v_pk_fma_f32 v[166:167], v[166:167], s[94:95], v[112:113] op_sel:[0,0,0] op_sel_hi:[1,1,0]\n"
      "s_add_u32 m0, s77, 17408\n"
      "v_pk_fma_f32 v[168:169], v[168:169], s[94:95], v[112:113] op_sel:[0,0,1] op_sel_hi:[1,1,1]\n"
      "buffer_load_dwordx4 v121, s[24:27], s61 offen nt lds\n"
      "v_pk_fma_f32 v[170:171], v[170:171], s[94:95], v[112:113] op_sel:[0,0,1] op_sel_hi:[1,1,1]\n"
      "v_pk_fma_f32 v[172:173], v[172:173], s[94:95], v[112:113] op_sel:[0,0,1] op_sel_hi:[1,1,1]\n"
      "v_pk_fma_f32 v[174:175], v[174:175], s[94:95], v[112:113] op_sel:[0,0,1] op_sel_hi:[1,1,1]\n"
      "s_add_u32 m0, s77, 24576\n"
      "v_pk_fma_f32 v[176:177], v[176:177], s[94:95], v[114:115] op_sel:[0,0,0] op_sel_hi:[1,1,0]\n"
      "buffer_load_dwordx4 v122, s[24:27], s61 offen nt lds\n"
      "v_pk_fma_f32 v[178:179], v[178:179], s[94:95], v[114:115] op_sel:[0,0,0] op_sel_hi:[1,1,0]\n"
      "v_pk_fma_f32 v[180:181], v[180:181], s[94:95], v[114:115] op_sel:[0,0,0] op_sel_hi:[1,1,0]\n"
      "v_pk_fma_f32 v[182:183], v[182:183], s[94:95], v[114:115] op_sel:[0,0,0] op_sel_hi:[1,1,0]\n"
      "s_add_u32 m0, s77, 25600\n"
      "v_pk_fma_f32 v[184:185], v[184:185], s[94:95], v[114:115] op_sel:[0,0,1] op_sel_hi:[1,1,1]\n"
      "buffer_load_dwordx4 v123, s[24:27], s61 offen nt lds\n"
      "v_pk_fma_f32 v[186:187], v[186:187], s[94:95], v[114:115] op_sel:[0,0,1] op_sel_hi:[1,1,1]\n"
      "v_pk_fma_f32 v[188:189], v[188:189], s[94:95], v[114:115] op_sel:[0,0,1] op_sel_hi:[1,1,1]\n"
      "v_pk_fma_f32 v[190:191], v[190:191], s[94:95], v[114:115] op_sel:[0,0,1] op_sel_hi:[1,1,1]\n"
      "s_add_u32 s77, s77, 0x8000\n"
      "s_and_b32 s77, s77, 0x1ffff\n"
      "s_add_u32 s61, s61, %[inc]\n"
      "v_exp_f32 v112, v160\n"
      "v_exp_f32 v113, v161\n"
      "v_exp_f32 v114, v162\n"
      "v_cvt_pk_bf16_f32 v208, v112, v113\n"
      "v_exp_f32 v115, v163\n"
      "v_exp_f32 v112, v164\n"
      "v_cvt_pk_bf16_f32 v209, v114, v115\n"
      "v_exp_f32 v113, v165\n"
      "v_or_b32 v116, v208, v209\n"
      "v_exp_f32 v114, v166\n"
      "v_cvt_pk_bf16_f32 v210, v112, v113\n"
      "v_exp_f32 v115, v167\n"
      "v_exp_f32 v112, v168\n"
      "v_cvt_pk_bf16_f32 v211, v114, v115\n"
      "v_exp_f32 v113, v169\n"
      "v_or3_b32 v116, v116, v210, v211\n"
      "v_exp_f32 v114, v170\n"
      "v_cvt_pk_bf16_f32 v212, v112, v113\n"
      "v_exp_f32 v115, v171\n"
      "v_exp_f32 v112, v172\n"
      "v_cvt_pk_bf16_f32 v213, v114, v115\n"
      "v_exp_f32 v113, v173\n"
      "v_or3_b32 v116, v116, v212, v213\n"
      "s_waitcnt lgkmcnt(0)\n"
      "v_cvt_pk_bf16_f32 v214, v112, v113\n"
      "s_sub_u32 s63, %[n1], 1\n"
      "s_branch ENTRY_%=\n"
      "LOOP1_%=:\n"
      "s_waitcnt vmcnt(12) lgkmcnt(0)\n"
      "s_sub_u32 s67, s68, s90\n"
      "s_cmp_lt_i32 s67, 0\n"
      "s_cbranch_scc1 GW0_%=\n"
      "GX0_%=:\n"
      "s_barrier\n"
      "v_mfma_f32_16x16x32_bf16 v[40:43], a[96:99], v[216:219], v[40:43]\n"
      "s_memrealtime s[68:69]\n"
      "buffer_load_dword v250, v251, s[28:31], 0 offen sc1\n"
      "ds_read_b128 a[64:67], v242 offset:0\n"
      "v_exp_f32 v114, v166\n"
      "v_mfma_f32_16x16x32_bf16 v[44:47], a[96:99], v[220:223], v[44:47]\n"
      "s_add_u32 m0, s77, 16384\n"
      "ds_read_b128 a[68:71], v242 offset:4096\n"
      "ds_read_b128 a[72:75], v243 offset:0\n"
      "v_mfma_f32_16x16x32_bf16 v[60:63], a[100:103], v[220:223], v[60:63]\n"
      "buffer_load_dwordx4 v249, s[24:27], s61 offen nt lds\n"
      "v_exp_f32 v115, v167\n"
      "v_mfma_f32_16x16x32_bf16 v[56:59], a[100:103], v[216:219], v[56:59]\n"
      "s_add_u32 m0, s77, 17408\n"
      "ds_read_b128 a[76:79], v243 offset:4096\n"
      "v_cvt_pk_bf16_f32 v211, v114, v115\n"
      "v_mfma_f32_16x16x32_bf16 a[136:139], a[104:107], v[216:219], a[136:139]\n"
      "buffer_load_dwordx4 v121, s[24:27], s61 offen nt lds\n"
      "v_exp_f32 v112, v168\n"
      "v_mfma_f32_16x16x32_bf16 a[140:143], a[104:107], v[220:223], a[140:143]\n"
      "s_add_u32 m0, s77, 24576\n"
      "ds_read_b128 a[80:83], v242 offset:1024\n"
      "v_or3_b32 v116, v116, v210, v211\n"
      "v_mfma_f32_16x16x32_bf16 a[156:159], a[108:111], v[220:223], a[156:159]\n"
      "buffer_load_dwordx4 v122, s[24:27], s61 offen nt lds\n"
      "v_exp_f32 v113, v169\n"
      "v_mfma_f32_16x16x32_bf16 a[152:155], a[108:111], v[216:219], a[152:155]\n"
      "s_add_u32 m0, s77, 25600\n"
      "ds_read_b128 a[84:87], v242 offset:5120\n"
      "v_cvt_pk_bf16_f32 v212, v112, v113\n"
      "v_mfma_f32_16x16x32_bf16 v[72:75], a[112:115], v[216:219], v[72:75]\n"
      "s_add_u32 s77, s77, 0x8000\n"
      "buffer_load_dwordx4 v123, s[24:27], s61 offen nt lds\n"
      "v_exp_f32 v114, v170\n"
      "v_mfma_f32_16x16x32_bf16 v[76:79], a[112:115], v[220:223], v[76:79]\n"
      "s_and_b32 s77, s77, 0x1ffff\n"
      "ds_read_b128 a[88:91], v243 offset:1024\n"
      "ds_read_b128 a[92:95], v243 offset:5120\n"
      "v_mfma_f32_16x16x32_bf16 v[92:95], a[116:119], v[220:223], v[92:95]\n"
      "s_add_u32 s61, s61, %[inc]\n"
      "v_exp_f32 v115, v171\n"
      "v_mfma_f32_16x16x32_bf16 v[88:91], a[116:119], v[216:219], v[88:91]\n"
      "v_cvt_pk_bf16_f32 v213, v114, v115\n"
      "v_mfma_f32_16x16x32_bf16 a[168:171], a[120:123], v[216:219], a[168:171]\n"
      "v_exp_f32 v112, v172\n"
      "v_mfma_f32_16x16x32_bf16 a[172:175], a[120:123], v[220:223], a[172:175]\n"
      "v_or3_b32 v116, v116, v212, v213\n"
      "v_mfma_f32_16x16x32_bf16 a[188:191], a[124:127], v[220:223], a[188:191]\n"
      "v_exp_f32 v113, v173\n"
      "v_mfma_f32_16x16x32_bf16 a[184:187], a[124:127], v[216:219], a[184:187]\n"
      "s_waitcnt lgkmcnt(0)\n"
      "v_cvt_pk_bf16_f32 v214, v112, v113\n"
      "ENTRY_%=:\n"
      "v_mfma_f32_16x16x32_bf16 v[128:131], a[64:67], a[0:3], v[224:227]\n"
      "s_sub_u32 s67, s68, s66\n"
      "v_exp_f32 v114, v174\n"
      "v_mfma_f32_16x16x32_bf16 v[128:131], a[72:75], a[16:19], v[128:131]\n"
      "s_cmp_gt_i32 s67, s70\n"
      "ds_read_b64_tr_b16 a[96:97], v244 offset:0\n"
      "ds_read_b64_tr_b16 a[98:99], v244 offset:4096\n"
      "v_mfma_f32_16x16x32_bf16 v[128:131], a[80:83], a[32:35], v[128:131]\n"
      "s_cselect_b32 s69, s86, 0\n"
      "v_exp_f32 v115, v175\n"
      "v_mfma_f32_16x16x32_bf16 v[128:131], a[88:91], a[48:51], v[128:131]\n"
      "s_add_u32 s66, s66, s69\n"
      "ds_read_b64_tr_b16 a[100:101], v245 offset:0\n"
      "v_cvt_pk_bf16_f32 v215, v114, v115\n"
      "v_mfma_f32_16x16x32_bf16 v[132:135], a[92:95], a[48:51], v[224:227]\n"
      "s_add_u32 s67, s67, s101\n"
      "v_exp_f32 v112, v176\n"
      "v_mfma_f32_16x16x32_bf16 v[132:135], a[84:87], a[32:35], v[132:135]\n"
      "s_min_i32 s70, s70, s67\n"
      "ds_read_b64_tr_b16 a[102:103], v245 offset:4096\n"
      "v_or3_b32 v116, v116, v214, v215\n"
      "v_mfma_f32_16x16x32_bf16 v[132:135], a[76:79], a[16:19], v[132:135]\n"
      "s_max_i32 s70, s70, 85\n"
      "v_exp_f32 v113, v177\n"
      "v_mfma_f32_16x16x32_bf16 v[132:135], a[68:71], a[0:3], v[132:135]\n"
      "s_add_u32 s66, s66, s86\n"
      "ds_read_b64_tr_b16 a[104:105], v246 offset:0\n"
      "v_cvt_pk_bf16_f32 v216, v112, v113\n"
      "v_mfma_f32_16x16x32_bf16 v[140:143], a[68:71], a[4:7], v[228:231]\n"
      "s_and_b32 s76, s80, 31\n"
      "v_exp_f32 v114, v178\n"
      "v_mfma_f32_16x16x32_bf16 v[140:143], a[76:79], a[20:23], v[140:143]\n"
      "s_lshr_b64 s[80:81], s[80:81], 5\n"
      "ds_read_b64_tr_b16 a[106:107], v246 offset:4096\n"
      "ds_read_b64_tr_b16 a[108:109], v247 offset:0\n"
      "v_mfma_f32_16x16x32_bf16 v[140:143], a[84:87], a[36:39], v[140:143]\n"
      "s_add_u32 s66, s66, s76\n"
      "v_exp_f32 v115, v179\n"
      "v_mfma_f32_16x16x32_bf16 v[140:143], a[92:95], a[52:55], v[140:143]\n"
      "s_sub_u32 s67, s68, s82\n"
      "ds_read_b64_tr_b16 a[110:111], v247 offset:4096\n"
      "v_cvt_pk_bf16_f32 v217, v114, v115\n"
      "v_mfma_f32_16x16x32_bf16 v[136:139], a[88:91], a[52:55], v[228:231]\n"
      "s_min_i32 s76, s67, s83\n"
      "v_exp_f32 v112, v180\n"
      "v_mfma_f32_16x16x32_bf16 v[136:139], a[80:83], a[36:39], v[136:139]\n"
      "s_mov_b32 s83, s67\n"
      "ds_read_b64_tr_b16 a[112:113], v244 offset:1024\n"
      "v_or3_b32 v116, v116, v216, v217\n"
      "v_mfma_f32_16x16x32_bf16 v[136:139], a[72:75], a[20:23], v[136:139]\n"
      "s_sub_u32 s76, s76, 36\n"
      "v_exp_f32 v113, v181\n"
      "v_mfma_f32_16x16x32_bf16 v[136:139], a[64:67], a[4:7], v[136:139]\n"
      "s_min_i32 s76, s76, 148\n"
      "ds_read_b64_tr_b16 a[114:115], v244 offset:5120\n"
      "v_cvt_pk_bf16_f32 v218, v112, v113\n"
      "v_mfma_f32_16x16x32_bf16 v[144:147], a[64:67], a[8:11], v[232:235]\n"
      "s_max_i32 s76, s76, 70\n"
      "v_exp_f32 v114, v182\n"
      "v_mfma_f32_16x16x32_bf16 v[144:147], a[72:75], a[24:27], v[144:147]\n"
      "s_mov_b32 s82, s68\n"
      "ds_read_b64_tr_b16 a[116:117], v245 offset:1024\n"
      "ds_read_b64_tr_b16 a[118:119], v245 offset:5120\n"
      "v_mfma_f32_16x16x32_bf16 v[144:147], a[80:83], a[40:43], v[144:147]\n"
      "s_add_u32 s65, s82, s76\n"
      "v_exp_f32 v115, v183\n"
      "v_mfma_f32_16x16x32_bf16 v[144:147], a[88:91], a[56:59], v[144:147]\n"
      "s_sub_u32 s93, s93, 1\n"
      "ds_read_b64_tr_b16 a[120:121], v246 offset:1024\n"
      "v_cvt_pk_bf16_f32 v219, v114, v115\n"
      "v_mfma_f32_16x16x32_bf16 v[148:151], a[92:95], a[56:59], v[232:235]\n"
      "s_cmp_le_i32 s93, 14\n"
      "v_exp_f32 v112, v184\n"
      "v_mfma_f32_16x16x32_bf16 v[148:151], a[84:87], a[40:43], v[148:151]\n"
      "s_cselect_b32 s86, 107, s86\n"
      "ds_read_b64_tr_b16 a[122:123], v246 offset:5120\n"
      "v_or3_b32 v116, v116, v218, v219\n"
      "v_mfma_f32_16x16x32_bf16 v[148:151], a[76:79], a[24:27], v[148:151]\n"
      "s_sub_u32 s67, s65, s66\n"
      "v_exp_f32 v113, v185\n"
      "v_mfma_f32_16x16x32_bf16 v[148:151], a[68:71], a[8:11], v[148:151]\n"
      "s_cmp_lt_i32 s67, 0\n"
      "ds_read_b64_tr_b16 a[124:125], v247 offset:1024\n"
      "v_cvt_pk_bf16_f32 v220, v112, v113\n"
      "v_mfma_f32_16x16x32_bf16 v[156:159], a[68:71], a[12:15], v[236:239]\n"
      "s_cselect_b32 s88, s66, s65\n"
      "v_exp_f32 v114, v186\n"
      "v_mfma_f32_16x16x32_bf16 v[156:159], a[76:79], a[28:31], v[156:159]\n"
      "s_add_u32 s90, s88, s91\n"
      "ds_read_b64_tr_b16 a[126:127], v247 offset:5120\n"
      "v_mfma_f32_16x16x32_bf16 v[156:159], a[84:87], a[44:47], v[156:159]\n"
      "v_exp_f32 v115, v187\n"
      "v_mfma_f32_16x16x32_bf16 v[156:159], a[92:95], a[60:63], v[156:159]\n"
      "v_cvt_pk_bf16_f32 v221, v114, v115\n"
      "v_mfma_f32_16x16x32_bf16 v[152:155], a[88:91], a[60:63], v[236:239]\n"
      "v_exp_f32 v112, v188\n"
      "v_mfma_f32_16x16x32_bf16 v[152:155], a[80:83], a[44:47], v[152:155]\n"
      "v_or3_b32 v116, v116, v220, v221\n"
      "v_mfma_f32_16x16x32_bf16 v[152:155], a[72:75], a[28:31], v[152:155]\n"
      "v_exp_f32 v113, v189\n"
      "v_mfma_f32_16x16x32_bf16 v[152:155], a[64:67], a[12:15], v[152:155]\n"
      "s_waitcnt lgkmcnt(10)\n"
      "v_cvt_pk_bf16_f32 v222, v112, v113\n"
      "v_mfma_f32_16x16x32_bf16 v[32:35], a[96:99], v[192:195], v[32:35]\n"
      "v_exp_f32 v114, v190\n"
      "v_mfma_f32_16x16x32_bf16 v[48:51], a[100:103], v[192:195], v[48:51]\n"
      "v_mfma_f32_16x16x32_bf16 a[128:131], a[104:107], v[192:195], a[128:131]\n"
      "s_waitcnt lgkmcnt(6)\n"
      "v_exp_f32 v115, v191\n"
      "v_mfma_f32_16x16x32_bf16 a[144:147], a[108:111], v[192:195], a[144:147]\n"
      "v_cvt_pk_bf16_f32 v223, v114, v115\n"
      "v_mfma_f32_16x16x32_bf16 v[64:67], a[112:115], v[192:195], v[64:67]\n"
      "s_waitcnt lgkmcnt(2)\n"
      "v_mfma_f32_16x16x32_bf16 v[80:83], a[116:119], v[192:195], v[80:83]\n"
      "v_or3_b32 v116, v116, v222, v223\n"
      "v_mfma_f32_16x16x32_bf16 a[160:163], a[120:123], v[192:195], a[160:163]\n"
      "s_mov_b32 s64, 0\n"
      "s_waitcnt lgkmcnt(0)\n"
      "v_and_b32 v116, 0x40004000, v116\n"
      "v_mfma_f32_16x16x32_bf16 a[176:179], a[124:127], v[192:195], a[176:179]\n"
      "v_cmp_ne_u32 vcc, 0, v116\n"
      "s_cbranch_vccnz FB1_%=\n"
      "RET1_0_%=:\n"
      "v_mfma_f32_16x16x32_bf16 a[180:183], a[124:127], v[196:199], a[180:183]\n"
      "v_exp_f32 v112, v128\n"
      "s_barrier\n"
      "v_mfma_f32_16x16x32_bf16 a[164:167], a[120:123], v[196:199], a[164:167]\n"
      "v_mfma_f32_16x16x32_bf16 a[192:195], a[196:199], v[208:211], a[192:195]\n"
      "s_add_u32 m0, s62, 0\n"
      "v_exp_f32 v113, v129\n"
      "v_mfma_f32_16x16x32_bf16 v[84:87], a[116:119], v[196:199], v[84:87]\n"
      "buffer_load_dwordx4 v248, s[20:23], s60 offen nt lds\n"
      "v_cvt_pk_bf16_f32 v192, v112, v113\n"
      "v_mfma_f32_16x16x32_bf16 v[68:71], a[112:115], v[196:199], v[68:71]\n"
      "s_add_u32 m0, s62, 896\n"
      "v_exp_f32 v114, v130\n"
      "v_mfma_f32_16x16x32_bf16 a[192:195], a[200:203], v[212:215], a[192:195]\n"
      "buffer_load_dwordx4 v248, s[20:23], s60 offen offset:128 nt lds\n"
      "v_mfma_f32_16x16x32_bf16 a[148:151], a[108:111], v[196:199], a[148:151]\n"
      "s_add_u32 m0, s62, 8192\n"
      "v_exp_f32 v115, v131\n"
      "v_mfma_f32_16x16x32_bf16 a[132:135], a[104:107], v[196:199], a[132:135]\n"
      "s_memrealtime s[68:69]\n"
      "buffer_load_dwordx4 v120, s[20:23], s60 offen nt lds\n"
      "v_cvt_pk_bf16_f32 v193, v114, v115\n"
      "v_mfma_f32_16x16x32_bf16 a[192:195], a[204:207], v[216:219], a[192:195]\n"
      "s_add_u32 m0, s62, 9088\n"
      "v_exp_f32 v112, v132\n"
      "v_mfma_f32_16x16x32_bf16 v[52:55], a[100:103], v[196:199], v[52:55]\n"
      "s_add_u32 s62, s62, 0x8000\n"
      "buffer_load_dwordx4 v120, s[20:23], s60 offen offset:128 nt lds\n"
      "v_or_b32 v116, v192, v193\n"
      "v_mfma_f32_16x16x32_bf16 v[36:39], a[96:99], v[196:199], v[36:39]\n"
      "s_and_b32 s62, s62, 0x1ffff\n"
      "v_exp_f32 v113, v133\n"
      "v_mfma_f32_16x16x32_bf16 a[192:195], a[208:211], v[220:223], a[192:195]\n"
      "s_add_u32 s60, s60, s78\n"
      "v_cvt_pk_bf16_f32 v194, v112, v113\n"
      "s_waitcnt vmcnt(12) lgkmcnt(0)\n"
      "s_sub_u32 s67, s68, s90\n"
      "s_cmp_lt_i32 s67, 0\n"
      "s_cbranch_scc1 GW1_%=\n"
      "GX1_%=:\n"
      "s_barrier\n"
      "v_mfma_f32_16x16x32_bf16 v[40:43], a[96:99], v[200:203], v[40:43]\n"
      "s_memrealtime s[68:69]\n"
      "ds_read_b128 a[64:67], v242 offset:32768\n"
      "v_exp_f32 v114, v134\n"
      "v_mfma_f32_16x16x32_bf16 v[44:47], a[96:99], v[204:207], v[44:47]\n"
      "s_add_u32 m0, s77, 16384\n"
      "ds_read_b128 a[68:71], v242 offset:36864\n"
      "ds_read_b128 a[72:75], v243 offset:32768\n"
      "v_mfma_f32_16x16x32_bf16 v[60:63], a[100:103], v[204:207], v[60:63]\n"
      "buffer_load_dwordx4 v249, s[24:27], s61 offen nt lds\n"
      "v_exp_f32 v115, v135\n"
      "v_mfma_f32_16x16x32_bf16 v[56:59], a[100:103], v[200:203], v[56:59]\n"
      "s_add_u32 m0, s77, 17408\n"
      "ds_read_b128 a[76:79], v243 offset:36864\n"
      "v_cvt_pk_bf16_f32 v195, v114, v115\n"
      "v_mfma_f32_16x16x32_bf16 a[136:139], a[104:107], v[200:203], a[136:139]\n"
      "buffer_load_dwordx4 v121, s[24:27], s61 offen nt lds\n"
      "v_exp_f32 v112, v136\n"
      "v_mfma_f32_16x16x32_bf16 a[140:143], a[104:107], v[204:207], a[140:143]\n"
      "s_add_u32 m0, s77, 24576\n"
      "ds_read_b128 a[80:83], v242 offset:33792\n"
      "v_or3_b32 v116, v116, v194, v195\n"
      "v_mfma_f32_16x16x32_bf16 a[156:159], a[108:111], v[204:207], a[156:159]\n"
      "buffer_load_dwordx4 v122, s[24:27], s61 offen nt lds\n"
      "v_exp_f32 v113, v137\n"
      "v_mfma_f32_16x16x32_bf16 a[152:155], a[108:111], v[200:203], a[152:155]\n"
      "s_add_u32 m0, s77, 25600\n"
      "ds_read_b128 a[84:87], v242 offset:37888\n"
      "v_cvt_pk_bf16_f32 v196, v112, v113\n"
      "v_mfma_f32_16x16x32_bf16 v[72:75], a[112:115], v[200:203], v[72:75]\n"
      "s_add_u32 s77, s77, 0x8000\n"
      "buffer_load_dwordx4 v123, s[24:27], s61 offen nt lds\n"
      "v_exp_f32 v114, v138\n"
      "v_mfma_f32_16x16x32_bf16 v[76:79], a[112:115], v[204:207], v[76:79]\n"
      "s_and_b32 s77, s77, 0x1ffff\n"
      "ds_read_b128 a[88:91], v243 offset:33792\n"
      "ds_read_b128 a[92:95], v243 offset:37888\n"
      "v_mfma_f32_16x16x32_bf16 v[92:95], a[116:119], v[204:207], v[92:95]\n"
      "s_add_u32 s61, s61, s78\n"
      "v_exp_f32 v115, v139\n"
      "v_mfma_f32_16x16x32_bf16 v[88:91], a[116:119], v[200:203], v[88:91]\n"
      "v_cvt_pk_bf16_f32 v197, v114, v115\n"
      "v_mfma_f32_16x16x32_bf16 a[168:171], a[120:123], v[200:203], a[168:171]\n"
      "v_exp_f32 v112, v140\n"
      "v_mfma_f32_16x16x32_bf16 a[172:175], a[120:123], v[204:207], a[172:175]\n"
      "v_or3_b32 v116, v116, v196, v197\n"
      "v_mfma_f32_16x16x32_bf16 a[188:191], a[124:127], v[204:207], a[188:191]\n"
      "v_exp_f32 v113, v141\n"
      "v_mfma_f32_16x16x32_bf16 a[184:187], a[124:127], v[200:203], a[184:187]\n"
      "s_waitcnt lgkmcnt(0)\n"
      "v_cvt_pk_bf16_f32 v198, v112, v113\n"
      "v_mfma_f32_16x16x32_bf16 v[160:163], a[64:67], a[0:3], v[224:227]\n"
      "s_sub_u32 s67, s68, s66\n"
      "v_exp_f32 v114, v142\n"
      "v_mfma_f32_16x16x32_bf16 v[160:163], a[72:75], a[16:19], v[160:163]\n"
      "s_cmp_gt_i32 s67, s70\n"
      "ds_read_b64_tr_b16 a[96:97], v244 offset:32768\n"
      "ds_read_b64_tr_b16 a[98:99], v244 offset:36864\n"
      "v_mfma_f32_16x16x32_bf16 v[160:163], a[80:83], a[32:35], v[160:163]\n"
      "s_cselect_b32 s69, s86, 0\n"
      "v_exp_f32 v115, v143\n"
      "v_mfma_f32_16x16x32_bf16 v[160:163], a[88:91], a[48:51], v[160:163]\n"
      "s_add_u32 s66, s66, s69\n"
      "ds_read_b64_tr_b16 a[100:101], v245 offset:32768\n"
      "v_cvt_pk_bf16_f32 v199, v114, v115\n"
      "v_mfma_f32_16x16x32_bf16 v[164:167], a[92:95], a[48:51], v[224:227]\n"
      "s_add_u32 s67, s67, s101\n"
      "v_exp_f32 v112, v144\n"
      "v_mfma_f32_16x16x32_bf16 v[164:167], a[84:87], a[32:35], v[164:167]\n"
      "s_min_i32 s70, s70, s67\n"
      "ds_read_b64_tr_b16 a[102:103], v245 offset:36864\n"
      "v_or3_b32 v116, v116, v198, v199\n"
      "v_mfma_f32_16x16x32_bf16 v[164:167], a[76:79], a[16:19], v[164:167]\n"
      "s_max_i32 s70, s70, 85\n"
      "v_exp_f32 v113, v145\n"
      "v_mfma_f32_16x16x32_bf16 v[164:167], a[68:71], a[0:3], v[164:167]\n"
      "s_add_u32 s66, s66, s86\n"
      "ds_read_b64_tr_b16 a[104:105], v246 offset:32768\n"
      "v_cvt_pk_bf16_f32 v200, v112, v113\n"
      "v_mfma_f32_16x16x32_bf16 v[172:175], a[68:71], a[4:7], v[228:231]\n"
      "s_and_b32 s76, s80, 31\n"
      "v_exp_f32 v114, v146\n"
      "v_mfma_f32_16x16x32_bf16 v[172:175], a[76:79], a[20:23], v[172:175]\n"
      "s_lshr_b64 s[80:81], s[80:81], 5\n"
      "ds_read_b64_tr_b16 a[106:107], v246 offset:36864\n"
      "ds_read_b64_tr_b16 a[108:109], v247 offset:32768\n"
      "v_mfma_f32_16x16x32_bf16 v[172:175], a[84:87], a[36:39], v[172:175]\n"
      "s_add_u32 s66, s66, s76\n"
      "v_exp_f32 v115, v147\n"
      "v_mfma_f32_16x16x32_bf16 v[172:175], a[92:95], a[52:55], v[172:175]\n"
      "s_sub_u32 s67, s68, s82\n"
      "ds_read_b64_tr_b16 a[110:111], v247 offset:36864\n"
      "v_cvt_pk_bf16_f32 v201, v114, v115\n"
      "v_mfma_f32_16x16x32_bf16 v[168:171], a[88:91], a[52:55], v[228:231]\n"
      "s_min_i32 s76, s67, s83\n"
      "v_exp_f32 v112, v148\n"
      "v_mfma_f32_16x16x32_bf16 v[168:171], a[80:83], a[36:39], v[168:171]\n"
      "s_mov_b32 s83, s67\n"
      "ds_read_b64_tr_b16 a[112:113], v244 offset:33792\n"
      "v_or3_b32 v116, v116, v200, v201\n"
      "v_mfma_f32_16x16x32_bf16 v[168:171], a[72:75], a[20:23], v[168:171]\n"
      "s_sub_u32 s76, s76, 36\n"
      "v_exp_f32 v113, v149\n"
      "v_mfma_f32_16x16x32_bf16 v[168:171], a[64:67], a[4:7], v[168:171]\n"
      "s_min_i32 s76, s76, 148\n"
      "ds_read_b64_tr_b16 a[114:115], v244 offset:37888\n"
      "v_cvt_pk_bf16_f32 v202, v112, v113\n"
      "v_mfma_f32_16x16x32_bf16 v[176:179], a[64:67], a[8:11], v[232:235]\n"
      "s_max_i32 s76, s76, 70\n"
      "v_exp_f32 v114, v150\n"
      "v_mfma_f32_16x16x32_bf16 v[176:179], a[72:75], a[24:27], v[176:179]\n"
      "s_mov_b32 s82, s68\n"
      "ds_read_b64_tr_b16 a[116:117], v245 offset:33792\n"
      "ds_read_b64_tr_b16 a[118:119], v245 offset:37888\n"
      "v_mfma_f32_16x16x32_bf16 v[176:179], a[80:83], a[40:43], v[176:179]\n"
      "s_add_u32 s65, s82, s76\n"
      "v_exp_f32 v115, v151\n"
      "v_mfma_f32_16x16x32_bf16 v[176:179], a[88:91], a[56:59], v[176:179]\n"
      "s_sub_u32 s93, s93, 1\n"
      "ds_read_b64_tr_b16 a[120:121], v246 offset:33792\n"
      "v_cvt_pk_bf16_f32 v203, v114, v115\n"
      "v_mfma_f32_16x16x32_bf16 v[180:183], a[92:95], a[56:59], v[232:235]\n"
      "s_cmp_le_i32 s93, 14\n"
      "v_exp_f32 v112, v152\n"
      "v_mfma_f32_16x16x32_bf16 v[180:183], a[84:87], a[40:43], v[180:183]\n"
      "s_cselect_b32 s86, 107, s86\n"
      "ds_read_b64_tr_b16 a[122:123], v246 offset:37888\n"
      "v_or3_b32 v116, v116, v202, v203\n"
      "v_mfma_f32_16x16x32_bf16 v[180:183], a[76:79], a[24:27], v[180:183]\n"
      "s_sub_u32 s67, s65, s66\n"
      "v_exp_f32 v113, v153\n"
      "v_mfma_f32_16x16x32_bf16 v[180:183], a[68:71], a[8:11], v[180:183]\n"
      "s_cmp_lt_i32 s67, 0\n"
      "ds_read_b64_tr_b16 a[124:125], v247 offset:33792\n"
      "v_cvt_pk_bf16_f32 v204, v112, v113\n"
      "v_mfma_f32_16x16x32_bf16 v[188:191], a[68:71], a[12:15], v[236:239]\n"
      "s_cselect_b32 s88, s66, s65\n"
      "v_exp_f32 v114, v154\n"
      "v_mfma_f32_16x16x32_bf16 v[188:191], a[76:79], a[28:31], v[188:191]\n"
      "s_add_u32 s90, s88, s91\n"
      "ds_read_b64_tr_b16 a[126:127], v247 offset:37888\n"
      "v_mfma_f32_16x16x32_bf16 v[188:191], a[84:87], a[44:47], v[188:191]\n"
      "v_exp_f32 v115, v155\n"
      "v_mfma_f32_16x16x32_bf16 v[188:191], a[92:95], a[60:63], v[188:191]\n"
      "v_cvt_pk_bf16_f32 v205, v114, v115\n"
      "v_mfma_f32_16x16x32_bf16 v[184:187], a[88:91], a[60:63], v[236:239]\n"
      "v_exp_f32 v112, v156\n"
      "v_mfma_f32_16x16x32_bf16 v[184:187], a[80:83], a[44:47], v[184:187]\n"
      "v_or3_b32 v116, v116, v204, v205\n"
      "v_mfma_f32_16x16x32_bf16 v[184:187], a[72:75], a[28:31], v[184:187]\n"
      "v_exp_f32 v113, v157\n"
      "v_mfma_f32_16x16x32_bf16 v[184:187], a[64:67], a[12:15], v[184:187]\n"
      "s_waitcnt lgkmcnt(10)\n"
      "v_cvt_pk_bf16_f32 v206, v112, v113\n"
      "v_mfma_f32_16x16x32_bf16 v[32:35], a[96:99], v[208:211], v[32:35]\n"
      "v_exp_f32 v114, v158\n"
      "v_mfma_f32_16x16x32_bf16 v[48:51], a[100:103], v[208:211], v[48:51]\n"
      "v_mfma_f32_16x16x32_bf16 a[128:131], a[104:107], v[208:211], a[128:131]\n"
      "s_waitcnt lgkmcnt(6)\n"
      "v_exp_f32 v115, v159\n"
      "v_mfma_f32_16x16x32_bf16 a[144:147], a[108:111], v[208:211], a[144:147]\n"
      "v_cvt_pk_bf16_f32 v207, v114, v115\n"
      "v_mfma_f32_16x16x32_bf16 v[64:67], a[112:115], v[208:211], v[64:67]\n"
      "s_waitcnt lgkmcnt(2)\n"
      "v_mfma_f32_16x16x32_bf16 v[80:83], a[116:119], v[208:211], v[80:83]\n"
      "v_or3_b32 v116, v116, v206, v207\n"
      "v_mfma_f32_16x16x32_bf16 a[160:163], a[120:123], v[208:211], a[160:163]\n"
      "s_mov_b32 s64, 0\n"
      "s_waitcnt lgkmcnt(0)\n"
      "v_and_b32 v116, 0x40004000, v116\n"
      "v_mfma_f32_16x16x32_bf16 a[176:179], a[124:127], v[208:211], a[176:179]\n"
      "v_cmp_ne_u32 vcc, 0, v116\n"
      "s_cbranch_vccnz FB0_%=\n"
      "RET0_0_%=:\n"
      "v_mfma_f32_16x16x32_bf16 a[180:183], a[124:127], v[212:215], a[180:183]\n"
      "v_exp_f32 v112, v160\n"
      "s_barrier\n"
      "v_mfma_f32_16x16x32_bf16 a[164:167], a[120:123], v[212:215], a[164:167]\n"
      "v_xor_b32 v242, 0x10000, v242\n"
      "v_mfma_f32_16x16x32_bf16 a[192:195], a[196:199], v[192:195], a[192:195]\n"
      "s_add_u32 m0, s62, 0\n"
      "v_exp_f32 v113, v161\n"
      "v_mfma_f32_16x16x32_bf16 v[84:87], a[116:119], v[212:215], v[84:87]\n"
      "buffer_load_dwordx4 v248, s[20:23], s60 offen nt lds\n"
      "v_cvt_pk_bf16_f32 v208, v112, v113\n"
      "v_xor_b32 v243, 0x10000, v243\n"
      "v_mfma_f32_16x16x32_bf16 v[68:71], a[112:115], v[212:215], v[68:71]\n"
      "s_add_u32 m0, s62, 896\n"
      "v_exp_f32 v114, v162\n"
      "v_mfma_f32_16x16x32_bf16 a[192:195], a[200:203], v[196:199], a[192:195]\n"
      "buffer_load_dwordx4 v248, s[20:23], s60 offen offset:128 nt lds\n"
      "v_xor_b32 v244, 0x10000, v244\n"
      "v_mfma_f32_16x16x32_bf16 a[148:151], a[108:111], v[212:215], a[148:151]\n"
      "s_add_u32 m0, s62, 8192\n"
      "v_exp_f32 v115, v163\n"
      "v_mfma_f32_16x16x32_bf16 a[132:135], a[104:107], v[212:215], a[132:135]\n"
      "s_memrealtime s[68:69]\n"
      "buffer_load_dwordx4 v120, s[20:23], s60 offen nt lds\n"
      "v_cvt_pk_bf16_f32 v209, v114, v115\n"
      "v_xor_b32 v245, 0x10000, v245\n"
      "v_mfma_f32_16x16x32_bf16 a[192:195], a[204:207], v[200:203], a[192:195]\n"
      "s_add_u32 m0, s62, 9088\n"
      "v_exp_f32 v112, v164\n"
      "v_mfma_f32_16x16x32_bf16 v[52:55], a[100:103], v[212:215], v[52:55]\n"
      "s_add_u32 s62, s62, 0x8000\n"
      "buffer_load_dwordx4 v120, s[20:23], s60 offen offset:128 nt lds\n"
      "v_or_b32 v116, v208, v209\n"
      "v_xor_b32 v246, 0x10000, v246\n"
      "v_mfma_f32_16x16x32_bf16 v[36:39], a[96:99], v[212:215], v[36:39]\n"
      "s_and_b32 s62, s62, 0x1ffff\n"
      "s_add_u32 s60, s60, %[inc]\n"
      "v_exp_f32 v113, v165\n"
      "v_mfma_f32_16x16x32_bf16 a[192:195], a[208:211], v[204:207], a[192:195]\n"
      "s_sub_u32 s63, s63, 1\n"
      "v_cvt_pk_bf16_f32 v210, v112, v113\n"
      "v_xor_b32 v247, 0x10000, v247\n"
      "s_cbranch_scc0 LOOP1_%=\n"
      "v_readfirstlane_b32 s72, v240\n"
      "v_readfirstlane_b32 s79, v241\n"
      "s_waitcnt vmcnt(12) lgkmcnt(0)\n"
      "s_sub_u32 s67, s68, s90\n"
      "s_cmp_lt_i32 s67, 0\n"
      "s_cbranch_scc1 GW2_%=\n"
      "GX2_%=:\n"
      "s_barrier\n"
      "v_mfma_f32_16x16x32_bf16 v[40:43], a[96:99], v[216:219], v[40:43]\n"
      "s_memrealtime s[68:69]\n"
      "buffer_load_dword v250, v251, s[28:31], 0 offen sc1\n"
      "ds_read_b128 a[64:67], v242 offset:0\n"
      "v_exp_f32 v114, v166\n"
      "v_mfma_f32_16x16x32_bf16 v[44:47], a[96:99], v[220:223], v[44:47]\n"
      "s_add_u32 m0, s77, 16384\n"
      "ds_read_b128 a[68:71], v242 offset:4096\n"
      "ds_read_b128 a[72:75], v243 offset:0\n"
      "v_mfma_f32_16x16x32_bf16 v[60:63], a[100:103], v[220:223], v[60:63]\n"
      "buffer_load_dwordx4 v249, s[24:27], s61 offen nt lds\n"
      "v_exp_f32 v115, v167\n"
      "v_mfma_f32_16x16x32_bf16 v[56:59], a[100:103], v[216:219], v[56:59]\n"
      "s_add_u32 m0, s77, 17408\n"
      "ds_read_b128 a[76:79], v243 offset:4096\n"
      "v_cvt_pk_bf16_f32 v211, v114, v115\n"
      "v_mfma_f32_16x16x32_bf16 a[136:139], a[104:107], v[216:219], a[136:139]\n"
      "buffer_load_dwordx4 v121, s[24:27], s61 offen nt lds\n"
      "v_exp_f32 v112, v168\n"
      "v_mfma_f32_16x16x32_bf16 a[140:143], a[104:107], v[220:223], a[140:143]\n"
      "s_add_u32 m0, s77, 24576\n"
      "ds_read_b128 a[80:83], v242 offset:1024\n"
      "v_or3_b32 v116, v116, v210, v211\n"
      "v_mfma_f32_16x16x32_bf16 a[156:159], a[108:111], v[220:223], a[156:159]\n"
      "buffer_load_dwordx4 v122, s[24:27], s61 offen nt lds\n"
      "v_exp_f32 v113, v169\n"
      "v_mfma_f32_16x16x32_bf16 a[152:155], a[108:111], v[216:219], a[152:155]\n"
      "s_add_u32 m0, s77, 25600\n"
      "ds_read_b128 a[84:87], v242 offset:5120\n"
      "v_cvt_pk_bf16_f32 v212, v112, v113\n"
      "v_mfma_f32_16x16x32_bf16 v[72:75], a[112:115], v[216:219], v[72:75]\n"
      "s_add_u32 s77, s77, 0x8000\n"
      "buffer_load_dwordx4 v123, s[24:27], s61 offen nt lds\n"
      "v_exp_f32 v114, v170\n"
      "v_mfma_f32_16x16x32_bf16 v[76:79], a[112:115], v[220:223], v[76:79]\n"
      "s_and_b32 s77, s77, 0x1ffff\n"
      "ds_read_b128 a[88:91], v243 offset:1024\n"
      "ds_read_b128 a[92:95], v243 offset:5120\n"
      "v_mfma_f32_16x16x32_bf16 v[92:95], a[116:119], v[220:223], v[92:95]\n"
      "s_add_u32 s61, s61, %[inc]\n"
      "v_exp_f32 v115, v171\n"
      "v_mfma_f32_16x16x32_bf16 v[88:91], a[116:119], v[216:219], v[88:91]\n"
      "v_cvt_pk_bf16_f32 v213, v114, v115\n"
      "v_mfma_f32_16x16x32_bf16 a[168:171], a[120:123], v[216:219], a[168:171]\n"
      "v_exp_f32 v112, v172\n"
      "v_mfma_f32_16x16x32_bf16 a[172:175], a[120:123], v[220:223], a[172:175]\n"
      "v_or3_b32 v116, v116, v212, v213\n"
      "v_mfma_f32_16x16x32_bf16 a[188:191], a[124:127], v[220:223], a[188:191]\n"
      "v_exp_f32 v113, v173\n"
      "v_mfma_f32_16x16x32_bf16 a[184:187], a[124:127], v[216:219], a[184:187]\n"
      "s_waitcnt lgkmcnt(0)\n"
      "v_cvt_pk_bf16_f32 v214, v112, v113\n"
      "v_mfma_f32_16x16x32_bf16 v[128:131], a[64:67], a[0:3], v[224:227]\n"
      "s_sub_u32 s67, s68, s66\n"
      "v_exp_f32 v114, v174\n"
      "v_mfma_f32_16x16x32_bf16 v[128:131], a[72:75], a[16:19], v[128:131]\n"
      "s_cmp_gt_i32 s67, s70\n"
      "ds_read_b64_tr_b16 a[96:97], v244 offset:0\n"
      "ds_read_b64_tr_b16 a[98:99], v244 offset:4096\n"
      "v_mfma_f32_16x16x32_bf16 v[128:131], a[80:83], a[32:35], v[128:131]\n"
      "s_cselect_b32 s69, s86, 0\n"
      "v_exp_f32 v115, v175\n"
      "v_mfma_f32_16x16x32_bf16 v[128:131], a[88:91], a[48:51], v[128:131]\n"
      "s_add_u32 s66, s66, s69\n"
      "ds_read_b64_tr_b16 a[100:101], v245 offset:0\n"
      "v_cvt_pk_bf16_f32 v215, v114, v115\n"
      "v_mfma_f32_16x16x32_bf16 v[132:135], a[92:95], a[48:51], v[224:227]\n"
      "s_add_u32 s67, s67, s101\n"
      "v_exp_f32 v112, v176\n"
      "v_mfma_f32_16x16x32_bf16 v[132:135], a[84:87], a[32:35], v[132:135]\n"
      "s_min_i32 s70, s70, s67\n"
      "ds_read_b64_tr_b16 a[102:103], v245 offset:4096\n"
      "v_or3_b32 v116, v116, v214, v215\n"
      "v_mfma_f32_16x16x32_bf16 v[132:135], a[76:79], a[16:19], v[132:135]\n"
      "s_max_i32 s70, s70, 85\n"
      "v_exp_f32 v113, v177\n"
      "v_mfma_f32_16x16x32_bf16 v[132:135], a[68:71], a[0:3], v[132:135]\n"
      "s_add_u32 s66, s66, s86\n"
      "ds_read_b64_tr_b16 a[104:105], v246 offset:0\n"
      "v_cvt_pk_bf16_f32 v216, v112, v113\n"
      "v_mfma_f32_16x16x32_bf16 v[140:143], a[68:71], a[4:7], v[228:231]\n"
      "s_and_b32 s76, s80, 31\n"
      "v_exp_f32 v114, v178\n"
      "v_mfma_f32_16x16x32_bf16 v[140:143], a[76:79], a[20:23], v[140:143]\n"
      "s_lshr_b64 s[80:81], s[80:81], 5\n"
      "ds_read_b64_tr_b16 a[106:107], v246 offset:4096\n"
      "ds_read_b64_tr_b16 a[108:109], v247 offset:0\n"
      "v_mfma_f32_16x16x32_bf16 v[140:143], a[84:87], a[36:39], v[140:143]\n"
      "s_add_u32 s66, s66, s76\n"
      "v_exp_f32 v115, v179\n"
      "v_mfma_f32_16x16x32_bf16 v[140:143], a[92:95], a[52:55], v[140:143]\n"
      "s_sub_u32 s67, s68, s82\n"
      "ds_read_b64_tr_b16 a[110:111], v247 offset:4096\n"
      "v_cvt_pk_bf16_f32 v217, v114, v115\n"
      "v_mfma_f32_16x16x32_bf16 v[136:139], a[88:91], a[52:55], v[228:231]\n"
      "s_min_i32 s76, s67, s83\n"
      "v_exp_f32 v112, v180\n"
      "v_mfma_f32_16x16x32_bf16 v[136:139], a[80:83], a[36:39], v[136:139]\n"
      "s_mov_b32 s83, s67\n"
      "ds_read_b64_tr_b16 a[112:113], v244 offset:1024\n"
      "v_or3_b32 v116, v116, v216, v217\n"
      "v_mfma_f32_16x16x32_bf16 v[136:139], a[72:75], a[20:23], v[136:139]\n"
      "s_sub_u32 s76, s76, 36\n"
      "v_exp_f32 v113, v181\n"
      "v_mfma_f32_16x16x32_bf16 v[136:139], a[64:67], a[4:7], v[136:139]\n"
      "s_min_i32 s76, s76, 148\n"
      "ds_read_b64_tr_b16 a[114:115], v244 offset:5120\n"
      "v_cvt_pk_bf16_f32 v218, v112, v113\n"
      "v_mfma_f32_16x16x32_bf16 v[144:147], a[64:67], a[8:11], v[232:235]\n"
      "s_max_i32 s76, s76, 70\n"
      "v_exp_f32 v114, v182\n"
      "v_mfma_f32_16x16x32_bf16 v[144:147], a[72:75], a[24:27], v[144:147]\n"
      "s_mov_b32 s82, s68\n"
      "ds_read_b64_tr_b16 a[116:117], v245 offset:1024\n"
      "ds_read_b64_tr_b16 a[118:119], v245 offset:5120\n"
      "v_mfma_f32_16x16x32_bf16 v[144:147], a[80:83], a[40:43], v[144:147]\n"
      "s_lshr_b32 s69, s86, 1\n"
      "v_exp_f32 v115, v183\n"
      "v_mfma_f32_16x16x32_bf16 v[144:147], a[88:91], a[56:59], v[144:147]\n"
      "s_add_u32 s66, s66, s69\n"
      "ds_read_b64_tr_b16 a[120:121], v246 offset:1024\n"
      "v_cvt_pk_bf16_f32 v219, v114, v115\n"
      "v_mfma_f32_16x16x32_bf16 v[148:151], a[92:95], a[56:59], v[232:235]\n"
      "s_add_u32 s82, s82, s69\n"
      "v_exp_f32 v112, v184\n"
      "v_mfma_f32_16x16x32_bf16 v[148:151], a[84:87], a[40:43], v[148:151]\n"
      "s_add_u32 s65, s82, s76\n"
      "ds_read_b64_tr_b16 a[122:123], v246 offset:5120\n"
      "v_or3_b32 v116, v116, v218, v219\n"
      "v_mfma_f32_16x16x32_bf16 v[148:151], a[76:79], a[24:27], v[148:151]\n"
      "s_sub_u32 s93, s93, 1\n"
      "v_exp_f32 v113, v185\n"
      "v_mfma_f32_16x16x32_bf16 v[148:151], a[68:71], a[8:11], v[148:151]\n"
      "s_cmp_le_i32 s93, 14\n"
      "ds_read_b64_tr_b16 a[124:125], v247 offset:1024\n"
      "v_cvt_pk_bf16_f32 v220, v112, v113\n"
      "v_mfma_f32_16x16x32_bf16 v[156:159], a[68:71], a[12:15], v[236:239]\n"
      "s_cselect_b32 s86, 107, s86\n"
      "v_exp_f32 v114, v186\n"
      "v_mfma_f32_16x16x32_bf16 v[156:159], a[76:79], a[28:31], v[156:159]\n"
      "s_sub_u32 s67, s65, s66\n"
      "ds_read_b64_tr_b16 a[126:127], v247 offset:5120\n"
      "v_mfma_f32_16x16x32_bf16 v[156:159], a[84:87], a[44:47], v[156:159]\n"
      "s_cmp_lt_i32 s67, 0\n"
      "v_exp_f32 v115, v187\n"
      "v_mfma_f32_16x16x32_bf16 v[156:159], a[92:95], a[60:63], v[156:159]\n"
      "s_cselect_b32 s88, s66, s65\n"
      "v_cvt_pk_bf16_f32 v221, v114, v115\n"
      "v_mfma_f32_16x16x32_bf16 v[152:155], a[88:91], a[60:63], v[236:239]\n"
      "s_add_u32 s90, s88, s91\n"
      "v_exp_f32 v112, v188\n"
      "v_mfma_f32_16x16x32_bf16 v[152:155], a[80:83], a[44:47], v[152:155]\n"
      "v_or3_b32 v116, v116, v220, v221\n"
      "v_mfma_f32_16x16x32_bf16 v[152:155], a[72:75], a[28:31], v[152:155]\n"
      "v_exp_f32 v113, v189\n"
      "v_mfma_f32_16x16x32_bf16 v[152:155], a[64:67], a[12:15], v[152:155]\n"
      "s_waitcnt lgkmcnt(10)\n"
      "v_cvt_pk_bf16_f32 v222, v112, v113\n"
      "v_mfma_f32_16x16x32_bf16 v[32:35], a[96:99], v[192:195], v[32:35]\n"
      "v_exp_f32 v114, v190\n"
      "v_mfma_f32_16x16x32_bf16 v[48:51], a[100:103], v[192:195], v[48:51]\n"
      "v_mfma_f32_16x16x32_bf16 a[128:131], a[104:107], v[192:195], a[128:131]\n"
      "s_waitcnt lgkmcnt(6)\n"
      "v_exp_f32 v115, v191\n"
      "v_mfma_f32_16x16x32_bf16 a[144:147], a[108:111], v[192:195], a[144:147]\n"
      "v_cvt_pk_bf16_f32 v223, v114, v115\n"
      "v_mfma_f32_16x16x32_bf16 v[64:67], a[112:115], v[192:195], v[64:67]\n"
      "s_waitcnt lgkmcnt(2)\n"
      "v_mfma_f32_16x16x32_bf16 v[80:83], a[116:119], v[192:195], v[80:83]\n"
      "v_or3_b32 v116, v116, v222, v223\n"
      "v_mfma_f32_16x16x32_bf16 a[160:163], a[120:123], v[192:195], a[160:163]\n"
      "s_mov_b32 s64, 1\n"
      "s_waitcnt lgkmcnt(0)\n"
      "v_and_b32 v116, 0x40004000, v116\n"
      "v_mfma_f32_16x16x32_bf16 a[176:179], a[124:127], v[192:195], a[176:179]\n"
      "v_cmp_ne_u32 vcc, 0, v116\n"
      "s_cbranch_vccnz FB1_%=\n"
      "RET1_1_%=:\n"
      "v_mfma_f32_16x16x32_bf16 a[180:183], a[124:127], v[196:199], a[180:183]\n"
      "v_exp_f32 v112, v128\n"
      "s_barrier\n"
      "v_mfma_f32_16x16x32_bf16 a[164:167], a[120:123], v[196:199], a[164:167]\n"
      "v_mfma_f32_16x16x32_bf16 a[192:195], a[196:199], v[208:211], a[192:195]\n"
      "s_add_u32 m0, s62, 0\n"
      "v_exp_f32 v113, v129\n"
      "v_mfma_f32_16x16x32_bf16 v[84:87], a[116:119], v[196:199], v[84:87]\n"
      "buffer_load_dwordx4 v248, s[20:23], s60 offen nt lds\n"
      "v_cvt_pk_bf16_f32 v192, v112, v113\n"
      "v_mfma_f32_16x16x32_bf16 v[68:71], a[112:115], v[196:199], v[68:71]\n"
      "s_add_u32 m0, s62, 896\n"
      "v_exp_f32 v114, v130\n"
      "v_mfma_f32_16x16x32_bf16 a[192:195], a[200:203], v[212:215], a[192:195]\n"
      "buffer_load_dwordx4 v248, s[20:23], s60 offen offset:128 nt lds\n"
      "v_mfma_f32_16x16x32_bf16 a[148:151], a[108:111], v[196:199], a[148:151]\n"
      "s_add_u32 m0, s62, 8192\n"
      "v_exp_f32 v115, v131\n"
      "v_mfma_f32_16x16x32_bf16 a[132:135], a[104:107], v[196:199], a[132:135]\n"
      "s_memrealtime s[68:69]\n"
      "buffer_load_dwordx4 v120, s[20:23], s60 offen nt lds\n"
      "v_cvt_pk_bf16_f32 v193, v114, v115\n"
      "v_mfma_f32_16x16x32_bf16 a[192:195], a[204:207], v[216:219], a[192:195]\n"
      "s_add_u32 m0, s62, 9088\n"
      "v_exp_f32 v112, v132\n"
      "v_mfma_f32_16x16x32_bf16 v[52:55], a[100:103], v[196:199], v[52:55]\n"
      "s_add_u32 s62, s62, 0x8000\n"
      "buffer_load_dwordx4 v120, s[20:23], s60 offen offset:128 nt lds\n"
      "v_or_b32 v116, v192, v193\n"
      "v_mfma_f32_16x16x32_bf16 v[36:39], a[96:99], v[196:199], v[36:39]\n"
      "s_and_b32 s62, s62, 0x1ffff\n"
      "v_exp_f32 v113, v133\n"
      "v_mfma_f32_16x16x32_bf16 a[192:195], a[208:211], v[220:223], a[192:195]\n"
      "s_add_u32 s60, s60, s78\n"
      "v_cvt_pk_bf16_f32 v194, v112, v113\n"
      "s_waitcnt vmcnt(12) lgkmcnt(0)\n"
      "s_barrier\n"
      "v_mfma_f32_16x16x32_bf16 v[40:43], a[96:99], v[200:203], v[40:43]\n"
      "ds_read_b128 a[64:67], v242 offset:32768\n"
      "v_exp_f32 v114, v134\n"
      "v_mfma_f32_16x16x32_bf16 v[44:47], a[96:99], v[204:207], v[44:47]\n"
      "ds_read_b128 a[68:71], v242 offset:36864\n"
      "ds_read_b128 a[72:75], v243 offset:32768\n"
      "v_mfma_f32_16x16x32_bf16 v[60:63], a[100:103], v[204:207], v[60:63]\n"
      "v_exp_f32 v115, v135\n"
      "v_mfma_f32_16x16x32_bf16 v[56:59], a[100:103], v[200:203], v[56:59]\n"
      "ds_read_b128 a[76:79], v243 offset:36864\n"
      "v_cvt_pk_bf16_f32 v195, v114, v115\n"
      "v_mfma_f32_16x16x32_bf16 a[136:139], a[104:107], v[200:203], a[136:139]\n"
      "v_exp_f32 v112, v136\n"
      "v_mfma_f32_16x16x32_bf16 a[140:143], a[104:107], v[204:207], a[140:143]\n"
      "ds_read_b128 a[80:83], v242 offset:33792\n"
      "v_or3_b32 v116, v116, v194, v195\n"
      "v_mfma_f32_16x16x32_bf16 a[156:159], a[108:111], v[204:207], a[156:159]\n"
      "v_exp_f32 v113, v137\n"
      "v_mfma_f32_16x16x32_bf16 a[152:155], a[108:111], v[200:203], a[152:155]\n"
      "ds_read_b128 a[84:87], v242 offset:37888\n"
      "v_cvt_pk_bf16_f32 v196, v112, v113\n"
      "v_mfma_f32_16x16x32_bf16 v[72:75], a[112:115], v[200:203], v[72:75]\n"
      "v_exp_f32 v114, v138\n"
      "v_mfma_f32_16x16x32_bf16 v[76:79], a[112:115], v[204:207], v[76:79]\n"
      "ds_read_b128 a[88:91], v243 offset:33792\n"
      "ds_read_b128 a[92:95], v243 offset:37888\n"
      "v_mfma_f32_16x16x32_bf16 v[92:95], a[116:119], v[204:207], v[92:95]\n"
      "v_exp_f32 v115, v139\n"
      "v_mfma_f32_16x16x32_bf16 v[88:91], a[116:119], v[200:203], v[88:91]\n"
      "v_cvt_pk_bf16_f32 v197, v114, v115\n"
      "v_mfma_f32_16x16x32_bf16 a[168:171], a[120:123], v[200:203], a[168:171]\n"
      "v_exp_f32 v112, v140\n"
      "v_mfma_f32_16x16x32_bf16 a[172:175], a[120:123], v[204:207], a[172:175]\n"
      "v_or3_b32 v116, v116, v196, v197\n"
      "v_mfma_f32_16x16x32_bf16 a[188:191], a[124:127], v[204:207], a[188:191]\n"
      "v_exp_f32 v113, v141\n"
      "v_mfma_f32_16x16x32_bf16 a[184:187], a[124:127], v[200:203], a[184:187]\n"
      "s_waitcnt lgkmcnt(0)\n"
      "v_cvt_pk_bf16_f32 v198, v112, v113\n"
      "v_mfma_f32_16x16x32_bf16 v[160:163], a[64:67], a[0:3], v[224:227]\n"
      "v_exp_f32 v114, v142\n"
      "v_mfma_f32_16x16x32_bf16 v[160:163], a[72:75], a[16:19], v[160:163]\n"
      "ds_read_b64_tr_b16 a[96:97], v244 offset:32768\n"
      "ds_read_b64_tr_b16 a[98:99], v244 offset:36864\n"
      "v_mfma_f32_16x16x32_bf16 v[160:163], a[80:83], a[32:35], v[160:163]\n"
      "v_exp_f32 v115, v143\n"
      "v_mfma_f32_16x16x32_bf16 v[160:163], a[88:91], a[48:51], v[160:163]\n"
      "ds_read_b64_tr_b16 a[100:101], v245 offset:32768\n"
      "v_cvt_pk_bf16_f32 v199, v114, v115\n"
      "v_mfma_f32_16x16x32_bf16 v[164:167], a[92:95], a[48:51], v[224:227]\n"
      "v_exp_f32 v112, v144\n"
      "v_mfma_f32_16x16x32_bf16 v[164:167], a[84:87], a[32:35], v[164:167]\n"
      "ds_read_b64_tr_b16 a[102:103], v245 offset:36864\n"
      "v_or3_b32 v116, v116, v198, v199\n"
      "v_mfma_f32_16x16x32_bf16 v[164:167], a[76:79], a[16:19], v[164:167]\n"
      "v_exp_f32 v113, v145\n"
      "v_mfma_f32_16x16x32_bf16 v[164:167], a[68:71], a[0:3], v[164:167]\n"
      "ds_read_b64_tr_b16 a[104:105], v246 offset:32768\n"
      "v_cvt_pk_bf16_f32 v200, v112, v113\n"
      "v_mfma_f32_16x16x32_bf16 v[172:175], a[68:71], a[4:7], v[228:231]\n"
      "v_exp_f32 v114, v146\n"
      "v_mfma_f32_16x16x32_bf16 v[172:175], a[76:79], a[20:23], v[172:175]\n"
      "ds_read_b64_tr_b16 a[106:107], v246 offset:36864\n"
      "ds_read_b64_tr_b16 a[108:109], v247 offset:32768\n"
      "v_mfma_f32_16x16x32_bf16 v[172:175], a[84:87], a[36:39], v[172:175]\n"
      "v_exp_f32 v115, v147\n"
      "v_mfma_f32_16x16x32_bf16 v[172:175], a[92:95], a[52:55], v[172:175]\n"
      "ds_read_b64_tr_b16 a[110:111], v247 offset:36864\n"
      "v_cvt_pk_bf16_f32 v201, v114, v115\n"
      "v_mfma_f32_16x16x32_bf16 v[168:171], a[88:91], a[52:55], v[228:231]\n"
      "v_exp_f32 v112, v148\n"
      "v_mfma_f32_16x16x32_bf16 v[168:171], a[80:83], a[36:39], v[168:171]\n"
      "ds_read_b64_tr_b16 a[112:113], v244 offset:33792\n"
      "v_or3_b32 v116, v116, v200, v201\n"
      "v_mfma_f32_16x16x32_bf16 v[168:171], a[72:75], a[20:23], v[168:171]\n"
      "v_exp_f32 v113, v149\n"
      "v_mfma_f32_16x16x32_bf16 v[168:171], a[64:67], a[4:7], v[168:171]\n"
      "ds_read_b64_tr_b16 a[114:115], v244 offset:37888\n"
      "v_cvt_pk_bf16_f32 v202, v112, v113\n"
      "v_mfma_f32_16x16x32_bf16 v[176:179], a[64:67], a[8:11], v[232:235]\n"
      "v_exp_f32 v114, v150\n"
      "v_mfma_f32_16x16x32_bf16 v[176:179], a[72:75], a[24:27], v[176:179]\n"
      "ds_read_b64_tr_b16 a[116:117], v245 offset:33792\n"
      "ds_read_b64_tr_b16 a[118:119], v245 offset:37888\n"
      "v_mfma_f32_16x16x32_bf16 v[176:179], a[80:83], a[40:43], v[176:179]\n"
      "v_exp_f32 v115, v151\n"
      "v_mfma_f32_16x16x32_bf16 v[176:179], a[88:91], a[56:59], v[176:179]\n"
      "ds_read_b64_tr_b16 a[120:121], v246 offset:33792\n"
      "v_cvt_pk_bf16_f32 v203, v114, v115\n"
      "v_mfma_f32_16x16x32_bf16 v[180:183], a[92:95], a[56:59], v[232:235]\n"
      "v_exp_f32 v112, v152\n"
      "v_mfma_f32_16x16x32_bf16 v[180:183], a[84:87], a[40:43], v[180:183]\n"
      "ds_read_b64_tr_b16 a[122:123], v246 offset:37888\n"
      "v_or3_b32 v116, v116, v202, v203\n"
      "v_mfma_f32_16x16x32_bf16 v[180:183], a[76:79], a[24:27], v[180:183]\n"
      "v_exp_f32 v113, v153\n"
      "v_mfma_f32_16x16x32_bf16 v[180:183], a[68:71], a[8:11], v[180:183]\n"
      "ds_read_b64_tr_b16 a[124:125], v247 offset:33792\n"
      "v_cvt_pk_bf16_f32 v204, v112, v113\n"
      "v_mfma_f32_16x16x32_bf16 v[188:191], a[68:71], a[12:15], v[236:239]\n"
      "v_exp_f32 v114, v154\n"
      "v_mfma_f32_16x16x32_bf16 v[188:191], a[76:79], a[28:31], v[188:191]\n"
      "ds_read_b64_tr_b16 a[126:127], v247 offset:37888\n"
      "v_mfma_f32_16x16x32_bf16 v[188:191], a[84:87], a[44:47], v[188:191]\n"
      "v_exp_f32 v115, v155\n"
      "v_mfma_f32_16x16x32_bf16 v[188:191], a[92:95], a[60:63], v[188:191]\n"
      "v_cvt_pk_bf16_f32 v205, v114, v115\n"
      "v_mfma_f32_16x16x32_bf16 v[184:187], a[88:91], a[60:63], v[236:239]\n"
      "v_exp_f32 v112, v156\n"
      "v_mfma_f32_16x16x32_bf16 v[184:187], a[80:83], a[44:47], v[184:187]\n"
      "v_or3_b32 v116, v116, v204, v205\n"
      "v_mfma_f32_16x16x32_bf16 v[184:187], a[72:75], a[28:31], v[184:187]\n"
      "v_exp_f32 v113, v157\n"
      "v_mfma_f32_16x16x32_bf16 v[184:187], a[64:67], a[12:15], v[184:187]\n"
      "s_waitcnt lgkmcnt(10)\n"
      "v_cvt_pk_bf16_f32 v206, v112, v113\n"
      "v_mfma_f32_16x16x32_bf16 v[32:35], a[96:99], v[208:211], v[32:35]\n"
      "v_exp_f32 v114, v158\n"
      "v_mfma_f32_16x16x32_bf16 v[48:51], a[100:103], v[208:211], v[48:51]\n"
      "v_mfma_f32_16x16x32_bf16 a[128:131], a[104:107], v[208:211], a[128:131]\n"
      "s_waitcnt lgkmcnt(6)\n"
      "v_exp_f32 v115, v159\n"
      "v_mfma_f32_16x16x32_bf16 a[144:147], a[108:111], v[208:211], a[144:147]\n"
      "v_cvt_pk_bf16_f32 v207, v114, v115\n"
      "v_mfma_f32_16x16x32_bf16 v[64:67], a[112:115], v[208:211], v[64:67]\n"
      "s_waitcnt lgkmcnt(2)\n"
      "v_mfma_f32_16x16x32_bf16 v[80:83], a[116:119], v[208:211], v[80:83]\n"
      "v_or3_b32 v116, v116, v206, v207\n"
      "v_mfma_f32_16x16x32_bf16 a[160:163], a[120:123], v[208:211], a[160:163]\n"
      "s_mov_b32 s64, 1\n"
      "s_waitcnt lgkmcnt(0)\n"
      "v_and_b32 v116, 0x40004000, v116\n"
      "v_mfma_f32_16x16x32_bf16 a[176:179], a[124:127], v[208:211], a[176:179]\n"
      "v_cmp_ne_u32 vcc, 0, v116\n"
      "s_cbranch_vccnz FB0_%=\n"
      "RET0_1_%=:\n"
      "v_mfma_f32_16x16x32_bf16 a[180:183], a[124:127], v[212:215], a[180:183]\n"
      "v_exp_f32 v112, v160\n"
      "v_mfma_f32_16x16x32_bf16 a[164:167], a[120:123], v[212:215], a[164:167]\n"
      "v_xor_b32 v242, 0x10000, v242\n"
      "v_mfma_f32_16x16x32_bf16 a[192:195], a[196:199], v[192:195], a[192:195]\n"
      "s_add_u32 m0, s77, 16384\n"
      "v_exp_f32 v113, v161\n"
      "v_mfma_f32_16x16x32_bf16 v[84:87], a[116:119], v[212:215], v[84:87]\n"
      "buffer_load_dwordx4 v249, s[24:27], s61 offen nt lds\n"
      "v_cvt_pk_bf16_f32 v208, v112, v113\n"
      "v_xor_b32 v243, 0x10000, v243\n"
      "v_mfma_f32_16x16x32_bf16 v[68:71], a[112:115], v[212:215], v[68:71]\n"
      "s_add_u32 m0, s77, 17408\n"
      "v_exp_f32 v114, v162\n"
      "v_mfma_f32_16x16x32_bf16 a[192:195], a[200:203], v[196:199], a[192:195]\n"
      "buffer_load_dwordx4 v121, s[24:27], s61 offen nt lds\n"
      "v_xor_b32 v244, 0x10000, v244\n"
      "v_mfma_f32_16x16x32_bf16 a[148:151], a[108:111], v[212:215], a[148:151]\n"
      "s_add_u32 m0, s77, 24576\n"
      "v_exp_f32 v115, v163\n"
      "v_mfma_f32_16x16x32_bf16 a[132:135], a[104:107], v[212:215], a[132:135]\n"
      "s_memrealtime s[68:69]\n"
      "buffer_load_dwordx4 v122, s[24:27], s61 offen nt lds\n"
      "v_cvt_pk_bf16_f32 v209, v114, v115\n"
      "v_xor_b32 v245, 0x10000, v245\n"
      "v_mfma_f32_16x16x32_bf16 a[192:195], a[204:207], v[200:203], a[192:195]\n"
      "s_add_u32 m0, s77, 25600\n"
      "v_exp_f32 v112, v164\n"
      "v_mfma_f32_16x16x32_bf16 v[52:55], a[100:103], v[212:215], v[52:55]\n"
      "s_add_u32 s77, s77, 0x8000\n"
      "buffer_load_dwordx4 v123, s[24:27], s61 offen nt lds\n"
      "v_or_b32 v116, v208, v209\n"
      "v_xor_b32 v246, 0x10000, v246\n"
      "v_mfma_f32_16x16x32_bf16 v[36:39], a[96:99], v[212:215], v[36:39]\n"
      "s_and_b32 s77, s77, 0x1ffff\n"
      "v_exp_f32 v113, v165\n"
      "v_mfma_f32_16x16x32_bf16 a[192:195], a[208:211], v[204:207], a[192:195]\n"
      "s_add_u32 s61, s61, s78\n"
      "v_cvt_pk_bf16_f32 v210, v112, v113\n"
      "v_xor_b32 v247, 0x10000, v247\n"
      "s_waitcnt vmcnt(8) lgkmcnt(0)\n"
      "s_sub_u32 s67, s68, s90\n"
      "s_cmp_lt_i32 s67, 0\n"
      "s_cbranch_scc1 GW3_%=\n"
      "GX3_%=:\n"
      "s_barrier\n"
      "v_mfma_f32_16x16x32_bf16 v[40:43], a[96:99], v[216:219], v[40:43]\n"
      "s_memrealtime s[68:69]\n"
      "buffer_load_dword v250, v251, s[28:31], 0 offen sc1\n"
      "ds_read_b128 a[64:67], v242 offset:0\n"
      "v_exp_f32 v114, v166\n"
      "v_mfma_f32_16x16x32_bf16 v[44:47], a[96:99], v[220:223], v[44:47]\n"
      "s_add_u32 m0, s62, 0\n"
      "ds_read_b128 a[68:71], v242 offset:4096\n"
      "ds_read_b128 a[72:75], v243 offset:0\n"
      "v_mfma_f32_16x16x32_bf16 v[60:63], a[100:103], v[220:223], v[60:63]\n"
      "buffer_load_dwordx4 v248, s[20:23], s60 offen nt lds\n"
      "v_exp_f32 v115, v167\n"
      "v_mfma_f32_16x16x32_bf16 v[56:59], a[100:103], v[216:219], v[56:59]\n"
      "s_add_u32 m0, s62, 896\n"
      "ds_read_b128 a[76:79], v243 offset:4096\n"
      "v_cvt_pk_bf16_f32 v211, v114, v115\n"
      "v_mfma_f32_16x16x32_bf16 a[136:139], a[104:107], v[216:219], a[136:139]\n"
      "buffer_load_dwordx4 v248, s[20:23], s60 offen offset:128 nt lds\n"
      "v_exp_f32 v112, v168\n"
      "v_mfma_f32_16x16x32_bf16 a[140:143], a[104:107], v[220:223], a[140:143]\n"
      "s_add_u32 m0, s62, 8192\n"
      "ds_read_b128 a[80:83], v242 offset:1024\n"
      "v_or3_b32 v116, v116, v210, v211\n"
      "v_mfma_f32_16x16x32_bf16 a[156:159], a[108:111], v[220:223], a[156:159]\n"
      "buffer_load_dwordx4 v120, s[20:23], s60 offen nt lds\n"
      "v_exp_f32 v113, v169\n"
      "v_mfma_f32_16x16x32_bf16 a[152:155], a[108:111], v[216:219], a[152:155]\n"
      "s_add_u32 m0, s62, 9088\n"
      "ds_read_b128 a[84:87], v242 offset:5120\n"
      "v_cvt_pk_bf16_f32 v212, v112, v113\n"
      "v_mfma_f32_16x16x32_bf16 v[72:75], a[112:115], v[216:219], v[72:75]\n"
      "s_add_u32 s62, s62, 0x8000\n"
      "buffer_load_dwordx4 v120, s[20:23], s60 offen offset:128 nt lds\n"
      "v_exp_f32 v114, v170\n"
      "v_mfma_f32_16x16x32_bf16 v[76:79], a[112:115], v[220:223], v[76:79]\n"
      "s_and_b32 s62, s62, 0x1ffff\n"
      "ds_read_b128 a[88:91], v243 offset:1024\n"
      "ds_read_b128 a[92:95], v243 offset:5120\n"
      "v_mfma_f32_16x16x32_bf16 v[92:95], a[116:119], v[220:223], v[92:95]\n"
      "s_add_u32 s60, s60, %[inc]\n"
      "v_exp_f32 v115, v171\n"
      "v_mfma_f32_16x16x32_bf16 v[88:91], a[116:119], v[216:219], v[88:91]\n"
      "v_cvt_pk_bf16_f32 v213, v114, v115\n"
      "v_mfma_f32_16x16x32_bf16 a[168:171], a[120:123], v[216:219], a[168:171]\n"
      "v_exp_f32 v112, v172\n"
      "v_mfma_f32_16x16x32_bf16 a[172:175], a[120:123], v[220:223], a[172:175]\n"
      "v_or3_b32 v116, v116, v212, v213\n"
      "v_mfma_f32_16x16x32_bf16 a[188:191], a[124:127], v[220:223], a[188:191]\n"
      "v_exp_f32 v113, v173\n"
      "v_mfma_f32_16x16x32_bf16 a[184:187], a[124:127], v[216:219], a[184:187]\n"
      "s_waitcnt lgkmcnt(0)\n"
      "v_cvt_pk_bf16_f32 v214, v112, v113\n"
      "v_mfma_f32_16x16x32_bf16 v[128:131], a[64:67], a[0:3], v[224:227]\n"
      "s_sub_u32 s67, s68, s66\n"
      "v_exp_f32 v114, v174\n"
      "v_mfma_f32_16x16x32_bf16 v[128:131], a[72:75], a[16:19], v[128:131]\n"
      "s_cmp_gt_i32 s67, s70\n"
      "ds_read_b64_tr_b16 a[96:97], v244 offset:0\n"
      "ds_read_b64_tr_b16 a[98:99], v244 offset:4096\n"
      "v_mfma_f32_16x16x32_bf16 v[128:131], a[80:83], a[32:35], v[128:131]\n"
      "s_cselect_b32 s69, s86, 0\n"
      "v_exp_f32 v115, v175\n"
      "v_mfma_f32_16x16x32_bf16 v[128:131], a[88:91], a[48:51], v[128:131]\n"
      "s_add_u32 s66, s66, s69\n"
      "ds_read_b64_tr_b16 a[100:101], v245 offset:0\n"
      "v_cvt_pk_bf16_f32 v215, v114, v115\n"
      "v_mfma_f32_16x16x32_bf16 v[132:135], a[92:95], a[48:51], v[224:227]\n"
      "s_add_u32 s67, s67, s101\n"
      "v_exp_f32 v112, v176\n"
      "v_mfma_f32_16x16x32_bf16 v[132:135], a[84:87], a[32:35], v[132:135]\n"
      "s_min_i32 s70, s70, s67\n"
      "ds_read_b64_tr_b16 a[102:103], v245 offset:4096\n"
      "v_or3_b32 v116, v116, v214, v215\n"
      "v_mfma_f32_16x16x32_bf16 v[132:135], a[76:79], a[16:19], v[132:135]\n"
      "s_max_i32 s70, s70, 85\n"
      "v_exp_f32 v113, v177\n"
      "v_mfma_f32_16x16x32_bf16 v[132:135], a[68:71], a[0:3], v[132:135]\n"
      "s_add_u32 s66, s66, s86\n"
      "ds_read_b64_tr_b16 a[104:105], v246 offset:0\n"
      "v_cvt_pk_bf16_f32 v216, v112, v113\n"
      "v_mfma_f32_16x16x32_bf16 v[140:143], a[68:71], a[4:7], v[228:231]\n"
      "s_and_b32 s76, s80, 31\n"
      "v_exp_f32 v114, v178\n"
      "v_mfma_f32_16x16x32_bf16 v[140:143], a[76:79], a[20:23], v[140:143]\n"
      "s_lshr_b64 s[80:81], s[80:81], 5\n"
      "ds_read_b64_tr_b16 a[106:107], v246 offset:4096\n"
      "ds_read_b64_tr_b16 a[108:109], v247 offset:0\n"
      "v_mfma_f32_16x16x32_bf16 v[140:143], a[84:87], a[36:39], v[140:143]\n"
      "s_add_u32 s66, s66, s76\n"
      "v_exp_f32 v115, v179\n"
      "v_mfma_f32_16x16x32_bf16 v[140:143], a[92:95], a[52:55], v[140:143]\n"
      "s_sub_u32 s67, s68, s82\n"
      "ds_read_b64_tr_b16 a[110:111], v247 offset:4096\n"
      "v_cvt_pk_bf16_f32 v217, v114, v115\n"
      "v_mfma_f32_16x16x32_bf16 v[136:139], a[88:91], a[52:55], v[228:231]\n"
      "s_min_i32 s76, s67, s83\n"
      "v_exp_f32 v112, v180\n"
      "v_mfma_f32_16x16x32_bf16 v[136:139], a[80:83], a[36:39], v[136:139]\n"
      "s_mov_b32 s83, s67\n"
      "ds_read_b64_tr_b16 a[112:113], v244 offset:1024\n"
      "v_or3_b32 v116, v116, v216, v217\n"
      "v_mfma_f32_16x16x32_bf16 v[136:139], a[72:75], a[20:23], v[136:139]\n"
      "s_sub_u32 s76, s76, 36\n"
      "v_exp_f32 v113, v181\n"
      "v_mfma_f32_16x16x32_bf16 v[136:139], a[64:67], a[4:7], v[136:139]\n"
      "s_min_i32 s76, s76, 148\n"
      "ds_read_b64_tr_b16 a[114:115], v244 offset:5120\n"
      "v_cvt_pk_bf16_f32 v218, v112, v113\n"
      "v_mfma_f32_16x16x32_bf16 v[144:147], a[64:67], a[8:11], v[232:235]\n"
      "s_max_i32 s76, s76, 70\n"
      "v_exp_f32 v114, v182\n"
      "v_mfma_f32_16x16x32_bf16 v[144:147], a[72:75], a[24:27], v[144:147]\n"
      "s_mov_b32 s82, s68\n"
      "ds_read_b64_tr_b16 a[116:117], v245 offset:1024\n"
      "ds_read_b64_tr_b16 a[118:119], v245 offset:5120\n"
      "v_mfma_f32_16x16x32_bf16 v[144:147], a[80:83], a[40:43], v[144:147]\n"
      "s_lshr_b32 s69, s86, 1\n"
      "v_exp_f32 v115, v183\n"
      "v_mfma_f32_16x16x32_bf16 v[144:147], a[88:91], a[56:59], v[144:147]\n"
      "s_add_u32 s66, s66, s69\n"
      "ds_read_b64_tr_b16 a[120:121], v246 offset:1024\n"
      "v_cvt_pk_bf16_f32 v219, v114, v115\n"
      "v_mfma_f32_16x16x32_bf16 v[148:151], a[92:95], a[56:59], v[232:235]\n"
      "s_add_u32 s82, s82, s69\n"
      "v_exp_f32 v112, v184\n"
      "v_mfma_f32_16x16x32_bf16 v[148:151], a[84:87], a[40:43], v[148:151]\n"
      "s_add_u32 s65, s82, s76\n"
      "ds_read_b64_tr_b16 a[122:123], v246 offset:5120\n"
      "v_or3_b32 v116, v116, v218, v219\n"
      "v_mfma_f32_16x16x32_bf16 v[148:151], a[76:79], a[24:27], v[148:151]\n"
      "s_sub_u32 s93, s93, 1\n"
      "v_exp_f32 v113, v185\n"
      "v_mfma_f32_16x16x32_bf16 v[148:151], a[68:71], a[8:11], v[148:151]\n"
      "s_cmp_le_i32 s93, 14\n"
      "ds_read_b64_tr_b16 a[124:125], v247 offset:1024\n"
      "v_cvt_pk_bf16_f32 v220, v112, v113\n"
      "v_mfma_f32_16x16x32_bf16 v[156:159], a[68:71], a[12:15], v[236:239]\n"
      "s_cselect_b32 s86, 107, s86\n"
      "v_exp_f32 v114, v186\n"
      "v_mfma_f32_16x16x32_bf16 v[156:159], a[76:79], a[28:31], v[156:159]\n"
      "s_sub_u32 s67, s65, s66\n"
      "ds_read_b64_tr_b16 a[126:127], v247 offset:5120\n"
      "v_mfma_f32_16x16x32_bf16 v[156:159], a[84:87], a[44:47], v[156:159]\n"
      "s_cmp_lt_i32 s67, 0\n"
      "v_exp_f32 v115, v187\n"
      "v_mfma_f32_16x16x32_bf16 v[156:159], a[92:95], a[60:63], v[156:159]\n"
      "s_cselect_b32 s88, s66, s65\n"
      "v_cvt_pk_bf16_f32 v221, v114, v115\n"
      "v_mfma_f32_16x16x32_bf16 v[152:155], a[88:91], a[60:63], v[236:239]\n"
      "s_add_u32 s90, s88, s91\n"
      "v_exp_f32 v112, v188\n"
      "v_mfma_f32_16x16x32_bf16 v[152:155], a[80:83], a[44:47], v[152:155]\n"
      "v_or3_b32 v116, v116, v220, v221\n"
      "v_mfma_f32_16x16x32_bf16 v[152:155], a[72:75], a[28:31], v[152:155]\n"
      "v_exp_f32 v113, v189\n"
      "v_mfma_f32_16x16x32_bf16 v[152:155], a[64:67], a[12:15], v[152:155]\n"
      "s_waitcnt lgkmcnt(10)\n"
      "v_cvt_pk_bf16_f32 v222, v112, v113\n"
      "v_mfma_f32_16x16x32_bf16 v[32:35], a[96:99], v[192:195], v[32:35]\n"
      "v_exp_f32 v114, v190\n"
      "v_mfma_f32_16x16x32_bf16 v[48:51], a[100:103], v[192:195], v[48:51]\n"
      "v_mfma_f32_16x16x32_bf16 a[128:131], a[104:107], v[192:195], a[128:131]\n"
      "s_waitcnt lgkmcnt(6)\n"
      "v_exp_f32 v115, v191\n"
      "v_mfma_f32_16x16x32_bf16 a[144:147], a[108:111], v[192:195], a[144:147]\n"
      "v_cvt_pk_bf16_f32 v223, v114, v115\n"
      "v_mfma_f32_16x16x32_bf16 v[64:67], a[112:115], v[192:195], v[64:67]\n"
      "s_waitcnt lgkmcnt(2)\n"
      "v_mfma_f32_16x16x32_bf16 v[80:83], a[116:119], v[192:195], v[80:83]\n"
      "v_or3_b32 v116, v116, v222, v223\n"
      "v_mfma_f32_16x16x32_bf16 a[160:163], a[120:123], v[192:195], a[160:163]\n"
      "s_mov_b32 s64, 2\n"
      "s_waitcnt lgkmcnt(0)\n"
      "v_and_b32 v116, 0x40004000, v116\n"
      "v_mfma_f32_16x16x32_bf16 a[176:179], a[124:127], v[192:195], a[176:179]\n"
      "v_cmp_ne_u32 vcc, 0, v116\n"
      "s_cbranch_vccnz FB1_%=\n"
      "RET1_2_%=:\n"
      "v_mfma_f32_16x16x32_bf16 a[180:183], a[124:127], v[196:199], a[180:183]\n"
      "v_exp_f32 v112, v128\n"
      "v_mfma_f32_16x16x32_bf16 a[164:167], a[120:123], v[196:199], a[164:167]\n"
      "v_mfma_f32_16x16x32_bf16 a[192:195], a[196:199], v[208:211], a[192:195]\n"
      "s_add_u32 m0, s77, 16384\n"
      "v_exp_f32 v113, v129\n"
      "v_mfma_f32_16x16x32_bf16 v[84:87], a[116:119], v[196:199], v[84:87]\n"
      "buffer_load_dwordx4 v249, s[24:27], s61 offen nt lds\n"
      "v_cvt_pk_bf16_f32 v192, v112, v113\n"
      "v_mfma_f32_16x16x32_bf16 v[68:71], a[112:115], v[196:199], v[68:71]\n"
      "s_add_u32 m0, s77, 17408\n"
      "v_exp_f32 v114, v130\n"
      "v_mfma_f32_16x16x32_bf16 a[192:195], a[200:203], v[212:215], a[192:195]\n"
      "buffer_load_dwordx4 v121, s[24:27], s61 offen nt lds\n"
      "v_mfma_f32_16x16x32_bf16 a[148:151], a[108:111], v[196:199], a[148:151]\n"
      "s_add_u32 m0, s77, 24576\n"
      "v_exp_f32 v115, v131\n"
      "v_mfma_f32_16x16x32_bf16 a[132:135], a[104:107], v[196:199], a[132:135]\n"
      "s_memrealtime s[68:69]\n"
      "buffer_load_dwordx4 v122, s[24:27], s61 offen nt lds\n"
      "v_cvt_pk_bf16_f32 v193, v114, v115\n"
      "v_mfma_f32_16x16x32_bf16 a[192:195], a[204:207], v[216:219], a[192:195]\n"
      "s_add_u32 m0, s77, 25600\n"
      "v_exp_f32 v112, v132\n"
      "v_mfma_f32_16x16x32_bf16 v[52:55], a[100:103], v[196:199], v[52:55]\n"
      "s_add_u32 s77, s77, 0x8000\n"
      "buffer_load_dwordx4 v123, s[24:27], s61 offen nt lds\n"
      "v_or_b32 v116, v192, v193\n"
      "v_mfma_f32_16x16x32_bf16 v[36:39], a[96:99], v[196:199], v[36:39]\n"
      "s_and_b32 s77, s77, 0x1ffff\n"
      "v_exp_f32 v113, v133\n"
      "v_mfma_f32_16x16x32_bf16 a[192:195], a[208:211], v[220:223], a[192:195]\n"
      "s_add_u32 s61, s61, %[inc]\n"
      "v_cvt_pk_bf16_f32 v194, v112, v113\n"
      "s_waitcnt vmcnt(9) lgkmcnt(0)\n"
      "s_barrier\n"
      "v_mfma_f32_16x16x32_bf16 v[40:43], a[96:99], v[200:203], v[40:43]\n"
      "ds_read_b128 a[64:67], v242 offset:32768\n"
      "v_exp_f32 v114, v134\n"
      "v_mfma_f32_16x16x32_bf16 v[44:47], a[96:99], v[204:207], v[44:47]\n"
      "ds_read_b128 a[68:71], v242 offset:36864\n"
      "ds_read_b128 a[72:75], v243 offset:32768\n"
      "v_mfma_f32_16x16x32_bf16 v[60:63], a[100:103], v[204:207], v[60:63]\n"
      "v_exp_f32 v115, v135\n"
      "v_mfma_f32_16x16x32_bf16 v[56:59], a[100:103], v[200:203], v[56:59]\n"
      "ds_read_b128 a[76:79], v243 offset:36864\n"
      "v_cvt_pk_bf16_f32 v195, v114, v115\n"
      "v_mfma_f32_16x16x32_bf16 a[136:139], a[104:107], v[200:203], a[136:139]\n"
      "v_exp_f32 v112, v136\n"
      "v_mfma_f32_16x16x32_bf16 a[140:143], a[104:107], v[204:207], a[140:143]\n"
      "ds_read_b128 a[80:83], v242 offset:33792\n"
      "v_or3_b32 v116, v116, v194, v195\n"
      "v_mfma_f32_16x16x32_bf16 a[156:159], a[108:111], v[204:207], a[156:159]\n"
      "v_exp_f32 v113, v137\n"
      "v_mfma_f32_16x16x32_bf16 a[152:155], a[108:111], v[200:203], a[152:155]\n"
      "ds_read_b128 a[84:87], v242 offset:37888\n"
      "v_cvt_pk_bf16_f32 v196, v112, v113\n"
      "v_mfma_f32_16x16x32_bf16 v[72:75], a[112:115], v[200:203], v[72:75]\n"
      "v_exp_f32 v114, v138\n"
      "v_mfma_f32_16x16x32_bf16 v[76:79], a[112:115], v[204:207], v[76:79]\n"
      "ds_read_b128 a[88:91], v243 offset:33792\n"
      "ds_read_b128 a[92:95], v243 offset:37888\n"
      "v_mfma_f32_16x16x32_bf16 v[92:95], a[116:119], v[204:207], v[92:95]\n"
      "v_exp_f32 v115, v139\n"
      "v_mfma_f32_16x16x32_bf16 v[88:91], a[116:119], v[200:203], v[88:91]\n"
      "v_cvt_pk_bf16_f32 v197, v114, v115\n"
      "v_mfma_f32_16x16x32_bf16 a[168:171], a[120:123], v[200:203], a[168:171]\n"
      "v_exp_f32 v112, v140\n"
      "v_mfma_f32_16x16x32_bf16 a[172:175], a[120:123], v[204:207], a[172:175]\n"
      "v_or3_b32 v116, v116, v196, v197\n"
      "v_mfma_f32_16x16x32_bf16 a[188:191], a[124:127], v[204:207], a[188:191]\n"
      "v_exp_f32 v113, v141\n"
      "v_mfma_f32_16x16x32_bf16 a[184:187], a[124:127], v[200:203], a[184:187]\n"
      "s_waitcnt lgkmcnt(0)\n"
      "v_cvt_pk_bf16_f32 v198, v112, v113\n"
      "v_mfma_f32_16x16x32_bf16 v[160:163], a[64:67], a[0:3], v[224:227]\n"
      "v_exp_f32 v114, v142\n"
      "v_mfma_f32_16x16x32_bf16 v[160:163], a[72:75], a[16:19], v[160:163]\n"
      "ds_read_b64_tr_b16 a[96:97], v244 offset:32768\n"
      "ds_read_b64_tr_b16 a[98:99], v244 offset:36864\n"
      "v_mfma_f32_16x16x32_bf16 v[160:163], a[80:83], a[32:35], v[160:163]\n"
      "v_exp_f32 v115, v143\n"
      "v_mfma_f32_16x16x32_bf16 v[160:163], a[88:91], a[48:51], v[160:163]\n"
      "ds_read_b64_tr_b16 a[100:101], v245 offset:32768\n"
      "v_cvt_pk_bf16_f32 v199, v114, v115\n"
      "v_mfma_f32_16x16x32_bf16 v[164:167], a[92:95], a[48:51], v[224:227]\n"
      "v_exp_f32 v112, v144\n"
      "v_mfma_f32_16x16x32_bf16 v[164:167], a[84:87], a[32:35], v[164:167]\n"
      "ds_read_b64_tr_b16 a[102:103], v245 offset:36864\n"
      "v_or3_b32 v116, v116, v198, v199\n"
      "v_mfma_f32_16x16x32_bf16 v[164:167], a[76:79], a[16:19], v[164:167]\n"
      "v_exp_f32 v113, v145\n"
      "v_mfma_f32_16x16x32_bf16 v[164:167], a[68:71], a[0:3], v[164:167]\n"
      "ds_read_b64_tr_b16 a[104:105], v246 offset:32768\n"
      "v_cvt_pk_bf16_f32 v200, v112, v113\n"
      "v_mfma_f32_16x16x32_bf16 v[172:175], a[68:71], a[4:7], v[228:231]\n"
      "v_exp_f32 v114, v146\n"
      "v_mfma_f32_16x16x32_bf16 v[172:175], a[76:79], a[20:23], v[172:175]\n"
      "ds_read_b64_tr_b16 a[106:107], v246 offset:36864\n"
      "ds_read_b64_tr_b16 a[108:109], v247 offset:32768\n"
      "v_mfma_f32_16x16x32_bf16 v[172:175], a[84:87], a[36:39], v[172:175]\n"
      "v_exp_f32 v115, v147\n"
      "v_mfma_f32_16x16x32_bf16 v[172:175], a[92:95], a[52:55], v[172:175]\n"
      "ds_read_b64_tr_b16 a[110:111], v247 offset:36864\n"
      "v_cvt_pk_bf16_f32 v201, v114, v115\n"
      "v_mfma_f32_16x16x32_bf16 v[168:171], a[88:91], a[52:55], v[228:231]\n"
      "v_exp_f32 v112, v148\n"
      "v_mfma_f32_16x16x32_bf16 v[168:171], a[80:83], a[36:39], v[168:171]\n"
      "ds_read_b64_tr_b16 a[112:113], v244 offset:33792\n"
      "v_or3_b32 v116, v116, v200, v201\n"
      "v_mfma_f32_16x16x32_bf16 v[168:171], a[72:75], a[20:23], v[168:171]\n"
      "v_exp_f32 v113, v149\n"
      "v_mfma_f32_16x16x32_bf16 v[168:171], a[64:67], a[4:7], v[168:171]\n"
      "ds_read_b64_tr_b16 a[114:115], v244 offset:37888\n"
      "v_cvt_pk_bf16_f32 v202, v112, v113\n"
      "v_mfma_f32_16x16x32_bf16 v[176:179], a[64:67], a[8:11], v[232:235]\n"
      "v_exp_f32 v114, v150\n"
      "v_mfma_f32_16x16x32_bf16 v[176:179], a[72:75], a[24:27], v[176:179]\n"
      "ds_read_b64_tr_b16 a[116:117], v245 offset:33792\n"
      "ds_read_b64_tr_b16 a[118:119], v245 offset:37888\n"
      "v_mfma_f32_16x16x32_bf16 v[176:179], a[80:83], a[40:43], v[176:179]\n"
      "v_exp_f32 v115, v151\n"
      "v_mfma_f32_16x16x32_bf16 v[176:179], a[88:91], a[56:59], v[176:179]\n"
      "ds_read_b64_tr_b16 a[120:121], v246 offset:33792\n"
      "v_cvt_pk_bf16_f32 v203, v114, v115\n"
      "v_mfma_f32_16x16x32_bf16 v[180:183], a[92:95], a[56:59], v[232:235]\n"
      "v_exp_f32 v112, v152\n"
      "v_mfma_f32_16x16x32_bf16 v[180:183], a[84:87], a[40:43], v[180:183]\n"
      "ds_read_b64_tr_b16 a[122:123], v246 offset:37888\n"
      "v_or3_b32 v116, v116, v202, v203\n"
      "v_mfma_f32_16x16x32_bf16 v[180:183], a[76:79], a[24:27], v[180:183]\n"
      "v_exp_f32 v113, v153\n"
      "v_mfma_f32_16x16x32_bf16 v[180:183], a[68:71], a[8:11], v[180:183]\n"
      "ds_read_b64_tr_b16 a[124:125], v247 offset:33792\n"
      "v_cvt_pk_bf16_f32 v204, v112, v113\n"
      "v_mfma_f32_16x16x32_bf16 v[188:191], a[68:71], a[12:15], v[236:239]\n"
      "v_exp_f32 v114, v154\n"
      "v_mfma_f32_16x16x32_bf16 v[188:191], a[76:79], a[28:31], v[188:191]\n"
      "ds_read_b64_tr_b16 a[126:127], v247 offset:37888\n"
      "v_mfma_f32_16x16x32_bf16 v[188:191], a[84:87], a[44:47], v[188:191]\n"
      "v_exp_f32 v115, v155\n"
      "v_mfma_f32_16x16x32_bf16 v[188:191], a[92:95], a[60:63], v[188:191]\n"
      "v_cvt_pk_bf16_f32 v205, v114, v115\n"
      "v_mfma_f32_16x16x32_bf16 v[184:187], a[88:91], a[60:63], v[236:239]\n"
      "v_exp_f32 v112, v156\n"
      "v_mfma_f32_16x16x32_bf16 v[184:187], a[80:83], a[44:47], v[184:187]\n"
      "v_or3_b32 v116, v116, v204, v205\n"
      "v_mfma_f32_16x16x32_bf16 v[184:187], a[72:75], a[28:31], v[184:187]\n"
      "v_exp_f32 v113, v157\n"
      "v_mfma_f32_16x16x32_bf16 v[184:187], a[64:67], a[12:15], v[184:187]\n"
      "s_waitcnt lgkmcnt(10)\n"
      "v_cvt_pk_bf16_f32 v206, v112, v113\n"
      "v_mfma_f32_16x16x32_bf16 v[32:35], a[96:99], v[208:211], v[32:35]\n"
      "v_exp_f32 v114, v158\n"
      "v_mfma_f32_16x16x32_bf16 v[48:51], a[100:103], v[208:211], v[48:51]\n"
      "v_mfma_f32_16x16x32_bf16 a[128:131], a[104:107], v[208:211], a[128:131]\n"
      "s_waitcnt lgkmcnt(6)\n"
      "v_exp_f32 v115, v159\n"
      "v_mfma_f32_16x16x32_bf16 a[144:147], a[108:111], v[208:211], a[144:147]\n"
      "v_cvt_pk_bf16_f32 v207, v114, v115\n"
      "v_mfma_f32_16x16x32_bf16 v[64:67], a[112:115], v[208:211], v[64:67]\n"
      "s_waitcnt lgkmcnt(2)\n"
      "v_mfma_f32_16x16x32_bf16 v[80:83], a[116:119], v[208:211], v[80:83]\n"
      "v_or3_b32 v116, v116, v206, v207\n"
      "v_mfma_f32_16x16x32_bf16 a[160:163], a[120:123], v[208:211], a[160:163]\n"
      "s_mov_b32 s64, 2\n"
      "s_waitcnt lgkmcnt(0)\n"
      "v_and_b32 v116, 0x40004000, v116\n"
      "v_mfma_f32_16x16x32_bf16 a[176:179], a[124:127], v[208:211], a[176:179]\n"
      "v_cmp_ne_u32 vcc, 0, v116\n"
      "s_cbranch_vccnz FB0_%=\n"
      "RET0_2_%=:\n"
      "v_mfma_f32_16x16x32_bf16 a[180:183], a[124:127], v[212:215], a[180:183]\n"
      "v_exp_f32 v112, v160\n"
      "v_mfma_f32_16x16x32_bf16 a[164:167], a[120:123], v[212:215], a[164:167]\n"
      "v_xor_b32 v242, 0x10000, v242\n"
      "v_mfma_f32_16x16x32_bf16 a[192:195], a[196:199], v[192:195], a[192:195]\n"
      "s_add_u32 m0, s62, 0\n"
      "v_exp_f32 v113, v161\n"
      "v_mfma_f32_16x16x32_bf16 v[84:87], a[116:119], v[212:215], v[84:87]\n"
      "buffer_load_dwordx4 v248, s[20:23], s60 offen nt lds\n"
      "v_cvt_pk_bf16_f32 v208, v112, v113\n"
      "v_xor_b32 v243, 0x10000, v243\n"
      "v_mfma_f32_16x16x32_bf16 v[68:71], a[112:115], v[212:215], v[68:71]\n"
      "s_add_u32 m0, s62, 896\n"
      "v_exp_f32 v114, v162\n"
      "v_mfma_f32_16x16x32_bf16 a[192:195], a[200:203], v[196:199], a[192:195]\n"
      "buffer_load_dwordx4 v248, s[20:23], s60 offen offset:128 nt lds\n"
      "v_xor_b32 v244, 0x10000, v244\n"
      "v_mfma_f32_16x16x32_bf16 a[148:151], a[108:111], v[212:215], a[148:151]\n"
      "s_add_u32 m0, s62, 8192\n"
      "v_exp_f32 v115, v163\n"
      "v_mfma_f32_16x16x32_bf16 a[132:135], a[104:107], v[212:215], a[132:135]\n"
      "s_memrealtime s[68:69]\n"
      "buffer_load_dwordx4 v120, s[20:23], s60 offen nt lds\n"
      "v_cvt_pk_bf16_f32 v209, v114, v115\n"
      "v_xor_b32 v245, 0x10000, v245\n"
      "v_mfma_f32_16x16x32_bf16 a[192:195], a[204:207], v[200:203], a[192:195]\n"
      "s_add_u32 m0, s62, 9088\n"
      "v_exp_f32 v112, v164\n"
      "v_mfma_f32_16x16x32_bf16 v[52:55], a[100:103], v[212:215], v[52:55]\n"
      "s_add_u32 s62, s62, 0x8000\n"
      "buffer_load_dwordx4 v120, s[20:23], s60 offen offset:128 nt lds\n"
      "v_or_b32 v116, v208, v209\n"
      "v_xor_b32 v246, 0x10000, v246\n"
      "v_mfma_f32_16x16x32_bf16 v[36:39], a[96:99], v[212:215], v[36:39]\n"
      "s_and_b32 s62, s62, 0x1ffff\n"
      "v_exp_f32 v113, v165\n"
      "v_mfma_f32_16x16x32_bf16 a[192:195], a[208:211], v[204:207], a[192:195]\n"
      "s_add_u32 s60, s60, s78\n"
      "v_cvt_pk_bf16_f32 v210, v112, v113\n"
      "v_xor_b32 v247, 0x10000, v247\n"
      "s_waitcnt vmcnt(8)\n"
      "s_sub_u32 s63, 11, %[n1]\n"
      "LOOP2_%=:\n"
      "s_waitcnt vmcnt(9) lgkmcnt(0)\n"
      "s_sub_u32 s67, s68, s90\n"
      "s_cmp_lt_i32 s67, 0\n"
      "s_cbranch_scc1 GW4_%=\n"
      "GX4_%=:\n"
      "s_barrier\n"
      "v_mfma_f32_16x16x32_bf16 v[40:43], a[96:99], v[216:219], v[40:43]\n"
      "s_memrealtime s[68:69]\n"
      "ds_read_b128 a[64:67], v242 offset:0\n"
      "v_exp_f32 v114, v166\n"
      "v_mfma_f32_16x16x32_bf16 v[44:47], a[96:99], v[220:223], v[44:47]\n"
      "s_add_u32 m0, s77, 16384\n"
      "ds_read_b128 a[68:71], v242 offset:4096\n"
      "ds_read_b128 a[72:75], v243 offset:0\n"
      "v_mfma_f32_16x16x32_bf16 v[60:63], a[100:103], v[220:223], v[60:63]\n"
      "buffer_load_dwordx4 v249, s[24:27], s61 offen nt lds\n"
      "v_exp_f32 v115, v167\n"
      "v_mfma_f32_16x16x32_bf16 v[56:59], a[100:103], v[216:219], v[56:59]\n"
      "s_add_u32 m0, s77, 17408\n"
      "ds_read_b128 a[76:79], v243 offset:4096\n"
      "v_cvt_pk_bf16_f32 v211, v114, v115\n"
      "v_mfma_f32_16x16x32_bf16 a[136:139], a[104:107], v[216:219], a[136:139]\n"
      "buffer_load_dwordx4 v121, s[24:27], s61 offen nt lds\n"
      "v_exp_f32 v112, v168\n"
      "v_mfma_f32_16x16x32_bf16 a[140:143], a[104:107], v[220:223], a[140:143]\n"
      "s_add_u32 m0, s77, 24576\n"
      "ds_read_b128 a[80:83], v242 offset:1024\n"
      "v_or3_b32 v116, v116, v210, v211\n"
      "v_mfma_f32_16x16x32_bf16 a[156:159], a[108:111], v[220:223], a[156:159]\n"
      "buffer_load_dwordx4 v122, s[24:27], s61 offen nt lds\n"
      "v_exp_f32 v113, v169\n"
      "v_mfma_f32_16x16x32_bf16 a[152:155], a[108:111], v[216:219], a[152:155]\n"
      "s_add_u32 m0, s77, 25600\n"
      "ds_read_b128 a[84:87], v242 offset:5120\n"
      "v_cvt_pk_bf16_f32 v212, v112, v113\n"
      "v_mfma_f32_16x16x32_bf16 v[72:75], a[112:115], v[216:219], v[72:75]\n"
      "s_add_u32 s77, s77, 0x8000\n"
      "buffer_load_dwordx4 v123, s[24:27], s61 offen nt lds\n"
      "v_exp_f32 v114, v170\n"
      "v_mfma_f32_16x16x32_bf16 v[76:79], a[112:115], v[220:223], v[76:79]\n"
      "s_and_b32 s77, s77, 0x1ffff\n"
      "ds_read_b128 a[88:91], v243 offset:1024\n"
      "ds_read_b128 a[92:95], v243 offset:5120\n"
      "v_mfma_f32_16x16x32_bf16 v[92:95], a[116:119], v[220:223], v[92:95]\n"
      "s_add_u32 s61, s61, s78\n"
      "v_exp_f32 v115, v171\n"
      "v_mfma_f32_16x16x32_bf16 v[88:91], a[116:119], v[216:219], v[88:91]\n"
      "v_cvt_pk_bf16_f32 v213, v114, v115\n"
      "v_mfma_f32_16x16x32_bf16 a[168:171], a[120:123], v[216:219], a[168:171]\n"
      "v_exp_f32 v112, v172\n"
      "v_mfma_f32_16x16x32_bf16 a[172:175], a[120:123], v[220:223], a[172:175]\n"
      "v_or3_b32 v116, v116, v212, v213\n"
      "v_mfma_f32_16x16x32_bf16 a[188:191], a[124:127], v[220:223], a[188:191]\n"
      "v_exp_f32 v113, v173\n"
      "v_mfma_f32_16x16x32_bf16 a[184:187], a[124:127], v[216:219], a[184:187]\n"
      "s_waitcnt lgkmcnt(0)\n"
      "v_cvt_pk_bf16_f32 v214, v112, v113\n"
      "v_mfma_f32_16x16x32_bf16 v[128:131], a[64:67], a[0:3], v[224:227]\n"
      "s_sub_u32 s67, s68, s66\n"
      "v_exp_f32 v114, v174\n"
      "v_mfma_f32_16x16x32_bf16 v[128:131], a[72:75], a[16:19], v[128:131]\n"
      "s_cmp_gt_i32 s67, s70\n"
      "v_mfma_f32_16x16x32_bf16 v[128:131], a[80:83], a[32:35], v[128:131]\n"
      "s_cselect_b32 s69, s86, 0\n"
      "v_exp_f32 v115, v175\n"
      "v_mfma_f32_16x16x32_bf16 v[128:131], a[88:91], a[48:51], v[128:131]\n"
      "s_add_u32 s66, s66, s69\n"
      "v_cvt_pk_bf16_f32 v215, v114, v115\n"
      "v_mfma_f32_16x16x32_bf16 v[132:135], a[92:95], a[48:51], v[224:227]\n"
      "s_add_u32 s67, s67, s101\n"
      "v_exp_f32 v112, v176\n"
      "v_mfma_f32_16x16x32_bf16 v[132:135], a[84:87], a[32:35], v[132:135]\n"
      "s_min_i32 s70, s70, s67\n"
      "v_or3_b32 v116, v116, v214, v215\n"
      "v_mfma_f32_16x16x32_bf16 v[132:135], a[76:79], a[16:19], v[132:135]\n"
      "s_max_i32 s70, s70, 85\n"
      "v_exp_f32 v113, v177\n"
      "v_mfma_f32_16x16x32_bf16 v[132:135], a[68:71], a[0:3], v[132:135]\n"
      "s_add_u32 s66, s66, s86\n"
      "v_cvt_pk_bf16_f32 v216, v112, v113\n"
      "v_mfma_f32_16x16x32_bf16 v[140:143], a[68:71], a[4:7], v[228:231]\n"
      "s_and_b32 s76, s80, 31\n"
      "v_exp_f32 v114, v178\n"
      "v_mfma_f32_16x16x32_bf16 v[140:143], a[76:79], a[20:23], v[140:143]\n"
      "s_lshr_b64 s[80:81], s[80:81], 5\n"
      "v_mfma_f32_16x16x32_bf16 v[140:143], a[84:87], a[36:39], v[140:143]\n"
      "s_add_u32 s66, s66, s76\n"
      "v_exp_f32 v115, v179\n"
      "v_mfma_f32_16x16x32_bf16 v[140:143], a[92:95], a[52:55], v[140:143]\n"
      "s_sub_u32 s67, s68, s82\n"
      "v_cvt_pk_bf16_f32 v217, v114, v115\n"
      "v_mfma_f32_16x16x32_bf16 v[136:139], a[88:91], a[52:55], v[228:231]\n"
      "s_min_i32 s76, s67, s83\n"
      "v_exp_f32 v112, v180\n"
      "s_waitcnt vmcnt(8)\n"
      "s_barrier\n"
      "v_mfma_f32_16x16x32_bf16 v[136:139], a[80:83], a[36:39], v[136:139]\n"
      "s_mov_b32 s83, s67\n"
      "ds_read_b64_tr_b16 a[96:97], v244 offset:0\n"
      "v_or3_b32 v116, v116, v216, v217\n"
      "v_mfma_f32_16x16x32_bf16 v[136:139], a[72:75], a[20:23], v[136:139]\n"
      "s_sub_u32 s76, s76, 36\n"
      "v_exp_f32 v113, v181\n"
      "v_mfma_f32_16x16x32_bf16 v[136:139], a[64:67], a[4:7], v[136:139]\n"
      "s_min_i32 s76, s76, 148\n"
      "ds_read_b64_tr_b16 a[98:99], v244 offset:4096\n"
      "v_cvt_pk_bf16_f32 v218, v112, v113\n"
      "v_mfma_f32_16x16x32_bf16 v[144:147], a[64:67], a[8:11], v[232:235]\n"
      "s_max_i32 s76, s76, 70\n"
      "v_exp_f32 v114, v182\n"
      "v_mfma_f32_16x16x32_bf16 v[144:147], a[72:75], a[24:27], v[144:147]\n"
      "s_mov_b32 s82, s68\n"
      "ds_read_b64_tr_b16 a[100:101], v245 offset:0\n"
      "ds_read_b64_tr_b16 a[102:103], v245 offset:4096\n"
      "v_mfma_f32_16x16x32_bf16 v[144:147], a[80:83], a[40:43], v[144:147]\n"
      "s_add_u32 s65, s82, s76\n"
      "v_exp_f32 v115, v183\n"
      "v_mfma_f32_16x16x32_bf16 v[144:147], a[88:91], a[56:59], v[144:147]\n"
      "s_sub_u32 s93, s93, 1\n"
      "ds_read_b64_tr_b16 a[104:105], v246 offset:0\n"
      "v_cvt_pk_bf16_f32 v219, v114, v115\n"
      "v_mfma_f32_16x16x32_bf16 v[148:151], a[92:95], a[56:59], v[232:235]\n"
      "s_cmp_le_i32 s93, 14\n"
      "v_exp_f32 v112, v184\n"
      "v_mfma_f32_16x16x32_bf16 v[148:151], a[84:87], a[40:43], v[148:151]\n"
      "s_cselect_b32 s86, 107, s86\n"
      "ds_read_b64_tr_b16 a[106:107], v246 offset:4096\n"
      "v_or3_b32 v116, v116, v218, v219\n"
      "v_mfma_f32_16x16x32_bf16 v[148:151], a[76:79], a[24:27], v[148:151]\n"
      "s_sub_u32 s67, s65, s66\n"
      "v_exp_f32 v113, v185\n"
      "v_mfma_f32_16x16x32_bf16 v[148:151], a[68:71], a[8:11], v[148:151]\n"
      "s_cmp_lt_i32 s67, 0\n"
      "ds_read_b64_tr_b16 a[108:109], v247 offset:0\n"
      "v_cvt_pk_bf16_f32 v220, v112, v113\n"
      "v_mfma_f32_16x16x32_bf16 v[156:159], a[68:71], a[12:15], v[236:239]\n"
      "s_cselect_b32 s88, s66, s65\n"
      "v_exp_f32 v114, v186\n"
      "v_mfma_f32_16x16x32_bf16 v[156:159], a[76:79], a[28:31], v[156:159]\n"
      "s_add_u32 s90, s88, s91\n"
      "ds_read_b64_tr_b16 a[110:111], v247 offset:4096\n"
      "ds_read_b64_tr_b16 a[112:113], v244 offset:1024\n"
      "v_mfma_f32_16x16x32_bf16 v[156:159], a[84:87], a[44:47], v[156:159]\n"
      "v_exp_f32 v115, v187\n"
      "v_mfma_f32_16x16x32_bf16 v[156:159], a[92:95], a[60:63], v[156:159]\n"
      "ds_read_b64_tr_b16 a[114:115], v244 offset:5120\n"
      "v_cvt_pk_bf16_f32 v221, v114, v115\n"
      "v_mfma_f32_16x16x32_bf16 v[152:155], a[88:91], a[60:63], v[236:239]\n"
      "v_exp_f32 v112, v188\n"
      "v_mfma_f32_16x16x32_bf16 v[152:155], a[80:83], a[44:47], v[152:155]\n"
      "ds_read_b64_tr_b16 a[116:117], v245 offset:1024\n"
      "v_or3_b32 v116, v116, v220, v221\n"
      "v_mfma_f32_16x16x32_bf16 v[152:155], a[72:75], a[28:31], v[152:155]\n"
      "v_exp_f32 v113, v189\n"
      "v_mfma_f32_16x16x32_bf16 v[152:155], a[64:67], a[12:15], v[152:155]\n"
      "ds_read_b64_tr_b16 a[118:119], v245 offset:5120\n"
      "v_cvt_pk_bf16_f32 v222, v112, v113\n"
      "s_waitcnt lgkmcnt(6)\n"
      "v_mfma_f32_16x16x32_bf16 v[32:35], a[96:99], v[192:195], v[32:35]\n"
      "v_exp_f32 v114, v190\n"
      "v_mfma_f32_16x16x32_bf16 v[48:51], a[100:103], v[192:195], v[48:51]\n"
      "ds_read_b64_tr_b16 a[120:121], v246 offset:1024\n"
      "ds_read_b64_tr_b16 a[122:123], v246 offset:5120\n"
      "v_mfma_f32_16x16x32_bf16 a[128:131], a[104:107], v[192:195], a[128:131]\n"
      "s_waitcnt lgkmcnt(4)\n"
      "v_exp_f32 v115, v191\n"
      "v_mfma_f32_16x16x32_bf16 a[144:147], a[108:111], v[192:195], a[144:147]\n"
      "ds_read_b64_tr_b16 a[124:125], v247 offset:1024\n"
      "v_cvt_pk_bf16_f32 v223, v114, v115\n"
      "v_mfma_f32_16x16x32_bf16 v[64:67], a[112:115], v[192:195], v[64:67]\n"
      "ds_read_b64_tr_b16 a[126:127], v247 offset:5120\n"
      "s_waitcnt lgkmcnt(2)\n"
      "v_mfma_f32_16x16x32_bf16 v[80:83], a[116:119], v[192:195], v[80:83]\n"
      "v_or3_b32 v116, v116, v222, v223\n"
      "v_mfma_f32_16x16x32_bf16 a[160:163], a[120:123], v[192:195], a[160:163]\n"
      "s_mov_b32 s64, 3\n"
      "s_waitcnt lgkmcnt(0)\n"
      "v_and_b32 v116, 0x40004000, v116\n"
      "v_mfma_f32_16x16x32_bf16 a[176:179], a[124:127], v[192:195], a[176:179]\n"
      "v_cmp_ne_u32 vcc, 0, v116\n"
      "s_cbranch_vccnz FB1_%=\n"
      "RET1_3_%=:\n"
      "v_mfma_f32_16x16x32_bf16 a[180:183], a[124:127], v[196:199], a[180:183]\n"
      "v_exp_f32 v112, v128\n"
      "v_mfma_f32_16x16x32_bf16 a[164:167], a[120:123], v[196:199], a[164:167]\n"
      "v_mfma_f32_16x16x32_bf16 a[192:195], a[196:199], v[208:211], a[192:195]\n"
      "s_add_u32 m0, s62, 0\n"
      "v_exp_f32 v113, v129\n"
      "v_mfma_f32_16x16x32_bf16 v[84:87], a[116:119], v[196:199], v[84:87]\n"
      "buffer_load_dwordx4 v248, s[20:23], s60 offen nt lds\n"
      "v_cvt_pk_bf16_f32 v192, v112, v113\n"
      "v_mfma_f32_16x16x32_bf16 v[68:71], a[112:115], v[196:199], v[68:71]\n"
      "s_add_u32 m0, s62, 896\n"
      "v_exp_f32 v114, v130\n"
      "v_mfma_f32_16x16x32_bf16 a[192:195], a[200:203], v[212:215], a[192:195]\n"
      "buffer_load_dwordx4 v248, s[20:23], s60 offen offset:128 nt lds\n"
      "v_mfma_f32_16x16x32_bf16 a[148:151], a[108:111], v[196:199], a[148:151]\n"
      "s_add_u32 m0, s62, 8192\n"
      "v_exp_f32 v115, v131\n"
      "v_mfma_f32_16x16x32_bf16 a[132:135], a[104:107], v[196:199], a[132:135]\n"
      "s_memrealtime s[68:69]\n"
      "buffer_load_dwordx4 v120, s[20:23], s60 offen nt lds\n"
      "v_cvt_pk_bf16_f32 v193, v114, v115\n"
      "v_mfma_f32_16x16x32_bf16 a[192:195], a[204:207], v[216:219], a[192:195]\n"
      "s_add_u32 m0, s62, 9088\n"
      "v_exp_f32 v112, v132\n"
      "v_mfma_f32_16x16x32_bf16 v[52:55], a[100:103], v[196:199], v[52:55]\n"
      "s_add_u32 s62, s62, 0x8000\n"
      "buffer_load_dwordx4 v120, s[20:23], s60 offen offset:128 nt lds\n"
      "v_or_b32 v116, v192, v193\n"
      "v_mfma_f32_16x16x32_bf16 v[36:39], a[96:99], v[196:199], v[36:39]\n"
      "s_and_b32 s62, s62, 0x1ffff\n"
      "v_exp_f32 v113, v133\n"
      "v_mfma_f32_16x16x32_bf16 a[192:195], a[208:211], v[220:223], a[192:195]\n"
      "s_add_u32 s60, s60, %[inc]\n"
      "v_cvt_pk_bf16_f32 v194, v112, v113\n"
      "s_waitcnt vmcnt(8) lgkmcnt(0)\n"
      "s_sub_u32 s67, s68, s90\n"
      "s_cmp_lt_i32 s67, 0\n"
      "s_cbranch_scc1 GW5_%=\n"
      "GX5_%=:\n"
      "s_barrier\n"
      "v_mfma_f32_16x16x32_bf16 v[40:43], a[96:99], v[200:203], v[40:43]\n"
      "s_memrealtime s[68:69]\n"
      "buffer_load_dword v250, v251, s[28:31], 0 offen sc1\n"
      "ds_read_b128 a[64:67], v242 offset:32768\n"
      "v_exp_f32 v114, v134\n"
      "v_mfma_f32_16x16x32_bf16 v[44:47], a[96:99], v[204:207], v[44:47]\n"
      "s_add_u32 m0, s77, 16384\n"
      "ds_read_b128 a[68:71], v242 offset:36864\n"
      "ds_read_b128 a[72:75], v243 offset:32768\n"
      "v_mfma_f32_16x16x32_bf16 v[60:63], a[100:103], v[204:207], v[60:63]\n"
      "buffer_load_dwordx4 v249, s[24:27], s61 offen nt lds\n"
      "v_exp_f32 v115, v135\n"
      "v_mfma_f32_16x16x32_bf16 v[56:59], a[100:103], v[200:203], v[56:59]\n"
      "s_add_u32 m0, s77, 17408\n"
      "ds_read_b128 a[76:79], v243 offset:36864\n"
      "v_cvt_pk_bf16_f32 v195, v114, v115\n"
      "v_mfma_f32_16x16x32_bf16 a[136:139], a[104:107], v[200:203], a[136:139]\n"
      "buffer_load_dwordx4 v121, s[24:27], s61 offen nt lds\n"
      "v_exp_f32 v112, v136\n"
      "v_mfma_f32_16x16x32_bf16 a[140:143], a[104:107], v[204:207], a[140:143]\n"
      "s_add_u32 m0, s77, 24576\n"
      "ds_read_b128 a[80:83], v242 offset:33792\n"
      "v_or3_b32 v116, v116, v194, v195\n"
      "v_mfma_f32_16x16x32_bf16 a[156:159], a[108:111], v[204:207], a[156:159]\n"
      "buffer_load_dwordx4 v122, s[24:27], s61 offen nt lds\n"
      "v_exp_f32 v113, v137\n"
      "v_mfma_f32_16x16x32_bf16 a[152:155], a[108:111], v[200:203], a[152:155]\n"
      "s_add_u32 m0, s77, 25600\n"
      "ds_read_b128 a[84:87], v242 offset:37888\n"
      "v_cvt_pk_bf16_f32 v196, v112, v113\n"
      "v_mfma_f32_16x16x32_bf16 v[72:75], a[112:115], v[200:203], v[72:75]\n"
      "s_add_u32 s77, s77, 0x8000\n"
      "buffer_load_dwordx4 v123, s[24:27], s61 offen nt lds\n"
      "v_exp_f32 v114, v138\n"
      "v_mfma_f32_16x16x32_bf16 v[76:79], a[112:115], v[204:207], v[76:79]\n"
      "s_and_b32 s77, s77, 0x1ffff\n"
      "ds_read_b128 a[88:91], v243 offset:33792\n"
      "ds_read_b128 a[92:95], v243 offset:37888\n"
      "v_mfma_f32_16x16x32_bf16 v[92:95], a[116:119], v[204:207], v[92:95]\n"
      "s_add_u32 s61, s61, %[inc]\n"
      "v_exp_f32 v115, v139\n"
      "v_mfma_f32_16x16x32_bf16 v[88:91], a[116:119], v[200:203], v[88:91]\n"
      "v_cvt_pk_bf16_f32 v197, v114, v115\n"
      "v_mfma_f32_16x16x32_bf16 a[168:171], a[120:123], v[200:203], a[168:171]\n"
      "v_exp_f32 v112, v140\n"
      "v_mfma_f32_16x16x32_bf16 a[172:175], a[120:123], v[204:207], a[172:175]\n"
      "v_or3_b32 v116, v116, v196, v197\n"
      "v_mfma_f32_16x16x32_bf16 a[188:191], a[124:127], v[204:207], a[188:191]\n"
      "v_exp_f32 v113, v141\n"
      "v_mfma_f32_16x16x32_bf16 a[184:187], a[124:127], v[200:203], a[184:187]\n"
      "s_waitcnt lgkmcnt(0)\n"
      "v_cvt_pk_bf16_f32 v198, v112, v113\n"
      "v_mfma_f32_16x16x32_bf16 v[160:163], a[64:67], a[0:3], v[224:227]\n"
      "s_sub_u32 s67, s68, s66\n"
      "v_exp_f32 v114, v142\n"
      "v_mfma_f32_16x16x32_bf16 v[160:163], a[72:75], a[16:19], v[160:163]\n"
      "s_cmp_gt_i32 s67, s70\n"
      "v_mfma_f32_16x16x32_bf16 v[160:163], a[80:83], a[32:35], v[160:163]\n"
      "s_cselect_b32 s69, s86, 0\n"
      "v_exp_f32 v115, v143\n"
      "v_mfma_f32_16x16x32_bf16 v[160:163], a[88:91], a[48:51], v[160:163]\n"
      "s_add_u32 s66, s66, s69\n"
      "v_cvt_pk_bf16_f32 v199, v114, v115\n"
      "v_mfma_f32_16x16x32_bf16 v[164:167], a[92:95], a[48:51], v[224:227]\n"
      "s_add_u32 s67, s67, s101\n"
      "v_exp_f32 v112, v144\n"
      "v_mfma_f32_16x16x32_bf16 v[164:167], a[84:87], a[32:35], v[164:167]\n"
      "s_min_i32 s70, s70, s67\n"
      "v_or3_b32 v116, v116, v198, v199\n"
      "v_mfma_f32_16x16x32_bf16 v[164:167], a[76:79], a[16:19], v[164:167]\n"
      "s_max_i32 s70, s70, 85\n"
      "v_exp_f32 v113, v145\n"
      "v_mfma_f32_16x16x32_bf16 v[164:167], a[68:71], a[0:3], v[164:167]\n"
      "s_add_u32 s66, s66, s86\n"
      "v_cvt_pk_bf16_f32 v200, v112, v113\n"
      "v_mfma_f32_16x16x32_bf16 v[172:175], a[68:71], a[4:7], v[228:231]\n"
      "s_and_b32 s76, s80, 31\n"
      "v_exp_f32 v114, v146\n"
      "v_mfma_f32_16x16x32_bf16 v[172:175], a[76:79], a[20:23], v[172:175]\n"
      "s_lshr_b64 s[80:81], s[80:81], 5\n"
      "v_mfma_f32_16x16x32_bf16 v[172:175], a[84:87], a[36:39], v[172:175]\n"
      "s_add_u32 s66, s66, s76\n"
      "v_exp_f32 v115, v147\n"
      "v_mfma_f32_16x16x32_bf16 v[172:175], a[92:95], a[52:55], v[172:175]\n"
      "s_sub_u32 s67, s68, s82\n"
      "v_cvt_pk_bf16_f32 v201, v114, v115\n"
      "v_mfma_f32_16x16x32_bf16 v[168:171], a[88:91], a[52:55], v[228:231]\n"
      "s_min_i32 s76, s67, s83\n"
      "v_exp_f32 v112, v148\n"
      "s_waitcnt vmcnt(9)\n"
      "s_barrier\n"
      "v_mfma_f32_16x16x32_bf16 v[168:171], a[80:83], a[36:39], v[168:171]\n"
      "s_mov_b32 s83, s67\n"
      "ds_read_b64_tr_b16 a[96:97], v244 offset:32768\n"
      "v_or3_b32 v116, v116, v200, v201\n"
      "v_mfma_f32_16x16x32_bf16 v[168:171], a[72:75], a[20:23], v[168:171]\n"
      "s_sub_u32 s76, s76, 36\n"
      "v_exp_f32 v113, v149\n"
      "v_mfma_f32_16x16x32_bf16 v[168:171], a[64:67], a[4:7], v[168:171]\n"
      "s_min_i32 s76, s76, 148\n"
      "ds_read_b64_tr_b16 a[98:99], v244 offset:36864\n"
      "v_cvt_pk_bf16_f32 v202, v112, v113\n"
      "v_mfma_f32_16x16x32_bf16 v[176:179], a[64:67], a[8:11], v[232:235]\n"
      "s_max_i32 s76, s76, 70\n"
      "v_exp_f32 v114, v150\n"
      "v_mfma_f32_16x16x32_bf16 v[176:179], a[72:75], a[24:27], v[176:179]\n"
      "s_mov_b32 s82, s68\n"
      "ds_read_b64_tr_b16 a[100:101], v245 offset:32768\n"
      "ds_read_b64_tr_b16 a[102:103], v245 offset:36864\n"
      "v_mfma_f32_16x16x32_bf16 v[176:179], a[80:83], a[40:43], v[176:179]\n"
      "s_add_u32 s65, s82, s76\n"
      "v_exp_f32 v115, v151\n"
      "v_mfma_f32_16x16x32_bf16 v[176:179], a[88:91], a[56:59], v[176:179]\n"
      "s_sub_u32 s93, s93, 1\n"
      "ds_read_b64_tr_b16 a[104:105], v246 offset:32768\n"
      "v_cvt_pk_bf16_f32 v203, v114, v115\n"
      "v_mfma_f32_16x16x32_bf16 v[180:183], a[92:95], a[56:59], v[232:235]\n"
      "s_cmp_le_i32 s93, 14\n"
      "v_exp_f32 v112, v152\n"
      "v_mfma_f32_16x16x32_bf16 v[180:183], a[84:87], a[40:43], v[180:183]\n"
      "s_cselect_b32 s86, 107, s86\n"
      "ds_read_b64_tr_b16 a[106:107], v246 offset:36864\n"
      "v_or3_b32 v116, v116, v202, v203\n"
      "v_mfma_f32_16x16x32_bf16 v[180:183], a[76:79], a[24:27], v[180:183]\n"
      "s_sub_u32 s67, s65, s66\n"
      "v_exp_f32 v113, v153\n"
      "v_mfma_f32_16x16x32_bf16 v[180:183], a[68:71], a[8:11], v[180:183]\n"
      "s_cmp_lt_i32 s67, 0\n"
      "ds_read_b64_tr_b16 a[108:109], v247 offset:32768\n"
      "v_cvt_pk_bf16_f32 v204, v112, v113\n"
      "v_mfma_f32_16x16x32_bf16 v[188:191], a[68:71], a[12:15], v[236:239]\n"
      "s_cselect_b32 s88, s66, s65\n"
      "v_exp_f32 v114, v154\n"
      "v_mfma_f32_16x16x32_bf16 v[188:191], a[76:79], a[28:31], v[188:191]\n"
      "s_add_u32 s90, s88, s91\n"
      "ds_read_b64_tr_b16 a[110:111], v247 offset:36864\n"
      "ds_read_b64_tr_b16 a[112:113], v244 offset:33792\n"
      "v_mfma_f32_16x16x32_bf16 v[188:191], a[84:87], a[44:47], v[188:191]\n"
      "v_exp_f32 v115, v155\n"
      "v_mfma_f32_16x16x32_bf16 v[188:191], a[92:95], a[60:63], v[188:191]\n"
      "ds_read_b64_tr_b16 a[114:115], v244 offset:37888\n"
      "v_cvt_pk_bf16_f32 v205, v114, v115\n"
      "v_mfma_f32_16x16x32_bf16 v[184:187], a[88:91], a[60:63], v[236:239]\n"
      "v_exp_f32 v112, v156\n"
      "v_mfma_f32_16x16x32_bf16 v[184:187], a[80:83], a[44:47], v[184:187]\n"
      "ds_read_b64_tr_b16 a[116:117], v245 offset:33792\n"
      "v_or3_b32 v116, v116, v204, v205\n"
      "v_mfma_f32_16x16x32_bf16 v[184:187], a[72:75], a[28:31], v[184:187]\n"
      "v_exp_f32 v113, v157\n"
      "v_mfma_f32_16x16x32_bf16 v[184:187], a[64:67], a[12:15], v[184:187]\n"
      "ds_read_b64_tr_b16 a[118:119], v245 offset:37888\n"
      "v_cvt_pk_bf16_f32 v206, v112, v113\n"
      "s_waitcnt lgkmcnt(6)\n"
      "v_mfma_f32_16x16x32_bf16 v[32:35], a[96:99], v[208:211], v[32:35]\n"
      "v_exp_f32 v114, v158\n"
      "v_mfma_f32_16x16x32_bf16 v[48:51], a[100:103], v[208:211], v[48:51]\n"
      "ds_read_b64_tr_b16 a[120:121], v246 offset:33792\n"
      "ds_read_b64_tr_b16 a[122:123], v246 offset:37888\n"
      "v_mfma_f32_16x16x32_bf16 a[128:131], a[104:107], v[208:211], a[128:131]\n"
      "s_waitcnt lgkmcnt(4)\n"
      "v_exp_f32 v115, v159\n"
      "v_mfma_f32_16x16x32_bf16 a[144:147], a[108:111], v[208:211], a[144:147]\n"
      "ds_read_b64_tr_b16 a[124:125], v247 offset:33792\n"
      "v_cvt_pk_bf16_f32 v207, v114, v115\n"
      "v_mfma_f32_16x16x32_bf16 v[64:67], a[112:115], v[208:211], v[64:67]\n"
      "ds_read_b64_tr_b16 a[126:127], v247 offset:37888\n"
      "s_waitcnt lgkmcnt(2)\n"
      "v_mfma_f32_16x16x32_bf16 v[80:83], a[116:119], v[208:211], v[80:83]\n"
      "v_or3_b32 v116, v116, v206, v207\n"
      "v_mfma_f32_16x16x32_bf16 a[160:163], a[120:123], v[208:211], a[160:163]\n"
      "s_mov_b32 s64, 3\n"
      "s_waitcnt lgkmcnt(0)\n"
      "v_and_b32 v116, 0x40004000, v116\n"
      "v_mfma_f32_16x16x32_bf16 a[176:179], a[124:127], v[208:211], a[176:179]\n"
      "v_cmp_ne_u32 vcc, 0, v116\n"
      "s_cbranch_vccnz FB0_%=\n"
      "RET0_3_%=:\n"
      "v_mfma_f32_16x16x32_bf16 a[180:183], a[124:127], v[212:215], a[180:183]\n"
      "v_exp_f32 v112, v160\n"
      "v_mfma_f32_16x16x32_bf16 a[164:167], a[120:123], v[212:215], a[164:167]\n"
      "v_xor_b32 v242, 0x10000, v242\n"
      "v_mfma_f32_16x16x32_bf16 a[192:195], a[196:199], v[192:195], a[192:195]\n"
      "s_add_u32 m0, s62, 0\n"
      "v_exp_f32 v113, v161\n"
      "v_mfma_f32_16x16x32_bf16 v[84:87], a[116:119], v[212:215], v[84:87]\n"
      "buffer_load_dwordx4 v248, s[20:23], s60 offen nt lds\n"
      "v_cvt_pk_bf16_f32 v208, v112, v113\n"
      "v_xor_b32 v243, 0x10000, v243\n"
      "v_mfma_f32_16x16x32_bf16 v[68:71], a[112:115], v[212:215], v[68:71]\n"
      "s_add_u32 m0, s62, 896\n"
      "v_exp_f32 v114, v162\n"
      "v_mfma_f32_16x16x32_bf16 a[192:195], a[200:203], v[196:199], a[192:195]\n"
      "buffer_load_dwordx4 v248, s[20:23], s60 offen offset:128 nt lds\n"
      "v_xor_b32 v244, 0x10000, v244\n"
      "v_mfma_f32_16x16x32_bf16 a[148:151], a[108:111], v[212:215], a[148:151]\n"
      "s_add_u32 m0, s62, 8192\n"
      "v_exp_f32 v115, v163\n"
      "v_mfma_f32_16x16x32_bf16 a[132:135], a[104:107], v[212:215], a[132:135]\n"
      "s_memrealtime s[68:69]\n"
      "buffer_load_dwordx4 v120, s[20:23], s60 offen nt lds\n"
      "v_cvt_pk_bf16_f32 v209, v114, v115\n"
      "v_xor_b32 v245, 0x10000, v245\n"
      "v_mfma_f32_16x16x32_bf16 a[192:195], a[204:207], v[200:203], a[192:195]\n"
      "s_add_u32 m0, s62, 9088\n"
      "v_exp_f32 v112, v164\n"
      "v_mfma_f32_16x16x32_bf16 v[52:55], a[100:103], v[212:215], v[52:55]\n"
      "s_add_u32 s62, s62, 0x8000\n"
      "buffer_load_dwordx4 v120, s[20:23], s60 offen offset:128 nt lds\n"
      "v_or_b32 v116, v208, v209\n"
      "v_xor_b32 v246, 0x10000, v246\n"
      "v_mfma_f32_16x16x32_bf16 v[36:39], a[96:99], v[212:215], v[36:39]\n"
      "s_and_b32 s62, s62, 0x1ffff\n"
      "s_add_u32 s60, s60, s78\n"
      "v_exp_f32 v113, v165\n"
      "v_mfma_f32_16x16x32_bf16 a[192:195], a[208:211], v[204:207], a[192:195]\n"
      "s_sub_u32 s63, s63, 1\n"
      "v_cvt_pk_bf16_f32 v210, v112, v113\n"
      "v_xor_b32 v247, 0x10000, v247\n"
      "s_cbranch_scc0 LOOP2_%=\n"
      "s_waitcnt vmcnt(9) lgkmcnt(0)\n"
      "s_sub_u32 s67, s68, s90\n"
      "s_cmp_lt_i32 s67, 0\n"
      "s_cbranch_scc1 GW6_%=\n"
      "GX6_%=:\n"
      "s_barrier\n"
      "v_mfma_f32_16x16x32_bf16 v[40:43], a[96:99], v[216:219], v[40:43]\n"
      "s_memrealtime s[68:69]\n"
      "ds_read_b128 a[64:67], v242 offset:0\n"
      "v_exp_f32 v114, v166\n"
      "v_mfma_f32_16x16x32_bf16 v[44:47], a[96:99], v[220:223], v[44:47]\n"
      "s_add_u32 m0, %[wb], 50176\n"
      "ds_read_b128 a[68:71], v242 offset:4096\n"
      "ds_read_b128 a[72:75], v243 offset:0\n"
      "v_mfma_f32_16x16x32_bf16 v[60:63], a[100:103], v[220:223], v[60:63]\n"
      "s_add_u32 s94, s61, s78\n"
      "buffer_load_dwordx4 v121, s[24:27], s61 offen nt lds\n"
      "v_exp_f32 v115, v167\n"
      "v_mfma_f32_16x16x32_bf16 v[56:59], a[100:103], v[216:219], v[56:59]\n"
      "s_add_u32 m0, %[wb], 58368\n"
      "ds_read_b128 a[76:79], v243 offset:4096\n"
      "v_cvt_pk_bf16_f32 v211, v114, v115\n"
      "v_mfma_f32_16x16x32_bf16 a[136:139], a[104:107], v[216:219], a[136:139]\n"
      "s_add_u32 s95, s61, 0x10000\n"
      "buffer_load_dwordx4 v123, s[24:27], s61 offen nt lds\n"
      "v_exp_f32 v112, v168\n"
      "v_mfma_f32_16x16x32_bf16 a[140:143], a[104:107], v[220:223], a[140:143]\n"
      "s_add_u32 s77, s94, 0x10000\n"
      "ds_read_b128 a[80:83], v242 offset:1024\n"
      "v_or3_b32 v116, v116, v210, v211\n"
      "v_mfma_f32_16x16x32_bf16 a[156:159], a[108:111], v[220:223], a[156:159]\n"
      "s_add_u32 s78, s61, 0x20000\n"
      "v_exp_f32 v113, v169\n"
      "v_mfma_f32_16x16x32_bf16 a[152:155], a[108:111], v[216:219], a[152:155]\n"
      "ds_read_b128 a[84:87], v242 offset:5120\n"
      "v_cvt_pk_bf16_f32 v212, v112, v113\n"
      "v_mfma_f32_16x16x32_bf16 v[72:75], a[112:115], v[216:219], v[72:75]\n"
      "v_exp_f32 v114, v170\n"
      "v_mfma_f32_16x16x32_bf16 v[76:79], a[112:115], v[220:223], v[76:79]\n"
      "ds_read_b128 a[88:91], v243 offset:1024\n"
      "ds_read_b128 a[92:95], v243 offset:5120\n"
      "v_mfma_f32_16x16x32_bf16 v[92:95], a[116:119], v[220:223], v[92:95]\n"
      "v_exp_f32 v115, v171\n"
      "v_mfma_f32_16x16x32_bf16 v[88:91], a[116:119], v[216:219], v[88:91]\n"
      "v_cvt_pk_bf16_f32 v213, v114, v115\n"
      "v_mfma_f32_16x16x32_bf16 a[168:171], a[120:123], v[216:219], a[168:171]\n"
      "v_exp_f32 v112, v172\n"
      "v_mfma_f32_16x16x32_bf16 a[172:175], a[120:123], v[220:223], a[172:175]\n"
      "v_or3_b32 v116, v116, v212, v213\n"
      "v_mfma_f32_16x16x32_bf16 a[188:191], a[124:127], v[220:223], a[188:191]\n"
      "v_exp_f32 v113, v173\n"
      "v_mfma_f32_16x16x32_bf16 a[184:187], a[124:127], v[216:219], a[184:187]\n"
      "s_waitcnt lgkmcnt(0)\n"
      "v_cvt_pk_bf16_f32 v214, v112, v113\n"
      "v_mfma_f32_16x16x32_bf16 v[128:131], a[64:67], a[0:3], v[224:227]\n"
      "s_sub_u32 s67, s68, s66\n"
      "v_exp_f32 v114, v174\n"
      "v_mfma_f32_16x16x32_bf16 v[128:131], a[72:75], a[16:19], v[128:131]\n"
      "s_cmp_gt_i32 s67, s70\n"
      "v_mfma_f32_16x16x32_bf16 v[128:131], a[80:83], a[32:35], v[128:131]\n"
      "s_cselect_b32 s69, s86, 0\n"
      "v_exp_f32 v115, v175\n"
      "v_mfma_f32_16x16x32_bf16 v[128:131], a[88:91], a[48:51], v[128:131]\n"
      "s_add_u32 s66, s66, s69\n"
      "v_cvt_pk_bf16_f32 v215, v114, v115\n"
      "v_mfma_f32_16x16x32_bf16 v[132:135], a[92:95], a[48:51], v[224:227]\n"
      "s_add_u32 s67, s67, s101\n"
      "v_exp_f32 v112, v176\n"
      "v_mfma_f32_16x16x32_bf16 v[132:135], a[84:87], a[32:35], v[132:135]\n"
      "s_min_i32 s70, s70, s67\n"
      "v_or3_b32 v116, v116, v214, v215\n"
      "v_mfma_f32_16x16x32_bf16 v[132:135], a[76:79], a[16:19], v[132:135]\n"
      "s_max_i32 s70, s70, 85\n"
      "v_exp_f32 v113, v177\n"
      "v_mfma_f32_16x16x32_bf16 v[132:135], a[68:71], a[0:3], v[132:135]\n"
      "s_add_u32 s66, s66, s86\n"
      "v_cvt_pk_bf16_f32 v216, v112, v113\n"
      "v_mfma_f32_16x16x32_bf16 v[140:143], a[68:71], a[4:7], v[228:231]\n"
      "s_and_b32 s76, s80, 31\n"
      "v_exp_f32 v114, v178\n"
      "v_mfma_f32_16x16x32_bf16 v[140:143], a[76:79], a[20:23], v[140:143]\n"
      "s_lshr_b64 s[80:81], s[80:81], 5\n"
      "v_mfma_f32_16x16x32_bf16 v[140:143], a[84:87], a[36:39], v[140:143]\n"
      "s_add_u32 s66, s66, s76\n"
      "v_exp_f32 v115, v179\n"
      "v_mfma_f32_16x16x32_bf16 v[140:143], a[92:95], a[52:55], v[140:143]\n"
      "s_sub_u32 s67, s68, s82\n"
      "v_cvt_pk_bf16_f32 v217, v114, v115\n"
      "v_mfma_f32_16x16x32_bf16 v[136:139], a[88:91], a[52:55], v[228:231]\n"
      "s_min_i32 s76, s67, s83\n"
      "v_exp_f32 v112, v180\n"
      "s_waitcnt vmcnt(6)\n"
      "s_barrier\n"
      "v_mfma_f32_16x16x32_bf16 v[136:139], a[80:83], a[36:39], v[136:139]\n"
      "s_mov_b32 s83, s67\n"
      "ds_read_b64_tr_b16 a[96:97], v244 offset:0\n"
      "v_or3_b32 v116, v116, v216, v217\n"
      "v_mfma_f32_16x16x32_bf16 v[136:139], a[72:75], a[20:23], v[136:139]\n"
      "s_add_u32 m0, s62, 0\n"
      "v_exp_f32 v113, v181\n"
      "v_mfma_f32_16x16x32_bf16 v[136:139], a[64:67], a[4:7], v[136:139]\n"
      "s_sub_u32 s76, s76, 36\n"
      "ds_read_b64_tr_b16 a[98:99], v244 offset:4096\n"
      "buffer_load_dwordx4 v248, s[20:23], s60 offen nt lds\n"
      "v_cvt_pk_bf16_f32 v218, v112, v113\n"
      "v_mfma_f32_16x16x32_bf16 v[144:147], a[64:67], a[8:11], v[232:235]\n"
      "s_add_u32 m0, s62, 896\n"
      "v_exp_f32 v114, v182\n"
      "v_mfma_f32_16x16x32_bf16 v[144:147], a[72:75], a[24:27], v[144:147]\n"
      "s_min_i32 s76, s76, 148\n"
      "ds_read_b64_tr_b16 a[100:101], v245 offset:0\n"
      "ds_read_b64_tr_b16 a[102:103], v245 offset:4096\n"
      "buffer_load_dwordx4 v248, s[20:23], s60 offen offset:128 nt lds\n"
      "v_mfma_f32_16x16x32_bf16 v[144:147], a[80:83], a[40:43], v[144:147]\n"
      "s_add_u32 m0, s62, 8192\n"
      "v_exp_f32 v115, v183\n"
      "v_mfma_f32_16x16x32_bf16 v[144:147], a[88:91], a[56:59], v[144:147]\n"
      "s_max_i32 s76, s76, 70\n"
      "ds_read_b64_tr_b16 a[104:105], v246 offset:0\n"
      "buffer_load_dwordx4 v120, s[20:23], s60 offen nt lds\n"
      "v_cvt_pk_bf16_f32 v219, v114, v115\n"
      "v_mfma_f32_16x16x32_bf16 v[148:151], a[92:95], a[56:59], v[232:235]\n"
      "s_add_u32 m0, s62, 9088\n"
      "v_exp_f32 v112, v184\n"
      "v_mfma_f32_16x16x32_bf16 v[148:151], a[84:87], a[40:43], v[148:151]\n"
      "s_add_u32 s62, s62, 0x8000\n"
      "ds_read_b64_tr_b16 a[106:107], v246 offset:4096\n"
      "buffer_load_dwordx4 v120, s[20:23], s60 offen offset:128 nt lds\n"
      "v_or3_b32 v116, v116, v218, v219\n"
      "v_mfma_f32_16x16x32_bf16 v[148:151], a[76:79], a[24:27], v[148:151]\n"
      "s_and_b32 s62, s62, 0x1ffff\n"
      "v_exp_f32 v113, v185\n"
      "v_mfma_f32_16x16x32_bf16 v[148:151], a[68:71], a[8:11], v[148:151]\n"
      "s_add_u32 s60, s60, %[inc]\n"
      "ds_read_b64_tr_b16 a[108:109], v247 offset:0\n"
      "v_cvt_pk_bf16_f32 v220, v112, v113\n"
      "v_mfma_f32_16x16x32_bf16 v[156:159], a[68:71], a[12:15], v[236:239]\n"
      "s_mov_b32 s82, s68\n"
      "v_exp_f32 v114, v186\n"
      "v_mfma_f32_16x16x32_bf16 v[156:159], a[76:79], a[28:31], v[156:159]\n"
      "s_sub_u32 s66, s66, s99\n"
      "ds_read_b64_tr_b16 a[110:111], v247 offset:4096\n"
      "ds_read_b64_tr_b16 a[112:113], v244 offset:1024\n"
      "v_mfma_f32_16x16x32_bf16 v[156:159], a[84:87], a[44:47], v[156:159]\n"
      "s_sub_u32 s82, s82, s99\n"
      "v_exp_f32 v115, v187\n"
      "v_mfma_f32_16x16x32_bf16 v[156:159], a[92:95], a[60:63], v[156:159]\n"
      "s_add_u32 s65, s82, s76\n"
      "ds_read_b64_tr_b16 a[114:115], v244 offset:5120\n"
      "v_cvt_pk_bf16_f32 v221, v114, v115\n"
      "v_mfma_f32_16x16x32_bf16 v[152:155], a[88:91], a[60:63], v[236:239]\n"
      "s_sub_u32 s93, s93, 1\n"
      "v_exp_f32 v112, v188\n"
      "v_mfma_f32_16x16x32_bf16 v[152:155], a[80:83], a[44:47], v[152:155]\n"
      "s_cmp_le_i32 s93, 14\n"
      "ds_read_b64_tr_b16 a[116:117], v245 offset:1024\n"
      "v_or3_b32 v116, v116, v220, v221\n"
      "v_mfma_f32_16x16x32_bf16 v[152:155], a[72:75], a[28:31], v[152:155]\n"
      "s_cselect_b32 s86, 107, s86\n"
      "v_exp_f32 v113, v189\n"
      "v_mfma_f32_16x16x32_bf16 v[152:155], a[64:67], a[12:15], v[152:155]\n"
      "ds_read_b64_tr_b16 a[118:119], v245 offset:5120\n"
      "v_cvt_pk_bf16_f32 v222, v112, v113\n"
      "s_waitcnt lgkmcnt(6)\n"
      "v_mfma_f32_16x16x32_bf16 v[32:35], a[96:99], v[192:195], v[32:35]\n"
      "s_sub_u32 s67, s65, s66\n"
      "v_exp_f32 v114, v190\n"
      "v_mfma_f32_16x16x32_bf16 v[48:51], a[100:103], v[192:195], v[48:51]\n"
      "s_cmp_lt_i32 s67, 0\n"
      "s_cselect_b32 s88, s66, s65\n"
      "ds_read_b64_tr_b16 a[120:121], v246 offset:1024\n"
      "ds_read_b64_tr_b16 a[122:123], v246 offset:5120\n"
      "v_mfma_f32_16x16x32_bf16 a[128:131], a[104:107], v[192:195], a[128:131]\n"
      "s_waitcnt lgkmcnt(4)\n"
      "v_exp_f32 v115, v191\n"
      "v_mfma_f32_16x16x32_bf16 a[144:147], a[108:111], v[192:195], a[144:147]\n"
      "s_add_u32 s90, s88, s91\n"
      "ds_read_b64_tr_b16 a[124:125], v247 offset:1024\n"
      "v_cvt_pk_bf16_f32 v223, v114, v115\n"
      "v_mfma_f32_16x16x32_bf16 v[64:67], a[112:115], v[192:195], v[64:67]\n"
      "ds_read_b64_tr_b16 a[126:127], v247 offset:5120\n"
      "s_waitcnt lgkmcnt(2)\n"
      "v_mfma_f32_16x16x32_bf16 v[80:83], a[116:119], v[192:195], v[80:83]\n"
      "v_or3_b32 v116, v116, v222, v223\n"
      "v_mfma_f32_16x16x32_bf16 a[160:163], a[120:123], v[192:195], a[160:163]\n"
      "s_mov_b32 s64, 4\n"
      "s_waitcnt lgkmcnt(0)\n"
      "v_and_b32 v116, 0x40004000, v116\n"
      "v_mfma_f32_16x16x32_bf16 a[176:179], a[124:127], v[192:195], a[176:179]\n"
      "v_cmp_ne_u32 vcc, 0, v116\n"
      "s_cbranch_vccnz FB1_%=\n"
      "RET1_4_%=:\n"
      "v_mfma_f32_16x16x32_bf16 a[180:183], a[124:127], v[196:199], a[180:183]\n"
      "v_exp_f32 v112, v128\n"
      "v_mfma_f32_16x16x32_bf16 a[164:167], a[120:123], v[196:199], a[164:167]\n"
      "v_xor_b32 v240, 0x10000, v244\n"
      "v_mfma_f32_16x16x32_bf16 a[192:195], a[196:199], v[208:211], a[192:195]\n"
      "v_exp_f32 v113, v129\n"
      "v_mfma_f32_16x16x32_bf16 v[84:87], a[116:119], v[196:199], v[84:87]\n"
      "v_cvt_pk_bf16_f32 v192, v112, v113\n"
      "v_xor_b32 v241, 0x10000, v245\n"
      "v_mfma_f32_16x16x32_bf16 v[68:71], a[112:115], v[196:199], v[68:71]\n"
      "v_exp_f32 v114, v130\n"
      "v_mfma_f32_16x16x32_bf16 a[192:195], a[200:203], v[212:215], a[192:195]\n"
      "v_xor_b32 v250, 0x10000, v246\n"
      "v_mfma_f32_16x16x32_bf16 a[148:151], a[108:111], v[196:199], a[148:151]\n"
      "v_exp_f32 v115, v131\n"
      "v_mfma_f32_16x16x32_bf16 a[132:135], a[104:107], v[196:199], a[132:135]\n"
      "s_memrealtime s[68:69]\n"
      "v_cvt_pk_bf16_f32 v193, v114, v115\n"
      "v_xor_b32 v251, 0x10000, v247\n"
      "v_mfma_f32_16x16x32_bf16 a[192:195], a[204:207], v[216:219], a[192:195]\n"
      "v_exp_f32 v112, v132\n"
      "v_mfma_f32_16x16x32_bf16 v[52:55], a[100:103], v[196:199], v[52:55]\n"
      "v_or_b32 v116, v192, v193\n"
      "v_mfma_f32_16x16x32_bf16 v[36:39], a[96:99], v[196:199], v[36:39]\n"
      "v_exp_f32 v113, v133\n"
      "v_mfma_f32_16x16x32_bf16 a[192:195], a[208:211], v[220:223], a[192:195]\n"
      "v_cvt_pk_bf16_f32 v194, v112, v113\n"
      "s_waitcnt vmcnt(6) lgkmcnt(0)\n"
      "s_sub_u32 s67, s68, s90\n"
      "s_cmp_lt_i32 s67, 0\n"
      "s_cbranch_scc1 GW7_%=\n"
      "GX7_%=:\n"
      "s_barrier\n"
      "v_mfma_f32_16x16x32_bf16 v[40:43], a[96:99], v[200:203], v[40:43]\n"
      "s_memrealtime s[68:69]\n"
      "ds_read_b128 a[64:67], v242 offset:32768\n"
      "v_exp_f32 v114, v134\n"
      "v_mfma_f32_16x16x32_bf16 v[44:47], a[96:99], v[204:207], v[44:47]\n"
      "s_add_u32 m0, %[wb], 82944\n"
      "ds_read_b128 a[68:71], v242 offset:36864\n"
      "v_mfma_f32_16x16x32_bf16 v[60:63], a[100:103], v[204:207], v[60:63]\n"
      "ds_read_b128 a[72:75], v243 offset:32768\n"
      "buffer_load_dwordx4 v121, s[24:27], s94 offen nt lds\n"
      "v_mfma_f32_16x16x32_bf16 v[56:59], a[100:103], v[200:203], v[56:59]\n"
      "s_add_u32 m0, %[wb], 91136\n"
      "ds_read_b128 a[76:79], v243 offset:36864\n"
      "v_mfma_f32_16x16x32_bf16 a[136:139], a[104:107], v[200:203], a[136:139]\n"
      "ds_read_b128 a[80:83], v242 offset:33792\n"
      "buffer_load_dwordx4 v123, s[24:27], s94 offen nt lds\n"
      "v_mfma_f32_16x16x32_bf16 a[140:143], a[104:107], v[204:207], a[140:143]\n"
      "ds_read_b128 a[84:87], v242 offset:37888\n"
      "v_mfma_f32_16x16x32_bf16 a[156:159], a[108:111], v[204:207], a[156:159]\n"
      "ds_read_b128 a[88:91], v243 offset:33792\n"
      "v_mfma_f32_16x16x32_bf16 a[152:155], a[108:111], v[200:203], a[152:155]\n"
      "ds_read_b128 a[92:95], v243 offset:37888\n"
      "v_mfma_f32_16x16x32_bf16 v[72:75], a[112:115], v[200:203], v[72:75]\n"
      "v_exp_f32 v115, v135\n"
      "v_mfma_f32_16x16x32_bf16 v[76:79], a[112:115], v[204:207], v[76:79]\n"
      "v_exp_f32 v112, v136\n"
      "v_mfma_f32_16x16x32_bf16 v[92:95], a[116:119], v[204:207], v[92:95]\n"
      "v_cvt_pk_bf16_f32 v195, v114, v115\n"
      "v_exp_f32 v113, v137\n"
      "v_mfma_f32_16x16x32_bf16 v[88:91], a[116:119], v[200:203], v[88:91]\n"
      "v_or3_b32 v116, v116, v194, v195\n"
      "v_mfma_f32_16x16x32_bf16 a[168:171], a[120:123], v[200:203], a[168:171]\n"
      "v_exp_f32 v114, v138\n"
      "v_mfma_f32_16x16x32_bf16 a[172:175], a[120:123], v[204:207], a[172:175]\n"
      "v_cvt_pk_bf16_f32 v196, v112, v113\n"
      "v_exp_f32 v115, v139\n"
      "v_mfma_f32_16x16x32_bf16 a[188:191], a[124:127], v[204:207], a[188:191]\n"
      "v_exp_f32 v112, v140\n"
      "v_mfma_f32_16x16x32_bf16 a[184:187], a[124:127], v[200:203], a[184:187]\n"
      "s_waitcnt lgkmcnt(0)\n"
      "v_cvt_pk_bf16_f32 v197, v114, v115\n"
      "v_mfma_f32_16x16x32_bf16 v[160:163], a[64:67], a[0:3], v[224:227]\n"
      "s_sub_u32 s67, s68, s66\n"
      "v_exp_f32 v113, v141\n"
      "v_mfma_f32_16x16x32_bf16 v[168:171], a[64:67], a[4:7], v[228:231]\n"
      "s_cmp_gt_i32 s67, s70\n"
      "v_or3_b32 v116, v116, v196, v197\n"
      "v_exp_f32 v114, v142\n"
      "v_mfma_f32_16x16x32_bf16 v[176:179], a[64:67], a[8:11], v[232:235]\n"
      "s_cselect_b32 s69, s86, 0\n"
      "v_cvt_pk_bf16_f32 v198, v112, v113\n"
      "v_mfma_f32_16x16x32_bf16 v[184:187], a[64:67], a[12:15], v[236:239]\n"
      "s_add_u32 s66, s66, s69\n"
      "v_exp_f32 v115, v143\n"
      "v_mfma_f32_16x16x32_bf16 v[164:167], a[68:71], a[0:3], v[224:227]\n"
      "s_add_u32 s67, s67, s101\n"
      "v_exp_f32 v112, v144\n"
      "v_mfma_f32_16x16x32_bf16 v[172:175], a[68:71], a[4:7], v[228:231]\n"
      "s_min_i32 s70, s70, s67\n"
      "v_cvt_pk_bf16_f32 v199, v114, v115\n"
      "v_exp_f32 v113, v145\n"
      "v_mfma_f32_16x16x32_bf16 v[180:183], a[68:71], a[8:11], v[232:235]\n"
      "s_max_i32 s70, s70, 85\n"
      "v_or3_b32 v116, v116, v198, v199\n"
      "v_mfma_f32_16x16x32_bf16 v[188:191], a[68:71], a[12:15], v[236:239]\n"
      "s_waitcnt lgkmcnt(4)\n"
      "v_exp_f32 v114, v146\n"
      "v_mfma_f32_16x16x32_bf16 v[160:163], a[72:75], a[16:19], v[160:163]\n"
      "s_add_u32 s66, s66, s86\n"
      "v_cvt_pk_bf16_f32 v200, v112, v113\n"
      "v_exp_f32 v115, v147\n"
      "v_mfma_f32_16x16x32_bf16 v[168:171], a[72:75], a[20:23], v[168:171]\n"
      "s_and_b32 s76, s80, 31\n"
      "v_exp_f32 v112, v148\n"
      "v_mfma_f32_16x16x32_bf16 v[176:179], a[72:75], a[24:27], v[176:179]\n"
      "s_lshr_b64 s[80:81], s[80:81], 5\n"
      "v_cvt_pk_bf16_f32 v201, v114, v115\n"
      "v_mfma_f32_16x16x32_bf16 v[184:187], a[72:75], a[28:31], v[184:187]\n"
      "s_add_u32 s66, s66, s76\n"
      "v_exp_f32 v113, v149\n"
      "v_mfma_f32_16x16x32_bf16 v[164:167], a[76:79], a[16:19], v[164:167]\n"
      "s_sub_u32 s67, s68, s82\n"
      "v_or3_b32 v116, v116, v200, v201\n"
      "v_exp_f32 v114, v150\n"
      "v_mfma_f32_16x16x32_bf16 v[172:175], a[76:79], a[20:23], v[172:175]\n"
      "s_min_i32 s76, s67, s83\n"
      "v_cvt_pk_bf16_f32 v202, v112, v113\n"
      "v_mfma_f32_16x16x32_bf16 v[180:183], a[76:79], a[24:27], v[180:183]\n"
      "s_add_u32 m0, s62, 0\n"
      "v_exp_f32 v115, v151\n"
      "v_mfma_f32_16x16x32_bf16 v[188:191], a[76:79], a[28:31], v[188:191]\n"
      "buffer_load_dwordx4 v248, s[20:23], s60 offen nt lds\n"
      "s_waitcnt lgkmcnt(2)\n"
      "v_mfma_f32_16x16x32_bf16 v[160:163], a[80:83], a[32:35], v[160:163]\n"
      "s_add_u32 m0, s62, 896\n"
      "v_exp_f32 v112, v152\n"
      "v_mfma_f32_16x16x32_bf16 v[168:171], a[80:83], a[36:39], v[168:171]\n"
      "s_mov_b32 s83, s67\n"
      "buffer_load_dwordx4 v248, s[20:23], s60 offen offset:128 nt lds\n"
      "v_mfma_f32_16x16x32_bf16 v[176:179], a[80:83], a[40:43], v[176:179]\n"
      "s_add_u32 m0, s62, 8192\n"
      "v_mfma_f32_16x16x32_bf16 v[184:187], a[80:83], a[44:47], v[184:187]\n"
      "s_sub_u32 s76, s76, 36\n"
      "buffer_load_dwordx4 v120, s[20:23], s60 offen nt lds\n"
      "s_waitcnt vmcnt(9)\n"
      "s_barrier\n"
      "v_mfma_f32_16x16x32_bf16 v[164:167], a[84:87], a[32:35], v[164:167]\n"
      "s_add_u32 m0, s62, 9088\n"
      "ds_read_b64_tr_b16 a[96:97], v246 offset:33792\n"
      "v_mfma_f32_16x16x32_bf16 v[172:175], a[84:87], a[36:39], v[172:175]\n"
      "s_min_i32 s76, s76, 148\n"
      "ds_read_b64_tr_b16 a[98:99], v246 offset:37888\n"
      "buffer_load_dwordx4 v120, s[20:23], s60 offen offset:128 nt lds\n"
      "v_mfma_f32_16x16x32_bf16 v[180:183], a[84:87], a[40:43], v[180:183]\n"
      "s_max_i32 s76, s76, 70\n"
      "ds_read_b64_tr_b16 a[100:101], v247 offset:33792\n"
      "v_mfma_f32_16x16x32_bf16 v[188:191], a[84:87], a[44:47], v[188:191]\n"
      "ds_read_b64_tr_b16 a[102:103], v247 offset:37888\n"
      "s_waitcnt lgkmcnt(4)\n"
      "v_mfma_f32_16x16x32_bf16 v[160:163], a[88:91], a[48:51], v[160:163]\n"
      "s_mov_b32 s82, s68\n"
      "ds_read_b64_tr_b16 a[104:105], v244 offset:33792\n"
      "v_cvt_pk_bf16_f32 v203, v114, v115\n"
      "v_mfma_f32_16x16x32_bf16 v[168:171], a[88:91], a[52:55], v[168:171]\n"
      "s_sub_u32 s66, s66, s99\n"
      "ds_read_b64_tr_b16 a[106:107], v244 offset:37888\n"
      "v_exp_f32 v113, v153\n"
      "v_mfma_f32_16x16x32_bf16 v[176:179], a[88:91], a[56:59], v[176:179]\n"
      "s_sub_u32 s82, s82, s99\n"
      "ds_read_b64_tr_b16 a[108:109], v245 offset:33792\n"
      "v_mfma_f32_16x16x32_bf16 v[184:187], a[88:91], a[60:63], v[184:187]\n"
      "s_add_u32 s65, s82, s76\n"
      "ds_read_b64_tr_b16 a[110:111], v245 offset:37888\n"
      "v_or3_b32 v116, v116, v202, v203\n"
      "v_mfma_f32_16x16x32_bf16 v[164:167], a[92:95], a[48:51], v[164:167]\n"
      "s_sub_u32 s93, s93, 1\n"
      "v_exp_f32 v114, v154\n"
      "v_mfma_f32_16x16x32_bf16 v[172:175], a[92:95], a[52:55], v[172:175]\n"
      "s_cmp_le_i32 s93, 14\n"
      "v_cvt_pk_bf16_f32 v204, v112, v113\n"
      "v_exp_f32 v115, v155\n"
      "v_mfma_f32_16x16x32_bf16 v[180:183], a[92:95], a[56:59], v[180:183]\n"
      "s_cselect_b32 s86, 107, s86\n"
      "v_exp_f32 v112, v156\n"
      "v_mfma_f32_16x16x32_bf16 v[188:191], a[92:95], a[60:63], v[188:191]\n"
      "s_waitcnt lgkmcnt(4)\n"
      "v_cvt_pk_bf16_f32 v205, v114, v115\n"
      "v_mfma_f32_16x16x32_bf16 a[160:163], a[96:99], v[208:211], a[160:163]\n"
      "s_sub_u32 s67, s65, s66\n"
      "v_exp_f32 v113, v157\n"
      "v_mfma_f32_16x16x32_bf16 a[164:167], a[96:99], v[212:215], a[164:167]\n"
      "s_cmp_lt_i32 s67, 0\n"
      "v_or3_b32 v116, v116, v204, v205\n"
      "v_exp_f32 v114, v158\n"
      "v_mfma_f32_16x16x32_bf16 a[168:171], a[96:99], v[216:219], a[168:171]\n"
      "s_cselect_b32 s88, s66, s65\n"
      "v_cvt_pk_bf16_f32 v206, v112, v113\n"
      "v_mfma_f32_16x16x32_bf16 a[172:175], a[96:99], v[220:223], a[172:175]\n"
      "s_add_u32 s90, s88, s91\n"
      "v_exp_f32 v115, v159\n"
      "s_nop 0\n"
      "v_mfma_f32_16x16x32_bf16 a[176:179], a[100:103], v[208:211], a[176:179]\n"
      "s_mov_b32 s64, 4\n"
      "v_cvt_pk_bf16_f32 v207, v114, v115\n"
      "v_or3_b32 v116, v116, v206, v207\n"
      "v_mfma_f32_16x16x32_bf16 a[180:183], a[100:103], v[212:215], a[180:183]\n"
      "v_and_b32 v116, 0x40004000, v116\n"
      "v_cmp_ne_u32 vcc, 0, v116\n"
      "s_cbranch_vccnz FB0_%=\n"
      "RET0_4_%=:\n"
      "v_mfma_f32_16x16x32_bf16 a[184:187], a[100:103], v[216:219], a[184:187]\n"
      "v_exp_f32 v112, v160\n"
      "v_mfma_f32_16x16x32_bf16 a[188:191], a[100:103], v[220:223], a[188:191]\n"
      "s_memrealtime s[68:69]\n"
      "v_xor_b32 v242, 0x10000, v242\n"
      "v_mfma_f32_16x16x32_bf16 a[192:195], a[196:199], v[192:195], a[192:195]\n"
      "v_exp_f32 v113, v161\n"
      "v_mfma_f32_16x16x32_bf16 a[192:195], a[200:203], v[196:199], a[192:195]\n"
      "v_xor_b32 v243, 0x10000, v243\n"
      "v_exp_f32 v114, v162\n"
      "v_mfma_f32_16x16x32_bf16 a[192:195], a[204:207], v[200:203], a[192:195]\n"
      "v_cvt_pk_bf16_f32 v0, v112, v113\n"
      "v_mfma_f32_16x16x32_bf16 a[192:195], a[208:211], v[204:207], a[192:195]\n"
      "v_exp_f32 v115, v163\n"
      "s_waitcnt vmcnt(6) lgkmcnt(0)\n"
      "s_sub_u32 s67, s68, s90\n"
      "s_cmp_lt_i32 s67, 0\n"
      "s_cbranch_scc1 GW8_%=\n"
      "GX8_%=:\n"
      "s_barrier\n"
      "v_mfma_f32_16x16x32_bf16 v[64:67], a[104:107], v[208:211], v[64:67]\n"
      "ds_read_b128 a[64:67], v242 offset:0\n"
      "ds_read_b128 a[68:71], v242 offset:4096\n"
      "v_mfma_f32_16x16x32_bf16 v[68:71], a[104:107], v[212:215], v[68:71]\n"
      "s_add_u32 m0, %[wb], 115712\n"
      "ds_read_b128 a[72:75], v243 offset:0\n"
      "ds_read_b128 a[76:79], v243 offset:4096\n"
      "v_mfma_f32_16x16x32_bf16 v[72:75], a[104:107], v[216:219], v[72:75]\n"
      "ds_read_b128 a[80:83], v242 offset:1024\n"
      "ds_read_b128 a[84:87], v242 offset:5120\n"
      "buffer_load_dwordx4 v121, s[24:27], s95 offen nt lds\n"
      "v_mfma_f32_16x16x32_bf16 v[76:79], a[104:107], v[220:223], v[76:79]\n"
      "s_add_u32 m0, %[wb], 123904\n"
      "ds_read_b128 a[88:91], v243 offset:1024\n"
      "ds_read_b128 a[92:95], v243 offset:5120\n"
      "v_mfma_f32_16x16x32_bf16 v[80:83], a[108:111], v[208:211], v[80:83]\n"
      "buffer_load_dwordx4 v123, s[24:27], s95 offen nt lds\n"
      "v_mfma_f32_16x16x32_bf16 v[84:87], a[108:111], v[212:215], v[84:87]\n"
      "s_add_u32 m0, %[wb], 17408\n"
      "v_mfma_f32_16x16x32_bf16 v[88:91], a[108:111], v[216:219], v[88:91]\n"
      "buffer_load_dwordx4 v121, s[24:27], s77 offen nt lds\n"
      "v_mfma_f32_16x16x32_bf16 v[92:95], a[108:111], v[220:223], v[92:95]\n"
      "s_waitcnt lgkmcnt(6)\n"
      "s_add_u32 m0, %[wb], 25600\n"
      "v_exp_f32 v112, v164\n"
      "v_mfma_f32_16x16x32_bf16 v[128:131], a[64:67], a[0:3], v[224:227]\n"
      "buffer_load_dwordx4 v123, s[24:27], s77 offen nt lds\n"
      "v_mfma_f32_16x16x32_bf16 v[136:139], a[64:67], a[4:7], v[228:231]\n"
      "v_cvt_pk_bf16_f32 v1, v114, v115\n"
      "v_exp_f32 v113, v165\n"
      "v_mfma_f32_16x16x32_bf16 v[144:147], a[64:67], a[8:11], v[232:235]\n"
      "v_or_b32 v116, v0, v1\n"
      "v_mfma_f32_16x16x32_bf16 v[152:155], a[64:67], a[12:15], v[236:239]\n"
      "v_exp_f32 v114, v166\n"
      "v_cvt_pk_bf16_f32 v2, v112, v113\n"
      "v_mfma_f32_16x16x32_bf16 v[132:135], a[68:71], a[0:3], v[224:227]\n"
      "v_exp_f32 v115, v167\n"
      "v_mfma_f32_16x16x32_bf16 v[140:143], a[68:71], a[4:7], v[228:231]\n"
      "v_exp_f32 v112, v168\n"
      "v_cvt_pk_bf16_f32 v3, v114, v115\n"
      "v_mfma_f32_16x16x32_bf16 v[148:151], a[68:71], a[8:11], v[232:235]\n"
      "v_exp_f32 v113, v169\n"
      "v_or3_b32 v116, v116, v2, v3\n"
      "v_mfma_f32_16x16x32_bf16 v[156:159], a[68:71], a[12:15], v[236:239]\n"
      "s_waitcnt lgkmcnt(4)\n"
      "v_exp_f32 v114, v170\n"
      "v_mfma_f32_16x16x32_bf16 v[128:131], a[72:75], a[16:19], v[128:131]\n"
      "v_cvt_pk_bf16_f32 v4, v112, v113\n"
      "v_exp_f32 v115, v171\n"
      "v_mfma_f32_16x16x32_bf16 v[136:139], a[72:75], a[20:23], v[136:139]\n"
      "v_exp_f32 v112, v172\n"
      "v_mfma_f32_16x16x32_bf16 v[144:147], a[72:75], a[24:27], v[144:147]\n"
      "v_cvt_pk_bf16_f32 v5, v114, v115\n"
      "v_exp_f32 v113, v173\n"
      "v_mfma_f32_16x16x32_bf16 v[152:155], a[72:75], a[28:31], v[152:155]\n"
      "v_or3_b32 v116, v116, v4, v5\n"
      "v_mfma_f32_16x16x32_bf16 v[132:135], a[76:79], a[16:19], v[132:135]\n"
      "v_exp_f32 v114, v174\n"
      "v_cvt_pk_bf16_f32 v6, v112, v113\n"
      "v_mfma_f32_16x16x32_bf16 v[140:143], a[76:79], a[20:23], v[140:143]\n"
      "v_exp_f32 v115, v175\n"
      "v_mfma_f32_16x16x32_bf16 v[148:151], a[76:79], a[24:27], v[148:151]\n"
      "v_exp_f32 v112, v176\n"
      "v_cvt_pk_bf16_f32 v7, v114, v115\n"
      "v_mfma_f32_16x16x32_bf16 v[156:159], a[76:79], a[28:31], v[156:159]\n"
      "s_waitcnt lgkmcnt(2)\n"
      "v_exp_f32 v113, v177\n"
      "v_mfma_f32_16x16x32_bf16 v[128:131], a[80:83], a[32:35], v[128:131]\n"
      "v_or3_b32 v116, v116, v6, v7\n"
      "v_exp_f32 v114, v178\n"
      "v_mfma_f32_16x16x32_bf16 v[136:139], a[80:83], a[36:39], v[136:139]\n"
      "v_cvt_pk_bf16_f32 v8, v112, v113\n"
      "v_exp_f32 v115, v179\n"
      "v_mfma_f32_16x16x32_bf16 v[144:147], a[80:83], a[40:43], v[144:147]\n"
      "v_exp_f32 v112, v180\n"
      "v_mfma_f32_16x16x32_bf16 v[152:155], a[80:83], a[44:47], v[152:155]\n"
      "v_cvt_pk_bf16_f32 v9, v114, v115\n"
      "v_exp_f32 v113, v181\n"
      "s_waitcnt vmcnt(8)\n"
      "s_barrier\n"
      "v_mfma_f32_16x16x32_bf16 v[132:135], a[84:87], a[32:35], v[132:135]\n"
      "ds_read_b64_tr_b16 a[112:113], v250 offset:1024\n"
      "v_or3_b32 v116, v116, v8, v9\n"
      "v_mfma_f32_16x16x32_bf16 v[140:143], a[84:87], a[36:39], v[140:143]\n"
      "ds_read_b64_tr_b16 a[114:115], v250 offset:5120\n"
      "v_exp_f32 v114, v182\n"
      "v_mfma_f32_16x16x32_bf16 v[148:151], a[84:87], a[40:43], v[148:151]\n"
      "ds_read_b64_tr_b16 a[116:117], v251 offset:1024\n"
      "v_mfma_f32_16x16x32_bf16 v[156:159], a[84:87], a[44:47], v[156:159]\n"
      "ds_read_b64_tr_b16 a[118:119], v251 offset:5120\n"
      "v_cvt_pk_bf16_f32 v10, v112, v113\n"
      "s_waitcnt lgkmcnt(4)\n"
      "v_mfma_f32_16x16x32_bf16 v[128:131], a[88:91], a[48:51], v[128:131]\n"
      "ds_read_b64_tr_b16 a[120:121], v240 offset:1024\n"
      "v_exp_f32 v115, v183\n"
      "v_mfma_f32_16x16x32_bf16 v[136:139], a[88:91], a[52:55], v[136:139]\n"
      "ds_read_b64_tr_b16 a[122:123], v240 offset:5120\n"
      "v_exp_f32 v112, v184\n"
      "v_mfma_f32_16x16x32_bf16 v[144:147], a[88:91], a[56:59], v[144:147]\n"
      "ds_read_b64_tr_b16 a[124:125], v241 offset:1024\n"
      "v_cvt_pk_bf16_f32 v11, v114, v115\n"
      "v_mfma_f32_16x16x32_bf16 v[152:155], a[88:91], a[60:63], v[152:155]\n"
      "ds_read_b64_tr_b16 a[126:127], v241 offset:5120\n"
      "v_exp_f32 v113, v185\n"
      "v_mfma_f32_16x16x32_bf16 v[132:135], a[92:95], a[48:51], v[132:135]\n"
      "v_or3_b32 v116, v116, v10, v11\n"
      "v_exp_f32 v114, v186\n"
      "v_mfma_f32_16x16x32_bf16 v[140:143], a[92:95], a[52:55], v[140:143]\n"
      "v_cvt_pk_bf16_f32 v12, v112, v113\n"
      "v_mfma_f32_16x16x32_bf16 v[148:151], a[92:95], a[56:59], v[148:151]\n"
      "v_exp_f32 v115, v187\n"
      "v_mfma_f32_16x16x32_bf16 v[156:159], a[92:95], a[60:63], v[156:159]\n"
      "s_waitcnt lgkmcnt(4)\n"
      "v_exp_f32 v112, v188\n"
      "v_cvt_pk_bf16_f32 v13, v114, v115\n"
      "v_mfma_f32_16x16x32_bf16 a[160:163], a[112:115], v[192:195], a[160:163]\n"
      "v_exp_f32 v113, v189\n"
      "v_or3_b32 v116, v116, v12, v13\n"
      "v_mfma_f32_16x16x32_bf16 a[164:167], a[112:115], v[196:199], a[164:167]\n"
      "v_exp_f32 v114, v190\n"
      "v_mfma_f32_16x16x32_bf16 a[168:171], a[112:115], v[200:203], a[168:171]\n"
      "v_cvt_pk_bf16_f32 v14, v112, v113\n"
      "v_exp_f32 v115, v191\n"
      "v_mfma_f32_16x16x32_bf16 a[172:175], a[112:115], v[204:207], a[172:175]\n"
      "s_add_u32 m0, %[wb], 50176\n"
      "s_nop 0\n"
      "v_cvt_pk_bf16_f32 v15, v114, v115\n"
      "v_or3_b32 v116, v116, v14, v15\n"
      "v_mfma_f32_16x16x32_bf16 a[176:179], a[116:119], v[192:195], a[176:179]\n"
      "s_mov_b32 s64, 5\n"
      "buffer_load_dwordx4 v121, s[24:27], s78 offen nt lds\n"
      "v_mfma_f32_16x16x32_bf16 a[180:183], a[116:119], v[196:199], a[180:183]\n"
      "s_add_u32 m0, %[wb], 58368\n"
      "v_and_b32 v116, 0x40004000, v116\n"
      "v_cmp_ne_u32 vcc, 0, v116\n"
      "s_cbranch_vccnz FB1_%=\n"
      "RET1_5_%=:\n"
      "v_mfma_f32_16x16x32_bf16 a[184:187], a[116:119], v[200:203], a[184:187]\n"
      "buffer_load_dwordx4 v123, s[24:27], s78 offen nt lds\n"
      "v_mfma_f32_16x16x32_bf16 a[188:191], a[116:119], v[204:207], a[188:191]\n"
      "s_memrealtime s[68:69]\n"
      "v_exp_f32 v112, v128\n"
      "v_exp_f32 v113, v129\n"
      "v_mfma_f32_16x16x32_bf16 a[192:195], a[196:199], v[0:3], a[192:195]\n"
      "v_exp_f32 v114, v130\n"
      "v_mfma_f32_16x16x32_bf16 a[192:195], a[200:203], v[4:7], a[192:195]\n"
      "v_cvt_pk_bf16_f32 v16, v112, v113\n"
      "v_exp_f32 v115, v131\n"
      "v_mfma_f32_16x16x32_bf16 a[192:195], a[204:207], v[8:11], a[192:195]\n"
      "v_exp_f32 v112, v132\n"
      "v_cvt_pk_bf16_f32 v17, v114, v115\n"
      "v_mfma_f32_16x16x32_bf16 a[192:195], a[208:211], v[12:15], a[192:195]\n"
      "v_exp_f32 v113, v133\n"
      "v_or_b32 v116, v16, v17\n"
      "s_add_u32 s72, s72, 0x7f4a7c15\n"
      "s_addc_u32 s76, s79, 0x9e3779b9\n"
      "s_waitcnt vmcnt(6) lgkmcnt(0)\n"
      "s_barrier\n"
      "v_mfma_f32_16x16x32_bf16 v[64:67], a[120:123], v[192:195], v[64:67]\n"
      "s_xor_b32 s80, %[xo], 0x400000\n"
      "ds_read_b128 a[64:67], v242 offset:32768\n"
      "ds_read_b128 a[68:71], v242 offset:36864\n"
      "v_mfma_f32_16x16x32_bf16 v[68:71], a[120:123], v[196:199], v[68:71]\n"
      "s_add_u32 s79, s80, 0x1000\n"
      "ds_read_b128 a[72:75], v243 offset:32768\n"
      "ds_read_b128 a[76:79], v243 offset:36864\n"
      "v_exp_f32 v114, v134\n"
      "v_mfma_f32_16x16x32_bf16 v[72:75], a[120:123], v[200:203], v[72:75]\n"
      "s_add_u32 s81, %[xo], 0x1000\n"
      "ds_read_b128 a[80:83], v242 offset:33792\n"
      "ds_read_b128 a[84:87], v242 offset:37888\n"
      "v_cvt_pk_bf16_f32 v18, v112, v113\n"
      "v_mfma_f32_16x16x32_bf16 v[76:79], a[120:123], v[204:207], v[76:79]\n"
      "s_mov_b32 s63, 0x100000\n"
      "ds_read_b128 a[88:91], v243 offset:33792\n"
      "ds_read_b128 a[92:95], v243 offset:37888\n"
      "v_mfma_f32_16x16x32_bf16 v[80:83], a[124:127], v[192:195], v[80:83]\n"
      "s_mov_b32 s60, 0x928b2f\n"
      "v_exp_f32 v115, v135\n"
      "v_exp_f32 v112, v136\n"
      "v_mfma_f32_16x16x32_bf16 v[84:87], a[124:127], v[196:199], v[84:87]\n"
      "s_mov_b32 s62, 0x9818e9\n"
      "v_cvt_pk_bf16_f32 v19, v114, v115\n"
      "v_exp_f32 v113, v137\n"
      "v_mfma_f32_16x16x32_bf16 v[88:91], a[124:127], v[200:203], v[88:91]\n"
      "s_mov_b32 s70, 0xdd9dc9\n"
      "v_xor_b32 v248, 0x2000, v244\n"
      "v_or3_b32 v116, v116, v18, v19\n"
      "v_mfma_f32_16x16x32_bf16 v[92:95], a[124:127], v[204:207], v[92:95]\n"
      "s_waitcnt lgkmcnt(6)\n"
      "v_exp_f32 v114, v138\n"
      "v_cvt_pk_bf16_f32 v20, v112, v113\n"
      "v_mfma_f32_16x16x32_bf16 v[160:163], a[64:67], a[0:3], v[224:227]\n"
      "s_mov_b32 s71, 0x8ed905\n"
      "v_exp_f32 v115, v139\n"
      "v_mfma_f32_16x16x32_bf16 v[168:171], a[64:67], a[4:7], v[228:231]\n"
      "s_sub_u32 s88, 364, %[cph]\n"
      "v_xor_b32 v120, 0x2000, v245\n"
      "v_exp_f32 v112, v140\n"
      "v_cvt_pk_bf16_f32 v21, v114, v115\n"
      "v_mfma_f32_16x16x32_bf16 v[176:179], a[64:67], a[8:11], v[232:235]\n"
      "s_add_u32 s88, s88, s66\n"
      "v_exp_f32 v113, v141\n"
      "v_mfma_f32_16x16x32_bf16 v[184:187], a[64:67], a[12:15], v[236:239]\n"
      "s_add_u32 m0, %[wb], 49152\n"
      "v_or3_b32 v116, v116, v20, v21\n"
      "v_exp_f32 v114, v142\n"
      "v_mfma_f32_16x16x32_bf16 v[164:167], a[68:71], a[0:3], v[224:227]\n"
      "s_add_u32 s88, s88, s100\n"
      "buffer_load_dwordx4 v249, s[24:27], s61 offen nt lds\n"
      "v_mbcnt_lo_u32_b32 v127, -1, 0\n"
      "v_mfma_f32_16x16x32_bf16 v[172:175], a[68:71], a[4:7], v[228:231]\n"
      "s_add_u32 m0, %[wb], 57344\n"
      "v_cvt_pk_bf16_f32 v22, v112, v113\n"
      "v_mfma_f32_16x16x32_bf16 v[180:183], a[68:71], a[8:11], v[232:235]\n"
      "buffer_load_dwordx4 v122, s[24:27], s61 offen nt lds\n"
      "v_mfma_f32_16x16x32_bf16 v[188:191], a[68:71], a[12:15], v[236:239]\n"
      "s_waitcnt lgkmcnt(4)\n"
      "v_mbcnt_hi_u32_b32 v127, -1, v127\n"
      "v_exp_f32 v115, v143\n"
      "v_mfma_f32_16x16x32_bf16 v[160:163], a[72:75], a[16:19], v[160:163]\n"
      "v_exp_f32 v112, v144\n"
      "v_cvt_pk_bf16_f32 v23, v114, v115\n"
      "v_mfma_f32_16x16x32_bf16 v[168:171], a[72:75], a[20:23], v[168:171]\n"
      "v_exp_f32 v113, v145\n"
      "v_or3_b32 v116, v116, v22, v23\n"
      "v_mfma_f32_16x16x32_bf16 v[176:179], a[72:75], a[24:27], v[176:179]\n"
      "v_lshlrev_b32 v127, 4, v127\n"
      "v_exp_f32 v114, v146\n"
      "v_mfma_f32_16x16x32_bf16 v[184:187], a[72:75], a[28:31], v[184:187]\n"
      "v_cvt_pk_bf16_f32 v24, v112, v113\n"
      "v_exp_f32 v115, v147\n"
      "v_mfma_f32_16x16x32_bf16 v[164:167], a[76:79], a[16:19], v[164:167]\n"
      "v_exp_f32 v112, v148\n"
      "v_cvt_pk_bf16_f32 v25, v114, v115\n"
      "v_mfma_f32_16x16x32_bf16 v[172:175], a[76:79], a[20:23], v[172:175]\n"
      "v_add_u32 v125, %[xw], v127\n"
      "v_exp_f32 v113, v149\n"
      "v_mfma_f32_16x16x32_bf16 v[180:183], a[76:79], a[24:27], v[180:183]\n"
      "v_or3_b32 v116, v116, v24, v25\n"
      "v_exp_f32 v114, v150\n"
      "v_mfma_f32_16x16x32_bf16 v[188:191], a[76:79], a[28:31], v[188:191]\n"
      "s_waitcnt lgkmcnt(2)\n"
      "v_cvt_pk_bf16_f32 v26, v112, v113\n"
      "v_exp_f32 v115, v151\n"
      "v_mfma_f32_16x16x32_bf16 v[160:163], a[80:83], a[32:35], v[160:163]\n"
      "v_add_u32 v126, %[xr], v127\n"
      "v_exp_f32 v112, v152\n"
      "v_mfma_f32_16x16x32_bf16 v[168:171], a[80:83], a[36:39], v[168:171]\n"
      "s_add_u32 m0, %[wb], 81920\n"
      "v_cvt_pk_bf16_f32 v27, v114, v115\n"
      "v_exp_f32 v113, v153\n"
      "v_mfma_f32_16x16x32_bf16 v[176:179], a[80:83], a[40:43], v[176:179]\n"
      "buffer_load_dwordx4 v249, s[24:27], s94 offen nt lds\n"
      "v_mfma_f32_16x16x32_bf16 v[184:187], a[80:83], a[44:47], v[184:187]\n"
      "s_add_u32 m0, %[wb], 90112\n"
      "v_or3_b32 v116, v116, v26, v27\n"
      "v_mfma_f32_16x16x32_bf16 v[164:167], a[84:87], a[32:35], v[164:167]\n"
      "buffer_load_dwordx4 v122, s[24:27], s94 offen nt lds\n"
      "v_mfma_f32_16x16x32_bf16 v[172:175], a[84:87], a[36:39], v[172:175]\n"
      "v_exp_f32 v114, v154\n"
      "v_cvt_pk_bf16_f32 v28, v112, v113\n"
      "v_mfma_f32_16x16x32_bf16 v[180:183], a[84:87], a[40:43], v[180:183]\n"
      "v_exp_f32 v115, v155\n"
      "v_exp_f32 v112, v156\n"
      "v_mov_b32 v123, s88\n"
      "v_mfma_f32_16x16x32_bf16 v[188:191], a[84:87], a[44:47], v[188:191]\n"
      "s_waitcnt lgkmcnt(0)\n"
      "v_cvt_pk_bf16_f32 v29, v114, v115\n"
      "v_exp_f32 v113, v157\n"
      "s_waitcnt vmcnt(6)\n"
      "s_barrier\n"
      "v_mfma_f32_16x16x32_bf16 v[160:163], a[88:91], a[48:51], v[160:163]\n"
      "ds_read_b64_tr_b16 a[96:97], v250 offset:33792\n"
      "ds_read_b64_tr_b16 a[98:99], v250 offset:37888\n"
      "v_mfma_f32_16x16x32_bf16 v[168:171], a[88:91], a[52:55], v[168:171]\n"
      "ds_read_b64_tr_b16 a[100:101], v251 offset:33792\n"
      "ds_read_b64_tr_b16 a[102:103], v251 offset:37888\n"
      "v_or3_b32 v116, v116, v28, v29\n"
      "v_mfma_f32_16x16x32_bf16 v[176:179], a[88:91], a[56:59], v[176:179]\n"
      "ds_read_b64_tr_b16 a[112:113], v246 offset:1024\n"
      "ds_read_b64_tr_b16 a[114:115], v246 offset:5120\n"
      "v_mfma_f32_16x16x32_bf16 v[184:187], a[88:91], a[60:63], v[184:187]\n"
      "ds_read_b64_tr_b16 a[116:117], v247 offset:1024\n"
      "ds_read_b64_tr_b16 a[118:119], v247 offset:5120\n"
      "v_exp_f32 v114, v158\n"
      "v_mfma_f32_16x16x32_bf16 v[164:167], a[92:95], a[48:51], v[164:167]\n"
      "ds_read_b64_tr_b16 a[104:105], v240 offset:33792\n"
      "v_cvt_pk_bf16_f32 v30, v112, v113\n"
      "v_mfma_f32_16x16x32_bf16 v[172:175], a[92:95], a[52:55], v[172:175]\n"
      "s_mov_b32 s64, 5\n"
      "ds_read_b64_tr_b16 a[106:107], v240 offset:37888\n"
      "v_exp_f32 v115, v159\n"
      "s_nop 0\n"
      "v_mfma_f32_16x16x32_bf16 v[180:183], a[92:95], a[56:59], v[180:183]\n"
      "ds_read_b64_tr_b16 a[108:109], v241 offset:33792\n"
      "v_cvt_pk_bf16_f32 v31, v114, v115\n"
      "v_or3_b32 v116, v116, v30, v31\n"
      "v_mfma_f32_16x16x32_bf16 v[188:191], a[92:95], a[60:63], v[188:191]\n"
      "ds_read_b64_tr_b16 a[110:111], v241 offset:37888\n"
      "v_and_b32 v116, 0x40004000, v116\n"
      "v_cmp_ne_u32 vcc, 0, v116\n"
      "s_waitcnt lgkmcnt(4)\n"
      "s_cbranch_vccnz FB0_%=\n"
      "RET0_5_%=:\n"
      "v_mfma_f32_16x16x32_bf16 a[160:163], a[96:99], v[0:3], a[160:163]\n"
      "ds_read_b64_tr_b16 a[120:121], v244 offset:1024\n"
      "v_mov_b32 v117, v224\n"
      "v_xor_b32 v242, 0x10000, v242\n"
      "v_xor_b32 v243, 0x10000, v243\n"
      "v_mfma_f32_16x16x32_bf16 a[176:179], a[100:103], v[0:3], a[176:179]\n"
      "ds_read_b64_tr_b16 a[122:123], v244 offset:5120\n"
      "v_exp_f32 v112, v160\n"
      "v_mfma_f32_16x16x32_bf16 a[160:163], a[112:115], v[16:19], a[160:163]\n"
      "s_mov_b32 s64, 6\n"
      "ds_read_b64_tr_b16 a[124:125], v245 offset:1024\n"
      "v_exp_f32 v113, v161\n"
      "v_exp_f32 v114, v162\n"
      "v_mfma_f32_16x16x32_bf16 a[176:179], a[116:119], v[16:19], a[176:179]\n"
      "s_add_u32 m0, %[wb], 114688\n"
      "ds_read_b64_tr_b16 a[126:127], v245 offset:5120\n"
      "v_cvt_pk_bf16_f32 v96, v112, v113\n"
      "v_exp_f32 v115, v163\n"
      "v_mfma_f32_16x16x32_bf16 a[164:167], a[96:99], v[4:7], a[164:167]\n"
      "buffer_load_dwordx4 v249, s[24:27], s95 offen nt lds\n"
      "v_mov_b32_dpp v117, v228 quad_perm:[0,1,2,3] row_mask:0x2 bank_mask:0xf\n"
      "v_mfma_f32_16x16x32_bf16 a[180:183], a[100:103], v[4:7], a[180:183]\n"
      "s_add_u32 m0, %[wb], 122880\n"
      "v_exp_f32 v112, v164\n"
      "v_cvt_pk_bf16_f32 v97, v114, v115\n"
      "v_mfma_f32_16x16x32_bf16 a[164:167], a[112:115], v[20:23], a[164:167]\n"
      "buffer_load_dwordx4 v122, s[24:27], s95 offen nt lds\n"
      "v_mfma_f32_16x16x32_bf16 a[180:183], a[116:119], v[20:23], a[180:183]\n"
      "ds_write_b128 v125, a[160:163] offset:0\n"
      "v_exp_f32 v113, v165\n"
      "v_or_b32 v116, v96, v97\n"
      "v_mfma_f32_16x16x32_bf16 a[168:171], a[96:99], v[8:11], a[168:171]\n"
      "v_mov_b32_dpp v117, v232 quad_perm:[0,1,2,3] row_mask:0x4 bank_mask:0xf\n"
      "v_exp_f32 v114, v166\n"
      "v_cvt_pk_bf16_f32 v98, v112, v113\n"
      "v_mfma_f32_16x16x32_bf16 a[184:187], a[100:103], v[8:11], a[184:187]\n"
      "ds_write_b128 v125, a[176:179] offset:4096\n"
      "v_exp_f32 v115, v167\n"
      "v_mfma_f32_16x16x32_bf16 a[168:171], a[112:115], v[24:27], a[168:171]\n"
      "v_exp_f32 v112, v168\n"
      "v_cvt_pk_bf16_f32 v99, v114, v115\n"
      "v_mfma_f32_16x16x32_bf16 a[184:187], a[116:119], v[24:27], a[184:187]\n"
      "v_exp_f32 v113, v169\n"
      "v_or3_b32 v116, v116, v98, v99\n"
      "v_mfma_f32_16x16x32_bf16 a[172:175], a[96:99], v[12:15], a[172:175]\n"
      "ds_write_b128 v125, v[96:99] offset:8192\n"
      "v_mov_b32_dpp v117, v236 quad_perm:[0,1,2,3] row_mask:0x8 bank_mask:0xf\n"
      "v_exp_f32 v114, v170\n"
      "v_mfma_f32_16x16x32_bf16 a[188:191], a[100:103], v[12:15], a[188:191]\n"
      "v_cvt_pk_bf16_f32 v100, v112, v113\n"
      "v_exp_f32 v115, v171\n"
      "v_mfma_f32_16x16x32_bf16 a[172:175], a[112:115], v[28:31], a[172:175]\n"
      "v_exp_f32 v112, v172\n"
      "v_cvt_pk_bf16_f32 v101, v114, v115\n"
      "v_exp_f32 v113, v173\n"
      "v_mfma_f32_16x16x32_bf16 a[188:191], a[116:119], v[28:31], a[188:191]\n"
      "s_add_u32 m0, %[wb], 16384\n"
      "ds_write_b128 v125, a[164:167] offset:1024\n"
      "v_or3_b32 v116, v116, v100, v101\n"
      "v_mfma_f32_16x16x32_bf16 a[192:195], a[196:199], v[16:19], a[192:195]\n"
      "buffer_load_dwordx4 v249, s[24:27], s77 offen nt lds\n"
      "ds_write_b32 v125, v117 offset:12292\n"
      "v_mfma_f32_16x16x32_bf16 a[192:195], a[200:203], v[20:23], a[192:195]\n"
      "s_add_u32 m0, %[wb], 24576\n"
      "v_exp_f32 v114, v174\n"
      "v_cvt_pk_bf16_f32 v102, v112, v113\n"
      "v_mfma_f32_16x16x32_bf16 a[192:195], a[204:207], v[24:27], a[192:195]\n"
      "buffer_load_dwordx4 v122, s[24:27], s77 offen nt lds\n"
      "ds_write_b128 v125, a[180:183] offset:5120\n"
      "ds_write_b32 v125, v123 offset:12296\n"
      "v_mfma_f32_16x16x32_bf16 a[192:195], a[208:211], v[28:31], a[192:195]\n"
      "s_waitcnt lgkmcnt(7)\n"
      "v_exp_f32 v115, v175\n"
      "v_exp_f32 v112, v176\n"
      "v_mfma_f32_16x16x32_bf16 v[64:67], a[104:107], v[0:3], v[64:67]\n"
      "v_cvt_pk_bf16_f32 v103, v114, v115\n"
      "v_exp_f32 v113, v177\n"
      "v_mfma_f32_16x16x32_bf16 v[68:71], a[104:107], v[4:7], v[68:71]\n"
      "ds_write_b128 v125, v[100:103] offset:9216\n"
      "v_or3_b32 v116, v116, v102, v103\n"
      "v_exp_f32 v114, v178\n"
      "s_waitcnt vmcnt(8)\n"
      "s_barrier\n"
      "v_mfma_f32_16x16x32_bf16 v[72:75], a[104:107], v[8:11], v[72:75]\n"
      "ds_read_b64_tr_b16 a[212:213], v244 offset:33792\n"
      "v_cvt_pk_bf16_f32 v104, v112, v113\n"
      "v_mfma_f32_16x16x32_bf16 v[76:79], a[104:107], v[12:15], v[76:79]\n"
      "ds_write_b128 v125, a[168:171] offset:2048\n"
      "ds_read_b64_tr_b16 a[214:215], v244 offset:37888\n"
      "v_exp_f32 v115, v179\n"
      "v_mfma_f32_16x16x32_bf16 a[228:231], a[196:199], v[96:99], a[192:195]\n"
      "ds_read_b64_tr_b16 a[216:217], v245 offset:33792\n"
      "v_exp_f32 v112, v180\n"
      "v_cvt_pk_bf16_f32 v105, v114, v115\n"
      "v_mfma_f32_16x16x32_bf16 v[80:83], a[108:111], v[0:3], v[80:83]\n"
      "ds_read_b64_tr_b16 a[218:219], v245 offset:37888\n"
      "v_exp_f32 v113, v181\n"
      "v_mfma_f32_16x16x32_bf16 v[84:87], a[108:111], v[4:7], v[84:87]\n"
      "ds_write_b128 v125, a[184:187] offset:6144\n"
      "ds_read_b64_tr_b16 a[220:221], v248 offset:33792\n"
      "v_or3_b32 v116, v116, v104, v105\n"
      "v_mfma_f32_16x16x32_bf16 v[88:91], a[108:111], v[8:11], v[88:91]\n"
      "ds_read_b64_tr_b16 a[222:223], v248 offset:37888\n"
      "v_exp_f32 v114, v182\n"
      "v_cvt_pk_bf16_f32 v106, v112, v113\n"
      "v_mfma_f32_16x16x32_bf16 v[92:95], a[108:111], v[12:15], v[92:95]\n"
      "ds_read_b64_tr_b16 a[224:225], v120 offset:33792\n"
      "v_exp_f32 v115, v183\n"
      "v_mfma_f32_16x16x32_bf16 v[64:67], a[120:123], v[16:19], v[64:67]\n"
      "s_add_u32 m0, %[wb], 82944\n"
      "ds_write_b128 v125, a[172:175] offset:3072\n"
      "ds_read_b64_tr_b16 a[226:227], v120 offset:37888\n"
      "v_exp_f32 v112, v184\n"
      "v_mfma_f32_16x16x32_bf16 v[68:71], a[120:123], v[20:23], v[68:71]\n"
      "buffer_load_dwordx4 v249, s[24:27], s78 offen nt lds\n"
      "v_mfma_f32_16x16x32_bf16 v[72:75], a[120:123], v[24:27], v[72:75]\n"
      "s_add_u32 m0, %[wb], 91136\n"
      "v_cvt_pk_bf16_f32 v107, v114, v115\n"
      "v_exp_f32 v113, v185\n"
      "v_or3_b32 v116, v116, v106, v107\n"
      "v_mfma_f32_16x16x32_bf16 v[76:79], a[120:123], v[28:31], v[76:79]\n"
      "buffer_load_dwordx4 v122, s[24:27], s78 offen nt lds\n"
      "ds_write_b128 v125, v[104:107] offset:10240\n"
      "v_mfma_f32_16x16x32_bf16 a[228:231], a[200:203], v[100:103], a[228:231]\n"
      "v_exp_f32 v114, v186\n"
      "v_cvt_pk_bf16_f32 v108, v112, v113\n"
      "v_mfma_f32_16x16x32_bf16 v[80:83], a[124:127], v[16:19], v[80:83]\n"
      "v_exp_f32 v115, v187\n"
      "v_exp_f32 v112, v188\n"
      "v_mfma_f32_16x16x32_bf16 v[84:87], a[124:127], v[20:23], v[84:87]\n"
      "ds_write_b128 v125, a[188:191] offset:7168\n"
      "v_cvt_pk_bf16_f32 v109, v114, v115\n"
      "v_mfma_f32_16x16x32_bf16 v[88:91], a[124:127], v[24:27], v[88:91]\n"
      "v_exp_f32 v113, v189\n"
      "v_or3_b32 v116, v116, v108, v109\n"
      "v_exp_f32 v114, v190\n"
      "v_mfma_f32_16x16x32_bf16 v[92:95], a[124:127], v[28:31], v[92:95]\n"
      "v_cvt_pk_bf16_f32 v110, v112, v113\n"
      "v_exp_f32 v115, v191\n"
      "s_nop 0\n"
      "v_mfma_f32_16x16x32_bf16 a[228:231], a[204:207], v[104:107], a[228:231]\n"
      "v_cvt_pk_bf16_f32 v111, v114, v115\n"
      "v_or3_b32 v116, v116, v110, v111\n"
      "v_and_b32 v116, 0x40004000, v116\n"
      "v_cmp_ne_u32 vcc, 0, v116\n"
      "s_cbranch_vccnz FB1_%=\n"
      "RET1_6_%=:\n"
      "ds_write_b128 v125, v[108:111] offset:11264\n"
      "v_mfma_f32_16x16x32_bf16 a[228:231], a[208:211], v[108:111], a[228:231]\n"
      "s_waitcnt lgkmcnt(9)\n"
      "v_mfma_f32_16x16x32_bf16 v[64:67], a[212:215], v[96:99], v[64:67]\n"
      "v_mov_b32 v148, 1.0\n"
      "v_mfma_f32_16x16x32_bf16 v[80:83], a[216:219], v[96:99], v[80:83]\n"
      "v_mov_b32 v149, 1.0\n"
      "v_mfma_f32_16x16x32_bf16 v[68:71], a[212:215], v[100:103], v[68:71]\n"
      "v_mov_b32 v157, %[fo]\n"
      "v_mfma_f32_16x16x32_bf16 v[84:87], a[216:219], v[100:103], v[84:87]\n"
      "v_mov_b32 v150, s72\n"
      "v_mfma_f32_16x16x32_bf16 v[72:75], a[212:215], v[104:107], v[72:75]\n"
      "v_mov_b32 v151, s76\n"
      "ds_write_b32 v125, a228 offset:12288\n"
      "v_mfma_f32_16x16x32_bf16 v[88:91], a[216:219], v[104:107], v[88:91]\n"
      "v_xor_b32 v158, 8, v157\n"
      "v_mfma_f32_16x16x32_bf16 v[76:79], a[212:215], v[108:111], v[76:79]\n"
      "v_mfma_f32_16x16x32_bf16 v[92:95], a[216:219], v[108:111], v[92:95]\n"
      "s_waitcnt lgkmcnt(0)\n"
      "s_barrier\n"
      "s_memrealtime s[68:69]\n"
      "ds_read_b32 v237, v127 offset:12296\n"
      "ds_read_b64 v[152:153], v126 offset:12288\n"
      "ds_read_b128 v[160:163], v126 offset:0\n"
      "ds_read_b128 v[176:179], v126 offset:4096\n"
      "ds_read_b128 v[164:167], v126 offset:1024\n"
      "ds_read_b128 v[180:183], v126 offset:5120\n"
      "ds_read_b128 v[168:171], v126 offset:2048\n"
      "ds_read_b128 v[184:187], v126 offset:6144\n"
      "ds_read_b128 v[172:175], v126 offset:3072\n"
      "ds_read_b128 v[188:191], v126 offset:7168\n"
      "ds_read_b128 a[64:67], v126 offset:8192\n"
      "ds_read_b128 a[68:71], v126 offset:9216\n"
      "ds_read_b128 a[72:75], v126 offset:10240\n"
      "ds_read_b128 a[76:79], v126 offset:11264\n"
      "v_accvgpr_read_b32 v144, a228\n"
      "v_and_b32 v118, 0xf0, v127\n"
      "v_lshrrev_b32 v156, 3, v127\n"
      "v_and_b32 v156, 32, v156\n"
      "v_lshl_or_b32 v156, v118, 4, v156\n"
      "v_lshrrev_b32 v118, 5, v127\n"
      "v_and_or_b32 v156, v118, 16, v156\n"
      "v_add_u32 v156, %[oo], v156\n"
      "v_add_u32 v155, 0x2000, v156\n"
      "v_xor_b32 v242, 0x2000, v240\n"
      "v_xor_b32 v243, 0x2000, v241\n"
      "v_mov_b32 v236, v150\n"
      "s_waitcnt lgkmcnt(12)\n"
      "v_cmp_neq_f32 vcc, v153, v117\n"
      "v_readfirstlane_b32 s88, v237\n"
      "s_cbranch_vccnz MIS_%=\n"
      "MR_%=:\n"
      "v_mul_f32 v144, v144, v148\n"
      "v_mov_b32 v145, v117\n"
      "v_fmac_f32 v144, v152, v149\n"
      "s_waitcnt lgkmcnt(10)\n"
      "v_pk_add_f32 v[64:65], v[64:65], v[160:161]\n"
      "v_pk_add_f32 v[66:67], v[66:67], v[162:163]\n"
      "v_pk_add_f32 v[80:81], v[80:81], v[176:177]\n"
      "v_pk_add_f32 v[82:83], v[82:83], v[178:179]\n"
      "s_waitcnt lgkmcnt(8)\n"
      "v_pk_add_f32 v[68:69], v[68:69], v[164:165]\n"
      "v_pk_add_f32 v[70:71], v[70:71], v[166:167]\n"
      "v_pk_add_f32 v[84:85], v[84:85], v[180:181]\n"
      "v_pk_add_f32 v[86:87], v[86:87], v[182:183]\n"
      "s_waitcnt lgkmcnt(6)\n"
      "v_pk_add_f32 v[72:73], v[72:73], v[168:169]\n"
      "v_pk_add_f32 v[74:75], v[74:75], v[170:171]\n"
      "v_pk_add_f32 v[88:89], v[88:89], v[184:185]\n"
      "v_pk_add_f32 v[90:91], v[90:91], v[186:187]\n"
      "s_waitcnt lgkmcnt(4)\n"
      "v_pk_add_f32 v[76:77], v[76:77], v[172:173]\n"
      "v_pk_add_f32 v[78:79], v[78:79], v[174:175]\n"
      "v_pk_add_f32 v[92:93], v[92:93], v[188:189]\n"
      "v_pk_add_f32 v[94:95], v[94:95], v[190:191]\n"
      "s_waitcnt lgkmcnt(0)\n"
      "s_add_u32 s65, s68, 55\n"
      "s_mov_b32 s66, s88\n"
      "s_sub_u32 s67, s65, s66\n"
      "s_cmp_lt_i32 s67, 0\n"
      "s_cselect_b32 s88, s66, s65\n"
      "v_mov_b32 v237, s88\n"
      "s_mov_b32 s88, s66\n"
      "s_cmp_lg_u32 %[xw], 0\n"
      "s_cbranch_scc1 NH_%=\n"
      "s_mov_b64 exec, 1\n"
      "global_store_dwordx2 v157, v[236:237], s[18:19] sc1\n"
      "s_mov_b64 exec, -1\n"
      "NH_%=:\n"
      "v_mfma_f32_16x16x32_bf16 v[64:67], a[220:223], a[64:67], v[64:67]\n"
      "v_mfma_f32_16x16x32_bf16 v[80:83], a[224:227], a[64:67], v[80:83]\n"
      "v_mfma_f32_16x16x32_bf16 v[68:71], a[220:223], a[68:71], v[68:71]\n"
      "v_mfma_f32_16x16x32_bf16 v[84:87], a[224:227], a[68:71], v[84:87]\n"
      "v_mfma_f32_16x16x32_bf16 v[72:75], a[220:223], a[72:75], v[72:75]\n"
      "v_mfma_f32_16x16x32_bf16 v[88:91], a[224:227], a[72:75], v[88:91]\n"
      "v_mfma_f32_16x16x32_bf16 v[76:79], a[220:223], a[76:79], v[76:79]\n"
      "v_mfma_f32_16x16x32_bf16 v[92:95], a[224:227], a[76:79], v[92:95]\n"
      "s_nop 1\n"
      "s_memrealtime s[68:69]\n"
      "v_cvt_pk_bf16_f32 v128, v64, v65\n"
      "v_cvt_pk_bf16_f32 v129, v66, v67\n"
      "v_cvt_pk_bf16_f32 v130, v80, v81\n"
      "v_cvt_pk_bf16_f32 v131, v82, v83\n"
      "v_cvt_pk_bf16_f32 v132, v68, v69\n"
      "v_cvt_pk_bf16_f32 v133, v70, v71\n"
      "v_cvt_pk_bf16_f32 v134, v84, v85\n"
      "v_cvt_pk_bf16_f32 v135, v86, v87\n"
      "v_cvt_pk_bf16_f32 v136, v72, v73\n"
      "v_cvt_pk_bf16_f32 v137, v74, v75\n"
      "v_cvt_pk_bf16_f32 v138, v88, v89\n"
      "v_cvt_pk_bf16_f32 v139, v90, v91\n"
      "v_cvt_pk_bf16_f32 v140, v76, v77\n"
      "v_cvt_pk_bf16_f32 v141, v78, v79\n"
      "v_cvt_pk_bf16_f32 v142, v92, v93\n"
      "v_cvt_pk_bf16_f32 v143, v94, v95\n"
      "v_mad_u32_u24 v147, v144, s60, v151\n"
      "v_mad_u32_u24 v147, v145, s62, v147\n"
      "v_add3_u32 v146, v150, v144, v145\n"
      "v_mad_u32_u24 v147, v128, s60, v147\n"
      "v_mad_u32_u24 v147, v129, s62, v147\n"
      "v_add3_u32 v146, v146, v128, v129\n"
      "v_mad_u32_u24 v147, v130, s70, v147\n"
      "v_mad_u32_u24 v147, v131, s71, v147\n"
      "v_add3_u32 v146, v146, v130, v131\n"
      "v_mad_u32_u24 v147, v132, s60, v147\n"
      "v_mad_u32_u24 v147, v133, s62, v147\n"
      "v_add3_u32 v146, v146, v132, v133\n"
      "v_mad_u32_u24 v147, v134, s70, v147\n"
      "v_mad_u32_u24 v147, v135, s71, v147\n"
      "v_add3_u32 v146, v146, v134, v135\n"
      "v_mad_u32_u24 v147, v136, s60, v147\n"
      "v_mad_u32_u24 v147, v137, s62, v147\n"
      "v_add3_u32 v146, v146, v136, v137\n"
      "v_mad_u32_u24 v147, v138, s70, v147\n"
      "v_mad_u32_u24 v147, v139, s71, v147\n"
      "v_add3_u32 v146, v146, v138, v139\n"
      "v_mad_u32_u24 v147, v140, s60, v147\n"
      "v_mad_u32_u24 v147, v141, s62, v147\n"
      "v_add3_u32 v146, v146, v140, v141\n"
      "v_mad_u32_u24 v147, v142, s70, v147\n"
      "v_mad_u32_u24 v147, v143, s71, v147\n"
      "v_add3_u32 v146, v146, v142, v143\n"
      "s_waitcnt lgkmcnt(0)\n"
      "s_call_b64 s[84:85], SPIN_%=\n"
      "global_load_dwordx2 v[232:233], v158, s[18:19] sc1\n"
      "buffer_store_dwordx4 v[128:131], v127, s[28:31], %[xo] offen offset:0 sc1\n"
      "buffer_store_dwordx4 v[132:135], v127, s[28:31], %[xo] offen offset:1024 sc1\n"
      "buffer_store_dwordx4 v[136:139], v127, s[28:31], %[xo] offen offset:2048 sc1\n"
      "buffer_store_dwordx4 v[140:143], v127, s[28:31], %[xo] offen offset:3072 sc1\n"
      "buffer_store_dwordx4 v[144:147], v127, s[28:31], s81 offen sc1\n"
      "global_load_dwordx2 v[234:235], v158, s[18:19] sc1\n"
      "s_waitcnt vmcnt(7)\n"
      "s_barrier\n"
      "ds_read_b64_tr_b16 a[96:97], v246 offset:32768\n"
      "ds_read_b64_tr_b16 a[98:99], v246 offset:36864\n"
      "ds_read_b64_tr_b16 a[100:101], v247 offset:32768\n"
      "ds_read_b64_tr_b16 a[102:103], v247 offset:36864\n"
      "ds_read_b64_tr_b16 a[112:113], v250 offset:0\n"
      "ds_read_b64_tr_b16 a[114:115], v250 offset:4096\n"
      "ds_read_b64_tr_b16 a[116:117], v251 offset:0\n"
      "ds_read_b64_tr_b16 a[118:119], v251 offset:4096\n"
      "ds_read_b64_tr_b16 a[0:1], v250 offset:32768\n"
      "ds_read_b64_tr_b16 a[2:3], v250 offset:36864\n"
      "ds_read_b64_tr_b16 a[4:5], v251 offset:32768\n"
      "ds_read_b64_tr_b16 a[6:7], v251 offset:36864\n"
      "s_waitcnt lgkmcnt(8)\n"
      "v_mfma_f32_16x16x32_bf16 a[128:131], a[96:99], v[208:211], a[128:131]\n"
      "ds_read_b64_tr_b16 a[104:105], v244 offset:32768\n"
      "ds_read_b64_tr_b16 a[106:107], v244 offset:36864\n"
      "v_mfma_f32_16x16x32_bf16 a[144:147], a[100:103], v[208:211], a[144:147]\n"
      "ds_read_b64_tr_b16 a[108:109], v245 offset:32768\n"
      "ds_read_b64_tr_b16 a[110:111], v245 offset:36864\n"
      "s_waitcnt lgkmcnt(8)\n"
      "v_mfma_f32_16x16x32_bf16 a[128:131], a[112:115], v[192:195], a[128:131]\n"
      "ds_read_b64_tr_b16 a[120:121], v240 offset:0\n"
      "ds_read_b64_tr_b16 a[122:123], v240 offset:4096\n"
      "v_mfma_f32_16x16x32_bf16 a[144:147], a[116:119], v[192:195], a[144:147]\n"
      "ds_read_b64_tr_b16 a[124:125], v241 offset:0\n"
      "ds_read_b64_tr_b16 a[126:127], v241 offset:4096\n"
      "s_waitcnt lgkmcnt(8)\n"
      "v_mfma_f32_16x16x32_bf16 a[128:131], a[0:3], v[0:3], a[128:131]\n"
      "ds_read_b64_tr_b16 a[8:9], v240 offset:32768\n"
      "ds_read_b64_tr_b16 a[10:11], v240 offset:36864\n"
      "v_mfma_f32_16x16x32_bf16 a[144:147], a[4:7], v[0:3], a[144:147]\n"
      "ds_read_b64_tr_b16 a[12:13], v241 offset:32768\n"
      "ds_read_b64_tr_b16 a[14:15], v241 offset:36864\n"
      "v_mfma_f32_16x16x32_bf16 a[132:135], a[96:99], v[212:215], a[132:135]\n"
      "ds_write_b128 v125, v[16:19] offset:8192\n"
      "v_lshrrev_b32 v118, 2, v127\n"
      "v_mfma_f32_16x16x32_bf16 a[148:151], a[100:103], v[212:215], a[148:151]\n"
      "v_xor_b32 v118, v118, v127\n"
      "v_mfma_f32_16x16x32_bf16 a[132:135], a[112:115], v[196:199], a[132:135]\n"
      "v_and_b32 v118, 0xc0, v118\n"
      "v_mfma_f32_16x16x32_bf16 a[148:151], a[116:119], v[196:199], a[148:151]\n"
      "v_and_or_b32 v118, v127, 32, v118\n"
      "v_mfma_f32_16x16x32_bf16 a[132:135], a[0:3], v[4:7], a[132:135]\n"
      "ds_write_b128 v125, v[20:23] offset:9216\n"
      "v_and_b32 v119, 16, v127\n"
      "v_mfma_f32_16x16x32_bf16 a[148:151], a[4:7], v[4:7], a[148:151]\n"
      "v_mov_b32 v84, 0x3f80\n"
      "v_mfma_f32_16x16x32_bf16 a[136:139], a[96:99], v[216:219], a[136:139]\n"
      "v_lshlrev_b32 v119, v119, v84\n"
      "v_mfma_f32_16x16x32_bf16 a[152:155], a[100:103], v[216:219], a[152:155]\n"
      "v_cmp_eq_u32 vcc, 0, v118\n"
      "v_mfma_f32_16x16x32_bf16 a[136:139], a[112:115], v[200:203], a[136:139]\n"
      "ds_write_b128 v125, v[24:27] offset:10240\n"
      "v_mov_b32 v84, 0\n"
      "v_mfma_f32_16x16x32_bf16 a[152:155], a[116:119], v[200:203], a[152:155]\n"
      "v_cndmask_b32 v86, 0, v119, vcc\n"
      "v_mfma_f32_16x16x32_bf16 a[136:139], a[0:3], v[8:11], a[136:139]\n"
      "v_cmp_eq_u32 vcc, 32, v118\n"
      "v_mfma_f32_16x16x32_bf16 a[152:155], a[4:7], v[8:11], a[152:155]\n"
      "v_mov_b32 v85, 0\n"
      "v_mfma_f32_16x16x32_bf16 a[140:143], a[96:99], v[220:223], a[140:143]\n"
      "ds_write_b128 v125, v[28:31] offset:11264\n"
      "v_cndmask_b32 v87, 0, v119, vcc\n"
      "v_mfma_f32_16x16x32_bf16 a[156:159], a[100:103], v[220:223], a[156:159]\n"
      "v_mov_b32 v88, 0\n"
      "v_mfma_f32_16x16x32_bf16 a[140:143], a[112:115], v[204:207], a[140:143]\n"
      "v_mov_b32 v89, 0\n"
      "v_mfma_f32_16x16x32_bf16 a[156:159], a[116:119], v[204:207], a[156:159]\n"
      "v_mfma_f32_16x16x32_bf16 a[140:143], a[0:3], v[12:15], a[140:143]\n"
      "ds_write_b128 v125, a[128:131] offset:0\n"
      "v_mfma_f32_16x16x32_bf16 a[156:159], a[4:7], v[12:15], a[156:159]\n"
      "s_waitcnt lgkmcnt(13)\n"
      "v_mfma_f32_16x16x32_bf16 v[32:35], a[104:107], v[208:211], v[32:35]\n"
      "ds_read_b64_tr_b16 a[32:33], v240 offset:1024\n"
      "v_mfma_f32_16x16x32_bf16 v[36:39], a[104:107], v[212:215], v[36:39]\n"
      "ds_read_b64_tr_b16 a[34:35], v240 offset:5120\n"
      "v_mfma_f32_16x16x32_bf16 v[40:43], a[104:107], v[216:219], v[40:43]\n"
      "ds_write_b128 v125, a[144:147] offset:4096\n"
      "ds_read_b64_tr_b16 a[36:37], v241 offset:1024\n"
      "v_mfma_f32_16x16x32_bf16 v[44:47], a[104:107], v[220:223], v[44:47]\n"
      "ds_read_b64_tr_b16 a[38:39], v241 offset:5120\n"
      "v_mfma_f32_16x16x32_bf16 v[48:51], a[108:111], v[208:211], v[48:51]\n"
      "ds_read_b64_tr_b16 a[40:41], v242 offset:1024\n"
      "v_mfma_f32_16x16x32_bf16 v[52:55], a[108:111], v[212:215], v[52:55]\n"
      "ds_read_b64_tr_b16 a[42:43], v242 offset:5120\n"
      "v_mfma_f32_16x16x32_bf16 v[56:59], a[108:111], v[216:219], v[56:59]\n"
      "ds_write_b128 v125, a[132:135] offset:1024\n"
      "ds_read_b64_tr_b16 a[44:45], v243 offset:1024\n"
      "v_mfma_f32_16x16x32_bf16 v[60:63], a[108:111], v[220:223], v[60:63]\n"
      "ds_read_b64_tr_b16 a[46:47], v243 offset:5120\n"
      "s_waitcnt lgkmcnt(15)\n"
      "v_mfma_f32_16x16x32_bf16 v[32:35], a[120:123], v[192:195], v[32:35]\n"
      "ds_read_b64_tr_b16 a[16:17], v244 offset:0\n"
      "v_mfma_f32_16x16x32_bf16 v[36:39], a[120:123], v[196:199], v[36:39]\n"
      "ds_read_b64_tr_b16 a[18:19], v244 offset:4096\n"
      "v_mfma_f32_16x16x32_bf16 v[40:43], a[120:123], v[200:203], v[40:43]\n"
      "ds_write_b128 v125, a[148:151] offset:5120\n"
      "ds_read_b64_tr_b16 a[20:21], v245 offset:0\n"
      "v_mfma_f32_16x16x32_bf16 v[44:47], a[120:123], v[204:207], v[44:47]\n"
      "ds_read_b64_tr_b16 a[22:23], v245 offset:4096\n"
      "v_mfma_f32_16x16x32_bf16 v[48:51], a[124:127], v[192:195], v[48:51]\n"
      "ds_read_b64_tr_b16 a[24:25], v248 offset:0\n"
      "v_mfma_f32_16x16x32_bf16 v[52:55], a[124:127], v[196:199], v[52:55]\n"
      "ds_read_b64_tr_b16 a[26:27], v248 offset:4096\n"
      "v_mfma_f32_16x16x32_bf16 v[56:59], a[124:127], v[200:203], v[56:59]\n"
      "ds_write_b128 v125, a[136:139] offset:2048\n"
      "ds_read_b64_tr_b16 a[28:29], v120 offset:0\n"
      "v_mfma_f32_16x16x32_bf16 v[60:63], a[124:127], v[204:207], v[60:63]\n"
      "ds_read_b64_tr_b16 a[30:31], v120 offset:4096\n"
      "s_waitcnt lgkmcnt(15)\n"
      "v_mfma_f32_16x16x32_bf16 v[32:35], a[8:11], v[0:3], v[32:35]\n"
      "v_mfma_f32_16x16x32_bf16 v[36:39], a[8:11], v[4:7], v[36:39]\n"
      "v_mfma_f32_16x16x32_bf16 v[40:43], a[8:11], v[8:11], v[40:43]\n"
      "ds_write_b128 v125, a[152:155] offset:6144\n"
      "v_mfma_f32_16x16x32_bf16 v[44:47], a[8:11], v[12:15], v[44:47]\n"
      "s_waitcnt vmcnt(6)\n"
      "v_cmp_ne_u32 vcc, v232, v150\n"
      "s_memrealtime s[68:69]\n"
      "s_cbranch_vccnz POLL_%=\n"
      "GOT_%=:\n"
      "v_readfirstlane_b32 s88, v233\n"
      "s_waitcnt lgkmcnt(0)\n"
      "s_add_u32 s88, s88, 60\n"
      "s_sub_u32 s67, s88, s68\n"
      "s_cmp_gt_u32 s67, 1000\n"
      "s_cbranch_scc1 LD_%=\n"
      "s_call_b64 s[84:85], SPIN_%=\n"
      "LD_%=:\n"
      "buffer_load_dwordx4 v[80:83], v127, s[28:31], s79 offen sc1\n"
      "buffer_load_dwordx4 v[64:67], v127, s[28:31], s80 offen offset:0 sc1\n"
      "buffer_load_dwordx4 v[68:71], v127, s[28:31], s80 offen offset:1024 sc1\n"
      "buffer_load_dwordx4 v[72:75], v127, s[28:31], s80 offen offset:2048 sc1\n"
      "buffer_load_dwordx4 v[76:79], v127, s[28:31], s80 offen offset:3072 sc1\n"
      "v_mfma_f32_16x16x32_bf16 v[48:51], a[12:15], v[0:3], v[48:51]\n"
      "v_mfma_f32_16x16x32_bf16 v[52:55], a[12:15], v[4:7], v[52:55]\n"
      "v_mfma_f32_16x16x32_bf16 v[56:59], a[12:15], v[8:11], v[56:59]\n"
      "ds_write_b128 v125, a[140:143] offset:3072\n"
      "v_mfma_f32_16x16x32_bf16 v[60:63], a[12:15], v[12:15], v[60:63]\n"
      "s_waitcnt lgkmcnt(15)\n"
      "v_mfma_f32_16x16x32_bf16 v[32:35], a[32:35], v[96:99], v[32:35]\n"
      "v_mfma_f32_16x16x32_bf16 v[48:51], a[36:39], v[96:99], v[48:51]\n"
      "v_mfma_f32_16x16x32_bf16 v[36:39], a[32:35], v[100:103], v[36:39]\n"
      "ds_write_b128 v125, a[156:159] offset:7168\n"
      "v_mfma_f32_16x16x32_bf16 v[52:55], a[36:39], v[100:103], v[52:55]\n"
      "v_mfma_f32_16x16x32_bf16 v[40:43], a[32:35], v[104:107], v[40:43]\n"
      "v_mfma_f32_16x16x32_bf16 v[56:59], a[36:39], v[104:107], v[56:59]\n"
      "v_mfma_f32_16x16x32_bf16 v[44:47], a[32:35], v[108:111], v[44:47]\n"
      "v_mfma_f32_16x16x32_bf16 v[60:63], a[36:39], v[108:111], v[60:63]\n"
      "s_waitcnt lgkmcnt(13)\n"
      "v_mfma_f32_16x16x32_bf16 v[32:35], a[40:43], a[64:67], v[32:35]\n"
      "v_mfma_f32_16x16x32_bf16 v[48:51], a[44:47], a[64:67], v[48:51]\n"
      "v_mfma_f32_16x16x32_bf16 v[36:39], a[40:43], a[68:71], v[36:39]\n"
      "v_mfma_f32_16x16x32_bf16 v[52:55], a[44:47], a[68:71], v[52:55]\n"
      "v_mfma_f32_16x16x32_bf16 v[40:43], a[40:43], a[72:75], v[40:43]\n"
      "v_mfma_f32_16x16x32_bf16 v[56:59], a[44:47], a[72:75], v[56:59]\n"
      "v_mfma_f32_16x16x32_bf16 v[44:47], a[40:43], a[76:79], v[44:47]\n"
      "v_mfma_f32_16x16x32_bf16 v[60:63], a[44:47], a[76:79], v[60:63]\n"
      "s_waitcnt lgkmcnt(8)\n"
      "v_mfma_f32_16x16x32_bf16 v[32:35], a[16:19], v[16:19], v[32:35]\n"
      "v_mfma_f32_16x16x32_bf16 v[48:51], a[20:23], v[16:19], v[48:51]\n"
      "v_mfma_f32_16x16x32_bf16 v[36:39], a[16:19], v[20:23], v[36:39]\n"
      "v_mfma_f32_16x16x32_bf16 v[52:55], a[20:23], v[20:23], v[52:55]\n"
      "v_mfma_f32_16x16x32_bf16 v[40:43], a[16:19], v[24:27], v[40:43]\n"
      "v_mfma_f32_16x16x32_bf16 v[56:59], a[20:23], v[24:27], v[56:59]\n"
      "v_mfma_f32_16x16x32_bf16 v[44:47], a[16:19], v[28:31], v[44:47]\n"
      "v_mfma_f32_16x16x32_bf16 v[60:63], a[20:23], v[28:31], v[60:63]\n"
      "s_waitcnt lgkmcnt(0)\n"
      "s_barrier\n"
      "ds_read_b128 v[160:163], v126 offset:0\n"
      "ds_read_b128 v[176:179], v126 offset:4096\n"
      "ds_read_b128 v[164:167], v126 offset:1024\n"
      "ds_read_b128 v[180:183], v126 offset:5120\n"
      "ds_read_b128 v[168:171], v126 offset:2048\n"
      "ds_read_b128 v[184:187], v126 offset:6144\n"
      "ds_read_b128 v[172:175], v126 offset:3072\n"
      "ds_read_b128 v[188:191], v126 offset:7168\n"
      "ds_read_b128 a[80:83], v126 offset:8192\n"
      "ds_read_b128 a[84:87], v126 offset:9216\n"
      "ds_read_b128 a[88:91], v126 offset:10240\n"
      "ds_read_b128 a[92:95], v126 offset:11264\n"
      "s_waitcnt lgkmcnt(10)\n"
      "v_pk_add_f32 v[32:33], v[32:33], v[160:161]\n"
      "v_pk_add_f32 v[34:35], v[34:35], v[162:163]\n"
      "v_pk_add_f32 v[48:49], v[48:49], v[176:177]\n"
      "v_pk_add_f32 v[50:51], v[50:51], v[178:179]\n"
      "s_waitcnt lgkmcnt(8)\n"
      "v_pk_add_f32 v[36:37], v[36:37], v[164:165]\n"
      "v_pk_add_f32 v[38:39], v[38:39], v[166:167]\n"
      "v_pk_add_f32 v[52:53], v[52:53], v[180:181]\n"
      "v_pk_add_f32 v[54:55], v[54:55], v[182:183]\n"
      "s_waitcnt lgkmcnt(6)\n"
      "v_pk_add_f32 v[40:41], v[40:41], v[168:169]\n"
      "v_pk_add_f32 v[42:43], v[42:43], v[170:171]\n"
      "v_pk_add_f32 v[56:57], v[56:57], v[184:185]\n"
      "v_pk_add_f32 v[58:59], v[58:59], v[186:187]\n"
      "s_waitcnt lgkmcnt(4)\n"
      "v_pk_add_f32 v[44:45], v[44:45], v[172:173]\n"
      "v_pk_add_f32 v[46:47], v[46:47], v[174:175]\n"
      "v_pk_add_f32 v[60:61], v[60:61], v[188:189]\n"
      "v_pk_add_f32 v[62:63], v[62:63], v[190:191]\n"
      "s_waitcnt lgkmcnt(0)\n"
      "v_mfma_f32_16x16x32_bf16 v[32:35], a[24:27], a[80:83], v[32:35]\n"
      "v_mfma_f32_16x16x32_bf16 v[48:51], a[28:31], a[80:83], v[48:51]\n"
      "v_mfma_f32_16x16x32_bf16 v[36:39], a[24:27], a[84:87], v[36:39]\n"
      "v_mfma_f32_16x16x32_bf16 v[52:55], a[28:31], a[84:87], v[52:55]\n"
      "v_mfma_f32_16x16x32_bf16 v[40:43], a[24:27], a[88:91], v[40:43]\n"
      "v_mfma_f32_16x16x32_bf16 v[56:59], a[28:31], a[88:91], v[56:59]\n"
      "v_mfma_f32_16x16x32_bf16 v[44:47], a[24:27], a[92:95], v[44:47]\n"
      "v_mfma_f32_16x16x32_bf16 v[60:63], a[28:31], a[92:95], v[60:63]\n"
      "CHK_%=:\n"
      "s_waitcnt vmcnt(4)\n"
      "v_add_f32 v224, v144, v80\n"
      "v_rcp_f32 v224, v224\n"
      "v_mad_u32_u24 v119, v80, s60, v151\n"
      "v_mad_u32_u24 v119, v81, s62, v119\n"
      "v_add3_u32 v118, v150, v80, v81\n"
      "s_waitcnt vmcnt(3)\n"
      "v_mfma_f32_16x16x32_bf16 v[160:163], v[86:89], v[64:67], v[32:35]\n"
      "v_mad_u32_u24 v119, v64, s60, v119\n"
      "v_mad_u32_u24 v119, v65, s62, v119\n"
      "v_add3_u32 v118, v118, v64, v65\n"
      "v_mov_b32 v226, v224\n"
      "v_mfma_f32_16x16x32_bf16 v[176:179], v[84:87], v[64:67], v[48:51]\n"
      "v_mad_u32_u24 v119, v66, s70, v119\n"
      "v_mad_u32_u24 v119, v67, s71, v119\n"
      "v_add3_u32 v118, v118, v66, v67\n"
      "v_permlane32_swap_b32 v224, v226\n"
      "s_waitcnt vmcnt(2)\n"
      "v_mfma_f32_16x16x32_bf16 v[164:167], v[86:89], v[68:71], v[36:39]\n"
      "v_mad_u32_u24 v119, v68, s60, v119\n"
      "v_mad_u32_u24 v119, v69, s62, v119\n"
      "v_add3_u32 v118, v118, v68, v69\n"
      "v_mov_b32 v225, v224\n"
      "v_mov_b32 v227, v226\n"
      "v_mfma_f32_16x16x32_bf16 v[180:183], v[84:87], v[68:71], v[52:55]\n"
      "v_mad_u32_u24 v119, v70, s70, v119\n"
      "v_mad_u32_u24 v119, v71, s71, v119\n"
      "v_add3_u32 v118, v118, v70, v71\n"
      "v_permlane16_swap_b32 v224, v225\n"
      "s_waitcnt vmcnt(1)\n"
      "v_mfma_f32_16x16x32_bf16 v[168:171], v[86:89], v[72:75], v[40:43]\n"
      "v_mad_u32_u24 v119, v72, s60, v119\n"
      "v_mad_u32_u24 v119, v73, s62, v119\n"
      "v_add3_u32 v118, v118, v72, v73\n"
      "v_permlane16_swap_b32 v226, v227\n"
      "v_mfma_f32_16x16x32_bf16 v[184:187], v[84:87], v[72:75], v[56:59]\n"
      "v_mad_u32_u24 v119, v74, s70, v119\n"
      "v_mad_u32_u24 v119, v75, s71, v119\n"
      "v_add3_u32 v118, v118, v74, v75\n"
      "s_waitcnt vmcnt(0)\n"
      "v_mfma_f32_16x16x32_bf16 v[172:175], v[86:89], v[76:79], v[44:47]\n"
      "v_mad_u32_u24 v119, v76, s60, v119\n"
      "v_mad_u32_u24 v119, v77, s62, v119\n"
      "v_add3_u32 v118, v118, v76, v77\n"
      "v_mfma_f32_16x16x32_bf16 v[188:191], v[84:87], v[76:79], v[60:63]\n"
      "v_mad_u32_u24 v119, v78, s70, v119\n"
      "v_mad_u32_u24 v119, v79, s71, v119\n"
      "v_add3_u32 v118, v118, v78, v79\n"
      "v_cmp_ne_u64 vcc, v[118:119], v[82:83]\n"
      "s_cbranch_vccnz RETRY_%=\n"
      "OK_%=:\n"
      "v_cmp_neq_f32 vcc, v81, v117\n"
      "s_cbranch_vccnz FIX_%=\n"
      "FR_%=:\n"
      "v_pk_mul_f32 v[160:161], v[160:161], v[224:225] op_sel:[0,0] op_sel_hi:[1,0]\n"
      "v_pk_mul_f32 v[162:163], v[162:163], v[224:225] op_sel:[0,0] op_sel_hi:[1,0]\n"
      "v_pk_mul_f32 v[176:177], v[176:177], v[224:225] op_sel:[0,0] op_sel_hi:[1,0]\n"
      "v_pk_mul_f32 v[178:179], v[178:179], v[224:225] op_sel:[0,0] op_sel_hi:[1,0]\n"
      "v_cvt_pk_bf16_f32 v128, v160, v161\n"
      "v_cvt_pk_bf16_f32 v130, v176, v177\n"
      "v_cvt_pk_bf16_f32 v129, v162, v163\n"
      "v_cvt_pk_bf16_f32 v131, v178, v179\n"
      "v_pk_mul_f32 v[164:165], v[164:165], v[224:225] op_sel:[0,1] op_sel_hi:[1,1]\n"
      "v_permlane16_swap_b32 v128, v130\n"
      "v_permlane16_swap_b32 v129, v131\n"
      "global_store_dwordx4 v156, v[128:131], s[16:17] offset:-4096 sc1\n"
      "v_pk_mul_f32 v[166:167], v[166:167], v[224:225] op_sel:[0,1] op_sel_hi:[1,1]\n"
      "v_pk_mul_f32 v[180:181], v[180:181], v[224:225] op_sel:[0,1] op_sel_hi:[1,1]\n"
      "v_pk_mul_f32 v[182:183], v[182:183], v[224:225] op_sel:[0,1] op_sel_hi:[1,1]\n"
      "v_cvt_pk_bf16_f32 v132, v164, v165\n"
      "v_cvt_pk_bf16_f32 v134, v180, v181\n"
      "v_cvt_pk_bf16_f32 v133, v166, v167\n"
      "v_cvt_pk_bf16_f32 v135, v182, v183\n"
      "v_pk_mul_f32 v[168:169], v[168:169], v[226:227] op_sel:[0,0] op_sel_hi:[1,0]\n"
      "v_permlane16_swap_b32 v132, v134\n"
      "v_permlane16_swap_b32 v133, v135\n"
      "global_store_dwordx4 v156, v[132:135], s[16:17] offset:0 sc1\n"
      "v_pk_mul_f32 v[170:171], v[170:171], v[226:227] op_sel:[0,0] op_sel_hi:[1,0]\n"
      "v_pk_mul_f32 v[184:185], v[184:185], v[226:227] op_sel:[0,0] op_sel_hi:[1,0]\n"
      "v_pk_mul_f32 v[186:187], v[186:187], v[226:227] op_sel:[0,0] op_sel_hi:[1,0]\n"
      "v_cvt_pk_bf16_f32 v136, v168, v169\n"
      "v_cvt_pk_bf16_f32 v138, v184, v185\n"
      "v_cvt_pk_bf16_f32 v137, v170, v171\n"
      "v_cvt_pk_bf16_f32 v139, v186, v187\n"
      "v_pk_mul_f32 v[172:173], v[172:173], v[226:227] op_sel:[0,1] op_sel_hi:[1,1]\n"
      "v_permlane16_swap_b32 v136, v138\n"
      "v_permlane16_swap_b32 v137, v139\n"
      "global_store_dwordx4 v155, v[136:139], s[16:17] offset:-4096 sc1\n"
      "v_pk_mul_f32 v[174:175], v[174:175], v[226:227] op_sel:[0,1] op_sel_hi:[1,1]\n"
      "v_pk_mul_f32 v[188:189], v[188:189], v[226:227] op_sel:[0,1] op_sel_hi:[1,1]\n"
      "v_pk_mul_f32 v[190:191], v[190:191], v[226:227] op_sel:[0,1] op_sel_hi:[1,1]\n"
      "v_cvt_pk_bf16_f32 v140, v172, v173\n"
      "v_cvt_pk_bf16_f32 v142, v188, v189\n"
      "v_cvt_pk_bf16_f32 v141, v174, v175\n"
      "v_cvt_pk_bf16_f32 v143, v190, v191\n"
      "v_and_b32 v157, 0xffffff80, v157\n"
      "v_permlane16_swap_b32 v140, v142\n"
      "v_permlane16_swap_b32 v141, v143\n"
      "global_store_dwordx4 v155, v[140:143], s[16:17] offset:0 sc1\n"
      "s_and_b32 s67, %[fo], 8\n"
      "s_or_b32 s67, s67, %[xw]\n"
      "s_cbranch_scc1 END_%=\n"
      "s_mov_b64 exec, 1\n"
      "global_store_dwordx2 v157, v[150:151], s[18:19] sc1\n"
      "s_mov_b64 exec, -1\n"
      "END_%=:\n"
      ".subsection 1\n"
      "POLL_%=:\n"
      "s_waitcnt vmcnt(0) lgkmcnt(0)\n"
      "v_cmp_ne_u32 vcc, v234, v150\n"
      "v_mov_b32 v233, v235\n"
      "s_memrealtime s[68:69]\n"
      "s_sub_u32 s63, s63, 1\n"
      "s_cbranch_vccz GOT_%=\n"
      "global_load_dwordx2 v[234:235], v158, s[18:19] sc1\n"
      "s_cbranch_scc0 POLL_%=\n"
      "s_branch GOT_%=\n"
      "RETRY_%=:\n"
      "s_sub_u32 s63, s63, 1\n"
      "s_cbranch_scc1 OK_%=\n"
      "s_sleep 4\n"
      "buffer_load_dwordx4 v[80:83], v127, s[28:31], s79 offen sc1\n"
      "buffer_load_dwordx4 v[64:67], v127, s[28:31], s80 offen offset:0 sc1\n"
      "buffer_load_dwordx4 v[68:71], v127, s[28:31], s80 offen offset:1024 sc1\n"
      "buffer_load_dwordx4 v[72:75], v127, s[28:31], s80 offen offset:2048 sc1\n"
      "buffer_load_dwordx4 v[76:79], v127, s[28:31], s80 offen offset:3072 sc1\n"
      "s_branch CHK_%=\n"
      "GW0_%=:\n"
      "s_call_b64 s[84:85], WAIT_%=\n"
      "s_branch GX0_%=\n"
      "GW1_%=:\n"
      "s_call_b64 s[84:85], WAIT_%=\n"
      "s_branch GX1_%=\n"
      "GW2_%=:\n"
      "s_call_b64 s[84:85], WAIT_%=\n"
      "s_branch GX2_%=\n"
      "GW3_%=:\n"
      "s_call_b64 s[84:85], WAIT_%=\n"
      "s_add_u32 s87, s87, 1\n"
      "s_branch GX3_%=\n"
      "GW4_%=:\n"
      "s_call_b64 s[84:85], WAIT_%=\n"
      "s_branch GX4_%=\n"
      "GW5_%=:\n"
      "s_call_b64 s[84:85], WAIT_%=\n"
      "s_branch GX5_%=\n"
      "GW6_%=:\n"
      "s_call_b64 s[84:85], WAIT_%=\n"
      "s_branch GX6_%=\n"
      "GW7_%=:\n"
      "s_call_b64 s[84:85], WAIT_%=\n"
      "s_branch GX7_%=\n"
      "GW8_%=:\n"
      "s_call_b64 s[84:85], WAIT_%=\n"
      "s_branch GX8_%=\n"
      "WAIT_%=:\n"
      "s_add_u32 s87, s87, 1\n"
      "s_bitcmp1_b32 s89, s87\n"
      "s_cbranch_scc1 H1_%=\n"
      "SPIN_%=:\n"
      "s_sub_u32 s67, s68, s88\n"
      "s_cmp_ge_i32 s67, 0\n"
      "s_cbranch_scc1 GO_%=\n"
      "s_sleep 1\n"
      "s_memrealtime s[68:69]\n"
      "s_waitcnt lgkmcnt(0)\n"
      "s_branch SPIN_%=\n"
      "GO_%=:\n"
      "s_setpc_b64 s[84:85]\n"
      "H1_%=:\n"
      "s_cmp_gt_i32 s93, 50\n"
      "s_cbranch_scc1 HS_%=\n"
      "s_bitcmp1_b32 0x2a80, s87\n"
      "s_cbranch_scc0 H2_%=\n"
      "s_add_u32 s67, s66, 400\n"
      "s_waitcnt vmcnt(8)\n"
      "v_sub_u32 v250, s67, v250\n"
      "v_cmp_gt_u32 vcc, 900, v250\n"
      "v_add_u32 v250, 0xa00, v250\n"
      "v_xor_b32 v251, 0x100, v251\n"
      "s_sub_u32 s69, s66, s71\n"
      "s_cmp_ge_u32 s87, 12\n"
      "s_cselect_b32 s101, 50, s101\n"
      "s_bcnt1_i32_b32 s74, vcc_lo\n"
      "s_bcnt1_i32_b32 s75, vcc_hi\n"
      "v_cmp_gt_u32 vcc, 0x800, v250\n"
      "s_add_u32 s67, s74, s75\n"
      "s_cmp_eq_u32 s87, 7\n"
      "s_cselect_b32 s76, 1, 0\n"
      "s_lshr_b32 s76, s67, s76\n"
      "s_cmp_le_u32 s76, 2\n"
      "s_cselect_b32 s86, 107, s98\n"
      "s_cmp_ge_u32 s76, 9\n"
      "s_cselect_b32 s86, 112, s86\n"
      "s_cbranch_vccnz H6_%=\n"
      "s_cmp_ge_u32 s74, 11\n"
      "s_cbranch_scc1 H3_%=\n"
      "s_cmp_eq_u32 s87, 13\n"
      "s_cbranch_scc1 H4_%=\n"
      "H8_%=:\n"
      "s_lshr_b32 s67, %[rep], 7\n"
      "s_cmp_eq_u32 s67, 1\n"
      "s_cbranch_scc0 SPIN_%=\n"
      "H5_%=:\n"
      "s_cmp_gt_i32 s93, 50\n"
      "s_cselect_b32 s75, -10, 0\n"
      "v_mov_b32 v127, s66\n"
      "s_add_u32 s74, s87, 2\n"
      "s_bfe_u32 s74, s74, 0x10001\n"
      "s_lshl_b32 s74, s74, 8\n"
      "s_add_u32 s74, s74, %[rep]\n"
      "RS_%=:\n"
      "s_sub_u32 s67, s68, s88\n"
      "s_cmp_ge_i32 s67, s75\n"
      "s_cbranch_scc1 RG_%=\n"
      "s_sleep 1\n"
      "s_memrealtime s[68:69]\n"
      "s_waitcnt lgkmcnt(0)\n"
      "s_branch RS_%=\n"
      "RG_%=:\n"
      "s_getreg_b32 s67, hwreg(HW_REG_IB_STS, 0, 4)\n"
      "s_cmp_le_u32 s67, 4\n"
      "s_cbranch_scc1 SPIN_%=\n"
      "buffer_store_dword v127, v124, s[28:31], s74 offen sc1\n"
      "s_branch SPIN_%=\n"
      "H6_%=:\n"
      "s_ff1_i32_b64 s67, vcc\n"
      "v_sub_u32 v250, s66, v250\n"
      "s_nop 3\n"
      "v_readlane_b32 s76, v250, s67\n"
      "s_add_u32 s76, s76, 2960\n"
      "s_and_b32 s76, s76, 31\n"
      "s_lshl_b32 s76, s76, 3\n"
      "s_lshl_b32 s89, 4, s87\n"
      "s_branch H7_%=\n"
      "H3_%=:\n"
      "s_mul_i32 s76, s69, 110\n"
      "s_ashr_i32 s76, s76, 10\n"
      "s_min_i32 s76, s76, 40\n"
      "s_add_u32 s76, s76, 84\n"
      "s_cmp_lt_u32 s75, 13\n"
      "s_cselect_b32 s76, 56, s76\n"
      "s_cmp_lt_u32 s75, 5\n"
      "s_cselect_b32 s76, 32, s76\n"
      "s_add_u32 s67, s66, 2000\n"
      "s_and_b32 s67, s67, -32\n"
      "s_lshr_b32 s75, s76, 3\n"
      "s_or_b32 s67, s67, s75\n"
      "v_mov_b32 v127, s67\n"
      "s_add_u32 s74, s87, 2\n"
      "s_bfe_u32 s74, s74, 0x10001\n"
      "s_lshl_b32 s74, s74, 8\n"
      "s_add_u32 s74, s74, %[rep]\n"
      "s_cmp_lt_u32 %[rep], 128\n"
      "s_cselect_b32 s89, 22, 20\n"
      "s_lshl_b32 s89, s89, s87\n"
      "s_sub_u32 s75, %[rep], 128\n"
      "s_cmp_lt_u32 s75, 32\n"
      "s_cbranch_scc0 H7_%=\n"
      "buffer_store_dword v127, v124, s[28:31], s74 offen sc1\n"
      "H7_%=:\n"
      "s_add_u32 s66, s66, s76\n"
      "s_mov_b32 s99, 13\n"
      "s_mov_b32 s100, 286\n"
      "s_add_u32 s93, s93, 99\n"
      "s_mov_b32 s86, 110\n"
      "s_mov_b32 s80, 0xc853212b\n"
      "s_mov_b32 s81, 0x842210\n"
      "s_cmp_le_u32 s76, 56\n"
      "s_cselect_b32 s75, 10, 0\n"
      "s_cmp_le_u32 s76, 32\n"
      "s_cselect_b32 s75, 25, s75\n"
      "s_lshr_b64 s[80:81], s[80:81], s75\n"
      "s_sub_u32 s67, s65, s66\n"
      "s_cmp_lt_i32 s67, 0\n"
      "s_cselect_b32 s88, s66, s65\n"
      "s_mov_b32 s101, 200\n"
      "s_branch SPIN_%=\n"
      "H4_%=:\n"
      "s_mov_b32 s91, 0\n"
      "s_mov_b32 s89, 0\n"
      "v_mov_b32 v251, 0x4000000\n"
      "s_branch SPIN_%=\n"
      "HS_%=:\n"
      "s_bitcmp1_b32 s87, 0\n"
      "s_cbranch_scc0 H5_%=\n"
      "v_xor_b32 v251, 0x100, v251\n"
      "s_lshr_b32 s67, s89, s87\n"
      "s_cmp_gt_u32 s67, 1\n"
      "s_cbranch_scc1 SPIN_%=\n"
      "s_add_u32 s67, s66, 400\n"
      "s_waitcnt vmcnt(8)\n"
      "v_sub_u32 v250, s67, v250\n"
      "v_cmp_gt_u32 vcc, 900, v250\n"
      "s_nop 3\n"
      "s_bcnt1_i32_b32 s74, vcc_lo\n"
      "s_cmp_gt_u32 s74, 2\n"
      "s_cbranch_scc1 H4_%=\n"
      "s_mov_b32 s99, 21\n"
      "s_mov_b32 s100, 282\n"
      "s_mov_b32 s86, 107\n"
      "s_mov_b64 s[80:81], 0\n"
      "s_branch H4_%=\n"
      "H0_%=:\n"
      "buffer_load_dword v250, v251, s[28:31], 0 offen sc1\n"
      "s_branch SPIN_%=\n"
      "H9_%=:\n"
      "s_bitcmp1_b32 s87, 0\n"
      "s_cbranch_scc0 H5_%=\n"
      "v_xor_b32 v251, 0x100, v251\n"
      "s_cmp_eq_u32 s87, 5\n"
      "s_cbranch_scc1 H8_%=\n"
      "s_branch SPIN_%=\n"
      "H2_%=:\n"
      "s_cmp_gt_u32 s87, 2\n"
      "s_cbranch_scc1 H9_%=\n"
      "s_cmp_eq_u32 s87, 1\n"
      "s_cbranch_scc1 H0_%=\n"
      "s_waitcnt vmcnt(8)\n"
      "v_subrev_u32 v250, s71, v250\n"
      "v_min_u32 v250, 0x258, v250\n"
      "v_mbcnt_lo_u32_b32 v251, -1, 0\n"
      "v_mbcnt_hi_u32_b32 v251, -1, v251\n"
      "v_add_u32_dpp v250, v250, v250 row_shr:1 row_mask:0xf bank_mask:0xf bound_ctrl:0\n"
      "v_lshlrev_b32 v124, 23, v251\n"
      "s_nop 0\n"
      "v_add_u32_dpp v250, v250, v250 row_shr:2 row_mask:0xf bank_mask:0xf bound_ctrl:0\n"
      "v_lshlrev_b32 v251, 2, v251\n"
      "v_add_u32 v124, 0x6000, v124\n"
      "v_add_u32_dpp v250, v250, v250 row_shr:4 row_mask:0xf bank_mask:0xf bound_ctrl:0\n"
      "v_add_u32 v251, 0x6100, v251\n"
      "v_readlane_b32 s66, v250, 7\n"
      "s_nop 0\n"
      "s_lshr_b32 s66, s66, 3\n"
      "s_add_u32 s66, s66, s71\n"
      "s_add_u32 s66, s66, %[cph]\n"
      "s_add_u32 s71, s66, 550\n"
      "s_sub_u32 s67, s65, s66\n"
      "s_cmp_lt_i32 s67, 0\n"
      "s_cselect_b32 s88, s66, s65\n"
      "s_branch SPIN_%=\n"
      "FB0_%=:\n"
      "s_nop 7\n"
      "s_nop 7\n"
      "v_xor_b32 v118, 0x0, v242\n"
      "v_xor_b32 v119, 0x0, v243\n"
      "ds_read_b128 a[64:67], v118 offset:0\n"
      "ds_read_b128 a[68:71], v118 offset:4096\n"
      "ds_read_b128 a[72:75], v119 offset:0\n"
      "ds_read_b128 a[76:79], v119 offset:4096\n"
      "ds_read_b128 a[80:83], v118 offset:1024\n"
      "ds_read_b128 a[84:87], v118 offset:5120\n"
      "ds_read_b128 a[88:91], v119 offset:1024\n"
      "ds_read_b128 a[92:95], v119 offset:5120\n"
      "s_waitcnt lgkmcnt(0)\n"
      "v_mfma_f32_16x16x32_bf16 v[128:131], a[64:67], a[0:3], 0\n"
      "v_mfma_f32_16x16x32_bf16 v[136:139], a[64:67], a[4:7], 0\n"
      "v_mfma_f32_16x16x32_bf16 v[144:147], a[64:67], a[8:11], 0\n"
      "v_mfma_f32_16x16x32_bf16 v[152:155], a[64:67], a[12:15], 0\n"
      "v_mfma_f32_16x16x32_bf16 v[132:135], a[68:71], a[0:3], 0\n"
      "v_mfma_f32_16x16x32_bf16 v[140:143], a[68:71], a[4:7], 0\n"
      "v_mfma_f32_16x16x32_bf16 v[148:151], a[68:71], a[8:11], 0\n"
      "v_mfma_f32_16x16x32_bf16 v[156:159], a[68:71], a[12:15], 0\n"
      "v_mfma_f32_16x16x32_bf16 v[128:131], a[72:75], a[16:19], v[128:131]\n"
      "v_mfma_f32_16x16x32_bf16 v[136:139], a[72:75], a[20:23], v[136:139]\n"
      "v_mfma_f32_16x16x32_bf16 v[144:147], a[72:75], a[24:27], v[144:147]\n"
      "v_mfma_f32_16x16x32_bf16 v[152:155], a[72:75], a[28:31], v[152:155]\n"
      "v_mfma_f32_16x16x32_bf16 v[132:135], a[76:79], a[16:19], v[132:135]\n"
      "v_mfma_f32_16x16x32_bf16 v[140:143], a[76:79], a[20:23], v[140:143]\n"
      "v_mfma_f32_16x16x32_bf16 v[148:151], a[76:79], a[24:27], v[148:151]\n"
      "v_mfma_f32_16x16x32_bf16 v[156:159], a[76:79], a[28:31], v[156:159]\n"
      "v_mfma_f32_16x16x32_bf16 v[128:131], a[80:83], a[32:35], v[128:131]\n"
      "v_mfma_f32_16x16x32_bf16 v[136:139], a[80:83], a[36:39], v[136:139]\n"
      "v_mfma_f32_16x16x32_bf16 v[144:147], a[80:83], a[40:43], v[144:147]\n"
      "v_mfma_f32_16x16x32_bf16 v[152:155], a[80:83], a[44:47], v[152:155]\n"
      "v_mfma_f32_16x16x32_bf16 v[132:135], a[84:87], a[32:35], v[132:135]\n"
      "v_mfma_f32_16x16x32_bf16 v[140:143], a[84:87], a[36:39], v[140:143]\n"
      "v_mfma_f32_16x16x32_bf16 v[148:151], a[84:87], a[40:43], v[148:151]\n"
      "v_mfma_f32_16x16x32_bf16 v[156:159], a[84:87], a[44:47], v[156:159]\n"
      "v_mfma_f32_16x16x32_bf16 v[128:131], a[88:91], a[48:51], v[128:131]\n"
      "v_mfma_f32_16x16x32_bf16 v[136:139], a[88:91], a[52:55], v[136:139]\n"
      "v_mfma_f32_16x16x32_bf16 v[144:147], a[88:91], a[56:59], v[144:147]\n"
      "v_mfma_f32_16x16x32_bf16 v[152:155], a[88:91], a[60:63], v[152:155]\n"
      "v_mfma_f32_16x16x32_bf16 v[132:135], a[92:95], a[48:51], v[132:135]\n"
      "v_mfma_f32_16x16x32_bf16 v[140:143], a[92:95], a[52:55], v[140:143]\n"
      "v_mfma_f32_16x16x32_bf16 v[148:151], a[92:95], a[56:59], v[148:151]\n"
      "v_mfma_f32_16x16x32_bf16 v[156:159], a[92:95], a[60:63], v[156:159]\n"
      "s_nop 7\n"
      "v_max3_f32 v112, v128, v129, v130\n"
      "v_max3_f32 v113, v136, v137, v138\n"
      "v_max3_f32 v114, v144, v145, v146\n"
      "v_max3_f32 v115, v152, v153, v154\n"
      "v_max3_f32 v112, v112, v131, v132\n"
      "v_max3_f32 v113, v113, v139, v140\n"
      "v_max3_f32 v114, v114, v147, v148\n"
      "v_max3_f32 v115, v115, v155, v156\n"
      "v_max3_f32 v112, v112, v133, v134\n"
      "v_max3_f32 v113, v113, v141, v142\n"
      "v_max3_f32 v114, v114, v149, v150\n"
      "v_max3_f32 v115, v115, v157, v158\n"
      "v_max_f32 v112, v112, v135\n"
      "v_max_f32 v113, v113, v143\n"
      "v_max_f32 v114, v114, v151\n"
      "v_max_f32 v115, v115, v159\n"
      "v_mov_b32 v160, v112\n"
      "v_mov_b32 v161, v113\n"
      "v_mov_b32 v162, v114\n"
      "v_mov_b32 v163, v115\n"
      "v_permlane16_swap_b32 v112, v160\n"
      "v_permlane16_swap_b32 v113, v161\n"
      "v_permlane16_swap_b32 v114, v162\n"
      "v_permlane16_swap_b32 v115, v163\n"
      "v_max_f32 v112, v112, v160\n"
      "v_max_f32 v113, v113, v161\n"
      "v_max_f32 v114, v114, v162\n"
      "v_max_f32 v115, v115, v163\n"
      "v_mov_b32 v160, v112\n"
      "v_mov_b32 v161, v113\n"
      "v_mov_b32 v162, v114\n"
      "v_mov_b32 v163, v115\n"
      "v_permlane32_swap_b32 v112, v160\n"
      "v_permlane32_swap_b32 v113, v161\n"
      "v_permlane32_swap_b32 v114, v162\n"
      "v_permlane32_swap_b32 v115, v163\n"
      "v_max_f32 v112, v112, v160\n"
      "v_max_f32 v113, v113, v161\n"
      "v_max_f32 v114, v114, v162\n"
      "v_max_f32 v115, v115, v163\n"
      "v_sub_f32 v112, 0xc1800000, v112\n"
      "v_sub_f32 v113, 0xc1800000, v113\n"
      "v_sub_f32 v114, 0xc1800000, v114\n"
      "v_sub_f32 v115, 0xc1800000, v115\n"
      "v_floor_f32 v112, v112\n"
      "v_floor_f32 v113, v113\n"
      "v_floor_f32 v114, v114\n"
      "v_floor_f32 v115, v115\n"
      "v_min_f32 v112, v112, v224\n"
      "v_min_f32 v113, v113, v228\n"
      "v_min_f32 v114, v114, v232\n"
      "v_min_f32 v115, v115, v236\n"
      "v_sub_f32 v164, v112, v224\n"
      "v_sub_f32 v165, v113, v228\n"
      "v_sub_f32 v166, v114, v232\n"
      "v_sub_f32 v167, v115, v236\n"
      "v_mov_b32 v224, v112\n"
      "v_mov_b32 v228, v113\n"
      "v_mov_b32 v232, v114\n"
      "v_mov_b32 v236, v115\n"
      "v_exp_f32 v112, v164\n"
      "v_exp_f32 v113, v165\n"
      "v_exp_f32 v114, v166\n"
      "v_exp_f32 v115, v167\n"
      "v_mov_b32 v225, v224\n"
      "v_mov_b32 v229, v228\n"
      "v_mov_b32 v233, v232\n"
      "v_mov_b32 v237, v236\n"
      "v_mov_b64 v[226:227], v[224:225]\n"
      "v_mov_b64 v[230:231], v[228:229]\n"
      "v_mov_b64 v[234:235], v[232:233]\n"
      "v_mov_b64 v[238:239], v[236:237]\n"
      "s_call_b64 s[84:85], SCALE_%=\n"
      "v_pk_add_f32 v[128:129], v[128:129], v[224:225]\n"
      "v_pk_add_f32 v[130:131], v[130:131], v[224:225]\n"
      "v_pk_add_f32 v[132:133], v[132:133], v[224:225]\n"
      "v_pk_add_f32 v[134:135], v[134:135], v[224:225]\n"
      "v_pk_add_f32 v[136:137], v[136:137], v[228:229]\n"
      "v_pk_add_f32 v[138:139], v[138:139], v[228:229]\n"
      "v_pk_add_f32 v[140:141], v[140:141], v[228:229]\n"
      "v_pk_add_f32 v[142:143], v[142:143], v[228:229]\n"
      "v_pk_add_f32 v[144:145], v[144:145], v[232:233]\n"
      "v_pk_add_f32 v[146:147], v[146:147], v[232:233]\n"
      "v_pk_add_f32 v[148:149], v[148:149], v[232:233]\n"
      "v_pk_add_f32 v[150:151], v[150:151], v[232:233]\n"
      "v_pk_add_f32 v[152:153], v[152:153], v[236:237]\n"
      "v_pk_add_f32 v[154:155], v[154:155], v[236:237]\n"
      "v_pk_add_f32 v[156:157], v[156:157], v[236:237]\n"
      "v_pk_add_f32 v[158:159], v[158:159], v[236:237]\n"
      "v_exp_f32 v112, v128\n"
      "v_exp_f32 v113, v129\n"
      "v_exp_f32 v114, v130\n"
      "v_cvt_pk_bf16_f32 v128, v112, v113\n"
      "v_exp_f32 v115, v131\n"
      "v_exp_f32 v112, v132\n"
      "v_cvt_pk_bf16_f32 v129, v114, v115\n"
      "v_exp_f32 v113, v133\n"
      "v_exp_f32 v114, v134\n"
      "v_cvt_pk_bf16_f32 v130, v112, v113\n"
      "v_exp_f32 v115, v135\n"
      "v_exp_f32 v112, v136\n"
      "v_cvt_pk_bf16_f32 v131, v114, v115\n"
      "v_exp_f32 v113, v137\n"
      "v_exp_f32 v114, v138\n"
      "v_cvt_pk_bf16_f32 v132, v112, v113\n"
      "v_exp_f32 v115, v139\n"
      "v_exp_f32 v112, v140\n"
      "v_cvt_pk_bf16_f32 v133, v114, v115\n"
      "v_exp_f32 v113, v141\n"
      "v_exp_f32 v114, v142\n"
      "v_cvt_pk_bf16_f32 v134, v112, v113\n"
      "v_exp_f32 v115, v143\n"
      "v_exp_f32 v112, v144\n"
      "v_cvt_pk_bf16_f32 v135, v114, v115\n"
      "v_exp_f32 v113, v145\n"
      "v_exp_f32 v114, v146\n"
      "v_cvt_pk_bf16_f32 v136, v112, v113\n"
      "v_exp_f32 v115, v147\n"
      "v_exp_f32 v112, v148\n"
      "v_cvt_pk_bf16_f32 v137, v114, v115\n"
      "v_exp_f32 v113, v149\n"
      "v_exp_f32 v114, v150\n"
      "v_cvt_pk_bf16_f32 v138, v112, v113\n"
      "v_exp_f32 v115, v151\n"
      "v_exp_f32 v112, v152\n"
      "v_cvt_pk_bf16_f32 v139, v114, v115\n"
      "v_exp_f32 v113, v153\n"
      "v_exp_f32 v114, v154\n"
      "v_cvt_pk_bf16_f32 v140, v112, v113\n"
      "v_exp_f32 v115, v155\n"
      "v_exp_f32 v112, v156\n"
      "v_cvt_pk_bf16_f32 v141, v114, v115\n"
      "v_exp_f32 v113, v157\n"
      "v_exp_f32 v114, v158\n"
      "v_cvt_pk_bf16_f32 v142, v112, v113\n"
      "v_exp_f32 v115, v159\n"
      "s_nop 0\n"
      "v_cvt_pk_bf16_f32 v143, v114, v115\n"
      "ds_read_b128 a[64:67], v242 offset:32768\n"
      "ds_read_b128 a[68:71], v242 offset:36864\n"
      "ds_read_b128 a[72:75], v243 offset:32768\n"
      "ds_read_b128 a[76:79], v243 offset:36864\n"
      "ds_read_b128 a[80:83], v242 offset:33792\n"
      "ds_read_b128 a[84:87], v242 offset:37888\n"
      "ds_read_b128 a[88:91], v243 offset:33792\n"
      "ds_read_b128 a[92:95], v243 offset:37888\n"
      "s_waitcnt lgkmcnt(0)\n"
      "v_mfma_f32_16x16x32_bf16 v[160:163], a[64:67], a[0:3], v[224:227]\n"
      "v_mfma_f32_16x16x32_bf16 v[168:171], a[64:67], a[4:7], v[228:231]\n"
      "v_mfma_f32_16x16x32_bf16 v[176:179], a[64:67], a[8:11], v[232:235]\n"
      "v_mfma_f32_16x16x32_bf16 v[184:187], a[64:67], a[12:15], v[236:239]\n"
      "v_mfma_f32_16x16x32_bf16 v[164:167], a[68:71], a[0:3], v[224:227]\n"
      "v_mfma_f32_16x16x32_bf16 v[172:175], a[68:71], a[4:7], v[228:231]\n"
      "v_mfma_f32_16x16x32_bf16 v[180:183], a[68:71], a[8:11], v[232:235]\n"
      "v_mfma_f32_16x16x32_bf16 v[188:191], a[68:71], a[12:15], v[236:239]\n"
      "v_mfma_f32_16x16x32_bf16 v[160:163], a[72:75], a[16:19], v[160:163]\n"
      "v_mfma_f32_16x16x32_bf16 v[168:171], a[72:75], a[20:23], v[168:171]\n"
      "v_mfma_f32_16x16x32_bf16 v[176:179], a[72:75], a[24:27], v[176:179]\n"
      "v_mfma_f32_16x16x32_bf16 v[184:187], a[72:75], a[28:31], v[184:187]\n"
      "v_mfma_f32_16x16x32_bf16 v[164:167], a[76:79], a[16:19], v[164:167]\n"
      "v_mfma_f32_16x16x32_bf16 v[172:175], a[76:79], a[20:23], v[172:175]\n"
      "v_mfma_f32_16x16x32_bf16 v[180:183], a[76:79], a[24:27], v[180:183]\n"
      "v_mfma_f32_16x16x32_bf16 v[188:191], a[76:79], a[28:31], v[188:191]\n"
      "v_mfma_f32_16x16x32_bf16 v[160:163], a[80:83], a[32:35], v[160:163]\n"
      "v_mfma_f32_16x16x32_bf16 v[168:171], a[80:83], a[36:39], v[168:171]\n"
      "v_mfma_f32_16x16x32_bf16 v[176:179], a[80:83], a[40:43], v[176:179]\n"
      "v_mfma_f32_16x16x32_bf16 v[184:187], a[80:83], a[44:47], v[184:187]\n"
      "v_mfma_f32_16x16x32_bf16 v[164:167], a[84:87], a[32:35], v[164:167]\n"
      "v_mfma_f32_16x16x32_bf16 v[172:175], a[84:87], a[36:39], v[172:175]\n"
      "v_mfma_f32_16x16x32_bf16 v[180:183], a[84:87], a[40:43], v[180:183]\n"
      "v_mfma_f32_16x16x32_bf16 v[188:191], a[84:87], a[44:47], v[188:191]\n"
      "v_mfma_f32_16x16x32_bf16 v[160:163], a[88:91], a[48:51], v[160:163]\n"
      "v_mfma_f32_16x16x32_bf16 v[168:171], a[88:91], a[52:55], v[168:171]\n"
      "v_mfma_f32_16x16x32_bf16 v[176:179], a[88:91], a[56:59], v[176:179]\n"
      "v_mfma_f32_16x16x32_bf16 v[184:187], a[88:91], a[60:63], v[184:187]\n"
      "v_mfma_f32_16x16x32_bf16 v[164:167], a[92:95], a[48:51], v[164:167]\n"
      "v_mfma_f32_16x16x32_bf16 v[172:175], a[92:95], a[52:55], v[172:175]\n"
      "v_mfma_f32_16x16x32_bf16 v[180:183], a[92:95], a[56:59], v[180:183]\n"
      "v_mfma_f32_16x16x32_bf16 v[188:191], a[92:95], a[60:63], v[188:191]\n"
      "s_cmp_eq_u32 s64, 0\n"
      "s_cbranch_scc0 NRET0_0_%=\n"
      "v_mov_b64 v[192:193], v[128:129]\n"
      "v_mov_b64 v[194:195], v[130:131]\n"
      "v_mov_b64 v[196:197], v[132:133]\n"
      "v_mov_b64 v[198:199], v[134:135]\n"
      "v_mov_b64 v[200:201], v[136:137]\n"
      "v_mov_b64 v[202:203], v[138:139]\n"
      "v_mov_b64 v[204:205], v[140:141]\n"
      "v_mov_b64 v[206:207], v[142:143]\n"
      "s_nop 1\n"
      "s_branch RET0_0_%=\n"
      "NRET0_0_%=:\n"
      "s_cmp_eq_u32 s64, 1\n"
      "s_cbranch_scc0 NRET0_1_%=\n"
      "v_mov_b64 v[192:193], v[128:129]\n"
      "v_mov_b64 v[194:195], v[130:131]\n"
      "v_mov_b64 v[196:197], v[132:133]\n"
      "v_mov_b64 v[198:199], v[134:135]\n"
      "v_mov_b64 v[200:201], v[136:137]\n"
      "v_mov_b64 v[202:203], v[138:139]\n"
      "v_mov_b64 v[204:205], v[140:141]\n"
      "v_mov_b64 v[206:207], v[142:143]\n"
      "s_nop 1\n"
      "s_branch RET0_1_%=\n"
      "NRET0_1_%=:\n"
      "s_cmp_eq_u32 s64, 2\n"
      "s_cbranch_scc0 NRET0_2_%=\n"
      "v_mov_b64 v[192:193], v[128:129]\n"
      "v_mov_b64 v[194:195], v[130:131]\n"
      "v_mov_b64 v[196:197], v[132:133]\n"
      "v_mov_b64 v[198:199], v[134:135]\n"
      "v_mov_b64 v[200:201], v[136:137]\n"
      "v_mov_b64 v[202:203], v[138:139]\n"
      "v_mov_b64 v[204:205], v[140:141]\n"
      "v_mov_b64 v[206:207], v[142:143]\n"
      "s_nop 1\n"
      "s_branch RET0_2_%=\n"
      "NRET0_2_%=:\n"
      "s_cmp_eq_u32 s64, 3\n"
      "s_cbranch_scc0 NRET0_3_%=\n"
      "v_mov_b64 v[192:193], v[128:129]\n"
      "v_mov_b64 v[194:195], v[130:131]\n"
      "v_mov_b64 v[196:197], v[132:133]\n"
      "v_mov_b64 v[198:199], v[134:135]\n"
      "v_mov_b64 v[200:201], v[136:137]\n"
      "v_mov_b64 v[202:203], v[138:139]\n"
      "v_mov_b64 v[204:205], v[140:141]\n"
      "v_mov_b64 v[206:207], v[142:143]\n"
      "s_nop 1\n"
      "s_branch RET0_3_%=\n"
      "NRET0_3_%=:\n"
      "s_cmp_eq_u32 s64, 4\n"
      "s_cbranch_scc0 NRET0_4_%=\n"
      "v_mov_b64 v[192:193], v[128:129]\n"
      "v_mov_b64 v[194:195], v[130:131]\n"
      "v_mov_b64 v[196:197], v[132:133]\n"
      "v_mov_b64 v[198:199], v[134:135]\n"
      "v_mov_b64 v[200:201], v[136:137]\n"
      "v_mov_b64 v[202:203], v[138:139]\n"
      "v_mov_b64 v[204:205], v[140:141]\n"
      "v_mov_b64 v[206:207], v[142:143]\n"
      "s_nop 1\n"
      "s_branch RET0_4_%=\n"
      "NRET0_4_%=:\n"
      "s_cmp_eq_u32 s64, 5\n"
      "s_cbranch_scc0 NRET0_5_%=\n"
      "v_mov_b64 v[16:17], v[128:129]\n"
      "v_mov_b64 v[18:19], v[130:131]\n"
      "v_mov_b64 v[20:21], v[132:133]\n"
      "v_mov_b64 v[22:23], v[134:135]\n"
      "v_mov_b64 v[24:25], v[136:137]\n"
      "v_mov_b64 v[26:27], v[138:139]\n"
      "v_mov_b64 v[28:29], v[140:141]\n"
      "v_mov_b64 v[30:31], v[142:143]\n"
      "s_nop 1\n"
      "s_branch RET0_5_%=\n"
      "NRET0_5_%=:\n"
      "FB1_%=:\n"
      "s_add_u32 s84, s63, 1\n"
      "s_sub_u32 s84, s84, %[n1]\n"
      "s_or_b32 s84, s84, s64\n"
      "s_cbranch_scc0 RET1_0_%=\n"
      "s_nop 7\n"
      "s_nop 7\n"
      "v_xor_b32 v118, 0x10000, v242\n"
      "v_xor_b32 v119, 0x10000, v243\n"
      "ds_read_b128 a[64:67], v118 offset:32768\n"
      "ds_read_b128 a[68:71], v118 offset:36864\n"
      "ds_read_b128 a[72:75], v119 offset:32768\n"
      "ds_read_b128 a[76:79], v119 offset:36864\n"
      "ds_read_b128 a[80:83], v118 offset:33792\n"
      "ds_read_b128 a[84:87], v118 offset:37888\n"
      "ds_read_b128 a[88:91], v119 offset:33792\n"
      "ds_read_b128 a[92:95], v119 offset:37888\n"
      "s_waitcnt lgkmcnt(0)\n"
      "v_mfma_f32_16x16x32_bf16 v[160:163], a[64:67], a[0:3], 0\n"
      "v_mfma_f32_16x16x32_bf16 v[168:171], a[64:67], a[4:7], 0\n"
      "v_mfma_f32_16x16x32_bf16 v[176:179], a[64:67], a[8:11], 0\n"
      "v_mfma_f32_16x16x32_bf16 v[184:187], a[64:67], a[12:15], 0\n"
      "v_mfma_f32_16x16x32_bf16 v[164:167], a[68:71], a[0:3], 0\n"
      "v_mfma_f32_16x16x32_bf16 v[172:175], a[68:71], a[4:7], 0\n"
      "v_mfma_f32_16x16x32_bf16 v[180:183], a[68:71], a[8:11], 0\n"
      "v_mfma_f32_16x16x32_bf16 v[188:191], a[68:71], a[12:15], 0\n"
      "v_mfma_f32_16x16x32_bf16 v[160:163], a[72:75], a[16:19], v[160:163]\n"
      "v_mfma_f32_16x16x32_bf16 v[168:171], a[72:75], a[20:23], v[168:171]\n"
      "v_mfma_f32_16x16x32_bf16 v[176:179], a[72:75], a[24:27], v[176:179]\n"
      "v_mfma_f32_16x16x32_bf16 v[184:187], a[72:75], a[28:31], v[184:187]\n"
      "v_mfma_f32_16x16x32_bf16 v[164:167], a[76:79], a[16:19], v[164:167]\n"
      "v_mfma_f32_16x16x32_bf16 v[172:175], a[76:79], a[20:23], v[172:175]\n"
      "v_mfma_f32_16x16x32_bf16 v[180:183], a[76:79], a[24:27], v[180:183]\n"
      "v_mfma_f32_16x16x32_bf16 v[188:191], a[76:79], a[28:31], v[188:191]\n"
      "v_mfma_f32_16x16x32_bf16 v[160:163], a[80:83], a[32:35], v[160:163]\n"
      "v_mfma_f32_16x16x32_bf16 v[168:171], a[80:83], a[36:39], v[168:171]\n"
      "v_mfma_f32_16x16x32_bf16 v[176:179], a[80:83], a[40:43], v[176:179]\n"
      "v_mfma_f32_16x16x32_bf16 v[184:187], a[80:83], a[44:47], v[184:187]\n"
      "v_mfma_f32_16x16x32_bf16 v[164:167], a[84:87], a[32:35], v[164:167]\n"
      "v_mfma_f32_16x16x32_bf16 v[172:175], a[84:87], a[36:39], v[172:175]\n"
      "v_mfma_f32_16x16x32_bf16 v[180:183], a[84:87], a[40:43], v[180:183]\n"
      "v_mfma_f32_16x16x32_bf16 v[188:191], a[84:87], a[44:47], v[188:191]\n"
      "v_mfma_f32_16x16x32_bf16 v[160:163], a[88:91], a[48:51], v[160:163]\n"
      "v_mfma_f32_16x16x32_bf16 v[168:171], a[88:91], a[52:55], v[168:171]\n"
      "v_mfma_f32_16x16x32_bf16 v[176:179], a[88:91], a[56:59], v[176:179]\n"
      "v_mfma_f32_16x16x32_bf16 v[184:187], a[88:91], a[60:63], v[184:187]\n"
      "v_mfma_f32_16x16x32_bf16 v[164:167], a[92:95], a[48:51], v[164:167]\n"
      "v_mfma_f32_16x16x32_bf16 v[172:175], a[92:95], a[52:55], v[172:175]\n"
      "v_mfma_f32_16x16x32_bf16 v[180:183], a[92:95], a[56:59], v[180:183]\n"
      "v_mfma_f32_16x16x32_bf16 v[188:191], a[92:95], a[60:63], v[188:191]\n"
      "s_nop 7\n"
      "v_max3_f32 v112, v160, v161, v162\n"
      "v_max3_f32 v113, v168, v169, v170\n"
      "v_max3_f32 v114, v176, v177, v178\n"
      "v_max3_f32 v115, v184, v185, v186\n"
      "v_max3_f32 v112, v112, v163, v164\n"
      "v_max3_f32 v113, v113, v171, v172\n"
      "v_max3_f32 v114, v114, v179, v180\n"
      "v_max3_f32 v115, v115, v187, v188\n"
      "v_max3_f32 v112, v112, v165, v166\n"
      "v_max3_f32 v113, v113, v173, v174\n"
      "v_max3_f32 v114, v114, v181, v182\n"
      "v_max3_f32 v115, v115, v189, v190\n"
      "v_max_f32 v112, v112, v167\n"
      "v_max_f32 v113, v113, v175\n"
      "v_max_f32 v114, v114, v183\n"
      "v_max_f32 v115, v115, v191\n"
      "v_mov_b32 v128, v112\n"
      "v_mov_b32 v129, v113\n"
      "v_mov_b32 v130, v114\n"
      "v_mov_b32 v131, v115\n"
      "v_permlane16_swap_b32 v112, v128\n"
      "v_permlane16_swap_b32 v113, v129\n"
      "v_permlane16_swap_b32 v114, v130\n"
      "v_permlane16_swap_b32 v115, v131\n"
      "v_max_f32 v112, v112, v128\n"
      "v_max_f32 v113, v113, v129\n"
      "v_max_f32 v114, v114, v130\n"
      "v_max_f32 v115, v115, v131\n"
      "v_mov_b32 v128, v112\n"
      "v_mov_b32 v129, v113\n"
      "v_mov_b32 v130, v114\n"
      "v_mov_b32 v131, v115\n"
      "v_permlane32_swap_b32 v112, v128\n"
      "v_permlane32_swap_b32 v113, v129\n"
      "v_permlane32_swap_b32 v114, v130\n"
      "v_permlane32_swap_b32 v115, v131\n"
      "v_max_f32 v112, v112, v128\n"
      "v_max_f32 v113, v113, v129\n"
      "v_max_f32 v114, v114, v130\n"
      "v_max_f32 v115, v115, v131\n"
      "v_sub_f32 v112, 0xc1800000, v112\n"
      "v_sub_f32 v113, 0xc1800000, v113\n"
      "v_sub_f32 v114, 0xc1800000, v114\n"
      "v_sub_f32 v115, 0xc1800000, v115\n"
      "v_floor_f32 v112, v112\n"
      "v_floor_f32 v113, v113\n"
      "v_floor_f32 v114, v114\n"
      "v_floor_f32 v115, v115\n"
      "v_min_f32 v112, v112, v224\n"
      "v_min_f32 v113, v113, v228\n"
      "v_min_f32 v114, v114, v232\n"
      "v_min_f32 v115, v115, v236\n"
      "v_sub_f32 v132, v112, v224\n"
      "v_sub_f32 v133, v113, v228\n"
      "v_sub_f32 v134, v114, v232\n"
      "v_sub_f32 v135, v115, v236\n"
      "v_mov_b32 v224, v112\n"
      "v_mov_b32 v228, v113\n"
      "v_mov_b32 v232, v114\n"
      "v_mov_b32 v236, v115\n"
      "v_exp_f32 v112, v132\n"
      "v_exp_f32 v113, v133\n"
      "v_exp_f32 v114, v134\n"
      "v_exp_f32 v115, v135\n"
      "v_mov_b32 v225, v224\n"
      "v_mov_b32 v229, v228\n"
      "v_mov_b32 v233, v232\n"
      "v_mov_b32 v237, v236\n"
      "v_mov_b64 v[226:227], v[224:225]\n"
      "v_mov_b64 v[230:231], v[228:229]\n"
      "v_mov_b64 v[234:235], v[232:233]\n"
      "v_mov_b64 v[238:239], v[236:237]\n"
      "s_call_b64 s[84:85], SCALE_%=\n"
      "v_pk_add_f32 v[160:161], v[160:161], v[224:225]\n"
      "v_pk_add_f32 v[162:163], v[162:163], v[224:225]\n"
      "v_pk_add_f32 v[164:165], v[164:165], v[224:225]\n"
      "v_pk_add_f32 v[166:167], v[166:167], v[224:225]\n"
      "v_pk_add_f32 v[168:169], v[168:169], v[228:229]\n"
      "v_pk_add_f32 v[170:171], v[170:171], v[228:229]\n"
      "v_pk_add_f32 v[172:173], v[172:173], v[228:229]\n"
      "v_pk_add_f32 v[174:175], v[174:175], v[228:229]\n"
      "v_pk_add_f32 v[176:177], v[176:177], v[232:233]\n"
      "v_pk_add_f32 v[178:179], v[178:179], v[232:233]\n"
      "v_pk_add_f32 v[180:181], v[180:181], v[232:233]\n"
      "v_pk_add_f32 v[182:183], v[182:183], v[232:233]\n"
      "v_pk_add_f32 v[184:185], v[184:185], v[236:237]\n"
      "v_pk_add_f32 v[186:187], v[186:187], v[236:237]\n"
      "v_pk_add_f32 v[188:189], v[188:189], v[236:237]\n"
      "v_pk_add_f32 v[190:191], v[190:191], v[236:237]\n"
      "v_exp_f32 v112, v160\n"
      "v_exp_f32 v113, v161\n"
      "v_exp_f32 v114, v162\n"
      "v_cvt_pk_bf16_f32 v160, v112, v113\n"
      "v_exp_f32 v115, v163\n"
      "v_exp_f32 v112, v164\n"
      "v_cvt_pk_bf16_f32 v161, v114, v115\n"
      "v_exp_f32 v113, v165\n"
      "v_exp_f32 v114, v166\n"
      "v_cvt_pk_bf16_f32 v162, v112, v113\n"
      "v_exp_f32 v115, v167\n"
      "v_exp_f32 v112, v168\n"
      "v_cvt_pk_bf16_f32 v163, v114, v115\n"
      "v_exp_f32 v113, v169\n"
      "v_exp_f32 v114, v170\n"
      "v_cvt_pk_bf16_f32 v164, v112, v113\n"
      "v_exp_f32 v115, v171\n"
      "v_exp_f32 v112, v172\n"
      "v_cvt_pk_bf16_f32 v165, v114, v115\n"
      "v_exp_f32 v113, v173\n"
      "v_exp_f32 v114, v174\n"
      "v_cvt_pk_bf16_f32 v166, v112, v113\n"
      "v_exp_f32 v115, v175\n"
      "v_exp_f32 v112, v176\n"
      "v_cvt_pk_bf16_f32 v167, v114, v115\n"
      "v_exp_f32 v113, v177\n"
      "v_exp_f32 v114, v178\n"
      "v_cvt_pk_bf16_f32 v168, v112, v113\n"
      "v_exp_f32 v115, v179\n"
      "v_exp_f32 v112, v180\n"
      "v_cvt_pk_bf16_f32 v169, v114, v115\n"
      "v_exp_f32 v113, v181\n"
      "v_exp_f32 v114, v182\n"
      "v_cvt_pk_bf16_f32 v170, v112, v113\n"
      "v_exp_f32 v115, v183\n"
      "v_exp_f32 v112, v184\n"
      "v_cvt_pk_bf16_f32 v171, v114, v115\n"
      "v_exp_f32 v113, v185\n"
      "v_exp_f32 v114, v186\n"
      "v_cvt_pk_bf16_f32 v172, v112, v113\n"
      "v_exp_f32 v115, v187\n"
      "v_exp_f32 v112, v188\n"
      "v_cvt_pk_bf16_f32 v173, v114, v115\n"
      "v_exp_f32 v113, v189\n"
      "v_exp_f32 v114, v190\n"
      "v_cvt_pk_bf16_f32 v174, v112, v113\n"
      "v_exp_f32 v115, v191\n"
      "s_nop 0\n"
      "v_cvt_pk_bf16_f32 v175, v114, v115\n"
      "ds_read_b128 a[64:67], v242 offset:0\n"
      "ds_read_b128 a[68:71], v242 offset:4096\n"
      "ds_read_b128 a[72:75], v243 offset:0\n"
      "ds_read_b128 a[76:79], v243 offset:4096\n"
      "ds_read_b128 a[80:83], v242 offset:1024\n"
      "ds_read_b128 a[84:87], v242 offset:5120\n"
      "ds_read_b128 a[88:91], v243 offset:1024\n"
      "ds_read_b128 a[92:95], v243 offset:5120\n"
      "s_waitcnt lgkmcnt(0)\n"
      "v_mfma_f32_16x16x32_bf16 v[128:131], a[64:67], a[0:3], v[224:227]\n"
      "v_mfma_f32_16x16x32_bf16 v[136:139], a[64:67], a[4:7], v[228:231]\n"
      "v_mfma_f32_16x16x32_bf16 v[144:147], a[64:67], a[8:11], v[232:235]\n"
      "v_mfma_f32_16x16x32_bf16 v[152:155], a[64:67], a[12:15], v[236:239]\n"
      "v_mfma_f32_16x16x32_bf16 v[132:135], a[68:71], a[0:3], v[224:227]\n"
      "v_mfma_f32_16x16x32_bf16 v[140:143], a[68:71], a[4:7], v[228:231]\n"
      "v_mfma_f32_16x16x32_bf16 v[148:151], a[68:71], a[8:11], v[232:235]\n"
      "v_mfma_f32_16x16x32_bf16 v[156:159], a[68:71], a[12:15], v[236:239]\n"
      "v_mfma_f32_16x16x32_bf16 v[128:131], a[72:75], a[16:19], v[128:131]\n"
      "v_mfma_f32_16x16x32_bf16 v[136:139], a[72:75], a[20:23], v[136:139]\n"
      "v_mfma_f32_16x16x32_bf16 v[144:147], a[72:75], a[24:27], v[144:147]\n"
      "v_mfma_f32_16x16x32_bf16 v[152:155], a[72:75], a[28:31], v[152:155]\n"
      "v_mfma_f32_16x16x32_bf16 v[132:135], a[76:79], a[16:19], v[132:135]\n"
      "v_mfma_f32_16x16x32_bf16 v[140:143], a[76:79], a[20:23], v[140:143]\n"
      "v_mfma_f32_16x16x32_bf16 v[148:151], a[76:79], a[24:27], v[148:151]\n"
      "v_mfma_f32_16x16x32_bf16 v[156:159], a[76:79], a[28:31], v[156:159]\n"
      "v_mfma_f32_16x16x32_bf16 v[128:131], a[80:83], a[32:35], v[128:131]\n"
      "v_mfma_f32_16x16x32_bf16 v[136:139], a[80:83], a[36:39], v[136:139]\n"
      "v_mfma_f32_16x16x32_bf16 v[144:147], a[80:83], a[40:43], v[144:147]\n"
      "v_mfma_f32_16x16x32_bf16 v[152:155], a[80:83], a[44:47], v[152:155]\n"
      "v_mfma_f32_16x16x32_bf16 v[132:135], a[84:87], a[32:35], v[132:135]\n"
      "v_mfma_f32_16x16x32_bf16 v[140:143], a[84:87], a[36:39], v[140:143]\n"
      "v_mfma_f32_16x16x32_bf16 v[148:151], a[84:87], a[40:43], v[148:151]\n"
      "v_mfma_f32_16x16x32_bf16 v[156:159], a[84:87], a[44:47], v[156:159]\n"
      "v_mfma_f32_16x16x32_bf16 v[128:131], a[88:91], a[48:51], v[128:131]\n"
      "v_mfma_f32_16x16x32_bf16 v[136:139], a[88:91], a[52:55], v[136:139]\n"
      "v_mfma_f32_16x16x32_bf16 v[144:147], a[88:91], a[56:59], v[144:147]\n"
      "v_mfma_f32_16x16x32_bf16 v[152:155], a[88:91], a[60:63], v[152:155]\n"
      "v_mfma_f32_16x16x32_bf16 v[132:135], a[92:95], a[48:51], v[132:135]\n"
      "v_mfma_f32_16x16x32_bf16 v[140:143], a[92:95], a[52:55], v[140:143]\n"
      "v_mfma_f32_16x16x32_bf16 v[148:151], a[92:95], a[56:59], v[148:151]\n"
      "v_mfma_f32_16x16x32_bf16 v[156:159], a[92:95], a[60:63], v[156:159]\n"
      "s_cmp_eq_u32 s64, 0\n"
      "s_cbranch_scc0 NRET1_0_%=\n"
      "v_mov_b64 v[208:209], v[160:161]\n"
      "v_mov_b64 v[210:211], v[162:163]\n"
      "v_mov_b64 v[212:213], v[164:165]\n"
      "v_mov_b64 v[214:215], v[166:167]\n"
      "v_mov_b64 v[216:217], v[168:169]\n"
      "v_mov_b64 v[218:219], v[170:171]\n"
      "v_mov_b64 v[220:221], v[172:173]\n"
      "v_mov_b64 v[222:223], v[174:175]\n"
      "s_nop 1\n"
      "s_branch RET1_0_%=\n"
      "NRET1_0_%=:\n"
      "s_cmp_eq_u32 s64, 1\n"
      "s_cbranch_scc0 NRET1_1_%=\n"
      "v_mov_b64 v[208:209], v[160:161]\n"
      "v_mov_b64 v[210:211], v[162:163]\n"
      "v_mov_b64 v[212:213], v[164:165]\n"
      "v_mov_b64 v[214:215], v[166:167]\n"
      "v_mov_b64 v[216:217], v[168:169]\n"
      "v_mov_b64 v[218:219], v[170:171]\n"
      "v_mov_b64 v[220:221], v[172:173]\n"
      "v_mov_b64 v[222:223], v[174:175]\n"
      "s_nop 1\n"
      "s_branch RET1_1_%=\n"
      "NRET1_1_%=:\n"
      "s_cmp_eq_u32 s64, 2\n"
      "s_cbranch_scc0 NRET1_2_%=\n"
      "v_mov_b64 v[208:209], v[160:161]\n"
      "v_mov_b64 v[210:211], v[162:163]\n"
      "v_mov_b64 v[212:213], v[164:165]\n"
      "v_mov_b64 v[214:215], v[166:167]\n"
      "v_mov_b64 v[216:217], v[168:169]\n"
      "v_mov_b64 v[218:219], v[170:171]\n"
      "v_mov_b64 v[220:221], v[172:173]\n"
      "v_mov_b64 v[222:223], v[174:175]\n"
      "s_nop 1\n"
      "s_branch RET1_2_%=\n"
      "NRET1_2_%=:\n"
      "s_cmp_eq_u32 s64, 3\n"
      "s_cbranch_scc0 NRET1_3_%=\n"
      "v_mov_b64 v[208:209], v[160:161]\n"
      "v_mov_b64 v[210:211], v[162:163]\n"
      "v_mov_b64 v[212:213], v[164:165]\n"
      "v_mov_b64 v[214:215], v[166:167]\n"
      "v_mov_b64 v[216:217], v[168:169]\n"
      "v_mov_b64 v[218:219], v[170:171]\n"
      "v_mov_b64 v[220:221], v[172:173]\n"
      "v_mov_b64 v[222:223], v[174:175]\n"
      "s_nop 1\n"
      "s_branch RET1_3_%=\n"
      "NRET1_3_%=:\n"
      "s_cmp_eq_u32 s64, 4\n"
      "s_cbranch_scc0 NRET1_4_%=\n"
      "v_mov_b64 v[208:209], v[160:161]\n"
      "v_mov_b64 v[210:211], v[162:163]\n"
      "v_mov_b64 v[212:213], v[164:165]\n"
      "v_mov_b64 v[214:215], v[166:167]\n"
      "v_mov_b64 v[216:217], v[168:169]\n"
      "v_mov_b64 v[218:219], v[170:171]\n"
      "v_mov_b64 v[220:221], v[172:173]\n"
      "v_mov_b64 v[222:223], v[174:175]\n"
      "s_nop 1\n"
      "s_branch RET1_4_%=\n"
      "NRET1_4_%=:\n"
      "s_cmp_eq_u32 s64, 5\n"
      "s_cbranch_scc0 NRET1_5_%=\n"
      "v_mov_b64 v[0:1], v[160:161]\n"
      "v_mov_b64 v[2:3], v[162:163]\n"
      "v_mov_b64 v[4:5], v[164:165]\n"
      "v_mov_b64 v[6:7], v[166:167]\n"
      "v_mov_b64 v[8:9], v[168:169]\n"
      "v_mov_b64 v[10:11], v[170:171]\n"
      "v_mov_b64 v[12:13], v[172:173]\n"
      "v_mov_b64 v[14:15], v[174:175]\n"
      "s_nop 1\n"
      "s_branch RET1_5_%=\n"
      "NRET1_5_%=:\n"
      "s_cmp_eq_u32 s64, 6\n"
      "s_cbranch_scc0 NRET1_6_%=\n"
      "v_mov_b64 v[96:97], v[160:161]\n"
      "v_mov_b64 v[98:99], v[162:163]\n"
      "v_mov_b64 v[100:101], v[164:165]\n"
      "v_mov_b64 v[102:103], v[166:167]\n"
      "v_mov_b64 v[104:105], v[168:169]\n"
      "v_mov_b64 v[106:107], v[170:171]\n"
      "v_mov_b64 v[108:109], v[172:173]\n"
      "v_mov_b64 v[110:111], v[174:175]\n"
      "v_mov_b32 v117, v224\n"
      "s_nop 1\n"
      "v_mov_b32_dpp v117, v228 quad_perm:[0,1,2,3] row_mask:0x2 bank_mask:0xf\n"
      "s_nop 1\n"
      "v_mov_b32_dpp v117, v232 quad_perm:[0,1,2,3] row_mask:0x4 bank_mask:0xf\n"
      "s_nop 1\n"
      "v_mov_b32_dpp v117, v236 quad_perm:[0,1,2,3] row_mask:0x8 bank_mask:0xf\n"
      "v_mfma_f32_16x16x32_bf16 a[228:231], a[196:199], v[96:99], a[192:195]\n"
      "v_mfma_f32_16x16x32_bf16 a[228:231], a[200:203], v[100:103], a[228:231]\n"
      "v_mfma_f32_16x16x32_bf16 a[228:231], a[204:207], v[104:107], a[228:231]\n"
      "ds_write_b128 v125, a[160:163] offset:0\n"
      "ds_write_b128 v125, a[164:167] offset:1024\n"
      "ds_write_b128 v125, a[168:171] offset:2048\n"
      "ds_write_b128 v125, a[172:175] offset:3072\n"
      "ds_write_b128 v125, a[176:179] offset:4096\n"
      "ds_write_b128 v125, a[180:183] offset:5120\n"
      "ds_write_b128 v125, a[184:187] offset:6144\n"
      "ds_write_b128 v125, a[188:191] offset:7168\n"
      "ds_write_b128 v125, v[96:99] offset:8192\n"
      "ds_write_b128 v125, v[100:103] offset:9216\n"
      "ds_write_b128 v125, v[104:107] offset:10240\n"
      "ds_write_b32 v125, v117 offset:12292\n"
      "s_nop 1\n"
      "s_branch RET1_6_%=\n"
      "NRET1_6_%=:\n"
      "SCALE_%=:\n"
      "v_pk_mul_f32 v[32:33], v[32:33], v[112:113] op_sel:[0,0] op_sel_hi:[1,0]\n"
      "v_pk_mul_f32 v[34:35], v[34:35], v[112:113] op_sel:[0,0] op_sel_hi:[1,0]\n"
      "v_pk_mul_f32 v[36:37], v[36:37], v[112:113] op_sel:[0,1] op_sel_hi:[1,1]\n"
      "v_pk_mul_f32 v[38:39], v[38:39], v[112:113] op_sel:[0,1] op_sel_hi:[1,1]\n"
      "v_pk_mul_f32 v[40:41], v[40:41], v[114:115] op_sel:[0,0] op_sel_hi:[1,0]\n"
      "v_pk_mul_f32 v[42:43], v[42:43], v[114:115] op_sel:[0,0] op_sel_hi:[1,0]\n"
      "v_pk_mul_f32 v[44:45], v[44:45], v[114:115] op_sel:[0,1] op_sel_hi:[1,1]\n"
      "v_pk_mul_f32 v[46:47], v[46:47], v[114:115] op_sel:[0,1] op_sel_hi:[1,1]\n"
      "v_pk_mul_f32 v[48:49], v[48:49], v[112:113] op_sel:[0,0] op_sel_hi:[1,0]\n"
      "v_pk_mul_f32 v[50:51], v[50:51], v[112:113] op_sel:[0,0] op_sel_hi:[1,0]\n"
      "v_pk_mul_f32 v[52:53], v[52:53], v[112:113] op_sel:[0,1] op_sel_hi:[1,1]\n"
      "v_pk_mul_f32 v[54:55], v[54:55], v[112:113] op_sel:[0,1] op_sel_hi:[1,1]\n"
      "v_pk_mul_f32 v[56:57], v[56:57], v[114:115] op_sel:[0,0] op_sel_hi:[1,0]\n"
      "v_pk_mul_f32 v[58:59], v[58:59], v[114:115] op_sel:[0,0] op_sel_hi:[1,0]\n"
      "v_pk_mul_f32 v[60:61], v[60:61], v[114:115] op_sel:[0,1] op_sel_hi:[1,1]\n"
      "v_pk_mul_f32 v[62:63], v[62:63], v[114:115] op_sel:[0,1] op_sel_hi:[1,1]\n"
      "v_pk_mul_f32 v[64:65], v[64:65], v[112:113] op_sel:[0,0] op_sel_hi:[1,0]\n"
      "v_pk_mul_f32 v[66:67], v[66:67], v[112:113] op_sel:[0,0] op_sel_hi:[1,0]\n"
      "v_pk_mul_f32 v[68:69], v[68:69], v[112:113] op_sel:[0,1] op_sel_hi:[1,1]\n"
      "v_pk_mul_f32 v[70:71], v[70:71], v[112:113] op_sel:[0,1] op_sel_hi:[1,1]\n"
      "v_pk_mul_f32 v[72:73], v[72:73], v[114:115] op_sel:[0,0] op_sel_hi:[1,0]\n"
      "v_pk_mul_f32 v[74:75], v[74:75], v[114:115] op_sel:[0,0] op_sel_hi:[1,0]\n"
      "v_pk_mul_f32 v[76:77], v[76:77], v[114:115] op_sel:[0,1] op_sel_hi:[1,1]\n"
      "v_pk_mul_f32 v[78:79], v[78:79], v[114:115] op_sel:[0,1] op_sel_hi:[1,1]\n"
      "v_pk_mul_f32 v[80:81], v[80:81], v[112:113] op_sel:[0,0] op_sel_hi:[1,0]\n"
      "v_pk_mul_f32 v[82:83], v[82:83], v[112:113] op_sel:[0,0] op_sel_hi:[1,0]\n"
      "v_pk_mul_f32 v[84:85], v[84:85], v[112:113] op_sel:[0,1] op_sel_hi:[1,1]\n"
      "v_pk_mul_f32 v[86:87], v[86:87], v[112:113] op_sel:[0,1] op_sel_hi:[1,1]\n"
      "v_pk_mul_f32 v[88:89], v[88:89], v[114:115] op_sel:[0,0] op_sel_hi:[1,0]\n"
      "v_pk_mul_f32 v[90:91], v[90:91], v[114:115] op_sel:[0,0] op_sel_hi:[1,0]\n"
      "v_pk_mul_f32 v[92:93], v[92:93], v[114:115] op_sel:[0,1] op_sel_hi:[1,1]\n"
      "v_pk_mul_f32 v[94:95], v[94:95], v[114:115] op_sel:[0,1] op_sel_hi:[1,1]\n"
      "v_accvgpr_read_b32 v118, a128\n"
      "v_accvgpr_read_b32 v119, a129\n"
      "v_pk_mul_f32 v[118:119], v[118:119], v[112:113] op_sel:[0,0] op_sel_hi:[1,0]\n"
      "v_accvgpr_write_b32 a128, v118\n"
      "v_accvgpr_write_b32 a129, v119\n"
      "v_accvgpr_read_b32 v118, a130\n"
      "v_accvgpr_read_b32 v119, a131\n"
      "v_pk_mul_f32 v[118:119], v[118:119], v[112:113] op_sel:[0,0] op_sel_hi:[1,0]\n"
      "v_accvgpr_write_b32 a130, v118\n"
      "v_accvgpr_write_b32 a131, v119\n"
      "v_accvgpr_read_b32 v118, a132\n"
      "v_accvgpr_read_b32 v119, a133\n"
      "v_pk_mul_f32 v[118:119], v[118:119], v[112:113] op_sel:[0,1] op_sel_hi:[1,1]\n"
      "v_accvgpr_write_b32 a132, v118\n"
      "v_accvgpr_write_b32 a133, v119\n"
      "v_accvgpr_read_b32 v118, a134\n"
      "v_accvgpr_read_b32 v119, a135\n"
      "v_pk_mul_f32 v[118:119], v[118:119], v[112:113] op_sel:[0,1] op_sel_hi:[1,1]\n"
      "v_accvgpr_write_b32 a134, v118\n"
      "v_accvgpr_write_b32 a135, v119\n"
      "v_accvgpr_read_b32 v118, a136\n"
      "v_accvgpr_read_b32 v119, a137\n"
      "v_pk_mul_f32 v[118:119], v[118:119], v[114:115] op_sel:[0,0] op_sel_hi:[1,0]\n"
      "v_accvgpr_write_b32 a136, v118\n"
      "v_accvgpr_write_b32 a137, v119\n"
      "v_accvgpr_read_b32 v118, a138\n"
      "v_accvgpr_read_b32 v119, a139\n"
      "v_pk_mul_f32 v[118:119], v[118:119], v[114:115] op_sel:[0,0] op_sel_hi:[1,0]\n"
      "v_accvgpr_write_b32 a138, v118\n"
      "v_accvgpr_write_b32 a139, v119\n"
      "v_accvgpr_read_b32 v118, a140\n"
      "v_accvgpr_read_b32 v119, a141\n"
      "v_pk_mul_f32 v[118:119], v[118:119], v[114:115] op_sel:[0,1] op_sel_hi:[1,1]\n"
      "v_accvgpr_write_b32 a140, v118\n"
      "v_accvgpr_write_b32 a141, v119\n"
      "v_accvgpr_read_b32 v118, a142\n"
      "v_accvgpr_read_b32 v119, a143\n"
      "v_pk_mul_f32 v[118:119], v[118:119], v[114:115] op_sel:[0,1] op_sel_hi:[1,1]\n"
      "v_accvgpr_write_b32 a142, v118\n"
      "v_accvgpr_write_b32 a143, v119\n"
      "v_accvgpr_read_b32 v118, a144\n"
      "v_accvgpr_read_b32 v119, a145\n"
      "v_pk_mul_f32 v[118:119], v[118:119], v[112:113] op_sel:[0,0] op_sel_hi:[1,0]\n"
      "v_accvgpr_write_b32 a144, v118\n"
      "v_accvgpr_write_b32 a145, v119\n"
      "v_accvgpr_read_b32 v118, a146\n"
      "v_accvgpr_read_b32 v119, a147\n"
      "v_pk_mul_f32 v[118:119], v[118:119], v[112:113] op_sel:[0,0] op_sel_hi:[1,0]\n"
      "v_accvgpr_write_b32 a146, v118\n"
      "v_accvgpr_write_b32 a147, v119\n"
      "v_accvgpr_read_b32 v118, a148\n"
      "v_accvgpr_read_b32 v119, a149\n"
      "v_pk_mul_f32 v[118:119], v[118:119], v[112:113] op_sel:[0,1] op_sel_hi:[1,1]\n"
      "v_accvgpr_write_b32 a148, v118\n"
      "v_accvgpr_write_b32 a149, v119\n"
      "v_accvgpr_read_b32 v118, a150\n"
      "v_accvgpr_read_b32 v119, a151\n"
      "v_pk_mul_f32 v[118:119], v[118:119], v[112:113] op_sel:[0,1] op_sel_hi:[1,1]\n"
      "v_accvgpr_write_b32 a150, v118\n"
      "v_accvgpr_write_b32 a151, v119\n"
      "v_accvgpr_read_b32 v118, a152\n"
      "v_accvgpr_read_b32 v119, a153\n"
      "v_pk_mul_f32 v[118:119], v[118:119], v[114:115] op_sel:[0,0] op_sel_hi:[1,0]\n"
      "v_accvgpr_write_b32 a152, v118\n"
      "v_accvgpr_write_b32 a153, v119\n"
      "v_accvgpr_read_b32 v118, a154\n"
      "v_accvgpr_read_b32 v119, a155\n"
      "v_pk_mul_f32 v[118:119], v[118:119], v[114:115] op_sel:[0,0] op_sel_hi:[1,0]\n"
      "v_accvgpr_write_b32 a154, v118\n"
      "v_accvgpr_write_b32 a155, v119\n"
      "v_accvgpr_read_b32 v118, a156\n"
      "v_accvgpr_read_b32 v119, a157\n"
      "v_pk_mul_f32 v[118:119], v[118:119], v[114:115] op_sel:[0,1] op_sel_hi:[1,1]\n"
      "v_accvgpr_write_b32 a156, v118\n"
      "v_accvgpr_write_b32 a157, v119\n"
      "v_accvgpr_read_b32 v118, a158\n"
      "v_accvgpr_read_b32 v119, a159\n"
      "v_pk_mul_f32 v[118:119], v[118:119], v[114:115] op_sel:[0,1] op_sel_hi:[1,1]\n"
      "v_accvgpr_write_b32 a158, v118\n"
      "v_accvgpr_write_b32 a159, v119\n"
      "v_accvgpr_read_b32 v118, a160\n"
      "v_accvgpr_read_b32 v119, a161\n"
      "v_pk_mul_f32 v[118:119], v[118:119], v[112:113] op_sel:[0,0] op_sel_hi:[1,0]\n"
      "v_accvgpr_write_b32 a160, v118\n"
      "v_accvgpr_write_b32 a161, v119\n"
      "v_accvgpr_read_b32 v118, a162\n"
      "v_accvgpr_read_b32 v119, a163\n"
      "v_pk_mul_f32 v[118:119], v[118:119], v[112:113] op_sel:[0,0] op_sel_hi:[1,0]\n"
      "v_accvgpr_write_b32 a162, v118\n"
      "v_accvgpr_write_b32 a163, v119\n"
      "v_accvgpr_read_b32 v118, a164\n"
      "v_accvgpr_read_b32 v119, a165\n"
      "v_pk_mul_f32 v[118:119], v[118:119], v[112:113] op_sel:[0,1] op_sel_hi:[1,1]\n"
      "v_accvgpr_write_b32 a164, v118\n"
      "v_accvgpr_write_b32 a165, v119\n"
      "v_accvgpr_read_b32 v118, a166\n"
      "v_accvgpr_read_b32 v119, a167\n"
      "v_pk_mul_f32 v[118:119], v[118:119], v[112:113] op_sel:[0,1] op_sel_hi:[1,1]\n"
      "v_accvgpr_write_b32 a166, v118\n"
      "v_accvgpr_write_b32 a167, v119\n"
      "v_accvgpr_read_b32 v118, a168\n"
      "v_accvgpr_read_b32 v119, a169\n"
      "v_pk_mul_f32 v[118:119], v[118:119], v[114:115] op_sel:[0,0] op_sel_hi:[1,0]\n"
      "v_accvgpr_write_b32 a168, v118\n"
      "v_accvgpr_write_b32 a169, v119\n"
      "v_accvgpr_read_b32 v118, a170\n"
      "v_accvgpr_read_b32 v119, a171\n"
      "v_pk_mul_f32 v[118:119], v[118:119], v[114:115] op_sel:[0,0] op_sel_hi:[1,0]\n"
      "v_accvgpr_write_b32 a170, v118\n"
      "v_accvgpr_write_b32 a171, v119\n"
      "v_accvgpr_read_b32 v118, a172\n"
      "v_accvgpr_read_b32 v119, a173\n"
      "v_pk_mul_f32 v[118:119], v[118:119], v[114:115] op_sel:[0,1] op_sel_hi:[1,1]\n"
      "v_accvgpr_write_b32 a172, v118\n"
      "v_accvgpr_write_b32 a173, v119\n"
      "v_accvgpr_read_b32 v118, a174\n"
      "v_accvgpr_read_b32 v119, a175\n"
      "v_pk_mul_f32 v[118:119], v[118:119], v[114:115] op_sel:[0,1] op_sel_hi:[1,1]\n"
      "v_accvgpr_write_b32 a174, v118\n"
      "v_accvgpr_write_b32 a175, v119\n"
      "v_accvgpr_read_b32 v118, a176\n"
      "v_accvgpr_read_b32 v119, a177\n"
      "v_pk_mul_f32 v[118:119], v[118:119], v[112:113] op_sel:[0,0] op_sel_hi:[1,0]\n"
      "v_accvgpr_write_b32 a176, v118\n"
      "v_accvgpr_write_b32 a177, v119\n"
      "v_accvgpr_read_b32 v118, a178\n"
      "v_accvgpr_read_b32 v119, a179\n"
      "v_pk_mul_f32 v[118:119], v[118:119], v[112:113] op_sel:[0,0] op_sel_hi:[1,0]\n"
      "v_accvgpr_write_b32 a178, v118\n"
      "v_accvgpr_write_b32 a179, v119\n"
      "v_accvgpr_read_b32 v118, a180\n"
      "v_accvgpr_read_b32 v119, a181\n"
      "v_pk_mul_f32 v[118:119], v[118:119], v[112:113] op_sel:[0,1] op_sel_hi:[1,1]\n"
      "v_accvgpr_write_b32 a180, v118\n"
      "v_accvgpr_write_b32 a181, v119\n"
      "v_accvgpr_read_b32 v118, a182\n"
      "v_accvgpr_read_b32 v119, a183\n"
      "v_pk_mul_f32 v[118:119], v[118:119], v[112:113] op_sel:[0,1] op_sel_hi:[1,1]\n"
      "v_accvgpr_write_b32 a182, v118\n"
      "v_accvgpr_write_b32 a183, v119\n"
      "v_accvgpr_read_b32 v118, a184\n"
      "v_accvgpr_read_b32 v119, a185\n"
      "v_pk_mul_f32 v[118:119], v[118:119], v[114:115] op_sel:[0,0] op_sel_hi:[1,0]\n"
      "v_accvgpr_write_b32 a184, v118\n"
      "v_accvgpr_write_b32 a185, v119\n"
      "v_accvgpr_read_b32 v118, a186\n"
      "v_accvgpr_read_b32 v119, a187\n"
      "v_pk_mul_f32 v[118:119], v[118:119], v[114:115] op_sel:[0,0] op_sel_hi:[1,0]\n"
      "v_accvgpr_write_b32 a186, v118\n"
      "v_accvgpr_write_b32 a187, v119\n"
      "v_accvgpr_read_b32 v118, a188\n"
      "v_accvgpr_read_b32 v119, a189\n"
      "v_pk_mul_f32 v[118:119], v[118:119], v[114:115] op_sel:[0,1] op_sel_hi:[1,1]\n"
      "v_accvgpr_write_b32 a188, v118\n"
      "v_accvgpr_write_b32 a189, v119\n"
      "v_accvgpr_read_b32 v118, a190\n"
      "v_accvgpr_read_b32 v119, a191\n"
      "v_pk_mul_f32 v[118:119], v[118:119], v[114:115] op_sel:[0,1] op_sel_hi:[1,1]\n"
      "v_accvgpr_write_b32 a190, v118\n"
      "v_accvgpr_write_b32 a191, v119\n"
      "v_mov_b32 v118, v112\n"
      "s_nop 1\n"
      "v_mov_b32_dpp v118, v113 quad_perm:[0,1,2,3] row_mask:0x2 bank_mask:0xf\n"
      "s_nop 1\n"
      "v_mov_b32_dpp v118, v114 quad_perm:[0,1,2,3] row_mask:0x4 bank_mask:0xf\n"
      "s_nop 1\n"
      "v_mov_b32_dpp v118, v115 quad_perm:[0,1,2,3] row_mask:0x8 bank_mask:0xf\n"
      "v_accvgpr_read_b32 v119, a192\n"
      "v_mul_f32 v118, v118, v119\n"
      "v_accvgpr_write_b32 a192, v118\n"
      "v_lshlrev_b32 v118, 16, v208\n"
      "v_and_b32 v119, 0xffff0000, v208\n"
      "v_pk_mul_f32 v[118:119], v[118:119], v[112:113] op_sel:[0,0] op_sel_hi:[1,0]\n"
      "v_cvt_pk_bf16_f32 v208, v118, v119\n"
      "v_lshlrev_b32 v118, 16, v209\n"
      "v_and_b32 v119, 0xffff0000, v209\n"
      "v_pk_mul_f32 v[118:119], v[118:119], v[112:113] op_sel:[0,0] op_sel_hi:[1,0]\n"
      "v_cvt_pk_bf16_f32 v209, v118, v119\n"
      "v_lshlrev_b32 v118, 16, v210\n"
      "v_and_b32 v119, 0xffff0000, v210\n"
      "v_pk_mul_f32 v[118:119], v[118:119], v[112:113] op_sel:[0,0] op_sel_hi:[1,0]\n"
      "v_cvt_pk_bf16_f32 v210, v118, v119\n"
      "v_lshlrev_b32 v118, 16, v211\n"
      "v_and_b32 v119, 0xffff0000, v211\n"
      "v_pk_mul_f32 v[118:119], v[118:119], v[112:113] op_sel:[0,0] op_sel_hi:[1,0]\n"
      "v_cvt_pk_bf16_f32 v211, v118, v119\n"
      "v_lshlrev_b32 v118, 16, v212\n"
      "v_and_b32 v119, 0xffff0000, v212\n"
      "v_pk_mul_f32 v[118:119], v[118:119], v[112:113] op_sel:[0,1] op_sel_hi:[1,1]\n"
      "v_cvt_pk_bf16_f32 v212, v118, v119\n"
      "v_lshlrev_b32 v118, 16, v213\n"
      "v_and_b32 v119, 0xffff0000, v213\n"
      "v_pk_mul_f32 v[118:119], v[118:119], v[112:113] op_sel:[0,1] op_sel_hi:[1,1]\n"
      "v_cvt_pk_bf16_f32 v213, v118, v119\n"
      "v_lshlrev_b32 v118, 16, v214\n"
      "v_and_b32 v119, 0xffff0000, v214\n"
      "v_pk_mul_f32 v[118:119], v[118:119], v[112:113] op_sel:[0,1] op_sel_hi:[1,1]\n"
      "v_cvt_pk_bf16_f32 v214, v118, v119\n"
      "v_lshlrev_b32 v118, 16, v215\n"
      "v_and_b32 v119, 0xffff0000, v215\n"
      "v_pk_mul_f32 v[118:119], v[118:119], v[112:113] op_sel:[0,1] op_sel_hi:[1,1]\n"
      "v_cvt_pk_bf16_f32 v215, v118, v119\n"
      "v_lshlrev_b32 v118, 16, v216\n"
      "v_and_b32 v119, 0xffff0000, v216\n"
      "v_pk_mul_f32 v[118:119], v[118:119], v[114:115] op_sel:[0,0] op_sel_hi:[1,0]\n"
      "v_cvt_pk_bf16_f32 v216, v118, v119\n"
      "v_lshlrev_b32 v118, 16, v217\n"
      "v_and_b32 v119, 0xffff0000, v217\n"
      "v_pk_mul_f32 v[118:119], v[118:119], v[114:115] op_sel:[0,0] op_sel_hi:[1,0]\n"
      "v_cvt_pk_bf16_f32 v217, v118, v119\n"
      "v_lshlrev_b32 v118, 16, v218\n"
      "v_and_b32 v119, 0xffff0000, v218\n"
      "v_pk_mul_f32 v[118:119], v[118:119], v[114:115] op_sel:[0,0] op_sel_hi:[1,0]\n"
      "v_cvt_pk_bf16_f32 v218, v118, v119\n"
      "v_lshlrev_b32 v118, 16, v219\n"
      "v_and_b32 v119, 0xffff0000, v219\n"
      "v_pk_mul_f32 v[118:119], v[118:119], v[114:115] op_sel:[0,0] op_sel_hi:[1,0]\n"
      "v_cvt_pk_bf16_f32 v219, v118, v119\n"
      "v_lshlrev_b32 v118, 16, v220\n"
      "v_and_b32 v119, 0xffff0000, v220\n"
      "v_pk_mul_f32 v[118:119], v[118:119], v[114:115] op_sel:[0,1] op_sel_hi:[1,1]\n"
      "v_cvt_pk_bf16_f32 v220, v118, v119\n"
      "v_lshlrev_b32 v118, 16, v221\n"
      "v_and_b32 v119, 0xffff0000, v221\n"
      "v_pk_mul_f32 v[118:119], v[118:119], v[114:115] op_sel:[0,1] op_sel_hi:[1,1]\n"
      "v_cvt_pk_bf16_f32 v221, v118, v119\n"
      "v_lshlrev_b32 v118, 16, v222\n"
      "v_and_b32 v119, 0xffff0000, v222\n"
      "v_pk_mul_f32 v[118:119], v[118:119], v[114:115] op_sel:[0,1] op_sel_hi:[1,1]\n"
      "v_cvt_pk_bf16_f32 v222, v118, v119\n"
      "v_lshlrev_b32 v118, 16, v223\n"
      "v_and_b32 v119, 0xffff0000, v223\n"
      "v_pk_mul_f32 v[118:119], v[118:119], v[114:115] op_sel:[0,1] op_sel_hi:[1,1]\n"
      "v_cvt_pk_bf16_f32 v223, v118, v119\n"
      "v_lshlrev_b32 v118, 16, v192\n"
      "v_and_b32 v119, 0xffff0000, v192\n"
      "v_pk_mul_f32 v[118:119], v[118:119], v[112:113] op_sel:[0,0] op_sel_hi:[1,0]\n"
      "v_cvt_pk_bf16_f32 v192, v118, v119\n"
      "v_lshlrev_b32 v118, 16, v193\n"
      "v_and_b32 v119, 0xffff0000, v193\n"
      "v_pk_mul_f32 v[118:119], v[118:119], v[112:113] op_sel:[0,0] op_sel_hi:[1,0]\n"
      "v_cvt_pk_bf16_f32 v193, v118, v119\n"
      "v_lshlrev_b32 v118, 16, v194\n"
      "v_and_b32 v119, 0xffff0000, v194\n"
      "v_pk_mul_f32 v[118:119], v[118:119], v[112:113] op_sel:[0,0] op_sel_hi:[1,0]\n"
      "v_cvt_pk_bf16_f32 v194, v118, v119\n"
      "v_lshlrev_b32 v118, 16, v195\n"
      "v_and_b32 v119, 0xffff0000, v195\n"
      "v_pk_mul_f32 v[118:119], v[118:119], v[112:113] op_sel:[0,0] op_sel_hi:[1,0]\n"
      "v_cvt_pk_bf16_f32 v195, v118, v119\n"
      "v_lshlrev_b32 v118, 16, v196\n"
      "v_and_b32 v119, 0xffff0000, v196\n"
      "v_pk_mul_f32 v[118:119], v[118:119], v[112:113] op_sel:[0,1] op_sel_hi:[1,1]\n"
      "v_cvt_pk_bf16_f32 v196, v118, v119\n"
      "v_lshlrev_b32 v118, 16, v197\n"
      "v_and_b32 v119, 0xffff0000, v197\n"
      "v_pk_mul_f32 v[118:119], v[118:119], v[112:113] op_sel:[0,1] op_sel_hi:[1,1]\n"
      "v_cvt_pk_bf16_f32 v197, v118, v119\n"
      "v_lshlrev_b32 v118, 16, v198\n"
      "v_and_b32 v119, 0xffff0000, v198\n"
      "v_pk_mul_f32 v[118:119], v[118:119], v[112:113] op_sel:[0,1] op_sel_hi:[1,1]\n"
      "v_cvt_pk_bf16_f32 v198, v118, v119\n"
      "v_lshlrev_b32 v118, 16, v199\n"
      "v_and_b32 v119, 0xffff0000, v199\n"
      "v_pk_mul_f32 v[118:119], v[118:119], v[112:113] op_sel:[0,1] op_sel_hi:[1,1]\n"
      "v_cvt_pk_bf16_f32 v199, v118, v119\n"
      "v_lshlrev_b32 v118, 16, v200\n"
      "v_and_b32 v119, 0xffff0000, v200\n"
      "v_pk_mul_f32 v[118:119], v[118:119], v[114:115] op_sel:[0,0] op_sel_hi:[1,0]\n"
      "v_cvt_pk_bf16_f32 v200, v118, v119\n"
      "v_lshlrev_b32 v118, 16, v201\n"
      "v_and_b32 v119, 0xffff0000, v201\n"
      "v_pk_mul_f32 v[118:119], v[118:119], v[114:115] op_sel:[0,0] op_sel_hi:[1,0]\n"
      "v_cvt_pk_bf16_f32 v201, v118, v119\n"
      "v_lshlrev_b32 v118, 16, v202\n"
      "v_and_b32 v119, 0xffff0000, v202\n"
      "v_pk_mul_f32 v[118:119], v[118:119], v[114:115] op_sel:[0,0] op_sel_hi:[1,0]\n"
      "v_cvt_pk_bf16_f32 v202, v118, v119\n"
      "v_lshlrev_b32 v118, 16, v203\n"
      "v_and_b32 v119, 0xffff0000, v203\n"
      "v_pk_mul_f32 v[118:119], v[118:119], v[114:115] op_sel:[0,0] op_sel_hi:[1,0]\n"
      "v_cvt_pk_bf16_f32 v203, v118, v119\n"
      "v_lshlrev_b32 v118, 16, v204\n"
      "v_and_b32 v119, 0xffff0000, v204\n"
      "v_pk_mul_f32 v[118:119], v[118:119], v[114:115] op_sel:[0,1] op_sel_hi:[1,1]\n"
      "v_cvt_pk_bf16_f32 v204, v118, v119\n"
      "v_lshlrev_b32 v118, 16, v205\n"
      "v_and_b32 v119, 0xffff0000, v205\n"
      "v_pk_mul_f32 v[118:119], v[118:119], v[114:115] op_sel:[0,1] op_sel_hi:[1,1]\n"
      "v_cvt_pk_bf16_f32 v205, v118, v119\n"
      "v_lshlrev_b32 v118, 16, v206\n"
      "v_and_b32 v119, 0xffff0000, v206\n"
      "v_pk_mul_f32 v[118:119], v[118:119], v[114:115] op_sel:[0,1] op_sel_hi:[1,1]\n"
      "v_cvt_pk_bf16_f32 v206, v118, v119\n"
      "v_lshlrev_b32 v118, 16, v207\n"
      "v_and_b32 v119, 0xffff0000, v207\n"
      "v_pk_mul_f32 v[118:119], v[118:119], v[114:115] op_sel:[0,1] op_sel_hi:[1,1]\n"
      "v_cvt_pk_bf16_f32 v207, v118, v119\n"
      "v_lshlrev_b32 v118, 16, v0\n"
      "v_and_b32 v119, 0xffff0000, v0\n"
      "v_pk_mul_f32 v[118:119], v[118:119], v[112:113] op_sel:[0,0] op_sel_hi:[1,0]\n"
      "v_cvt_pk_bf16_f32 v0, v118, v119\n"
      "v_lshlrev_b32 v118, 16, v1\n"
      "v_and_b32 v119, 0xffff0000, v1\n"
      "v_pk_mul_f32 v[118:119], v[118:119], v[112:113] op_sel:[0,0] op_sel_hi:[1,0]\n"
      "v_cvt_pk_bf16_f32 v1, v118, v119\n"
      "v_lshlrev_b32 v118, 16, v2\n"
      "v_and_b32 v119, 0xffff0000, v2\n"
      "v_pk_mul_f32 v[118:119], v[118:119], v[112:113] op_sel:[0,0] op_sel_hi:[1,0]\n"
      "v_cvt_pk_bf16_f32 v2, v118, v119\n"
      "v_lshlrev_b32 v118, 16, v3\n"
      "v_and_b32 v119, 0xffff0000, v3\n"
      "v_pk_mul_f32 v[118:119], v[118:119], v[112:113] op_sel:[0,0] op_sel_hi:[1,0]\n"
      "v_cvt_pk_bf16_f32 v3, v118, v119\n"
      "v_lshlrev_b32 v118, 16, v4\n"
      "v_and_b32 v119, 0xffff0000, v4\n"
      "v_pk_mul_f32 v[118:119], v[118:119], v[112:113] op_sel:[0,1] op_sel_hi:[1,1]\n"
      "v_cvt_pk_bf16_f32 v4, v118, v119\n"
      "v_lshlrev_b32 v118, 16, v5\n"
      "v_and_b32 v119, 0xffff0000, v5\n"
      "v_pk_mul_f32 v[118:119], v[118:119], v[112:113] op_sel:[0,1] op_sel_hi:[1,1]\n"
      "v_cvt_pk_bf16_f32 v5, v118, v119\n"
      "v_lshlrev_b32 v118, 16, v6\n"
      "v_and_b32 v119, 0xffff0000, v6\n"
      "v_pk_mul_f32 v[118:119], v[118:119], v[112:113] op_sel:[0,1] op_sel_hi:[1,1]\n"
      "v_cvt_pk_bf16_f32 v6, v118, v119\n"
      "v_lshlrev_b32 v118, 16, v7\n"
      "v_and_b32 v119, 0xffff0000, v7\n"
      "v_pk_mul_f32 v[118:119], v[118:119], v[112:113] op_sel:[0,1] op_sel_hi:[1,1]\n"
      "v_cvt_pk_bf16_f32 v7, v118, v119\n"
      "v_lshlrev_b32 v118, 16, v8\n"
      "v_and_b32 v119, 0xffff0000, v8\n"
      "v_pk_mul_f32 v[118:119], v[118:119], v[114:115] op_sel:[0,0] op_sel_hi:[1,0]\n"
      "v_cvt_pk_bf16_f32 v8, v118, v119\n"
      "v_lshlrev_b32 v118, 16, v9\n"
      "v_and_b32 v119, 0xffff0000, v9\n"
      "v_pk_mul_f32 v[118:119], v[118:119], v[114:115] op_sel:[0,0] op_sel_hi:[1,0]\n"
      "v_cvt_pk_bf16_f32 v9, v118, v119\n"
      "v_lshlrev_b32 v118, 16, v10\n"
      "v_and_b32 v119, 0xffff0000, v10\n"
      "v_pk_mul_f32 v[118:119], v[118:119], v[114:115] op_sel:[0,0] op_sel_hi:[1,0]\n"
      "v_cvt_pk_bf16_f32 v10, v118, v119\n"
      "v_lshlrev_b32 v118, 16, v11\n"
      "v_and_b32 v119, 0xffff0000, v11\n"
      "v_pk_mul_f32 v[118:119], v[118:119], v[114:115] op_sel:[0,0] op_sel_hi:[1,0]\n"
      "v_cvt_pk_bf16_f32 v11, v118, v119\n"
      "v_lshlrev_b32 v118, 16, v12\n"
      "v_and_b32 v119, 0xffff0000, v12\n"
      "v_pk_mul_f32 v[118:119], v[118:119], v[114:115] op_sel:[0,1] op_sel_hi:[1,1]\n"
      "v_cvt_pk_bf16_f32 v12, v118, v119\n"
      "v_lshlrev_b32 v118, 16, v13\n"
      "v_and_b32 v119, 0xffff0000, v13\n"
      "v_pk_mul_f32 v[118:119], v[118:119], v[114:115] op_sel:[0,1] op_sel_hi:[1,1]\n"
      "v_cvt_pk_bf16_f32 v13, v118, v119\n"
      "v_lshlrev_b32 v118, 16, v14\n"
      "v_and_b32 v119, 0xffff0000, v14\n"
      "v_pk_mul_f32 v[118:119], v[118:119], v[114:115] op_sel:[0,1] op_sel_hi:[1,1]\n"
      "v_cvt_pk_bf16_f32 v14, v118, v119\n"
      "v_lshlrev_b32 v118, 16, v15\n"
      "v_and_b32 v119, 0xffff0000, v15\n"
      "v_pk_mul_f32 v[118:119], v[118:119], v[114:115] op_sel:[0,1] op_sel_hi:[1,1]\n"
      "v_cvt_pk_bf16_f32 v15, v118, v119\n"
      "v_lshlrev_b32 v118, 16, v16\n"
      "v_and_b32 v119, 0xffff0000, v16\n"
      "v_pk_mul_f32 v[118:119], v[118:119], v[112:113] op_sel:[0,0] op_sel_hi:[1,0]\n"
      "v_cvt_pk_bf16_f32 v16, v118, v119\n"
      "v_lshlrev_b32 v118, 16, v17\n"
      "v_and_b32 v119, 0xffff0000, v17\n"
      "v_pk_mul_f32 v[118:119], v[118:119], v[112:113] op_sel:[0,0] op_sel_hi:[1,0]\n"
      "v_cvt_pk_bf16_f32 v17, v118, v119\n"
      "v_lshlrev_b32 v118, 16, v18\n"
      "v_and_b32 v119, 0xffff0000, v18\n"
      "v_pk_mul_f32 v[118:119], v[118:119], v[112:113] op_sel:[0,0] op_sel_hi:[1,0]\n"
      "v_cvt_pk_bf16_f32 v18, v118, v119\n"
      "v_lshlrev_b32 v118, 16, v19\n"
      "v_and_b32 v119, 0xffff0000, v19\n"
      "v_pk_mul_f32 v[118:119], v[118:119], v[112:113] op_sel:[0,0] op_sel_hi:[1,0]\n"
      "v_cvt_pk_bf16_f32 v19, v118, v119\n"
      "v_lshlrev_b32 v118, 16, v20\n"
      "v_and_b32 v119, 0xffff0000, v20\n"
      "v_pk_mul_f32 v[118:119], v[118:119], v[112:113] op_sel:[0,1] op_sel_hi:[1,1]\n"
      "v_cvt_pk_bf16_f32 v20, v118, v119\n"
      "v_lshlrev_b32 v118, 16, v21\n"
      "v_and_b32 v119, 0xffff0000, v21\n"
      "v_pk_mul_f32 v[118:119], v[118:119], v[112:113] op_sel:[0,1] op_sel_hi:[1,1]\n"
      "v_cvt_pk_bf16_f32 v21, v118, v119\n"
      "v_lshlrev_b32 v118, 16, v22\n"
      "v_and_b32 v119, 0xffff0000, v22\n"
      "v_pk_mul_f32 v[118:119], v[118:119], v[112:113] op_sel:[0,1] op_sel_hi:[1,1]\n"
      "v_cvt_pk_bf16_f32 v22, v118, v119\n"
      "v_lshlrev_b32 v118, 16, v23\n"
      "v_and_b32 v119, 0xffff0000, v23\n"
      "v_pk_mul_f32 v[118:119], v[118:119], v[112:113] op_sel:[0,1] op_sel_hi:[1,1]\n"
      "v_cvt_pk_bf16_f32 v23, v118, v119\n"
      "v_lshlrev_b32 v118, 16, v24\n"
      "v_and_b32 v119, 0xffff0000, v24\n"
      "v_pk_mul_f32 v[118:119], v[118:119], v[114:115] op_sel:[0,0] op_sel_hi:[1,0]\n"
      "v_cvt_pk_bf16_f32 v24, v118, v119\n"
      "v_lshlrev_b32 v118, 16, v25\n"
      "v_and_b32 v119, 0xffff0000, v25\n"
      "v_pk_mul_f32 v[118:119], v[118:119], v[114:115] op_sel:[0,0] op_sel_hi:[1,0]\n"
      "v_cvt_pk_bf16_f32 v25, v118, v119\n"
      "v_lshlrev_b32 v118, 16, v26\n"
      "v_and_b32 v119, 0xffff0000, v26\n"
      "v_pk_mul_f32 v[118:119], v[118:119], v[114:115] op_sel:[0,0] op_sel_hi:[1,0]\n"
      "v_cvt_pk_bf16_f32 v26, v118, v119\n"
      "v_lshlrev_b32 v118, 16, v27\n"
      "v_and_b32 v119, 0xffff0000, v27\n"
      "v_pk_mul_f32 v[118:119], v[118:119], v[114:115] op_sel:[0,0] op_sel_hi:[1,0]\n"
      "v_cvt_pk_bf16_f32 v27, v118, v119\n"
      "v_lshlrev_b32 v118, 16, v28\n"
      "v_and_b32 v119, 0xffff0000, v28\n"
      "v_pk_mul_f32 v[118:119], v[118:119], v[114:115] op_sel:[0,1] op_sel_hi:[1,1]\n"
      "v_cvt_pk_bf16_f32 v28, v118, v119\n"
      "v_lshlrev_b32 v118, 16, v29\n"
      "v_and_b32 v119, 0xffff0000, v29\n"
      "v_pk_mul_f32 v[118:119], v[118:119], v[114:115] op_sel:[0,1] op_sel_hi:[1,1]\n"
      "v_cvt_pk_bf16_f32 v29, v118, v119\n"
      "v_lshlrev_b32 v118, 16, v30\n"
      "v_and_b32 v119, 0xffff0000, v30\n"
      "v_pk_mul_f32 v[118:119], v[118:119], v[114:115] op_sel:[0,1] op_sel_hi:[1,1]\n"
      "v_cvt_pk_bf16_f32 v30, v118, v119\n"
      "v_lshlrev_b32 v118, 16, v31\n"
      "v_and_b32 v119, 0xffff0000, v31\n"
      "v_pk_mul_f32 v[118:119], v[118:119], v[114:115] op_sel:[0,1] op_sel_hi:[1,1]\n"
      "v_cvt_pk_bf16_f32 v31, v118, v119\n"
      "v_lshlrev_b32 v118, 16, v96\n"
      "v_and_b32 v119, 0xffff0000, v96\n"
      "v_pk_mul_f32 v[118:119], v[118:119], v[112:113] op_sel:[0,0] op_sel_hi:[1,0]\n"
      "v_cvt_pk_bf16_f32 v96, v118, v119\n"
      "v_lshlrev_b32 v118, 16, v97\n"
      "v_and_b32 v119, 0xffff0000, v97\n"
      "v_pk_mul_f32 v[118:119], v[118:119], v[112:113] op_sel:[0,0] op_sel_hi:[1,0]\n"
      "v_cvt_pk_bf16_f32 v97, v118, v119\n"
      "v_lshlrev_b32 v118, 16, v98\n"
      "v_and_b32 v119, 0xffff0000, v98\n"
      "v_pk_mul_f32 v[118:119], v[118:119], v[112:113] op_sel:[0,0] op_sel_hi:[1,0]\n"
      "v_cvt_pk_bf16_f32 v98, v118, v119\n"
      "v_lshlrev_b32 v118, 16, v99\n"
      "v_and_b32 v119, 0xffff0000, v99\n"
      "v_pk_mul_f32 v[118:119], v[118:119], v[112:113] op_sel:[0,0] op_sel_hi:[1,0]\n"
      "v_cvt_pk_bf16_f32 v99, v118, v119\n"
      "v_lshlrev_b32 v118, 16, v100\n"
      "v_and_b32 v119, 0xffff0000, v100\n"
      "v_pk_mul_f32 v[118:119], v[118:119], v[112:113] op_sel:[0,1] op_sel_hi:[1,1]\n"
      "v_cvt_pk_bf16_f32 v100, v118, v119\n"
      "v_lshlrev_b32 v118, 16, v101\n"
      "v_and_b32 v119, 0xffff0000, v101\n"
      "v_pk_mul_f32 v[118:119], v[118:119], v[112:113] op_sel:[0,1] op_sel_hi:[1,1]\n"
      "v_cvt_pk_bf16_f32 v101, v118, v119\n"
      "v_lshlrev_b32 v118, 16, v102\n"
      "v_and_b32 v119, 0xffff0000, v102\n"
      "v_pk_mul_f32 v[118:119], v[118:119], v[112:113] op_sel:[0,1] op_sel_hi:[1,1]\n"
      "v_cvt_pk_bf16_f32 v102, v118, v119\n"
      "v_lshlrev_b32 v118, 16, v103\n"
      "v_and_b32 v119, 0xffff0000, v103\n"
      "v_pk_mul_f32 v[118:119], v[118:119], v[112:113] op_sel:[0,1] op_sel_hi:[1,1]\n"
      "v_cvt_pk_bf16_f32 v103, v118, v119\n"
      "v_lshlrev_b32 v118, 16, v104\n"
      "v_and_b32 v119, 0xffff0000, v104\n"
      "v_pk_mul_f32 v[118:119], v[118:119], v[114:115] op_sel:[0,0] op_sel_hi:[1,0]\n"
      "v_cvt_pk_bf16_f32 v104, v118, v119\n"
      "v_lshlrev_b32 v118, 16, v105\n"
      "v_and_b32 v119, 0xffff0000, v105\n"
      "v_pk_mul_f32 v[118:119], v[118:119], v[114:115] op_sel:[0,0] op_sel_hi:[1,0]\n"
      "v_cvt_pk_bf16_f32 v105, v118, v119\n"
      "v_lshlrev_b32 v118, 16, v106\n"
      "v_and_b32 v119, 0xffff0000, v106\n"
      "v_pk_mul_f32 v[118:119], v[118:119], v[114:115] op_sel:[0,0] op_sel_hi:[1,0]\n"
      "v_cvt_pk_bf16_f32 v106, v118, v119\n"
      "v_lshlrev_b32 v118, 16, v107\n"
      "v_and_b32 v119, 0xffff0000, v107\n"
      "v_pk_mul_f32 v[118:119], v[118:119], v[114:115] op_sel:[0,0] op_sel_hi:[1,0]\n"
      "v_cvt_pk_bf16_f32 v107, v118, v119\n"
      "v_lshlrev_b32 v118, 16, v108\n"
      "v_and_b32 v119, 0xffff0000, v108\n"
      "v_pk_mul_f32 v[118:119], v[118:119], v[114:115] op_sel:[0,1] op_sel_hi:[1,1]\n"
      "v_cvt_pk_bf16_f32 v108, v118, v119\n"
      "v_lshlrev_b32 v118, 16, v109\n"
      "v_and_b32 v119, 0xffff0000, v109\n"
      "v_pk_mul_f32 v[118:119], v[118:119], v[114:115] op_sel:[0,1] op_sel_hi:[1,1]\n"
      "v_cvt_pk_bf16_f32 v109, v118, v119\n"
      "v_lshlrev_b32 v118, 16, v110\n"
      "v_and_b32 v119, 0xffff0000, v110\n"
      "v_pk_mul_f32 v[118:119], v[118:119], v[114:115] op_sel:[0,1] op_sel_hi:[1,1]\n"
      "v_cvt_pk_bf16_f32 v110, v118, v119\n"
      "v_lshlrev_b32 v118, 16, v111\n"
      "v_and_b32 v119, 0xffff0000, v111\n"
      "v_pk_mul_f32 v[118:119], v[118:119], v[114:115] op_sel:[0,1] op_sel_hi:[1,1]\n"
      "v_cvt_pk_bf16_f32 v111, v118, v119\n"
      "s_setpc_b64 s[84:85]\n"
      "MIS_%=:\n"
      "s_waitcnt lgkmcnt(0)\n"
      "v_sub_f32 v148, v153, v117\n"
      "v_min_f32 v149, 0, -v148\n"
      "v_min_f32 v148, 0, v148\n"
      "v_exp_f32 v149, v149\n"
      "v_exp_f32 v148, v148\n"
      "v_min_f32 v117, v117, v153\n"
      "v_mov_b32 v112, v148\n"
      "v_mov_b32 v228, v149\n"
      "v_mov_b32 v114, v112\n"
      "v_mov_b32 v230, v228\n"
      "s_nop 0\n"
      "v_permlane32_swap_b32 v112, v114\n"
      "v_permlane32_swap_b32 v228, v230\n"
      "v_mov_b32 v113, v112\n"
      "v_mov_b32 v229, v228\n"
      "v_mov_b32 v115, v114\n"
      "v_mov_b32 v231, v230\n"
      "v_permlane16_swap_b32 v112, v113\n"
      "v_permlane16_swap_b32 v228, v229\n"
      "v_permlane16_swap_b32 v114, v115\n"
      "v_permlane16_swap_b32 v230, v231\n"
      "s_call_b64 s[84:85], SCALE_%=\n"
      "v_pk_mul_f32 v[160:161], v[160:161], v[228:229] op_sel:[0,0] op_sel_hi:[1,0]\n"
      "v_pk_mul_f32 v[162:163], v[162:163], v[228:229] op_sel:[0,0] op_sel_hi:[1,0]\n"
      "v_pk_mul_f32 v[164:165], v[164:165], v[228:229] op_sel:[0,1] op_sel_hi:[1,1]\n"
      "v_pk_mul_f32 v[166:167], v[166:167], v[228:229] op_sel:[0,1] op_sel_hi:[1,1]\n"
      "v_pk_mul_f32 v[168:169], v[168:169], v[230:231] op_sel:[0,0] op_sel_hi:[1,0]\n"
      "v_pk_mul_f32 v[170:171], v[170:171], v[230:231] op_sel:[0,0] op_sel_hi:[1,0]\n"
      "v_pk_mul_f32 v[172:173], v[172:173], v[230:231] op_sel:[0,1] op_sel_hi:[1,1]\n"
      "v_pk_mul_f32 v[174:175], v[174:175], v[230:231] op_sel:[0,1] op_sel_hi:[1,1]\n"
      "v_pk_mul_f32 v[176:177], v[176:177], v[228:229] op_sel:[0,0] op_sel_hi:[1,0]\n"
      "v_pk_mul_f32 v[178:179], v[178:179], v[228:229] op_sel:[0,0] op_sel_hi:[1,0]\n"
      "v_pk_mul_f32 v[180:181], v[180:181], v[228:229] op_sel:[0,1] op_sel_hi:[1,1]\n"
      "v_pk_mul_f32 v[182:183], v[182:183], v[228:229] op_sel:[0,1] op_sel_hi:[1,1]\n"
      "v_pk_mul_f32 v[184:185], v[184:185], v[230:231] op_sel:[0,0] op_sel_hi:[1,0]\n"
      "v_pk_mul_f32 v[186:187], v[186:187], v[230:231] op_sel:[0,0] op_sel_hi:[1,0]\n"
      "v_pk_mul_f32 v[188:189], v[188:189], v[230:231] op_sel:[0,1] op_sel_hi:[1,1]\n"
      "v_pk_mul_f32 v[190:191], v[190:191], v[230:231] op_sel:[0,1] op_sel_hi:[1,1]\n"
      "v_mov_b32 v112, v228\n"
      "v_mov_b32 v113, v229\n"
      "v_mov_b32 v114, v230\n"
      "v_mov_b32 v115, v231\n"
      "v_accvgpr_read_b32 v128, a64\n"
      "v_accvgpr_read_b32 v129, a65\n"
      "v_accvgpr_read_b32 v130, a66\n"
      "v_accvgpr_read_b32 v131, a67\n"
      "v_accvgpr_read_b32 v132, a68\n"
      "v_accvgpr_read_b32 v133, a69\n"
      "v_accvgpr_read_b32 v134, a70\n"
      "v_accvgpr_read_b32 v135, a71\n"
      "v_accvgpr_read_b32 v136, a72\n"
      "v_accvgpr_read_b32 v137, a73\n"
      "v_accvgpr_read_b32 v138, a74\n"
      "v_accvgpr_read_b32 v139, a75\n"
      "v_accvgpr_read_b32 v140, a76\n"
      "v_accvgpr_read_b32 v141, a77\n"
      "v_accvgpr_read_b32 v142, a78\n"
      "v_accvgpr_read_b32 v143, a79\n"
      "v_lshlrev_b32 v118, 16, v128\n"
      "v_and_b32 v119, 0xffff0000, v128\n"
      "v_pk_mul_f32 v[118:119], v[118:119], v[112:113] op_sel:[0,0] op_sel_hi:[1,0]\n"
      "v_cvt_pk_bf16_f32 v128, v118, v119\n"
      "v_lshlrev_b32 v118, 16, v129\n"
      "v_and_b32 v119, 0xffff0000, v129\n"
      "v_pk_mul_f32 v[118:119], v[118:119], v[112:113] op_sel:[0,0] op_sel_hi:[1,0]\n"
      "v_cvt_pk_bf16_f32 v129, v118, v119\n"
      "v_lshlrev_b32 v118, 16, v130\n"
      "v_and_b32 v119, 0xffff0000, v130\n"
      "v_pk_mul_f32 v[118:119], v[118:119], v[112:113] op_sel:[0,0] op_sel_hi:[1,0]\n"
      "v_cvt_pk_bf16_f32 v130, v118, v119\n"
      "v_lshlrev_b32 v118, 16, v131\n"
      "v_and_b32 v119, 0xffff0000, v131\n"
      "v_pk_mul_f32 v[118:119], v[118:119], v[112:113] op_sel:[0,0] op_sel_hi:[1,0]\n"
      "v_cvt_pk_bf16_f32 v131, v118, v119\n"
      "v_lshlrev_b32 v118, 16, v132\n"
      "v_and_b32 v119, 0xffff0000, v132\n"
      "v_pk_mul_f32 v[118:119], v[118:119], v[112:113] op_sel:[0,1] op_sel_hi:[1,1]\n"
      "v_cvt_pk_bf16_f32 v132, v118, v119\n"
      "v_lshlrev_b32 v118, 16, v133\n"
      "v_and_b32 v119, 0xffff0000, v133\n"
      "v_pk_mul_f32 v[118:119], v[118:119], v[112:113] op_sel:[0,1] op_sel_hi:[1,1]\n"
      "v_cvt_pk_bf16_f32 v133, v118, v119\n"
      "v_lshlrev_b32 v118, 16, v134\n"
      "v_and_b32 v119, 0xffff0000, v134\n"
      "v_pk_mul_f32 v[118:119], v[118:119], v[112:113] op_sel:[0,1] op_sel_hi:[1,1]\n"
      "v_cvt_pk_bf16_f32 v134, v118, v119\n"
      "v_lshlrev_b32 v118, 16, v135\n"
      "v_and_b32 v119, 0xffff0000, v135\n"
      "v_pk_mul_f32 v[118:119], v[118:119], v[112:113] op_sel:[0,1] op_sel_hi:[1,1]\n"
      "v_cvt_pk_bf16_f32 v135, v118, v119\n"
      "v_lshlrev_b32 v118, 16, v136\n"
      "v_and_b32 v119, 0xffff0000, v136\n"
      "v_pk_mul_f32 v[118:119], v[118:119], v[114:115] op_sel:[0,0] op_sel_hi:[1,0]\n"
      "v_cvt_pk_bf16_f32 v136, v118, v119\n"
      "v_lshlrev_b32 v118, 16, v137\n"
      "v_and_b32 v119, 0xffff0000, v137\n"
      "v_pk_mul_f32 v[118:119], v[118:119], v[114:115] op_sel:[0,0] op_sel_hi:[1,0]\n"
      "v_cvt_pk_bf16_f32 v137, v118, v119\n"
      "v_lshlrev_b32 v118, 16, v138\n"
      "v_and_b32 v119, 0xffff0000, v138\n"
      "v_pk_mul_f32 v[118:119], v[118:119], v[114:115] op_sel:[0,0] op_sel_hi:[1,0]\n"
      "v_cvt_pk_bf16_f32 v138, v118, v119\n"
      "v_lshlrev_b32 v118, 16, v139\n"
      "v_and_b32 v119, 0xffff0000, v139\n"
      "v_pk_mul_f32 v[118:119], v[118:119], v[114:115] op_sel:[0,0] op_sel_hi:[1,0]\n"
      "v_cvt_pk_bf16_f32 v139, v118, v119\n"
      "v_lshlrev_b32 v118, 16, v140\n"
      "v_and_b32 v119, 0xffff0000, v140\n"
      "v_pk_mul_f32 v[118:119], v[118:119], v[114:115] op_sel:[0,1] op_sel_hi:[1,1]\n"
      "v_cvt_pk_bf16_f32 v140, v118, v119\n"
      "v_lshlrev_b32 v118, 16, v141\n"
      "v_and_b32 v119, 0xffff0000, v141\n"
      "v_pk_mul_f32 v[118:119], v[118:119], v[114:115] op_sel:[0,1] op_sel_hi:[1,1]\n"
      "v_cvt_pk_bf16_f32 v141, v118, v119\n"
      "v_lshlrev_b32 v118, 16, v142\n"
      "v_and_b32 v119, 0xffff0000, v142\n"
      "v_pk_mul_f32 v[118:119], v[118:119], v[114:115] op_sel:[0,1] op_sel_hi:[1,1]\n"
      "v_cvt_pk_bf16_f32 v142, v118, v119\n"
      "v_lshlrev_b32 v118, 16, v143\n"
      "v_and_b32 v119, 0xffff0000, v143\n"
      "v_pk_mul_f32 v[118:119], v[118:119], v[114:115] op_sel:[0,1] op_sel_hi:[1,1]\n"
      "v_cvt_pk_bf16_f32 v143, v118, v119\n"
      "v_accvgpr_write_b32 a64, v128\n"
      "v_accvgpr_write_b32 a65, v129\n"
      "v_accvgpr_write_b32 a66, v130\n"
      "v_accvgpr_write_b32 a67, v131\n"
      "v_accvgpr_write_b32 a68, v132\n"
      "v_accvgpr_write_b32 a69, v133\n"
      "v_accvgpr_write_b32 a70, v134\n"
      "v_accvgpr_write_b32 a71, v135\n"
      "v_accvgpr_write_b32 a72, v136\n"
      "v_accvgpr_write_b32 a73, v137\n"
      "v_accvgpr_write_b32 a74, v138\n"
      "v_accvgpr_write_b32 a75, v139\n"
      "v_accvgpr_write_b32 a76, v140\n"
      "v_accvgpr_write_b32 a77, v141\n"
      "v_accvgpr_write_b32 a78, v142\n"
      "v_accvgpr_write_b32 a79, v143\n"
      "s_branch MR_%=\n"
      "FIX_%=:\n"
      "s_nop 7\n"
      "v_sub_f32 v224, v81, v117\n"
      "v_min_f32 v112, 0, -v224\n"
      "v_min_f32 v224, 0, v224\n"
      "v_exp_f32 v112, v112\n"
      "v_exp_f32 v224, v224\n"
      "s_nop 0\n"
      "v_mul_f32 v144, v144, v224\n"
      "v_mul_f32 v80, v80, v112\n"
      "v_mov_b32 v226, v224\n"
      "v_mov_b32 v114, v112\n"
      "s_nop 0\n"
      "v_permlane32_swap_b32 v224, v226\n"
      "v_permlane32_swap_b32 v112, v114\n"
      "v_mov_b32 v225, v224\n"
      "v_mov_b32 v113, v112\n"
      "v_mov_b32 v227, v226\n"
      "v_mov_b32 v115, v114\n"
      "v_permlane16_swap_b32 v224, v225\n"
      "v_permlane16_swap_b32 v112, v113\n"
      "v_permlane16_swap_b32 v226, v227\n"
      "v_permlane16_swap_b32 v114, v115\n"
      "v_pk_mul_f32 v[32:33], v[32:33], v[224:225] op_sel:[0,0] op_sel_hi:[1,0]\n"
      "v_pk_mul_f32 v[34:35], v[34:35], v[224:225] op_sel:[0,0] op_sel_hi:[1,0]\n"
      "v_pk_mul_f32 v[36:37], v[36:37], v[224:225] op_sel:[0,1] op_sel_hi:[1,1]\n"
      "v_pk_mul_f32 v[38:39], v[38:39], v[224:225] op_sel:[0,1] op_sel_hi:[1,1]\n"
      "v_pk_mul_f32 v[40:41], v[40:41], v[226:227] op_sel:[0,0] op_sel_hi:[1,0]\n"
      "v_pk_mul_f32 v[42:43], v[42:43], v[226:227] op_sel:[0,0] op_sel_hi:[1,0]\n"
      "v_pk_mul_f32 v[44:45], v[44:45], v[226:227] op_sel:[0,1] op_sel_hi:[1,1]\n"
      "v_pk_mul_f32 v[46:47], v[46:47], v[226:227] op_sel:[0,1] op_sel_hi:[1,1]\n"
      "v_pk_mul_f32 v[48:49], v[48:49], v[224:225] op_sel:[0,0] op_sel_hi:[1,0]\n"
      "v_pk_mul_f32 v[50:51], v[50:51], v[224:225] op_sel:[0,0] op_sel_hi:[1,0]\n"
      "v_pk_mul_f32 v[52:53], v[52:53], v[224:225] op_sel:[0,1] op_sel_hi:[1,1]\n"
      "v_pk_mul_f32 v[54:55], v[54:55], v[224:225] op_sel:[0,1] op_sel_hi:[1,1]\n"
      "v_pk_mul_f32 v[56:57], v[56:57], v[226:227] op_sel:[0,0] op_sel_hi:[1,0]\n"
      "v_pk_mul_f32 v[58:59], v[58:59], v[226:227] op_sel:[0,0] op_sel_hi:[1,0]\n"
      "v_pk_mul_f32 v[60:61], v[60:61], v[226:227] op_sel:[0,1] op_sel_hi:[1,1]\n"
      "v_pk_mul_f32 v[62:63], v[62:63], v[226:227] op_sel:[0,1] op_sel_hi:[1,1]\n"
      "v_lshlrev_b32 v118, 16, v64\n"
      "v_and_b32 v119, 0xffff0000, v64\n"
      "v_pk_mul_f32 v[118:119], v[118:119], v[112:113] op_sel:[0,0] op_sel_hi:[1,0]\n"
      "v_cvt_pk_bf16_f32 v64, v118, v119\n"
      "v_lshlrev_b32 v118, 16, v65\n"
      "v_and_b32 v119, 0xffff0000, v65\n"
      "v_pk_mul_f32 v[118:119], v[118:119], v[112:113] op_sel:[0,0] op_sel_hi:[1,0]\n"
      "v_cvt_pk_bf16_f32 v65, v118, v119\n"
      "v_lshlrev_b32 v118, 16, v66\n"
      "v_and_b32 v119, 0xffff0000, v66\n"
      "v_pk_mul_f32 v[118:119], v[118:119], v[112:113] op_sel:[0,0] op_sel_hi:[1,0]\n"
      "v_cvt_pk_bf16_f32 v66, v118, v119\n"
      "v_lshlrev_b32 v118, 16, v67\n"
      "v_and_b32 v119, 0xffff0000, v67\n"
      "v_pk_mul_f32 v[118:119], v[118:119], v[112:113] op_sel:[0,0] op_sel_hi:[1,0]\n"
      "v_cvt_pk_bf16_f32 v67, v118, v119\n"
      "v_lshlrev_b32 v118, 16, v68\n"
      "v_and_b32 v119, 0xffff0000, v68\n"
      "v_pk_mul_f32 v[118:119], v[118:119], v[112:113] op_sel:[0,1] op_sel_hi:[1,1]\n"
      "v_cvt_pk_bf16_f32 v68, v118, v119\n"
      "v_lshlrev_b32 v118, 16, v69\n"
      "v_and_b32 v119, 0xffff0000, v69\n"
      "v_pk_mul_f32 v[118:119], v[118:119], v[112:113] op_sel:[0,1] op_sel_hi:[1,1]\n"
      "v_cvt_pk_bf16_f32 v69, v118, v119\n"
      "v_lshlrev_b32 v118, 16, v70\n"
      "v_and_b32 v119, 0xffff0000, v70\n"
      "v_pk_mul_f32 v[118:119], v[118:119], v[112:113] op_sel:[0,1] op_sel_hi:[1,1]\n"
      "v_cvt_pk_bf16_f32 v70, v118, v119\n"
      "v_lshlrev_b32 v118, 16, v71\n"
      "v_and_b32 v119, 0xffff0000, v71\n"
      "v_pk_mul_f32 v[118:119], v[118:119], v[112:113] op_sel:[0,1] op_sel_hi:[1,1]\n"
      "v_cvt_pk_bf16_f32 v71, v118, v119\n"
      "v_lshlrev_b32 v118, 16, v72\n"
      "v_and_b32 v119, 0xffff0000, v72\n"
      "v_pk_mul_f32 v[118:119], v[118:119], v[114:115] op_sel:[0,0] op_sel_hi:[1,0]\n"
      "v_cvt_pk_bf16_f32 v72, v118, v119\n"
      "v_lshlrev_b32 v118, 16, v73\n"
      "v_and_b32 v119, 0xffff0000, v73\n"
      "v_pk_mul_f32 v[118:119], v[118:119], v[114:115] op_sel:[0,0] op_sel_hi:[1,0]\n"
      "v_cvt_pk_bf16_f32 v73, v118, v119\n"
      "v_lshlrev_b32 v118, 16, v74\n"
      "v_and_b32 v119, 0xffff0000, v74\n"
      "v_pk_mul_f32 v[118:119], v[118:119], v[114:115] op_sel:[0,0] op_sel_hi:[1,0]\n"
      "v_cvt_pk_bf16_f32 v74, v118, v119\n"
      "v_lshlrev_b32 v118, 16, v75\n"
      "v_and_b32 v119, 0xffff0000, v75\n"
      "v_pk_mul_f32 v[118:119], v[118:119], v[114:115] op_sel:[0,0] op_sel_hi:[1,0]\n"
      "v_cvt_pk_bf16_f32 v75, v118, v119\n"
      "v_lshlrev_b32 v118, 16, v76\n"
      "v_and_b32 v119, 0xffff0000, v76\n"
      "v_pk_mul_f32 v[118:119], v[118:119], v[114:115] op_sel:[0,1] op_sel_hi:[1,1]\n"
      "v_cvt_pk_bf16_f32 v76, v118, v119\n"
      "v_lshlrev_b32 v118, 16, v77\n"
      "v_and_b32 v119, 0xffff0000, v77\n"
      "v_pk_mul_f32 v[118:119], v[118:119], v[114:115] op_sel:[0,1] op_sel_hi:[1,1]\n"
      "v_cvt_pk_bf16_f32 v77, v118, v119\n"
      "v_lshlrev_b32 v118, 16, v78\n"
      "v_and_b32 v119, 0xffff0000, v78\n"
      "v_pk_mul_f32 v[118:119], v[118:119], v[114:115] op_sel:[0,1] op_sel_hi:[1,1]\n"
      "v_cvt_pk_bf16_f32 v78, v118, v119\n"
      "v_lshlrev_b32 v118, 16, v79\n"
      "v_and_b32 v119, 0xffff0000, v79\n"
      "v_pk_mul_f32 v[118:119], v[118:119], v[114:115] op_sel:[0,1] op_sel_hi:[1,1]\n"
      "v_cvt_pk_bf16_f32 v79, v118, v119\n"
      "v_add_f32 v224, v144, v80\n"
      "v_rcp_f32 v224, v224\n"
      "v_mfma_f32_16x16x32_bf16 v[160:163], v[86:89], v[64:67], v[32:35]\n"
      "v_mfma_f32_16x16x32_bf16 v[176:179], v[84:87], v[64:67], v[48:51]\n"
      "v_mfma_f32_16x16x32_bf16 v[164:167], v[86:89], v[68:71], v[36:39]\n"
      "v_mfma_f32_16x16x32_bf16 v[180:183], v[84:87], v[68:71], v[52:55]\n"
      "v_mfma_f32_16x16x32_bf16 v[168:171], v[86:89], v[72:75], v[40:43]\n"
      "v_mfma_f32_16x16x32_bf16 v[184:187], v[84:87], v[72:75], v[56:59]\n"
      "v_mfma_f32_16x16x32_bf16 v[172:175], v[86:89], v[76:79], v[44:47]\n"
      "v_mfma_f32_16x16x32_bf16 v[188:191], v[84:87], v[76:79], v[60:63]\n"
      "v_mov_b32 v226, v224\n"
      "s_nop 1\n"
      "s_nop 0\n"
      "s_nop 1\n"
      "v_permlane32_swap_b32 v224, v226\n"
      "s_nop 1\n"
      "v_mov_b32 v225, v224\n"
      "s_nop 1\n"
      "v_mov_b32 v227, v226\n"
      "s_nop 1\n"
      "v_permlane16_swap_b32 v224, v225\n"
      "s_nop 1\n"
      "v_permlane16_swap_b32 v226, v227\n"
      "s_nop 1\n"
      "s_branch FR_%=\n"
      ".subsection 0\n"
      : "+{s[4:19]}"(ka), "+{v128}"(pre), "+{s92}"(tch), "+{v[242:249]}"(ad), "+{v[250:251]}"(ra)
      : [qo] "s"(bk * 128 * D_ * 2), [oo] "s"((bk * 128 + rh * 64) * (D_ * 2) + (2 * split + kh) * 64 + 4096),
        [fo] "s"((unsigned)WS_EPOCH + bk * 128 + 64 + split * 8), [xo] "s"((unsigned)(WS_X + split * WS_X1 + (bk * 4 + wid) * XW_BYTES)),
        [xw] "s"(xarea(wid)), [xr] "s"(xarea(wid ^ 2)),
        [so0] "s"(so0), [inc] "s"(inc), [wb] "s"(wbase), [tv] "s"(wbase + STAGE_BYTES + KIMG + split * 8192), [n1] "s"(n1),
        [qoff] "s"(Q_OFF + rh * 16384 - kh * (8192 - 1024)), [toff] "s"(TREE_OFF - kh * (8192 - 1024)),
        [neg] "s"((wid ^ (3 * split)) ? 0xff800000u : 0u),
        [lead] "s"(blk * 4 + wid * 32), [cph] "s"(C2 + (unsigned)((jj & ~3) * 8 + xg * 4) * WIN / 256u),
        [rep] "s"((jj >> 2) == 160 / 32 ? 128 + (blk & 31) * 4 : (blk - 224) * 4)
      : "v0", "v1", "v2", "v3", "v4", "v5", "v6", "v7", "v8", "v9", "v10", "v11", "v12", "v13", "v14", "v15", "v16", "v17", "v18", "v19", "v20", "v21", "v22", "v23", "v24", "v25", "v26", "v27", "v28", "v29", "v30", "v31", "v32", "v33", "v34", "v35", "v36", "v37", "v38", "v39", "v40", "v41", "v42", "v43", "v44", "v45", "v46", "v47", "v48", "v49", "v50", "v51", "v52", "v53", "v54", "v55", "v56", "v57", "v58", "v59", "v60", "v61", "v62", "v63", "v64", "v65", "v66", "v67", "v68", "v69", "v70", "v71", "v72", "v73", "v74", "v75", "v76", "v77", "v78", "v79", "v80", "v81", "v82", "v83", "v84", "v85", "v86", "v87", "v88", "v89", "v90", "v91", "v92", "v93", "v94", "v95", "v96", "v97", "v98", "v99", "v100", "v101", "v102", "v103", "v104", "v105", "v106", "v107", "v108", "v109", "v110", "v111", "v112", "v113", "v114", "v115", "v116", "v117", "v118", "v119", "v120", "v121", "v122", "v123", "v124", "v125", "v126", "v127", "v129", "v130", "v131", "v132", "v133", "v134", "v135", "v136", "v137", "v138", "v139", "v140", "v141", "v142", "v143", "v144", "v145", "v146", "v147", "v148", "v149", "v150", "v151", "v152", "v153", "v154", "v155", "v156", "v157", "v158", "v159", "v160", "v161", "v162", "v163", "v164", "v165", "v166", "v167", "v168", "v169", "v170", "v171", "v172", "v173", "v174", "v175", "v176", "v177", "v178", "v179", "v180", "v181", "v182", "v183", "v184", "v185", "v186", "v187", "v188", "v189", "v190", "v191", "v192", "v193", "v194", "v195", "v196", "v197", "v198", "v199", "v200", "v201", "v202", "v203", "v204", "v205", "v206", "v207", "v208", "v209", "v210", "v211", "v212", "v213", "v214", "v215", "v216", "v217", "v218", "v219", "v220", "v221", "v222", "v223", "v224", "v225", "v226", "v227", "v228", "v229", "v230", "v231", "v232", "v233", "v234", "v235", "v236", "v237", "v238", "v239", "v240", "v241", "a0", "a1", "a2", "a3", "a4", "a5", "a6", "a7", "a8", "a9", "a10", "a11", "a12", "a13", "a14", "a15", "a16", "a17", "a18", "a19", "a20", "a21", "a22", "a23", "a24", "a25", "a26", "a27", "a28", "a29", "a30", "a31", "a32", "a33", "a34", "a35", "a36", "a37", "a38", "a39", "a40", "a41", "a42", "a43", "a44", "a45", "a46", "a47", "a48", "a49", "a50", "a51", "a52", "a53", "a54", "a55", "a56", "a57", "a58", "a59", "a60", "a61", "a62", "a63", "a64", "a65", "a66", "a67", "a68", "a69", "a70", "a71", "a72", "a73", "a74", "a75", "a76", "a77", "a78", "a79", "a80", "a81", "a82", "a83", "a84", "a85", "a86", "a87", "a88", "a89", "a90", "a91", "a92", "a93", "a94", "a95", "a96", "a97", "a98", "a99", "a100", "a101", "a102", "a103", "a104", "a105", "a106", "a107", "a108", "a109", "a110", "a111", "a112", "a113", "a114", "a115", "a116", "a117", "a118", "a119", "a120", "a121", "a122", "a123", "a124", "a125", "a126", "a127", "a128", "a129", "a130", "a131", "a132", "a133", "a134", "a135", "a136", "a137", "a138", "a139", "a140", "a141", "a142", "a143", "a144", "a145", "a146", "a147", "a148", "a149", "a150", "a151", "a152", "a153", "a154", "a155", "a156", "a157", "a158", "a159", "a160", "a161", "a162", "a163", "a164", "a165", "a166", "a167", "a168", "a169", "a170", "a171", "a172", "a173", "a174", "a175", "a176", "a177", "a178", "a179", "a180", "a181", "a182", "a183", "a184", "a185", "a186", "a187", "a188", "a189", "a190", "a191", "a192", "a193", "a194", "a195", "a196", "a197", "a198", "a199", "a200", "a201", "a202", "a203", "a204", "a205", "a206", "a207", "a208", "a209", "a210", "a211", "a212", "a213", "a214", "a215", "a216", "a217", "a218", "a219", "a220", "a221", "a222", "a223", "a224", "a225", "a226", "a227", "a228", "a229", "a230", "a231", "s20", "s21", "s22", "s23", "s24", "s25", "s26", "s27", "s28", "s29", "s30", "s31", "s60", "s61", "s62", "s63", "s64", "s65", "s66", "s67", "s68", "s69", "s70", "s71", "s72", "s73", "s74", "s75", "s76", "s77", "s78", "s79", "s80", "s81", "s82", "s83", "s84", "s85", "s86", "s87", "s88", "s89", "s90", "s91", "s93", "s94", "s95", "s98", "s99", "s100", "s101", "vcc", "scc", "memory");
}

}

extern "C" void kernel_launch(void* const* d_in, const int* in_sizes, int n_in, void* d_out, int out_size,
                              void* d_ws, size_t ws_size, hipStream_t stream) {
  if (n_in != 6 || ws_size < WS_END) return;
  const __hip_bfloat16* q = (const __hip_bfloat16*)d_in[0];
  const __hip_bfloat16* kc = (const __hip_bfloat16*)d_in[1];
  const __hip_bfloat16* vc = (const __hip_bfloat16*)d_in[2];
  const __hip_bfloat16* kt = (const __hip_bfloat16*)d_in[3];
  const __hip_bfloat16* vt = (const __hip_bfloat16*)d_in[4];
  const __hip_bfloat16* tm = (const __hip_bfloat16*)d_in[5];
  hipLaunchKernelGGL(tree_attn_main, dim3(B_ * HKV_ * 2), dim3(256), 0, stream, q, kc, vc, kt, vt, tm,
                     (__hip_bfloat16*)d_out, (char*)d_ws);
}
```

```cpp
#include <hip/hip_runtime.h>
#include <hip/hip_bf16.h>
#include <stdint.h>

#pragma clang diagnostic ignored "-Wint-to-pointer-cast"
#pragma clang diagnostic ignored "-Winline-asm"

namespace {

constexpr int B_ = 16, HQ_ = 32, HKV_ = 8, T_ = 32, C_ = 4096, D_ = 128;
constexpr int TILE = 64;
constexpr int KIMG = TILE * D_ * 2;
constexpr int STAGE_BYTES = 2 * KIMG;
constexpr int TREE_OFF = 4 * STAGE_BYTES;
constexpr int Q_OFF = 3 * STAGE_BYTES;
constexpr int LDS_BYTES = TREE_OFF + 2 * 13312;
constexpr unsigned C2 = 299, WIN = 52;

constexpr size_t WS_EPOCH = 0;
constexpr size_t WS_REF = WS_EPOCH + 128 * 128;
constexpr size_t WS_X = WS_REF + 16384, WS_X1 = 4 << 20;
constexpr size_t XW_BYTES = 4096 + 1024;
constexpr size_t WS_END = 0x680000 + WS_X;
static_assert(WS_X1 + WS_X + 128 * 4 * XW_BYTES <= WS_END, "");

typedef float    f32x4  __attribute__((ext_vector_type(4)));
typedef unsigned long long u64x8 __attribute__((ext_vector_type(8)));
typedef unsigned u32x32 __attribute__((ext_vector_type(32)));
typedef unsigned u32x8  __attribute__((ext_vector_type(8)));
typedef unsigned u32x4  __attribute__((ext_vector_type(4)));
typedef unsigned u32x2  __attribute__((ext_vector_type(2)));
typedef __attribute__((address_space(3))) f32x4 lds_f32x4;
typedef __attribute__((address_space(1))) unsigned long long gu64;

__global__ __launch_bounds__(256, 1)
void tree_attn_main(const __hip_bfloat16*, const __hip_bfloat16*, const __hip_bfloat16*, const __hip_bfloat16*,
                    const __hip_bfloat16*, const __hip_bfloat16*, __hip_bfloat16*, char*) {
  __shared__ __attribute__((aligned(1024))) char smem[LDS_BYTES];
  unsigned blk = blockIdx.x;
  u64x8 ka; u32x32 qq; unsigned pre, tch, voffK; int tid = threadIdx.x;
  asm volatile(
      "s_load_dwordx16 s[4:19], %[kp], 0x0\n"
      "s_getpc_b64 s[84:85]\n"
      "v_lshlrev_b32 v112, 4, %[tid]\n"
      "s_lshl_b32 s69, %[blk], 7\n"
      "s_and_b32 s69, s69, 0x7c00\n"
      "v_lshl_add_u32 v120, %[tid], 2, s69\n"
      "global_load_dword v128, v120, s[84:85]\n"
      "v_readfirstlane_b32 s77, %[tid]\n"
      "v_lshrrev_b32 %[vk], 4, %[tid]\n"
      "s_and_b32 s78, %[blk], 7\n"
      "v_xor_b32 %[vk], %[vk], %[tid]\n"
      "s_lshr_b32 s74, %[blk], 4\n"
      "v_and_b32 %[vk], 7, %[vk]\n"
      "s_lshl4_add_u32 s78, s78, s74\n"
      "v_lshrrev_b32 v120, 3, %[tid]\n"
      "s_lshl_b32 s78, s78, 15\n"
      "v_lshlrev_b32 %[vk], 4, %[vk]\n"
      "s_lshl_b32 s77, s77, 5\n"
      "v_lshl_or_b32 %[vk], v120, 8, %[vk]\n"
      "s_bfm_b32 s66, 1, 22\n"
      "s_bfm_b32 s67, 1, 17\n"
      "v_add_u32 v120, 0x2000, %[vk]\n"
      "s_add_u32 s74, s78, 0x4000\n"
      "s_lshl_b32 s76, s78, 5\n"
      "s_add_u32 m0, s77, 0x20000\n"
      "s_mov_b32 s86, 0x100000\n"
      "s_mov_b32 s87, 0x20000\n"
      "s_lshr_b32 s75, s78, 2\n"
      "s_waitcnt lgkmcnt(0)\n"
      "s_mov_b32 s64, s4\n"
      "s_and_b32 s65, s5, 0xffff\n"
      "buffer_load_dwordx4 v[160:163], %[vk], s[64:67], s78 offen nt\n"
      "buffer_load_dwordx4 v[164:167], %[vk], s[64:67], s78 offen offset:128 nt\n"
      "buffer_load_dwordx4 v[168:171], v120, s[64:67], s78 offen nt\n"
      "buffer_load_dwordx4 v[172:175], v120, s[64:67], s78 offen offset:128 nt\n"
      "s_load_dword s92, s[6:7], s76\n"
      "s_load_dword s92, s[8:9], s76\n"
      "buffer_load_dwordx4 v[176:179], %[vk], s[64:67], s74 offen nt\n"
      "buffer_load_dwordx4 v[180:183], %[vk], s[64:67], s74 offen offset:128 nt\n"
      "buffer_load_dwordx4 v[184:187], v120, s[64:67], s74 offen nt\n"
      "buffer_load_dwordx4 v[188:191], v120, s[64:67], s74 offen offset:128 nt\n"
      "s_mov_b32 s84, s10\n"
      "s_and_b32 s85, s11, 0xffff\n"
      "s_lshr_b32 s76, s78, 18\n"
      "s_lshl_b32 s76, s76, 11\n"
      "s_lshr_b32 s69, s77, 1\n"
      "buffer_load_dwordx4 %[vk], s[84:87], s75 offen nt lds\n"
      "s_add_u32 m0, s77, 131968\n"
      "s_cmp_gt_u32 s77, 2048\n"
      "buffer_load_dwordx4 %[vk], s[84:87], s75 offen offset:128 nt lds\n"
      "s_cbranch_scc1 NOMASK_%=\n"
      "s_add_u32 m0, s69, 140288\n"
      "s_mov_b32 s64, s14\n"
      "s_and_b32 s65, s15, 0xffff\n"
      "s_mov_b32 s66, 0x8000\n"
      "buffer_load_dwordx4 v112, s[64:67], s76 offen lds\n"
      "NOMASK_%=:\n"
      "s_mov_b32 s64, s6\n"
      "s_and_b32 s65, s7, 0xffff\n"
      "s_bfm_b32 s66, 1, 27\n"
      "s_lshl_b32 s76, s78, 5\n"
      "s_bfe_u32 s74, %[blk], 0x10003\n"
      "s_lshl_b32 s74, s74, 15\n"
      "s_add_u32 s76, s76, s74\n"
      "s_bfe_u32 s74, %[blk], 0x10004\n"
      "s_lshl_b32 s74, s74, 14\n"
      "s_add_u32 s76, s76, s74\n"
      "s_add_u32 m0, s77, 0\n"
      "s_nop 0\n"
      "buffer_load_dwordx4 %[vk], s[64:67], s76 offen nt lds\n"
      "s_add_u32 m0, s77, 896\n"
      "s_nop 0\n"
      "buffer_load_dwordx4 %[vk], s[64:67], s76 offen offset:128 nt lds\n"
      "s_add_u32 m0, s77, 8192\n"
      "s_nop 0\n"
      "buffer_load_dwordx4 v120, s[64:67], s76 offen nt lds\n"
      : "=&{s[4:19]}"(ka), "=&{v128}"(pre), "=&{v[160:191]}"(qq), "=&{s92}"(tch), [vk] "=&v"(voffK), [tid] "+v"(tid)
      : [kp] "s"(__builtin_amdgcn_kernarg_segment_ptr()), [blk] "s"(blk)
      : "v112", "v120", "s64", "s65", "s66", "s67", "s69", "s70", "s71", "s74", "s75", "s76", "s77", "s78", "s84", "s85", "s86", "s87", "s96", "s97", "scc", "memory");
  asm volatile("" : "+s"(blk));
  const int lane = tid & 63;
  const int wid = __builtin_amdgcn_readfirstlane(tid >> 6);
  const unsigned wbase = wid * 2048;
  const int rh = wid & 1, kh = wid >> 1;
  const int xg = blk & 7, jj = blk >> 3, bk = xg * 16 + (jj >> 1), split = jj & 1;
  const int c16 = lane & 15, g = lane >> 4;
  const unsigned lds0 = (unsigned)(uintptr_t)smem;

  const unsigned voffV = (unsigned)((8 * wid + (lane >> 3)) * 256 + (((lane & 7) ^ (g << 1)) << 4));
  const unsigned so0 = (unsigned)(bk * C_ + split * 2 * TILE) * (D_ * 2) + (bk & 1) * KIMG, inc = KIMG - 2 * (bk & 1) * KIMG;
#pragma unroll
  for (int j = 0; j < 4; ++j) *(lds_f32x4*)(lds0 + KIMG + 4096 * j + tid * 16) = f32x4{0.f, 0.f, 0.f, 0.f};
#pragma unroll
  for (int j = 0; j < 2; ++j) *(lds_f32x4*)(lds0 + STAGE_BYTES + KIMG + (1 - split) * 8192 + 4096 * j + tid * 16) = f32x4{0.f, 0.f, 0.f, 0.f};
  u32x8 ad;
#pragma unroll
  for (int j = 0; j < 2; ++j)
    ad[j] = lds0 + (unsigned)(2048 * (c16 >> 3) + 128 * (c16 & 7) + (((4 * j + g) ^ (c16 >> 1)) << 4) + kh * 8192);
  {
    const int q4 = c16 >> 2, p = lane & 3;
#pragma unroll
    for (int j = 0; j < 4; ++j)
      ad[2 + j] = lds0 + (unsigned)(2048 * (g >> 1) + 128 * (4 * (g & 1) + q4) + kh * 8192 + KIMG +
                                    (((2 * (j ^ (2 * kh)) + (p >> 1)) ^ ((2 * (g & 1) + (q4 >> 1)) << 1)) << 4) + 8 * (p & 1));
  }
  ad[6] = voffK; ad[7] = voffV + 128 * split;
  u32x2 ra = {(unsigned)(lane < 4 ? WS_REF + lane * 512 : 1 << 26), (unsigned)(WS_REF + (jj & (4 - 1)) * 512 + (lane & 7) * 4 + (wid << 26))};
  auto xarea = [&](int w) { return lds0 + ((0x8d802000u >> (8 * w)) & 255u) * 1024; };
  const unsigned pg = jj >> 2, px = pg - (pg > 160 / 32) + xg * 5;
  const unsigned n1 = 4 + max(px - 6 * (px * 43 >> 8), ((0x70000000u | 6u << 160 / 8) >> 4 * pg) & 7);
  asm volatile(
      "s_memrealtime s[82:83]\n"
      "s_mov_b32 s60, %[so0]\n"
      "s_mov_b32 s62, %[wb]\n"
      "v_add_u32 v120, 0x2000, v248\n"
      "s_and_b32 s80, %[fo], 0xffffff80\n"
      "v_mov_b32 v240, s80\n"
      "s_mov_b32 s22, 0x8000000\n"
      "s_mov_b32 s23, 0x20000\n"
      "s_mov_b32 s20, s6\n"
      "s_and_b32 s21, s7, 0xffff\n"
      "s_nop 0\n"
      "v_mov_b64 v[216:217], 0\n"
      "v_mov_b64 v[218:219], 0\n"
      "s_mov_b32 s86, 0x100000\n"
      "s_mov_b32 s87, 0x20000\n"
      "v_mfma_f32_32x32x16_bf16 v[32:47], v[216:219], v[216:219], 0\n"
      "s_lshr_b32 s75, %[qo], 2\n"
      "s_mov_b32 s84, s12\n"
      "s_and_b32 s85, s13, 0xffff\n"
      "v_mfma_f32_32x32x16_bf16 v[48:63], v[216:219], v[216:219], 0\n"
      "s_nop 0\n"
      "v_xor_b32 v121, 0x80, v249\n"
      "s_sub_u32 s78, 0x10000, %[inc]\n"
      "s_mov_b32 s94, 0x3e0293ee\n"
      "v_mfma_f32_32x32x16_bf16 v[64:79], v[216:219], v[216:219], 0\n"
      "s_mov_b32 s95, 0x3e0293ee\n"
      "s_mov_b32 s74, 0xffff0000\n"
      "s_mov_b32 s76, 0xceabfb8f\n"
      "v_mfma_f32_32x32x16_bf16 v[80:95], v[216:219], v[216:219], 0\n"
      "v_mbcnt_lo_u32_b32 v112, -1, 0\n"
      "s_nop 0\n"
      "v_mbcnt_hi_u32_b32 v112, -1, v112\n"
      "s_add_u32 s69, %[wb], 0x18000\n"
      "v_mfma_f32_32x32x16_bf16 v[192:207], v[216:219], v[216:219], 0\n"
      "v_lshl_add_u32 v226, v112, 4, s69\n"
      "v_lshrrev_b32 v115, 4, v112\n"
      "v_and_b32 v112, 15, v112\n"
      "v_mfma_f32_32x32x16_bf16 a[128:143], v[216:219], v[216:219], 0\n"
      "v_lshlrev_b32 v115, 3, v115\n"
      "v_lshl_add_u32 v115, v112, 6, v115\n"
      "s_add_u32 m0, s62, 9088\n"
      "s_nop 0\n"
      "buffer_load_dwordx4 v120, s[20:23], s60 offen offset:128 nt lds\n"
      "v_add_u32 v115, 140288, v115\n"
      "v_mfma_f32_32x32x16_bf16 a[144:159], v[216:219], v[216:219], 0\n"
      "v_add_u32 v118, %[qoff], v242\n"
      "v_add_u32 v224, %[toff], v242\n"
      "v_add_u32 v119, %[qoff], v243\n"
      "v_mfma_f32_32x32x16_bf16 a[160:175], v[216:219], v[216:219], 0\n"
      "v_add_u32 v225, %[toff], v243\n"
      "v_mfma_f32_32x32x16_bf16 a[176:191], v[216:219], v[216:219], 0\n"
      "s_add_u32 s62, s62, 0x8000\n"
      "s_and_b32 s62, s62, 0x1ffff\n"
      "s_add_u32 s60, s60, %[inc]\n"
      "s_waitcnt vmcnt(13)\n"
      "v_lshlrev_b32 v232, 16, v160\n"
      "v_and_b32 v233, s74, v160\n"
      "v_lshlrev_b32 v234, 16, v161\n"
      "v_and_b32 v235, s74, v161\n"
      "v_lshlrev_b32 v236, 16, v162\n"
      "v_and_b32 v237, s74, v162\n"
      "v_lshlrev_b32 v238, 16, v163\n"
      "v_and_b32 v239, s74, v163\n"
      "v_pk_mul_f32 v[232:233], v[232:233], s[94:95]\n"
      "v_pk_mul_f32 v[234:235], v[234:235], s[94:95]\n"
      "v_pk_mul_f32 v[236:237], v[236:237], s[94:95]\n"
      "v_pk_mul_f32 v[238:239], v[238:239], s[94:95]\n"
      "v_cvt_pk_bf16_f32 v160, v232, v233\n"
      "v_cvt_pk_bf16_f32 v161, v234, v235\n"
      "v_cvt_pk_bf16_f32 v162, v236, v237\n"
      "v_cvt_pk_bf16_f32 v163, v238, v239\n"
      "ds_write_b128 v226, v[160:163] offset:0\n"
      "s_waitcnt vmcnt(12)\n"
      "v_lshlrev_b32 v232, 16, v164\n"
      "v_and_b32 v233, s74, v164\n"
      "v_lshlrev_b32 v234, 16, v165\n"
      "v_and_b32 v235, s74, v165\n"
      "v_lshlrev_b32 v236, 16, v166\n"
      "v_and_b32 v237, s74, v166\n"
      "v_lshlrev_b32 v238, 16, v167\n"
      "v_and_b32 v239, s74, v167\n"
      "v_pk_mul_f32 v[232:233], v[232:233], s[94:95]\n"
      "v_pk_mul_f32 v[234:235], v[234:235], s[94:95]\n"
      "v_pk_mul_f32 v[236:237], v[236:237], s[94:95]\n"
      "v_pk_mul_f32 v[238:239], v[238:239], s[94:95]\n"
      "v_cvt_pk_bf16_f32 v164, v232, v233\n"
      "v_cvt_pk_bf16_f32 v165, v234, v235\n"
      "v_cvt_pk_bf16_f32 v166, v236, v237\n"
      "v_cvt_pk_bf16_f32 v167, v238, v239\n"
      "ds_write_b128 v226, v[164:167] offset:1024\n"
      "s_waitcnt vmcnt(11)\n"
      "v_lshlrev_b32 v232, 16, v168\n"
      "v_and_b32 v233, s74, v168\n"
      "v_lshlrev_b32 v234, 16, v169\n"
      "v_and_b32 v235, s74, v169\n"
      "v_lshlrev_b32 v236, 16, v170\n"
      "v_and_b32 v237, s74, v170\n"
      "v_lshlrev_b32 v238, 16, v171\n"
      "v_and_b32 v239, s74, v171\n"
      "v_pk_mul_f32 v[232:233], v[232:233], s[94:95]\n"
      "v_pk_mul_f32 v[234:235], v[234:235], s[94:95]\n"
      "v_pk_mul_f32 v[236:237], v[236:237], s[94:95]\n"
      "v_pk_mul_f32 v[238:239], v[238:239], s[94:95]\n"
      "v_cvt_pk_bf16_f32 v168, v232, v233\n"
      "v_cvt_pk_bf16_f32 v169, v234, v235\n"
      "v_cvt_pk_bf16_f32 v170, v236, v237\n"
      "v_cvt_pk_bf16_f32 v171, v238, v239\n"
      "ds_write_b128 v226, v[168:171] offset:8192\n"
      "s_add_u32 m0, s62, 0\n"
      "s_nop 0\n"
      "buffer_load_dwordx4 v248, s[20:23], s60 offen nt lds\n"
      "s_waitcnt vmcnt(11)\n"
      "v_lshlrev_b32 v232, 16, v172\n"
      "v_and_b32 v233, s74, v172\n"
      "v_lshlrev_b32 v234, 16, v173\n"
      "v_and_b32 v235, s74, v173\n"
      "v_lshlrev_b32 v236, 16, v174\n"
      "v_and_b32 v237, s74, v174\n"
      "v_lshlrev_b32 v238, 16, v175\n"
      "v_and_b32 v239, s74, v175\n"
      "v_pk_mul_f32 v[232:233], v[232:233], s[94:95]\n"
      "v_pk_mul_f32 v[234:235], v[234:235], s[94:95]\n"
      "v_pk_mul_f32 v[236:237], v[236:237], s[94:95]\n"
      "v_pk_mul_f32 v[238:239], v[238:239], s[94:95]\n"
      "v_cvt_pk_bf16_f32 v172, v232, v233\n"
      "v_cvt_pk_bf16_f32 v173, v234, v235\n"
      "v_cvt_pk_bf16_f32 v174, v236, v237\n"
      "v_cvt_pk_bf16_f32 v175, v238, v239\n"
      "ds_write_b128 v226, v[172:175] offset:9216\n"
      "s_add_u32 m0, s62, 896\n"
      "s_nop 0\n"
      "buffer_load_dwordx4 v248, s[20:23], s60 offen offset:128 nt lds\n"
      "s_waitcnt vmcnt(11)\n"
      "v_lshlrev_b32 v232, 16, v176\n"
      "v_and_b32 v233, s74, v176\n"
      "v_lshlrev_b32 v234, 16, v177\n"
      "v_and_b32 v235, s74, v177\n"
      "v_lshlrev_b32 v236, 16, v178\n"
      "v_and_b32 v237, s74, v178\n"
      "v_lshlrev_b32 v238, 16, v179\n"
      "v_and_b32 v239, s74, v179\n"
      "v_pk_mul_f32 v[232:233], v[232:233], s[94:95]\n"
      "v_pk_mul_f32 v[234:235], v[234:235], s[94:95]\n"
      "v_pk_mul_f32 v[236:237], v[236:237], s[94:95]\n"
      "v_pk_mul_f32 v[238:239], v[238:239], s[94:95]\n"
      "v_cvt_pk_bf16_f32 v176, v232, v233\n"
      "v_cvt_pk_bf16_f32 v177, v234, v235\n"
      "v_cvt_pk_bf16_f32 v178, v236, v237\n"
      "v_cvt_pk_bf16_f32 v179, v238, v239\n"
      "ds_write_b128 v226, v[176:179] offset:16384\n"
      "s_add_u32 m0, s62, 8192\n"
      "s_nop 0\n"
      "buffer_load_dwordx4 v120, s[20:23], s60 offen nt lds\n"
      "s_waitcnt vmcnt(11)\n"
      "v_lshlrev_b32 v232, 16, v180\n"
      "v_and_b32 v233, s74, v180\n"
      "v_lshlrev_b32 v234, 16, v181\n"
      "v_and_b32 v235, s74, v181\n"
      "v_lshlrev_b32 v236, 16, v182\n"
      "v_and_b32 v237, s74, v182\n"
      "v_lshlrev_b32 v238, 16, v183\n"
      "v_and_b32 v239, s74, v183\n"
      "v_pk_mul_f32 v[232:233], v[232:233], s[94:95]\n"
      "v_pk_mul_f32 v[234:235], v[234:235], s[94:95]\n"
      "v_pk_mul_f32 v[236:237], v[236:237], s[94:95]\n"
      "v_pk_mul_f32 v[238:239], v[238:239], s[94:95]\n"
      "v_cvt_pk_bf16_f32 v180, v232, v233\n"
      "v_cvt_pk_bf16_f32 v181, v234, v235\n"
      "v_cvt_pk_bf16_f32 v182, v236, v237\n"
      "v_cvt_pk_bf16_f32 v183, v238, v239\n"
      "ds_write_b128 v226, v[180:183] offset:17408\n"
      "s_add_u32 m0, s62, 9088\n"
      "s_nop 0\n"
      "buffer_load_dwordx4 v120, s[20:23], s60 offen offset:128 nt lds\n"
      "s_waitcnt vmcnt(11)\n"
      "v_lshlrev_b32 v232, 16, v184\n"
      "v_and_b32 v233, s74, v184\n"
      "v_lshlrev_b32 v234, 16, v185\n"
      "v_and_b32 v235, s74, v185\n"
      "v_lshlrev_b32 v236, 16, v186\n"
      "v_and_b32 v237, s74, v186\n"
      "v_lshlrev_b32 v238, 16, v187\n"
      "v_and_b32 v239, s74, v187\n"
      "v_pk_mul_f32 v[232:233], v[232:233], s[94:95]\n"
      "v_pk_mul_f32 v[234:235], v[234:235], s[94:95]\n"
      "v_pk_mul_f32 v[236:237], v[236:237], s[94:95]\n"
      "v_pk_mul_f32 v[238:239], v[238:239], s[94:95]\n"
      "v_cvt_pk_bf16_f32 v184, v232, v233\n"
      "v_cvt_pk_bf16_f32 v185, v234, v235\n"
      "v_cvt_pk_bf16_f32 v186, v236, v237\n"
      "v_cvt_pk_bf16_f32 v187, v238, v239\n"
      "ds_write_b128 v226, v[184:187] offset:24576\n"
      "s_add_u32 m0, %[tv], 0\n"
      "s_nop 0\n"
      "buffer_load_dwordx4 v249, s[84:87], s75 offen nt lds\n"
      "s_add_u32 m0, %[tv], 1024\n"
      "s_nop 0\n"
      "buffer_load_dwordx4 v121, s[84:87], s75 offen nt lds\n"
      "s_waitcnt vmcnt(12)\n"
      "v_lshlrev_b32 v232, 16, v188\n"
      "v_and_b32 v233, s74, v188\n"
      "v_lshlrev_b32 v234, 16, v189\n"
      "v_and_b32 v235, s74, v189\n"
      "v_lshlrev_b32 v236, 16, v190\n"
      "v_and_b32 v237, s74, v190\n"
      "v_lshlrev_b32 v238, 16, v191\n"
      "v_and_b32 v239, s74, v191\n"
      "v_pk_mul_f32 v[232:233], v[232:233], s[94:95]\n"
      "v_pk_mul_f32 v[234:235], v[234:235], s[94:95]\n"
      "v_pk_mul_f32 v[236:237], v[236:237], s[94:95]\n"
      "v_pk_mul_f32 v[238:239], v[238:239], s[94:95]\n"
      "v_cvt_pk_bf16_f32 v188, v232, v233\n"
      "v_cvt_pk_bf16_f32 v189, v234, v235\n"
      "v_cvt_pk_bf16_f32 v190, v236, v237\n"
      "v_cvt_pk_bf16_f32 v191, v238, v239\n"
      "ds_write_b128 v226, v[188:191] offset:25600\n"
      "s_add_u32 s62, s62, 0x8000\n"
      "s_and_b32 s62, s62, 0x1ffff\n"
      "s_add_u32 s60, s60, s78\n"
      "s_waitcnt vmcnt(10) lgkmcnt(0)\n"
      "s_barrier\n"
      "s_memrealtime s[68:69]\n"
      "ds_read_b64 v[208:209], v115 offset:0\n"
      "ds_read_b64 v[210:211], v115 offset:32\n"
      "ds_read_b64 v[212:213], v115 offset:1024\n"
      "ds_read_b64 v[214:215], v115 offset:1056\n"
      "ds_read_b128 a[96:99], v224 offset:0\n"
      "ds_read_b128 a[100:103], v224 offset:4096\n"
      "ds_read_b128 a[0:3], v118 offset:0\n"
      "ds_read_b128 a[4:7], v118 offset:4096\n"
      "ds_read_b128 a[8:11], v118 offset:8192\n"
      "ds_read_b128 a[12:15], v118 offset:12288\n"
      "ds_read_b128 a[104:107], v225 offset:0\n"
      "ds_read_b128 a[108:111], v225 offset:4096\n"
      "global_load_dwordx2 v[240:241], v240, s[18:19] sc1\n"
      "s_mov_b32 s28, s18\n"
      "s_and_b32 s29, s19, 0xffff\n"
      "s_mov_b32 s30, 0x688000\n"
      "s_mov_b32 s31, 0x20000\n"
      "s_cmp_lt_u32 %[rep], 128\n"
      "s_cselect_b32 s89, 0x2ffe, 0\n"
      "s_or_b32 s89, s89, 0x2aae\n"
      "s_waitcnt lgkmcnt(8)\n"
      "ds_read_b128 a[16:19], v119 offset:0\n"
      "ds_read_b128 a[20:23], v119 offset:4096\n"
      "ds_read_b128 a[24:27], v119 offset:8192\n"
      "ds_read_b128 a[28:31], v119 offset:12288\n"
      "v_lshlrev_b32 v96, 16, v208\n"
      "v_and_b32 v97, s74, v208\n"
      "v_lshlrev_b32 v98, 16, v209\n"
      "v_and_b32 v99, s74, v209\n"
      "v_lshlrev_b32 v100, 16, v210\n"
      "v_and_b32 v101, s74, v210\n"
      "v_lshlrev_b32 v102, 16, v211\n"
      "v_and_b32 v103, s74, v211\n"
      "v_lshlrev_b32 v104, 16, v212\n"
      "v_and_b32 v105, s74, v212\n"
      "v_lshlrev_b32 v106, 16, v213\n"
      "v_and_b32 v107, s74, v213\n"
      "v_lshlrev_b32 v108, 16, v214\n"
      "v_and_b32 v109, s74, v214\n"
      "v_lshlrev_b32 v110, 16, v215\n"
      "v_and_b32 v111, s74, v215\n"
      "v_fma_f32 v96, -v96, s76, s76\n"
      "v_fma_f32 v97, -v97, s76, s76\n"
      "v_fma_f32 v98, -v98, s76, s76\n"
      "v_fma_f32 v99, -v99, s76, s76\n"
      "v_fma_f32 v100, -v100, s76, s76\n"
      "v_fma_f32 v101, -v101, s76, s76\n"
      "v_fma_f32 v102, -v102, s76, s76\n"
      "v_fma_f32 v103, -v103, s76, s76\n"
      "v_fma_f32 v104, -v104, s76, s76\n"
      "v_fma_f32 v105, -v105, s76, s76\n"
      "v_fma_f32 v106, -v106, s76, s76\n"
      "v_fma_f32 v107, -v107, s76, s76\n"
      "v_fma_f32 v108, -v108, s76, s76\n"
      "v_fma_f32 v109, -v109, s76, s76\n"
      "v_fma_f32 v110, -v110, s76, s76\n"
      "v_fma_f32 v111, -v111, s76, s76\n"
      "s_waitcnt lgkmcnt(0)\n"
      "ds_read_b128 a[112:115], v224 offset:1024\n"
      "ds_read_b128 a[116:119], v224 offset:5120\n"
      "ds_read_b128 a[32:35], v118 offset:1024\n"
      "ds_read_b128 a[36:39], v118 offset:5120\n"
      "ds_read_b128 a[40:43], v118 offset:9216\n"
      "ds_read_b128 a[44:47], v118 offset:13312\n"
      "ds_read_b128 a[120:123], v225 offset:1024\n"
      "ds_read_b128 a[124:127], v225 offset:5120\n"
      "ds_read_b128 a[48:51], v119 offset:1024\n"
      "ds_read_b128 a[52:55], v119 offset:5120\n"
      "ds_read_b128 a[56:59], v119 offset:9216\n"
      "ds_read_b128 a[60:63], v119 offset:13312\n"
      "s_cmp_lt_u32 %[lead], 32\n"
      "s_cbranch_scc0 NOLEAD_%=\n"
      "v_mov_b32 v115, s68\n"
      "buffer_store_dword v115, v250, s[28:31], %[lead] offen sc1\n"
      "NOLEAD_%=:\n"
      "v_mfma_f32_16x16x32_bf16 v[160:163], a[96:99], a[0:3], v[96:99]\n"
      "s_mov_b32 s83, 0\n"
      "v_add_u32 v122, 0x2000, v249\n"
      "v_add_u32 v123, 0x2000, v121\n"
      "v_mfma_f32_16x16x32_bf16 v[168:171], a[96:99], a[4:7], v[104:107]\n"
      "s_add_u32 m0, s62, 0\n"
      "s_sub_u32 s67, s68, s82\n"
      "v_mov_b32 v113, 0x3f803f80\n"
      "v_accvgpr_write_b32 a192, 0\n"
      "v_mfma_f32_16x16x32_bf16 v[176:179], a[96:99], a[8:11], v[96:99]\n"
      "s_min_u32 s67, s67, 140\n"
      "buffer_load_dwordx4 v248, s[20:23], s60 offen nt lds\n"
      "v_cmp_eq_u32 vcc, 0, v112\n"
      "v_mfma_f32_16x16x32_bf16 v[184:187], a[96:99], a[12:15], v[104:107]\n"
      "s_add_u32 m0, s62, 896\n"
      "s_add_u32 s66, s82, s67\n"
      "v_mfma_f32_16x16x32_bf16 v[164:167], a[100:103], a[0:3], v[100:103]\n"
      "s_add_u32 s66, s66, 70\n"
      "buffer_load_dwordx4 v248, s[20:23], s60 offen offset:128 nt lds\n"
      "v_cndmask_b32 v114, 0, v113, vcc\n"
      "v_mfma_f32_16x16x32_bf16 v[172:175], a[100:103], a[4:7], v[108:111]\n"
      "s_add_u32 m0, s62, 8192\n"
      "s_sub_u32 s71, s68, 400\n"
      "v_accvgpr_write_b32 a196, v114\n"
      "v_accvgpr_write_b32 a197, v114\n"
      "v_mfma_f32_16x16x32_bf16 v[180:183], a[100:103], a[8:11], v[100:103]\n"
      "s_mov_b32 s68, s66\n"
      "buffer_load_dwordx4 v120, s[20:23], s60 offen nt lds\n"
      "v_accvgpr_write_b32 a198, v114\n"
      "v_accvgpr_write_b32 a199, v114\n"
      "v_mfma_f32_16x16x32_bf16 v[188:191], a[100:103], a[12:15], v[108:111]\n"
      "s_add_u32 m0, s62, 9088\n"
      "s_mov_b32 s82, s66\n"
      "v_cmp_eq_u32 vcc, 4, v112\n"
      "v_mfma_f32_16x16x32_bf16 v[160:163], a[104:107], a[16:19], v[160:163]\n"
      "s_add_u32 s62, s62, 0x8000\n"
      "buffer_load_dwordx4 v120, s[20:23], s60 offen offset:128 nt lds\n"
      "s_mov_b32 s61, %[so0]\n"
      "v_mfma_f32_16x16x32_bf16 v[168:171], a[104:107], a[20:23], v[168:171]\n"
      "s_and_b32 s62, s62, 0x1ffff\n"
      "s_add_u32 s77, %[wb], 0x10000\n"
      "v_cndmask_b32 v114, 0, v113, vcc\n"
      "v_mfma_f32_16x16x32_bf16 v[176:179], a[104:107], a[24:27], v[176:179]\n"
      "s_add_u32 s60, s60, %[inc]\n"
      "s_mov_b32 s70, 300\n"
      "v_accvgpr_write_b32 a200, v114\n"
      "v_accvgpr_write_b32 a201, v114\n"
      "v_mfma_f32_16x16x32_bf16 v[184:187], a[104:107], a[28:31], v[184:187]\n"
      "s_mov_b32 s80, 0x12a\n"
      "v_accvgpr_write_b32 a202, v114\n"
      "v_accvgpr_write_b32 a203, v114\n"
      "v_mfma_f32_16x16x32_bf16 v[164:167], a[108:111], a[16:19], v[164:167]\n"
      "s_mov_b32 s81, 0x0\n"
      "v_cmp_eq_u32 vcc, 8, v112\n"
      "v_mfma_f32_16x16x32_bf16 v[172:175], a[108:111], a[20:23], v[172:175]\n"
      "s_mov_b32 s101, 1000\n"
      "v_mfma_f32_16x16x32_bf16 v[180:183], a[108:111], a[24:27], v[180:183]\n"
      "s_mov_b32 s98, 110\n"
      "v_cndmask_b32 v114, 0, v113, vcc\n"
      "v_mfma_f32_16x16x32_bf16 v[188:191], a[108:111], a[28:31], v[188:191]\n"
      "s_waitcnt lgkmcnt(0)\n"
      "s_mov_b32 s93, 28\n"
      "v_accvgpr_write_b32 a204, v114\n"
      "v_accvgpr_write_b32 a205, v114\n"
      "v_mfma_f32_16x16x32_bf16 v[160:163], a[112:115], a[32:35], v[160:163]\n"
      "s_mov_b32 s99, 21\n"
      "v_accvgpr_write_b32 a206, v114\n"
      "v_accvgpr_write_b32 a207, v114\n"
      "v_mfma_f32_16x16x32_bf16 v[168:171], a[112:115], a[36:39], v[168:171]\n"
      "s_mov_b32 s100, 282\n"
      "v_cmp_eq_u32 vcc, 12, v112\n"
      "v_mfma_f32_16x16x32_bf16 v[176:179], a[112:115], a[40:43], v[176:179]\n"
      "s_lshl_b32 s91, %[wb], 18\n"
      "v_mfma_f32_16x16x32_bf16 v[184:187], a[112:115], a[44:47], v[184:187]\n"
      "s_sub_u32 s91, 0x10000000, s91\n"
      "v_cndmask_b32 v114, 0, v113, vcc\n"
      "v_mfma_f32_16x16x32_bf16 v[164:167], a[116:119], a[32:35], v[164:167]\n"
      "s_mov_b32 s86, 110\n"
      "v_accvgpr_write_b32 a208, v114\n"
      "v_accvgpr_write_b32 a209, v114\n"
      "v_mfma_f32_16x16x32_bf16 v[172:175], a[116:119], a[36:39], v[172:175]\n"
      "s_mov_b32 s87, 0\n"
      "v_accvgpr_write_b32 a210, v114\n"
      "v_accvgpr_write_b32 a211, v114\n"
      "s_waitcnt vmcnt(11)\n"
      "s_barrier\n"
      "v_mfma_f32_16x16x32_bf16 v[180:183], a[116:119], a[40:43], v[180:183]\n"
      "s_mov_b32 s26, 0x8000000\n"
      "ds_read_b128 a[64:67], v242 offset:0\n"
      "v_mfma_f32_16x16x32_bf16 v[188:191], a[116:119], a[44:47], v[188:191]\n"
      "s_mov_b32 s27, 0x20000\n"
      "ds_read_b128 a[68:71], v242 offset:4096\n"
      "v_mfma_f32_16x16x32_bf16 v[160:163], a[120:123], a[48:51], v[160:163]\n"
      "s_mov_b32 s24, s8\n"
      "ds_read_b128 a[72:75], v243 offset:0\n"
      "v_mfma_f32_16x16x32_bf16 v[168:171], a[120:123], a[52:55], v[168:171]\n"
      "s_and_b32 s25, s9, 0xffff\n"
      "ds_read_b128 a[76:79], v243 offset:4096\n"
      "v_mfma_f32_16x16x32_bf16 v[176:179], a[120:123], a[56:59], v[176:179]\n"
      "ds_read_b128 a[80:83], v242 offset:1024\n"
      "v_mfma_f32_16x16x32_bf16 v[184:187], a[120:123], a[60:63], v[184:187]\n"
      "ds_read_b128 a[84:87], v242 offset:5120\n"
      "v_mfma_f32_16x16x32_bf16 v[164:167], a[124:127], a[48:51], v[164:167]\n"
      "ds_read_b128 a[88:91], v243 offset:1024\n"
      "v_mfma_f32_16x16x32_bf16 v[172:175], a[124:127], a[52:55], v[172:175]\n"
      "ds_read_b128 a[92:95], v243 offset:5120\n"
      "v_mfma_f32_16x16x32_bf16 v[180:183], a[124:127], a[56:59], v[180:183]\n"
      "v_mfma_f32_16x16x32_bf16 v[188:191], a[124:127], a[60:63], v[188:191]\n"
      "s_nop 3\n"
      "v_max3_f32 v112, v160, v161, v162\n"
      "v_max3_f32 v113, v168, v169, v170\n"
      "v_max3_f32 v114, v176, v177, v178\n"
      "v_max3_f32 v115, v184, v185, v186\n"
      "v_max3_f32 v112, v112, v163, v164\n"
      "v_max3_f32 v113, v113, v171, v172\n"
      "v_max3_f32 v114, v114, v179, v180\n"
      "v_max3_f32 v115, v115, v187, v188\n"
      "v_max3_f32 v112, v112, v165, v166\n"
      "v_max3_f32 v113, v113, v173, v174\n"
      "v_max3_f32 v114, v114, v181, v182\n"
      "v_max3_f32 v115, v115, v189, v190\n"
      "v_max_f32 v112, v112, v167\n"
      "v_max_f32 v113, v113, v175\n"
      "v_max_f32 v114, v114, v183\n"
      "v_max_f32 v115, v115, v191\n"
      "v_mov_b32 v208, v112\n"
      "v_mov_b32 v209, v113\n"
      "v_mov_b32 v210, v114\n"
      "v_mov_b32 v211, v115\n"
      "v_permlane16_swap_b32 v112, v208\n"
      "v_permlane16_swap_b32 v113, v209\n"
      "v_permlane16_swap_b32 v114, v210\n"
      "v_permlane16_swap_b32 v115, v211\n"
      "v_max_f32 v112, v112, v208\n"
      "v_max_f32 v113, v113, v209\n"
      "v_max_f32 v114, v114, v210\n"
      "v_max_f32 v115, v115, v211\n"
      "v_mov_b32 v208, v112\n"
      "v_mov_b32 v209, v113\n"
      "v_mov_b32 v210, v114\n"
      "v_mov_b32 v211, v115\n"
      "v_permlane32_swap_b32 v112, v208\n"
      "v_permlane32_swap_b32 v113, v209\n"
      "v_permlane32_swap_b32 v114, v210\n"
      "v_permlane32_swap_b32 v115, v211\n"
      "v_max_f32 v112, v112, v208\n"
      "v_max_f32 v113, v113, v209\n"
      "v_max_f32 v114, v114, v210\n"
      "v_max_f32 v115, v115, v211\n"
      "v_sub_f32 v112, 0xc1800000, v112\n"
      "v_sub_f32 v113, 0xc1800000, v113\n"
      "v_sub_f32 v114, 0xc1800000, v114\n"
      "v_sub_f32 v115, 0xc1800000, v115\n"
      "v_floor_f32 v112, v112\n"
      "v_floor_f32 v113, v113\n"
      "v_floor_f32 v114, v114\n"
      "v_floor_f32 v115, v115\n"
      "v_min_f32 v224, 0x7f000000, v112\n"
      "v_min_f32 v228, 0x7f000000, v113\n"
      "v_min_f32 v232, 0x7f000000, v114\n"
      "v_min_f32 v236, 0x7f000000, v115\n"
      "v_add_f32 v112, %[neg], v224\n"
      "v_add_f32 v113, %[neg], v228\n"
      "v_add_f32 v114, %[neg], v232\n"
      "v_add_f32 v115, %[neg], v236\n"
      "v_mov_b32 v225, v224\n"
      "v_mov_b32 v229, v228\n"
      "v_mov_b32 v233, v232\n"
      "v_mov_b32 v237, v236\n"
      "v_mov_b64 v[226:227], v[224:225]\n"
      "v_mov_b64 v[230:231], v[228:229]\n"
      "v_mov_b64 v[234:235], v[232:233]\n"
      "v_mov_b64 v[238:239], v[236:237]\n"
      "s_add_u32 m0, s77, 16384\n"
      "v_pk_add_f32 v[160:161], v[160:161], v[112:113] op_sel:[0,0] op_sel_hi:[1,0]\n"
      "buffer_load_dwordx4 v249, s[24:27], s61 offen nt lds\n"
      "v_pk_add_f32 v[162:163], v[162:163], v[112:113] op_sel:[0,0] op_sel_hi:[1,0]\n"
      "v_pk_add_f32 v[164:165], v[164:165], v[112:113] op_sel:[0,0] op_sel_hi:[1,0]\n"
      "v_pk_add_f32 v[166:167], v[166:167], v[112:113] op_sel:[0,0] op_sel_hi:[1,0]\n"
      "s_add_u32 m0, s77, 17408\n"
      "v_pk_add_f32 v[168:169], v[168:169], v[112:113] op_sel:[0,1] op_sel_hi:[1,1]\n"
      "buffer_load_dwordx4 v121, s[24:27], s61 offen nt lds\n"
      "v_pk_add_f32 v[170:171], v[170:171], v[112:113] op_sel:[0,1] op_sel_hi:[1,1]\n"
      "v_pk_add_f32 v[172:173], v[172:173], v[112:113] op_sel:[0,1] op_sel_hi:[1,1]\n"
      "v_pk_add_f32 v[174:175], v[174:175], v[112:113] op_sel:[0,1] op_sel_hi:[1,1]\n"
      "s_add_u32 m0, s77, 24576\n"
      "v_pk_add_f32 v[176:177], v[176:177], v[114:115] op_sel:[0,0] op_sel_hi:[1,0]\n"
      "buffer_load_dwordx4 v122, s[24:27], s61 offen nt lds\n"
      "v_pk_add_f32 v[178:179], v[178:179], v[114:115] op_sel:[0,0] op_sel_hi:[1,0]\n"
      "v_pk_add_f32 v[180:181], v[180:181], v[114:115] op_sel:[0,0] op_sel_hi:[1,0]\n"
      "v_pk_add_f32 v[182:183], v[182:183], v[114:115] op_sel:[0,0] op_sel_hi:[1,0]\n"
      "s_add_u32 m0, s77, 25600\n"
      "v_pk_add_f32 v[184:185], v[184:185], v[114:115] op_sel:[0,1] op_sel_hi:[1,1]\n"
      "buffer_load_dwordx4 v123, s[24:27], s61 offen nt lds\n"
      "v_pk_add_f32 v[186:187], v[186:187], v[114:115] op_sel:[0,1] op_sel_hi:[1,1]\n"
      "v_pk_add_f32 v[188:189], v[188:189], v[114:115] op_sel:[0,1] op_sel_hi:[1,1]\n"
      "v_pk_add_f32 v[190:191], v[190:191], v[114:115] op_sel:[0,1] op_sel_hi:[1,1]\n"
      "s_add_u32 s77, s77, 0x8000\n"
      "s_and_b32 s77, s77, 0x1ffff\n"
      "s_add_u32 s61, s61, %[inc]\n"
      "v_exp_f32 v112, v160\n"
      "v_exp_f32 v113, v161\n"
      "v_exp_f32 v114, v162\n"
      "v_cvt_pk_bf16_f32 v208, v112, v113\n"
      "v_exp_f32 v115, v163\n"
      "v_exp_f32 v112, v164\n"
      "v_cvt_pk_bf16_f32 v209, v114, v115\n"
      "v_exp_f32 v113, v165\n"
      "v_or_b32 v116, v208, v209\n"
      "v_exp_f32 v114, v166\n"
      "v_cvt_pk_bf16_f32 v210, v112, v113\n"
      "v_exp_f32 v115, v167\n"
      "v_exp_f32 v112, v168\n"
      "v_cvt_pk_bf16_f32 v211, v114, v115\n"
      "v_exp_f32 v113, v169\n"
      "v_or3_b32 v116, v116, v210, v211\n"
      "v_exp_f32 v114, v170\n"
      "v_cvt_pk_bf16_f32 v212, v112, v113\n"
      "v_exp_f32 v115, v171\n"
      "v_exp_f32 v112, v172\n"
      "v_cvt_pk_bf16_f32 v213, v114, v115\n"
      "v_exp_f32 v113, v173\n"
      "v_or3_b32 v116, v116, v212, v213\n"
      "s_waitcnt lgkmcnt(0)\n"
      "v_cvt_pk_bf16_f32 v214, v112, v113\n"
      "s_sub_u32 s63, %[n1], 1\n"
      "s_branch ENTRY_%=\n"
      "LOOP1_%=:\n"
      "s_waitcnt vmcnt(12) lgkmcnt(0)\n"
      "s_sub_u32 s67, s68, s90\n"
      "s_cmp_lt_i32 s67, 0\n"
      "s_cbranch_scc1 GW0_%=\n"
      "GX0_%=:\n"
      "s_barrier\n"
      "v_mfma_f32_16x16x32_bf16 v[40:43], a[96:99], v[216:219], v[40:43]\n"
      "s_memrealtime s[68:69]\n"
      "buffer_load_dword v250, v251, s[28:31], 0 offen sc1\n"
      "ds_read_b128 a[64:67], v242 offset:0\n"
      "v_exp_f32 v114, v166\n"
      "v_mfma_f32_16x16x32_bf16 v[44:47], a[96:99], v[220:223], v[44:47]\n"
      "s_add_u32 m0, s77, 16384\n"
      "ds_read_b128 a[68:71], v242 offset:4096\n"
      "ds_read_b128 a[72:75], v243 offset:0\n"
      "v_mfma_f32_16x16x32_bf16 v[60:63], a[100:103], v[220:223], v[60:63]\n"
      "buffer_load_dwordx4 v249, s[24:27], s61 offen nt lds\n"
      "v_exp_f32 v115, v167\n"
      "v_mfma_f32_16x16x32_bf16 v[56:59], a[100:103], v[216:219], v[56:59]\n"
      "s_add_u32 m0, s77, 17408\n"
      "ds_read_b128 a[76:79], v243 offset:4096\n"
      "v_cvt_pk_bf16_f32 v211, v114, v115\n"
      "v_mfma_f32_16x16x32_bf16 a[136:139], a[104:107], v[216:219], a[136:139]\n"
      "buffer_load_dwordx4 v121, s[24:27], s61 offen nt lds\n"
      "v_exp_f32 v112, v168\n"
      "v_mfma_f32_16x16x32_bf16 a[140:143], a[104:107], v[220:223], a[140:143]\n"
      "s_add_u32 m0, s77, 24576\n"
      "ds_read_b128 a[80:83], v242 offset:1024\n"
      "v_or3_b32 v116, v116, v210, v211\n"
      "v_mfma_f32_16x16x32_bf16 a[156:159], a[108:111], v[220:223], a[156:159]\n"
      "buffer_load_dwordx4 v122, s[24:27], s61 offen nt lds\n"
      "v_exp_f32 v113, v169\n"
      "v_mfma_f32_16x16x32_bf16 a[152:155], a[108:111], v[216:219], a[152:155]\n"
      "s_add_u32 m0, s77, 25600\n"
      "ds_read_b128 a[84:87], v242 offset:5120\n"
      "v_cvt_pk_bf16_f32 v212, v112, v113\n"
      "v_mfma_f32_16x16x32_bf16 v[72:75], a[112:115], v[216:219], v[72:75]\n"
      "s_add_u32 s77, s77, 0x8000\n"
      "buffer_load_dwordx4 v123, s[24:27], s61 offen nt lds\n"
      "v_exp_f32 v114, v170\n"
      "v_mfma_f32_16x16x32_bf16 v[76:79], a[112:115], v[220:223], v[76:79]\n"
      "s_and_b32 s77, s77, 0x1ffff\n"
      "ds_read_b128 a[88:91], v243 offset:1024\n"
      "ds_read_b128 a[92:95], v243 offset:5120\n"
      "v_mfma_f32_16x16x32_bf16 v[92:95], a[116:119], v[220:223], v[92:95]\n"
      "s_add_u32 s61, s61, %[inc]\n"
      "v_exp_f32 v115, v171\n"
      "v_mfma_f32_16x16x32_bf16 v[88:91], a[116:119], v[216:219], v[88:91]\n"
      "v_cvt_pk_bf16_f32 v213, v114, v115\n"
      "v_mfma_f32_16x16x32_bf16 a[168:171], a[120:123], v[216:219], a[168:171]\n"
      "v_exp_f32 v112, v172\n"
      "v_mfma_f32_16x16x32_bf16 a[172:175], a[120:123], v[220:223], a[172:175]\n"
      "v_or3_b32 v116, v116, v212, v213\n"
      "v_mfma_f32_16x16x32_bf16 a[188:191], a[124:127], v[220:223], a[188:191]\n"
      "v_exp_f32 v113, v173\n"
      "v_mfma_f32_16x16x32_bf16 a[184:187], a[124:127], v[216:219], a[184:187]\n"
      "s_waitcnt lgkmcnt(0)\n"
      "v_cvt_pk_bf16_f32 v214, v112, v113\n"
      "ENTRY_%=:\n"
      "v_mfma_f32_16x16x32_bf16 v[128:131], a[64:67], a[0:3], v[224:227]\n"
      "s_sub_u32 s67, s68, s66\n"
      "v_exp_f32 v114, v174\n"
      "v_mfma_f32_16x16x32_bf16 v[128:131], a[72:75], a[16:19], v[128:131]\n"
      "s_cmp_gt_i32 s67, s70\n"
      "ds_read_b64_tr_b16 a[96:97], v244 offset:0\n"
      "ds_read_b64_tr_b16 a[98:99], v244 offset:4096\n"
      "v_mfma_f32_16x16x32_bf16 v[128:131], a[80:83], a[32:35], v[128:131]\n"
      "s_cselect_b32 s69, s86, 0\n"
      "v_exp_f32 v115, v175\n"
      "v_mfma_f32_16x16x32_bf16 v[128:131], a[88:91], a[48:51], v[128:131]\n"
      "s_add_u32 s66, s66, s69\n"
      "ds_read_b64_tr_b16 a[100:101], v245 offset:0\n"
      "v_cvt_pk_bf16_f32 v215, v114, v115\n"
      "v_mfma_f32_16x16x32_bf16 v[132:135], a[92:95], a[48:51], v[224:227]\n"
      "s_add_u32 s67, s67, s101\n"
      "v_exp_f32 v112, v176\n"
      "v_mfma_f32_16x16x32_bf16 v[132:135], a[84:87], a[32:35], v[132:135]\n"
      "s_min_i32 s70, s70, s67\n"
      "ds_read_b64_tr_b16 a[102:103], v245 offset:4096\n"
      "v_or3_b32 v116, v116, v214, v215\n"
      "v_mfma_f32_16x16x32_bf16 v[132:135], a[76:79], a[16:19], v[132:135]\n"
      "s_max_i32 s70, s70, 85\n"
      "v_exp_f32 v113, v177\n"
      "v_mfma_f32_16x16x32_bf16 v[132:135], a[68:71], a[0:3], v[132:135]\n"
      "s_add_u32 s66, s66, s86\n"
      "ds_read_b64_tr_b16 a[104:105], v246 offset:0\n"
      "v_cvt_pk_bf16_f32 v216, v112, v113\n"
      "v_mfma_f32_16x16x32_bf16 v[140:143], a[68:71], a[4:7], v[228:231]\n"
      "s_and_b32 s76, s80, 31\n"
      "v_exp_f32 v114, v178\n"
      "v_mfma_f32_16x16x32_bf16 v[140:143], a[76:79], a[20:23], v[140:143]\n"
      "s_lshr_b64 s[80:81], s[80:81], 5\n"
      "ds_read_b64_tr_b16 a[106:107], v246 offset:4096\n"
      "ds_read_b64_tr_b16 a[108:109], v247 offset:0\n"
      "v_mfma_f32_16x16x32_bf16 v[140:143], a[84:87], a[36:39], v[140:143]\n"
      "s_add_u32 s66, s66, s76\n"
      "v_exp_f32 v115, v179\n"
      "v_mfma_f32_16x16x32_bf16 v[140:143], a[92:95], a[52:55], v[140:143]\n"
      "s_sub_u32 s67, s68, s82\n"
      "ds_read_b64_tr_b16 a[110:111], v247 offset:4096\n"
      "v_cvt_pk_bf16_f32 v217, v114, v115\n"
      "v_mfma_f32_16x16x32_bf16 v[136:139], a[88:91], a[52:55], v[228:231]\n"
      "s_min_i32 s76, s67, s83\n"
      "v_exp_f32 v112, v180\n"
      "v_mfma_f32_16x16x32_bf16 v[136:139], a[80:83], a[36:39], v[136:139]\n"
      "s_mov_b32 s83, s67\n"
      "ds_read_b64_tr_b16 a[112:113], v244 offset:1024\n"
      "v_or3_b32 v116, v116, v216, v217\n"
      "v_mfma_f32_16x16x32_bf16 v[136:139], a[72:75], a[20:23], v[136:139]\n"
      "s_sub_u32 s76, s76, 36\n"
      "v_exp_f32 v113, v181\n"
      "v_mfma_f32_16x16x32_bf16 v[136:139], a[64:67], a[4:7], v[136:139]\n"
      "s_min_i32 s76, s76, 148\n"
      "ds_read_b64_tr_b16 a[114:115], v244 offset:5120\n"
      "v_cvt_pk_bf16_f32 v218, v112, v113\n"
      "v_mfma_f32_16x16x32_bf16 v[144:147], a[64:67], a[8:11], v[232:235]\n"
      "s_max_i32 s76, s76, 70\n"
      "v_exp_f32 v114, v182\n"
      "v_mfma_f32_16x16x32_bf16 v[144:147], a[72:75], a[24:27], v[144:147]\n"
      "s_mov_b32 s82, s68\n"
      "ds_read_b64_tr_b16 a[116:117], v245 offset:1024\n"
      "ds_read_b64_tr_b16 a[118:119], v245 offset:5120\n"
      "v_mfma_f32_16x16x32_bf16 v[144:147], a[80:83], a[40:43], v[144:147]\n"
      "s_add_u32 s65, s82, s76\n"
      "v_exp_f32 v115, v183\n"
      "v_mfma_f32_16x16x32_bf16 v[144:147], a[88:91], a[56:59], v[144:147]\n"
      "s_sub_u32 s93, s93, 1\n"
      "ds_read_b64_tr_b16 a[120:121], v246 offset:1024\n"
      "v_cvt_pk_bf16_f32 v219, v114, v115\n"
      "v_mfma_f32_16x16x32_bf16 v[148:151], a[92:95], a[56:59], v[232:235]\n"
      "s_cmp_le_i32 s93, 14\n"
      "v_exp_f32 v112, v184\n"
      "v_mfma_f32_16x16x32_bf16 v[148:151], a[84:87], a[40:43], v[148:151]\n"
      "s_cselect_b32 s86, 107, s86\n"
      "ds_read_b64_tr_b16 a[122:123], v246 offset:5120\n"
      "v_or3_b32 v116, v116, v218, v219\n"
      "v_mfma_f32_16x16x32_bf16 v[148:151], a[76:79], a[24:27], v[148:151]\n"
      "s_sub_u32 s67, s65, s66\n"
      "v_exp_f32 v113, v185\n"
      "v_mfma_f32_16x16x32_bf16 v[148:151], a[68:71], a[8:11], v[148:151]\n"
      "s_cmp_lt_i32 s67, 0\n"
      "ds_read_b64_tr_b16 a[124:125], v247 offset:1024\n"
      "v_cvt_pk_bf16_f32 v220, v112, v113\n"
      "v_mfma_f32_16x16x32_bf16 v[156:159], a[68:71], a[12:15], v[236:239]\n"
      "s_cselect_b32 s88, s66, s65\n"
      "v_exp_f32 v114, v186\n"
      "v_mfma_f32_16x16x32_bf16 v[156:159], a[76:79], a[28:31], v[156:159]\n"
      "s_add_u32 s90, s88, s91\n"
      "ds_read_b64_tr_b16 a[126:127], v247 offset:5120\n"
      "v_mfma_f32_16x16x32_bf16 v[156:159], a[84:87], a[44:47], v[156:159]\n"
      "v_exp_f32 v115, v187\n"
      "v_mfma_f32_16x16x32_bf16 v[156:159], a[92:95], a[60:63], v[156:159]\n"
      "v_cvt_pk_bf16_f32 v221, v114, v115\n"
      "v_mfma_f32_16x16x32_bf16 v[152:155], a[88:91], a[60:63], v[236:239]\n"
      "v_exp_f32 v112, v188\n"
      "v_mfma_f32_16x16x32_bf16 v[152:155], a[80:83], a[44:47], v[152:155]\n"
      "v_or3_b32 v116, v116, v220, v221\n"
      "v_mfma_f32_16x16x32_bf16 v[152:155], a[72:75], a[28:31], v[152:155]\n"
      "v_exp_f32 v113, v189\n"
      "v_mfma_f32_16x16x32_bf16 v[152:155], a[64:67], a[12:15], v[152:155]\n"
      "s_waitcnt lgkmcnt(10)\n"
      "v_cvt_pk_bf16_f32 v222, v112, v113\n"
      "v_mfma_f32_16x16x32_bf16 v[32:35], a[96:99], v[192:195], v[32:35]\n"
      "v_exp_f32 v114, v190\n"
      "v_mfma_f32_16x16x32_bf16 v[48:51], a[100:103], v[192:195], v[48:51]\n"
      "v_mfma_f32_16x16x32_bf16 a[128:131], a[104:107], v[192:195], a[128:131]\n"
      "s_waitcnt lgkmcnt(6)\n"
      "v_exp_f32 v115, v191\n"
      "v_mfma_f32_16x16x32_bf16 a[144:147], a[108:111], v[192:195], a[144:147]\n"
      "v_cvt_pk_bf16_f32 v223, v114, v115\n"
      "v_mfma_f32_16x16x32_bf16 v[64:67], a[112:115], v[192:195], v[64:67]\n"
      "s_waitcnt lgkmcnt(2)\n"
      "v_mfma_f32_16x16x32_bf16 v[80:83], a[116:119], v[192:195], v[80:83]\n"
      "v_or3_b32 v116, v116, v222, v223\n"
      "v_mfma_f32_16x16x32_bf16 a[160:163], a[120:123], v[192:195], a[160:163]\n"
      "s_mov_b32 s64, 0\n"
      "s_waitcnt lgkmcnt(0)\n"
      "v_and_b32 v116, 0x40004000, v116\n"
      "v_mfma_f32_16x16x32_bf16 a[176:179], a[124:127], v[192:195], a[176:179]\n"
      "v_cmp_ne_u32 vcc, 0, v116\n"
      "s_cbranch_vccnz FB1_%=\n"
      "RET1_0_%=:\n"
      "v_mfma_f32_16x16x32_bf16 a[180:183], a[124:127], v[196:199], a[180:183]\n"
      "v_exp_f32 v112, v128\n"
      "s_barrier\n"
      "v_mfma_f32_16x16x32_bf16 a[164:167], a[120:123], v[196:199], a[164:167]\n"
      "v_mfma_f32_16x16x32_bf16 a[192:195], a[196:199], v[208:211], a[192:195]\n"
      "s_add_u32 m0, s62, 0\n"
      "v_exp_f32 v113, v129\n"
      "v_mfma_f32_16x16x32_bf16 v[84:87], a[116:119], v[196:199], v[84:87]\n"
      "buffer_load_dwordx4 v248, s[20:23], s60 offen nt lds\n"
      "v_cvt_pk_bf16_f32 v192, v112, v113\n"
      "v_mfma_f32_16x16x32_bf16 v[68:71], a[112:115], v[196:199], v[68:71]\n"
      "s_add_u32 m0, s62, 896\n"
      "v_exp_f32 v114, v130\n"
      "v_mfma_f32_16x16x32_bf16 a[192:195], a[200:203], v[212:215], a[192:195]\n"
      "buffer_load_dwordx4 v248, s[20:23], s60 offen offset:128 nt lds\n"
      "v_mfma_f32_16x16x32_bf16 a[148:151], a[108:111], v[196:199], a[148:151]\n"
      "s_add_u32 m0, s62, 8192\n"
      "v_exp_f32 v115, v131\n"
      "v_mfma_f32_16x16x32_bf16 a[132:135], a[104:107], v[196:199], a[132:135]\n"
      "s_memrealtime s[68:69]\n"
      "buffer_load_dwordx4 v120, s[20:23], s60 offen nt lds\n"
      "v_cvt_pk_bf16_f32 v193, v114, v115\n"
      "v_mfma_f32_16x16x32_bf16 a[192:195], a[204:207], v[216:219], a[192:195]\n"
      "s_add_u32 m0, s62, 9088\n"
      "v_exp_f32 v112, v132\n"
      "v_mfma_f32_16x16x32_bf16 v[52:55], a[100:103], v[196:199], v[52:55]\n"
      "s_add_u32 s62, s62, 0x8000\n"
      "buffer_load_dwordx4 v120, s[20:23], s60 offen offset:128 nt lds\n"
      "v_or_b32 v116, v192, v193\n"
      "v_mfma_f32_16x16x32_bf16 v[36:39], a[96:99], v[196:199], v[36:39]\n"
      "s_and_b32 s62, s62, 0x1ffff\n"
      "v_exp_f32 v113, v133\n"
      "v_mfma_f32_16x16x32_bf16 a[192:195], a[208:211], v[220:223], a[192:195]\n"
      "s_add_u32 s60, s60, s78\n"
      "v_cvt_pk_bf16_f32 v194, v112, v113\n"
      "s_waitcnt vmcnt(12) lgkmcnt(0)\n"
      "s_sub_u32 s67, s68, s90\n"
      "s_cmp_lt_i32 s67, 0\n"
      "s_cbranch_scc1 GW1_%=\n"
      "GX1_%=:\n"
      "s_barrier\n"
      "v_mfma_f32_16x16x32_bf16 v[40:43], a[96:99], v[200:203], v[40:43]\n"
      "s_memrealtime s[68:69]\n"
      "ds_read_b128 a[64:67], v242 offset:32768\n"
      "v_exp_f32 v114, v134\n"
      "v_mfma_f32_16x16x32_bf16 v[44:47], a[96:99], v[204:207], v[44:47]\n"
      "s_add_u32 m0, s77, 16384\n"
      "ds_read_b128 a[68:71], v242 offset:36864\n"
      "ds_read_b128 a[72:75], v243 offset:32768\n"
      "v_mfma_f32_16x16x32_bf16 v[60:63], a[100:103], v[204:207], v[60:63]\n"
      "buffer_load_dwordx4 v249, s[24:27], s61 offen nt lds\n"
      "v_exp_f32 v115, v135\n"
      "v_mfma_f32_16x16x32_bf16 v[56:59], a[100:103], v[200:203], v[56:59]\n"
      "s_add_u32 m0, s77, 17408\n"
      "ds_read_b128 a[76:79], v243 offset:36864\n"
      "v_cvt_pk_bf16_f32 v195, v114, v115\n"
      "v_mfma_f32_16x16x32_bf16 a[136:139], a[104:107], v[200:203], a[136:139]\n"
      "buffer_load_dwordx4 v121, s[24:27], s61 offen nt lds\n"
      "v_exp_f32 v112, v136\n"
      "v_mfma_f32_16x16x32_bf16 a[140:143], a[104:107], v[204:207], a[140:143]\n"
      "s_add_u32 m0, s77, 24576\n"
      "ds_read_b128 a[80:83], v242 offset:33792\n"
      "v_or3_b32 v116, v116, v194, v195\n"
      "v_mfma_f32_16x16x32_bf16 a[156:159], a[108:111], v[204:207], a[156:159]\n"
      "buffer_load_dwordx4 v122, s[24:27], s61 offen nt lds\n"
      "v_exp_f32 v113, v137\n"
      "v_mfma_f32_16x16x32_bf16 a[152:155], a[108:111], v[200:203], a[152:155]\n"
      "s_add_u32 m0, s77, 25600\n"
      "ds_read_b128 a[84:87], v242 offset:37888\n"
      "v_cvt_pk_bf16_f32 v196, v112, v113\n"
      "v_mfma_f32_16x16x32_bf16 v[72:75], a[112:115], v[200:203], v[72:75]\n"
      "s_add_u32 s77, s77, 0x8000\n"
      "buffer_load_dwordx4 v123, s[24:27], s61 offen nt lds\n"
      "v_exp_f32 v114, v138\n"
      "v_mfma_f32_16x16x32_bf16 v[76:79], a[112:115], v[204:207], v[76:79]\n"
      "s_and_b32 s77, s77, 0x1ffff\n"
      "ds_read_b128 a[88:91], v243 offset:33792\n"
      "ds_read_b128 a[92:95], v243 offset:37888\n"
      "v_mfma_f32_16x16x32_bf16 v[92:95], a[116:119], v[204:207], v[92:95]\n"
      "s_add_u32 s61, s61, s78\n"
      "v_exp_f32 v115, v139\n"
      "v_mfma_f32_16x16x32_bf16 v[88:91], a[116:119], v[200:203], v[88:91]\n"
      "v_cvt_pk_bf16_f32 v197, v114, v115\n"
      "v_mfma_f32_16x16x32_bf16 a[168:171], a[120:123], v[200:203], a[168:171]\n"
      "v_exp_f32 v112, v140\n"
      "v_mfma_f32_16x16x32_bf16 a[172:175], a[120:123], v[204:207], a[172:175]\n"
      "v_or3_b32 v116, v116, v196, v197\n"
      "v_mfma_f32_16x16x32_bf16 a[188:191], a[124:127], v[204:207], a[188:191]\n"
      "v_exp_f32 v113, v141\n"
      "v_mfma_f32_16x16x32_bf16 a[184:187], a[124:127], v[200:203], a[184:187]\n"
      "s_waitcnt lgkmcnt(0)\n"
      "v_cvt_pk_bf16_f32 v198, v112, v113\n"
      "v_mfma_f32_16x16x32_bf16 v[160:163], a[64:67], a[0:3], v[224:227]\n"
      "s_sub_u32 s67, s68, s66\n"
      "v_exp_f32 v114, v142\n"
      "v_mfma_f32_16x16x32_bf16 v[160:163], a[72:75], a[16:19], v[160:163]\n"
      "s_cmp_gt_i32 s67, s70\n"
      "ds_read_b64_tr_b16 a[96:97], v244 offset:32768\n"
      "ds_read_b64_tr_b16 a[98:99], v244 offset:36864\n"
      "v_mfma_f32_16x16x32_bf16 v[160:163], a[80:83], a[32:35], v[160:163]\n"
      "s_cselect_b32 s69, s86, 0\n"
      "v_exp_f32 v115, v143\n"
      "v_mfma_f32_16x16x32_bf16 v[160:163], a[88:91], a[48:51], v[160:163]\n"
      "s_add_u32 s66, s66, s69\n"
      "ds_read_b64_tr_b16 a[100:101], v245 offset:32768\n"
      "v_cvt_pk_bf16_f32 v199, v114, v115\n"
      "v_mfma_f32_16x16x32_bf16 v[164:167], a[92:95], a[48:51], v[224:227]\n"
      "s_add_u32 s67, s67, s101\n"
      "v_exp_f32 v112, v144\n"
      "v_mfma_f32_16x16x32_bf16 v[164:167], a[84:87], a[32:35], v[164:167]\n"
      "s_min_i32 s70, s70, s67\n"
      "ds_read_b64_tr_b16 a[102:103], v245 offset:36864\n"
      "v_or3_b32 v116, v116, v198, v199\n"
      "v_mfma_f32_16x16x32_bf16 v[164:167], a[76:79], a[16:19], v[164:167]\n"
      "s_max_i32 s70, s70, 85\n"
      "v_exp_f32 v113, v145\n"
      "v_mfma_f32_16x16x32_bf16 v[164:167], a[68:71], a[0:3], v[164:167]\n"
      "s_add_u32 s66, s66, s86\n"
      "ds_read_b64_tr_b16 a[104:105], v246 offset:32768\n"
      "v_cvt_pk_bf16_f32 v200, v112, v113\n"
      "v_mfma_f32_16x16x32_bf16 v[172:175], a[68:71], a[4:7], v[228:231]\n"
      "s_and_b32 s76, s80, 31\n"
      "v_exp_f32 v114, v146\n"
      "v_mfma_f32_16x16x32_bf16 v[172:175], a[76:79], a[20:23], v[172:175]\n"
      "s_lshr_b64 s[80:81], s[80:81], 5\n"
      "ds_read_b64_tr_b16 a[106:107], v246 offset:36864\n"
      "ds_read_b64_tr_b16 a[108:109], v247 offset:32768\n"
      "v_mfma_f32_16x16x32_bf16 v[172:175], a[84:87], a[36:39], v[172:175]\n"
      "s_add_u32 s66, s66, s76\n"
      "v_exp_f32 v115, v147\n"
      "v_mfma_f32_16x16x32_bf16 v[172:175], a[92:95], a[52:55], v[172:175]\n"
      "s_sub_u32 s67, s68, s82\n"
      "ds_read_b64_tr_b16 a[110:111], v247 offset:36864\n"
      "v_cvt_pk_bf16_f32 v201, v114, v115\n"
      "v_mfma_f32_16x16x32_bf16 v[168:171], a[88:91], a[52:55], v[228:231]\n"
      "s_min_i32 s76, s67, s83\n"
      "v_exp_f32 v112, v148\n"
      "v_mfma_f32_16x16x32_bf16 v[168:171], a[80:83], a[36:39], v[168:171]\n"
      "s_mov_b32 s83, s67\n"
      "ds_read_b64_tr_b16 a[112:113], v244 offset:33792\n"
      "v_or3_b32 v116, v116, v200, v201\n"
      "v_mfma_f32_16x16x32_bf16 v[168:171], a[72:75], a[20:23], v[168:171]\n"
      "s_sub_u32 s76, s76, 36\n"
      "v_exp_f32 v113, v149\n"
      "v_mfma_f32_16x16x32_bf16 v[168:171], a[64:67], a[4:7], v[168:171]\n"
      "s_min_i32 s76, s76, 148\n"
      "ds_read_b64_tr_b16 a[114:115], v244 offset:37888\n"
      "v_cvt_pk_bf16_f32 v202, v112, v113\n"
      "v_mfma_f32_16x16x32_bf16 v[176:179], a[64:67], a[8:11], v[232:235]\n"
      "s_max_i32 s76, s76, 70\n"
      "v_exp_f32 v114, v150\n"
      "v_mfma_f32_16x16x32_bf16 v[176:179], a[72:75], a[24:27], v[176:179]\n"
      "s_mov_b32 s82, s68\n"
      "ds_read_b64_tr_b16 a[116:117], v245 offset:33792\n"
      "ds_read_b64_tr_b16 a[118:119], v245 offset:37888\n"
      "v_mfma_f32_16x16x32_bf16 v[176:179], a[80:83], a[40:43], v[176:179]\n"
      "s_add_u32 s65, s82, s76\n"
      "v_exp_f32 v115, v151\n"
      "v_mfma_f32_16x16x32_bf16 v[176:179], a[88:91], a[56:59], v[176:179]\n"
      "s_sub_u32 s93, s93, 1\n"
      "ds_read_b64_tr_b16 a[120:121], v246 offset:33792\n"
      "v_cvt_pk_bf16_f32 v203, v114, v115\n"
      "v_mfma_f32_16x16x32_bf16 v[180:183], a[92:95], a[56:59], v[232:235]\n"
      "s_cmp_le_i32 s93, 14\n"
      "v_exp_f32 v112, v152\n"
      "v_mfma_f32_16x16x32_bf16 v[180:183], a[84:87], a[40:43], v[180:183]\n"
      "s_cselect_b32 s86, 107, s86\n"
      "ds_read_b64_tr_b16 a[122:123], v246 offset:37888\n"
      "v_or3_b32 v116, v116, v202, v203\n"
      "v_mfma_f32_16x16x32_bf16 v[180:183], a[76:79], a[24:27], v[180:183]\n"
      "s_sub_u32 s67, s65, s66\n"
      "v_exp_f32 v113, v153\n"
      "v_mfma_f32_16x16x32_bf16 v[180:183], a[68:71], a[8:11], v[180:183]\n"
      "s_cmp_lt_i32 s67, 0\n"
      "ds_read_b64_tr_b16 a[124:125], v247 offset:33792\n"
      "v_cvt_pk_bf16_f32 v204, v112, v113\n"
      "v_mfma_f32_16x16x32_bf16 v[188:191], a[68:71], a[12:15], v[236:239]\n"
      "s_cselect_b32 s88, s66, s65\n"
      "v_exp_f32 v114, v154\n"
      "v_mfma_f32_16x16x32_bf16 v[188:191], a[76:79], a[28:31], v[188:191]\n"
      "s_add_u32 s90, s88, s91\n"
      "ds_read_b64_tr_b16 a[126:127], v247 offset:37888\n"
      "v_mfma_f32_16x16x32_bf16 v[188:191], a[84:87], a[44:47], v[188:191]\n"
      "v_exp_f32 v115, v155\n"
      "v_mfma_f32_16x16x32_bf16 v[188:191], a[92:95], a[60:63], v[188:191]\n"
      "v_cvt_pk_bf16_f32 v205, v114, v115\n"
      "v_mfma_f32_16x16x32_bf16 v[184:187], a[88:91], a[60:63], v[236:239]\n"
      "v_exp_f32 v112, v156\n"
      "v_mfma_f32_16x16x32_bf16 v[184:187], a[80:83], a[44:47], v[184:187]\n"
      "v_or3_b32 v116, v116, v204, v205\n"
      "v_mfma_f32_16x16x32_bf16 v[184:187], a[72:75], a[28:31], v[184:187]\n"
      "v_exp_f32 v113, v157\n"
      "v_mfma_f32_16x16x32_bf16 v[184:187], a[64:67], a[12:15], v[184:187]\n"
      "s_waitcnt lgkmcnt(10)\n"
      "v_cvt_pk_bf16_f32 v206, v112, v113\n"
      "v_mfma_f32_16x16x32_bf16 v[32:35], a[96:99], v[208:211], v[32:35]\n"
      "v_exp_f32 v114, v158\n"
      "v_mfma_f32_16x16x32_bf16 v[48:51], a[100:103], v[208:211], v[48:51]\n"
      "v_mfma_f32_16x16x32_bf16 a[128:131], a[104:107], v[208:211], a[128:131]\n"
      "s_waitcnt lgkmcnt(6)\n"
      "v_exp_f32 v115, v159\n"
      "v_mfma_f32_16x16x32_bf16 a[144:147], a[108:111], v[208:211], a[144:147]\n"
      "v_cvt_pk_bf16_f32 v207, v114, v115\n"
      "v_mfma_f32_16x16x32_bf16 v[64:67], a[112:115], v[208:211], v[64:67]\n"
      "s_waitcnt lgkmcnt(2)\n"
      "v_mfma_f32_16x16x32_bf16 v[80:83], a[116:119], v[208:211], v[80:83]\n"
      "v_or3_b32 v116, v116, v206, v207\n"
      "v_mfma_f32_16x16x32_bf16 a[160:163], a[120:123], v[208:211], a[160:163]\n"
      "s_mov_b32 s64, 0\n"
      "s_waitcnt lgkmcnt(0)\n"
      "v_and_b32 v116, 0x40004000, v116\n"
      "v_mfma_f32_16x16x32_bf16 a[176:179], a[124:127], v[208:211], a[176:179]\n"
      "v_cmp_ne_u32 vcc, 0, v116\n"
      "s_cbranch_vccnz FB0_%=\n"
      "RET0_0_%=:\n"
      "v_mfma_f32_16x16x32_bf16 a[180:183], a[124:127], v[212:215], a[180:183]\n"
      "v_exp_f32 v112, v160\n"
      "s_barrier\n"
      "v_mfma_f32_16x16x32_bf16 a[164:167], a[120:123], v[212:215], a[164:167]\n"
      "v_xor_b32 v242, 0x10000, v242\n"
      "v_mfma_f32_16x16x32_bf16 a[192:195], a[196:199], v[192:195], a[192:195]\n"
      "s_add_u32 m0, s62, 0\n"
      "v_exp_f32 v113, v161\n"
      "v_mfma_f32_16x16x32_bf16 v[84:87], a[116:119], v[212:215], v[84:87]\n"
      "buffer_load_dwordx4 v248, s[20:23], s60 offen nt lds\n"
      "v_cvt_pk_bf16_f32 v208, v112, v113\n"
      "v_xor_b32 v243, 0x10000, v243\n"
      "v_mfma_f32_16x16x32_bf16 v[68:71], a[112:115], v[212:215], v[68:71]\n"
      "s_add_u32 m0, s62, 896\n"
      "v_exp_f32 v114, v162\n"
      "v_mfma_f32_16x16x32_bf16 a[192:195], a[200:203], v[196:199], a[192:195]\n"
      "buffer_load_dwordx4 v248, s[20:23], s60 offen offset:128 nt lds\n"
      "v_xor_b32 v244, 0x10000, v244\n"
      "v_mfma_f32_16x16x32_bf16 a[148:151], a[108:111], v[212:215], a[148:151]\n"
      "s_add_u32 m0, s62, 8192\n"
      "v_exp_f32 v115, v163\n"
      "v_mfma_f32_16x16x32_bf16 a[132:135], a[104:107], v[212:215], a[132:135]\n"
      "s_memrealtime s[68:69]\n"
      "buffer_load_dwordx4 v120, s[20:23], s60 offen nt lds\n"
      "v_cvt_pk_bf16_f32 v209, v114, v115\n"
      "v_xor_b32 v245, 0x10000, v245\n"
      "v_mfma_f32_16x16x32_bf16 a[192:195], a[204:207], v[200:203], a[192:195]\n"
      "s_add_u32 m0, s62, 9088\n"
      "v_exp_f32 v112, v164\n"
      "v_mfma_f32_16x16x32_bf16 v[52:55], a[100:103], v[212:215], v[52:55]\n"
      "s_add_u32 s62, s62, 0x8000\n"
      "buffer_load_dwordx4 v120, s[20:23], s60 offen offset:128 nt lds\n"
      "v_or_b32 v116, v208, v209\n"
      "v_xor_b32 v246, 0x10000, v246\n"
      "v_mfma_f32_16x16x32_bf16 v[36:39], a[96:99], v[212:215], v[36:39]\n"
      "s_and_b32 s62, s62, 0x1ffff\n"
      "s_add_u32 s60, s60, %[inc]\n"
      "v_exp_f32 v113, v165\n"
      "v_mfma_f32_16x16x32_bf16 a[192:195], a[208:211], v[204:207], a[192:195]\n"
      "s_sub_u32 s63, s63, 1\n"
      "v_cvt_pk_bf16_f32 v210, v112, v113\n"
      "v_xor_b32 v247, 0x10000, v247\n"
      "s_cbranch_scc0 LOOP1_%=\n"
      "v_readfirstlane_b32 s72, v240\n"
      "v_readfirstlane_b32 s79, v241\n"
      "s_waitcnt vmcnt(12) lgkmcnt(0)\n"
      "s_sub_u32 s67, s68, s90\n"
      "s_cmp_lt_i32 s67, 0\n"
      "s_cbranch_scc1 GW2_%=\n"
      "GX2_%=:\n"
      "s_barrier\n"
      "v_mfma_f32_16x16x32_bf16 v[40:43], a[96:99], v[216:219], v[40:43]\n"
      "s_memrealtime s[68:69]\n"
      "buffer_load_dword v250, v251, s[28:31], 0 offen sc1\n"
      "ds_read_b128 a[64:67], v242 offset:0\n"
      "v_exp_f32 v114, v166\n"
      "v_mfma_f32_16x16x32_bf16 v[44:47], a[96:99], v[220:223], v[44:47]\n"
      "s_add_u32 m0, s77, 16384\n"
      "ds_read_b128 a[68:71], v242 offset:4096\n"
      "ds_read_b128 a[72:75], v243 offset:0\n"
      "v_mfma_f32_16x16x32_bf16 v[60:63], a[100:103], v[220:223], v[60:63]\n"
      "buffer_load_dwordx4 v249, s[24:27], s61 offen nt lds\n"
      "v_exp_f32 v115, v167\n"
      "v_mfma_f32_16x16x32_bf16 v[56:59], a[100:103], v[216:219], v[56:59]\n"
      "s_add_u32 m0, s77, 17408\n"
      "ds_read_b128 a[76:79], v243 offset:4096\n"
      "v_cvt_pk_bf16_f32 v211, v114, v115\n"
      "v_mfma_f32_16x16x32_bf16 a[136:139], a[104:107], v[216:219], a[136:139]\n"
      "buffer_load_dwordx4 v121, s[24:27], s61 offen nt lds\n"
      "v_exp_f32 v112, v168\n"
      "v_mfma_f32_16x16x32_bf16 a[140:143], a[104:107], v[220:223], a[140:143]\n"
      "s_add_u32 m0, s77, 24576\n"
      "ds_read_b128 a[80:83], v242 offset:1024\n"
      "v_or3_b32 v116, v116, v210, v211\n"
      "v_mfma_f32_16x16x32_bf16 a[156:159], a[108:111], v[220:223], a[156:159]\n"
      "buffer_load_dwordx4 v122, s[24:27], s61 offen nt lds\n"
      "v_exp_f32 v113, v169\n"
      "v_mfma_f32_16x16x32_bf16 a[152:155], a[108:111], v[216:219], a[152:155]\n"
      "s_add_u32 m0, s77, 25600\n"
      "ds_read_b128 a[84:87], v242 offset:5120\n"
      "v_cvt_pk_bf16_f32 v212, v112, v113\n"
      "v_mfma_f32_16x16x32_bf16 v[72:75], a[112:115], v[216:219], v[72:75]\n"
      "s_add_u32 s77, s77, 0x8000\n"
      "buffer_load_dwordx4 v123, s[24:27], s61 offen nt lds\n"
      "v_exp_f32 v114, v170\n"
      "v_mfma_f32_16x16x32_bf16 v[76:79], a[112:115], v[220:223], v[76:79]\n"
      "s_and_b32 s77, s77, 0x1ffff\n"
      "ds_read_b128 a[88:91], v243 offset:1024\n"
      "ds_read_b128 a[92:95], v243 offset:5120\n"
      "v_mfma_f32_16x16x32_bf16 v[92:95], a[116:119], v[220:223], v[92:95]\n"
      "s_add_u32 s61, s61, %[inc]\n"
      "v_exp_f32 v115, v171\n"
      "v_mfma_f32_16x16x32_bf16 v[88:91], a[116:119], v[216:219], v[88:91]\n"
      "v_cvt_pk_bf16_f32 v213, v114, v115\n"
      "v_mfma_f32_16x16x32_bf16 a[168:171], a[120:123], v[216:219], a[168:171]\n"
      "v_exp_f32 v112, v172\n"
      "v_mfma_f32_16x16x32_bf16 a[172:175], a[120:123], v[220:223], a[172:175]\n"
      "v_or3_b32 v116, v116, v212, v213\n"
      "v_mfma_f32_16x16x32_bf16 a[188:191], a[124:127], v[220:223], a[188:191]\n"
      "v_exp_f32 v113, v173\n"
      "v_mfma_f32_16x16x32_bf16 a[184:187], a[124:127], v[216:219], a[184:187]\n"
      "s_waitcnt lgkmcnt(0)\n"
      "v_cvt_pk_bf16_f32 v214, v112, v113\n"
      "v_mfma_f32_16x16x32_bf16 v[128:131], a[64:67], a[0:3], v[224:227]\n"
      "s_sub_u32 s67, s68, s66\n"
      "v_exp_f32 v114, v174\n"
      "v_mfma_f32_16x16x32_bf16 v[128:131], a[72:75], a[16:19], v[128:131]\n"
      "s_cmp_gt_i32 s67, s70\n"
      "ds_read_b64_tr_b16 a[96:97], v244 offset:0\n"
      "ds_read_b64_tr_b16 a[98:99], v244 offset:4096\n"
      "v_mfma_f32_16x16x32_bf16 v[128:131], a[80:83], a[32:35], v[128:131]\n"
      "s_cselect_b32 s69, s86, 0\n"
      "v_exp_f32 v115, v175\n"
      "v_mfma_f32_16x16x32_bf16 v[128:131], a[88:91], a[48:51], v[128:131]\n"
      "s_add_u32 s66, s66, s69\n"
      "ds_read_b64_tr_b16 a[100:101], v245 offset:0\n"
      "v_cvt_pk_bf16_f32 v215, v114, v115\n"
      "v_mfma_f32_16x16x32_bf16 v[132:135], a[92:95], a[48:51], v[224:227]\n"
      "s_add_u32 s67, s67, s101\n"
      "v_exp_f32 v112, v176\n"
      "v_mfma_f32_16x16x32_bf16 v[132:135], a[84:87], a[32:35], v[132:135]\n"
      "s_min_i32 s70, s70, s67\n"
      "ds_read_b64_tr_b16 a[102:103], v245 offset:4096\n"
      "v_or3_b32 v116, v116, v214, v215\n"
      "v_mfma_f32_16x16x32_bf16 v[132:135], a[76:79], a[16:19], v[132:135]\n"
      "s_max_i32 s70, s70, 85\n"
      "v_exp_f32 v113, v177\n"
      "v_mfma_f32_16x16x32_bf16 v[132:135], a[68:71], a[0:3], v[132:135]\n"
      "s_add_u32 s66, s66, s86\n"
      "ds_read_b64_tr_b16 a[104:105], v246 offset:0\n"
      "v_cvt_pk_bf16_f32 v216, v112, v113\n"
      "v_mfma_f32_16x16x32_bf16 v[140:143], a[68:71], a[4:7], v[228:231]\n"
      "s_and_b32 s76, s80, 31\n"
      "v_exp_f32 v114, v178\n"
      "v_mfma_f32_16x16x32_bf16 v[140:143], a[76:79], a[20:23], v[140:143]\n"
      "s_lshr_b64 s[80:81], s[80:81], 5\n"
      "ds_read_b64_tr_b16 a[106:107], v246 offset:4096\n"
      "ds_read_b64_tr_b16 a[108:109], v247 offset:0\n"
      "v_mfma_f32_16x16x32_bf16 v[140:143], a[84:87], a[36:39], v[140:143]\n"
      "s_add_u32 s66, s66, s76\n"
      "v_exp_f32 v115, v179\n"
      "v_mfma_f32_16x16x32_bf16 v[140:143], a[92:95], a[52:55], v[140:143]\n"
      "s_sub_u32 s67, s68, s82\n"
      "ds_read_b64_tr_b16 a[110:111], v247 offset:4096\n"
      "v_cvt_pk_bf16_f32 v217, v114, v115\n"
      "v_mfma_f32_16x16x32_bf16 v[136:139], a[88:91], a[52:55], v[228:231]\n"
      "s_min_i32 s76, s67, s83\n"
      "v_exp_f32 v112, v180\n"
      "v_mfma_f32_16x16x32_bf16 v[136:139], a[80:83], a[36:39], v[136:139]\n"
      "s_mov_b32 s83, s67\n"
      "ds_read_b64_tr_b16 a[112:113], v244 offset:1024\n"
      "v_or3_b32 v116, v116, v216, v217\n"
      "v_mfma_f32_16x16x32_bf16 v[136:139], a[72:75], a[20:23], v[136:139]\n"
      "s_sub_u32 s76, s76, 36\n"
      "v_exp_f32 v113, v181\n"
      "v_mfma_f32_16x16x32_bf16 v[136:139], a[64:67], a[4:7], v[136:139]\n"
      "s_min_i32 s76, s76, 148\n"
      "ds_read_b64_tr_b16 a[114:115], v244 offset:5120\n"
      "v_cvt_pk_bf16_f32 v218, v112, v113\n"
      "v_mfma_f32_16x16x32_bf16 v[144:147], a[64:67], a[8:11], v[232:235]\n"
      "s_max_i32 s76, s76, 70\n"
      "v_exp_f32 v114, v182\n"
      "v_mfma_f32_16x16x32_bf16 v[144:147], a[72:75], a[24:27], v[144:147]\n"
      "s_mov_b32 s82, s68\n"
      "ds_read_b64_tr_b16 a[116:117], v245 offset:1024\n"
      "ds_read_b64_tr_b16 a[118:119], v245 offset:5120\n"
      "v_mfma_f32_16x16x32_bf16 v[144:147], a[80:83], a[40:43], v[144:147]\n"
      "s_lshr_b32 s69, s86, 1\n"
      "v_exp_f32 v115, v183\n"
      "v_mfma_f32_16x16x32_bf16 v[144:147], a[88:91], a[56:59], v[144:147]\n"
      "s_add_u32 s66, s66, s69\n"
      "ds_read_b64_tr_b16 a[120:121], v246 offset:1024\n"
      "v_cvt_pk_bf16_f32 v219, v114, v115\n"
      "v_mfma_f32_16x16x32_bf16 v[148:151], a[92:95], a[56:59], v[232:235]\n"
      "s_add_u32 s82, s82, s69\n"
      "v_exp_f32 v112, v184\n"
      "v_mfma_f32_16x16x32_bf16 v[148:151], a[84:87], a[40:43], v[148:151]\n"
      "s_add_u32 s65, s82, s76\n"
      "ds_read_b64_tr_b16 a[122:123], v246 offset:5120\n"
      "v_or3_b32 v116, v116, v218, v219\n"
      "v_mfma_f32_16x16x32_bf16 v[148:151], a[76:79], a[24:27], v[148:151]\n"
      "s_sub_u32 s93, s93, 1\n"
      "v_exp_f32 v113, v185\n"
      "v_mfma_f32_16x16x32_bf16 v[148:151], a[68:71], a[8:11], v[148:151]\n"
      "s_cmp_le_i32 s93, 14\n"
      "ds_read_b64_tr_b16 a[124:125], v247 offset:1024\n"
      "v_cvt_pk_bf16_f32 v220, v112, v113\n"
      "v_mfma_f32_16x16x32_bf16 v[156:159], a[68:71], a[12:15], v[236:239]\n"
      "s_cselect_b32 s86, 107, s86\n"
      "v_exp_f32 v114, v186\n"
      "v_mfma_f32_16x16x32_bf16 v[156:159], a[76:79], a[28:31], v[156:159]\n"
      "s_sub_u32 s67, s65, s66\n"
      "ds_read_b64_tr_b16 a[126:127], v247 offset:5120\n"
      "v_mfma_f32_16x16x32_bf16 v[156:159], a[84:87], a[44:47], v[156:159]\n"
      "s_cmp_lt_i32 s67, 0\n"
      "v_exp_f32 v115, v187\n"
      "v_mfma_f32_16x16x32_bf16 v[156:159], a[92:95], a[60:63], v[156:159]\n"
      "s_cselect_b32 s88, s66, s65\n"
      "v_cvt_pk_bf16_f32 v221, v114, v115\n"
      "v_mfma_f32_16x16x32_bf16 v[152:155], a[88:91], a[60:63], v[236:239]\n"
      "s_add_u32 s90, s88, s91\n"
      "v_exp_f32 v112, v188\n"
      "v_mfma_f32_16x16x32_bf16 v[152:155], a[80:83], a[44:47], v[152:155]\n"
      "v_or3_b32 v116, v116, v220, v221\n"
      "v_mfma_f32_16x16x32_bf16 v[152:155], a[72:75], a[28:31], v[152:155]\n"
      "v_exp_f32 v113, v189\n"
      "v_mfma_f32_16x16x32_bf16 v[152:155], a[64:67], a[12:15], v[152:155]\n"
      "s_waitcnt lgkmcnt(10)\n"
      "v_cvt_pk_bf16_f32 v222, v112, v113\n"
      "v_mfma_f32_16x16x32_bf16 v[32:35], a[96:99], v[192:195], v[32:35]\n"
      "v_exp_f32 v114, v190\n"
      "v_mfma_f32_16x16x32_bf16 v[48:51], a[100:103], v[192:195], v[48:51]\n"
      "v_mfma_f32_16x16x32_bf16 a[128:131], a[104:107], v[192:195], a[128:131]\n"
      "s_waitcnt lgkmcnt(6)\n"
      "v_exp_f32 v115, v191\n"
      "v_mfma_f32_16x16x32_bf16 a[144:147], a[108:111], v[192:195], a[144:147]\n"
      "v_cvt_pk_bf16_f32 v223, v114, v115\n"
      "v_mfma_f32_16x16x32_bf16 v[64:67], a[112:115], v[192:195], v[64:67]\n"
      "s_waitcnt lgkmcnt(2)\n"
      "v_mfma_f32_16x16x32_bf16 v[80:83], a[116:119], v[192:195], v[80:83]\n"
      "v_or3_b32 v116, v116, v222, v223\n"
      "v_mfma_f32_16x16x32_bf16 a[160:163], a[120:123], v[192:195], a[160:163]\n"
      "s_mov_b32 s64, 1\n"
      "s_waitcnt lgkmcnt(0)\n"
      "v_and_b32 v116, 0x40004000, v116\n"
      "v_mfma_f32_16x16x32_bf16 a[176:179], a[124:127], v[192:195], a[176:179]\n"
      "v_cmp_ne_u32 vcc, 0, v116\n"
      "s_cbranch_vccnz FB1_%=\n"
      "RET1_1_%=:\n"
      "v_mfma_f32_16x16x32_bf16 a[180:183], a[124:127], v[196:199], a[180:183]\n"
      "v_exp_f32 v112, v128\n"
      "s_barrier\n"
      "v_mfma_f32_16x16x32_bf16 a[164:167], a[120:123], v[196:199], a[164:167]\n"
      "v_mfma_f32_16x16x32_bf16 a[192:195], a[196:199], v[208:211], a[192:195]\n"
      "s_add_u32 m0, s62, 0\n"
      "v_exp_f32 v113, v129\n"
      "v_mfma_f32_16x16x32_bf16 v[84:87], a[116:119], v[196:199], v[84:87]\n"
      "buffer_load_dwordx4 v248, s[20:23], s60 offen nt lds\n"
      "v_cvt_pk_bf16_f32 v192, v112, v113\n"
      "v_mfma_f32_16x16x32_bf16 v[68:71], a[112:115], v[196:199], v[68:71]\n"
      "s_add_u32 m0, s62, 896\n"
      "v_exp_f32 v114, v130\n"
      "v_mfma_f32_16x16x32_bf16 a[192:195], a[200:203], v[212:215], a[192:195]\n"
      "buffer_load_dwordx4 v248, s[20:23], s60 offen offset:128 nt lds\n"
      "v_mfma_f32_16x16x32_bf16 a[148:151], a[108:111], v[196:199], a[148:151]\n"
      "s_add_u32 m0, s62, 8192\n"
      "v_exp_f32 v115, v131\n"
      "v_mfma_f32_16x16x32_bf16 a[132:135], a[104:107], v[196:199], a[132:135]\n"
      "s_memrealtime s[68:69]\n"
      "buffer_load_dwordx4 v120, s[20:23], s60 offen nt lds\n"
      "v_cvt_pk_bf16_f32 v193, v114, v115\n"
      "v_mfma_f32_16x16x32_bf16 a[192:195], a[204:207], v[216:219], a[192:195]\n"
      "s_add_u32 m0, s62, 9088\n"
      "v_exp_f32 v112, v132\n"
      "v_mfma_f32_16x16x32_bf16 v[52:55], a[100:103], v[196:199], v[52:55]\n"
      "s_add_u32 s62, s62, 0x8000\n"
      "buffer_load_dwordx4 v120, s[20:23], s60 offen offset:128 nt lds\n"
      "v_or_b32 v116, v192, v193\n"
      "v_mfma_f32_16x16x32_bf16 v[36:39], a[96:99], v[196:199], v[36:39]\n"
      "s_and_b32 s62, s62, 0x1ffff\n"
      "v_exp_f32 v113, v133\n"
      "v_mfma_f32_16x16x32_bf16 a[192:195], a[208:211], v[220:223], a[192:195]\n"
      "s_add_u32 s60, s60, s78\n"
      "v_cvt_pk_bf16_f32 v194, v112, v113\n"
      "s_waitcnt vmcnt(12) lgkmcnt(0)\n"
      "s_barrier\n"
      "v_mfma_f32_16x16x32_bf16 v[40:43], a[96:99], v[200:203], v[40:43]\n"
      "ds_read_b128 a[64:67], v242 offset:32768\n"
      "v_exp_f32 v114, v134\n"
      "v_mfma_f32_16x16x32_bf16 v[44:47], a[96:99], v[204:207], v[44:47]\n"
      "ds_read_b128 a[68:71], v242 offset:36864\n"
      "ds_read_b128 a[72:75], v243 offset:32768\n"
      "v_mfma_f32_16x16x32_bf16 v[60:63], a[100:103], v[204:207], v[60:63]\n"
      "v_exp_f32 v115, v135\n"
      "v_mfma_f32_16x16x32_bf16 v[56:59], a[100:103], v[200:203], v[56:59]\n"
      "ds_read_b128 a[76:79], v243 offset:36864\n"
      "v_cvt_pk_bf16_f32 v195, v114, v115\n"
      "v_mfma_f32_16x16x32_bf16 a[136:139], a[104:107], v[200:203], a[136:139]\n"
      "v_exp_f32 v112, v136\n"
      "v_mfma_f32_16x16x32_bf16 a[140:143], a[104:107], v[204:207], a[140:143]\n"
      "ds_read_b128 a[80:83], v242 offset:33792\n"
      "v_or3_b32 v116, v116, v194, v195\n"
      "v_mfma_f32_16x16x32_bf16 a[156:159], a[108:111], v[204:207], a[156:159]\n"
      "v_exp_f32 v113, v137\n"
      "v_mfma_f32_16x16x32_bf16 a[152:155], a[108:111], v[200:203], a[152:155]\n"
      "ds_read_b128 a[84:87], v242 offset:37888\n"
      "v_cvt_pk_bf16_f32 v196, v112, v113\n"
      "v_mfma_f32_16x16x32_bf16 v[72:75], a[112:115], v[200:203], v[72:75]\n"
      "v_exp_f32 v114, v138\n"
      "v_mfma_f32_16x16x32_bf16 v[76:79], a[112:115], v[204:207], v[76:79]\n"
      "ds_read_b128 a[88:91], v243 offset:33792\n"
      "ds_read_b128 a[92:95], v243 offset:37888\n"
      "v_mfma_f32_16x16x32_bf16 v[92:95], a[116:119], v[204:207], v[92:95]\n"
      "v_exp_f32 v115, v139\n"
      "v_mfma_f32_16x16x32_bf16 v[88:91], a[116:119], v[200:203], v[88:91]\n"
      "v_cvt_pk_bf16_f32 v197, v114, v115\n"
      "v_mfma_f32_16x16x32_bf16 a[168:171], a[120:123], v[200:203], a[168:171]\n"
      "v_exp_f32 v112, v140\n"
      "v_mfma_f32_16x16x32_bf16 a[172:175], a[120:123], v[204:207], a[172:175]\n"
      "v_or3_b32 v116, v116, v196, v197\n"
      "v_mfma_f32_16x16x32_bf16 a[188:191], a[124:127], v[204:207], a[188:191]\n"
      "v_exp_f32 v113, v141\n"
      "v_mfma_f32_16x16x32_bf16 a[184:187], a[124:127], v[200:203], a[184:187]\n"
      "s_waitcnt lgkmcnt(0)\n"
      "v_cvt_pk_bf16_f32 v198, v112, v113\n"
      "v_mfma_f32_16x16x32_bf16 v[160:163], a[64:67], a[0:3], v[224:227]\n"
      "v_exp_f32 v114, v142\n"
      "v_mfma_f32_16x16x32_bf16 v[160:163], a[72:75], a[16:19], v[160:163]\n"
      "ds_read_b64_tr_b16 a[96:97], v244 offset:32768\n"
      "ds_read_b64_tr_b16 a[98:99], v244 offset:36864\n"
      "v_mfma_f32_16x16x32_bf16 v[160:163], a[80:83], a[32:35], v[160:163]\n"
      "v_exp_f32 v115, v143\n"
      "v_mfma_f32_16x16x32_bf16 v[160:163], a[88:91], a[48:51], v[160:163]\n"
      "ds_read_b64_tr_b16 a[100:101], v245 offset:32768\n"
      "v_cvt_pk_bf16_f32 v199, v114, v115\n"
      "v_mfma_f32_16x16x32_bf16 v[164:167], a[92:95], a[48:51], v[224:227]\n"
      "v_exp_f32 v112, v144\n"
      "v_mfma_f32_16x16x32_bf16 v[164:167], a[84:87], a[32:35], v[164:167]\n"
      "ds_read_b64_tr_b16 a[102:103], v245 offset:36864\n"
      "v_or3_b32 v116, v116, v198, v199\n"
      "v_mfma_f32_16x16x32_bf16 v[164:167], a[76:79], a[16:19], v[164:167]\n"
      "v_exp_f32 v113, v145\n"
      "v_mfma_f32_16x16x32_bf16 v[164:167], a[68:71], a[0:3], v[164:167]\n"
      "ds_read_b64_tr_b16 a[104:105], v246 offset:32768\n"
      "v_cvt_pk_bf16_f32 v200, v112, v113\n"
      "v_mfma_f32_16x16x32_bf16 v[172:175], a[68:71], a[4:7], v[228:231]\n"
      "v_exp_f32 v114, v146\n"
      "v_mfma_f32_16x16x32_bf16 v[172:175], a[76:79], a[20:23], v[172:175]\n"
      "ds_read_b64_tr_b16 a[106:107], v246 offset:36864\n"
      "ds_read_b64_tr_b16 a[108:109], v247 offset:32768\n"
      "v_mfma_f32_16x16x32_bf16 v[172:175], a[84:87], a[36:39], v[172:175]\n"
      "v_exp_f32 v115, v147\n"
      "v_mfma_f32_16x16x32_bf16 v[172:175], a[92:95], a[52:55], v[172:175]\n"
      "ds_read_b64_tr_b16 a[110:111], v247 offset:36864\n"
      "v_cvt_pk_bf16_f32 v201, v114, v115\n"
      "v_mfma_f32_16x16x32_bf16 v[168:171], a[88:91], a[52:55], v[228:231]\n"
      "v_exp_f32 v112, v148\n"
      "v_mfma_f32_16x16x32_bf16 v[168:171], a[80:83], a[36:39], v[168:171]\n"
      "ds_read_b64_tr_b16 a[112:113], v244 offset:33792\n"
      "v_or3_b32 v116, v116, v200, v201\n"
      "v_mfma_f32_16x16x32_bf16 v[168:171], a[72:75], a[20:23], v[168:171]\n"
      "v_exp_f32 v113, v149\n"
      "v_mfma_f32_16x16x32_bf16 v[168:171], a[64:67], a[4:7], v[168:171]\n"
      "ds_read_b64_tr_b16 a[114:115], v244 offset:37888\n"
      "v_cvt_pk_bf16_f32 v202, v112, v113\n"
      "v_mfma_f32_16x16x32_bf16 v[176:179], a[64:67], a[8:11], v[232:235]\n"
      "v_exp_f32 v114, v150\n"
      "v_mfma_f32_16x16x32_bf16 v[176:179], a[72:75], a[24:27], v[176:179]\n"
      "ds_read_b64_tr_b16 a[116:117], v245 offset:33792\n"
      "ds_read_b64_tr_b16 a[118:119], v245 offset:37888\n"
      "v_mfma_f32_16x16x32_bf16 v[176:179], a[80:83], a[40:43], v[176:179]\n"
      "v_exp_f32 v115, v151\n"
      "v_mfma_f32_16x16x32_bf16 v[176:179], a[88:91], a[56:59], v[176:179]\n"
      "ds_read_b64_tr_b16 a[120:121], v246 offset:33792\n"
      "v_cvt_pk_bf16_f32 v203, v114, v115\n"
      "v_mfma_f32_16x16x32_bf16 v[180:183], a[92:95], a[56:59], v[232:235]\n"
      "v_exp_f32 v112, v152\n"
      "v_mfma_f32_16x16x32_bf16 v[180:183], a[84:87], a[40:43], v[180:183]\n"
      "ds_read_b64_tr_b16 a[122:123], v246 offset:37888\n"
      "v_or3_b32 v116, v116, v202, v203\n"
      "v_mfma_f32_16x16x32_bf16 v[180:183], a[76:79], a[24:27], v[180:183]\n"
      "v_exp_f32 v113, v153\n"
      "v_mfma_f32_16x16x32_bf16 v[180:183], a[68:71], a[8:11], v[180:183]\n"
      "ds_read_b64_tr_b16 a[124:125], v247 offset:33792\n"
      "v_cvt_pk_bf16_f32 v204, v112, v113\n"
      "v_mfma_f32_16x16x32_bf16 v[188:191], a[68:71], a[12:15], v[236:239]\n"
      "v_exp_f32 v114, v154\n"
      "v_mfma_f32_16x16x32_bf16 v[188:191], a[76:79], a[28:31], v[188:191]\n"
      "ds_read_b64_tr_b16 a[126:127], v247 offset:37888\n"
      "v_mfma_f32_16x16x32_bf16 v[188:191], a[84:87], a[44:47], v[188:191]\n"
      "v_exp_f32 v115, v155\n"
      "v_mfma_f32_16x16x32_bf16 v[188:191], a[92:95], a[60:63], v[188:191]\n"
      "v_cvt_pk_bf16_f32 v205, v114, v115\n"
      "v_mfma_f32_16x16x32_bf16 v[184:187], a[88:91], a[60:63], v[236:239]\n"
      "v_exp_f32 v112, v156\n"
      "v_mfma_f32_16x16x32_bf16 v[184:187], a[80:83], a[44:47], v[184:187]\n"
      "v_or3_b32 v116, v116, v204, v205\n"
      "v_mfma_f32_16x16x32_bf16 v[184:187], a[72:75], a[28:31], v[184:187]\n"
      "v_exp_f32 v113, v157\n"
      "v_mfma_f32_16x16x32_bf16 v[184:187], a[64:67], a[12:15], v[184:187]\n"
      "s_waitcnt lgkmcnt(10)\n"
      "v_cvt_pk_bf16_f32 v206, v112, v113\n"
      "v_mfma_f32_16x16x32_bf16 v[32:35], a[96:99], v[208:211], v[32:35]\n"
      "v_exp_f32 v114, v158\n"
      "v_mfma_f32_16x16x32_bf16 v[48:51], a[100:103], v[208:211], v[48:51]\n"
      "v_mfma_f32_16x16x32_bf16 a[128:131], a[104:107], v[208:211], a[128:131]\n"
      "s_waitcnt lgkmcnt(6)\n"
      "v_exp_f32 v115, v159\n"
      "v_mfma_f32_16x16x32_bf16 a[144:147], a[108:111], v[208:211], a[144:147]\n"
      "v_cvt_pk_bf16_f32 v207, v114, v115\n"
      "v_mfma_f32_16x16x32_bf16 v[64:67], a[112:115], v[208:211], v[64:67]\n"
      "s_waitcnt lgkmcnt(2)\n"
      "v_mfma_f32_16x16x32_bf16 v[80:83], a[116:119], v[208:211], v[80:83]\n"
      "v_or3_b32 v116, v116, v206, v207\n"
      "v_mfma_f32_16x16x32_bf16 a[160:163], a[120:123], v[208:211], a[160:163]\n"
      "s_mov_b32 s64, 1\n"
      "s_waitcnt lgkmcnt(0)\n"
      "v_and_b32 v116, 0x40004000, v116\n"
      "v_mfma_f32_16x16x32_bf16 a[176:179], a[124:127], v[208:211], a[176:179]\n"
      "v_cmp_ne_u32 vcc, 0, v116\n"
      "s_cbranch_vccnz FB0_%=\n"
      "RET0_1_%=:\n"
      "v_mfma_f32_16x16x32_bf16 a[180:183], a[124:127], v[212:215], a[180:183]\n"
      "v_exp_f32 v112, v160\n"
      "v_mfma_f32_16x16x32_bf16 a[164:167], a[120:123], v[212:215], a[164:167]\n"
      "v_xor_b32 v242, 0x10000, v242\n"
      "v_mfma_f32_16x16x32_bf16 a[192:195], a[196:199], v[192:195], a[192:195]\n"
      "s_add_u32 m0, s77, 16384\n"
      "v_exp_f32 v113, v161\n"
      "v_mfma_f32_16x16x32_bf16 v[84:87], a[116:119], v[212:215], v[84:87]\n"
      "buffer_load_dwordx4 v249, s[24:27], s61 offen nt lds\n"
      "v_cvt_pk_bf16_f32 v208, v112, v113\n"
      "v_xor_b32 v243, 0x10000, v243\n"
      "v_mfma_f32_16x16x32_bf16 v[68:71], a[112:115], v[212:215], v[68:71]\n"
      "s_add_u32 m0, s77, 17408\n"
      "v_exp_f32 v114, v162\n"
      "v_mfma_f32_16x16x32_bf16 a[192:195], a[200:203], v[196:199], a[192:195]\n"
      "buffer_load_dwordx4 v121, s[24:27], s61 offen nt lds\n"
      "v_xor_b32 v244, 0x10000, v244\n"
      "v_mfma_f32_16x16x32_bf16 a[148:151], a[108:111], v[212:215], a[148:151]\n"
      "s_add_u32 m0, s77, 24576\n"
      "v_exp_f32 v115, v163\n"
      "v_mfma_f32_16x16x32_bf16 a[132:135], a[104:107], v[212:215], a[132:135]\n"
      "s_memrealtime s[68:69]\n"
      "buffer_load_dwordx4 v122, s[24:27], s61 offen nt lds\n"
      "v_cvt_pk_bf16_f32 v209, v114, v115\n"
      "v_xor_b32 v245, 0x10000, v245\n"
      "v_mfma_f32_16x16x32_bf16 a[192:195], a[204:207], v[200:203], a[192:195]\n"
      "s_add_u32 m0, s77, 25600\n"
      "v_exp_f32 v112, v164\n"
      "v_mfma_f32_16x16x32_bf16 v[52:55], a[100:103], v[212:215], v[52:55]\n"
      "s_add_u32 s77, s77, 0x8000\n"
      "buffer_load_dwordx4 v123, s[24:27], s61 offen nt lds\n"
      "v_or_b32 v116, v208, v209\n"
      "v_xor_b32 v246, 0x10000, v246\n"
      "v_mfma_f32_16x16x32_bf16 v[36:39], a[96:99], v[212:215], v[36:39]\n"
      "s_and_b32 s77, s77, 0x1ffff\n"
      "v_exp_f32 v113, v165\n"
      "v_mfma_f32_16x16x32_bf16 a[192:195], a[208:211], v[204:207], a[192:195]\n"
      "s_add_u32 s61, s61, s78\n"
      "v_cvt_pk_bf16_f32 v210, v112, v113\n"
      "v_xor_b32 v247, 0x10000, v247\n"
      "s_waitcnt vmcnt(8) lgkmcnt(0)\n"
      "s_sub_u32 s67, s68, s90\n"
      "s_cmp_lt_i32 s67, 0\n"
      "s_cbranch_scc1 GW3_%=\n"
      "GX3_%=:\n"
      "s_barrier\n"
      "v_mfma_f32_16x16x32_bf16 v[40:43], a[96:99], v[216:219], v[40:43]\n"
      "s_memrealtime s[68:69]\n"
      "buffer_load_dword v250, v251, s[28:31], 0 offen sc1\n"
      "ds_read_b128 a[64:67], v242 offset:0\n"
      "v_exp_f32 v114, v166\n"
      "v_mfma_f32_16x16x32_bf16 v[44:47], a[96:99], v[220:223], v[44:47]\n"
      "s_add_u32 m0, s62, 0\n"
      "ds_read_b128 a[68:71], v242 offset:4096\n"
      "ds_read_b128 a[72:75], v243 offset:0\n"
      "v_mfma_f32_16x16x32_bf16 v[60:63], a[100:103], v[220:223], v[60:63]\n"
      "buffer_load_dwordx4 v248, s[20:23], s60 offen nt lds\n"
      "v_exp_f32 v115, v167\n"
      "v_mfma_f32_16x16x32_bf16 v[56:59], a[100:103], v[216:219], v[56:59]\n"
      "s_add_u32 m0, s62, 896\n"
      "ds_read_b128 a[76:79], v243 offset:4096\n"
      "v_cvt_pk_bf16_f32 v211, v114, v115\n"
      "v_mfma_f32_16x16x32_bf16 a[136:139], a[104:107], v[216:219], a[136:139]\n"
      "buffer_load_dwordx4 v248, s[20:23], s60 offen offset:128 nt lds\n"
      "v_exp_f32 v112, v168\n"
      "v_mfma_f32_16x16x32_bf16 a[140:143], a[104:107], v[220:223], a[140:143]\n"
      "s_add_u32 m0, s62, 8192\n"
      "ds_read_b128 a[80:83], v242 offset:1024\n"
      "v_or3_b32 v116, v116, v210, v211\n"
      "v_mfma_f32_16x16x32_bf16 a[156:159], a[108:111], v[220:223], a[156:159]\n"
      "buffer_load_dwordx4 v120, s[20:23], s60 offen nt lds\n"
      "v_exp_f32 v113, v169\n"
      "v_mfma_f32_16x16x32_bf16 a[152:155], a[108:111], v[216:219], a[152:155]\n"
      "s_add_u32 m0, s62, 9088\n"
      "ds_read_b128 a[84:87], v242 offset:5120\n"
      "v_cvt_pk_bf16_f32 v212, v112, v113\n"
      "v_mfma_f32_16x16x32_bf16 v[72:75], a[112:115], v[216:219], v[72:75]\n"
      "s_add_u32 s62, s62, 0x8000\n"
      "buffer_load_dwordx4 v120, s[20:23], s60 offen offset:128 nt lds\n"
      "v_exp_f32 v114, v170\n"
      "v_mfma_f32_16x16x32_bf16 v[76:79], a[112:115], v[220:223], v[76:79]\n"
      "s_and_b32 s62, s62, 0x1ffff\n"
      "ds_read_b128 a[88:91], v243 offset:1024\n"
      "ds_read_b128 a[92:95], v243 offset:5120\n"
      "v_mfma_f32_16x16x32_bf16 v[92:95], a[116:119], v[220:223], v[92:95]\n"
      "s_add_u32 s60, s60, %[inc]\n"
      "v_exp_f32 v115, v171\n"
      "v_mfma_f32_16x16x32_bf16 v[88:91], a[116:119], v[216:219], v[88:91]\n"
      "v_cvt_pk_bf16_f32 v213, v114, v115\n"
      "v_mfma_f32_16x16x32_bf16 a[168:171], a[120:123], v[216:219], a[168:171]\n"
      "v_exp_f32 v112, v172\n"
      "v_mfma_f32_16x16x32_bf16 a[172:175], a[120:123], v[220:223], a[172:175]\n"
      "v_or3_b32 v116, v116, v212, v213\n"
      "v_mfma_f32_16x16x32_bf16 a[188:191], a[124:127], v[220:223], a[188:191]\n"
      "v_exp_f32 v113, v173\n"
      "v_mfma_f32_16x16x32_bf16 a[184:187], a[124:127], v[216:219], a[184:187]\n"
      "s_waitcnt lgkmcnt(0)\n"
      "v_cvt_pk_bf16_f32 v214, v112, v113\n"
      "v_mfma_f32_16x16x32_bf16 v[128:131], a[64:67], a[0:3], v[224:227]\n"
      "s_sub_u32 s67, s68, s66\n"
      "v_exp_f32 v114, v174\n"
      "v_mfma_f32_16x16x32_bf16 v[128:131], a[72:75], a[16:19], v[128:131]\n"
      "s_cmp_gt_i32 s67, s70\n"
      "ds_read_b64_tr_b16 a[96:97], v244 offset:0\n"
      "ds_read_b64_tr_b16 a[98:99], v244 offset:4096\n"
      "v_mfma_f32_16x16x32_bf16 v[128:131], a[80:83], a[32:35], v[128:131]\n"
      "s_cselect_b32 s69, s86, 0\n"
      "v_exp_f32 v115, v175\n"
      "v_mfma_f32_16x16x32_bf16 v[128:131], a[88:91], a[48:51], v[128:131]\n"
      "s_add_u32 s66, s66, s69\n"
      "ds_read_b64_tr_b16 a[100:101], v245 offset:0\n"
      "v_cvt_pk_bf16_f32 v215, v114, v115\n"
      "v_mfma_f32_16x16x32_bf16 v[132:135], a[92:95], a[48:51], v[224:227]\n"
      "s_add_u32 s67, s67, s101\n"
      "v_exp_f32 v112, v176\n"
      "v_mfma_f32_16x16x32_bf16 v[132:135], a[84:87], a[32:35], v[132:135]\n"
      "s_min_i32 s70, s70, s67\n"
      "ds_read_b64_tr_b16 a[102:103], v245 offset:4096\n"
      "v_or3_b32 v116, v116, v214, v215\n"
      "v_mfma_f32_16x16x32_bf16 v[132:135], a[76:79], a[16:19], v[132:135]\n"
      "s_max_i32 s70, s70, 85\n"
      "v_exp_f32 v113, v177\n"
      "v_mfma_f32_16x16x32_bf16 v[132:135], a[68:71], a[0:3], v[132:135]\n"
      "s_add_u32 s66, s66, s86\n"
      "ds_read_b64_tr_b16 a[104:105], v246 offset:0\n"
      "v_cvt_pk_bf16_f32 v216, v112, v113\n"
      "v_mfma_f32_16x16x32_bf16 v[140:143], a[68:71], a[4:7], v[228:231]\n"
      "s_and_b32 s76, s80, 31\n"
      "v_exp_f32 v114, v178\n"
      "v_mfma_f32_16x16x32_bf16 v[140:143], a[76:79], a[20:23], v[140:143]\n"
      "s_lshr_b64 s[80:81], s[80:81], 5\n"
      "ds_read_b64_tr_b16 a[106:107], v246 offset:4096\n"
      "ds_read_b64_tr_b16 a[108:109], v247 offset:0\n"
      "v_mfma_f32_16x16x32_bf16 v[140:143], a[84:87], a[36:39], v[140:143]\n"
      "s_add_u32 s66, s66, s76\n"
      "v_exp_f32 v115, v179\n"
      "v_mfma_f32_16x16x32_bf16 v[140:143], a[92:95], a[52:55], v[140:143]\n"
      "s_sub_u32 s67, s68, s82\n"
      "ds_read_b64_tr_b16 a[110:111], v247 offset:4096\n"
      "v_cvt_pk_bf16_f32 v217, v114, v115\n"
      "v_mfma_f32_16x16x32_bf16 v[136:139], a[88:91], a[52:55], v[228:231]\n"
      "s_min_i32 s76, s67, s83\n"
      "v_exp_f32 v112, v180\n"
      "v_mfma_f32_16x16x32_bf16 v[136:139], a[80:83], a[36:39], v[136:139]\n"
      "s_mov_b32 s83, s67\n"
      "ds_read_b64_tr_b16 a[112:113], v244 offset:1024\n"
      "v_or3_b32 v116, v116, v216, v217\n"
      "v_mfma_f32_16x16x32_bf16 v[136:139], a[72:75], a[20:23], v[136:139]\n"
      "s_sub_u32 s76, s76, 36\n"
      "v_exp_f32 v113, v181\n"
      "v_mfma_f32_16x16x32_bf16 v[136:139], a[64:67], a[4:7], v[136:139]\n"
      "s_min_i32 s76, s76, 148\n"
      "ds_read_b64_tr_b16 a[114:115], v244 offset:5120\n"
      "v_cvt_pk_bf16_f32 v218, v112, v113\n"
      "v_mfma_f32_16x16x32_bf16 v[144:147], a[64:67], a[8:11], v[232:235]\n"
      "s_max_i32 s76, s76, 70\n"
      "v_exp_f32 v114, v182\n"
      "v_mfma_f32_16x16x32_bf16 v[144:147], a[72:75], a[24:27], v[144:147]\n"
      "s_mov_b32 s82, s68\n"
      "ds_read_b64_tr_b16 a[116:117], v245 offset:1024\n"
      "ds_read_b64_tr_b16 a[118:119], v245 offset:5120\n"
      "v_mfma_f32_16x16x32_bf16 v[144:147], a[80:83], a[40:43], v[144:147]\n"
      "s_lshr_b32 s69, s86, 1\n"
      "v_exp_f32 v115, v183\n"
      "v_mfma_f32_16x16x32_bf16 v[144:147], a[88:91], a[56:59], v[144:147]\n"
      "s_add_u32 s66, s66, s69\n"
      "ds_read_b64_tr_b16 a[120:121], v246 offset:1024\n"
      "v_cvt_pk_bf16_f32 v219, v114, v115\n"
      "v_mfma_f32_16x16x32_bf16 v[148:151], a[92:95], a[56:59], v[232:235]\n"
      "s_add_u32 s82, s82, s69\n"
      "v_exp_f32 v112, v184\n"
      "v_mfma_f32_16x16x32_bf16 v[148:151], a[84:87], a[40:43], v[148:151]\n"
      "s_add_u32 s65, s82, s76\n"
      "ds_read_b64_tr_b16 a[122:123], v246 offset:5120\n"
      "v_or3_b32 v116, v116, v218, v219\n"
      "v_mfma_f32_16x16x32_bf16 v[148:151], a[76:79], a[24:27], v[148:151]\n"
      "s_sub_u32 s93, s93, 1\n"
      "v_exp_f32 v113, v185\n"
      "v_mfma_f32_16x16x32_bf16 v[148:151], a[68:71], a[8:11], v[148:151]\n"
      "s_cmp_le_i32 s93, 14\n"
      "ds_read_b64_tr_b16 a[124:125], v247 offset:1024\n"
      "v_cvt_pk_bf16_f32 v220, v112, v113\n"
      "v_mfma_f32_16x16x32_bf16 v[156:159], a[68:71], a[12:15], v[236:239]\n"
      "s_cselect_b32 s86, 107, s86\n"
      "v_exp_f32 v114, v186\n"
      "v_mfma_f32_16x16x32_bf16 v[156:159], a[76:79], a[28:31], v[156:159]\n"
      "s_sub_u32 s67, s65, s66\n"
      "ds_read_b64_tr_b16 a[126:127], v247 offset:5120\n"
      "v_mfma_f32_16x16x32_bf16 v[156:159], a[84:87], a[44:47], v[156:159]\n"
      "s_cmp_lt_i32 s67, 0\n"
      "v_exp_f32 v115, v187\n"
      "v_mfma_f32_16x16x32_bf16 v[156:159], a[92:95], a[60:63], v[156:159]\n"
      "s_cselect_b32 s88, s66, s65\n"
      "v_cvt_pk_bf16_f32 v221, v114, v115\n"
      "v_mfma_f32_16x16x32_bf16 v[152:155], a[88:91], a[60:63], v[236:239]\n"
      "s_add_u32 s90, s88, s91\n"
      "v_exp_f32 v112, v188\n"
      "v_mfma_f32_16x16x32_bf16 v[152:155], a[80:83], a[44:47], v[152:155]\n"
      "v_or3_b32 v116, v116, v220, v221\n"
      "v_mfma_f32_16x16x32_bf16 v[152:155], a[72:75], a[28:31], v[152:155]\n"
      "v_exp_f32 v113, v189\n"
      "v_mfma_f32_16x16x32_bf16 v[152:155], a[64:67], a[12:15], v[152:155]\n"
      "s_waitcnt lgkmcnt(10)\n"
      "v_cvt_pk_bf16_f32 v222, v112, v113\n"
      "v_mfma_f32_16x16x32_bf16 v[32:35], a[96:99], v[192:195], v[32:35]\n"
      "v_exp_f32 v114, v190\n"
      "v_mfma_f32_16x16x32_bf16 v[48:51], a[100:103], v[192:195], v[48:51]\n"
      "v_mfma_f32_16x16x32_bf16 a[128:131], a[104:107], v[192:195], a[128:131]\n"
      "s_waitcnt lgkmcnt(6)\n"
      "v_exp_f32 v115, v191\n"
      "v_mfma_f32_16x16x32_bf16 a[144:147], a[108:111], v[192:195], a[144:147]\n"
      "v_cvt_pk_bf16_f32 v223, v114, v115\n"
      "v_mfma_f32_16x16x32_bf16 v[64:67], a[112:115], v[192:195], v[64:67]\n"
      "s_waitcnt lgkmcnt(2)\n"
      "v_mfma_f32_16x16x32_bf16 v[80:83], a[116:119], v[192:195], v[80:83]\n"
      "v_or3_b32 v116, v116, v222, v223\n"
      "v_mfma_f32_16x16x32_bf16 a[160:163], a[120:123], v[192:195], a[160:163]\n"
      "s_mov_b32 s64, 2\n"
      "s_waitcnt lgkmcnt(0)\n"
      "v_and_b32 v116, 0x40004000, v116\n"
      "v_mfma_f32_16x16x32_bf16 a[176:179], a[124:127], v[192:195], a[176:179]\n"
      "v_cmp_ne_u32 vcc, 0, v116\n"
      "s_cbranch_vccnz FB1_%=\n"
      "RET1_2_%=:\n"
      "v_mfma_f32_16x16x32_bf16 a[180:183], a[124:127], v[196:199], a[180:183]\n"
      "v_exp_f32 v112, v128\n"
      "v_mfma_f32_16x16x32_bf16 a[164:167], a[120:123], v[196:199], a[164:167]\n"
      "v_mfma_f32_16x16x32_bf16 a[192:195], a[196:199], v[208:211], a[192:195]\n"
      "s_add_u32 m0, s77, 16384\n"
      "v_exp_f32 v113, v129\n"
      "v_mfma_f32_16x16x32_bf16 v[84:87], a[116:119], v[196:199], v[84:87]\n"
      "buffer_load_dwordx4 v249, s[24:27], s61 offen nt lds\n"
      "v_cvt_pk_bf16_f32 v192, v112, v113\n"
      "v_mfma_f32_16x16x32_bf16 v[68:71], a[112:115], v[196:199], v[68:71]\n"
      "s_add_u32 m0, s77, 17408\n"
      "v_exp_f32 v114, v130\n"
      "v_mfma_f32_16x16x32_bf16 a[192:195], a[200:203], v[212:215], a[192:195]\n"
      "buffer_load_dwordx4 v121, s[24:27], s61 offen nt lds\n"
      "v_mfma_f32_16x16x32_bf16 a[148:151], a[108:111], v[196:199], a[148:151]\n"
      "s_add_u32 m0, s77, 24576\n"
      "v_exp_f32 v115, v131\n"
      "v_mfma_f32_16x16x32_bf16 a[132:135], a[104:107], v[196:199], a[132:135]\n"
      "s_memrealtime s[68:69]\n"
      "buffer_load_dwordx4 v122, s[24:27], s61 offen nt lds\n"
      "v_cvt_pk_bf16_f32 v193, v114, v115\n"
      "v_mfma_f32_16x16x32_bf16 a[192:195], a[204:207], v[216:219], a[192:195]\n"
      "s_add_u32 m0, s77, 25600\n"
      "v_exp_f32 v112, v132\n"
      "v_mfma_f32_16x16x32_bf16 v[52:55], a[100:103], v[196:199], v[52:55]\n"
      "s_add_u32 s77, s77, 0x8000\n"
      "buffer_load_dwordx4 v123, s[24:27], s61 offen nt lds\n"
      "v_or_b32 v116, v192, v193\n"
      "v_mfma_f32_16x16x32_bf16 v[36:39], a[96:99], v[196:199], v[36:39]\n"
      "s_and_b32 s77, s77, 0x1ffff\n"
      "v_exp_f32 v113, v133\n"
      "v_mfma_f32_16x16x32_bf16 a[192:195], a[208:211], v[220:223], a[192:195]\n"
      "s_add_u32 s61, s61, %[inc]\n"
      "v_cvt_pk_bf16_f32 v194, v112, v113\n"
      "s_waitcnt vmcnt(9) lgkmcnt(0)\n"
      "s_barrier\n"
      "v_mfma_f32_16x16x32_bf16 v[40:43], a[96:99], v[200:203], v[40:43]\n"
      "ds_read_b128 a[64:67], v242 offset:32768\n"
      "v_exp_f32 v114, v134\n"
      "v_mfma_f32_16x16x32_bf16 v[44:47], a[96:99], v[204:207], v[44:47]\n"
      "ds_read_b128 a[68:71], v242 offset:36864\n"
      "ds_read_b128 a[72:75], v243 offset:32768\n"
      "v_mfma_f32_16x16x32_bf16 v[60:63], a[100:103], v[204:207], v[60:63]\n"
      "v_exp_f32 v115, v135\n"
      "v_mfma_f32_16x16x32_bf16 v[56:59], a[100:103], v[200:203], v[56:59]\n"
      "ds_read_b128 a[76:79], v243 offset:36864\n"
      "v_cvt_pk_bf16_f32 v195, v114, v115\n"
      "v_mfma_f32_16x16x32_bf16 a[136:139], a[104:107], v[200:203], a[136:139]\n"
      "v_exp_f32 v112, v136\n"
      "v_mfma_f32_16x16x32_bf16 a[140:143], a[104:107], v[204:207], a[140:143]\n"
      "ds_read_b128 a[80:83], v242 offset:33792\n"
      "v_or3_b32 v116, v116, v194, v195\n"
      "v_mfma_f32_16x16x32_bf16 a[156:159], a[108:111], v[204:207], a[156:159]\n"
      "v_exp_f32 v113, v137\n"
      "v_mfma_f32_16x16x32_bf16 a[152:155], a[108:111], v[200:203], a[152:155]\n"
      "ds_read_b128 a[84:87], v242 offset:37888\n"
      "v_cvt_pk_bf16_f32 v196, v112, v113\n"
      "v_mfma_f32_16x16x32_bf16 v[72:75], a[112:115], v[200:203], v[72:75]\n"
      "v_exp_f32 v114, v138\n"
      "v_mfma_f32_16x16x32_bf16 v[76:79], a[112:115], v[204:207], v[76:79]\n"
      "ds_read_b128 a[88:91], v243 offset:33792\n"
      "ds_read_b128 a[92:95], v243 offset:37888\n"
      "v_mfma_f32_16x16x32_bf16 v[92:95], a[116:119], v[204:207], v[92:95]\n"
      "v_exp_f32 v115, v139\n"
      "v_mfma_f32_16x16x32_bf16 v[88:91], a[116:119], v[200:203], v[88:91]\n"
      "v_cvt_pk_bf16_f32 v197, v114, v115\n"
      "v_mfma_f32_16x16x32_bf16 a[168:171], a[120:123], v[200:203], a[168:171]\n"
      "v_exp_f32 v112, v140\n"
      "v_mfma_f32_16x16x32_bf16 a[172:175], a[120:123], v[204:207], a[172:175]\n"
      "v_or3_b32 v116, v116, v196, v197\n"
      "v_mfma_f32_16x16x32_bf16 a[188:191], a[124:127], v[204:207], a[188:191]\n"
      "v_exp_f32 v113, v141\n"
      "v_mfma_f32_16x16x32_bf16 a[184:187], a[124:127], v[200:203], a[184:187]\n"
      "s_waitcnt lgkmcnt(0)\n"
      "v_cvt_pk_bf16_f32 v198, v112, v113\n"
      "v_mfma_f32_16x16x32_bf16 v[160:163], a[64:67], a[0:3], v[224:227]\n"
      "v_exp_f32 v114, v142\n"
      "v_mfma_f32_16x16x32_bf16 v[160:163], a[72:75], a[16:19], v[160:163]\n"
      "ds_read_b64_tr_b16 a[96:97], v244 offset:32768\n"
      "ds_read_b64_tr_b16 a[98:99], v244 offset:36864\n"
      "v_mfma_f32_16x16x32_bf16 v[160:163], a[80:83], a[32:35], v[160:163]\n"
      "v_exp_f32 v115, v143\n"
      "v_mfma_f32_16x16x32_bf16 v[160:163], a[88:91], a[48:51], v[160:163]\n"
      "ds_read_b64_tr_b16 a[100:101], v245 offset:32768\n"
      "v_cvt_pk_bf16_f32 v199, v114, v115\n"
      "v_mfma_f32_16x16x32_bf16 v[164:167], a[92:95], a[48:51], v[224:227]\n"
      "v_exp_f32 v112, v144\n"
      "v_mfma_f32_16x16x32_bf16 v[164:167], a[84:87], a[32:35], v[164:167]\n"
      "ds_read_b64_tr_b16 a[102:103], v245 offset:36864\n"
      "v_or3_b32 v116, v116, v198, v199\n"
      "v_mfma_f32_16x16x32_bf16 v[164:167], a[76:79], a[16:19], v[164:167]\n"
      "v_exp_f32 v113, v145\n"
      "v_mfma_f32_16x16x32_bf16 v[164:167], a[68:71], a[0:3], v[164:167]\n"
      "ds_read_b64_tr_b16 a[104:105], v246 offset:32768\n"
      "v_cvt_pk_bf16_f32 v200, v112, v113\n"
      "v_mfma_f32_16x16x32_bf16 v[172:175], a[68:71], a[4:7], v[228:231]\n"
      "v_exp_f32 v114, v146\n"
      "v_mfma_f32_16x16x32_bf16 v[172:175], a[76:79], a[20:23], v[172:175]\n"
      "ds_read_b64_tr_b16 a[106:107], v246 offset:36864\n"
      "ds_read_b64_tr_b16 a[108:109], v247 offset:32768\n"
      "v_mfma_f32_16x16x32_bf16 v[172:175], a[84:87], a[36:39], v[172:175]\n"
      "v_exp_f32 v115, v147\n"
      "v_mfma_f32_16x16x32_bf16 v[172:175], a[92:95], a[52:55], v[172:175]\n"
      "ds_read_b64_tr_b16 a[110:111], v247 offset:36864\n"
      "v_cvt_pk_bf16_f32 v201, v114, v115\n"
      "v_mfma_f32_16x16x32_bf16 v[168:171], a[88:91], a[52:55], v[228:231]\n"
      "v_exp_f32 v112, v148\n"
      "v_mfma_f32_16x16x32_bf16 v[168:171], a[80:83], a[36:39], v[168:171]\n"
      "ds_read_b64_tr_b16 a[112:113], v244 offset:33792\n"
      "v_or3_b32 v116, v116, v200, v201\n"
      "v_mfma_f32_16x16x32_bf16 v[168:171], a[72:75], a[20:23], v[168:171]\n"
      "v_exp_f32 v113, v149\n"
      "v_mfma_f32_16x16x32_bf16 v[168:171], a[64:67], a[4:7], v[168:171]\n"
      "ds_read_b64_tr_b16 a[114:115], v244 offset:37888\n"
      "v_cvt_pk_bf16_f32 v202, v112, v113\n"
      "v_mfma_f32_16x16x32_bf16 v[176:179], a[64:67], a[8:11], v[232:235]\n"
      "v_exp_f32 v114, v150\n"
      "v_mfma_f32_16x16x32_bf16 v[176:179], a[72:75], a[24:27], v[176:179]\n"
      "ds_read_b64_tr_b16 a[116:117], v245 offset:33792\n"
      "ds_read_b64_tr_b16 a[118:119], v245 offset:37888\n"
      "v_mfma_f32_16x16x32_bf16 v[176:179], a[80:83], a[40:43], v[176:179]\n"
      "v_exp_f32 v115, v151\n"
      "v_mfma_f32_16x16x32_bf16 v[176:179], a[88:91], a[56:59], v[176:179]\n"
      "ds_read_b64_tr_b16 a[120:121], v246 offset:33792\n"
      "v_cvt_pk_bf16_f32 v203, v114, v115\n"
      "v_mfma_f32_16x16x32_bf16 v[180:183], a[92:95], a[56:59], v[232:235]\n"
      "v_exp_f32 v112, v152\n"
      "v_mfma_f32_16x16x32_bf16 v[180:183], a[84:87], a[40:43], v[180:183]\n"
      "ds_read_b64_tr_b16 a[122:123], v246 offset:37888\n"
      "v_or3_b32 v116, v116, v202, v203\n"
      "v_mfma_f32_16x16x32_bf16 v[180:183], a[76:79], a[24:27], v[180:183]\n"
      "v_exp_f32 v113, v153\n"
      "v_mfma_f32_16x16x32_bf16 v[180:183], a[68:71], a[8:11], v[180:183]\n"
      "ds_read_b64_tr_b16 a[124:125], v247 offset:33792\n"
      "v_cvt_pk_bf16_f32 v204, v112, v113\n"
      "v_mfma_f32_16x16x32_bf16 v[188:191], a[68:71], a[12:15], v[236:239]\n"
      "v_exp_f32 v114, v154\n"
      "v_mfma_f32_16x16x32_bf16 v[188:191], a[76:79], a[28:31], v[188:191]\n"
      "ds_read_b64_tr_b16 a[126:127], v247 offset:37888\n"
      "v_mfma_f32_16x16x32_bf16 v[188:191], a[84:87], a[44:47], v[188:191]\n"
      "v_exp_f32 v115, v155\n"
      "v_mfma_f32_16x16x32_bf16 v[188:191], a[92:95], a[60:63], v[188:191]\n"
      "v_cvt_pk_bf16_f32 v205, v114, v115\n"
      "v_mfma_f32_16x16x32_bf16 v[184:187], a[88:91], a[60:63], v[236:239]\n"
      "v_exp_f32 v112, v156\n"
      "v_mfma_f32_16x16x32_bf16 v[184:187], a[80:83], a[44:47], v[184:187]\n"
      "v_or3_b32 v116, v116, v204, v205\n"
      "v_mfma_f32_16x16x32_bf16 v[184:187], a[72:75], a[28:31], v[184:187]\n"
      "v_exp_f32 v113, v157\n"
      "v_mfma_f32_16x16x32_bf16 v[184:187], a[64:67], a[12:15], v[184:187]\n"
      "s_waitcnt lgkmcnt(10)\n"
      "v_cvt_pk_bf16_f32 v206, v112, v113\n"
      "v_mfma_f32_16x16x32_bf16 v[32:35], a[96:99], v[208:211], v[32:35]\n"
      "v_exp_f32 v114, v158\n"
      "v_mfma_f32_16x16x32_bf16 v[48:51], a[100:103], v[208:211], v[48:51]\n"
      "v_mfma_f32_16x16x32_bf16 a[128:131], a[104:107], v[208:211], a[128:131]\n"
      "s_waitcnt lgkmcnt(6)\n"
      "v_exp_f32 v115, v159\n"
      "v_mfma_f32_16x16x32_bf16 a[144:147], a[108:111], v[208:211], a[144:147]\n"
      "v_cvt_pk_bf16_f32 v207, v114, v115\n"
      "v_mfma_f32_16x16x32_bf16 v[64:67], a[112:115], v[208:211], v[64:67]\n"
      "s_waitcnt lgkmcnt(2)\n"
      "v_mfma_f32_16x16x32_bf16 v[80:83], a[116:119], v[208:211], v[80:83]\n"
      "v_or3_b32 v116, v116, v206, v207\n"
      "v_mfma_f32_16x16x32_bf16 a[160:163], a[120:123], v[208:211], a[160:163]\n"
      "s_mov_b32 s64, 2\n"
      "s_waitcnt lgkmcnt(0)\n"
      "v_and_b32 v116, 0x40004000, v116\n"
      "v_mfma_f32_16x16x32_bf16 a[176:179], a[124:127], v[208:211], a[176:179]\n"
      "v_cmp_ne_u32 vcc, 0, v116\n"
      "s_cbranch_vccnz FB0_%=\n"
      "RET0_2_%=:\n"
      "v_mfma_f32_16x16x32_bf16 a[180:183], a[124:127], v[212:215], a[180:183]\n"
      "v_exp_f32 v112, v160\n"
      "v_mfma_f32_16x16x32_bf16 a[164:167], a[120:123], v[212:215], a[164:167]\n"
      "v_xor_b32 v242, 0x10000, v242\n"
      "v_mfma_f32_16x16x32_bf16 a[192:195], a[196:199], v[192:195], a[192:195]\n"
      "s_add_u32 m0, s62, 0\n"
      "v_exp_f32 v113, v161\n"
      "v_mfma_f32_16x16x32_bf16 v[84:87], a[116:119], v[212:215], v[84:87]\n"
      "buffer_load_dwordx4 v248, s[20:23], s60 offen nt lds\n"
      "v_cvt_pk_bf16_f32 v208, v112, v113\n"
      "v_xor_b32 v243, 0x10000, v243\n"
      "v_mfma_f32_16x16x32_bf16 v[68:71], a[112:115], v[212:215], v[68:71]\n"
      "s_add_u32 m0, s62, 896\n"
      "v_exp_f32 v114, v162\n"
      "v_mfma_f32_16x16x32_bf16 a[192:195], a[200:203], v[196:199], a[192:195]\n"
      "buffer_load_dwordx4 v248, s[20:23], s60 offen offset:128 nt lds\n"
      "v_xor_b32 v244, 0x10000, v244\n"
      "v_mfma_f32_16x16x32_bf16 a[148:151], a[108:111], v[212:215], a[148:151]\n"
      "s_add_u32 m0, s62, 8192\n"
      "v_exp_f32 v115, v163\n"
      "v_mfma_f32_16x16x32_bf16 a[132:135], a[104:107], v[212:215], a[132:135]\n"
      "s_memrealtime s[68:69]\n"
      "buffer_load_dwordx4 v120, s[20:23], s60 offen nt lds\n"
      "v_cvt_pk_bf16_f32 v209, v114, v115\n"
      "v_xor_b32 v245, 0x10000, v245\n"
      "v_mfma_f32_16x16x32_bf16 a[192:195], a[204:207], v[200:203], a[192:195]\n"
      "s_add_u32 m0, s62, 9088\n"
      "v_exp_f32 v112, v164\n"
      "v_mfma_f32_16x16x32_bf16 v[52:55], a[100:103], v[212:215], v[52:55]\n"
      "s_add_u32 s62, s62, 0x8000\n"
      "buffer_load_dwordx4 v120, s[20:23], s60 offen offset:128 nt lds\n"
      "v_or_b32 v116, v208, v209\n"
      "v_xor_b32 v246, 0x10000, v246\n"
      "v_mfma_f32_16x16x32_bf16 v[36:39], a[96:99], v[212:215], v[36:39]\n"
      "s_and_b32 s62, s62, 0x1ffff\n"
      "v_exp_f32 v113, v165\n"
      "v_mfma_f32_16x16x32_bf16 a[192:195], a[208:211], v[204:207], a[192:195]\n"
      "s_add_u32 s60, s60, s78\n"
      "v_cvt_pk_bf16_f32 v210, v112, v113\n"
      "v_xor_b32 v247, 0x10000, v247\n"
      "s_waitcnt vmcnt(8)\n"
      "s_sub_u32 s63, 11, %[n1]\n"
      "LOOP2_%=:\n"
      "s_waitcnt vmcnt(9) lgkmcnt(0)\n"
      "s_sub_u32 s67, s68, s90\n"
      "s_cmp_lt_i32 s67, 0\n"
      "s_cbranch_scc1 GW4_%=\n"
      "GX4_%=:\n"
      "s_barrier\n"
      "v_mfma_f32_16x16x32_bf16 v[40:43], a[96:99], v[216:219], v[40:43]\n"
      "s_memrealtime s[68:69]\n"
      "ds_read_b128 a[64:67], v242 offset:0\n"
      "v_exp_f32 v114, v166\n"
      "v_mfma_f32_16x16x32_bf16 v[44:47], a[96:99], v[220:223], v[44:47]\n"
      "s_add_u32 m0, s77, 16384\n"
      "ds_read_b128 a[68:71], v242 offset:4096\n"
      "ds_read_b128 a[72:75], v243 offset:0\n"
      "v_mfma_f32_16x16x32_bf16 v[60:63], a[100:103], v[220:223], v[60:63]\n"
      "buffer_load_dwordx4 v249, s[24:27], s61 offen nt lds\n"
      "v_exp_f32 v115, v167\n"
      "v_mfma_f32_16x16x32_bf16 v[56:59], a[100:103], v[216:219], v[56:59]\n"
      "s_add_u32 m0, s77, 17408\n"
      "ds_read_b128 a[76:79], v243 offset:4096\n"
      "v_cvt_pk_bf16_f32 v211, v114, v115\n"
      "v_mfma_f32_16x16x32_bf16 a[136:139], a[104:107], v[216:219], a[136:139]\n"
      "buffer_load_dwordx4 v121, s[24:27], s61 offen nt lds\n"
      "v_exp_f32 v112, v168\n"
      "v_mfma_f32_16x16x32_bf16 a[140:143], a[104:107], v[220:223], a[140:143]\n"
      "s_add_u32 m0, s77, 24576\n"
      "ds_read_b128 a[80:83], v242 offset:1024\n"
      "v_or3_b32 v116, v116, v210, v211\n"
      "v_mfma_f32_16x16x32_bf16 a[156:159], a[108:111], v[220:223], a[156:159]\n"
      "buffer_load_dwordx4 v122, s[24:27], s61 offen nt lds\n"
      "v_exp_f32 v113, v169\n"
      "v_mfma_f32_16x16x32_bf16 a[152:155], a[108:111], v[216:219], a[152:155]\n"
      "s_add_u32 m0, s77, 25600\n"
      "ds_read_b128 a[84:87], v242 offset:5120\n"
      "v_cvt_pk_bf16_f32 v212, v112, v113\n"
      "v_mfma_f32_16x16x32_bf16 v[72:75], a[112:115], v[216:219], v[72:75]\n"
      "s_add_u32 s77, s77, 0x8000\n"
      "buffer_load_dwordx4 v123, s[24:27], s61 offen nt lds\n"
      "v_exp_f32 v114, v170\n"
      "v_mfma_f32_16x16x32_bf16 v[76:79], a[112:115], v[220:223], v[76:79]\n"
      "s_and_b32 s77, s77, 0x1ffff\n"
      "ds_read_b128 a[88:91], v243 offset:1024\n"
      "ds_read_b128 a[92:95], v243 offset:5120\n"
      "v_mfma_f32_16x16x32_bf16 v[92:95], a[116:119], v[220:223], v[92:95]\n"
      "s_add_u32 s61, s61, s78\n"
      "v_exp_f32 v115, v171\n"
      "v_mfma_f32_16x16x32_bf16 v[88:91], a[116:119], v[216:219], v[88:91]\n"
      "v_cvt_pk_bf16_f32 v213, v114, v115\n"
      "v_mfma_f32_16x16x32_bf16 a[168:171], a[120:123], v[216:219], a[168:171]\n"
      "v_exp_f32 v112, v172\n"
      "v_mfma_f32_16x16x32_bf16 a[172:175], a[120:123], v[220:223], a[172:175]\n"
      "v_or3_b32 v116, v116, v212, v213\n"
      "v_mfma_f32_16x16x32_bf16 a[188:191], a[124:127], v[220:223], a[188:191]\n"
      "v_exp_f32 v113, v173\n"
      "v_mfma_f32_16x16x32_bf16 a[184:187], a[124:127], v[216:219], a[184:187]\n"
      "s_waitcnt lgkmcnt(0)\n"
      "v_cvt_pk_bf16_f32 v214, v112, v113\n"
      "v_mfma_f32_16x16x32_bf16 v[128:131], a[64:67], a[0:3], v[224:227]\n"
      "s_sub_u32 s67, s68, s66\n"
      "v_exp_f32 v114, v174\n"
      "v_mfma_f32_16x16x32_bf16 v[128:131], a[72:75], a[16:19], v[128:131]\n"
      "s_cmp_gt_i32 s67, s70\n"
      "v_mfma_f32_16x16x32_bf16 v[128:131], a[80:83], a[32:35], v[128:131]\n"
      "s_cselect_b32 s69, s86, 0\n"
      "v_exp_f32 v115, v175\n"
      "v_mfma_f32_16x16x32_bf16 v[128:131], a[88:91], a[48:51], v[128:131]\n"
      "s_add_u32 s66, s66, s69\n"
      "v_cvt_pk_bf16_f32 v215, v114, v115\n"
      "v_mfma_f32_16x16x32_bf16 v[132:135], a[92:95], a[48:51], v[224:227]\n"
      "s_add_u32 s67, s67, s101\n"
      "v_exp_f32 v112, v176\n"
      "v_mfma_f32_16x16x32_bf16 v[132:135], a[84:87], a[32:35], v[132:135]\n"
      "s_min_i32 s70, s70, s67\n"
      "v_or3_b32 v116, v116, v214, v215\n"
      "v_mfma_f32_16x16x32_bf16 v[132:135], a[76:79], a[16:19], v[132:135]\n"
      "s_max_i32 s70, s70, 85\n"
      "v_exp_f32 v113, v177\n"
      "v_mfma_f32_16x16x32_bf16 v[132:135], a[68:71], a[0:3], v[132:135]\n"
      "s_add_u32 s66, s66, s86\n"
      "v_cvt_pk_bf16_f32 v216, v112, v113\n"
      "v_mfma_f32_16x16x32_bf16 v[140:143], a[68:71], a[4:7], v[228:231]\n"
      "s_and_b32 s76, s80, 31\n"
      "v_exp_f32 v114, v178\n"
      "v_mfma_f32_16x16x32_bf16 v[140:143], a[76:79], a[20:23], v[140:143]\n"
      "s_lshr_b64 s[80:81], s[80:81], 5\n"
      "v_mfma_f32_16x16x32_bf16 v[140:143], a[84:87], a[36:39], v[140:143]\n"
      "s_add_u32 s66, s66, s76\n"
      "v_exp_f32 v115, v179\n"
      "v_mfma_f32_16x16x32_bf16 v[140:143], a[92:95], a[52:55], v[140:143]\n"
      "s_sub_u32 s67, s68, s82\n"
      "v_cvt_pk_bf16_f32 v217, v114, v115\n"
      "v_mfma_f32_16x16x32_bf16 v[136:139], a[88:91], a[52:55], v[228:231]\n"
      "s_min_i32 s76, s67, s83\n"
      "v_exp_f32 v112, v180\n"
      "s_waitcnt vmcnt(8)\n"
      "s_barrier\n"
      "v_mfma_f32_16x16x32_bf16 v[136:139], a[80:83], a[36:39], v[136:139]\n"
      "s_mov_b32 s83, s67\n"
      "ds_read_b64_tr_b16 a[96:97], v244 offset:0\n"
      "v_or3_b32 v116, v116, v216, v217\n"
      "v_mfma_f32_16x16x32_bf16 v[136:139], a[72:75], a[20:23], v[136:139]\n"
      "s_sub_u32 s76, s76, 36\n"
      "v_exp_f32 v113, v181\n"
      "v_mfma_f32_16x16x32_bf16 v[136:139], a[64:67], a[4:7], v[136:139]\n"
      "s_min_i32 s76, s76, 148\n"
      "ds_read_b64_tr_b16 a[98:99], v244 offset:4096\n"
      "v_cvt_pk_bf16_f32 v218, v112, v113\n"
      "v_mfma_f32_16x16x32_bf16 v[144:147], a[64:67], a[8:11], v[232:235]\n"
      "s_max_i32 s76, s76, 70\n"
      "v_exp_f32 v114, v182\n"
      "v_mfma_f32_16x16x32_bf16 v[144:147], a[72:75], a[24:27], v[144:147]\n"
      "s_mov_b32 s82, s68\n"
      "ds_read_b64_tr_b16 a[100:101], v245 offset:0\n"
      "ds_read_b64_tr_b16 a[102:103], v245 offset:4096\n"
      "v_mfma_f32_16x16x32_bf16 v[144:147], a[80:83], a[40:43], v[144:147]\n"
      "s_add_u32 s65, s82, s76\n"
      "v_exp_f32 v115, v183\n"
      "v_mfma_f32_16x16x32_bf16 v[144:147], a[88:91], a[56:59], v[144:147]\n"
      "s_sub_u32 s93, s93, 1\n"
      "ds_read_b64_tr_b16 a[104:105], v246 offset:0\n"
      "v_cvt_pk_bf16_f32 v219, v114, v115\n"
      "v_mfma_f32_16x16x32_bf16 v[148:151], a[92:95], a[56:59], v[232:235]\n"
      "s_cmp_le_i32 s93, 14\n"
      "v_exp_f32 v112, v184\n"
      "v_mfma_f32_16x16x32_bf16 v[148:151], a[84:87], a[40:43], v[148:151]\n"
      "s_cselect_b32 s86, 107, s86\n"
      "ds_read_b64_tr_b16 a[106:107], v246 offset:4096\n"
      "v_or3_b32 v116, v116, v218, v219\n"
      "v_mfma_f32_16x16x32_bf16 v[148:151], a[76:79], a[24:27], v[148:151]\n"
      "s_sub_u32 s67, s65, s66\n"
      "v_exp_f32 v113, v185\n"
      "v_mfma_f32_16x16x32_bf16 v[148:151], a[68:71], a[8:11], v[148:151]\n"
      "s_cmp_lt_i32 s67, 0\n"
      "ds_read_b64_tr_b16 a[108:109], v247 offset:0\n"
      "v_cvt_pk_bf16_f32 v220, v112, v113\n"
      "v_mfma_f32_16x16x32_bf16 v[156:159], a[68:71], a[12:15], v[236:239]\n"
      "s_cselect_b32 s88, s66, s65\n"
      "v_exp_f32 v114, v186\n"
      "v_mfma_f32_16x16x32_bf16 v[156:159], a[76:79], a[28:31], v[156:159]\n"
      "s_add_u32 s90, s88, s91\n"
      "ds_read_b64_tr_b16 a[110:111], v247 offset:4096\n"
      "ds_read_b64_tr_b16 a[112:113], v244 offset:1024\n"
      "v_mfma_f32_16x16x32_bf16 v[156:159], a[84:87], a[44:47], v[156:159]\n"
      "v_exp_f32 v115, v187\n"
      "v_mfma_f32_16x16x32_bf16 v[156:159], a[92:95], a[60:63], v[156:159]\n"
      "ds_read_b64_tr_b16 a[114:115], v244 offset:5120\n"
      "v_cvt_pk_bf16_f32 v221, v114, v115\n"
      "v_mfma_f32_16x16x32_bf16 v[152:155], a[88:91], a[60:63], v[236:239]\n"
      "v_exp_f32 v112, v188\n"
      "v_mfma_f32_16x16x32_bf16 v[152:155], a[80:83], a[44:47], v[152:155]\n"
      "ds_read_b64_tr_b16 a[116:117], v245 offset:1024\n"
      "v_or3_b32 v116, v116, v220, v221\n"
      "v_mfma_f32_16x16x32_bf16 v[152:155], a[72:75], a[28:31], v[152:155]\n"
      "v_exp_f32 v113, v189\n"
      "v_mfma_f32_16x16x32_bf16 v[152:155], a[64:67], a[12:15], v[152:155]\n"
      "ds_read_b64_tr_b16 a[118:119], v245 offset:5120\n"
      "v_cvt_pk_bf16_f32 v222, v112, v113\n"
      "s_waitcnt lgkmcnt(6)\n"
      "v_mfma_f32_16x16x32_bf16 v[32:35], a[96:99], v[192:195], v[32:35]\n"
      "v_exp_f32 v114, v190\n"
      "v_mfma_f32_16x16x32_bf16 v[48:51], a[100:103], v[192:195], v[48:51]\n"
      "ds_read_b64_tr_b16 a[120:121], v246 offset:1024\n"
      "ds_read_b64_tr_b16 a[122:123], v246 offset:5120\n"
      "v_mfma_f32_16x16x32_bf16 a[128:131], a[104:107], v[192:195], a[128:131]\n"
      "s_waitcnt lgkmcnt(4)\n"
      "v_exp_f32 v115, v191\n"
      "v_mfma_f32_16x16x32_bf16 a[144:147], a[108:111], v[192:195], a[144:147]\n"
      "ds_read_b64_tr_b16 a[124:125], v247 offset:1024\n"
      "v_cvt_pk_bf16_f32 v223, v114, v115\n"
      "v_mfma_f32_16x16x32_bf16 v[64:67], a[112:115], v[192:195], v[64:67]\n"
      "ds_read_b64_tr_b16 a[126:127], v247 offset:5120\n"
      "s_waitcnt lgkmcnt(2)\n"
      "v_mfma_f32_16x16x32_bf16 v[80:83], a[116:119], v[192:195], v[80:83]\n"
      "v_or3_b32 v116, v116, v222, v223\n"
      "v_mfma_f32_16x16x32_bf16 a[160:163], a[120:123], v[192:195], a[160:163]\n"
      "s_mov_b32 s64, 3\n"
      "s_waitcnt lgkmcnt(0)\n"
      "v_and_b32 v116, 0x40004000, v116\n"
      "v_mfma_f32_16x16x32_bf16 a[176:179], a[124:127], v[192:195], a[176:179]\n"
      "v_cmp_ne_u32 vcc, 0, v116\n"
      "s_cbranch_vccnz FB1_%=\n"
      "RET1_3_%=:\n"
      "v_mfma_f32_16x16x32_bf16 a[180:183], a[124:127], v[196:199], a[180:183]\n"
      "v_exp_f32 v112, v128\n"
      "v_mfma_f32_16x16x32_bf16 a[164:167], a[120:123], v[196:199], a[164:167]\n"
      "v_mfma_f32_16x16x32_bf16 a[192:195], a[196:199], v[208:211], a[192:195]\n"
      "s_add_u32 m0, s62, 0\n"
      "v_exp_f32 v113, v129\n"
      "v_mfma_f32_16x16x32_bf16 v[84:87], a[116:119], v[196:199], v[84:87]\n"
      "buffer_load_dwordx4 v248, s[20:23], s60 offen nt lds\n"
      "v_cvt_pk_bf16_f32 v192, v112, v113\n"
      "v_mfma_f32_16x16x32_bf16 v[68:71], a[112:115], v[196:199], v[68:71]\n"
      "s_add_u32 m0, s62, 896\n"
      "v_exp_f32 v114, v130\n"
      "v_mfma_f32_16x16x32_bf16 a[192:195], a[200:203], v[212:215], a[192:195]\n"
      "buffer_load_dwordx4 v248, s[20:23], s60 offen offset:128 nt lds\n"
      "v_mfma_f32_16x16x32_bf16 a[148:151], a[108:111], v[196:199], a[148:151]\n"
      "s_add_u32 m0, s62, 8192\n"
      "v_exp_f32 v115, v131\n"
      "v_mfma_f32_16x16x32_bf16 a[132:135], a[104:107], v[196:199], a[132:135]\n"
      "s_memrealtime s[68:69]\n"
      "buffer_load_dwordx4 v120, s[20:23], s60 offen nt lds\n"
      "v_cvt_pk_bf16_f32 v193, v114, v115\n"
      "v_mfma_f32_16x16x32_bf16 a[192:195], a[204:207], v[216:219], a[192:195]\n"
      "s_add_u32 m0, s62, 9088\n"
      "v_exp_f32 v112, v132\n"
      "v_mfma_f32_16x16x32_bf16 v[52:55], a[100:103], v[196:199], v[52:55]\n"
      "s_add_u32 s62, s62, 0x8000\n"
      "buffer_load_dwordx4 v120, s[20:23], s60 offen offset:128 nt lds\n"
      "v_or_b32 v116, v192, v193\n"
      "v_mfma_f32_16x16x32_bf16 v[36:39], a[96:99], v[196:199], v[36:39]\n"
      "s_and_b32 s62, s62, 0x1ffff\n"
      "v_exp_f32 v113, v133\n"
      "v_mfma_f32_16x16x32_bf16 a[192:195], a[208:211], v[220:223], a[192:195]\n"
      "s_add_u32 s60, s60, %[inc]\n"
      "v_cvt_pk_bf16_f32 v194, v112, v113\n"
      "s_waitcnt vmcnt(8) lgkmcnt(0)\n"
      "s_sub_u32 s67, s68, s90\n"
      "s_cmp_lt_i32 s67, 0\n"
      "s_cbranch_scc1 GW5_%=\n"
      "GX5_%=:\n"
      "s_barrier\n"
      "v_mfma_f32_16x16x32_bf16 v[40:43], a[96:99], v[200:203], v[40:43]\n"
      "s_memrealtime s[68:69]\n"
      "buffer_load_dword v250, v251, s[28:31], 0 offen sc1\n"
      "ds_read_b128 a[64:67], v242 offset:32768\n"
      "v_exp_f32 v114, v134\n"
      "v_mfma_f32_16x16x32_bf16 v[44:47], a[96:99], v[204:207], v[44:47]\n"
      "s_add_u32 m0, s77, 16384\n"
      "ds_read_b128 a[68:71], v242 offset:36864\n"
      "ds_read_b128 a[72:75], v243 offset:32768\n"
      "v_mfma_f32_16x16x32_bf16 v[60:63], a[100:103], v[204:207], v[60:63]\n"
      "buffer_load_dwordx4 v249, s[24:27], s61 offen nt lds\n"
      "v_exp_f32 v115, v135\n"
      "v_mfma_f32_16x16x32_bf16 v[56:59], a[100:103], v[200:203], v[56:59]\n"
      "s_add_u32 m0, s77, 17408\n"
      "ds_read_b128 a[76:79], v243 offset:36864\n"
      "v_cvt_pk_bf16_f32 v195, v114, v115\n"
      "v_mfma_f32_16x16x32_bf16 a[136:139], a[104:107], v[200:203], a[136:139]\n"
      "buffer_load_dwordx4 v121, s[24:27], s61 offen nt lds\n"
      "v_exp_f32 v112, v136\n"
      "v_mfma_f32_16x16x32_bf16 a[140:143], a[104:107], v[204:207], a[140:143]\n"
      "s_add_u32 m0, s77, 24576\n"
      "ds_read_b128 a[80:83], v242 offset:33792\n"
      "v_or3_b32 v116, v116, v194, v195\n"
      "v_mfma_f32_16x16x32_bf16 a[156:159], a[108:111], v[204:207], a[156:159]\n"
      "buffer_load_dwordx4 v122, s[24:27], s61 offen nt lds\n"
      "v_exp_f32 v113, v137\n"
      "v_mfma_f32_16x16x32_bf16 a[152:155], a[108:111], v[200:203], a[152:155]\n"
      "s_add_u32 m0, s77, 25600\n"
      "ds_read_b128 a[84:87], v242 offset:37888\n"
      "v_cvt_pk_bf16_f32 v196, v112, v113\n"
      "v_mfma_f32_16x16x32_bf16 v[72:75], a[112:115], v[200:203], v[72:75]\n"
      "s_add_u32 s77, s77, 0x8000\n"
      "buffer_load_dwordx4 v123, s[24:27], s61 offen nt lds\n"
      "v_exp_f32 v114, v138\n"
      "v_mfma_f32_16x16x32_bf16 v[76:79], a[112:115], v[204:207], v[76:79]\n"
      "s_and_b32 s77, s77, 0x1ffff\n"
      "ds_read_b128 a[88:91], v243 offset:33792\n"
      "ds_read_b128 a[92:95], v243 offset:37888\n"
      "v_mfma_f32_16x16x32_bf16 v[92:95], a[116:119], v[204:207], v[92:95]\n"
      "s_add_u32 s61, s61, %[inc]\n"
      "v_exp_f32 v115, v139\n"
      "v_mfma_f32_16x16x32_bf16 v[88:91], a[116:119], v[200:203], v[88:91]\n"
      "v_cvt_pk_bf16_f32 v197, v114, v115\n"
      "v_mfma_f32_16x16x32_bf16 a[168:171], a[120:123], v[200:203], a[168:171]\n"
      "v_exp_f32 v112, v140\n"
      "v_mfma_f32_16x16x32_bf16 a[172:175], a[120:123], v[204:207], a[172:175]\n"
      "v_or3_b32 v116, v116, v196, v197\n"
      "v_mfma_f32_16x16x32_bf16 a[188:191], a[124:127], v[204:207], a[188:191]\n"
      "v_exp_f32 v113, v141\n"
      "v_mfma_f32_16x16x32_bf16 a[184:187], a[124:127], v[200:203], a[184:187]\n"
      "s_waitcnt lgkmcnt(0)\n"
      "v_cvt_pk_bf16_f32 v198, v112, v113\n"
      "v_mfma_f32_16x16x32_bf16 v[160:163], a[64:67], a[0:3], v[224:227]\n"
      "s_sub_u32 s67, s68, s66\n"
      "v_exp_f32 v114, v142\n"
      "v_mfma_f32_16x16x32_bf16 v[160:163], a[72:75], a[16:19], v[160:163]\n"
      "s_cmp_gt_i32 s67, s70\n"
      "v_mfma_f32_16x16x32_bf16 v[160:163], a[80:83], a[32:35], v[160:163]\n"
      "s_cselect_b32 s69, s86, 0\n"
      "v_exp_f32 v115, v143\n"
      "v_mfma_f32_16x16x32_bf16 v[160:163], a[88:91], a[48:51], v[160:163]\n"
      "s_add_u32 s66, s66, s69\n"
      "v_cvt_pk_bf16_f32 v199, v114, v115\n"
      "v_mfma_f32_16x16x32_bf16 v[164:167], a[92:95], a[48:51], v[224:227]\n"
      "s_add_u32 s67, s67, s101\n"
      "v_exp_f32 v112, v144\n"
      "v_mfma_f32_16x16x32_bf16 v[164:167], a[84:87], a[32:35], v[164:167]\n"
      "s_min_i32 s70, s70, s67\n"
      "v_or3_b32 v116, v116, v198, v199\n"
      "v_mfma_f32_16x16x32_bf16 v[164:167], a[76:79], a[16:19], v[164:167]\n"
      "s_max_i32 s70, s70, 85\n"
      "v_exp_f32 v113, v145\n"
      "v_mfma_f32_16x16x32_bf16 v[164:167], a[68:71], a[0:3], v[164:167]\n"
      "s_add_u32 s66, s66, s86\n"
      "v_cvt_pk_bf16_f32 v200, v112, v113\n"
      "v_mfma_f32_16x16x32_bf16 v[172:175], a[68:71], a[4:7], v[228:231]\n"
      "s_and_b32 s76, s80, 31\n"
      "v_exp_f32 v114, v146\n"
      "v_mfma_f32_16x16x32_bf16 v[172:175], a[76:79], a[20:23], v[172:175]\n"
      "s_lshr_b64 s[80:81], s[80:81], 5\n"
      "v_mfma_f32_16x16x32_bf16 v[172:175], a[84:87], a[36:39], v[172:175]\n"
      "s_add_u32 s66, s66, s76\n"
      "v_exp_f32 v115, v147\n"
      "v_mfma_f32_16x16x32_bf16 v[172:175], a[92:95], a[52:55], v[172:175]\n"
      "s_sub_u32 s67, s68, s82\n"
      "v_cvt_pk_bf16_f32 v201, v114, v115\n"
      "v_mfma_f32_16x16x32_bf16 v[168:171], a[88:91], a[52:55], v[228:231]\n"
      "s_min_i32 s76, s67, s83\n"
      "v_exp_f32 v112, v148\n"
      "s_waitcnt vmcnt(9)\n"
      "s_barrier\n"
      "v_mfma_f32_16x16x32_bf16 v[168:171], a[80:83], a[36:39], v[168:171]\n"
      "s_mov_b32 s83, s67\n"
      "ds_read_b64_tr_b16 a[96:97], v244 offset:32768\n"
      "v_or3_b32 v116, v116, v200, v201\n"
      "v_mfma_f32_16x16x32_bf16 v[168:171], a[72:75], a[20:23], v[168:171]\n"
      "s_sub_u32 s76, s76, 36\n"
      "v_exp_f32 v113, v149\n"
      "v_mfma_f32_16x16x32_bf16 v[168:171], a[64:67], a[4:7], v[168:171]\n"
      "s_min_i32 s76, s76, 148\n"
      "ds_read_b64_tr_b16 a[98:99], v244 offset:36864\n"
      "v_cvt_pk_bf16_f32 v202, v112, v113\n"
      "v_mfma_f32_16x16x32_bf16 v[176:179], a[64:67], a[8:11], v[232:235]\n"
      "s_max_i32 s76, s76, 70\n"
      "v_exp_f32 v114, v150\n"
      "v_mfma_f32_16x16x32_bf16 v[176:179], a[72:75], a[24:27], v[176:179]\n"
      "s_mov_b32 s82, s68\n"
      "ds_read_b64_tr_b16 a[100:101], v245 offset:32768\n"
      "ds_read_b64_tr_b16 a[102:103], v245 offset:36864\n"
      "v_mfma_f32_16x16x32_bf16 v[176:179], a[80:83], a[40:43], v[176:179]\n"
      "s_add_u32 s65, s82, s76\n"
      "v_exp_f32 v115, v151\n"
      "v_mfma_f32_16x16x32_bf16 v[176:179], a[88:91], a[56:59], v[176:179]\n"
      "s_sub_u32 s93, s93, 1\n"
      "ds_read_b64_tr_b16 a[104:105], v246 offset:32768\n"
      "v_cvt_pk_bf16_f32 v203, v114, v115\n"
      "v_mfma_f32_16x16x32_bf16 v[180:183], a[92:95], a[56:59], v[232:235]\n"
      "s_cmp_le_i32 s93, 14\n"
      "v_exp_f32 v112, v152\n"
      "v_mfma_f32_16x16x32_bf16 v[180:183], a[84:87], a[40:43], v[180:183]\n"
      "s_cselect_b32 s86, 107, s86\n"
      "ds_read_b64_tr_b16 a[106:107], v246 offset:36864\n"
      "v_or3_b32 v116, v116, v202, v203\n"
      "v_mfma_f32_16x16x32_bf16 v[180:183], a[76:79], a[24:27], v[180:183]\n"
      "s_sub_u32 s67, s65, s66\n"
      "v_exp_f32 v113, v153\n"
      "v_mfma_f32_16x16x32_bf16 v[180:183], a[68:71], a[8:11], v[180:183]\n"
      "s_cmp_lt_i32 s67, 0\n"
      "ds_read_b64_tr_b16 a[108:109], v247 offset:32768\n"
      "v_cvt_pk_bf16_f32 v204, v112, v113\n"
      "v_mfma_f32_16x16x32_bf16 v[188:191], a[68:71], a[12:15], v[236:239]\n"
      "s_cselect_b32 s88, s66, s65\n"
      "v_exp_f32 v114, v154\n"
      "v_mfma_f32_16x16x32_bf16 v[188:191], a[76:79], a[28:31], v[188:191]\n"
      "s_add_u32 s90, s88, s91\n"
      "ds_read_b64_tr_b16 a[110:111], v247 offset:36864\n"
      "ds_read_b64_tr_b16 a[112:113], v244 offset:33792\n"
      "v_mfma_f32_16x16x32_bf16 v[188:191], a[84:87], a[44:47], v[188:191]\n"
      "v_exp_f32 v115, v155\n"
      "v_mfma_f32_16x16x32_bf16 v[188:191], a[92:95], a[60:63], v[188:191]\n"
      "ds_read_b64_tr_b16 a[114:115], v244 offset:37888\n"
      "v_cvt_pk_bf16_f32 v205, v114, v115\n"
      "v_mfma_f32_16x16x32_bf16 v[184:187], a[88:91], a[60:63], v[236:239]\n"
      "v_exp_f32 v112, v156\n"
      "v_mfma_f32_16x16x32_bf16 v[184:187], a[80:83], a[44:47], v[184:187]\n"
      "ds_read_b64_tr_b16 a[116:117], v245 offset:33792\n"
      "v_or3_b32 v116, v116, v204, v205\n"
      "v_mfma_f32_16x16x32_bf16 v[184:187], a[72:75], a[28:31], v[184:187]\n"
      "v_exp_f32 v113, v157\n"
      "v_mfma_f32_16x16x32_bf16 v[184:187], a[64:67], a[12:15], v[184:187]\n"
      "ds_read_b64_tr_b16 a[118:119], v245 offset:37888\n"
      "v_cvt_pk_bf16_f32 v206, v112, v113\n"
      "s_waitcnt lgkmcnt(6)\n"
      "v_mfma_f32_16x16x32_bf16 v[32:35], a[96:99], v[208:211], v[32:35]\n"
      "v_exp_f32 v114, v158\n"
      "v_mfma_f32_16x16x32_bf16 v[48:51], a[100:103], v[208:211], v[48:51]\n"
      "ds_read_b64_tr_b16 a[120:121], v246 offset:33792\n"
      "ds_read_b64_tr_b16 a[122:123], v246 offset:37888\n"
      "v_mfma_f32_16x16x32_bf16 a[128:131], a[104:107], v[208:211], a[128:131]\n"
      "s_waitcnt lgkmcnt(4)\n"
      "v_exp_f32 v115, v159\n"
      "v_mfma_f32_16x16x32_bf16 a[144:147], a[108:111], v[208:211], a[144:147]\n"
      "ds_read_b64_tr_b16 a[124:125], v247 offset:33792\n"
      "v_cvt_pk_bf16_f32 v207, v114, v115\n"
      "v_mfma_f32_16x16x32_bf16 v[64:67], a[112:115], v[208:211], v[64:67]\n"
      "ds_read_b64_tr_b16 a[126:127], v247 offset:37888\n"
      "s_waitcnt lgkmcnt(2)\n"
      "v_mfma_f32_16x16x32_bf16 v[80:83], a[116:119], v[208:211], v[80:83]\n"
      "v_or3_b32 v116, v116, v206, v207\n"
      "v_mfma_f32_16x16x32_bf16 a[160:163], a[120:123], v[208:211], a[160:163]\n"
      "s_mov_b32 s64, 3\n"
      "s_waitcnt lgkmcnt(0)\n"
      "v_and_b32 v116, 0x40004000, v116\n"
      "v_mfma_f32_16x16x32_bf16 a[176:179], a[124:127], v[208:211], a[176:179]\n"
      "v_cmp_ne_u32 vcc, 0, v116\n"
      "s_cbranch_vccnz FB0_%=\n"
      "RET0_3_%=:\n"
      "v_mfma_f32_16x16x32_bf16 a[180:183], a[124:127], v[212:215], a[180:183]\n"
      "v_exp_f32 v112, v160\n"
      "v_mfma_f32_16x16x32_bf16 a[164:167], a[120:123], v[212:215], a[164:167]\n"
      "v_xor_b32 v242, 0x10000, v242\n"
      "v_mfma_f32_16x16x32_bf16 a[192:195], a[196:199], v[192:195], a[192:195]\n"
      "s_add_u32 m0, s62, 0\n"
      "v_exp_f32 v113, v161\n"
      "v_mfma_f32_16x16x32_bf16 v[84:87], a[116:119], v[212:215], v[84:87]\n"
      "buffer_load_dwordx4 v248, s[20:23], s60 offen nt lds\n"
      "v_cvt_pk_bf16_f32 v208, v112, v113\n"
      "v_xor_b32 v243, 0x10000, v243\n"
      "v_mfma_f32_16x16x32_bf16 v[68:71], a[112:115], v[212:215], v[68:71]\n"
      "s_add_u32 m0, s62, 896\n"
      "v_exp_f32 v114, v162\n"
      "v_mfma_f32_16x16x32_bf16 a[192:195], a[200:203], v[196:199], a[192:195]\n"
      "buffer_load_dwordx4 v248, s[20:23], s60 offen offset:128 nt lds\n"
      "v_xor_b32 v244, 0x10000, v244\n"
      "v_mfma_f32_16x16x32_bf16 a[148:151], a[108:111], v[212:215], a[148:151]\n"
      "s_add_u32 m0, s62, 8192\n"
      "v_exp_f32 v115, v163\n"
      "v_mfma_f32_16x16x32_bf16 a[132:135], a[104:107], v[212:215], a[132:135]\n"
      "s_memrealtime s[68:69]\n"
      "buffer_load_dwordx4 v120, s[20:23], s60 offen nt lds\n"
      "v_cvt_pk_bf16_f32 v209, v114, v115\n"
      "v_xor_b32 v245, 0x10000, v245\n"
      "v_mfma_f32_16x16x32_bf16 a[192:195], a[204:207], v[200:203], a[192:195]\n"
      "s_add_u32 m0, s62, 9088\n"
      "v_exp_f32 v112, v164\n"
      "v_mfma_f32_16x16x32_bf16 v[52:55], a[100:103], v[212:215], v[52:55]\n"
      "s_add_u32 s62, s62, 0x8000\n"
      "buffer_load_dwordx4 v120, s[20:23], s60 offen offset:128 nt lds\n"
      "v_or_b32 v116, v208, v209\n"
      "v_xor_b32 v246, 0x10000, v246\n"
      "v_mfma_f32_16x16x32_bf16 v[36:39], a[96:99], v[212:215], v[36:39]\n"
      "s_and_b32 s62, s62, 0x1ffff\n"
      "s_add_u32 s60, s60, s78\n"
      "v_exp_f32 v113, v165\n"
      "v_mfma_f32_16x16x32_bf16 a[192:195], a[208:211], v[204:207], a[192:195]\n"
      "s_sub_u32 s63, s63, 1\n"
      "v_cvt_pk_bf16_f32 v210, v112, v113\n"
      "v_xor_b32 v247, 0x10000, v247\n"
      "s_cbranch_scc0 LOOP2_%=\n"
      "s_waitcnt vmcnt(9) lgkmcnt(0)\n"
      "s_sub_u32 s67, s68, s90\n"
      "s_cmp_lt_i32 s67, 0\n"
      "s_cbranch_scc1 GW6_%=\n"
      "GX6_%=:\n"
      "s_barrier\n"
      "v_mfma_f32_16x16x32_bf16 v[40:43], a[96:99], v[216:219], v[40:43]\n"
      "s_memrealtime s[68:69]\n"
      "ds_read_b128 a[64:67], v242 offset:0\n"
      "v_exp_f32 v114, v166\n"
      "v_mfma_f32_16x16x32_bf16 v[44:47], a[96:99], v[220:223], v[44:47]\n"
      "s_add_u32 m0, %[wb], 50176\n"
      "ds_read_b128 a[68:71], v242 offset:4096\n"
      "ds_read_b128 a[72:75], v243 offset:0\n"
      "v_mfma_f32_16x16x32_bf16 v[60:63], a[100:103], v[220:223], v[60:63]\n"
      "s_add_u32 s94, s61, s78\n"
      "buffer_load_dwordx4 v121, s[24:27], s61 offen nt lds\n"
      "v_exp_f32 v115, v167\n"
      "v_mfma_f32_16x16x32_bf16 v[56:59], a[100:103], v[216:219], v[56:59]\n"
      "s_add_u32 m0, %[wb], 58368\n"
      "ds_read_b128 a[76:79], v243 offset:4096\n"
      "v_cvt_pk_bf16_f32 v211, v114, v115\n"
      "v_mfma_f32_16x16x32_bf16 a[136:139], a[104:107], v[216:219], a[136:139]\n"
      "s_add_u32 s95, s61, 0x10000\n"
      "buffer_load_dwordx4 v123, s[24:27], s61 offen nt lds\n"
      "v_exp_f32 v112, v168\n"
      "v_mfma_f32_16x16x32_bf16 a[140:143], a[104:107], v[220:223], a[140:143]\n"
      "s_add_u32 s77, s94, 0x10000\n"
      "ds_read_b128 a[80:83], v242 offset:1024\n"
      "v_or3_b32 v116, v116, v210, v211\n"
      "v_mfma_f32_16x16x32_bf16 a[156:159], a[108:111], v[220:223], a[156:159]\n"
      "s_add_u32 s78, s61, 0x20000\n"
      "v_exp_f32 v113, v169\n"
      "v_mfma_f32_16x16x32_bf16 a[152:155], a[108:111], v[216:219], a[152:155]\n"
      "ds_read_b128 a[84:87], v242 offset:5120\n"
      "v_cvt_pk_bf16_f32 v212, v112, v113\n"
      "v_mfma_f32_16x16x32_bf16 v[72:75], a[112:115], v[216:219], v[72:75]\n"
      "v_exp_f32 v114, v170\n"
      "v_mfma_f32_16x16x32_bf16 v[76:79], a[112:115], v[220:223], v[76:79]\n"
      "ds_read_b128 a[88:91], v243 offset:1024\n"
      "ds_read_b128 a[92:95], v243 offset:5120\n"
      "v_mfma_f32_16x16x32_bf16 v[92:95], a[116:119], v[220:223], v[92:95]\n"
      "v_exp_f32 v115, v171\n"
      "v_mfma_f32_16x16x32_bf16 v[88:91], a[116:119], v[216:219], v[88:91]\n"
      "v_cvt_pk_bf16_f32 v213, v114, v115\n"
      "v_mfma_f32_16x16x32_bf16 a[168:171], a[120:123], v[216:219], a[168:171]\n"
      "v_exp_f32 v112, v172\n"
      "v_mfma_f32_16x16x32_bf16 a[172:175], a[120:123], v[220:223], a[172:175]\n"
      "v_or3_b32 v116, v116, v212, v213\n"
      "v_mfma_f32_16x16x32_bf16 a[188:191], a[124:127], v[220:223], a[188:191]\n"
      "v_exp_f32 v113, v173\n"
      "v_mfma_f32_16x16x32_bf16 a[184:187], a[124:127], v[216:219], a[184:187]\n"
      "s_waitcnt lgkmcnt(0)\n"
      "v_cvt_pk_bf16_f32 v214, v112, v113\n"
      "v_mfma_f32_16x16x32_bf16 v[128:131], a[64:67], a[0:3], v[224:227]\n"
      "s_sub_u32 s67, s68, s66\n"
      "v_exp_f32 v114, v174\n"
      "v_mfma_f32_16x16x32_bf16 v[128:131], a[72:75], a[16:19], v[128:131]\n"
      "s_cmp_gt_i32 s67, s70\n"
      "v_mfma_f32_16x16x32_bf16 v[128:131], a[80:83], a[32:35], v[128:131]\n"
      "s_cselect_b32 s69, s86, 0\n"
      "v_exp_f32 v115, v175\n"
      "v_mfma_f32_16x16x32_bf16 v[128:131], a[88:91], a[48:51], v[128:131]\n"
      "s_add_u32 s66, s66, s69\n"
      "v_cvt_pk_bf16_f32 v215, v114, v115\n"
      "v_mfma_f32_16x16x32_bf16 v[132:135], a[92:95], a[48:51], v[224:227]\n"
      "s_add_u32 s67, s67, s101\n"
      "v_exp_f32 v112, v176\n"
      "v_mfma_f32_16x16x32_bf16 v[132:135], a[84:87], a[32:35], v[132:135]\n"
      "s_min_i32 s70, s70, s67\n"
      "v_or3_b32 v116, v116, v214, v215\n"
      "v_mfma_f32_16x16x32_bf16 v[132:135], a[76:79], a[16:19], v[132:135]\n"
      "s_max_i32 s70, s70, 85\n"
      "v_exp_f32 v113, v177\n"
      "v_mfma_f32_16x16x32_bf16 v[132:135], a[68:71], a[0:3], v[132:135]\n"
      "s_add_u32 s66, s66, s86\n"
      "v_cvt_pk_bf16_f32 v216, v112, v113\n"
      "v_mfma_f32_16x16x32_bf16 v[140:143], a[68:71], a[4:7], v[228:231]\n"
      "s_and_b32 s76, s80, 31\n"
      "v_exp_f32 v114, v178\n"
      "v_mfma_f32_16x16x32_bf16 v[140:143], a[76:79], a[20:23], v[140:143]\n"
      "s_lshr_b64 s[80:81], s[80:81], 5\n"
      "v_mfma_f32_16x16x32_bf16 v[140:143], a[84:87], a[36:39], v[140:143]\n"
      "s_add_u32 s66, s66, s76\n"
      "v_exp_f32 v115, v179\n"
      "v_mfma_f32_16x16x32_bf16 v[140:143], a[92:95], a[52:55], v[140:143]\n"
      "s_sub_u32 s67, s68, s82\n"
      "v_cvt_pk_bf16_f32 v217, v114, v115\n"
      "v_mfma_f32_16x16x32_bf16 v[136:139], a[88:91], a[52:55], v[228:231]\n"
      "s_min_i32 s76, s67, s83\n"
      "v_exp_f32 v112, v180\n"
      "s_waitcnt vmcnt(6)\n"
      "s_barrier\n"
      "v_mfma_f32_16x16x32_bf16 v[136:139], a[80:83], a[36:39], v[136:139]\n"
      "s_mov_b32 s83, s67\n"
      "ds_read_b64_tr_b16 a[96:97], v244 offset:0\n"
      "v_or3_b32 v116, v116, v216, v217\n"
      "v_mfma_f32_16x16x32_bf16 v[136:139], a[72:75], a[20:23], v[136:139]\n"
      "s_add_u32 m0, s62, 0\n"
      "v_exp_f32 v113, v181\n"
      "v_mfma_f32_16x16x32_bf16 v[136:139], a[64:67], a[4:7], v[136:139]\n"
      "s_sub_u32 s76, s76, 36\n"
      "ds_read_b64_tr_b16 a[98:99], v244 offset:4096\n"
      "buffer_load_dwordx4 v248, s[20:23], s60 offen nt lds\n"
      "v_cvt_pk_bf16_f32 v218, v112, v113\n"
      "v_mfma_f32_16x16x32_bf16 v[144:147], a[64:67], a[8:11], v[232:235]\n"
      "s_add_u32 m0, s62, 896\n"
      "v_exp_f32 v114, v182\n"
      "v_mfma_f32_16x16x32_bf16 v[144:147], a[72:75], a[24:27], v[144:147]\n"
      "s_min_i32 s76, s76, 148\n"
      "ds_read_b64_tr_b16 a[100:101], v245 offset:0\n"
      "ds_read_b64_tr_b16 a[102:103], v245 offset:4096\n"
      "buffer_load_dwordx4 v248, s[20:23], s60 offen offset:128 nt lds\n"
      "v_mfma_f32_16x16x32_bf16 v[144:147], a[80:83], a[40:43], v[144:147]\n"
      "s_add_u32 m0, s62, 8192\n"
      "v_exp_f32 v115, v183\n"
      "v_mfma_f32_16x16x32_bf16 v[144:147], a[88:91], a[56:59], v[144:147]\n"
      "s_max_i32 s76, s76, 70\n"
      "ds_read_b64_tr_b16 a[104:105], v246 offset:0\n"
      "buffer_load_dwordx4 v120, s[20:23], s60 offen nt lds\n"
      "v_cvt_pk_bf16_f32 v219, v114, v115\n"
      "v_mfma_f32_16x16x32_bf16 v[148:151], a[92:95], a[56:59], v[232:235]\n"
      "s_add_u32 m0, s62, 9088\n"
      "v_exp_f32 v112, v184\n"
      "v_mfma_f32_16x16x32_bf16 v[148:151], a[84:87], a[40:43], v[148:151]\n"
      "s_add_u32 s62, s62, 0x8000\n"
      "ds_read_b64_tr_b16 a[106:107], v246 offset:4096\n"
      "buffer_load_dwordx4 v120, s[20:23], s60 offen offset:128 nt lds\n"
      "v_or3_b32 v116, v116, v218, v219\n"
      "v_mfma_f32_16x16x32_bf16 v[148:151], a[76:79], a[24:27], v[148:151]\n"
      "s_and_b32 s62, s62, 0x1ffff\n"
      "v_exp_f32 v113, v185\n"
      "v_mfma_f32_16x16x32_bf16 v[148:151], a[68:71], a[8:11], v[148:151]\n"
      "s_add_u32 s60, s60, %[inc]\n"
      "ds_read_b64_tr_b16 a[108:109], v247 offset:0\n"
      "v_cvt_pk_bf16_f32 v220, v112, v113\n"
      "v_mfma_f32_16x16x32_bf16 v[156:159], a[68:71], a[12:15], v[236:239]\n"
      "s_mov_b32 s82, s68\n"
      "v_exp_f32 v114, v186\n"
      "v_mfma_f32_16x16x32_bf16 v[156:159], a[76:79], a[28:31], v[156:159]\n"
      "s_sub_u32 s66, s66, s99\n"
      "ds_read_b64_tr_b16 a[110:111], v247 offset:4096\n"
      "ds_read_b64_tr_b16 a[112:113], v244 offset:1024\n"
      "v_mfma_f32_16x16x32_bf16 v[156:159], a[84:87], a[44:47], v[156:159]\n"
      "s_sub_u32 s82, s82, s99\n"
      "v_exp_f32 v115, v187\n"
      "v_mfma_f32_16x16x32_bf16 v[156:159], a[92:95], a[60:63], v[156:159]\n"
      "s_add_u32 s65, s82, s76\n"
      "ds_read_b64_tr_b16 a[114:115], v244 offset:5120\n"
      "v_cvt_pk_bf16_f32 v221, v114, v115\n"
      "v_mfma_f32_16x16x32_bf16 v[152:155], a[88:91], a[60:63], v[236:239]\n"
      "s_sub_u32 s93, s93, 1\n"
      "v_exp_f32 v112, v188\n"
      "v_mfma_f32_16x16x32_bf16 v[152:155], a[80:83], a[44:47], v[152:155]\n"
      "s_cmp_le_i32 s93, 14\n"
      "ds_read_b64_tr_b16 a[116:117], v245 offset:1024\n"
      "v_or3_b32 v116, v116, v220, v221\n"
      "v_mfma_f32_16x16x32_bf16 v[152:155], a[72:75], a[28:31], v[152:155]\n"
      "s_cselect_b32 s86, 107, s86\n"
      "v_exp_f32 v113, v189\n"
      "v_mfma_f32_16x16x32_bf16 v[152:155], a[64:67], a[12:15], v[152:155]\n"
      "ds_read_b64_tr_b16 a[118:119], v245 offset:5120\n"
      "v_cvt_pk_bf16_f32 v222, v112, v113\n"
      "s_waitcnt lgkmcnt(6)\n"
      "v_mfma_f32_16x16x32_bf16 v[32:35], a[96:99], v[192:195], v[32:35]\n"
      "s_sub_u32 s67, s65, s66\n"
      "v_exp_f32 v114, v190\n"
      "v_mfma_f32_16x16x32_bf16 v[48:51], a[100:103], v[192:195], v[48:51]\n"
      "s_cmp_lt_i32 s67, 0\n"
      "s_cselect_b32 s88, s66, s65\n"
      "ds_read_b64_tr_b16 a[120:121], v246 offset:1024\n"
      "ds_read_b64_tr_b16 a[122:123], v246 offset:5120\n"
      "v_mfma_f32_16x16x32_bf16 a[128:131], a[104:107], v[192:195], a[128:131]\n"
      "s_waitcnt lgkmcnt(4)\n"
      "v_exp_f32 v115, v191\n"
      "v_mfma_f32_16x16x32_bf16 a[144:147], a[108:111], v[192:195], a[144:147]\n"
      "s_add_u32 s90, s88, s91\n"
      "ds_read_b64_tr_b16 a[124:125], v247 offset:1024\n"
      "v_cvt_pk_bf16_f32 v223, v114, v115\n"
      "v_mfma_f32_16x16x32_bf16 v[64:67], a[112:115], v[192:195], v[64:67]\n"
      "ds_read_b64_tr_b16 a[126:127], v247 offset:5120\n"
      "s_waitcnt lgkmcnt(2)\n"
      "v_mfma_f32_16x16x32_bf16 v[80:83], a[116:119], v[192:195], v[80:83]\n"
      "v_or3_b32 v116, v116, v222, v223\n"
      "v_mfma_f32_16x16x32_bf16 a[160:163], a[120:123], v[192:195], a[160:163]\n"
      "s_mov_b32 s64, 4\n"
      "s_waitcnt lgkmcnt(0)\n"
      "v_and_b32 v116, 0x40004000, v116\n"
      "v_mfma_f32_16x16x32_bf16 a[176:179], a[124:127], v[192:195], a[176:179]\n"
      "v_cmp_ne_u32 vcc, 0, v116\n"
      "s_cbranch_vccnz FB1_%=\n"
      "RET1_4_%=:\n"
      "v_mfma_f32_16x16x32_bf16 a[180:183], a[124:127], v[196:199], a[180:183]\n"
      "v_exp_f32 v112, v128\n"
      "v_mfma_f32_16x16x32_bf16 a[164:167], a[120:123], v[196:199], a[164:167]\n"
      "v_xor_b32 v240, 0x10000, v244\n"
      "v_mfma_f32_16x16x32_bf16 a[192:195], a[196:199], v[208:211], a[192:195]\n"
      "v_exp_f32 v113, v129\n"
      "v_mfma_f32_16x16x32_bf16 v[84:87], a[116:119], v[196:199], v[84:87]\n"
      "v_cvt_pk_bf16_f32 v192, v112, v113\n"
      "v_xor_b32 v241, 0x10000, v245\n"
      "v_mfma_f32_16x16x32_bf16 v[68:71], a[112:115], v[196:199], v[68:71]\n"
      "v_exp_f32 v114, v130\n"
      "v_mfma_f32_16x16x32_bf16 a[192:195], a[200:203], v[212:215], a[192:195]\n"
      "v_xor_b32 v250, 0x10000, v246\n"
      "v_mfma_f32_16x16x32_bf16 a[148:151], a[108:111], v[196:199], a[148:151]\n"
      "v_exp_f32 v115, v131\n"
      "v_mfma_f32_16x16x32_bf16 a[132:135], a[104:107], v[196:199], a[132:135]\n"
      "s_memrealtime s[68:69]\n"
      "v_cvt_pk_bf16_f32 v193, v114, v115\n"
      "v_xor_b32 v251, 0x10000, v247\n"
      "v_mfma_f32_16x16x32_bf16 a[192:195], a[204:207], v[216:219], a[192:195]\n"
      "v_exp_f32 v112, v132\n"
      "v_mfma_f32_16x16x32_bf16 v[52:55], a[100:103], v[196:199], v[52:55]\n"
      "v_or_b32 v116, v192, v193\n"
      "v_mfma_f32_16x16x32_bf16 v[36:39], a[96:99], v[196:199], v[36:39]\n"
      "v_exp_f32 v113, v133\n"
      "v_mfma_f32_16x16x32_bf16 a[192:195], a[208:211], v[220:223], a[192:195]\n"
      "v_cvt_pk_bf16_f32 v194, v112, v113\n"
      "s_waitcnt vmcnt(6) lgkmcnt(0)\n"
      "s_sub_u32 s67, s68, s90\n"
      "s_cmp_lt_i32 s67, 0\n"
      "s_cbranch_scc1 GW7_%=\n"
      "GX7_%=:\n"
      "s_barrier\n"
      "v_mfma_f32_16x16x32_bf16 v[40:43], a[96:99], v[200:203], v[40:43]\n"
      "s_memrealtime s[68:69]\n"
      "ds_read_b128 a[64:67], v242 offset:32768\n"
      "v_exp_f32 v114, v134\n"
      "v_mfma_f32_16x16x32_bf16 v[44:47], a[96:99], v[204:207], v[44:47]\n"
      "s_add_u32 m0, %[wb], 82944\n"
      "ds_read_b128 a[68:71], v242 offset:36864\n"
      "v_mfma_f32_16x16x32_bf16 v[60:63], a[100:103], v[204:207], v[60:63]\n"
      "ds_read_b128 a[72:75], v243 offset:32768\n"
      "buffer_load_dwordx4 v121, s[24:27], s94 offen nt lds\n"
      "v_mfma_f32_16x16x32_bf16 v[56:59], a[100:103], v[200:203], v[56:59]\n"
      "s_add_u32 m0, %[wb], 91136\n"
      "ds_read_b128 a[76:79], v243 offset:36864\n"
      "v_mfma_f32_16x16x32_bf16 a[136:139], a[104:107], v[200:203], a[136:139]\n"
      "ds_read_b128 a[80:83], v242 offset:33792\n"
      "buffer_load_dwordx4 v123, s[24:27], s94 offen nt lds\n"
      "v_mfma_f32_16x16x32_bf16 a[140:143], a[104:107], v[204:207], a[140:143]\n"
      "ds_read_b128 a[84:87], v242 offset:37888\n"
      "v_mfma_f32_16x16x32_bf16 a[156:159], a[108:111], v[204:207], a[156:159]\n"
      "ds_read_b128 a[88:91], v243 offset:33792\n"
      "v_mfma_f32_16x16x32_bf16 a[152:155], a[108:111], v[200:203], a[152:155]\n"
      "ds_read_b128 a[92:95], v243 offset:37888\n"
      "v_mfma_f32_16x16x32_bf16 v[72:75], a[112:115], v[200:203], v[72:75]\n"
      "v_exp_f32 v115, v135\n"
      "v_mfma_f32_16x16x32_bf16 v[76:79], a[112:115], v[204:207], v[76:79]\n"
      "v_exp_f32 v112, v136\n"
      "v_mfma_f32_16x16x32_bf16 v[92:95], a[116:119], v[204:207], v[92:95]\n"
      "v_cvt_pk_bf16_f32 v195, v114, v115\n"
      "v_exp_f32 v113, v137\n"
      "v_mfma_f32_16x16x32_bf16 v[88:91], a[116:119], v[200:203], v[88:91]\n"
      "v_or3_b32 v116, v116, v194, v195\n"
      "v_mfma_f32_16x16x32_bf16 a[168:171], a[120:123], v[200:203], a[168:171]\n"
      "v_exp_f32 v114, v138\n"
      "v_mfma_f32_16x16x32_bf16 a[172:175], a[120:123], v[204:207], a[172:175]\n"
      "v_cvt_pk_bf16_f32 v196, v112, v113\n"
      "v_exp_f32 v115, v139\n"
      "v_mfma_f32_16x16x32_bf16 a[188:191], a[124:127], v[204:207], a[188:191]\n"
      "v_exp_f32 v112, v140\n"
      "v_mfma_f32_16x16x32_bf16 a[184:187], a[124:127], v[200:203], a[184:187]\n"
      "s_waitcnt lgkmcnt(0)\n"
      "v_cvt_pk_bf16_f32 v197, v114, v115\n"
      "v_mfma_f32_16x16x32_bf16 v[160:163], a[64:67], a[0:3], v[224:227]\n"
      "s_sub_u32 s67, s68, s66\n"
      "v_exp_f32 v113, v141\n"
      "v_mfma_f32_16x16x32_bf16 v[168:171], a[64:67], a[4:7], v[228:231]\n"
      "s_cmp_gt_i32 s67, s70\n"
      "v_or3_b32 v116, v116, v196, v197\n"
      "v_exp_f32 v114, v142\n"
      "v_mfma_f32_16x16x32_bf16 v[176:179], a[64:67], a[8:11], v[232:235]\n"
      "s_cselect_b32 s69, s86, 0\n"
      "v_cvt_pk_bf16_f32 v198, v112, v113\n"
      "v_mfma_f32_16x16x32_bf16 v[184:187], a[64:67], a[12:15], v[236:239]\n"
      "s_add_u32 s66, s66, s69\n"
      "v_exp_f32 v115, v143\n"
      "v_mfma_f32_16x16x32_bf16 v[164:167], a[68:71], a[0:3], v[224:227]\n"
      "s_add_u32 s67, s67, s101\n"
      "v_exp_f32 v112, v144\n"
      "v_mfma_f32_16x16x32_bf16 v[172:175], a[68:71], a[4:7], v[228:231]\n"
      "s_min_i32 s70, s70, s67\n"
      "v_cvt_pk_bf16_f32 v199, v114, v115\n"
      "v_exp_f32 v113, v145\n"
      "v_mfma_f32_16x16x32_bf16 v[180:183], a[68:71], a[8:11], v[232:235]\n"
      "s_max_i32 s70, s70, 85\n"
      "v_or3_b32 v116, v116, v198, v199\n"
      "v_mfma_f32_16x16x32_bf16 v[188:191], a[68:71], a[12:15], v[236:239]\n"
      "s_waitcnt lgkmcnt(4)\n"
      "v_exp_f32 v114, v146\n"
      "v_mfma_f32_16x16x32_bf16 v[160:163], a[72:75], a[16:19], v[160:163]\n"
      "s_add_u32 s66, s66, s86\n"
      "v_cvt_pk_bf16_f32 v200, v112, v113\n"
      "v_exp_f32 v115, v147\n"
      "v_mfma_f32_16x16x32_bf16 v[168:171], a[72:75], a[20:23], v[168:171]\n"
      "s_and_b32 s76, s80, 31\n"
      "v_exp_f32 v112, v148\n"
      "v_mfma_f32_16x16x32_bf16 v[176:179], a[72:75], a[24:27], v[176:179]\n"
      "s_lshr_b64 s[80:81], s[80:81], 5\n"
      "v_cvt_pk_bf16_f32 v201, v114, v115\n"
      "v_mfma_f32_16x16x32_bf16 v[184:187], a[72:75], a[28:31], v[184:187]\n"
      "s_add_u32 s66, s66, s76\n"
      "v_exp_f32 v113, v149\n"
      "v_mfma_f32_16x16x32_bf16 v[164:167], a[76:79], a[16:19], v[164:167]\n"
      "s_sub_u32 s67, s68, s82\n"
      "v_or3_b32 v116, v116, v200, v201\n"
      "v_exp_f32 v114, v150\n"
      "v_mfma_f32_16x16x32_bf16 v[172:175], a[76:79], a[20:23], v[172:175]\n"
      "s_min_i32 s76, s67, s83\n"
      "v_cvt_pk_bf16_f32 v202, v112, v113\n"
      "v_mfma_f32_16x16x32_bf16 v[180:183], a[76:79], a[24:27], v[180:183]\n"
      "s_add_u32 m0, s62, 0\n"
      "v_exp_f32 v115, v151\n"
      "v_mfma_f32_16x16x32_bf16 v[188:191], a[76:79], a[28:31], v[188:191]\n"
      "buffer_load_dwordx4 v248, s[20:23], s60 offen nt lds\n"
      "s_waitcnt lgkmcnt(2)\n"
      "v_mfma_f32_16x16x32_bf16 v[160:163], a[80:83], a[32:35], v[160:163]\n"
      "s_add_u32 m0, s62, 896\n"
      "v_exp_f32 v112, v152\n"
      "v_mfma_f32_16x16x32_bf16 v[168:171], a[80:83], a[36:39], v[168:171]\n"
      "s_mov_b32 s83, s67\n"
      "buffer_load_dwordx4 v248, s[20:23], s60 offen offset:128 nt lds\n"
      "v_mfma_f32_16x16x32_bf16 v[176:179], a[80:83], a[40:43], v[176:179]\n"
      "s_add_u32 m0, s62, 8192\n"
      "v_mfma_f32_16x16x32_bf16 v[184:187], a[80:83], a[44:47], v[184:187]\n"
      "s_sub_u32 s76, s76, 36\n"
      "buffer_load_dwordx4 v120, s[20:23], s60 offen nt lds\n"
      "s_waitcnt vmcnt(9)\n"
      "s_barrier\n"
      "v_mfma_f32_16x16x32_bf16 v[164:167], a[84:87], a[32:35], v[164:167]\n"
      "s_add_u32 m0, s62, 9088\n"
      "ds_read_b64_tr_b16 a[96:97], v246 offset:33792\n"
      "v_mfma_f32_16x16x32_bf16 v[172:175], a[84:87], a[36:39], v[172:175]\n"
      "s_min_i32 s76, s76, 148\n"
      "ds_read_b64_tr_b16 a[98:99], v246 offset:37888\n"
      "buffer_load_dwordx4 v120, s[20:23], s60 offen offset:128 nt lds\n"
      "v_mfma_f32_16x16x32_bf16 v[180:183], a[84:87], a[40:43], v[180:183]\n"
      "s_max_i32 s76, s76, 70\n"
      "ds_read_b64_tr_b16 a[100:101], v247 offset:33792\n"
      "v_mfma_f32_16x16x32_bf16 v[188:191], a[84:87], a[44:47], v[188:191]\n"
      "ds_read_b64_tr_b16 a[102:103], v247 offset:37888\n"
      "s_waitcnt lgkmcnt(4)\n"
      "v_mfma_f32_16x16x32_bf16 v[160:163], a[88:91], a[48:51], v[160:163]\n"
      "s_mov_b32 s82, s68\n"
      "ds_read_b64_tr_b16 a[104:105], v244 offset:33792\n"
      "v_cvt_pk_bf16_f32 v203, v114, v115\n"
      "v_mfma_f32_16x16x32_bf16 v[168:171], a[88:91], a[52:55], v[168:171]\n"
      "s_sub_u32 s66, s66, s99\n"
      "ds_read_b64_tr_b16 a[106:107], v244 offset:37888\n"
      "v_exp_f32 v113, v153\n"
      "v_mfma_f32_16x16x32_bf16 v[176:179], a[88:91], a[56:59], v[176:179]\n"
      "s_sub_u32 s82, s82, s99\n"
      "ds_read_b64_tr_b16 a[108:109], v245 offset:33792\n"
      "v_mfma_f32_16x16x32_bf16 v[184:187], a[88:91], a[60:63], v[184:187]\n"
      "s_add_u32 s65, s82, s76\n"
      "ds_read_b64_tr_b16 a[110:111], v245 offset:37888\n"
      "v_or3_b32 v116, v116, v202, v203\n"
      "v_mfma_f32_16x16x32_bf16 v[164:167], a[92:95], a[48:51], v[164:167]\n"
      "s_sub_u32 s93, s93, 1\n"
      "v_exp_f32 v114, v154\n"
      "v_mfma_f32_16x16x32_bf16 v[172:175], a[92:95], a[52:55], v[172:175]\n"
      "s_cmp_le_i32 s93, 14\n"
      "v_cvt_pk_bf16_f32 v204, v112, v113\n"
      "v_exp_f32 v115, v155\n"
      "v_mfma_f32_16x16x32_bf16 v[180:183], a[92:95], a[56:59], v[180:183]\n"
      "s_cselect_b32 s86, 107, s86\n"
      "v_exp_f32 v112, v156\n"
      "v_mfma_f32_16x16x32_bf16 v[188:191], a[92:95], a[60:63], v[188:191]\n"
      "s_waitcnt lgkmcnt(4)\n"
      "v_cvt_pk_bf16_f32 v205, v114, v115\n"
      "v_mfma_f32_16x16x32_bf16 a[160:163], a[96:99], v[208:211], a[160:163]\n"
      "s_sub_u32 s67, s65, s66\n"
      "v_exp_f32 v113, v157\n"
      "v_mfma_f32_16x16x32_bf16 a[164:167], a[96:99], v[212:215], a[164:167]\n"
      "s_cmp_lt_i32 s67, 0\n"
      "v_or3_b32 v116, v116, v204, v205\n"
      "v_exp_f32 v114, v158\n"
      "v_mfma_f32_16x16x32_bf16 a[168:171], a[96:99], v[216:219], a[168:171]\n"
      "s_cselect_b32 s88, s66, s65\n"
      "v_cvt_pk_bf16_f32 v206, v112, v113\n"
      "v_mfma_f32_16x16x32_bf16 a[172:175], a[96:99], v[220:223], a[172:175]\n"
      "s_add_u32 s90, s88, s91\n"
      "v_exp_f32 v115, v159\n"
      "s_nop 0\n"
      "v_mfma_f32_16x16x32_bf16 a[176:179], a[100:103], v[208:211], a[176:179]\n"
      "s_mov_b32 s64, 4\n"
      "v_cvt_pk_bf16_f32 v207, v114, v115\n"
      "v_or3_b32 v116, v116, v206, v207\n"
      "v_mfma_f32_16x16x32_bf16 a[180:183], a[100:103], v[212:215], a[180:183]\n"
      "v_and_b32 v116, 0x40004000, v116\n"
      "v_cmp_ne_u32 vcc, 0, v116\n"
      "s_cbranch_vccnz FB0_%=\n"
      "RET0_4_%=:\n"
      "v_mfma_f32_16x16x32_bf16 a[184:187], a[100:103], v[216:219], a[184:187]\n"
      "v_exp_f32 v112, v160\n"
      "v_mfma_f32_16x16x32_bf16 a[188:191], a[100:103], v[220:223], a[188:191]\n"
      "s_memrealtime s[68:69]\n"
      "v_xor_b32 v242, 0x10000, v242\n"
      "v_mfma_f32_16x16x32_bf16 a[192:195], a[196:199], v[192:195], a[192:195]\n"
      "v_exp_f32 v113, v161\n"
      "v_mfma_f32_16x16x32_bf16 a[192:195], a[200:203], v[196:199], a[192:195]\n"
      "v_xor_b32 v243, 0x10000, v243\n"
      "v_exp_f32 v114, v162\n"
      "v_mfma_f32_16x16x32_bf16 a[192:195], a[204:207], v[200:203], a[192:195]\n"
      "v_cvt_pk_bf16_f32 v0, v112, v113\n"
      "v_mfma_f32_16x16x32_bf16 a[192:195], a[208:211], v[204:207], a[192:195]\n"
      "v_exp_f32 v115, v163\n"
      "s_waitcnt vmcnt(6) lgkmcnt(0)\n"
      "s_sub_u32 s67, s68, s90\n"
      "s_cmp_lt_i32 s67, 0\n"
      "s_cbranch_scc1 GW8_%=\n"
      "GX8_%=:\n"
      "s_barrier\n"
      "v_mfma_f32_16x16x32_bf16 v[64:67], a[104:107], v[208:211], v[64:67]\n"
      "ds_read_b128 a[64:67], v242 offset:0\n"
      "ds_read_b128 a[68:71], v242 offset:4096\n"
      "v_mfma_f32_16x16x32_bf16 v[68:71], a[104:107], v[212:215], v[68:71]\n"
      "s_add_u32 m0, %[wb], 115712\n"
      "ds_read_b128 a[72:75], v243 offset:0\n"
      "ds_read_b128 a[76:79], v243 offset:4096\n"
      "v_mfma_f32_16x16x32_bf16 v[72:75], a[104:107], v[216:219], v[72:75]\n"
      "ds_read_b128 a[80:83], v242 offset:1024\n"
      "ds_read_b128 a[84:87], v242 offset:5120\n"
      "buffer_load_dwordx4 v121, s[24:27], s95 offen nt lds\n"
      "v_mfma_f32_16x16x32_bf16 v[76:79], a[104:107], v[220:223], v[76:79]\n"
      "s_add_u32 m0, %[wb], 123904\n"
      "ds_read_b128 a[88:91], v243 offset:1024\n"
      "ds_read_b128 a[92:95], v243 offset:5120\n"
      "v_mfma_f32_16x16x32_bf16 v[80:83], a[108:111], v[208:211], v[80:83]\n"
      "buffer_load_dwordx4 v123, s[24:27], s95 offen nt lds\n"
      "v_mfma_f32_16x16x32_bf16 v[84:87], a[108:111], v[212:215], v[84:87]\n"
      "s_add_u32 m0, %[wb], 17408\n"
      "v_mfma_f32_16x16x32_bf16 v[88:91], a[108:111], v[216:219], v[88:91]\n"
      "buffer_load_dwordx4 v121, s[24:27], s77 offen nt lds\n"
      "v_mfma_f32_16x16x32_bf16 v[92:95], a[108:111], v[220:223], v[92:95]\n"
      "s_waitcnt lgkmcnt(6)\n"
      "s_add_u32 m0, %[wb], 25600\n"
      "v_exp_f32 v112, v164\n"
      "v_mfma_f32_16x16x32_bf16 v[128:131], a[64:67], a[0:3], v[224:227]\n"
      "buffer_load_dwordx4 v123, s[24:27], s77 offen nt lds\n"
      "v_mfma_f32_16x16x32_bf16 v[136:139], a[64:67], a[4:7], v[228:231]\n"
      "v_cvt_pk_bf16_f32 v1, v114, v115\n"
      "v_exp_f32 v113, v165\n"
      "v_mfma_f32_16x16x32_bf16 v[144:147], a[64:67], a[8:11], v[232:235]\n"
      "v_or_b32 v116, v0, v1\n"
      "v_mfma_f32_16x16x32_bf16 v[152:155], a[64:67], a[12:15], v[236:239]\n"
      "v_exp_f32 v114, v166\n"
      "v_cvt_pk_bf16_f32 v2, v112, v113\n"
      "v_mfma_f32_16x16x32_bf16 v[132:135], a[68:71], a[0:3], v[224:227]\n"
      "v_exp_f32 v115, v167\n"
      "v_mfma_f32_16x16x32_bf16 v[140:143], a[68:71], a[4:7], v[228:231]\n"
      "v_exp_f32 v112, v168\n"
      "v_cvt_pk_bf16_f32 v3, v114, v115\n"
      "v_mfma_f32_16x16x32_bf16 v[148:151], a[68:71], a[8:11], v[232:235]\n"
      "v_exp_f32 v113, v169\n"
      "v_or3_b32 v116, v116, v2, v3\n"
      "v_mfma_f32_16x16x32_bf16 v[156:159], a[68:71], a[12:15], v[236:239]\n"
      "s_waitcnt lgkmcnt(4)\n"
      "v_exp_f32 v114, v170\n"
      "v_mfma_f32_16x16x32_bf16 v[128:131], a[72:75], a[16:19], v[128:131]\n"
      "v_cvt_pk_bf16_f32 v4, v112, v113\n"
      "v_exp_f32 v115, v171\n"
      "v_mfma_f32_16x16x32_bf16 v[136:139], a[72:75], a[20:23], v[136:139]\n"
      "v_exp_f32 v112, v172\n"
      "v_mfma_f32_16x16x32_bf16 v[144:147], a[72:75], a[24:27], v[144:147]\n"
      "v_cvt_pk_bf16_f32 v5, v114, v115\n"
      "v_exp_f32 v113, v173\n"
      "v_mfma_f32_16x16x32_bf16 v[152:155], a[72:75], a[28:31], v[152:155]\n"
      "v_or3_b32 v116, v116, v4, v5\n"
      "v_mfma_f32_16x16x32_bf16 v[132:135], a[76:79], a[16:19], v[132:135]\n"
      "v_exp_f32 v114, v174\n"
      "v_cvt_pk_bf16_f32 v6, v112, v113\n"
      "v_mfma_f32_16x16x32_bf16 v[140:143], a[76:79], a[20:23], v[140:143]\n"
      "v_exp_f32 v115, v175\n"
      "v_mfma_f32_16x16x32_bf16 v[148:151], a[76:79], a[24:27], v[148:151]\n"
      "v_exp_f32 v112, v176\n"
      "v_cvt_pk_bf16_f32 v7, v114, v115\n"
      "v_mfma_f32_16x16x32_bf16 v[156:159], a[76:79], a[28:31], v[156:159]\n"
      "s_waitcnt lgkmcnt(2)\n"
      "v_exp_f32 v113, v177\n"
      "v_mfma_f32_16x16x32_bf16 v[128:131], a[80:83], a[32:35], v[128:131]\n"
      "v_or3_b32 v116, v116, v6, v7\n"
      "v_exp_f32 v114, v178\n"
      "v_mfma_f32_16x16x32_bf16 v[136:139], a[80:83], a[36:39], v[136:139]\n"
      "v_cvt_pk_bf16_f32 v8, v112, v113\n"
      "v_exp_f32 v115, v179\n"
      "v_mfma_f32_16x16x32_bf16 v[144:147], a[80:83], a[40:43], v[144:147]\n"
      "v_exp_f32 v112, v180\n"
      "v_mfma_f32_16x16x32_bf16 v[152:155], a[80:83], a[44:47], v[152:155]\n"
      "v_cvt_pk_bf16_f32 v9, v114, v115\n"
      "v_exp_f32 v113, v181\n"
      "s_waitcnt vmcnt(8)\n"
      "s_barrier\n"
      "v_mfma_f32_16x16x32_bf16 v[132:135], a[84:87], a[32:35], v[132:135]\n"
      "ds_read_b64_tr_b16 a[112:113], v250 offset:1024\n"
      "v_or3_b32 v116, v116, v8, v9\n"
      "v_mfma_f32_16x16x32_bf16 v[140:143], a[84:87], a[36:39], v[140:143]\n"
      "ds_read_b64_tr_b16 a[114:115], v250 offset:5120\n"
      "v_exp_f32 v114, v182\n"
      "v_mfma_f32_16x16x32_bf16 v[148:151], a[84:87], a[40:43], v[148:151]\n"
      "ds_read_b64_tr_b16 a[116:117], v251 offset:1024\n"
      "v_mfma_f32_16x16x32_bf16 v[156:159], a[84:87], a[44:47], v[156:159]\n"
      "ds_read_b64_tr_b16 a[118:119], v251 offset:5120\n"
      "v_cvt_pk_bf16_f32 v10, v112, v113\n"
      "s_waitcnt lgkmcnt(4)\n"
      "v_mfma_f32_16x16x32_bf16 v[128:131], a[88:91], a[48:51], v[128:131]\n"
      "ds_read_b64_tr_b16 a[120:121], v240 offset:1024\n"
      "v_exp_f32 v115, v183\n"
      "v_mfma_f32_16x16x32_bf16 v[136:139], a[88:91], a[52:55], v[136:139]\n"
      "ds_read_b64_tr_b16 a[122:123], v240 offset:5120\n"
      "v_exp_f32 v112, v184\n"
      "v_mfma_f32_16x16x32_bf16 v[144:147], a[88:91], a[56:59], v[144:147]\n"
      "ds_read_b64_tr_b16 a[124:125], v241 offset:1024\n"
      "v_cvt_pk_bf16_f32 v11, v114, v115\n"
      "v_mfma_f32_16x16x32_bf16 v[152:155], a[88:91], a[60:63], v[152:155]\n"
      "ds_read_b64_tr_b16 a[126:127], v241 offset:5120\n"
      "v_exp_f32 v113, v185\n"
      "v_mfma_f32_16x16x32_bf16 v[132:135], a[92:95], a[48:51], v[132:135]\n"
      "v_or3_b32 v116, v116, v10, v11\n"
      "v_exp_f32 v114, v186\n"
      "v_mfma_f32_16x16x32_bf16 v[140:143], a[92:95], a[52:55], v[140:143]\n"
      "v_cvt_pk_bf16_f32 v12, v112, v113\n"
      "v_mfma_f32_16x16x32_bf16 v[148:151], a[92:95], a[56:59], v[148:151]\n"
      "v_exp_f32 v115, v187\n"
      "v_mfma_f32_16x16x32_bf16 v[156:159], a[92:95], a[60:63], v[156:159]\n"
      "s_waitcnt lgkmcnt(4)\n"
      "v_exp_f32 v112, v188\n"
      "v_cvt_pk_bf16_f32 v13, v114, v115\n"
      "v_mfma_f32_16x16x32_bf16 a[160:163], a[112:115], v[192:195], a[160:163]\n"
      "v_exp_f32 v113, v189\n"
      "v_or3_b32 v116, v116, v12, v13\n"
      "v_mfma_f32_16x16x32_bf16 a[164:167], a[112:115], v[196:199], a[164:167]\n"
      "v_exp_f32 v114, v190\n"
      "v_mfma_f32_16x16x32_bf16 a[168:171], a[112:115], v[200:203], a[168:171]\n"
      "v_cvt_pk_bf16_f32 v14, v112, v113\n"
      "v_exp_f32 v115, v191\n"
      "v_mfma_f32_16x16x32_bf16 a[172:175], a[112:115], v[204:207], a[172:175]\n"
      "s_add_u32 m0, %[wb], 50176\n"
      "s_nop 0\n"
      "v_cvt_pk_bf16_f32 v15, v114, v115\n"
      "v_or3_b32 v116, v116, v14, v15\n"
      "v_mfma_f32_16x16x32_bf16 a[176:179], a[116:119], v[192:195], a[176:179]\n"
      "s_mov_b32 s64, 5\n"
      "buffer_load_dwordx4 v121, s[24:27], s78 offen nt lds\n"
      "v_mfma_f32_16x16x32_bf16 a[180:183], a[116:119], v[196:199], a[180:183]\n"
      "s_add_u32 m0, %[wb], 58368\n"
      "v_and_b32 v116, 0x40004000, v116\n"
      "v_cmp_ne_u32 vcc, 0, v116\n"
      "s_cbranch_vccnz FB1_%=\n"
      "RET1_5_%=:\n"
      "v_mfma_f32_16x16x32_bf16 a[184:187], a[116:119], v[200:203], a[184:187]\n"
      "buffer_load_dwordx4 v123, s[24:27], s78 offen nt lds\n"
      "v_mfma_f32_16x16x32_bf16 a[188:191], a[116:119], v[204:207], a[188:191]\n"
      "s_memrealtime s[68:69]\n"
      "v_exp_f32 v112, v128\n"
      "v_exp_f32 v113, v129\n"
      "v_mfma_f32_16x16x32_bf16 a[192:195], a[196:199], v[0:3], a[192:195]\n"
      "v_exp_f32 v114, v130\n"
      "v_mfma_f32_16x16x32_bf16 a[192:195], a[200:203], v[4:7], a[192:195]\n"
      "v_cvt_pk_bf16_f32 v16, v112, v113\n"
      "v_exp_f32 v115, v131\n"
      "v_mfma_f32_16x16x32_bf16 a[192:195], a[204:207], v[8:11], a[192:195]\n"
      "v_exp_f32 v112, v132\n"
      "v_cvt_pk_bf16_f32 v17, v114, v115\n"
      "v_mfma_f32_16x16x32_bf16 a[192:195], a[208:211], v[12:15], a[192:195]\n"
      "v_exp_f32 v113, v133\n"
      "v_or_b32 v116, v16, v17\n"
      "s_add_u32 s72, s72, 0x7f4a7c15\n"
      "s_addc_u32 s76, s79, 0x9e3779b9\n"
      "s_waitcnt vmcnt(6) lgkmcnt(0)\n"
      "s_barrier\n"
      "v_mfma_f32_16x16x32_bf16 v[64:67], a[120:123], v[192:195], v[64:67]\n"
      "s_xor_b32 s80, %[xo], 0x400000\n"
      "ds_read_b128 a[64:67], v242 offset:32768\n"
      "ds_read_b128 a[68:71], v242 offset:36864\n"
      "v_mfma_f32_16x16x32_bf16 v[68:71], a[120:123], v[196:199], v[68:71]\n"
      "s_add_u32 s79, s80, 0x1000\n"
      "ds_read_b128 a[72:75], v243 offset:32768\n"
      "ds_read_b128 a[76:79], v243 offset:36864\n"
      "v_exp_f32 v114, v134\n"
      "v_mfma_f32_16x16x32_bf16 v[72:75], a[120:123], v[200:203], v[72:75]\n"
      "s_add_u32 s81, %[xo], 0x1000\n"
      "ds_read_b128 a[80:83], v242 offset:33792\n"
      "ds_read_b128 a[84:87], v242 offset:37888\n"
      "v_cvt_pk_bf16_f32 v18, v112, v113\n"
      "v_mfma_f32_16x16x32_bf16 v[76:79], a[120:123], v[204:207], v[76:79]\n"
      "s_mov_b32 s63, 0x100000\n"
      "ds_read_b128 a[88:91], v243 offset:33792\n"
      "ds_read_b128 a[92:95], v243 offset:37888\n"
      "v_mfma_f32_16x16x32_bf16 v[80:83], a[124:127], v[192:195], v[80:83]\n"
      "s_mov_b32 s60, 0x928b2f\n"
      "v_exp_f32 v115, v135\n"
      "v_exp_f32 v112, v136\n"
      "v_mfma_f32_16x16x32_bf16 v[84:87], a[124:127], v[196:199], v[84:87]\n"
      "s_mov_b32 s62, 0x9818e9\n"
      "v_cvt_pk_bf16_f32 v19, v114, v115\n"
      "v_exp_f32 v113, v137\n"
      "v_mfma_f32_16x16x32_bf16 v[88:91], a[124:127], v[200:203], v[88:91]\n"
      "s_mov_b32 s70, 0xdd9dc9\n"
      "v_xor_b32 v248, 0x2000, v244\n"
      "v_or3_b32 v116, v116, v18, v19\n"
      "v_mfma_f32_16x16x32_bf16 v[92:95], a[124:127], v[204:207], v[92:95]\n"
      "s_waitcnt lgkmcnt(6)\n"
      "v_exp_f32 v114, v138\n"
      "v_cvt_pk_bf16_f32 v20, v112, v113\n"
      "v_mfma_f32_16x16x32_bf16 v[160:163], a[64:67], a[0:3], v[224:227]\n"
      "s_mov_b32 s71, 0x8ed905\n"
      "v_exp_f32 v115, v139\n"
      "v_mfma_f32_16x16x32_bf16 v[168:171], a[64:67], a[4:7], v[228:231]\n"
      "s_sub_u32 s88, 299, %[cph]\n"
      "v_xor_b32 v120, 0x2000, v245\n"
      "v_exp_f32 v112, v140\n"
      "v_cvt_pk_bf16_f32 v21, v114, v115\n"
      "v_mfma_f32_16x16x32_bf16 v[176:179], a[64:67], a[8:11], v[232:235]\n"
      "s_add_u32 s88, s88, s66\n"
      "v_exp_f32 v113, v141\n"
      "v_mfma_f32_16x16x32_bf16 v[184:187], a[64:67], a[12:15], v[236:239]\n"
      "s_add_u32 m0, %[wb], 49152\n"
      "v_or3_b32 v116, v116, v20, v21\n"
      "v_exp_f32 v114, v142\n"
      "v_mfma_f32_16x16x32_bf16 v[164:167], a[68:71], a[0:3], v[224:227]\n"
      "s_add_u32 s88, s88, s100\n"
      "buffer_load_dwordx4 v249, s[24:27], s61 offen nt lds\n"
      "v_mbcnt_lo_u32_b32 v127, -1, 0\n"
      "v_mfma_f32_16x16x32_bf16 v[172:175], a[68:71], a[4:7], v[228:231]\n"
      "s_add_u32 m0, %[wb], 57344\n"
      "v_cvt_pk_bf16_f32 v22, v112, v113\n"
      "v_mfma_f32_16x16x32_bf16 v[180:183], a[68:71], a[8:11], v[232:235]\n"
      "buffer_load_dwordx4 v122, s[24:27], s61 offen nt lds\n"
      "v_mfma_f32_16x16x32_bf16 v[188:191], a[68:71], a[12:15], v[236:239]\n"
      "s_waitcnt lgkmcnt(4)\n"
      "v_mbcnt_hi_u32_b32 v127, -1, v127\n"
      "v_exp_f32 v115, v143\n"
      "v_mfma_f32_16x16x32_bf16 v[160:163], a[72:75], a[16:19], v[160:163]\n"
      "v_exp_f32 v112, v144\n"
      "v_cvt_pk_bf16_f32 v23, v114, v115\n"
      "v_mfma_f32_16x16x32_bf16 v[168:171], a[72:75], a[20:23], v[168:171]\n"
      "v_exp_f32 v113, v145\n"
      "v_or3_b32 v116, v116, v22, v23\n"
      "v_mfma_f32_16x16x32_bf16 v[176:179], a[72:75], a[24:27], v[176:179]\n"
      "v_lshlrev_b32 v127, 4, v127\n"
      "v_exp_f32 v114, v146\n"
      "v_mfma_f32_16x16x32_bf16 v[184:187], a[72:75], a[28:31], v[184:187]\n"
      "v_cvt_pk_bf16_f32 v24, v112, v113\n"
      "v_exp_f32 v115, v147\n"
      "v_mfma_f32_16x16x32_bf16 v[164:167], a[76:79], a[16:19], v[164:167]\n"
      "v_exp_f32 v112, v148\n"
      "v_cvt_pk_bf16_f32 v25, v114, v115\n"
      "v_mfma_f32_16x16x32_bf16 v[172:175], a[76:79], a[20:23], v[172:175]\n"
      "v_add_u32 v125, %[xw], v127\n"
      "v_exp_f32 v113, v149\n"
      "v_mfma_f32_16x16x32_bf16 v[180:183], a[76:79], a[24:27], v[180:183]\n"
      "v_or3_b32 v116, v116, v24, v25\n"
      "v_exp_f32 v114, v150\n"
      "v_mfma_f32_16x16x32_bf16 v[188:191], a[76:79], a[28:31], v[188:191]\n"
      "s_waitcnt lgkmcnt(2)\n"
      "v_cvt_pk_bf16_f32 v26, v112, v113\n"
      "v_exp_f32 v115, v151\n"
      "v_mfma_f32_16x16x32_bf16 v[160:163], a[80:83], a[32:35], v[160:163]\n"
      "v_add_u32 v126, %[xr], v127\n"
      "v_exp_f32 v112, v152\n"
      "v_mfma_f32_16x16x32_bf16 v[168:171], a[80:83], a[36:39], v[168:171]\n"
      "s_add_u32 m0, %[wb], 81920\n"
      "v_cvt_pk_bf16_f32 v27, v114, v115\n"
      "v_exp_f32 v113, v153\n"
      "v_mfma_f32_16x16x32_bf16 v[176:179], a[80:83], a[40:43], v[176:179]\n"
      "buffer_load_dwordx4 v249, s[24:27], s94 offen nt lds\n"
      "v_mfma_f32_16x16x32_bf16 v[184:187], a[80:83], a[44:47], v[184:187]\n"
      "s_add_u32 m0, %[wb], 90112\n"
      "v_or3_b32 v116, v116, v26, v27\n"
      "v_mfma_f32_16x16x32_bf16 v[164:167], a[84:87], a[32:35], v[164:167]\n"
      "buffer_load_dwordx4 v122, s[24:27], s94 offen nt lds\n"
      "v_mfma_f32_16x16x32_bf16 v[172:175], a[84:87], a[36:39], v[172:175]\n"
      "v_exp_f32 v114, v154\n"
      "v_cvt_pk_bf16_f32 v28, v112, v113\n"
      "v_mfma_f32_16x16x32_bf16 v[180:183], a[84:87], a[40:43], v[180:183]\n"
      "v_exp_f32 v115, v155\n"
      "v_exp_f32 v112, v156\n"
      "v_mov_b32 v123, s88\n"
      "v_mfma_f32_16x16x32_bf16 v[188:191], a[84:87], a[44:47], v[188:191]\n"
      "s_waitcnt lgkmcnt(0)\n"
      "v_cvt_pk_bf16_f32 v29, v114, v115\n"
      "v_exp_f32 v113, v157\n"
      "s_waitcnt vmcnt(6)\n"
      "s_barrier\n"
      "v_mfma_f32_16x16x32_bf16 v[160:163], a[88:91], a[48:51], v[160:163]\n"
      "ds_read_b64_tr_b16 a[96:97], v250 offset:33792\n"
      "ds_read_b64_tr_b16 a[98:99], v250 offset:37888\n"
      "v_mfma_f32_16x16x32_bf16 v[168:171], a[88:91], a[52:55], v[168:171]\n"
      "ds_read_b64_tr_b16 a[100:101], v251 offset:33792\n"
      "ds_read_b64_tr_b16 a[102:103], v251 offset:37888\n"
      "v_or3_b32 v116, v116, v28, v29\n"
      "v_mfma_f32_16x16x32_bf16 v[176:179], a[88:91], a[56:59], v[176:179]\n"
      "ds_read_b64_tr_b16 a[112:113], v246 offset:1024\n"
      "ds_read_b64_tr_b16 a[114:115], v246 offset:5120\n"
      "v_mfma_f32_16x16x32_bf16 v[184:187], a[88:91], a[60:63], v[184:187]\n"
      "ds_read_b64_tr_b16 a[116:117], v247 offset:1024\n"
      "ds_read_b64_tr_b16 a[118:119], v247 offset:5120\n"
      "v_exp_f32 v114, v158\n"
      "v_mfma_f32_16x16x32_bf16 v[164:167], a[92:95], a[48:51], v[164:167]\n"
      "ds_read_b64_tr_b16 a[104:105], v240 offset:33792\n"
      "v_cvt_pk_bf16_f32 v30, v112, v113\n"
      "v_mfma_f32_16x16x32_bf16 v[172:175], a[92:95], a[52:55], v[172:175]\n"
      "s_mov_b32 s64, 5\n"
      "ds_read_b64_tr_b16 a[106:107], v240 offset:37888\n"
      "v_exp_f32 v115, v159\n"
      "s_nop 0\n"
      "v_mfma_f32_16x16x32_bf16 v[180:183], a[92:95], a[56:59], v[180:183]\n"
      "ds_read_b64_tr_b16 a[108:109], v241 offset:33792\n"
      "v_cvt_pk_bf16_f32 v31, v114, v115\n"
      "v_or3_b32 v116, v116, v30, v31\n"
      "v_mfma_f32_16x16x32_bf16 v[188:191], a[92:95], a[60:63], v[188:191]\n"
      "ds_read_b64_tr_b16 a[110:111], v241 offset:37888\n"
      "v_and_b32 v116, 0x40004000, v116\n"
      "v_cmp_ne_u32 vcc, 0, v116\n"
      "s_waitcnt lgkmcnt(4)\n"
      "s_cbranch_vccnz FB0_%=\n"
      "RET0_5_%=:\n"
      "v_mfma_f32_16x16x32_bf16 a[160:163], a[96:99], v[0:3], a[160:163]\n"
      "ds_read_b64_tr_b16 a[120:121], v244 offset:1024\n"
      "v_mov_b32 v117, v224\n"
      "v_xor_b32 v242, 0x10000, v242\n"
      "v_xor_b32 v243, 0x10000, v243\n"
      "v_mfma_f32_16x16x32_bf16 a[176:179], a[100:103], v[0:3], a[176:179]\n"
      "ds_read_b64_tr_b16 a[122:123], v244 offset:5120\n"
      "v_exp_f32 v112, v160\n"
      "v_mfma_f32_16x16x32_bf16 a[160:163], a[112:115], v[16:19], a[160:163]\n"
      "s_mov_b32 s64, 6\n"
      "ds_read_b64_tr_b16 a[124:125], v245 offset:1024\n"
      "v_exp_f32 v113, v161\n"
      "v_exp_f32 v114, v162\n"
      "v_mfma_f32_16x16x32_bf16 a[176:179], a[116:119], v[16:19], a[176:179]\n"
      "s_add_u32 m0, %[wb], 114688\n"
      "ds_read_b64_tr_b16 a[126:127], v245 offset:5120\n"
      "v_cvt_pk_bf16_f32 v96, v112, v113\n"
      "v_exp_f32 v115, v163\n"
      "v_mfma_f32_16x16x32_bf16 a[164:167], a[96:99], v[4:7], a[164:167]\n"
      "buffer_load_dwordx4 v249, s[24:27], s95 offen nt lds\n"
      "v_mov_b32_dpp v117, v228 quad_perm:[0,1,2,3] row_mask:0x2 bank_mask:0xf\n"
      "v_mfma_f32_16x16x32_bf16 a[180:183], a[100:103], v[4:7], a[180:183]\n"
      "s_add_u32 m0, %[wb], 122880\n"
      "v_exp_f32 v112, v164\n"
      "v_cvt_pk_bf16_f32 v97, v114, v115\n"
      "v_mfma_f32_16x16x32_bf16 a[164:167], a[112:115], v[20:23], a[164:167]\n"
      "buffer_load_dwordx4 v122, s[24:27], s95 offen nt lds\n"
      "v_mfma_f32_16x16x32_bf16 a[180:183], a[116:119], v[20:23], a[180:183]\n"
      "ds_write_b128 v125, a[160:163] offset:0\n"
      "v_exp_f32 v113, v165\n"
      "v_or_b32 v116, v96, v97\n"
      "v_mfma_f32_16x16x32_bf16 a[168:171], a[96:99], v[8:11], a[168:171]\n"
      "v_mov_b32_dpp v117, v232 quad_perm:[0,1,2,3] row_mask:0x4 bank_mask:0xf\n"
      "v_exp_f32 v114, v166\n"
      "v_cvt_pk_bf16_f32 v98, v112, v113\n"
      "v_mfma_f32_16x16x32_bf16 a[184:187], a[100:103], v[8:11], a[184:187]\n"
      "ds_write_b128 v125, a[176:179] offset:4096\n"
      "v_exp_f32 v115, v167\n"
      "v_mfma_f32_16x16x32_bf16 a[168:171], a[112:115], v[24:27], a[168:171]\n"
      "v_exp_f32 v112, v168\n"
      "v_cvt_pk_bf16_f32 v99, v114, v115\n"
      "v_mfma_f32_16x16x32_bf16 a[184:187], a[116:119], v[24:27], a[184:187]\n"
      "v_exp_f32 v113, v169\n"
      "v_or3_b32 v116, v116, v98, v99\n"
      "v_mfma_f32_16x16x32_bf16 a[172:175], a[96:99], v[12:15], a[172:175]\n"
      "ds_write_b128 v125, v[96:99] offset:8192\n"
      "v_mov_b32_dpp v117, v236 quad_perm:[0,1,2,3] row_mask:0x8 bank_mask:0xf\n"
      "v_exp_f32 v114, v170\n"
      "v_mfma_f32_16x16x32_bf16 a[188:191], a[100:103], v[12:15], a[188:191]\n"
      "v_cvt_pk_bf16_f32 v100, v112, v113\n"
      "v_exp_f32 v115, v171\n"
      "v_mfma_f32_16x16x32_bf16 a[172:175], a[112:115], v[28:31], a[172:175]\n"
      "v_exp_f32 v112, v172\n"
      "v_cvt_pk_bf16_f32 v101, v114, v115\n"
      "v_exp_f32 v113, v173\n"
      "v_mfma_f32_16x16x32_bf16 a[188:191], a[116:119], v[28:31], a[188:191]\n"
      "s_add_u32 m0, %[wb], 16384\n"
      "ds_write_b128 v125, a[164:167] offset:1024\n"
      "v_or3_b32 v116, v116, v100, v101\n"
      "v_mfma_f32_16x16x32_bf16 a[192:195], a[196:199], v[16:19], a[192:195]\n"
      "buffer_load_dwordx4 v249, s[24:27], s77 offen nt lds\n"
      "ds_write_b32 v125, v117 offset:12292\n"
      "v_mfma_f32_16x16x32_bf16 a[192:195], a[200:203], v[20:23], a[192:195]\n"
      "s_add_u32 m0, %[wb], 24576\n"
      "v_exp_f32 v114, v174\n"
      "v_cvt_pk_bf16_f32 v102, v112, v113\n"
      "v_mfma_f32_16x16x32_bf16 a[192:195], a[204:207], v[24:27], a[192:195]\n"
      "buffer_load_dwordx4 v122, s[24:27], s77 offen nt lds\n"
      "ds_write_b128 v125, a[180:183] offset:5120\n"
      "ds_write_b32 v125, v123 offset:12296\n"
      "v_mfma_f32_16x16x32_bf16 a[192:195], a[208:211], v[28:31], a[192:195]\n"
      "s_waitcnt lgkmcnt(7)\n"
      "v_exp_f32 v115, v175\n"
      "v_exp_f32 v112, v176\n"
      "v_mfma_f32_16x16x32_bf16 v[64:67], a[104:107], v[0:3], v[64:67]\n"
      "v_cvt_pk_bf16_f32 v103, v114, v115\n"
      "v_exp_f32 v113, v177\n"
      "v_mfma_f32_16x16x32_bf16 v[68:71], a[104:107], v[4:7], v[68:71]\n"
      "ds_write_b128 v125, v[100:103] offset:9216\n"
      "v_or3_b32 v116, v116, v102, v103\n"
      "v_exp_f32 v114, v178\n"
      "s_waitcnt vmcnt(8)\n"
      "s_barrier\n"
      "v_mfma_f32_16x16x32_bf16 v[72:75], a[104:107], v[8:11], v[72:75]\n"
      "ds_read_b64_tr_b16 a[212:213], v244 offset:33792\n"
      "v_cvt_pk_bf16_f32 v104, v112, v113\n"
      "v_mfma_f32_16x16x32_bf16 v[76:79], a[104:107], v[12:15], v[76:79]\n"
      "ds_write_b128 v125, a[168:171] offset:2048\n"
      "ds_read_b64_tr_b16 a[214:215], v244 offset:37888\n"
      "v_exp_f32 v115, v179\n"
      "v_mfma_f32_16x16x32_bf16 a[228:231], a[196:199], v[96:99], a[192:195]\n"
      "ds_read_b64_tr_b16 a[216:217], v245 offset:33792\n"
      "v_exp_f32 v112, v180\n"
      "v_cvt_pk_bf16_f32 v105, v114, v115\n"
      "v_mfma_f32_16x16x32_bf16 v[80:83], a[108:111], v[0:3], v[80:83]\n"
      "ds_read_b64_tr_b16 a[218:219], v245 offset:37888\n"
      "v_exp_f32 v113, v181\n"
      "v_mfma_f32_16x16x32_bf16 v[84:87], a[108:111], v[4:7], v[84:87]\n"
      "ds_write_b128 v125, a[184:187] offset:6144\n"
      "ds_read_b64_tr_b16 a[220:221], v248 offset:33792\n"
      "v_or3_b32 v116, v116, v104, v105\n"
      "v_mfma_f32_16x16x32_bf16 v[88:91], a[108:111], v[8:11], v[88:91]\n"
      "ds_read_b64_tr_b16 a[222:223], v248 offset:37888\n"
      "v_exp_f32 v114, v182\n"
      "v_cvt_pk_bf16_f32 v106, v112, v113\n"
      "v_mfma_f32_16x16x32_bf16 v[92:95], a[108:111], v[12:15], v[92:95]\n"
      "ds_read_b64_tr_b16 a[224:225], v120 offset:33792\n"
      "v_exp_f32 v115, v183\n"
      "v_mfma_f32_16x16x32_bf16 v[64:67], a[120:123], v[16:19], v[64:67]\n"
      "s_add_u32 m0, %[wb], 82944\n"
      "ds_write_b128 v125, a[172:175] offset:3072\n"
      "ds_read_b64_tr_b16 a[226:227], v120 offset:37888\n"
      "v_exp_f32 v112, v184\n"
      "v_mfma_f32_16x16x32_bf16 v[68:71], a[120:123], v[20:23], v[68:71]\n"
      "buffer_load_dwordx4 v249, s[24:27], s78 offen nt lds\n"
      "v_mfma_f32_16x16x32_bf16 v[72:75], a[120:123], v[24:27], v[72:75]\n"
      "s_add_u32 m0, %[wb], 91136\n"
      "v_cvt_pk_bf16_f32 v107, v114, v115\n"
      "v_exp_f32 v113, v185\n"
      "v_or3_b32 v116, v116, v106, v107\n"
      "v_mfma_f32_16x16x32_bf16 v[76:79], a[120:123], v[28:31], v[76:79]\n"
      "buffer_load_dwordx4 v122, s[24:27], s78 offen nt lds\n"
      "ds_write_b128 v125, v[104:107] offset:10240\n"
      "v_mfma_f32_16x16x32_bf16 a[228:231], a[200:203], v[100:103], a[228:231]\n"
      "v_exp_f32 v114, v186\n"
      "v_cvt_pk_bf16_f32 v108, v112, v113\n"
      "v_mfma_f32_16x16x32_bf16 v[80:83], a[124:127], v[16:19], v[80:83]\n"
      "v_exp_f32 v115, v187\n"
      "v_exp_f32 v112, v188\n"
      "v_mfma_f32_16x16x32_bf16 v[84:87], a[124:127], v[20:23], v[84:87]\n"
      "ds_write_b128 v125, a[188:191] offset:7168\n"
      "v_cvt_pk_bf16_f32 v109, v114, v115\n"
      "v_mfma_f32_16x16x32_bf16 v[88:91], a[124:127], v[24:27], v[88:91]\n"
      "v_exp_f32 v113, v189\n"
      "v_or3_b32 v116, v116, v108, v109\n"
      "v_exp_f32 v114, v190\n"
      "v_mfma_f32_16x16x32_bf16 v[92:95], a[124:127], v[28:31], v[92:95]\n"
      "v_cvt_pk_bf16_f32 v110, v112, v113\n"
      "v_exp_f32 v115, v191\n"
      "s_nop 0\n"
      "v_mfma_f32_16x16x32_bf16 a[228:231], a[204:207], v[104:107], a[228:231]\n"
      "v_cvt_pk_bf16_f32 v111, v114, v115\n"
      "v_or3_b32 v116, v116, v110, v111\n"
      "v_and_b32 v116, 0x40004000, v116\n"
      "v_cmp_ne_u32 vcc, 0, v116\n"
      "s_cbranch_vccnz FB1_%=\n"
      "RET1_6_%=:\n"
      "ds_write_b128 v125, v[108:111] offset:11264\n"
      "v_mfma_f32_16x16x32_bf16 a[228:231], a[208:211], v[108:111], a[228:231]\n"
      "s_waitcnt lgkmcnt(9)\n"
      "v_mfma_f32_16x16x32_bf16 v[64:67], a[212:215], v[96:99], v[64:67]\n"
      "v_mov_b32 v148, 1.0\n"
      "v_mfma_f32_16x16x32_bf16 v[80:83], a[216:219], v[96:99], v[80:83]\n"
      "v_mov_b32 v149, 1.0\n"
      "v_mfma_f32_16x16x32_bf16 v[68:71], a[212:215], v[100:103], v[68:71]\n"
      "v_mov_b32 v157, %[fo]\n"
      "v_mfma_f32_16x16x32_bf16 v[84:87], a[216:219], v[100:103], v[84:87]\n"
      "v_mov_b32 v150, s72\n"
      "v_mfma_f32_16x16x32_bf16 v[72:75], a[212:215], v[104:107], v[72:75]\n"
      "v_mov_b32 v151, s76\n"
      "ds_write_b32 v125, a228 offset:12288\n"
      "v_mfma_f32_16x16x32_bf16 v[88:91], a[216:219], v[104:107], v[88:91]\n"
      "v_xor_b32 v158, 8, v157\n"
      "v_mfma_f32_16x16x32_bf16 v[76:79], a[212:215], v[108:111], v[76:79]\n"
      "v_mfma_f32_16x16x32_bf16 v[92:95], a[216:219], v[108:111], v[92:95]\n"
      "s_waitcnt lgkmcnt(0)\n"
      "s_barrier\n"
      "s_memrealtime s[68:69]\n"
      "ds_read_b32 v237, v127 offset:12296\n"
      "ds_read_b64 v[152:153], v126 offset:12288\n"
      "ds_read_b128 v[160:163], v126 offset:0\n"
      "ds_read_b128 v[176:179], v126 offset:4096\n"
      "ds_read_b128 v[164:167], v126 offset:1024\n"
      "ds_read_b128 v[180:183], v126 offset:5120\n"
      "ds_read_b128 v[168:171], v126 offset:2048\n"
      "ds_read_b128 v[184:187], v126 offset:6144\n"
      "ds_read_b128 v[172:175], v126 offset:3072\n"
      "ds_read_b128 v[188:191], v126 offset:7168\n"
      "ds_read_b128 a[64:67], v126 offset:8192\n"
      "ds_read_b128 a[68:71], v126 offset:9216\n"
      "ds_read_b128 a[72:75], v126 offset:10240\n"
      "ds_read_b128 a[76:79], v126 offset:11264\n"
      "v_accvgpr_read_b32 v144, a228\n"
      "v_and_b32 v118, 0xf0, v127\n"
      "v_lshrrev_b32 v156, 3, v127\n"
      "v_and_b32 v156, 32, v156\n"
      "v_lshl_or_b32 v156, v118, 4, v156\n"
      "v_lshrrev_b32 v118, 5, v127\n"
      "v_and_or_b32 v156, v118, 16, v156\n"
      "v_add_u32 v156, %[oo], v156\n"
      "v_add_u32 v155, 0x2000, v156\n"
      "v_xor_b32 v242, 0x2000, v240\n"
      "v_xor_b32 v243, 0x2000, v241\n"
      "v_mov_b32 v236, v150\n"
      "s_waitcnt lgkmcnt(12)\n"
      "v_cmp_neq_f32 vcc, v153, v117\n"
      "v_readfirstlane_b32 s88, v237\n"
      "s_cbranch_vccnz MIS_%=\n"
      "MR_%=:\n"
      "v_mul_f32 v144, v144, v148\n"
      "v_mov_b32 v145, v117\n"
      "v_fmac_f32 v144, v152, v149\n"
      "s_waitcnt lgkmcnt(10)\n"
      "v_pk_add_f32 v[64:65], v[64:65], v[160:161]\n"
      "v_pk_add_f32 v[66:67], v[66:67], v[162:163]\n"
      "v_pk_add_f32 v[80:81], v[80:81], v[176:177]\n"
      "v_pk_add_f32 v[82:83], v[82:83], v[178:179]\n"
      "s_waitcnt lgkmcnt(8)\n"
      "v_pk_add_f32 v[68:69], v[68:69], v[164:165]\n"
      "v_pk_add_f32 v[70:71], v[70:71], v[166:167]\n"
      "v_pk_add_f32 v[84:85], v[84:85], v[180:181]\n"
      "v_pk_add_f32 v[86:87], v[86:87], v[182:183]\n"
      "s_waitcnt lgkmcnt(6)\n"
      "v_pk_add_f32 v[72:73], v[72:73], v[168:169]\n"
      "v_pk_add_f32 v[74:75], v[74:75], v[170:171]\n"
      "v_pk_add_f32 v[88:89], v[88:89], v[184:185]\n"
      "v_pk_add_f32 v[90:91], v[90:91], v[186:187]\n"
      "s_waitcnt lgkmcnt(4)\n"
      "v_pk_add_f32 v[76:77], v[76:77], v[172:173]\n"
      "v_pk_add_f32 v[78:79], v[78:79], v[174:175]\n"
      "v_pk_add_f32 v[92:93], v[92:93], v[188:189]\n"
      "v_pk_add_f32 v[94:95], v[94:95], v[190:191]\n"
      "s_waitcnt lgkmcnt(0)\n"
      "s_add_u32 s65, s68, 55\n"
      "s_mov_b32 s66, s88\n"
      "s_sub_u32 s67, s65, s66\n"
      "s_cmp_lt_i32 s67, 0\n"
      "s_cselect_b32 s88, s66, s65\n"
      "v_mov_b32 v237, s88\n"
      "s_mov_b32 s88, s66\n"
      "s_cmp_lg_u32 %[xw], 0\n"
      "s_cbranch_scc1 NH_%=\n"
      "s_mov_b64 exec, 1\n"
      "global_store_dwordx2 v157, v[236:237], s[18:19] sc1\n"
      "s_mov_b64 exec, -1\n"
      "NH_%=:\n"
      "v_mfma_f32_16x16x32_bf16 v[64:67], a[220:223], a[64:67], v[64:67]\n"
      "v_mfma_f32_16x16x32_bf16 v[80:83], a[224:227], a[64:67], v[80:83]\n"
      "v_mfma_f32_16x16x32_bf16 v[68:71], a[220:223], a[68:71], v[68:71]\n"
      "v_mfma_f32_16x16x32_bf16 v[84:87], a[224:227], a[68:71], v[84:87]\n"
      "v_mfma_f32_16x16x32_bf16 v[72:75], a[220:223], a[72:75], v[72:75]\n"
      "v_mfma_f32_16x16x32_bf16 v[88:91], a[224:227], a[72:75], v[88:91]\n"
      "v_mfma_f32_16x16x32_bf16 v[76:79], a[220:223], a[76:79], v[76:79]\n"
      "v_mfma_f32_16x16x32_bf16 v[92:95], a[224:227], a[76:79], v[92:95]\n"
      "s_nop 1\n"
      "s_memrealtime s[68:69]\n"
      "v_cvt_pk_bf16_f32 v128, v64, v65\n"
      "v_cvt_pk_bf16_f32 v129, v66, v67\n"
      "v_cvt_pk_bf16_f32 v130, v80, v81\n"
      "v_cvt_pk_bf16_f32 v131, v82, v83\n"
      "v_cvt_pk_bf16_f32 v132, v68, v69\n"
      "v_cvt_pk_bf16_f32 v133, v70, v71\n"
      "v_cvt_pk_bf16_f32 v134, v84, v85\n"
      "v_cvt_pk_bf16_f32 v135, v86, v87\n"
      "v_cvt_pk_bf16_f32 v136, v72, v73\n"
      "v_cvt_pk_bf16_f32 v137, v74, v75\n"
      "v_cvt_pk_bf16_f32 v138, v88, v89\n"
      "v_cvt_pk_bf16_f32 v139, v90, v91\n"
      "v_cvt_pk_bf16_f32 v140, v76, v77\n"
      "v_cvt_pk_bf16_f32 v141, v78, v79\n"
      "v_cvt_pk_bf16_f32 v142, v92, v93\n"
      "v_cvt_pk_bf16_f32 v143, v94, v95\n"
      "v_mad_u32_u24 v147, v144, s60, v151\n"
      "v_mad_u32_u24 v147, v145, s62, v147\n"
      "v_add3_u32 v146, v150, v144, v145\n"
      "v_mad_u32_u24 v147, v128, s60, v147\n"
      "v_mad_u32_u24 v147, v129, s62, v147\n"
      "v_add3_u32 v146, v146, v128, v129\n"
      "v_mad_u32_u24 v147, v130, s70, v147\n"
      "v_mad_u32_u24 v147, v131, s71, v147\n"
      "v_add3_u32 v146, v146, v130, v131\n"
      "v_mad_u32_u24 v147, v132, s60, v147\n"
      "v_mad_u32_u24 v147, v133, s62, v147\n"
      "v_add3_u32 v146, v146, v132, v133\n"
      "v_mad_u32_u24 v147, v134, s70, v147\n"
      "v_mad_u32_u24 v147, v135, s71, v147\n"
      "v_add3_u32 v146, v146, v134, v135\n"
      "v_mad_u32_u24 v147, v136, s60, v147\n"
      "v_mad_u32_u24 v147, v137, s62, v147\n"
      "v_add3_u32 v146, v146, v136, v137\n"
      "v_mad_u32_u24 v147, v138, s70, v147\n"
      "v_mad_u32_u24 v147, v139, s71, v147\n"
      "v_add3_u32 v146, v146, v138, v139\n"
      "v_mad_u32_u24 v147, v140, s60, v147\n"
      "v_mad_u32_u24 v147, v141, s62, v147\n"
      "v_add3_u32 v146, v146, v140, v141\n"
      "v_mad_u32_u24 v147, v142, s70, v147\n"
      "v_mad_u32_u24 v147, v143, s71, v147\n"
      "v_add3_u32 v146, v146, v142, v143\n"
      "s_waitcnt lgkmcnt(0)\n"
      "s_call_b64 s[84:85], SPIN_%=\n"
      "global_load_dwordx2 v[232:233], v158, s[18:19] sc1\n"
      "buffer_store_dwordx4 v[128:131], v127, s[28:31], %[xo] offen offset:0 sc1\n"
      "buffer_store_dwordx4 v[132:135], v127, s[28:31], %[xo] offen offset:1024 sc1\n"
      "buffer_store_dwordx4 v[136:139], v127, s[28:31], %[xo] offen offset:2048 sc1\n"
      "buffer_store_dwordx4 v[140:143], v127, s[28:31], %[xo] offen offset:3072 sc1\n"
      "buffer_store_dwordx4 v[144:147], v127, s[28:31], s81 offen sc1\n"
      "global_load_dwordx2 v[234:235], v158, s[18:19] sc1\n"
      "s_waitcnt vmcnt(7)\n"
      "s_barrier\n"
      "ds_read_b64_tr_b16 a[96:97], v246 offset:32768\n"
      "ds_read_b64_tr_b16 a[98:99], v246 offset:36864\n"
      "ds_read_b64_tr_b16 a[100:101], v247 offset:32768\n"
      "ds_read_b64_tr_b16 a[102:103], v247 offset:36864\n"
      "ds_read_b64_tr_b16 a[112:113], v250 offset:0\n"
      "ds_read_b64_tr_b16 a[114:115], v250 offset:4096\n"
      "ds_read_b64_tr_b16 a[116:117], v251 offset:0\n"
      "ds_read_b64_tr_b16 a[118:119], v251 offset:4096\n"
      "ds_read_b64_tr_b16 a[0:1], v250 offset:32768\n"
      "ds_read_b64_tr_b16 a[2:3], v250 offset:36864\n"
      "ds_read_b64_tr_b16 a[4:5], v251 offset:32768\n"
      "ds_read_b64_tr_b16 a[6:7], v251 offset:36864\n"
      "s_waitcnt lgkmcnt(8)\n"
      "v_mfma_f32_16x16x32_bf16 a[128:131], a[96:99], v[208:211], a[128:131]\n"
      "ds_read_b64_tr_b16 a[104:105], v244 offset:32768\n"
      "ds_read_b64_tr_b16 a[106:107], v244 offset:36864\n"
      "v_mfma_f32_16x16x32_bf16 a[144:147], a[100:103], v[208:211], a[144:147]\n"
      "ds_read_b64_tr_b16 a[108:109], v245 offset:32768\n"
      "ds_read_b64_tr_b16 a[110:111], v245 offset:36864\n"
      "s_waitcnt lgkmcnt(8)\n"
      "v_mfma_f32_16x16x32_bf16 a[128:131], a[112:115], v[192:195], a[128:131]\n"
      "ds_read_b64_tr_b16 a[120:121], v240 offset:0\n"
      "ds_read_b64_tr_b16 a[122:123], v240 offset:4096\n"
      "v_mfma_f32_16x16x32_bf16 a[144:147], a[116:119], v[192:195], a[144:147]\n"
      "ds_read_b64_tr_b16 a[124:125], v241 offset:0\n"
      "ds_read_b64_tr_b16 a[126:127], v241 offset:4096\n"
      "s_waitcnt lgkmcnt(8)\n"
      "v_mfma_f32_16x16x32_bf16 a[128:131], a[0:3], v[0:3], a[128:131]\n"
      "ds_read_b64_tr_b16 a[8:9], v240 offset:32768\n"
      "ds_read_b64_tr_b16 a[10:11], v240 offset:36864\n"
      "v_mfma_f32_16x16x32_bf16 a[144:147], a[4:7], v[0:3], a[144:147]\n"
      "ds_read_b64_tr_b16 a[12:13], v241 offset:32768\n"
      "ds_read_b64_tr_b16 a[14:15], v241 offset:36864\n"
      "v_mfma_f32_16x16x32_bf16 a[132:135], a[96:99], v[212:215], a[132:135]\n"
      "ds_write_b128 v125, v[16:19] offset:8192\n"
      "v_lshrrev_b32 v118, 2, v127\n"
      "v_mfma_f32_16x16x32_bf16 a[148:151], a[100:103], v[212:215], a[148:151]\n"
      "v_xor_b32 v118, v118, v127\n"
      "v_mfma_f32_16x16x32_bf16 a[132:135], a[112:115], v[196:199], a[132:135]\n"
      "v_and_b32 v118, 0xc0, v118\n"
      "v_mfma_f32_16x16x32_bf16 a[148:151], a[116:119], v[196:199], a[148:151]\n"
      "v_and_or_b32 v118, v127, 32, v118\n"
      "v_mfma_f32_16x16x32_bf16 a[132:135], a[0:3], v[4:7], a[132:135]\n"
      "ds_write_b128 v125, v[20:23] offset:9216\n"
      "v_and_b32 v119, 16, v127\n"
      "v_mfma_f32_16x16x32_bf16 a[148:151], a[4:7], v[4:7], a[148:151]\n"
      "v_mov_b32 v84, 0x3f80\n"
      "v_mfma_f32_16x16x32_bf16 a[136:139], a[96:99], v[216:219], a[136:139]\n"
      "v_lshlrev_b32 v119, v119, v84\n"
      "v_mfma_f32_16x16x32_bf16 a[152:155], a[100:103], v[216:219], a[152:155]\n"
      "v_cmp_eq_u32 vcc, 0, v118\n"
      "v_mfma_f32_16x16x32_bf16 a[136:139], a[112:115], v[200:203], a[136:139]\n"
      "ds_write_b128 v125, v[24:27] offset:10240\n"
      "v_mov_b32 v84, 0\n"
      "v_mfma_f32_16x16x32_bf16 a[152:155], a[116:119], v[200:203], a[152:155]\n"
      "v_cndmask_b32 v86, 0, v119, vcc\n"
      "v_mfma_f32_16x16x32_bf16 a[136:139], a[0:3], v[8:11], a[136:139]\n"
      "v_cmp_eq_u32 vcc, 32, v118\n"
      "v_mfma_f32_16x16x32_bf16 a[152:155], a[4:7], v[8:11], a[152:155]\n"
      "v_mov_b32 v85, 0\n"
      "v_mfma_f32_16x16x32_bf16 a[140:143], a[96:99], v[220:223], a[140:143]\n"
      "ds_write_b128 v125, v[28:31] offset:11264\n"
      "v_cndmask_b32 v87, 0, v119, vcc\n"
      "v_mfma_f32_16x16x32_bf16 a[156:159], a[100:103], v[220:223], a[156:159]\n"
      "v_mov_b32 v88, 0\n"
      "v_mfma_f32_16x16x32_bf16 a[140:143], a[112:115], v[204:207], a[140:143]\n"
      "v_mov_b32 v89, 0\n"
      "v_mfma_f32_16x16x32_bf16 a[156:159], a[116:119], v[204:207], a[156:159]\n"
      "v_mfma_f32_16x16x32_bf16 a[140:143], a[0:3], v[12:15], a[140:143]\n"
      "ds_write_b128 v125, a[128:131] offset:0\n"
      "v_mfma_f32_16x16x32_bf16 a[156:159], a[4:7], v[12:15], a[156:159]\n"
      "s_waitcnt lgkmcnt(13)\n"
      "v_mfma_f32_16x16x32_bf16 v[32:35], a[104:107], v[208:211], v[32:35]\n"
      "ds_read_b64_tr_b16 a[32:33], v240 offset:1024\n"
      "v_mfma_f32_16x16x32_bf16 v[36:39], a[104:107], v[212:215], v[36:39]\n"
      "ds_read_b64_tr_b16 a[34:35], v240 offset:5120\n"
      "v_mfma_f32_16x16x32_bf16 v[40:43], a[104:107], v[216:219], v[40:43]\n"
      "ds_write_b128 v125, a[144:147] offset:4096\n"
      "ds_read_b64_tr_b16 a[36:37], v241 offset:1024\n"
      "v_mfma_f32_16x16x32_bf16 v[44:47], a[104:107], v[220:223], v[44:47]\n"
      "ds_read_b64_tr_b16 a[38:39], v241 offset:5120\n"
      "v_mfma_f32_16x16x32_bf16 v[48:51], a[108:111], v[208:211], v[48:51]\n"
      "ds_read_b64_tr_b16 a[40:41], v242 offset:1024\n"
      "v_mfma_f32_16x16x32_bf16 v[52:55], a[108:111], v[212:215], v[52:55]\n"
      "ds_read_b64_tr_b16 a[42:43], v242 offset:5120\n"
      "v_mfma_f32_16x16x32_bf16 v[56:59], a[108:111], v[216:219], v[56:59]\n"
      "ds_write_b128 v125, a[132:135] offset:1024\n"
      "ds_read_b64_tr_b16 a[44:45], v243 offset:1024\n"
      "v_mfma_f32_16x16x32_bf16 v[60:63], a[108:111], v[220:223], v[60:63]\n"
      "ds_read_b64_tr_b16 a[46:47], v243 offset:5120\n"
      "s_waitcnt lgkmcnt(15)\n"
      "v_mfma_f32_16x16x32_bf16 v[32:35], a[120:123], v[192:195], v[32:35]\n"
      "ds_read_b64_tr_b16 a[16:17], v244 offset:0\n"
      "v_mfma_f32_16x16x32_bf16 v[36:39], a[120:123], v[196:199], v[36:39]\n"
      "ds_read_b64_tr_b16 a[18:19], v244 offset:4096\n"
      "v_mfma_f32_16x16x32_bf16 v[40:43], a[120:123], v[200:203], v[40:43]\n"
      "ds_write_b128 v125, a[148:151] offset:5120\n"
      "ds_read_b64_tr_b16 a[20:21], v245 offset:0\n"
      "v_mfma_f32_16x16x32_bf16 v[44:47], a[120:123], v[204:207], v[44:47]\n"
      "ds_read_b64_tr_b16 a[22:23], v245 offset:4096\n"
      "v_mfma_f32_16x16x32_bf16 v[48:51], a[124:127], v[192:195], v[48:51]\n"
      "ds_read_b64_tr_b16 a[24:25], v248 offset:0\n"
      "v_mfma_f32_16x16x32_bf16 v[52:55], a[124:127], v[196:199], v[52:55]\n"
      "ds_read_b64_tr_b16 a[26:27], v248 offset:4096\n"
      "v_mfma_f32_16x16x32_bf16 v[56:59], a[124:127], v[200:203], v[56:59]\n"
      "ds_write_b128 v125, a[136:139] offset:2048\n"
      "ds_read_b64_tr_b16 a[28:29], v120 offset:0\n"
      "v_mfma_f32_16x16x32_bf16 v[60:63], a[124:127], v[204:207], v[60:63]\n"
      "ds_read_b64_tr_b16 a[30:31], v120 offset:4096\n"
      "s_waitcnt lgkmcnt(15)\n"
      "v_mfma_f32_16x16x32_bf16 v[32:35], a[8:11], v[0:3], v[32:35]\n"
      "v_mfma_f32_16x16x32_bf16 v[36:39], a[8:11], v[4:7], v[36:39]\n"
      "v_mfma_f32_16x16x32_bf16 v[40:43], a[8:11], v[8:11], v[40:43]\n"
      "ds_write_b128 v125, a[152:155] offset:6144\n"
      "v_mfma_f32_16x16x32_bf16 v[44:47], a[8:11], v[12:15], v[44:47]\n"
      "s_waitcnt vmcnt(6)\n"
      "v_cmp_ne_u32 vcc, v232, v150\n"
      "s_memrealtime s[68:69]\n"
      "s_cbranch_vccnz POLL_%=\n"
      "GOT_%=:\n"
      "v_readfirstlane_b32 s88, v233\n"
      "s_waitcnt lgkmcnt(0)\n"
      "s_add_u32 s88, s88, 60\n"
      "s_sub_u32 s67, s88, s68\n"
      "s_cmp_gt_u32 s67, 1000\n"
      "s_cbranch_scc1 LD_%=\n"
      "s_call_b64 s[84:85], SPIN_%=\n"
      "LD_%=:\n"
      "buffer_load_dwordx4 v[80:83], v127, s[28:31], s79 offen sc1\n"
      "buffer_load_dwordx4 v[64:67], v127, s[28:31], s80 offen offset:0 sc1\n"
      "buffer_load_dwordx4 v[68:71], v127, s[28:31], s80 offen offset:1024 sc1\n"
      "buffer_load_dwordx4 v[72:75], v127, s[28:31], s80 offen offset:2048 sc1\n"
      "buffer_load_dwordx4 v[76:79], v127, s[28:31], s80 offen offset:3072 sc1\n"
      "v_mfma_f32_16x16x32_bf16 v[48:51], a[12:15], v[0:3], v[48:51]\n"
      "v_mfma_f32_16x16x32_bf16 v[52:55], a[12:15], v[4:7], v[52:55]\n"
      "v_mfma_f32_16x16x32_bf16 v[56:59], a[12:15], v[8:11], v[56:59]\n"
      "ds_write_b128 v125, a[140:143] offset:3072\n"
      "v_mfma_f32_16x16x32_bf16 v[60:63], a[12:15], v[12:15], v[60:63]\n"
      "s_waitcnt lgkmcnt(15)\n"
      "v_mfma_f32_16x16x32_bf16 v[32:35], a[32:35], v[96:99], v[32:35]\n"
      "v_mfma_f32_16x16x32_bf16 v[48:51], a[36:39], v[96:99], v[48:51]\n"
      "v_mfma_f32_16x16x32_bf16 v[36:39], a[32:35], v[100:103], v[36:39]\n"
      "ds_write_b128 v125, a[156:159] offset:7168\n"
      "v_mfma_f32_16x16x32_bf16 v[52:55], a[36:39], v[100:103], v[52:55]\n"
      "v_mfma_f32_16x16x32_bf16 v[40:43], a[32:35], v[104:107], v[40:43]\n"
      "v_mfma_f32_16x16x32_bf16 v[56:59], a[36:39], v[104:107], v[56:59]\n"
      "v_mfma_f32_16x16x32_bf16 v[44:47], a[32:35], v[108:111], v[44:47]\n"
      "v_mfma_f32_16x16x32_bf16 v[60:63], a[36:39], v[108:111], v[60:63]\n"
      "s_waitcnt lgkmcnt(13)\n"
      "v_mfma_f32_16x16x32_bf16 v[32:35], a[40:43], a[64:67], v[32:35]\n"
      "v_mfma_f32_16x16x32_bf16 v[48:51], a[44:47], a[64:67], v[48:51]\n"
      "v_mfma_f32_16x16x32_bf16 v[36:39], a[40:43], a[68:71], v[36:39]\n"
      "v_mfma_f32_16x16x32_bf16 v[52:55], a[44:47], a[68:71], v[52:55]\n"
      "v_mfma_f32_16x16x32_bf16 v[40:43], a[40:43], a[72:75], v[40:43]\n"
      "v_mfma_f32_16x16x32_bf16 v[56:59], a[44:47], a[72:75], v[56:59]\n"
      "v_mfma_f32_16x16x32_bf16 v[44:47], a[40:43], a[76:79], v[44:47]\n"
      "v_mfma_f32_16x16x32_bf16 v[60:63], a[44:47], a[76:79], v[60:63]\n"
      "s_waitcnt lgkmcnt(8)\n"
      "v_mfma_f32_16x16x32_bf16 v[32:35], a[16:19], v[16:19], v[32:35]\n"
      "v_mfma_f32_16x16x32_bf16 v[48:51], a[20:23], v[16:19], v[48:51]\n"
      "v_mfma_f32_16x16x32_bf16 v[36:39], a[16:19], v[20:23], v[36:39]\n"
      "v_mfma_f32_16x16x32_bf16 v[52:55], a[20:23], v[20:23], v[52:55]\n"
      "v_mfma_f32_16x16x32_bf16 v[40:43], a[16:19], v[24:27], v[40:43]\n"
      "v_mfma_f32_16x16x32_bf16 v[56:59], a[20:23], v[24:27], v[56:59]\n"
      "v_mfma_f32_16x16x32_bf16 v[44:47], a[16:19], v[28:31], v[44:47]\n"
      "v_mfma_f32_16x16x32_bf16 v[60:63], a[20:23], v[28:31], v[60:63]\n"
      "s_waitcnt lgkmcnt(0)\n"
      "s_barrier\n"
      "ds_read_b128 v[160:163], v126 offset:0\n"
      "ds_read_b128 v[176:179], v126 offset:4096\n"
      "ds_read_b128 v[164:167], v126 offset:1024\n"
      "ds_read_b128 v[180:183], v126 offset:5120\n"
      "ds_read_b128 v[168:171], v126 offset:2048\n"
      "ds_read_b128 v[184:187], v126 offset:6144\n"
      "ds_read_b128 v[172:175], v126 offset:3072\n"
      "ds_read_b128 v[188:191], v126 offset:7168\n"
      "ds_read_b128 a[80:83], v126 offset:8192\n"
      "ds_read_b128 a[84:87], v126 offset:9216\n"
      "ds_read_b128 a[88:91], v126 offset:10240\n"
      "ds_read_b128 a[92:95], v126 offset:11264\n"
      "s_waitcnt lgkmcnt(10)\n"
      "v_pk_add_f32 v[32:33], v[32:33], v[160:161]\n"
      "v_pk_add_f32 v[34:35], v[34:35], v[162:163]\n"
      "v_pk_add_f32 v[48:49], v[48:49], v[176:177]\n"
      "v_pk_add_f32 v[50:51], v[50:51], v[178:179]\n"
      "s_waitcnt lgkmcnt(8)\n"
      "v_pk_add_f32 v[36:37], v[36:37], v[164:165]\n"
      "v_pk_add_f32 v[38:39], v[38:39], v[166:167]\n"
      "v_pk_add_f32 v[52:53], v[52:53], v[180:181]\n"
      "v_pk_add_f32 v[54:55], v[54:55], v[182:183]\n"
      "s_waitcnt lgkmcnt(6)\n"
      "v_pk_add_f32 v[40:41], v[40:41], v[168:169]\n"
      "v_pk_add_f32 v[42:43], v[42:43], v[170:171]\n"
      "v_pk_add_f32 v[56:57], v[56:57], v[184:185]\n"
      "v_pk_add_f32 v[58:59], v[58:59], v[186:187]\n"
      "s_waitcnt lgkmcnt(4)\n"
      "v_pk_add_f32 v[44:45], v[44:45], v[172:173]\n"
      "v_pk_add_f32 v[46:47], v[46:47], v[174:175]\n"
      "v_pk_add_f32 v[60:61], v[60:61], v[188:189]\n"
      "v_pk_add_f32 v[62:63], v[62:63], v[190:191]\n"
      "s_waitcnt lgkmcnt(0)\n"
      "v_mfma_f32_16x16x32_bf16 v[32:35], a[24:27], a[80:83], v[32:35]\n"
      "v_mfma_f32_16x16x32_bf16 v[48:51], a[28:31], a[80:83], v[48:51]\n"
      "v_mfma_f32_16x16x32_bf16 v[36:39], a[24:27], a[84:87], v[36:39]\n"
      "v_mfma_f32_16x16x32_bf16 v[52:55], a[28:31], a[84:87], v[52:55]\n"
      "v_mfma_f32_16x16x32_bf16 v[40:43], a[24:27], a[88:91], v[40:43]\n"
      "v_mfma_f32_16x16x32_bf16 v[56:59], a[28:31], a[88:91], v[56:59]\n"
      "v_mfma_f32_16x16x32_bf16 v[44:47], a[24:27], a[92:95], v[44:47]\n"
      "v_mfma_f32_16x16x32_bf16 v[60:63], a[28:31], a[92:95], v[60:63]\n"
      "CHK_%=:\n"
      "s_waitcnt vmcnt(4)\n"
      "v_add_f32 v224, v144, v80\n"
      "v_rcp_f32 v224, v224\n"
      "v_mad_u32_u24 v119, v80, s60, v151\n"
      "v_mad_u32_u24 v119, v81, s62, v119\n"
      "v_add3_u32 v118, v150, v80, v81\n"
      "s_waitcnt vmcnt(3)\n"
      "v_mfma_f32_16x16x32_bf16 v[160:163], v[86:89], v[64:67], v[32:35]\n"
      "v_mad_u32_u24 v119, v64, s60, v119\n"
      "v_mad_u32_u24 v119, v65, s62, v119\n"
      "v_add3_u32 v118, v118, v64, v65\n"
      "v_mov_b32 v226, v224\n"
      "v_mfma_f32_16x16x32_bf16 v[176:179], v[84:87], v[64:67], v[48:51]\n"
      "v_mad_u32_u24 v119, v66, s70, v119\n"
      "v_mad_u32_u24 v119, v67, s71, v119\n"
      "v_add3_u32 v118, v118, v66, v67\n"
      "v_permlane32_swap_b32 v224, v226\n"
      "s_waitcnt vmcnt(2)\n"
      "v_mfma_f32_16x16x32_bf16 v[164:167], v[86:89], v[68:71], v[36:39]\n"
      "v_mad_u32_u24 v119, v68, s60, v119\n"
      "v_mad_u32_u24 v119, v69, s62, v119\n"
      "v_add3_u32 v118, v118, v68, v69\n"
      "v_mov_b32 v225, v224\n"
      "v_mov_b32 v227, v226\n"
      "v_mfma_f32_16x16x32_bf16 v[180:183], v[84:87], v[68:71], v[52:55]\n"
      "v_mad_u32_u24 v119, v70, s70, v119\n"
      "v_mad_u32_u24 v119, v71, s71, v119\n"
      "v_add3_u32 v118, v118, v70, v71\n"
      "v_permlane16_swap_b32 v224, v225\n"
      "s_waitcnt vmcnt(1)\n"
      "v_mfma_f32_16x16x32_bf16 v[168:171], v[86:89], v[72:75], v[40:43]\n"
      "v_mad_u32_u24 v119, v72, s60, v119\n"
      "v_mad_u32_u24 v119, v73, s62, v119\n"
      "v_add3_u32 v118, v118, v72, v73\n"
      "v_permlane16_swap_b32 v226, v227\n"
      "v_mfma_f32_16x16x32_bf16 v[184:187], v[84:87], v[72:75], v[56:59]\n"
      "v_mad_u32_u24 v119, v74, s70, v119\n"
      "v_mad_u32_u24 v119, v75, s71, v119\n"
      "v_add3_u32 v118, v118, v74, v75\n"
      "s_waitcnt vmcnt(0)\n"
      "v_mfma_f32_16x16x32_bf16 v[172:175], v[86:89], v[76:79], v[44:47]\n"
      "v_mad_u32_u24 v119, v76, s60, v119\n"
      "v_mad_u32_u24 v119, v77, s62, v119\n"
      "v_add3_u32 v118, v118, v76, v77\n"
      "v_mfma_f32_16x16x32_bf16 v[188:191], v[84:87], v[76:79], v[60:63]\n"
      "v_mad_u32_u24 v119, v78, s70, v119\n"
      "v_mad_u32_u24 v119, v79, s71, v119\n"
      "v_add3_u32 v118, v118, v78, v79\n"
      "v_cmp_ne_u64 vcc, v[118:119], v[82:83]\n"
      "s_cbranch_vccnz RETRY_%=\n"
      "OK_%=:\n"
      "v_cmp_neq_f32 vcc, v81, v117\n"
      "s_cbranch_vccnz FIX_%=\n"
      "FR_%=:\n"
      "v_pk_mul_f32 v[160:161], v[160:161], v[224:225] op_sel:[0,0] op_sel_hi:[1,0]\n"
      "v_pk_mul_f32 v[162:163], v[162:163], v[224:225] op_sel:[0,0] op_sel_hi:[1,0]\n"
      "v_pk_mul_f32 v[176:177], v[176:177], v[224:225] op_sel:[0,0] op_sel_hi:[1,0]\n"
      "v_pk_mul_f32 v[178:179], v[178:179], v[224:225] op_sel:[0,0] op_sel_hi:[1,0]\n"
      "v_cvt_pk_bf16_f32 v128, v160, v161\n"
      "v_cvt_pk_bf16_f32 v130, v176, v177\n"
      "v_cvt_pk_bf16_f32 v129, v162, v163\n"
      "v_cvt_pk_bf16_f32 v131, v178, v179\n"
      "v_pk_mul_f32 v[164:165], v[164:165], v[224:225] op_sel:[0,1] op_sel_hi:[1,1]\n"
      "v_permlane16_swap_b32 v128, v130\n"
      "v_permlane16_swap_b32 v129, v131\n"
      "global_store_dwordx4 v156, v[128:131], s[16:17] offset:-4096 sc1\n"
      "v_pk_mul_f32 v[166:167], v[166:167], v[224:225] op_sel:[0,1] op_sel_hi:[1,1]\n"
      "v_pk_mul_f32 v[180:181], v[180:181], v[224:225] op_sel:[0,1] op_sel_hi:[1,1]\n"
      "v_pk_mul_f32 v[182:183], v[182:183], v[224:225] op_sel:[0,1] op_sel_hi:[1,1]\n"
      "v_cvt_pk_bf16_f32 v132, v164, v165\n"
      "v_cvt_pk_bf16_f32 v134, v180, v181\n"
      "v_cvt_pk_bf16_f32 v133, v166, v167\n"
      "v_cvt_pk_bf16_f32 v135, v182, v183\n"
      "v_pk_mul_f32 v[168:169], v[168:169], v[226:227] op_sel:[0,0] op_sel_hi:[1,0]\n"
      "v_permlane16_swap_b32 v132, v134\n"
      "v_permlane16_swap_b32 v133, v135\n"
      "global_store_dwordx4 v156, v[132:135], s[16:17] offset:0 sc1\n"
      "v_pk_mul_f32 v[170:171], v[170:171], v[226:227] op_sel:[0,0] op_sel_hi:[1,0]\n"
      "v_pk_mul_f32 v[184:185], v[184:185], v[226:227] op_sel:[0,0] op_sel_hi:[1,0]\n"
      "v_pk_mul_f32 v[186:187], v[186:187], v[226:227] op_sel:[0,0] op_sel_hi:[1,0]\n"
      "v_cvt_pk_bf16_f32 v136, v168, v169\n"
      "v_cvt_pk_bf16_f32 v138, v184, v185\n"
      "v_cvt_pk_bf16_f32 v137, v170, v171\n"
      "v_cvt_pk_bf16_f32 v139, v186, v187\n"
      "v_pk_mul_f32 v[172:173], v[172:173], v[226:227] op_sel:[0,1] op_sel_hi:[1,1]\n"
      "v_permlane16_swap_b32 v136, v138\n"
      "v_permlane16_swap_b32 v137, v139\n"
      "global_store_dwordx4 v155, v[136:139], s[16:17] offset:-4096 sc1\n"
      "v_pk_mul_f32 v[174:175], v[174:175], v[226:227] op_sel:[0,1] op_sel_hi:[1,1]\n"
      "v_pk_mul_f32 v[188:189], v[188:189], v[226:227] op_sel:[0,1] op_sel_hi:[1,1]\n"
      "v_pk_mul_f32 v[190:191], v[190:191], v[226:227] op_sel:[0,1] op_sel_hi:[1,1]\n"
      "v_cvt_pk_bf16_f32 v140, v172, v173\n"
      "v_cvt_pk_bf16_f32 v142, v188, v189\n"
      "v_cvt_pk_bf16_f32 v141, v174, v175\n"
      "v_cvt_pk_bf16_f32 v143, v190, v191\n"
      "v_and_b32 v157, 0xffffff80, v157\n"
      "v_permlane16_swap_b32 v140, v142\n"
      "v_permlane16_swap_b32 v141, v143\n"
      "global_store_dwordx4 v155, v[140:143], s[16:17] offset:0 sc1\n"
      "s_and_b32 s67, %[fo], 8\n"
      "s_or_b32 s67, s67, %[xw]\n"
      "s_cbranch_scc1 END_%=\n"
      "s_mov_b64 exec, 1\n"
      "global_store_dwordx2 v157, v[150:151], s[18:19] sc1\n"
      "s_mov_b64 exec, -1\n"
      "END_%=:\n"
      ".subsection 1\n"
      "POLL_%=:\n"
      "s_waitcnt vmcnt(0) lgkmcnt(0)\n"
      "v_cmp_ne_u32 vcc, v234, v150\n"
      "v_mov_b32 v233, v235\n"
      "s_memrealtime s[68:69]\n"
      "s_sub_u32 s63, s63, 1\n"
      "s_cbranch_vccz GOT_%=\n"
      "global_load_dwordx2 v[234:235], v158, s[18:19] sc1\n"
      "s_cbranch_scc0 POLL_%=\n"
      "s_branch GOT_%=\n"
      "RETRY_%=:\n"
      "s_sub_u32 s63, s63, 1\n"
      "s_cbranch_scc1 OK_%=\n"
      "s_sleep 4\n"
      "buffer_load_dwordx4 v[80:83], v127, s[28:31], s79 offen sc1\n"
      "buffer_load_dwordx4 v[64:67], v127, s[28:31], s80 offen offset:0 sc1\n"
      "buffer_load_dwordx4 v[68:71], v127, s[28:31], s80 offen offset:1024 sc1\n"
      "buffer_load_dwordx4 v[72:75], v127, s[28:31], s80 offen offset:2048 sc1\n"
      "buffer_load_dwordx4 v[76:79], v127, s[28:31], s80 offen offset:3072 sc1\n"
      "s_branch CHK_%=\n"
      "GW0_%=:\n"
      "s_call_b64 s[84:85], WAIT_%=\n"
      "s_branch GX0_%=\n"
      "GW1_%=:\n"
      "s_call_b64 s[84:85], WAIT_%=\n"
      "s_branch GX1_%=\n"
      "GW2_%=:\n"
      "s_call_b64 s[84:85], WAIT_%=\n"
      "s_branch GX2_%=\n"
      "GW3_%=:\n"
      "s_call_b64 s[84:85], WAIT_%=\n"
      "s_add_u32 s87, s87, 1\n"
      "s_branch GX3_%=\n"
      "GW4_%=:\n"
      "s_call_b64 s[84:85], WAIT_%=\n"
      "s_branch GX4_%=\n"
      "GW5_%=:\n"
      "s_call_b64 s[84:85], WAIT_%=\n"
      "s_branch GX5_%=\n"
      "GW6_%=:\n"
      "s_call_b64 s[84:85], WAIT_%=\n"
      "s_branch GX6_%=\n"
      "GW7_%=:\n"
      "s_call_b64 s[84:85], WAIT_%=\n"
      "s_branch GX7_%=\n"
      "GW8_%=:\n"
      "s_call_b64 s[84:85], WAIT_%=\n"
      "s_branch GX8_%=\n"
      "WAIT_%=:\n"
      "s_add_u32 s87, s87, 1\n"
      "s_bitcmp1_b32 s89, s87\n"
      "s_cbranch_scc1 H1_%=\n"
      "SPIN_%=:\n"
      "s_sub_u32 s67, s68, s88\n"
      "s_cmp_ge_i32 s67, 0\n"
      "s_cbranch_scc1 GO_%=\n"
      "s_sleep 1\n"
      "s_memrealtime s[68:69]\n"
      "s_waitcnt lgkmcnt(0)\n"
      "s_branch SPIN_%=\n"
      "GO_%=:\n"
      "s_setpc_b64 s[84:85]\n"
      "H1_%=:\n"
      "s_cmp_gt_i32 s93, 50\n"
      "s_cbranch_scc1 HS_%=\n"
      "s_bitcmp1_b32 0x2a80, s87\n"
      "s_cbranch_scc0 H2_%=\n"
      "s_add_u32 s67, s66, 400\n"
      "s_waitcnt vmcnt(8)\n"
      "v_sub_u32 v250, s67, v250\n"
      "v_cmp_gt_u32 vcc, 900, v250\n"
      "v_add_u32 v250, 0xa00, v250\n"
      "v_xor_b32 v251, 0x100, v251\n"
      "s_sub_u32 s69, s66, s71\n"
      "s_cmp_ge_u32 s87, 12\n"
      "s_cselect_b32 s101, 50, s101\n"
      "s_bcnt1_i32_b32 s74, vcc_lo\n"
      "s_bcnt1_i32_b32 s75, vcc_hi\n"
      "v_cmp_gt_u32 vcc, 0x800, v250\n"
      "s_add_u32 s67, s74, s75\n"
      "s_cmp_eq_u32 s87, 7\n"
      "s_cselect_b32 s76, 1, 0\n"
      "s_lshr_b32 s76, s67, s76\n"
      "s_cmp_le_u32 s76, 2\n"
      "s_cselect_b32 s86, 107, s98\n"
      "s_cmp_ge_u32 s76, 9\n"
      "s_cselect_b32 s86, 112, s86\n"
      "s_cbranch_vccnz H6_%=\n"
      "s_cmp_ge_u32 s74, 12\n"
      "s_cbranch_scc1 H3_%=\n"
      "s_cmp_eq_u32 s87, 13\n"
      "s_cbranch_scc1 H4_%=\n"
      "H8_%=:\n"
      "s_lshr_b32 s67, %[rep], 7\n"
      "s_cmp_eq_u32 s67, 1\n"
      "s_cbranch_scc0 SPIN_%=\n"
      "H5_%=:\n"
      "s_cmp_gt_i32 s93, 50\n"
      "s_cselect_b32 s75, -10, 0\n"
      "v_mov_b32 v127, s66\n"
      "s_add_u32 s74, s87, 2\n"
      "s_bfe_u32 s74, s74, 0x10001\n"
      "s_lshl_b32 s74, s74, 8\n"
      "s_add_u32 s74, s74, %[rep]\n"
      "RS_%=:\n"
      "s_sub_u32 s67, s68, s88\n"
      "s_cmp_ge_i32 s67, s75\n"
      "s_cbranch_scc1 RG_%=\n"
      "s_sleep 1\n"
      "s_memrealtime s[68:69]\n"
      "s_waitcnt lgkmcnt(0)\n"
      "s_branch RS_%=\n"
      "RG_%=:\n"
      "s_getreg_b32 s67, hwreg(HW_REG_IB_STS, 0, 4)\n"
      "s_cmp_le_u32 s67, 4\n"
      "s_cbranch_scc1 SPIN_%=\n"
      "buffer_store_dword v127, v124, s[28:31], s74 offen sc1\n"
      "s_branch SPIN_%=\n"
      "H6_%=:\n"
      "s_ff1_i32_b64 s67, vcc\n"
      "v_sub_u32 v250, s66, v250\n"
      "s_nop 3\n"
      "v_readlane_b32 s76, v250, s67\n"
      "s_add_u32 s76, s76, 2960\n"
      "s_and_b32 s76, s76, 31\n"
      "s_lshl_b32 s76, s76, 3\n"
      "s_lshl_b32 s89, 4, s87\n"
      "s_branch H7_%=\n"
      "H3_%=:\n"
      "s_mul_i32 s76, s69, 110\n"
      "s_ashr_i32 s76, s76, 10\n"
      "s_min_i32 s76, s76, 40\n"
      "s_add_u32 s76, s76, 84\n"
      "s_cmp_lt_u32 s75, 13\n"
      "s_cselect_b32 s76, 56, s76\n"
      "s_cmp_lt_u32 s75, 5\n"
      "s_cselect_b32 s76, 32, s76\n"
      "s_add_u32 s67, s66, 2000\n"
      "s_and_b32 s67, s67, -32\n"
      "s_lshr_b32 s75, s76, 3\n"
      "s_or_b32 s67, s67, s75\n"
      "v_mov_b32 v127, s67\n"
      "s_add_u32 s74, s87, 2\n"
      "s_bfe_u32 s74, s74, 0x10001\n"
      "s_lshl_b32 s74, s74, 8\n"
      "s_add_u32 s74, s74, %[rep]\n"
      "s_cmp_lt_u32 %[rep], 128\n"
      "s_cselect_b32 s89, 22, 20\n"
      "s_lshl_b32 s89, s89, s87\n"
      "s_sub_u32 s75, %[rep], 128\n"
      "s_cmp_lt_u32 s75, 32\n"
      "s_cbranch_scc0 H7_%=\n"
      "buffer_store_dword v127, v124, s[28:31], s74 offen sc1\n"
      "H7_%=:\n"
      "s_add_u32 s66, s66, s76\n"
      "s_mov_b32 s99, 13\n"
      "s_mov_b32 s100, 286\n"
      "s_add_u32 s93, s93, 99\n"
      "s_mov_b32 s86, 110\n"
      "s_mov_b32 s80, 0xc853212b\n"
      "s_mov_b32 s81, 0x842210\n"
      "s_cmp_le_u32 s76, 56\n"
      "s_cselect_b32 s75, 10, 0\n"
      "s_cmp_le_u32 s76, 32\n"
      "s_cselect_b32 s75, 25, s75\n"
      "s_lshr_b64 s[80:81], s[80:81], s75\n"
      "s_sub_u32 s67, s65, s66\n"
      "s_cmp_lt_i32 s67, 0\n"
      "s_cselect_b32 s88, s66, s65\n"
      "s_mov_b32 s101, 200\n"
      "s_branch SPIN_%=\n"
      "H4_%=:\n"
      "s_mov_b32 s91, 0\n"
      "s_mov_b32 s89, 0\n"
      "v_mov_b32 v251, 0x4000000\n"
      "s_branch SPIN_%=\n"
      "HS_%=:\n"
      "s_bitcmp1_b32 s87, 0\n"
      "s_cbranch_scc0 H5_%=\n"
      "v_xor_b32 v251, 0x100, v251\n"
      "s_lshr_b32 s67, s89, s87\n"
      "s_cmp_gt_u32 s67, 1\n"
      "s_cbranch_scc1 SPIN_%=\n"
      "s_add_u32 s67, s66, 400\n"
      "s_waitcnt vmcnt(8)\n"
      "v_sub_u32 v250, s67, v250\n"
      "v_cmp_gt_u32 vcc, 900, v250\n"
      "s_nop 3\n"
      "s_bcnt1_i32_b32 s74, vcc_lo\n"
      "s_cmp_gt_u32 s74, 2\n"
      "s_cbranch_scc1 H4_%=\n"
      "s_mov_b32 s99, 21\n"
      "s_mov_b32 s100, 282\n"
      "s_mov_b32 s86, 107\n"
      "s_mov_b64 s[80:81], 0\n"
      "s_branch H4_%=\n"
      "H0_%=:\n"
      "buffer_load_dword v250, v251, s[28:31], 0 offen sc1\n"
      "s_branch SPIN_%=\n"
      "H9_%=:\n"
      "s_bitcmp1_b32 s87, 0\n"
      "s_cbranch_scc0 H5_%=\n"
      "v_xor_b32 v251, 0x100, v251\n"
      "s_cmp_eq_u32 s87, 5\n"
      "s_cbranch_scc1 H8_%=\n"
      "s_branch SPIN_%=\n"
      "H2_%=:\n"
      "s_cmp_gt_u32 s87, 2\n"
      "s_cbranch_scc1 H9_%=\n"
      "s_cmp_eq_u32 s87, 1\n"
      "s_cbranch_scc1 H0_%=\n"
      "s_waitcnt vmcnt(8)\n"
      "v_subrev_u32 v250, s71, v250\n"
      "v_min_u32 v250, 0x258, v250\n"
      "v_mbcnt_lo_u32_b32 v251, -1, 0\n"
      "v_mbcnt_hi_u32_b32 v251, -1, v251\n"
      "v_add_u32_dpp v250, v250, v250 row_shr:1 row_mask:0xf bank_mask:0xf bound_ctrl:0\n"
      "v_lshlrev_b32 v124, 23, v251\n"
      "s_nop 0\n"
      "v_add_u32_dpp v250, v250, v250 row_shr:2 row_mask:0xf bank_mask:0xf bound_ctrl:0\n"
      "v_lshlrev_b32 v251, 2, v251\n"
      "v_add_u32 v124, 0x6000, v124\n"
      "v_add_u32_dpp v250, v250, v250 row_shr:4 row_mask:0xf bank_mask:0xf bound_ctrl:0\n"
      "v_add_u32 v251, 0x6100, v251\n"
      "v_readlane_b32 s66, v250, 7\n"
      "s_nop 0\n"
      "s_lshr_b32 s66, s66, 3\n"
      "s_add_u32 s66, s66, s71\n"
      "s_add_u32 s66, s66, %[cph]\n"
      "s_add_u32 s71, s66, 550\n"
      "s_sub_u32 s67, s65, s66\n"
      "s_cmp_lt_i32 s67, 0\n"
      "s_cselect_b32 s88, s66, s65\n"
      "s_branch SPIN_%=\n"
      "FB0_%=:\n"
      "s_nop 7\n"
      "s_nop 7\n"
      "v_xor_b32 v118, 0x0, v242\n"
      "v_xor_b32 v119, 0x0, v243\n"
      "ds_read_b128 a[64:67], v118 offset:0\n"
      "ds_read_b128 a[68:71], v118 offset:4096\n"
      "ds_read_b128 a[72:75], v119 offset:0\n"
      "ds_read_b128 a[76:79], v119 offset:4096\n"
      "ds_read_b128 a[80:83], v118 offset:1024\n"
      "ds_read_b128 a[84:87], v118 offset:5120\n"
      "ds_read_b128 a[88:91], v119 offset:1024\n"
      "ds_read_b128 a[92:95], v119 offset:5120\n"
      "s_waitcnt lgkmcnt(0)\n"
      "v_mfma_f32_16x16x32_bf16 v[128:131], a[64:67], a[0:3], 0\n"
      "v_mfma_f32_16x16x32_bf16 v[136:139], a[64:67], a[4:7], 0\n"
      "v_mfma_f32_16x16x32_bf16 v[144:147], a[64:67], a[8:11], 0\n"
      "v_mfma_f32_16x16x32_bf16 v[152:155], a[64:67], a[12:15], 0\n"
      "v_mfma_f32_16x16x32_bf16 v[132:135], a[68:71], a[0:3], 0\n"
      "v_mfma_f32_16x16x32_bf16 v[140:143], a[68:71], a[4:7], 0\n"
      "v_mfma_f32_16x16x32_bf16 v[148:151], a[68:71], a[8:11], 0\n"
      "v_mfma_f32_16x16x32_bf16 v[156:159], a[68:71], a[12:15], 0\n"
      "v_mfma_f32_16x16x32_bf16 v[128:131], a[72:75], a[16:19], v[128:131]\n"
      "v_mfma_f32_16x16x32_bf16 v[136:139], a[72:75], a[20:23], v[136:139]\n"
      "v_mfma_f32_16x16x32_bf16 v[144:147], a[72:75], a[24:27], v[144:147]\n"
      "v_mfma_f32_16x16x32_bf16 v[152:155], a[72:75], a[28:31], v[152:155]\n"
      "v_mfma_f32_16x16x32_bf16 v[132:135], a[76:79], a[16:19], v[132:135]\n"
      "v_mfma_f32_16x16x32_bf16 v[140:143], a[76:79], a[20:23], v[140:143]\n"
      "v_mfma_f32_16x16x32_bf16 v[148:151], a[76:79], a[24:27], v[148:151]\n"
      "v_mfma_f32_16x16x32_bf16 v[156:159], a[76:79], a[28:31], v[156:159]\n"
      "v_mfma_f32_16x16x32_bf16 v[128:131], a[80:83], a[32:35], v[128:131]\n"
      "v_mfma_f32_16x16x32_bf16 v[136:139], a[80:83], a[36:39], v[136:139]\n"
      "v_mfma_f32_16x16x32_bf16 v[144:147], a[80:83], a[40:43], v[144:147]\n"
      "v_mfma_f32_16x16x32_bf16 v[152:155], a[80:83], a[44:47], v[152:155]\n"
      "v_mfma_f32_16x16x32_bf16 v[132:135], a[84:87], a[32:35], v[132:135]\n"
      "v_mfma_f32_16x16x32_bf16 v[140:143], a[84:87], a[36:39], v[140:143]\n"
      "v_mfma_f32_16x16x32_bf16 v[148:151], a[84:87], a[40:43], v[148:151]\n"
      "v_mfma_f32_16x16x32_bf16 v[156:159], a[84:87], a[44:47], v[156:159]\n"
      "v_mfma_f32_16x16x32_bf16 v[128:131], a[88:91], a[48:51], v[128:131]\n"
      "v_mfma_f32_16x16x32_bf16 v[136:139], a[88:91], a[52:55], v[136:139]\n"
      "v_mfma_f32_16x16x32_bf16 v[144:147], a[88:91], a[56:59], v[144:147]\n"
      "v_mfma_f32_16x16x32_bf16 v[152:155], a[88:91], a[60:63], v[152:155]\n"
      "v_mfma_f32_16x16x32_bf16 v[132:135], a[92:95], a[48:51], v[132:135]\n"
      "v_mfma_f32_16x16x32_bf16 v[140:143], a[92:95], a[52:55], v[140:143]\n"
      "v_mfma_f32_16x16x32_bf16 v[148:151], a[92:95], a[56:59], v[148:151]\n"
      "v_mfma_f32_16x16x32_bf16 v[156:159], a[92:95], a[60:63], v[156:159]\n"
      "s_nop 7\n"
      "v_max3_f32 v112, v128, v129, v130\n"
      "v_max3_f32 v113, v136, v137, v138\n"
      "v_max3_f32 v114, v144, v145, v146\n"
      "v_max3_f32 v115, v152, v153, v154\n"
      "v_max3_f32 v112, v112, v131, v132\n"
      "v_max3_f32 v113, v113, v139, v140\n"
      "v_max3_f32 v114, v114, v147, v148\n"
      "v_max3_f32 v115, v115, v155, v156\n"
      "v_max3_f32 v112, v112, v133, v134\n"
      "v_max3_f32 v113, v113, v141, v142\n"
      "v_max3_f32 v114, v114, v149, v150\n"
      "v_max3_f32 v115, v115, v157, v158\n"
      "v_max_f32 v112, v112, v135\n"
      "v_max_f32 v113, v113, v143\n"
      "v_max_f32 v114, v114, v151\n"
      "v_max_f32 v115, v115, v159\n"
      "v_mov_b32 v160, v112\n"
      "v_mov_b32 v161, v113\n"
      "v_mov_b32 v162, v114\n"
      "v_mov_b32 v163, v115\n"
      "v_permlane16_swap_b32 v112, v160\n"
      "v_permlane16_swap_b32 v113, v161\n"
      "v_permlane16_swap_b32 v114, v162\n"
      "v_permlane16_swap_b32 v115, v163\n"
      "v_max_f32 v112, v112, v160\n"
      "v_max_f32 v113, v113, v161\n"
      "v_max_f32 v114, v114, v162\n"
      "v_max_f32 v115, v115, v163\n"
      "v_mov_b32 v160, v112\n"
      "v_mov_b32 v161, v113\n"
      "v_mov_b32 v162, v114\n"
      "v_mov_b32 v163, v115\n"
      "v_permlane32_swap_b32 v112, v160\n"
      "v_permlane32_swap_b32 v113, v161\n"
      "v_permlane32_swap_b32 v114, v162\n"
      "v_permlane32_swap_b32 v115, v163\n"
      "v_max_f32 v112, v112, v160\n"
      "v_max_f32 v113, v113, v161\n"
      "v_max_f32 v114, v114, v162\n"
      "v_max_f32 v115, v115, v163\n"
      "v_sub_f32 v112, 0xc1800000, v112\n"
      "v_sub_f32 v113, 0xc1800000, v113\n"
      "v_sub_f32 v114, 0xc1800000, v114\n"
      "v_sub_f32 v115, 0xc1800000, v115\n"
      "v_floor_f32 v112, v112\n"
      "v_floor_f32 v113, v113\n"
      "v_floor_f32 v114, v114\n"
      "v_floor_f32 v115, v115\n"
      "v_min_f32 v112, v112, v224\n"
      "v_min_f32 v113, v113, v228\n"
      "v_min_f32 v114, v114, v232\n"
      "v_min_f32 v115, v115, v236\n"
      "v_sub_f32 v164, v112, v224\n"
      "v_sub_f32 v165, v113, v228\n"
      "v_sub_f32 v166, v114, v232\n"
      "v_sub_f32 v167, v115, v236\n"
      "v_mov_b32 v224, v112\n"
      "v_mov_b32 v228, v113\n"
      "v_mov_b32 v232, v114\n"
      "v_mov_b32 v236, v115\n"
      "v_exp_f32 v112, v164\n"
      "v_exp_f32 v113, v165\n"
      "v_exp_f32 v114, v166\n"
      "v_exp_f32 v115, v167\n"
      "v_mov_b32 v225, v224\n"
      "v_mov_b32 v229, v228\n"
      "v_mov_b32 v233, v232\n"
      "v_mov_b32 v237, v236\n"
      "v_mov_b64 v[226:227], v[224:225]\n"
      "v_mov_b64 v[230:231], v[228:229]\n"
      "v_mov_b64 v[234:235], v[232:233]\n"
      "v_mov_b64 v[238:239], v[236:237]\n"
      "s_call_b64 s[84:85], SCALE_%=\n"
      "v_pk_add_f32 v[128:129], v[128:129], v[224:225]\n"
      "v_pk_add_f32 v[130:131], v[130:131], v[224:225]\n"
      "v_pk_add_f32 v[132:133], v[132:133], v[224:225]\n"
      "v_pk_add_f32 v[134:135], v[134:135], v[224:225]\n"
      "v_pk_add_f32 v[136:137], v[136:137], v[228:229]\n"
      "v_pk_add_f32 v[138:139], v[138:139], v[228:229]\n"
      "v_pk_add_f32 v[140:141], v[140:141], v[228:229]\n"
      "v_pk_add_f32 v[142:143], v[142:143], v[228:229]\n"
      "v_pk_add_f32 v[144:145], v[144:145], v[232:233]\n"
      "v_pk_add_f32 v[146:147], v[146:147], v[232:233]\n"
      "v_pk_add_f32 v[148:149], v[148:149], v[232:233]\n"
      "v_pk_add_f32 v[150:151], v[150:151], v[232:233]\n"
      "v_pk_add_f32 v[152:153], v[152:153], v[236:237]\n"
      "v_pk_add_f32 v[154:155], v[154:155], v[236:237]\n"
      "v_pk_add_f32 v[156:157], v[156:157], v[236:237]\n"
      "v_pk_add_f32 v[158:159], v[158:159], v[236:237]\n"
      "v_exp_f32 v112, v128\n"
      "v_exp_f32 v113, v129\n"
      "v_exp_f32 v114, v130\n"
      "v_cvt_pk_bf16_f32 v128, v112, v113\n"
      "v_exp_f32 v115, v131\n"
      "v_exp_f32 v112, v132\n"
      "v_cvt_pk_bf16_f32 v129, v114, v115\n"
      "v_exp_f32 v113, v133\n"
      "v_exp_f32 v114, v134\n"
      "v_cvt_pk_bf16_f32 v130, v112, v113\n"
      "v_exp_f32 v115, v135\n"
      "v_exp_f32 v112, v136\n"
      "v_cvt_pk_bf16_f32 v131, v114, v115\n"
      "v_exp_f32 v113, v137\n"
      "v_exp_f32 v114, v138\n"
      "v_cvt_pk_bf16_f32 v132, v112, v113\n"
      "v_exp_f32 v115, v139\n"
      "v_exp_f32 v112, v140\n"
      "v_cvt_pk_bf16_f32 v133, v114, v115\n"
      "v_exp_f32 v113, v141\n"
      "v_exp_f32 v114, v142\n"
      "v_cvt_pk_bf16_f32 v134, v112, v113\n"
      "v_exp_f32 v115, v143\n"
      "v_exp_f32 v112, v144\n"
      "v_cvt_pk_bf16_f32 v135, v114, v115\n"
      "v_exp_f32 v113, v145\n"
      "v_exp_f32 v114, v146\n"
      "v_cvt_pk_bf16_f32 v136, v112, v113\n"
      "v_exp_f32 v115, v147\n"
      "v_exp_f32 v112, v148\n"
      "v_cvt_pk_bf16_f32 v137, v114, v115\n"
      "v_exp_f32 v113, v149\n"
      "v_exp_f32 v114, v150\n"
      "v_cvt_pk_bf16_f32 v138, v112, v113\n"
      "v_exp_f32 v115, v151\n"
      "v_exp_f32 v112, v152\n"
      "v_cvt_pk_bf16_f32 v139, v114, v115\n"
      "v_exp_f32 v113, v153\n"
      "v_exp_f32 v114, v154\n"
      "v_cvt_pk_bf16_f32 v140, v112, v113\n"
      "v_exp_f32 v115, v155\n"
      "v_exp_f32 v112, v156\n"
      "v_cvt_pk_bf16_f32 v141, v114, v115\n"
      "v_exp_f32 v113, v157\n"
      "v_exp_f32 v114, v158\n"
      "v_cvt_pk_bf16_f32 v142, v112, v113\n"
      "v_exp_f32 v115, v159\n"
      "s_nop 0\n"
      "v_cvt_pk_bf16_f32 v143, v114, v115\n"
      "ds_read_b128 a[64:67], v242 offset:32768\n"
      "ds_read_b128 a[68:71], v242 offset:36864\n"
      "ds_read_b128 a[72:75], v243 offset:32768\n"
      "ds_read_b128 a[76:79], v243 offset:36864\n"
      "ds_read_b128 a[80:83], v242 offset:33792\n"
      "ds_read_b128 a[84:87], v242 offset:37888\n"
      "ds_read_b128 a[88:91], v243 offset:33792\n"
      "ds_read_b128 a[92:95], v243 offset:37888\n"
      "s_waitcnt lgkmcnt(0)\n"
      "v_mfma_f32_16x16x32_bf16 v[160:163], a[64:67], a[0:3], v[224:227]\n"
      "v_mfma_f32_16x16x32_bf16 v[168:171], a[64:67], a[4:7], v[228:231]\n"
      "v_mfma_f32_16x16x32_bf16 v[176:179], a[64:67], a[8:11], v[232:235]\n"
      "v_mfma_f32_16x16x32_bf16 v[184:187], a[64:67], a[12:15], v[236:239]\n"
      "v_mfma_f32_16x16x32_bf16 v[164:167], a[68:71], a[0:3], v[224:227]\n"
      "v_mfma_f32_16x16x32_bf16 v[172:175], a[68:71], a[4:7], v[228:231]\n"
      "v_mfma_f32_16x16x32_bf16 v[180:183], a[68:71], a[8:11], v[232:235]\n"
      "v_mfma_f32_16x16x32_bf16 v[188:191], a[68:71], a[12:15], v[236:239]\n"
      "v_mfma_f32_16x16x32_bf16 v[160:163], a[72:75], a[16:19], v[160:163]\n"
      "v_mfma_f32_16x16x32_bf16 v[168:171], a[72:75], a[20:23], v[168:171]\n"
      "v_mfma_f32_16x16x32_bf16 v[176:179], a[72:75], a[24:27], v[176:179]\n"
      "v_mfma_f32_16x16x32_bf16 v[184:187], a[72:75], a[28:31], v[184:187]\n"
      "v_mfma_f32_16x16x32_bf16 v[164:167], a[76:79], a[16:19], v[164:167]\n"
      "v_mfma_f32_16x16x32_bf16 v[172:175], a[76:79], a[20:23], v[172:175]\n"
      "v_mfma_f32_16x16x32_bf16 v[180:183], a[76:79], a[24:27], v[180:183]\n"
      "v_mfma_f32_16x16x32_bf16 v[188:191], a[76:79], a[28:31], v[188:191]\n"
      "v_mfma_f32_16x16x32_bf16 v[160:163], a[80:83], a[32:35], v[160:163]\n"
      "v_mfma_f32_16x16x32_bf16 v[168:171], a[80:83], a[36:39], v[168:171]\n"
      "v_mfma_f32_16x16x32_bf16 v[176:179], a[80:83], a[40:43], v[176:179]\n"
      "v_mfma_f32_16x16x32_bf16 v[184:187], a[80:83], a[44:47], v[184:187]\n"
      "v_mfma_f32_16x16x32_bf16 v[164:167], a[84:87], a[32:35], v[164:167]\n"
      "v_mfma_f32_16x16x32_bf16 v[172:175], a[84:87], a[36:39], v[172:175]\n"
      "v_mfma_f32_16x16x32_bf16 v[180:183], a[84:87], a[40:43], v[180:183]\n"
      "v_mfma_f32_16x16x32_bf16 v[188:191], a[84:87], a[44:47], v[188:191]\n"
      "v_mfma_f32_16x16x32_bf16 v[160:163], a[88:91], a[48:51], v[160:163]\n"
      "v_mfma_f32_16x16x32_bf16 v[168:171], a[88:91], a[52:55], v[168:171]\n"
      "v_mfma_f32_16x16x32_bf16 v[176:179], a[88:91], a[56:59], v[176:179]\n"
      "v_mfma_f32_16x16x32_bf16 v[184:187], a[88:91], a[60:63], v[184:187]\n"
      "v_mfma_f32_16x16x32_bf16 v[164:167], a[92:95], a[48:51], v[164:167]\n"
      "v_mfma_f32_16x16x32_bf16 v[172:175], a[92:95], a[52:55], v[172:175]\n"
      "v_mfma_f32_16x16x32_bf16 v[180:183], a[92:95], a[56:59], v[180:183]\n"
      "v_mfma_f32_16x16x32_bf16 v[188:191], a[92:95], a[60:63], v[188:191]\n"
      "s_cmp_eq_u32 s64, 0\n"
      "s_cbranch_scc0 NRET0_0_%=\n"
      "v_mov_b64 v[192:193], v[128:129]\n"
      "v_mov_b64 v[194:195], v[130:131]\n"
      "v_mov_b64 v[196:197], v[132:133]\n"
      "v_mov_b64 v[198:199], v[134:135]\n"
      "v_mov_b64 v[200:201], v[136:137]\n"
      "v_mov_b64 v[202:203], v[138:139]\n"
      "v_mov_b64 v[204:205], v[140:141]\n"
      "v_mov_b64 v[206:207], v[142:143]\n"
      "s_nop 1\n"
      "s_branch RET0_0_%=\n"
      "NRET0_0_%=:\n"
      "s_cmp_eq_u32 s64, 1\n"
      "s_cbranch_scc0 NRET0_1_%=\n"
      "v_mov_b64 v[192:193], v[128:129]\n"
      "v_mov_b64 v[194:195], v[130:131]\n"
      "v_mov_b64 v[196:197], v[132:133]\n"
      "v_mov_b64 v[198:199], v[134:135]\n"
      "v_mov_b64 v[200:201], v[136:137]\n"
      "v_mov_b64 v[202:203], v[138:139]\n"
      "v_mov_b64 v[204:205], v[140:141]\n"
      "v_mov_b64 v[206:207], v[142:143]\n"
      "s_nop 1\n"
      "s_branch RET0_1_%=\n"
      "NRET0_1_%=:\n"
      "s_cmp_eq_u32 s64, 2\n"
      "s_cbranch_scc0 NRET0_2_%=\n"
      "v_mov_b64 v[192:193], v[128:129]\n"
      "v_mov_b64 v[194:195], v[130:131]\n"
      "v_mov_b64 v[196:197], v[132:133]\n"
      "v_mov_b64 v[198:199], v[134:135]\n"
      "v_mov_b64 v[200:201], v[136:137]\n"
      "v_mov_b64 v[202:203], v[138:139]\n"
      "v_mov_b64 v[204:205], v[140:141]\n"
      "v_mov_b64 v[206:207], v[142:143]\n"
      "s_nop 1\n"
      "s_branch RET0_2_%=\n"
      "NRET0_2_%=:\n"
      "s_cmp_eq_u32 s64, 3\n"
      "s_cbranch_scc0 NRET0_3_%=\n"
      "v_mov_b64 v[192:193], v[128:129]\n"
      "v_mov_b64 v[194:195], v[130:131]\n"
      "v_mov_b64 v[196:197], v[132:133]\n"
      "v_mov_b64 v[198:199], v[134:135]\n"
      "v_mov_b64 v[200:201], v[136:137]\n"
      "v_mov_b64 v[202:203], v[138:139]\n"
      "v_mov_b64 v[204:205], v[140:141]\n"
      "v_mov_b64 v[206:207], v[142:143]\n"
      "s_nop 1\n"
      "s_branch RET0_3_%=\n"
      "NRET0_3_%=:\n"
      "s_cmp_eq_u32 s64, 4\n"
      "s_cbranch_scc0 NRET0_4_%=\n"
      "v_mov_b64 v[192:193], v[128:129]\n"
      "v_mov_b64 v[194:195], v[130:131]\n"
      "v_mov_b64 v[196:197], v[132:133]\n"
      "v_mov_b64 v[198:199], v[134:135]\n"
      "v_mov_b64 v[200:201], v[136:137]\n"
      "v_mov_b64 v[202:203], v[138:139]\n"
      "v_mov_b64 v[204:205], v[140:141]\n"
      "v_mov_b64 v[206:207], v[142:143]\n"
      "s_nop 1\n"
      "s_branch RET0_4_%=\n"
      "NRET0_4_%=:\n"
      "s_cmp_eq_u32 s64, 5\n"
      "s_cbranch_scc0 NRET0_5_%=\n"
      "v_mov_b64 v[16:17], v[128:129]\n"
      "v_mov_b64 v[18:19], v[130:131]\n"
      "v_mov_b64 v[20:21], v[132:133]\n"
      "v_mov_b64 v[22:23], v[134:135]\n"
      "v_mov_b64 v[24:25], v[136:137]\n"
      "v_mov_b64 v[26:27], v[138:139]\n"
      "v_mov_b64 v[28:29], v[140:141]\n"
      "v_mov_b64 v[30:31], v[142:143]\n"
      "s_nop 1\n"
      "s_branch RET0_5_%=\n"
      "NRET0_5_%=:\n"
      "FB1_%=:\n"
      "s_add_u32 s84, s63, 1\n"
      "s_sub_u32 s84, s84, %[n1]\n"
      "s_or_b32 s84, s84, s64\n"
      "s_cbranch_scc0 RET1_0_%=\n"
      "s_nop 7\n"
      "s_nop 7\n"
      "v_xor_b32 v118, 0x10000, v242\n"
      "v_xor_b32 v119, 0x10000, v243\n"
      "ds_read_b128 a[64:67], v118 offset:32768\n"
      "ds_read_b128 a[68:71], v118 offset:36864\n"
      "ds_read_b128 a[72:75], v119 offset:32768\n"
      "ds_read_b128 a[76:79], v119 offset:36864\n"
      "ds_read_b128 a[80:83], v118 offset:33792\n"
      "ds_read_b128 a[84:87], v118 offset:37888\n"
      "ds_read_b128 a[88:91], v119 offset:33792\n"
      "ds_read_b128 a[92:95], v119 offset:37888\n"
      "s_waitcnt lgkmcnt(0)\n"
      "v_mfma_f32_16x16x32_bf16 v[160:163], a[64:67], a[0:3], 0\n"
      "v_mfma_f32_16x16x32_bf16 v[168:171], a[64:67], a[4:7], 0\n"
      "v_mfma_f32_16x16x32_bf16 v[176:179], a[64:67], a[8:11], 0\n"
      "v_mfma_f32_16x16x32_bf16 v[184:187], a[64:67], a[12:15], 0\n"
      "v_mfma_f32_16x16x32_bf16 v[164:167], a[68:71], a[0:3], 0\n"
      "v_mfma_f32_16x16x32_bf16 v[172:175], a[68:71], a[4:7], 0\n"
      "v_mfma_f32_16x16x32_bf16 v[180:183], a[68:71], a[8:11], 0\n"
      "v_mfma_f32_16x16x32_bf16 v[188:191], a[68:71], a[12:15], 0\n"
      "v_mfma_f32_16x16x32_bf16 v[160:163], a[72:75], a[16:19], v[160:163]\n"
      "v_mfma_f32_16x16x32_bf16 v[168:171], a[72:75], a[20:23], v[168:171]\n"
      "v_mfma_f32_16x16x32_bf16 v[176:179], a[72:75], a[24:27], v[176:179]\n"
      "v_mfma_f32_16x16x32_bf16 v[184:187], a[72:75], a[28:31], v[184:187]\n"
      "v_mfma_f32_16x16x32_bf16 v[164:167], a[76:79], a[16:19], v[164:167]\n"
      "v_mfma_f32_16x16x32_bf16 v[172:175], a[76:79], a[20:23], v[172:175]\n"
      "v_mfma_f32_16x16x32_bf16 v[180:183], a[76:79], a[24:27], v[180:183]\n"
      "v_mfma_f32_16x16x32_bf16 v[188:191], a[76:79], a[28:31], v[188:191]\n"
      "v_mfma_f32_16x16x32_bf16 v[160:163], a[80:83], a[32:35], v[160:163]\n"
      "v_mfma_f32_16x16x32_bf16 v[168:171], a[80:83], a[36:39], v[168:171]\n"
      "v_mfma_f32_16x16x32_bf16 v[176:179], a[80:83], a[40:43], v[176:179]\n"
      "v_mfma_f32_16x16x32_bf16 v[184:187], a[80:83], a[44:47], v[184:187]\n"
      "v_mfma_f32_16x16x32_bf16 v[164:167], a[84:87], a[32:35], v[164:167]\n"
      "v_mfma_f32_16x16x32_bf16 v[172:175], a[84:87], a[36:39], v[172:175]\n"
      "v_mfma_f32_16x16x32_bf16 v[180:183], a[84:87], a[40:43], v[180:183]\n"
      "v_mfma_f32_16x16x32_bf16 v[188:191], a[84:87], a[44:47], v[188:191]\n"
      "v_mfma_f32_16x16x32_bf16 v[160:163], a[88:91], a[48:51], v[160:163]\n"
      "v_mfma_f32_16x16x32_bf16 v[168:171], a[88:91], a[52:55], v[168:171]\n"
      "v_mfma_f32_16x16x32_bf16 v[176:179], a[88:91], a[56:59], v[176:179]\n"
      "v_mfma_f32_16x16x32_bf16 v[184:187], a[88:91], a[60:63], v[184:187]\n"
      "v_mfma_f32_16x16x32_bf16 v[164:167], a[92:95], a[48:51], v[164:167]\n"
      "v_mfma_f32_16x16x32_bf16 v[172:175], a[92:95], a[52:55], v[172:175]\n"
      "v_mfma_f32_16x16x32_bf16 v[180:183], a[92:95], a[56:59], v[180:183]\n"
      "v_mfma_f32_16x16x32_bf16 v[188:191], a[92:95], a[60:63], v[188:191]\n"
      "s_nop 7\n"
      "v_max3_f32 v112, v160, v161, v162\n"
      "v_max3_f32 v113, v168, v169, v170\n"
      "v_max3_f32 v114, v176, v177, v178\n"
      "v_max3_f32 v115, v184, v185, v186\n"
      "v_max3_f32 v112, v112, v163, v164\n"
      "v_max3_f32 v113, v113, v171, v172\n"
      "v_max3_f32 v114, v114, v179, v180\n"
      "v_max3_f32 v115, v115, v187, v188\n"
      "v_max3_f32 v112, v112, v165, v166\n"
      "v_max3_f32 v113, v113, v173, v174\n"
      "v_max3_f32 v114, v114, v181, v182\n"
      "v_max3_f32 v115, v115, v189, v190\n"
      "v_max_f32 v112, v112, v167\n"
      "v_max_f32 v113, v113, v175\n"
      "v_max_f32 v114, v114, v183\n"
      "v_max_f32 v115, v115, v191\n"
      "v_mov_b32 v128, v112\n"
      "v_mov_b32 v129, v113\n"
      "v_mov_b32 v130, v114\n"
      "v_mov_b32 v131, v115\n"
      "v_permlane16_swap_b32 v112, v128\n"
      "v_permlane16_swap_b32 v113, v129\n"
      "v_permlane16_swap_b32 v114, v130\n"
      "v_permlane16_swap_b32 v115, v131\n"
      "v_max_f32 v112, v112, v128\n"
      "v_max_f32 v113, v113, v129\n"
      "v_max_f32 v114, v114, v130\n"
      "v_max_f32 v115, v115, v131\n"
      "v_mov_b32 v128, v112\n"
      "v_mov_b32 v129, v113\n"
      "v_mov_b32 v130, v114\n"
      "v_mov_b32 v131, v115\n"
      "v_permlane32_swap_b32 v112, v128\n"
      "v_permlane32_swap_b32 v113, v129\n"
      "v_permlane32_swap_b32 v114, v130\n"
      "v_permlane32_swap_b32 v115, v131\n"
      "v_max_f32 v112, v112, v128\n"
      "v_max_f32 v113, v113, v129\n"
      "v_max_f32 v114, v114, v130\n"
      "v_max_f32 v115, v115, v131\n"
      "v_sub_f32 v112, 0xc1800000, v112\n"
      "v_sub_f32 v113, 0xc1800000, v113\n"
      "v_sub_f32 v114, 0xc1800000, v114\n"
      "v_sub_f32 v115, 0xc1800000, v115\n"
      "v_floor_f32 v112, v112\n"
      "v_floor_f32 v113, v113\n"
      "v_floor_f32 v114, v114\n"
      "v_floor_f32 v115, v115\n"
      "v_min_f32 v112, v112, v224\n"
      "v_min_f32 v113, v113, v228\n"
      "v_min_f32 v114, v114, v232\n"
      "v_min_f32 v115, v115, v236\n"
      "v_sub_f32 v132, v112, v224\n"
      "v_sub_f32 v133, v113, v228\n"
      "v_sub_f32 v134, v114, v232\n"
      "v_sub_f32 v135, v115, v236\n"
      "v_mov_b32 v224, v112\n"
      "v_mov_b32 v228, v113\n"
      "v_mov_b32 v232, v114\n"
      "v_mov_b32 v236, v115\n"
      "v_exp_f32 v112, v132\n"
      "v_exp_f32 v113, v133\n"
      "v_exp_f32 v114, v134\n"
      "v_exp_f32 v115, v135\n"
      "v_mov_b32 v225, v224\n"
      "v_mov_b32 v229, v228\n"
      "v_mov_b32 v233, v232\n"
      "v_mov_b32 v237, v236\n"
      "v_mov_b64 v[226:227], v[224:225]\n"
      "v_mov_b64 v[230:231], v[228:229]\n"
      "v_mov_b64 v[234:235], v[232:233]\n"
      "v_mov_b64 v[238:239], v[236:237]\n"
      "s_call_b64 s[84:85], SCALE_%=\n"
      "v_pk_add_f32 v[160:161], v[160:161], v[224:225]\n"
      "v_pk_add_f32 v[162:163], v[162:163], v[224:225]\n"
      "v_pk_add_f32 v[164:165], v[164:165], v[224:225]\n"
      "v_pk_add_f32 v[166:167], v[166:167], v[224:225]\n"
      "v_pk_add_f32 v[168:169], v[168:169], v[228:229]\n"
      "v_pk_add_f32 v[170:171], v[170:171], v[228:229]\n"
      "v_pk_add_f32 v[172:173], v[172:173], v[228:229]\n"
      "v_pk_add_f32 v[174:175], v[174:175], v[228:229]\n"
      "v_pk_add_f32 v[176:177], v[176:177], v[232:233]\n"
      "v_pk_add_f32 v[178:179], v[178:179], v[232:233]\n"
      "v_pk_add_f32 v[180:181], v[180:181], v[232:233]\n"
      "v_pk_add_f32 v[182:183], v[182:183], v[232:233]\n"
      "v_pk_add_f32 v[184:185], v[184:185], v[236:237]\n"
      "v_pk_add_f32 v[186:187], v[186:187], v[236:237]\n"
      "v_pk_add_f32 v[188:189], v[188:189], v[236:237]\n"
      "v_pk_add_f32 v[190:191], v[190:191], v[236:237]\n"
      "v_exp_f32 v112, v160\n"
      "v_exp_f32 v113, v161\n"
      "v_exp_f32 v114, v162\n"
      "v_cvt_pk_bf16_f32 v160, v112, v113\n"
      "v_exp_f32 v115, v163\n"
      "v_exp_f32 v112, v164\n"
      "v_cvt_pk_bf16_f32 v161, v114, v115\n"
      "v_exp_f32 v113, v165\n"
      "v_exp_f32 v114, v166\n"
      "v_cvt_pk_bf16_f32 v162, v112, v113\n"
      "v_exp_f32 v115, v167\n"
      "v_exp_f32 v112, v168\n"
      "v_cvt_pk_bf16_f32 v163, v114, v115\n"
      "v_exp_f32 v113, v169\n"
      "v_exp_f32 v114, v170\n"
      "v_cvt_pk_bf16_f32 v164, v112, v113\n"
      "v_exp_f32 v115, v171\n"
      "v_exp_f32 v112, v172\n"
      "v_cvt_pk_bf16_f32 v165, v114, v115\n"
      "v_exp_f32 v113, v173\n"
      "v_exp_f32 v114, v174\n"
      "v_cvt_pk_bf16_f32 v166, v112, v113\n"
      "v_exp_f32 v115, v175\n"
      "v_exp_f32 v112, v176\n"
      "v_cvt_pk_bf16_f32 v167, v114, v115\n"
      "v_exp_f32 v113, v177\n"
      "v_exp_f32 v114, v178\n"
      "v_cvt_pk_bf16_f32 v168, v112, v113\n"
      "v_exp_f32 v115, v179\n"
      "v_exp_f32 v112, v180\n"
      "v_cvt_pk_bf16_f32 v169, v114, v115\n"
      "v_exp_f32 v113, v181\n"
      "v_exp_f32 v114, v182\n"
      "v_cvt_pk_bf16_f32 v170, v112, v113\n"
      "v_exp_f32 v115, v183\n"
      "v_exp_f32 v112, v184\n"
      "v_cvt_pk_bf16_f32 v171, v114, v115\n"
      "v_exp_f32 v113, v185\n"
      "v_exp_f32 v114, v186\n"
      "v_cvt_pk_bf16_f32 v172, v112, v113\n"
      "v_exp_f32 v115, v187\n"
      "v_exp_f32 v112, v188\n"
      "v_cvt_pk_bf16_f32 v173, v114, v115\n"
      "v_exp_f32 v113, v189\n"
      "v_exp_f32 v114, v190\n"
      "v_cvt_pk_bf16_f32 v174, v112, v113\n"
      "v_exp_f32 v115, v191\n"
      "s_nop 0\n"
      "v_cvt_pk_bf16_f32 v175, v114, v115\n"
      "ds_read_b128 a[64:67], v242 offset:0\n"
      "ds_read_b128 a[68:71], v242 offset:4096\n"
      "ds_read_b128 a[72:75], v243 offset:0\n"
      "ds_read_b128 a[76:79], v243 offset:4096\n"
      "ds_read_b128 a[80:83], v242 offset:1024\n"
      "ds_read_b128 a[84:87], v242 offset:5120\n"
      "ds_read_b128 a[88:91], v243 offset:1024\n"
      "ds_read_b128 a[92:95], v243 offset:5120\n"
      "s_waitcnt lgkmcnt(0)\n"
      "v_mfma_f32_16x16x32_bf16 v[128:131], a[64:67], a[0:3], v[224:227]\n"
      "v_mfma_f32_16x16x32_bf16 v[136:139], a[64:67], a[4:7], v[228:231]\n"
      "v_mfma_f32_16x16x32_bf16 v[144:147], a[64:67], a[8:11], v[232:235]\n"
      "v_mfma_f32_16x16x32_bf16 v[152:155], a[64:67], a[12:15], v[236:239]\n"
      "v_mfma_f32_16x16x32_bf16 v[132:135], a[68:71], a[0:3], v[224:227]\n"
      "v_mfma_f32_16x16x32_bf16 v[140:143], a[68:71], a[4:7], v[228:231]\n"
      "v_mfma_f32_16x16x32_bf16 v[148:151], a[68:71], a[8:11], v[232:235]\n"
      "v_mfma_f32_16x16x32_bf16 v[156:159], a[68:71], a[12:15], v[236:239]\n"
      "v_mfma_f32_16x16x32_bf16 v[128:131], a[72:75], a[16:19], v[128:131]\n"
      "v_mfma_f32_16x16x32_bf16 v[136:139], a[72:75], a[20:23], v[136:139]\n"
      "v_mfma_f32_16x16x32_bf16 v[144:147], a[72:75], a[24:27], v[144:147]\n"
      "v_mfma_f32_16x16x32_bf16 v[152:155], a[72:75], a[28:31], v[152:155]\n"
      "v_mfma_f32_16x16x32_bf16 v[132:135], a[76:79], a[16:19], v[132:135]\n"
      "v_mfma_f32_16x16x32_bf16 v[140:143], a[76:79], a[20:23], v[140:143]\n"
      "v_mfma_f32_16x16x32_bf16 v[148:151], a[76:79], a[24:27], v[148:151]\n"
      "v_mfma_f32_16x16x32_bf16 v[156:159], a[76:79], a[28:31], v[156:159]\n"
      "v_mfma_f32_16x16x32_bf16 v[128:131], a[80:83], a[32:35], v[128:131]\n"
      "v_mfma_f32_16x16x32_bf16 v[136:139], a[80:83], a[36:39], v[136:139]\n"
      "v_mfma_f32_16x16x32_bf16 v[144:147], a[80:83], a[40:43], v[144:147]\n"
      "v_mfma_f32_16x16x32_bf16 v[152:155], a[80:83], a[44:47], v[152:155]\n"
      "v_mfma_f32_16x16x32_bf16 v[132:135], a[84:87], a[32:35], v[132:135]\n"
      "v_mfma_f32_16x16x32_bf16 v[140:143], a[84:87], a[36:39], v[140:143]\n"
      "v_mfma_f32_16x16x32_bf16 v[148:151], a[84:87], a[40:43], v[148:151]\n"
      "v_mfma_f32_16x16x32_bf16 v[156:159], a[84:87], a[44:47], v[156:159]\n"
      "v_mfma_f32_16x16x32_bf16 v[128:131], a[88:91], a[48:51], v[128:131]\n"
      "v_mfma_f32_16x16x32_bf16 v[136:139], a[88:91], a[52:55], v[136:139]\n"
      "v_mfma_f32_16x16x32_bf16 v[144:147], a[88:91], a[56:59], v[144:147]\n"
      "v_mfma_f32_16x16x32_bf16 v[152:155], a[88:91], a[60:63], v[152:155]\n"
      "v_mfma_f32_16x16x32_bf16 v[132:135], a[92:95], a[48:51], v[132:135]\n"
      "v_mfma_f32_16x16x32_bf16 v[140:143], a[92:95], a[52:55], v[140:143]\n"
      "v_mfma_f32_16x16x32_bf16 v[148:151], a[92:95], a[56:59], v[148:151]\n"
      "v_mfma_f32_16x16x32_bf16 v[156:159], a[92:95], a[60:63], v[156:159]\n"
      "s_cmp_eq_u32 s64, 0\n"
      "s_cbranch_scc0 NRET1_0_%=\n"
      "v_mov_b64 v[208:209], v[160:161]\n"
      "v_mov_b64 v[210:211], v[162:163]\n"
      "v_mov_b64 v[212:213], v[164:165]\n"
      "v_mov_b64 v[214:215], v[166:167]\n"
      "v_mov_b64 v[216:217], v[168:169]\n"
      "v_mov_b64 v[218:219], v[170:171]\n"
      "v_mov_b64 v[220:221], v[172:173]\n"
      "v_mov_b64 v[222:223], v[174:175]\n"
      "s_nop 1\n"
      "s_branch RET1_0_%=\n"
      "NRET1_0_%=:\n"
      "s_cmp_eq_u32 s64, 1\n"
      "s_cbranch_scc0 NRET1_1_%=\n"
      "v_mov_b64 v[208:209], v[160:161]\n"
      "v_mov_b64 v[210:211], v[162:163]\n"
      "v_mov_b64 v[212:213], v[164:165]\n"
      "v_mov_b64 v[214:215], v[166:167]\n"
      "v_mov_b64 v[216:217], v[168:169]\n"
      "v_mov_b64 v[218:219], v[170:171]\n"
      "v_mov_b64 v[220:221], v[172:173]\n"
      "v_mov_b64 v[222:223], v[174:175]\n"
      "s_nop 1\n"
      "s_branch RET1_1_%=\n"
      "NRET1_1_%=:\n"
      "s_cmp_eq_u32 s64, 2\n"
      "s_cbranch_scc0 NRET1_2_%=\n"
      "v_mov_b64 v[208:209], v[160:161]\n"
      "v_mov_b64 v[210:211], v[162:163]\n"
      "v_mov_b64 v[212:213], v[164:165]\n"
      "v_mov_b64 v[214:215], v[166:167]\n"
      "v_mov_b64 v[216:217], v[168:169]\n"
      "v_mov_b64 v[218:219], v[170:171]\n"
      "v_mov_b64 v[220:221], v[172:173]\n"
      "v_mov_b64 v[222:223], v[174:175]\n"
      "s_nop 1\n"
      "s_branch RET1_2_%=\n"
      "NRET1_2_%=:\n"
      "s_cmp_eq_u32 s64, 3\n"
      "s_cbranch_scc0 NRET1_3_%=\n"
      "v_mov_b64 v[208:209], v[160:161]\n"
      "v_mov_b64 v[210:211], v[162:163]\n"
      "v_mov_b64 v[212:213], v[164:165]\n"
      "v_mov_b64 v[214:215], v[166:167]\n"
      "v_mov_b64 v[216:217], v[168:169]\n"
      "v_mov_b64 v[218:219], v[170:171]\n"
      "v_mov_b64 v[220:221], v[172:173]\n"
      "v_mov_b64 v[222:223], v[174:175]\n"
      "s_nop 1\n"
      "s_branch RET1_3_%=\n"
      "NRET1_3_%=:\n"
      "s_cmp_eq_u32 s64, 4\n"
      "s_cbranch_scc0 NRET1_4_%=\n"
      "v_mov_b64 v[208:209], v[160:161]\n"
      "v_mov_b64 v[210:211], v[162:163]\n"
      "v_mov_b64 v[212:213], v[164:165]\n"
      "v_mov_b64 v[214:215], v[166:167]\n"
      "v_mov_b64 v[216:217], v[168:169]\n"
      "v_mov_b64 v[218:219], v[170:171]\n"
      "v_mov_b64 v[220:221], v[172:173]\n"
      "v_mov_b64 v[222:223], v[174:175]\n"
      "s_nop 1\n"
      "s_branch RET1_4_%=\n"
      "NRET1_4_%=:\n"
      "s_cmp_eq_u32 s64, 5\n"
      "s_cbranch_scc0 NRET1_5_%=\n"
      "v_mov_b64 v[0:1], v[160:161]\n"
      "v_mov_b64 v[2:3], v[162:163]\n"
      "v_mov_b64 v[4:5], v[164:165]\n"
      "v_mov_b64 v[6:7], v[166:167]\n"
      "v_mov_b64 v[8:9], v[168:169]\n"
      "v_mov_b64 v[10:11], v[170:171]\n"
      "v_mov_b64 v[12:13], v[172:173]\n"
      "v_mov_b64 v[14:15], v[174:175]\n"
      "s_nop 1\n"
      "s_branch RET1_5_%=\n"
      "NRET1_5_%=:\n"
      "s_cmp_eq_u32 s64, 6\n"
      "s_cbranch_scc0 NRET1_6_%=\n"
      "v_mov_b64 v[96:97], v[160:161]\n"
      "v_mov_b64 v[98:99], v[162:163]\n"
      "v_mov_b64 v[100:101], v[164:165]\n"
      "v_mov_b64 v[102:103], v[166:167]\n"
      "v_mov_b64 v[104:105], v[168:169]\n"
      "v_mov_b64 v[106:107], v[170:171]\n"
      "v_mov_b64 v[108:109], v[172:173]\n"
      "v_mov_b64 v[110:111], v[174:175]\n"
      "v_mov_b32 v117, v224\n"
      "s_nop 1\n"
      "v_mov_b32_dpp v117, v228 quad_perm:[0,1,2,3] row_mask:0x2 bank_mask:0xf\n"
      "s_nop 1\n"
      "v_mov_b32_dpp v117, v232 quad_perm:[0,1,2,3] row_mask:0x4 bank_mask:0xf\n"
      "s_nop 1\n"
      "v_mov_b32_dpp v117, v236 quad_perm:[0,1,2,3] row_mask:0x8 bank_mask:0xf\n"
      "v_mfma_f32_16x16x32_bf16 a[228:231], a[196:199], v[96:99], a[192:195]\n"
      "v_mfma_f32_16x16x32_bf16 a[228:231], a[200:203], v[100:103], a[228:231]\n"
      "v_mfma_f32_16x16x32_bf16 a[228:231], a[204:207], v[104:107], a[228:231]\n"
      "ds_write_b128 v125, a[160:163] offset:0\n"
      "ds_write_b128 v125, a[164:167] offset:1024\n"
      "ds_write_b128 v125, a[168:171] offset:2048\n"
      "ds_write_b128 v125, a[172:175] offset:3072\n"
      "ds_write_b128 v125, a[176:179] offset:4096\n"
      "ds_write_b128 v125, a[180:183] offset:5120\n"
      "ds_write_b128 v125, a[184:187] offset:6144\n"
      "ds_write_b128 v125, a[188:191] offset:7168\n"
      "ds_write_b128 v125, v[96:99] offset:8192\n"
      "ds_write_b128 v125, v[100:103] offset:9216\n"
      "ds_write_b128 v125, v[104:107] offset:10240\n"
      "ds_write_b32 v125, v117 offset:12292\n"
      "s_nop 1\n"
      "s_branch RET1_6_%=\n"
      "NRET1_6_%=:\n"
      "SCALE_%=:\n"
      "v_pk_mul_f32 v[32:33], v[32:33], v[112:113] op_sel:[0,0] op_sel_hi:[1,0]\n"
      "v_pk_mul_f32 v[34:35], v[34:35], v[112:113] op_sel:[0,0] op_sel_hi:[1,0]\n"
      "v_pk_mul_f32 v[36:37], v[36:37], v[112:113] op_sel:[0,1] op_sel_hi:[1,1]\n"
      "v_pk_mul_f32 v[38:39], v[38:39], v[112:113] op_sel:[0,1] op_sel_hi:[1,1]\n"
      "v_pk_mul_f32 v[40:41], v[40:41], v[114:115] op_sel:[0,0] op_sel_hi:[1,0]\n"
      "v_pk_mul_f32 v[42:43], v[42:43], v[114:115] op_sel:[0,0] op_sel_hi:[1,0]\n"
      "v_pk_mul_f32 v[44:45], v[44:45], v[114:115] op_sel:[0,1] op_sel_hi:[1,1]\n"
      "v_pk_mul_f32 v[46:47], v[46:47], v[114:115] op_sel:[0,1] op_sel_hi:[1,1]\n"
      "v_pk_mul_f32 v[48:49], v[48:49], v[112:113] op_sel:[0,0] op_sel_hi:[1,0]\n"
      "v_pk_mul_f32 v[50:51], v[50:51], v[112:113] op_sel:[0,0] op_sel_hi:[1,0]\n"
      "v_pk_mul_f32 v[52:53], v[52:53], v[112:113] op_sel:[0,1] op_sel_hi:[1,1]\n"
      "v_pk_mul_f32 v[54:55], v[54:55], v[112:113] op_sel:[0,1] op_sel_hi:[1,1]\n"
      "v_pk_mul_f32 v[56:57], v[56:57], v[114:115] op_sel:[0,0] op_sel_hi:[1,0]\n"
      "v_pk_mul_f32 v[58:59], v[58:59], v[114:115] op_sel:[0,0] op_sel_hi:[1,0]\n"
      "v_pk_mul_f32 v[60:61], v[60:61], v[114:115] op_sel:[0,1] op_sel_hi:[1,1]\n"
      "v_pk_mul_f32 v[62:63], v[62:63], v[114:115] op_sel:[0,1] op_sel_hi:[1,1]\n"
      "v_pk_mul_f32 v[64:65], v[64:65], v[112:113] op_sel:[0,0] op_sel_hi:[1,0]\n"
      "v_pk_mul_f32 v[66:67], v[66:67], v[112:113] op_sel:[0,0] op_sel_hi:[1,0]\n"
      "v_pk_mul_f32 v[68:69], v[68:69], v[112:113] op_sel:[0,1] op_sel_hi:[1,1]\n"
      "v_pk_mul_f32 v[70:71], v[70:71], v[112:113] op_sel:[0,1] op_sel_hi:[1,1]\n"
      "v_pk_mul_f32 v[72:73], v[72:73], v[114:115] op_sel:[0,0] op_sel_hi:[1,0]\n"
      "v_pk_mul_f32 v[74:75], v[74:75], v[114:115] op_sel:[0,0] op_sel_hi:[1,0]\n"
      "v_pk_mul_f32 v[76:77], v[76:77], v[114:115] op_sel:[0,1] op_sel_hi:[1,1]\n"
      "v_pk_mul_f32 v[78:79], v[78:79], v[114:115] op_sel:[0,1] op_sel_hi:[1,1]\n"
      "v_pk_mul_f32 v[80:81], v[80:81], v[112:113] op_sel:[0,0] op_sel_hi:[1,0]\n"
      "v_pk_mul_f32 v[82:83], v[82:83], v[112:113] op_sel:[0,0] op_sel_hi:[1,0]\n"
      "v_pk_mul_f32 v[84:85], v[84:85], v[112:113] op_sel:[0,1] op_sel_hi:[1,1]\n"
      "v_pk_mul_f32 v[86:87], v[86:87], v[112:113] op_sel:[0,1] op_sel_hi:[1,1]\n"
      "v_pk_mul_f32 v[88:89], v[88:89], v[114:115] op_sel:[0,0] op_sel_hi:[1,0]\n"
      "v_pk_mul_f32 v[90:91], v[90:91], v[114:115] op_sel:[0,0] op_sel_hi:[1,0]\n"
      "v_pk_mul_f32 v[92:93], v[92:93], v[114:115] op_sel:[0,1] op_sel_hi:[1,1]\n"
      "v_pk_mul_f32 v[94:95], v[94:95], v[114:115] op_sel:[0,1] op_sel_hi:[1,1]\n"
      "v_accvgpr_read_b32 v118, a128\n"
      "v_accvgpr_read_b32 v119, a129\n"
      "v_pk_mul_f32 v[118:119], v[118:119], v[112:113] op_sel:[0,0] op_sel_hi:[1,0]\n"
      "v_accvgpr_write_b32 a128, v118\n"
      "v_accvgpr_write_b32 a129, v119\n"
      "v_accvgpr_read_b32 v118, a130\n"
      "v_accvgpr_read_b32 v119, a131\n"
      "v_pk_mul_f32 v[118:119], v[118:119], v[112:113] op_sel:[0,0] op_sel_hi:[1,0]\n"
      "v_accvgpr_write_b32 a130, v118\n"
      "v_accvgpr_write_b32 a131, v119\n"
      "v_accvgpr_read_b32 v118, a132\n"
      "v_accvgpr_read_b32 v119, a133\n"
      "v_pk_mul_f32 v[118:119], v[118:119], v[112:113] op_sel:[0,1] op_sel_hi:[1,1]\n"
      "v_accvgpr_write_b32 a132, v118\n"
      "v_accvgpr_write_b32 a133, v119\n"
      "v_accvgpr_read_b32 v118, a134\n"
      "v_accvgpr_read_b32 v119, a135\n"
      "v_pk_mul_f32 v[118:119], v[118:119], v[112:113] op_sel:[0,1] op_sel_hi:[1,1]\n"
      "v_accvgpr_write_b32 a134, v118\n"
      "v_accvgpr_write_b32 a135, v119\n"
      "v_accvgpr_read_b32 v118, a136\n"
      "v_accvgpr_read_b32 v119, a137\n"
      "v_pk_mul_f32 v[118:119], v[118:119], v[114:115] op_sel:[0,0] op_sel_hi:[1,0]\n"
      "v_accvgpr_write_b32 a136, v118\n"
      "v_accvgpr_write_b32 a137, v119\n"
      "v_accvgpr_read_b32 v118, a138\n"
      "v_accvgpr_read_b32 v119, a139\n"
      "v_pk_mul_f32 v[118:119], v[118:119], v[114:115] op_sel:[0,0] op_sel_hi:[1,0]\n"
      "v_accvgpr_write_b32 a138, v118\n"
      "v_accvgpr_write_b32 a139, v119\n"
      "v_accvgpr_read_b32 v118, a140\n"
      "v_accvgpr_read_b32 v119, a141\n"
      "v_pk_mul_f32 v[118:119], v[118:119], v[114:115] op_sel:[0,1] op_sel_hi:[1,1]\n"
      "v_accvgpr_write_b32 a140, v118\n"
      "v_accvgpr_write_b32 a141, v119\n"
      "v_accvgpr_read_b32 v118, a142\n"
      "v_accvgpr_read_b32 v119, a143\n"
      "v_pk_mul_f32 v[118:119], v[118:119], v[114:115] op_sel:[0,1] op_sel_hi:[1,1]\n"
      "v_accvgpr_write_b32 a142, v118\n"
      "v_accvgpr_write_b32 a143, v119\n"
      "v_accvgpr_read_b32 v118, a144\n"
      "v_accvgpr_read_b32 v119, a145\n"
      "v_pk_mul_f32 v[118:119], v[118:119], v[112:113] op_sel:[0,0] op_sel_hi:[1,0]\n"
      "v_accvgpr_write_b32 a144, v118\n"
      "v_accvgpr_write_b32 a145, v119\n"
      "v_accvgpr_read_b32 v118, a146\n"
      "v_accvgpr_read_b32 v119, a147\n"
      "v_pk_mul_f32 v[118:119], v[118:119], v[112:113] op_sel:[0,0] op_sel_hi:[1,0]\n"
      "v_accvgpr_write_b32 a146, v118\n"
      "v_accvgpr_write_b32 a147, v119\n"
      "v_accvgpr_read_b32 v118, a148\n"
      "v_accvgpr_read_b32 v119, a149\n"
      "v_pk_mul_f32 v[118:119], v[118:119], v[112:113] op_sel:[0,1] op_sel_hi:[1,1]\n"
      "v_accvgpr_write_b32 a148, v118\n"
      "v_accvgpr_write_b32 a149, v119\n"
      "v_accvgpr_read_b32 v118, a150\n"
      "v_accvgpr_read_b32 v119, a151\n"
      "v_pk_mul_f32 v[118:119], v[118:119], v[112:113] op_sel:[0,1] op_sel_hi:[1,1]\n"
      "v_accvgpr_write_b32 a150, v118\n"
      "v_accvgpr_write_b32 a151, v119\n"
      "v_accvgpr_read_b32 v118, a152\n"
      "v_accvgpr_read_b32 v119, a153\n"
      "v_pk_mul_f32 v[118:119], v[118:119], v[114:115] op_sel:[0,0] op_sel_hi:[1,0]\n"
      "v_accvgpr_write_b32 a152, v118\n"
      "v_accvgpr_write_b32 a153, v119\n"
      "v_accvgpr_read_b32 v118, a154\n"
      "v_accvgpr_read_b32 v119, a155\n"
      "v_pk_mul_f32 v[118:119], v[118:119], v[114:115] op_sel:[0,0] op_sel_hi:[1,0]\n"
      "v_accvgpr_write_b32 a154, v118\n"
      "v_accvgpr_write_b32 a155, v119\n"
      "v_accvgpr_read_b32 v118, a156\n"
      "v_accvgpr_read_b32 v119, a157\n"
      "v_pk_mul_f32 v[118:119], v[118:119], v[114:115] op_sel:[0,1] op_sel_hi:[1,1]\n"
      "v_accvgpr_write_b32 a156, v118\n"
      "v_accvgpr_write_b32 a157, v119\n"
      "v_accvgpr_read_b32 v118, a158\n"
      "v_accvgpr_read_b32 v119, a159\n"
      "v_pk_mul_f32 v[118:119], v[118:119], v[114:115] op_sel:[0,1] op_sel_hi:[1,1]\n"
      "v_accvgpr_write_b32 a158, v118\n"
      "v_accvgpr_write_b32 a159, v119\n"
      "v_accvgpr_read_b32 v118, a160\n"
      "v_accvgpr_read_b32 v119, a161\n"
      "v_pk_mul_f32 v[118:119], v[118:119], v[112:113] op_sel:[0,0] op_sel_hi:[1,0]\n"
      "v_accvgpr_write_b32 a160, v118\n"
      "v_accvgpr_write_b32 a161, v119\n"
      "v_accvgpr_read_b32 v118, a162\n"
      "v_accvgpr_read_b32 v119, a163\n"
      "v_pk_mul_f32 v[118:119], v[118:119], v[112:113] op_sel:[0,0] op_sel_hi:[1,0]\n"
      "v_accvgpr_write_b32 a162, v118\n"
      "v_accvgpr_write_b32 a163, v119\n"
      "v_accvgpr_read_b32 v118, a164\n"
      "v_accvgpr_read_b32 v119, a165\n"
      "v_pk_mul_f32 v[118:119], v[118:119], v[112:113] op_sel:[0,1] op_sel_hi:[1,1]\n"
      "v_accvgpr_write_b32 a164, v118\n"
      "v_accvgpr_write_b32 a165, v119\n"
      "v_accvgpr_read_b32 v118, a166\n"
      "v_accvgpr_read_b32 v119, a167\n"
      "v_pk_mul_f32 v[118:119], v[118:119], v[112:113] op_sel:[0,1] op_sel_hi:[1,1]\n"
      "v_accvgpr_write_b32 a166, v118\n"
      "v_accvgpr_write_b32 a167, v119\n"
      "v_accvgpr_read_b32 v118, a168\n"
      "v_accvgpr_read_b32 v119, a169\n"
      "v_pk_mul_f32 v[118:119], v[118:119], v[114:115] op_sel:[0,0] op_sel_hi:[1,0]\n"
      "v_accvgpr_write_b32 a168, v118\n"
      "v_accvgpr_write_b32 a169, v119\n"
      "v_accvgpr_read_b32 v118, a170\n"
      "v_accvgpr_read_b32 v119, a171\n"
      "v_pk_mul_f32 v[118:119], v[118:119], v[114:115] op_sel:[0,0] op_sel_hi:[1,0]\n"
      "v_accvgpr_write_b32 a170, v118\n"
      "v_accvgpr_write_b32 a171, v119\n"
      "v_accvgpr_read_b32 v118, a172\n"
      "v_accvgpr_read_b32 v119, a173\n"
      "v_pk_mul_f32 v[118:119], v[118:119], v[114:115] op_sel:[0,1] op_sel_hi:[1,1]\n"
      "v_accvgpr_write_b32 a172, v118\n"
      "v_accvgpr_write_b32 a173, v119\n"
      "v_accvgpr_read_b32 v118, a174\n"
      "v_accvgpr_read_b32 v119, a175\n"
      "v_pk_mul_f32 v[118:119], v[118:119], v[114:115] op_sel:[0,1] op_sel_hi:[1,1]\n"
      "v_accvgpr_write_b32 a174, v118\n"
      "v_accvgpr_write_b32 a175, v119\n"
      "v_accvgpr_read_b32 v118, a176\n"
      "v_accvgpr_read_b32 v119, a177\n"
      "v_pk_mul_f32 v[118:119], v[118:119], v[112:113] op_sel:[0,0] op_sel_hi:[1,0]\n"
      "v_accvgpr_write_b32 a176, v118\n"
      "v_accvgpr_write_b32 a177, v119\n"
      "v_accvgpr_read_b32 v118, a178\n"
      "v_accvgpr_read_b32 v119, a179\n"
      "v_pk_mul_f32 v[118:119], v[118:119], v[112:113] op_sel:[0,0] op_sel_hi:[1,0]\n"
      "v_accvgpr_write_b32 a178, v118\n"
      "v_accvgpr_write_b32 a179, v119\n"
      "v_accvgpr_read_b32 v118, a180\n"
      "v_accvgpr_read_b32 v119, a181\n"
      "v_pk_mul_f32 v[118:119], v[118:119], v[112:113] op_sel:[0,1] op_sel_hi:[1,1]\n"
      "v_accvgpr_write_b32 a180, v118\n"
      "v_accvgpr_write_b32 a181, v119\n"
      "v_accvgpr_read_b32 v118, a182\n"
      "v_accvgpr_read_b32 v119, a183\n"
      "v_pk_mul_f32 v[118:119], v[118:119], v[112:113] op_sel:[0,1] op_sel_hi:[1,1]\n"
      "v_accvgpr_write_b32 a182, v118\n"
      "v_accvgpr_write_b32 a183, v119\n"
      "v_accvgpr_read_b32 v118, a184\n"
      "v_accvgpr_read_b32 v119, a185\n"
      "v_pk_mul_f32 v[118:119], v[118:119], v[114:115] op_sel:[0,0] op_sel_hi:[1,0]\n"
      "v_accvgpr_write_b32 a184, v118\n"
      "v_accvgpr_write_b32 a185, v119\n"
      "v_accvgpr_read_b32 v118, a186\n"
      "v_accvgpr_read_b32 v119, a187\n"
      "v_pk_mul_f32 v[118:119], v[118:119], v[114:115] op_sel:[0,0] op_sel_hi:[1,0]\n"
      "v_accvgpr_write_b32 a186, v118\n"
      "v_accvgpr_write_b32 a187, v119\n"
      "v_accvgpr_read_b32 v118, a188\n"
      "v_accvgpr_read_b32 v119, a189\n"
      "v_pk_mul_f32 v[118:119], v[118:119], v[114:115] op_sel:[0,1] op_sel_hi:[1,1]\n"
      "v_accvgpr_write_b32 a188, v118\n"
      "v_accvgpr_write_b32 a189, v119\n"
      "v_accvgpr_read_b32 v118, a190\n"
      "v_accvgpr_read_b32 v119, a191\n"
      "v_pk_mul_f32 v[118:119], v[118:119], v[114:115] op_sel:[0,1] op_sel_hi:[1,1]\n"
      "v_accvgpr_write_b32 a190, v118\n"
      "v_accvgpr_write_b32 a191, v119\n"
      "v_mov_b32 v118, v112\n"
      "s_nop 1\n"
      "v_mov_b32_dpp v118, v113 quad_perm:[0,1,2,3] row_mask:0x2 bank_mask:0xf\n"
      "s_nop 1\n"
      "v_mov_b32_dpp v118, v114 quad_perm:[0,1,2,3] row_mask:0x4 bank_mask:0xf\n"
      "s_nop 1\n"
      "v_mov_b32_dpp v118, v115 quad_perm:[0,1,2,3] row_mask:0x8 bank_mask:0xf\n"
      "v_accvgpr_read_b32 v119, a192\n"
      "v_mul_f32 v118, v118, v119\n"
      "v_accvgpr_write_b32 a192, v118\n"
      "v_lshlrev_b32 v118, 16, v208\n"
      "v_and_b32 v119, 0xffff0000, v208\n"
      "v_pk_mul_f32 v[118:119], v[118:119], v[112:113] op_sel:[0,0] op_sel_hi:[1,0]\n"
      "v_cvt_pk_bf16_f32 v208, v118, v119\n"
      "v_lshlrev_b32 v118, 16, v209\n"
      "v_and_b32 v119, 0xffff0000, v209\n"
      "v_pk_mul_f32 v[118:119], v[118:119], v[112:113] op_sel:[0,0] op_sel_hi:[1,0]\n"
      "v_cvt_pk_bf16_f32 v209, v118, v119\n"
      "v_lshlrev_b32 v118, 16, v210\n"
      "v_and_b32 v119, 0xffff0000, v210\n"
      "v_pk_mul_f32 v[118:119], v[118:119], v[112:113] op_sel:[0,0] op_sel_hi:[1,0]\n"
      "v_cvt_pk_bf16_f32 v210, v118, v119\n"
      "v_lshlrev_b32 v118, 16, v211\n"
      "v_and_b32 v119, 0xffff0000, v211\n"
      "v_pk_mul_f32 v[118:119], v[118:119], v[112:113] op_sel:[0,0] op_sel_hi:[1,0]\n"
      "v_cvt_pk_bf16_f32 v211, v118, v119\n"
      "v_lshlrev_b32 v118, 16, v212\n"
      "v_and_b32 v119, 0xffff0000, v212\n"
      "v_pk_mul_f32 v[118:119], v[118:119], v[112:113] op_sel:[0,1] op_sel_hi:[1,1]\n"
      "v_cvt_pk_bf16_f32 v212, v118, v119\n"
      "v_lshlrev_b32 v118, 16, v213\n"
      "v_and_b32 v119, 0xffff0000, v213\n"
      "v_pk_mul_f32 v[118:119], v[118:119], v[112:113] op_sel:[0,1] op_sel_hi:[1,1]\n"
      "v_cvt_pk_bf16_f32 v213, v118, v119\n"
      "v_lshlrev_b32 v118, 16, v214\n"
      "v_and_b32 v119, 0xffff0000, v214\n"
      "v_pk_mul_f32 v[118:119], v[118:119], v[112:113] op_sel:[0,1] op_sel_hi:[1,1]\n"
      "v_cvt_pk_bf16_f32 v214, v118, v119\n"
      "v_lshlrev_b32 v118, 16, v215\n"
      "v_and_b32 v119, 0xffff0000, v215\n"
      "v_pk_mul_f32 v[118:119], v[118:119], v[112:113] op_sel:[0,1] op_sel_hi:[1,1]\n"
      "v_cvt_pk_bf16_f32 v215, v118, v119\n"
      "v_lshlrev_b32 v118, 16, v216\n"
      "v_and_b32 v119, 0xffff0000, v216\n"
      "v_pk_mul_f32 v[118:119], v[118:119], v[114:115] op_sel:[0,0] op_sel_hi:[1,0]\n"
      "v_cvt_pk_bf16_f32 v216, v118, v119\n"
      "v_lshlrev_b32 v118, 16, v217\n"
      "v_and_b32 v119, 0xffff0000, v217\n"
      "v_pk_mul_f32 v[118:119], v[118:119], v[114:115] op_sel:[0,0] op_sel_hi:[1,0]\n"
      "v_cvt_pk_bf16_f32 v217, v118, v119\n"
      "v_lshlrev_b32 v118, 16, v218\n"
      "v_and_b32 v119, 0xffff0000, v218\n"
      "v_pk_mul_f32 v[118:119], v[118:119], v[114:115] op_sel:[0,0] op_sel_hi:[1,0]\n"
      "v_cvt_pk_bf16_f32 v218, v118, v119\n"
      "v_lshlrev_b32 v118, 16, v219\n"
      "v_and_b32 v119, 0xffff0000, v219\n"
      "v_pk_mul_f32 v[118:119], v[118:119], v[114:115] op_sel:[0,0] op_sel_hi:[1,0]\n"
      "v_cvt_pk_bf16_f32 v219, v118, v119\n"
      "v_lshlrev_b32 v118, 16, v220\n"
      "v_and_b32 v119, 0xffff0000, v220\n"
      "v_pk_mul_f32 v[118:119], v[118:119], v[114:115] op_sel:[0,1] op_sel_hi:[1,1]\n"
      "v_cvt_pk_bf16_f32 v220, v118, v119\n"
      "v_lshlrev_b32 v118, 16, v221\n"
      "v_and_b32 v119, 0xffff0000, v221\n"
      "v_pk_mul_f32 v[118:119], v[118:119], v[114:115] op_sel:[0,1] op_sel_hi:[1,1]\n"
      "v_cvt_pk_bf16_f32 v221, v118, v119\n"
      "v_lshlrev_b32 v118, 16, v222\n"
      "v_and_b32 v119, 0xffff0000, v222\n"
      "v_pk_mul_f32 v[118:119], v[118:119], v[114:115] op_sel:[0,1] op_sel_hi:[1,1]\n"
      "v_cvt_pk_bf16_f32 v222, v118, v119\n"
      "v_lshlrev_b32 v118, 16, v223\n"
      "v_and_b32 v119, 0xffff0000, v223\n"
      "v_pk_mul_f32 v[118:119], v[118:119], v[114:115] op_sel:[0,1] op_sel_hi:[1,1]\n"
      "v_cvt_pk_bf16_f32 v223, v118, v119\n"
      "v_lshlrev_b32 v118, 16, v192\n"
      "v_and_b32 v119, 0xffff0000, v192\n"
      "v_pk_mul_f32 v[118:119], v[118:119], v[112:113] op_sel:[0,0] op_sel_hi:[1,0]\n"
      "v_cvt_pk_bf16_f32 v192, v118, v119\n"
      "v_lshlrev_b32 v118, 16, v193\n"
      "v_and_b32 v119, 0xffff0000, v193\n"
      "v_pk_mul_f32 v[118:119], v[118:119], v[112:113] op_sel:[0,0] op_sel_hi:[1,0]\n"
      "v_cvt_pk_bf16_f32 v193, v118, v119\n"
      "v_lshlrev_b32 v118, 16, v194\n"
      "v_and_b32 v119, 0xffff0000, v194\n"
      "v_pk_mul_f32 v[118:119], v[118:119], v[112:113] op_sel:[0,0] op_sel_hi:[1,0]\n"
      "v_cvt_pk_bf16_f32 v194, v118, v119\n"
      "v_lshlrev_b32 v118, 16, v195\n"
      "v_and_b32 v119, 0xffff0000, v195\n"
      "v_pk_mul_f32 v[118:119], v[118:119], v[112:113] op_sel:[0,0] op_sel_hi:[1,0]\n"
      "v_cvt_pk_bf16_f32 v195, v118, v119\n"
      "v_lshlrev_b32 v118, 16, v196\n"
      "v_and_b32 v119, 0xffff0000, v196\n"
      "v_pk_mul_f32 v[118:119], v[118:119], v[112:113] op_sel:[0,1] op_sel_hi:[1,1]\n"
      "v_cvt_pk_bf16_f32 v196, v118, v119\n"
      "v_lshlrev_b32 v118, 16, v197\n"
      "v_and_b32 v119, 0xffff0000, v197\n"
      "v_pk_mul_f32 v[118:119], v[118:119], v[112:113] op_sel:[0,1] op_sel_hi:[1,1]\n"
      "v_cvt_pk_bf16_f32 v197, v118, v119\n"
      "v_lshlrev_b32 v118, 16, v198\n"
      "v_and_b32 v119, 0xffff0000, v198\n"
      "v_pk_mul_f32 v[118:119], v[118:119], v[112:113] op_sel:[0,1] op_sel_hi:[1,1]\n"
      "v_cvt_pk_bf16_f32 v198, v118, v119\n"
      "v_lshlrev_b32 v118, 16, v199\n"
      "v_and_b32 v119, 0xffff0000, v199\n"
      "v_pk_mul_f32 v[118:119], v[118:119], v[112:113] op_sel:[0,1] op_sel_hi:[1,1]\n"
      "v_cvt_pk_bf16_f32 v199, v118, v119\n"
      "v_lshlrev_b32 v118, 16, v200\n"
      "v_and_b32 v119, 0xffff0000, v200\n"
      "v_pk_mul_f32 v[118:119], v[118:119], v[114:115] op_sel:[0,0] op_sel_hi:[1,0]\n"
      "v_cvt_pk_bf16_f32 v200, v118, v119\n"
      "v_lshlrev_b32 v118, 16, v201\n"
      "v_and_b32 v119, 0xffff0000, v201\n"
      "v_pk_mul_f32 v[118:119], v[118:119], v[114:115] op_sel:[0,0] op_sel_hi:[1,0]\n"
      "v_cvt_pk_bf16_f32 v201, v118, v119\n"
      "v_lshlrev_b32 v118, 16, v202\n"
      "v_and_b32 v119, 0xffff0000, v202\n"
      "v_pk_mul_f32 v[118:119], v[118:119], v[114:115] op_sel:[0,0] op_sel_hi:[1,0]\n"
      "v_cvt_pk_bf16_f32 v202, v118, v119\n"
      "v_lshlrev_b32 v118, 16, v203\n"
      "v_and_b32 v119, 0xffff0000, v203\n"
      "v_pk_mul_f32 v[118:119], v[118:119], v[114:115] op_sel:[0,0] op_sel_hi:[1,0]\n"
      "v_cvt_pk_bf16_f32 v203, v118, v119\n"
      "v_lshlrev_b32 v118, 16, v204\n"
      "v_and_b32 v119, 0xffff0000, v204\n"
      "v_pk_mul_f32 v[118:119], v[118:119], v[114:115] op_sel:[0,1] op_sel_hi:[1,1]\n"
      "v_cvt_pk_bf16_f32 v204, v118, v119\n"
      "v_lshlrev_b32 v118, 16, v205\n"
      "v_and_b32 v119, 0xffff0000, v205\n"
      "v_pk_mul_f32 v[118:119], v[118:119], v[114:115] op_sel:[0,1] op_sel_hi:[1,1]\n"
      "v_cvt_pk_bf16_f32 v205, v118, v119\n"
      "v_lshlrev_b32 v118, 16, v206\n"
      "v_and_b32 v119, 0xffff0000, v206\n"
      "v_pk_mul_f32 v[118:119], v[118:119], v[114:115] op_sel:[0,1] op_sel_hi:[1,1]\n"
      "v_cvt_pk_bf16_f32 v206, v118, v119\n"
      "v_lshlrev_b32 v118, 16, v207\n"
      "v_and_b32 v119, 0xffff0000, v207\n"
      "v_pk_mul_f32 v[118:119], v[118:119], v[114:115] op_sel:[0,1] op_sel_hi:[1,1]\n"
      "v_cvt_pk_bf16_f32 v207, v118, v119\n"
      "v_lshlrev_b32 v118, 16, v0\n"
      "v_and_b32 v119, 0xffff0000, v0\n"
      "v_pk_mul_f32 v[118:119], v[118:119], v[112:113] op_sel:[0,0] op_sel_hi:[1,0]\n"
      "v_cvt_pk_bf16_f32 v0, v118, v119\n"
      "v_lshlrev_b32 v118, 16, v1\n"
      "v_and_b32 v119, 0xffff0000, v1\n"
      "v_pk_mul_f32 v[118:119], v[118:119], v[112:113] op_sel:[0,0] op_sel_hi:[1,0]\n"
      "v_cvt_pk_bf16_f32 v1, v118, v119\n"
      "v_lshlrev_b32 v118, 16, v2\n"
      "v_and_b32 v119, 0xffff0000, v2\n"
      "v_pk_mul_f32 v[118:119], v[118:119], v[112:113] op_sel:[0,0] op_sel_hi:[1,0]\n"
      "v_cvt_pk_bf16_f32 v2, v118, v119\n"
      "v_lshlrev_b32 v118, 16, v3\n"
      "v_and_b32 v119, 0xffff0000, v3\n"
      "v_pk_mul_f32 v[118:119], v[118:119], v[112:113] op_sel:[0,0] op_sel_hi:[1,0]\n"
      "v_cvt_pk_bf16_f32 v3, v118, v119\n"
      "v_lshlrev_b32 v118, 16, v4\n"
      "v_and_b32 v119, 0xffff0000, v4\n"
      "v_pk_mul_f32 v[118:119], v[118:119], v[112:113] op_sel:[0,1] op_sel_hi:[1,1]\n"
      "v_cvt_pk_bf16_f32 v4, v118, v119\n"
      "v_lshlrev_b32 v118, 16, v5\n"
      "v_and_b32 v119, 0xffff0000, v5\n"
      "v_pk_mul_f32 v[118:119], v[118:119], v[112:113] op_sel:[0,1] op_sel_hi:[1,1]\n"
      "v_cvt_pk_bf16_f32 v5, v118, v119\n"
      "v_lshlrev_b32 v118, 16, v6\n"
      "v_and_b32 v119, 0xffff0000, v6\n"
      "v_pk_mul_f32 v[118:119], v[118:119], v[112:113] op_sel:[0,1] op_sel_hi:[1,1]\n"
      "v_cvt_pk_bf16_f32 v6, v118, v119\n"
      "v_lshlrev_b32 v118, 16, v7\n"
      "v_and_b32 v119, 0xffff0000, v7\n"
      "v_pk_mul_f32 v[118:119], v[118:119], v[112:113] op_sel:[0,1] op_sel_hi:[1,1]\n"
      "v_cvt_pk_bf16_f32 v7, v118, v119\n"
      "v_lshlrev_b32 v118, 16, v8\n"
      "v_and_b32 v119, 0xffff0000, v8\n"
      "v_pk_mul_f32 v[118:119], v[118:119], v[114:115] op_sel:[0,0] op_sel_hi:[1,0]\n"
      "v_cvt_pk_bf16_f32 v8, v118, v119\n"
      "v_lshlrev_b32 v118, 16, v9\n"
      "v_and_b32 v119, 0xffff0000, v9\n"
      "v_pk_mul_f32 v[118:119], v[118:119], v[114:115] op_sel:[0,0] op_sel_hi:[1,0]\n"
      "v_cvt_pk_bf16_f32 v9, v118, v119\n"
      "v_lshlrev_b32 v118, 16, v10\n"
      "v_and_b32 v119, 0xffff0000, v10\n"
      "v_pk_mul_f32 v[118:119], v[118:119], v[114:115] op_sel:[0,0] op_sel_hi:[1,0]\n"
      "v_cvt_pk_bf16_f32 v10, v118, v119\n"
      "v_lshlrev_b32 v118, 16, v11\n"
      "v_and_b32 v119, 0xffff0000, v11\n"
      "v_pk_mul_f32 v[118:119], v[118:119], v[114:115] op_sel:[0,0] op_sel_hi:[1,0]\n"
      "v_cvt_pk_bf16_f32 v11, v118, v119\n"
      "v_lshlrev_b32 v118, 16, v12\n"
      "v_and_b32 v119, 0xffff0000, v12\n"
      "v_pk_mul_f32 v[118:119], v[118:119], v[114:115] op_sel:[0,1] op_sel_hi:[1,1]\n"
      "v_cvt_pk_bf16_f32 v12, v118, v119\n"
      "v_lshlrev_b32 v118, 16, v13\n"
      "v_and_b32 v119, 0xffff0000, v13\n"
      "v_pk_mul_f32 v[118:119], v[118:119], v[114:115] op_sel:[0,1] op_sel_hi:[1,1]\n"
      "v_cvt_pk_bf16_f32 v13, v118, v119\n"
      "v_lshlrev_b32 v118, 16, v14\n"
      "v_and_b32 v119, 0xffff0000, v14\n"
      "v_pk_mul_f32 v[118:119], v[118:119], v[114:115] op_sel:[0,1] op_sel_hi:[1,1]\n"
      "v_cvt_pk_bf16_f32 v14, v118, v119\n"
      "v_lshlrev_b32 v118, 16, v15\n"
      "v_and_b32 v119, 0xffff0000, v15\n"
      "v_pk_mul_f32 v[118:119], v[118:119], v[114:115] op_sel:[0,1] op_sel_hi:[1,1]\n"
      "v_cvt_pk_bf16_f32 v15, v118, v119\n"
      "v_lshlrev_b32 v118, 16, v16\n"
      "v_and_b32 v119, 0xffff0000, v16\n"
      "v_pk_mul_f32 v[118:119], v[118:119], v[112:113] op_sel:[0,0] op_sel_hi:[1,0]\n"
      "v_cvt_pk_bf16_f32 v16, v118, v119\n"
      "v_lshlrev_b32 v118, 16, v17\n"
      "v_and_b32 v119, 0xffff0000, v17\n"
      "v_pk_mul_f32 v[118:119], v[118:119], v[112:113] op_sel:[0,0] op_sel_hi:[1,0]\n"
      "v_cvt_pk_bf16_f32 v17, v118, v119\n"
      "v_lshlrev_b32 v118, 16, v18\n"
      "v_and_b32 v119, 0xffff0000, v18\n"
      "v_pk_mul_f32 v[118:119], v[118:119], v[112:113] op_sel:[0,0] op_sel_hi:[1,0]\n"
      "v_cvt_pk_bf16_f32 v18, v118, v119\n"
      "v_lshlrev_b32 v118, 16, v19\n"
      "v_and_b32 v119, 0xffff0000, v19\n"
      "v_pk_mul_f32 v[118:119], v[118:119], v[112:113] op_sel:[0,0] op_sel_hi:[1,0]\n"
      "v_cvt_pk_bf16_f32 v19, v118, v119\n"
      "v_lshlrev_b32 v118, 16, v20\n"
      "v_and_b32 v119, 0xffff0000, v20\n"
      "v_pk_mul_f32 v[118:119], v[118:119], v[112:113] op_sel:[0,1] op_sel_hi:[1,1]\n"
      "v_cvt_pk_bf16_f32 v20, v118, v119\n"
      "v_lshlrev_b32 v118, 16, v21\n"
      "v_and_b32 v119, 0xffff0000, v21\n"
      "v_pk_mul_f32 v[118:119], v[118:119], v[112:113] op_sel:[0,1] op_sel_hi:[1,1]\n"
      "v_cvt_pk_bf16_f32 v21, v118, v119\n"
      "v_lshlrev_b32 v118, 16, v22\n"
      "v_and_b32 v119, 0xffff0000, v22\n"
      "v_pk_mul_f32 v[118:119], v[118:119], v[112:113] op_sel:[0,1] op_sel_hi:[1,1]\n"
      "v_cvt_pk_bf16_f32 v22, v118, v119\n"
      "v_lshlrev_b32 v118, 16, v23\n"
      "v_and_b32 v119, 0xffff0000, v23\n"
      "v_pk_mul_f32 v[118:119], v[118:119], v[112:113] op_sel:[0,1] op_sel_hi:[1,1]\n"
      "v_cvt_pk_bf16_f32 v23, v118, v119\n"
      "v_lshlrev_b32 v118, 16, v24\n"
      "v_and_b32 v119, 0xffff0000, v24\n"
      "v_pk_mul_f32 v[118:119], v[118:119], v[114:115] op_sel:[0,0] op_sel_hi:[1,0]\n"
      "v_cvt_pk_bf16_f32 v24, v118, v119\n"
      "v_lshlrev_b32 v118, 16, v25\n"
      "v_and_b32 v119, 0xffff0000, v25\n"
      "v_pk_mul_f32 v[118:119], v[118:119], v[114:115] op_sel:[0,0] op_sel_hi:[1,0]\n"
      "v_cvt_pk_bf16_f32 v25, v118, v119\n"
      "v_lshlrev_b32 v118, 16, v26\n"
      "v_and_b32 v119, 0xffff0000, v26\n"
      "v_pk_mul_f32 v[118:119], v[118:119], v[114:115] op_sel:[0,0] op_sel_hi:[1,0]\n"
      "v_cvt_pk_bf16_f32 v26, v118, v119\n"
      "v_lshlrev_b32 v118, 16, v27\n"
      "v_and_b32 v119, 0xffff0000, v27\n"
      "v_pk_mul_f32 v[118:119], v[118:119], v[114:115] op_sel:[0,0] op_sel_hi:[1,0]\n"
      "v_cvt_pk_bf16_f32 v27, v118, v119\n"
      "v_lshlrev_b32 v118, 16, v28\n"
      "v_and_b32 v119, 0xffff0000, v28\n"
      "v_pk_mul_f32 v[118:119], v[118:119], v[114:115] op_sel:[0,1] op_sel_hi:[1,1]\n"
      "v_cvt_pk_bf16_f32 v28, v118, v119\n"
      "v_lshlrev_b32 v118, 16, v29\n"
      "v_and_b32 v119, 0xffff0000, v29\n"
      "v_pk_mul_f32 v[118:119], v[118:119], v[114:115] op_sel:[0,1] op_sel_hi:[1,1]\n"
      "v_cvt_pk_bf16_f32 v29, v118, v119\n"
      "v_lshlrev_b32 v118, 16, v30\n"
      "v_and_b32 v119, 0xffff0000, v30\n"
      "v_pk_mul_f32 v[118:119], v[118:119], v[114:115] op_sel:[0,1] op_sel_hi:[1,1]\n"
      "v_cvt_pk_bf16_f32 v30, v118, v119\n"
      "v_lshlrev_b32 v118, 16, v31\n"
      "v_and_b32 v119, 0xffff0000, v31\n"
      "v_pk_mul_f32 v[118:119], v[118:119], v[114:115] op_sel:[0,1] op_sel_hi:[1,1]\n"
      "v_cvt_pk_bf16_f32 v31, v118, v119\n"
      "v_lshlrev_b32 v118, 16, v96\n"
      "v_and_b32 v119, 0xffff0000, v96\n"
      "v_pk_mul_f32 v[118:119], v[118:119], v[112:113] op_sel:[0,0] op_sel_hi:[1,0]\n"
      "v_cvt_pk_bf16_f32 v96, v118, v119\n"
      "v_lshlrev_b32 v118, 16, v97\n"
      "v_and_b32 v119, 0xffff0000, v97\n"
      "v_pk_mul_f32 v[118:119], v[118:119], v[112:113] op_sel:[0,0] op_sel_hi:[1,0]\n"
      "v_cvt_pk_bf16_f32 v97, v118, v119\n"
      "v_lshlrev_b32 v118, 16, v98\n"
      "v_and_b32 v119, 0xffff0000, v98\n"
      "v_pk_mul_f32 v[118:119], v[118:119], v[112:113] op_sel:[0,0] op_sel_hi:[1,0]\n"
      "v_cvt_pk_bf16_f32 v98, v118, v119\n"
      "v_lshlrev_b32 v118, 16, v99\n"
      "v_and_b32 v119, 0xffff0000, v99\n"
      "v_pk_mul_f32 v[118:119], v[118:119], v[112:113] op_sel:[0,0] op_sel_hi:[1,0]\n"
      "v_cvt_pk_bf16_f32 v99, v118, v119\n"
      "v_lshlrev_b32 v118, 16, v100\n"
      "v_and_b32 v119, 0xffff0000, v100\n"
      "v_pk_mul_f32 v[118:119], v[118:119], v[112:113] op_sel:[0,1] op_sel_hi:[1,1]\n"
      "v_cvt_pk_bf16_f32 v100, v118, v119\n"
      "v_lshlrev_b32 v118, 16, v101\n"
      "v_and_b32 v119, 0xffff0000, v101\n"
      "v_pk_mul_f32 v[118:119], v[118:119], v[112:113] op_sel:[0,1] op_sel_hi:[1,1]\n"
      "v_cvt_pk_bf16_f32 v101, v118, v119\n"
      "v_lshlrev_b32 v118, 16, v102\n"
      "v_and_b32 v119, 0xffff0000, v102\n"
      "v_pk_mul_f32 v[118:119], v[118:119], v[112:113] op_sel:[0,1] op_sel_hi:[1,1]\n"
      "v_cvt_pk_bf16_f32 v102, v118, v119\n"
      "v_lshlrev_b32 v118, 16, v103\n"
      "v_and_b32 v119, 0xffff0000, v103\n"
      "v_pk_mul_f32 v[118:119], v[118:119], v[112:113] op_sel:[0,1] op_sel_hi:[1,1]\n"
      "v_cvt_pk_bf16_f32 v103, v118, v119\n"
      "v_lshlrev_b32 v118, 16, v104\n"
      "v_and_b32 v119, 0xffff0000, v104\n"
      "v_pk_mul_f32 v[118:119], v[118:119], v[114:115] op_sel:[0,0] op_sel_hi:[1,0]\n"
      "v_cvt_pk_bf16_f32 v104, v118, v119\n"
      "v_lshlrev_b32 v118, 16, v105\n"
      "v_and_b32 v119, 0xffff0000, v105\n"
      "v_pk_mul_f32 v[118:119], v[118:119], v[114:115] op_sel:[0,0] op_sel_hi:[1,0]\n"
      "v_cvt_pk_bf16_f32 v105, v118, v119\n"
      "v_lshlrev_b32 v118, 16, v106\n"
      "v_and_b32 v119, 0xffff0000, v106\n"
      "v_pk_mul_f32 v[118:119], v[118:119], v[114:115] op_sel:[0,0] op_sel_hi:[1,0]\n"
      "v_cvt_pk_bf16_f32 v106, v118, v119\n"
      "v_lshlrev_b32 v118, 16, v107\n"
      "v_and_b32 v119, 0xffff0000, v107\n"
      "v_pk_mul_f32 v[118:119], v[118:119], v[114:115] op_sel:[0,0] op_sel_hi:[1,0]\n"
      "v_cvt_pk_bf16_f32 v107, v118, v119\n"
      "v_lshlrev_b32 v118, 16, v108\n"
      "v_and_b32 v119, 0xffff0000, v108\n"
      "v_pk_mul_f32 v[118:119], v[118:119], v[114:115] op_sel:[0,1] op_sel_hi:[1,1]\n"
      "v_cvt_pk_bf16_f32 v108, v118, v119\n"
      "v_lshlrev_b32 v118, 16, v109\n"
      "v_and_b32 v119, 0xffff0000, v109\n"
      "v_pk_mul_f32 v[118:119], v[118:119], v[114:115] op_sel:[0,1] op_sel_hi:[1,1]\n"
      "v_cvt_pk_bf16_f32 v109, v118, v119\n"
      "v_lshlrev_b32 v118, 16, v110\n"
      "v_and_b32 v119, 0xffff0000, v110\n"
      "v_pk_mul_f32 v[118:119], v[118:119], v[114:115] op_sel:[0,1] op_sel_hi:[1,1]\n"
      "v_cvt_pk_bf16_f32 v110, v118, v119\n"
      "v_lshlrev_b32 v118, 16, v111\n"
      "v_and_b32 v119, 0xffff0000, v111\n"
      "v_pk_mul_f32 v[118:119], v[118:119], v[114:115] op_sel:[0,1] op_sel_hi:[1,1]\n"
      "v_cvt_pk_bf16_f32 v111, v118, v119\n"
      "s_setpc_b64 s[84:85]\n"
      "MIS_%=:\n"
      "s_waitcnt lgkmcnt(0)\n"
      "v_sub_f32 v148, v153, v117\n"
      "v_min_f32 v149, 0, -v148\n"
      "v_min_f32 v148, 0, v148\n"
      "v_exp_f32 v149, v149\n"
      "v_exp_f32 v148, v148\n"
      "v_min_f32 v117, v117, v153\n"
      "v_mov_b32 v112, v148\n"
      "v_mov_b32 v228, v149\n"
      "v_mov_b32 v114, v112\n"
      "v_mov_b32 v230, v228\n"
      "s_nop 0\n"
      "v_permlane32_swap_b32 v112, v114\n"
      "v_permlane32_swap_b32 v228, v230\n"
      "v_mov_b32 v113, v112\n"
      "v_mov_b32 v229, v228\n"
      "v_mov_b32 v115, v114\n"
      "v_mov_b32 v231, v230\n"
      "v_permlane16_swap_b32 v112, v113\n"
      "v_permlane16_swap_b32 v228, v229\n"
      "v_permlane16_swap_b32 v114, v115\n"
      "v_permlane16_swap_b32 v230, v231\n"
      "s_call_b64 s[84:85], SCALE_%=\n"
      "v_pk_mul_f32 v[160:161], v[160:161], v[228:229] op_sel:[0,0] op_sel_hi:[1,0]\n"
      "v_pk_mul_f32 v[162:163], v[162:163], v[228:229] op_sel:[0,0] op_sel_hi:[1,0]\n"
      "v_pk_mul_f32 v[164:165], v[164:165], v[228:229] op_sel:[0,1] op_sel_hi:[1,1]\n"
      "v_pk_mul_f32 v[166:167], v[166:167], v[228:229] op_sel:[0,1] op_sel_hi:[1,1]\n"
      "v_pk_mul_f32 v[168:169], v[168:169], v[230:231] op_sel:[0,0] op_sel_hi:[1,0]\n"
      "v_pk_mul_f32 v[170:171], v[170:171], v[230:231] op_sel:[0,0] op_sel_hi:[1,0]\n"
      "v_pk_mul_f32 v[172:173], v[172:173], v[230:231] op_sel:[0,1] op_sel_hi:[1,1]\n"
      "v_pk_mul_f32 v[174:175], v[174:175], v[230:231] op_sel:[0,1] op_sel_hi:[1,1]\n"
      "v_pk_mul_f32 v[176:177], v[176:177], v[228:229] op_sel:[0,0] op_sel_hi:[1,0]\n"
      "v_pk_mul_f32 v[178:179], v[178:179], v[228:229] op_sel:[0,0] op_sel_hi:[1,0]\n"
      "v_pk_mul_f32 v[180:181], v[180:181], v[228:229] op_sel:[0,1] op_sel_hi:[1,1]\n"
      "v_pk_mul_f32 v[182:183], v[182:183], v[228:229] op_sel:[0,1] op_sel_hi:[1,1]\n"
      "v_pk_mul_f32 v[184:185], v[184:185], v[230:231] op_sel:[0,0] op_sel_hi:[1,0]\n"
      "v_pk_mul_f32 v[186:187], v[186:187], v[230:231] op_sel:[0,0] op_sel_hi:[1,0]\n"
      "v_pk_mul_f32 v[188:189], v[188:189], v[230:231] op_sel:[0,1] op_sel_hi:[1,1]\n"
      "v_pk_mul_f32 v[190:191], v[190:191], v[230:231] op_sel:[0,1] op_sel_hi:[1,1]\n"
      "v_mov_b32 v112, v228\n"
      "v_mov_b32 v113, v229\n"
      "v_mov_b32 v114, v230\n"
      "v_mov_b32 v115, v231\n"
      "v_accvgpr_read_b32 v128, a64\n"
      "v_accvgpr_read_b32 v129, a65\n"
      "v_accvgpr_read_b32 v130, a66\n"
      "v_accvgpr_read_b32 v131, a67\n"
      "v_accvgpr_read_b32 v132, a68\n"
      "v_accvgpr_read_b32 v133, a69\n"
      "v_accvgpr_read_b32 v134, a70\n"
      "v_accvgpr_read_b32 v135, a71\n"
      "v_accvgpr_read_b32 v136, a72\n"
      "v_accvgpr_read_b32 v137, a73\n"
      "v_accvgpr_read_b32 v138, a74\n"
      "v_accvgpr_read_b32 v139, a75\n"
      "v_accvgpr_read_b32 v140, a76\n"
      "v_accvgpr_read_b32 v141, a77\n"
      "v_accvgpr_read_b32 v142, a78\n"
      "v_accvgpr_read_b32 v143, a79\n"
      "v_lshlrev_b32 v118, 16, v128\n"
      "v_and_b32 v119, 0xffff0000, v128\n"
      "v_pk_mul_f32 v[118:119], v[118:119], v[112:113] op_sel:[0,0] op_sel_hi:[1,0]\n"
      "v_cvt_pk_bf16_f32 v128, v118, v119\n"
      "v_lshlrev_b32 v118, 16, v129\n"
      "v_and_b32 v119, 0xffff0000, v129\n"
      "v_pk_mul_f32 v[118:119], v[118:119], v[112:113] op_sel:[0,0] op_sel_hi:[1,0]\n"
      "v_cvt_pk_bf16_f32 v129, v118, v119\n"
      "v_lshlrev_b32 v118, 16, v130\n"
      "v_and_b32 v119, 0xffff0000, v130\n"
      "v_pk_mul_f32 v[118:119], v[118:119], v[112:113] op_sel:[0,0] op_sel_hi:[1,0]\n"
      "v_cvt_pk_bf16_f32 v130, v118, v119\n"
      "v_lshlrev_b32 v118, 16, v131\n"
      "v_and_b32 v119, 0xffff0000, v131\n"
      "v_pk_mul_f32 v[118:119], v[118:119], v[112:113] op_sel:[0,0] op_sel_hi:[1,0]\n"
      "v_cvt_pk_bf16_f32 v131, v118, v119\n"
      "v_lshlrev_b32 v118, 16, v132\n"
      "v_and_b32 v119, 0xffff0000, v132\n"
      "v_pk_mul_f32 v[118:119], v[118:119], v[112:113] op_sel:[0,1] op_sel_hi:[1,1]\n"
      "v_cvt_pk_bf16_f32 v132, v118, v119\n"
      "v_lshlrev_b32 v118, 16, v133\n"
      "v_and_b32 v119, 0xffff0000, v133\n"
      "v_pk_mul_f32 v[118:119], v[118:119], v[112:113] op_sel:[0,1] op_sel_hi:[1,1]\n"
      "v_cvt_pk_bf16_f32 v133, v118, v119\n"
      "v_lshlrev_b32 v118, 16, v134\n"
      "v_and_b32 v119, 0xffff0000, v134\n"
      "v_pk_mul_f32 v[118:119], v[118:119], v[112:113] op_sel:[0,1] op_sel_hi:[1,1]\n"
      "v_cvt_pk_bf16_f32 v134, v118, v119\n"
      "v_lshlrev_b32 v118, 16, v135\n"
      "v_and_b32 v119, 0xffff0000, v135\n"
      "v_pk_mul_f32 v[118:119], v[118:119], v[112:113] op_sel:[0,1] op_sel_hi:[1,1]\n"
      "v_cvt_pk_bf16_f32 v135, v118, v119\n"
      "v_lshlrev_b32 v118, 16, v136\n"
      "v_and_b32 v119, 0xffff0000, v136\n"
      "v_pk_mul_f32 v[118:119], v[118:119], v[114:115] op_sel:[0,0] op_sel_hi:[1,0]\n"
      "v_cvt_pk_bf16_f32 v136, v118, v119\n"
      "v_lshlrev_b32 v118, 16, v137\n"
      "v_and_b32 v119, 0xffff0000, v137\n"
      "v_pk_mul_f32 v[118:119], v[118:119], v[114:115] op_sel:[0,0] op_sel_hi:[1,0]\n"
      "v_cvt_pk_bf16_f32 v137, v118, v119\n"
      "v_lshlrev_b32 v118, 16, v138\n"
      "v_and_b32 v119, 0xffff0000, v138\n"
      "v_pk_mul_f32 v[118:119], v[118:119], v[114:115] op_sel:[0,0] op_sel_hi:[1,0]\n"
      "v_cvt_pk_bf16_f32 v138, v118, v119\n"
      "v_lshlrev_b32 v118, 16, v139\n"
      "v_and_b32 v119, 0xffff0000, v139\n"
      "v_pk_mul_f32 v[118:119], v[118:119], v[114:115] op_sel:[0,0] op_sel_hi:[1,0]\n"
      "v_cvt_pk_bf16_f32 v139, v118, v119\n"
      "v_lshlrev_b32 v118, 16, v140\n"
      "v_and_b32 v119, 0xffff0000, v140\n"
      "v_pk_mul_f32 v[118:119], v[118:119], v[114:115] op_sel:[0,1] op_sel_hi:[1,1]\n"
      "v_cvt_pk_bf16_f32 v140, v118, v119\n"
      "v_lshlrev_b32 v118, 16, v141\n"
      "v_and_b32 v119, 0xffff0000, v141\n"
      "v_pk_mul_f32 v[118:119], v[118:119], v[114:115] op_sel:[0,1] op_sel_hi:[1,1]\n"
      "v_cvt_pk_bf16_f32 v141, v118, v119\n"
      "v_lshlrev_b32 v118, 16, v142\n"
      "v_and_b32 v119, 0xffff0000, v142\n"
      "v_pk_mul_f32 v[118:119], v[118:119], v[114:115] op_sel:[0,1] op_sel_hi:[1,1]\n"
      "v_cvt_pk_bf16_f32 v142, v118, v119\n"
      "v_lshlrev_b32 v118, 16, v143\n"
      "v_and_b32 v119, 0xffff0000, v143\n"
      "v_pk_mul_f32 v[118:119], v[118:119], v[114:115] op_sel:[0,1] op_sel_hi:[1,1]\n"
      "v_cvt_pk_bf16_f32 v143, v118, v119\n"
      "v_accvgpr_write_b32 a64, v128\n"
      "v_accvgpr_write_b32 a65, v129\n"
      "v_accvgpr_write_b32 a66, v130\n"
      "v_accvgpr_write_b32 a67, v131\n"
      "v_accvgpr_write_b32 a68, v132\n"
      "v_accvgpr_write_b32 a69, v133\n"
      "v_accvgpr_write_b32 a70, v134\n"
      "v_accvgpr_write_b32 a71, v135\n"
      "v_accvgpr_write_b32 a72, v136\n"
      "v_accvgpr_write_b32 a73, v137\n"
      "v_accvgpr_write_b32 a74, v138\n"
      "v_accvgpr_write_b32 a75, v139\n"
      "v_accvgpr_write_b32 a76, v140\n"
      "v_accvgpr_write_b32 a77, v141\n"
      "v_accvgpr_write_b32 a78, v142\n"
      "v_accvgpr_write_b32 a79, v143\n"
      "s_branch MR_%=\n"
      "FIX_%=:\n"
      "s_nop 7\n"
      "v_sub_f32 v224, v81, v117\n"
      "v_min_f32 v112, 0, -v224\n"
      "v_min_f32 v224, 0, v224\n"
      "v_exp_f32 v112, v112\n"
      "v_exp_f32 v224, v224\n"
      "s_nop 0\n"
      "v_mul_f32 v144, v144, v224\n"
      "v_mul_f32 v80, v80, v112\n"
      "v_mov_b32 v226, v224\n"
      "v_mov_b32 v114, v112\n"
      "s_nop 0\n"
      "v_permlane32_swap_b32 v224, v226\n"
      "v_permlane32_swap_b32 v112, v114\n"
      "v_mov_b32 v225, v224\n"
      "v_mov_b32 v113, v112\n"
      "v_mov_b32 v227, v226\n"
      "v_mov_b32 v115, v114\n"
      "v_permlane16_swap_b32 v224, v225\n"
      "v_permlane16_swap_b32 v112, v113\n"
      "v_permlane16_swap_b32 v226, v227\n"
      "v_permlane16_swap_b32 v114, v115\n"
      "v_pk_mul_f32 v[32:33], v[32:33], v[224:225] op_sel:[0,0] op_sel_hi:[1,0]\n"
      "v_pk_mul_f32 v[34:35], v[34:35], v[224:225] op_sel:[0,0] op_sel_hi:[1,0]\n"
      "v_pk_mul_f32 v[36:37], v[36:37], v[224:225] op_sel:[0,1] op_sel_hi:[1,1]\n"
      "v_pk_mul_f32 v[38:39], v[38:39], v[224:225] op_sel:[0,1] op_sel_hi:[1,1]\n"
      "v_pk_mul_f32 v[40:41], v[40:41], v[226:227] op_sel:[0,0] op_sel_hi:[1,0]\n"
      "v_pk_mul_f32 v[42:43], v[42:43], v[226:227] op_sel:[0,0] op_sel_hi:[1,0]\n"
      "v_pk_mul_f32 v[44:45], v[44:45], v[226:227] op_sel:[0,1] op_sel_hi:[1,1]\n"
      "v_pk_mul_f32 v[46:47], v[46:47], v[226:227] op_sel:[0,1] op_sel_hi:[1,1]\n"
      "v_pk_mul_f32 v[48:49], v[48:49], v[224:225] op_sel:[0,0] op_sel_hi:[1,0]\n"
      "v_pk_mul_f32 v[50:51], v[50:51], v[224:225] op_sel:[0,0] op_sel_hi:[1,0]\n"
      "v_pk_mul_f32 v[52:53], v[52:53], v[224:225] op_sel:[0,1] op_sel_hi:[1,1]\n"
      "v_pk_mul_f32 v[54:55], v[54:55], v[224:225] op_sel:[0,1] op_sel_hi:[1,1]\n"
      "v_pk_mul_f32 v[56:57], v[56:57], v[226:227] op_sel:[0,0] op_sel_hi:[1,0]\n"
      "v_pk_mul_f32 v[58:59], v[58:59], v[226:227] op_sel:[0,0] op_sel_hi:[1,0]\n"
      "v_pk_mul_f32 v[60:61], v[60:61], v[226:227] op_sel:[0,1] op_sel_hi:[1,1]\n"
      "v_pk_mul_f32 v[62:63], v[62:63], v[226:227] op_sel:[0,1] op_sel_hi:[1,1]\n"
      "v_lshlrev_b32 v118, 16, v64\n"
      "v_and_b32 v119, 0xffff0000, v64\n"
      "v_pk_mul_f32 v[118:119], v[118:119], v[112:113] op_sel:[0,0] op_sel_hi:[1,0]\n"
      "v_cvt_pk_bf16_f32 v64, v118, v119\n"
      "v_lshlrev_b32 v118, 16, v65\n"
      "v_and_b32 v119, 0xffff0000, v65\n"
      "v_pk_mul_f32 v[118:119], v[118:119], v[112:113] op_sel:[0,0] op_sel_hi:[1,0]\n"
      "v_cvt_pk_bf16_f32 v65, v118, v119\n"
      "v_lshlrev_b32 v118, 16, v66\n"
      "v_and_b32 v119, 0xffff0000, v66\n"
      "v_pk_mul_f32 v[118:119], v[118:119], v[112:113] op_sel:[0,0] op_sel_hi:[1,0]\n"
      "v_cvt_pk_bf16_f32 v66, v118, v119\n"
      "v_lshlrev_b32 v118, 16, v67\n"
      "v_and_b32 v119, 0xffff0000, v67\n"
      "v_pk_mul_f32 v[118:119], v[118:119], v[112:113] op_sel:[0,0] op_sel_hi:[1,0]\n"
      "v_cvt_pk_bf16_f32 v67, v118, v119\n"
      "v_lshlrev_b32 v118, 16, v68\n"
      "v_and_b32 v119, 0xffff0000, v68\n"
      "v_pk_mul_f32 v[118:119], v[118:119], v[112:113] op_sel:[0,1] op_sel_hi:[1,1]\n"
      "v_cvt_pk_bf16_f32 v68, v118, v119\n"
      "v_lshlrev_b32 v118, 16, v69\n"
      "v_and_b32 v119, 0xffff0000, v69\n"
      "v_pk_mul_f32 v[118:119], v[118:119], v[112:113] op_sel:[0,1] op_sel_hi:[1,1]\n"
      "v_cvt_pk_bf16_f32 v69, v118, v119\n"
      "v_lshlrev_b32 v118, 16, v70\n"
      "v_and_b32 v119, 0xffff0000, v70\n"
      "v_pk_mul_f32 v[118:119], v[118:119], v[112:113] op_sel:[0,1] op_sel_hi:[1,1]\n"
      "v_cvt_pk_bf16_f32 v70, v118, v119\n"
      "v_lshlrev_b32 v118, 16, v71\n"
      "v_and_b32 v119, 0xffff0000, v71\n"
      "v_pk_mul_f32 v[118:119], v[118:119], v[112:113] op_sel:[0,1] op_sel_hi:[1,1]\n"
      "v_cvt_pk_bf16_f32 v71, v118, v119\n"
      "v_lshlrev_b32 v118, 16, v72\n"
      "v_and_b32 v119, 0xffff0000, v72\n"
      "v_pk_mul_f32 v[118:119], v[118:119], v[114:115] op_sel:[0,0] op_sel_hi:[1,0]\n"
      "v_cvt_pk_bf16_f32 v72, v118, v119\n"
      "v_lshlrev_b32 v118, 16, v73\n"
      "v_and_b32 v119, 0xffff0000, v73\n"
      "v_pk_mul_f32 v[118:119], v[118:119], v[114:115] op_sel:[0,0] op_sel_hi:[1,0]\n"
      "v_cvt_pk_bf16_f32 v73, v118, v119\n"
      "v_lshlrev_b32 v118, 16, v74\n"
      "v_and_b32 v119, 0xffff0000, v74\n"
      "v_pk_mul_f32 v[118:119], v[118:119], v[114:115] op_sel:[0,0] op_sel_hi:[1,0]\n"
      "v_cvt_pk_bf16_f32 v74, v118, v119\n"
      "v_lshlrev_b32 v118, 16, v75\n"
      "v_and_b32 v119, 0xffff0000, v75\n"
      "v_pk_mul_f32 v[118:119], v[118:119], v[114:115] op_sel:[0,0] op_sel_hi:[1,0]\n"
      "v_cvt_pk_bf16_f32 v75, v118, v119\n"
      "v_lshlrev_b32 v118, 16, v76\n"
      "v_and_b32 v119, 0xffff0000, v76\n"
      "v_pk_mul_f32 v[118:119], v[118:119], v[114:115] op_sel:[0,1] op_sel_hi:[1,1]\n"
      "v_cvt_pk_bf16_f32 v76, v118, v119\n"
      "v_lshlrev_b32 v118, 16, v77\n"
      "v_and_b32 v119, 0xffff0000, v77\n"
      "v_pk_mul_f32 v[118:119], v[118:119], v[114:115] op_sel:[0,1] op_sel_hi:[1,1]\n"
      "v_cvt_pk_bf16_f32 v77, v118, v119\n"
      "v_lshlrev_b32 v118, 16, v78\n"
      "v_and_b32 v119, 0xffff0000, v78\n"
      "v_pk_mul_f32 v[118:119], v[118:119], v[114:115] op_sel:[0,1] op_sel_hi:[1,1]\n"
      "v_cvt_pk_bf16_f32 v78, v118, v119\n"
      "v_lshlrev_b32 v118, 16, v79\n"
      "v_and_b32 v119, 0xffff0000, v79\n"
      "v_pk_mul_f32 v[118:119], v[118:119], v[114:115] op_sel:[0,1] op_sel_hi:[1,1]\n"
      "v_cvt_pk_bf16_f32 v79, v118, v119\n"
      "v_add_f32 v224, v144, v80\n"
      "v_rcp_f32 v224, v224\n"
      "v_mfma_f32_16x16x32_bf16 v[160:163], v[86:89], v[64:67], v[32:35]\n"
      "v_mfma_f32_16x16x32_bf16 v[176:179], v[84:87], v[64:67], v[48:51]\n"
      "v_mfma_f32_16x16x32_bf16 v[164:167], v[86:89], v[68:71], v[36:39]\n"
      "v_mfma_f32_16x16x32_bf16 v[180:183], v[84:87], v[68:71], v[52:55]\n"
      "v_mfma_f32_16x16x32_bf16 v[168:171], v[86:89], v[72:75], v[40:43]\n"
      "v_mfma_f32_16x16x32_bf16 v[184:187], v[84:87], v[72:75], v[56:59]\n"
      "v_mfma_f32_16x16x32_bf16 v[172:175], v[86:89], v[76:79], v[44:47]\n"
      "v_mfma_f32_16x16x32_bf16 v[188:191], v[84:87], v[76:79], v[60:63]\n"
      "v_mov_b32 v226, v224\n"
      "s_nop 1\n"
      "s_nop 0\n"
      "s_nop 1\n"
      "v_permlane32_swap_b32 v224, v226\n"
      "s_nop 1\n"
      "v_mov_b32 v225, v224\n"
      "s_nop 1\n"
      "v_mov_b32 v227, v226\n"
      "s_nop 1\n"
      "v_permlane16_swap_b32 v224, v225\n"
      "s_nop 1\n"
      "v_permlane16_swap_b32 v226, v227\n"
      "s_nop 1\n"
      "s_branch FR_%=\n"
      ".subsection 0\n"
      : "+{s[4:19]}"(ka), "+{v128}"(pre), "+{v[160:191]}"(qq), "+{s92}"(tch), "+{v[242:249]}"(ad), "+{v[250:251]}"(ra)
      : [qo] "s"(bk * 128 * D_ * 2), [oo] "s"((bk * 128 + rh * 64) * (D_ * 2) + (2 * split + kh) * 64 + 4096),
        [fo] "s"((unsigned)WS_EPOCH + bk * 128 + 64 + split * 8), [xo] "s"((unsigned)(WS_X + split * WS_X1 + (bk * 4 + wid) * XW_BYTES)),
        [xw] "s"(xarea(wid)), [xr] "s"(xarea(wid ^ 2)),
        [so0] "s"(so0), [inc] "s"(inc), [wb] "s"(wbase), [tv] "s"(wbase + STAGE_BYTES + KIMG + split * 8192), [n1] "s"(n1),
        [qoff] "s"(Q_OFF + rh * 16384 - kh * 8192), [toff] "s"(TREE_OFF - kh * 8192),
        [neg] "s"((wid ^ (3 * split)) ? 0xff800000u : 0u),
        [lead] "s"(blk * 4 + wid * 32), [cph] "s"(C2 + (unsigned)((jj & ~3) * 8 + xg * 4) * WIN / 256u),
        [rep] "s"((jj >> 2) == 160 / 32 ? 128 + (blk & 31) * 4 : (blk - 224) * 4)
      : "v0", "v1", "v2", "v3", "v4", "v5", "v6", "v7", "v8", "v9", "v10", "v11", "v12", "v13", "v14", "v15", "v16", "v17", "v18", "v19", "v20", "v21", "v22", "v23", "v24", "v25", "v26", "v27", "v28", "v29", "v30", "v31", "v32", "v33", "v34", "v35", "v36", "v37", "v38", "v39", "v40", "v41", "v42", "v43", "v44", "v45", "v46", "v47", "v48", "v49", "v50", "v51", "v52", "v53", "v54", "v55", "v56", "v57", "v58", "v59", "v60", "v61", "v62", "v63", "v64", "v65", "v66", "v67", "v68", "v69", "v70", "v71", "v72", "v73", "v74", "v75", "v76", "v77", "v78", "v79", "v80", "v81", "v82", "v83", "v84", "v85", "v86", "v87", "v88", "v89", "v90", "v91", "v92", "v93", "v94", "v95", "v96", "v97", "v98", "v99", "v100", "v101", "v102", "v103", "v104", "v105", "v106", "v107", "v108", "v109", "v110", "v111", "v112", "v113", "v114", "v115", "v116", "v117", "v118", "v119", "v120", "v121", "v122", "v123", "v124", "v125", "v126", "v127", "v129", "v130", "v131", "v132", "v133", "v134", "v135", "v136", "v137", "v138", "v139", "v140", "v141", "v142", "v143", "v144", "v145", "v146", "v147", "v148", "v149", "v150", "v151", "v152", "v153", "v154", "v155", "v156", "v157", "v158", "v159", "v192", "v193", "v194", "v195", "v196", "v197", "v198", "v199", "v200", "v201", "v202", "v203", "v204", "v205", "v206", "v207", "v208", "v209", "v210", "v211", "v212", "v213", "v214", "v215", "v216", "v217", "v218", "v219", "v220", "v221", "v222", "v223", "v224", "v225", "v226", "v227", "v228", "v229", "v230", "v231", "v232", "v233", "v234", "v235", "v236", "v237", "v238", "v239", "v240", "v241", "a0", "a1", "a2", "a3", "a4", "a5", "a6", "a7", "a8", "a9", "a10", "a11", "a12", "a13", "a14", "a15", "a16", "a17", "a18", "a19", "a20", "a21", "a22", "a23", "a24", "a25", "a26", "a27", "a28", "a29", "a30", "a31", "a32", "a33", "a34", "a35", "a36", "a37", "a38", "a39", "a40", "a41", "a42", "a43", "a44", "a45", "a46", "a47", "a48", "a49", "a50", "a51", "a52", "a53", "a54", "a55", "a56", "a57", "a58", "a59", "a60", "a61", "a62", "a63", "a64", "a65", "a66", "a67", "a68", "a69", "a70", "a71", "a72", "a73", "a74", "a75", "a76", "a77", "a78", "a79", "a80", "a81", "a82", "a83", "a84", "a85", "a86", "a87", "a88", "a89", "a90", "a91", "a92", "a93", "a94", "a95", "a96", "a97", "a98", "a99", "a100", "a101", "a102", "a103", "a104", "a105", "a106", "a107", "a108", "a109", "a110", "a111", "a112", "a113", "a114", "a115", "a116", "a117", "a118", "a119", "a120", "a121", "a122", "a123", "a124", "a125", "a126", "a127", "a128", "a129", "a130", "a131", "a132", "a133", "a134", "a135", "a136", "a137", "a138", "a139", "a140", "a141", "a142", "a143", "a144", "a145", "a146", "a147", "a148", "a149", "a150", "a151", "a152", "a153", "a154", "a155", "a156", "a157", "a158", "a159", "a160", "a161", "a162", "a163", "a164", "a165", "a166", "a167", "a168", "a169", "a170", "a171", "a172", "a173", "a174", "a175", "a176", "a177", "a178", "a179", "a180", "a181", "a182", "a183", "a184", "a185", "a186", "a187", "a188", "a189", "a190", "a191", "a192", "a193", "a194", "a195", "a196", "a197", "a198", "a199", "a200", "a201", "a202", "a203", "a204", "a205", "a206", "a207", "a208", "a209", "a210", "a211", "a212", "a213", "a214", "a215", "a216", "a217", "a218", "a219", "a220", "a221", "a222", "a223", "a224", "a225", "a226", "a227", "a228", "a229", "a230", "a231", "s20", "s21", "s22", "s23", "s24", "s25", "s26", "s27", "s28", "s29", "s30", "s31", "s60", "s61", "s62", "s63", "s64", "s65", "s66", "s67", "s68", "s69", "s70", "s71", "s72", "s73", "s74", "s75", "s76", "s77", "s78", "s79", "s80", "s81", "s82", "s83", "s84", "s85", "s86", "s87", "s88", "s89", "s90", "s91", "s93", "s94", "s95", "s98", "s99", "s100", "s101", "vcc", "scc", "memory");
}

}

extern "C" void kernel_launch(void* const* d_in, const int* in_sizes, int n_in, void* d_out, int out_size,
                              void* d_ws, size_t ws_size, hipStream_t stream) {
  if (n_in != 6 || ws_size < WS_END) return;
  const __hip_bfloat16* q = (const __hip_bfloat16*)d_in[0];
  const __hip_bfloat16* kc = (const __hip_bfloat16*)d_in[1];
  const __hip_bfloat16* vc = (const __hip_bfloat16*)d_in[2];
  const __hip_bfloat16* kt = (const __hip_bfloat16*)d_in[3];
  const __hip_bfloat16* vt = (const __hip_bfloat16*)d_in[4];
  const __hip_bfloat16* tm = (const __hip_bfloat16*)d_in[5];
  hipLaunchKernelGGL(tree_attn_main, dim3(B_ * HKV_ * 2), dim3(256), 0, stream, q, kc, vc, kt, vt, tm,
                     (__hip_bfloat16*)d_out, (char*)d_ws);
}
```

```cpp
#include <hip/hip_runtime.h>
#include <hip/hip_bf16.h>
#include <stdint.h>

#pragma clang diagnostic ignored "-Wint-to-pointer-cast"
#pragma clang diagnostic ignored "-Winline-asm"

namespace {

constexpr int B_ = 16, HQ_ = 32, HKV_ = 8, T_ = 32, C_ = 4096, D_ = 128;
constexpr int TILE = 64;
constexpr int KIMG = TILE * D_ * 2;
constexpr int STAGE_BYTES = 2 * KIMG;
constexpr int TREE_OFF = 4 * STAGE_BYTES;
constexpr int Q_OFF = 3 * STAGE_BYTES;
constexpr int LDS_BYTES = TREE_OFF + 2 * 13312;
constexpr unsigned C2 = 299, WIN = 52;

constexpr size_t WS_EPOCH = 0;
constexpr size_t WS_REF = WS_EPOCH + 128 * 128;
constexpr size_t WS_X = WS_REF + 16384, WS_X1 = 4 << 20;
constexpr size_t XW_BYTES = 4096 + 1024;
constexpr size_t WS_END = 0x680000 + WS_X;
static_assert(WS_X1 + WS_X + 128 * 4 * XW_BYTES <= WS_END, "");

typedef float    f32x4  __attribute__((ext_vector_type(4)));
typedef unsigned long long u64x8 __attribute__((ext_vector_type(8)));
typedef unsigned u32x32 __attribute__((ext_vector_type(32)));
typedef unsigned u32x8  __attribute__((ext_vector_type(8)));
typedef unsigned u32x4  __attribute__((ext_vector_type(4)));
typedef unsigned u32x2  __attribute__((ext_vector_type(2)));
typedef __attribute__((address_space(3))) f32x4 lds_f32x4;
typedef __attribute__((address_space(1))) unsigned long long gu64;

__global__ __launch_bounds__(256, 1)
void tree_attn_main(const __hip_bfloat16*, const __hip_bfloat16*, const __hip_bfloat16*, const __hip_bfloat16*,
                    const __hip_bfloat16*, const __hip_bfloat16*, __hip_bfloat16*, char*) {
  __shared__ __attribute__((aligned(1024))) char smem[LDS_BYTES];
  unsigned blk = blockIdx.x;
  u64x8 ka; u32x32 qq; unsigned pre, tch, voffK; int tid = threadIdx.x;
  asm volatile(
      "s_load_dwordx16 s[4:19], %[kp], 0x0\n"
      "s_getpc_b64 s[84:85]\n"
      "v_lshlrev_b32 v112, 4, %[tid]\n"
      "s_lshl_b32 s69, %[blk], 7\n"
      "s_and_b32 s69, s69, 0x7c00\n"
      "v_lshl_add_u32 v120, %[tid], 2, s69\n"
      "global_load_dword v128, v120, s[84:85]\n"
      "v_readfirstlane_b32 s77, %[tid]\n"
      "v_lshrrev_b32 %[vk], 4, %[tid]\n"
      "s_and_b32 s78, %[blk], 7\n"
      "v_xor_b32 %[vk], %[vk], %[tid]\n"
      "s_lshr_b32 s74, %[blk], 4\n"
      "v_and_b32 %[vk], 7, %[vk]\n"
      "s_lshl4_add_u32 s78, s78, s74\n"
      "v_lshrrev_b32 v120, 3, %[tid]\n"
      "s_lshl_b32 s78, s78, 15\n"
      "v_lshlrev_b32 %[vk], 4, %[vk]\n"
      "s_lshl_b32 s77, s77, 5\n"
      "v_lshl_or_b32 %[vk], v120, 8, %[vk]\n"
      "s_bfm_b32 s66, 1, 22\n"
      "s_bfm_b32 s67, 1, 17\n"
      "v_add_u32 v120, 0x2000, %[vk]\n"
      "s_add_u32 s74, s78, 0x4000\n"
      "s_lshl_b32 s76, s78, 5\n"
      "s_add_u32 m0, s77, 0x20000\n"
      "s_mov_b32 s86, 0x100000\n"
      "s_mov_b32 s87, 0x20000\n"
      "s_lshr_b32 s75, s78, 2\n"
      "s_waitcnt lgkmcnt(0)\n"
      "s_mov_b32 s64, s4\n"
      "s_and_b32 s65, s5, 0xffff\n"
      "buffer_load_dwordx4 v[160:163], %[vk], s[64:67], s78 offen nt\n"
      "buffer_load_dwordx4 v[164:167], %[vk], s[64:67], s78 offen offset:128 nt\n"
      "buffer_load_dwordx4 v[168:171], v120, s[64:67], s78 offen nt\n"
      "buffer_load_dwordx4 v[172:175], v120, s[64:67], s78 offen offset:128 nt\n"
      "s_load_dword s92, s[6:7], s76\n"
      "s_load_dword s92, s[8:9], s76\n"
      "buffer_load_dwordx4 v[176:179], %[vk], s[64:67], s74 offen nt\n"
      "buffer_load_dwordx4 v[180:183], %[vk], s[64:67], s74 offen offset:128 nt\n"
      "buffer_load_dwordx4 v[184:187], v120, s[64:67], s74 offen nt\n"
      "buffer_load_dwordx4 v[188:191], v120, s[64:67], s74 offen offset:128 nt\n"
      "s_mov_b32 s84, s10\n"
      "s_and_b32 s85, s11, 0xffff\n"
      "s_lshr_b32 s76, s78, 18\n"
      "s_lshl_b32 s76, s76, 11\n"
      "s_lshr_b32 s69, s77, 1\n"
      "buffer_load_dwordx4 %[vk], s[84:87], s75 offen nt lds\n"
      "s_add_u32 m0, s77, 131968\n"
      "s_cmp_gt_u32 s77, 2048\n"
      "buffer_load_dwordx4 %[vk], s[84:87], s75 offen offset:128 nt lds\n"
      "s_cbranch_scc1 NOMASK_%=\n"
      "s_add_u32 m0, s69, 140288\n"
      "s_mov_b32 s64, s14\n"
      "s_and_b32 s65, s15, 0xffff\n"
      "s_mov_b32 s66, 0x8000\n"
      "buffer_load_dwordx4 v112, s[64:67], s76 offen lds\n"
      "NOMASK_%=:\n"
      "s_mov_b32 s64, s6\n"
      "s_and_b32 s65, s7, 0xffff\n"
      "s_bfm_b32 s66, 1, 27\n"
      "s_lshl_b32 s76, s78, 5\n"
      "s_bfe_u32 s74, %[blk], 0x10003\n"
      "s_lshl_b32 s74, s74, 15\n"
      "s_add_u32 s76, s76, s74\n"
      "s_bfe_u32 s74, %[blk], 0x10004\n"
      "s_lshl_b32 s74, s74, 14\n"
      "s_add_u32 s76, s76, s74\n"
      "s_add_u32 m0, s77, 0\n"
      "s_nop 0\n"
      "buffer_load_dwordx4 %[vk], s[64:67], s76 offen nt lds\n"
      "s_add_u32 m0, s77, 896\n"
      "s_nop 0\n"
      "buffer_load_dwordx4 %[vk], s[64:67], s76 offen offset:128 nt lds\n"
      "s_add_u32 m0, s77, 8192\n"
      "s_nop 0\n"
      "buffer_load_dwordx4 v120, s[64:67], s76 offen nt lds\n"
      : "=&{s[4:19]}"(ka), "=&{v128}"(pre), "=&{v[160:191]}"(qq), "=&{s92}"(tch), [vk] "=&v"(voffK), [tid] "+v"(tid)
      : [kp] "s"(__builtin_amdgcn_kernarg_segment_ptr()), [blk] "s"(blk)
      : "v112", "v120", "s64", "s65", "s66", "s67", "s69", "s70", "s71", "s74", "s75", "s76", "s77", "s78", "s84", "s85", "s86", "s87", "s96", "s97", "scc", "memory");
  asm volatile("" : "+s"(blk));
  const int lane = tid & 63;
  const int wid = __builtin_amdgcn_readfirstlane(tid >> 6);
  const unsigned wbase = wid * 2048;
  const int rh = wid & 1, kh = wid >> 1;
  const int xg = blk & 7, jj = blk >> 3, bk = xg * 16 + (jj >> 1), split = jj & 1;
  const int c16 = lane & 15, g = lane >> 4;
  const unsigned lds0 = (unsigned)(uintptr_t)smem;

  const unsigned voffV = (unsigned)((8 * wid + (lane >> 3)) * 256 + (((lane & 7) ^ (g << 1)) << 4));
  const unsigned so0 = (unsigned)(bk * C_ + split * 2 * TILE) * (D_ * 2) + (bk & 1) * KIMG, inc = KIMG - 2 * (bk & 1) * KIMG;
#pragma unroll
  for (int j = 0; j < 4; ++j) *(lds_f32x4*)(lds0 + KIMG + 4096 * j + tid * 16) = f32x4{0.f, 0.f, 0.f, 0.f};
#pragma unroll
  for (int j = 0; j < 2; ++j) *(lds_f32x4*)(lds0 + STAGE_BYTES + KIMG + (1 - split) * 8192 + 4096 * j + tid * 16) = f32x4{0.f, 0.f, 0.f, 0.f};
  u32x8 ad;
#pragma unroll
  for (int j = 0; j < 2; ++j)
    ad[j] = lds0 + (unsigned)(2048 * (c16 >> 3) + 128 * (c16 & 7) + (((4 * j + g) ^ (c16 >> 1)) << 4) + kh * 8192);
  {
    const int q4 = c16 >> 2, p = lane & 3;
#pragma unroll
    for (int j = 0; j < 4; ++j)
      ad[2 + j] = lds0 + (unsigned)(2048 * (g >> 1) + 128 * (4 * (g & 1) + q4) + kh * 8192 + KIMG +
                                    (((2 * (j ^ (2 * kh)) + (p >> 1)) ^ ((2 * (g & 1) + (q4 >> 1)) << 1)) << 4) + 8 * (p & 1));
  }
  ad[6] = voffK; ad[7] = voffV + 128 * split;
  u32x2 ra = {(unsigned)(lane < 4 ? WS_REF + lane * 512 : 1 << 26), (unsigned)(WS_REF + (jj & (4 - 1)) * 512 + (lane & 7) * 4 + (wid << 26))};
  auto xarea = [&](int w) { return lds0 + ((0x8d802000u >> (8 * w)) & 255u) * 1024; };
  const unsigned pg = jj >> 2, px = pg - (pg > 160 / 32) + xg * 5;
  const unsigned n1 = 4 + max(px - 6 * (px * 43 >> 8), ((0x70000000u | 6u << 160 / 8) >> 4 * pg) & 7);
  asm volatile(
      "s_memrealtime s[82:83]\n"
      "s_mov_b32 s60, %[so0]\n"
      "s_mov_b32 s62, %[wb]\n"
      "v_add_u32 v120, 0x2000, v248\n"
      "s_and_b32 s80, %[fo], 0xffffff80\n"
      "v_mov_b32 v240, s80\n"
      "s_mov_b32 s22, 0x8000000\n"
      "s_mov_b32 s23, 0x20000\n"
      "s_mov_b32 s20, s6\n"
      "s_and_b32 s21, s7, 0xffff\n"
      "s_nop 0\n"
      "v_mov_b64 v[216:217], 0\n"
      "v_mov_b64 v[218:219], 0\n"
      "s_mov_b32 s86, 0x100000\n"
      "s_mov_b32 s87, 0x20000\n"
      "v_mfma_f32_32x32x16_bf16 v[32:47], v[216:219], v[216:219], 0\n"
      "s_lshr_b32 s75, %[qo], 2\n"
      "s_mov_b32 s84, s12\n"
      "s_and_b32 s85, s13, 0xffff\n"
      "v_mfma_f32_32x32x16_bf16 v[48:63], v[216:219], v[216:219], 0\n"
      "s_nop 0\n"
      "v_xor_b32 v121, 0x80, v249\n"
      "s_sub_u32 s78, 0x10000, %[inc]\n"
      "s_mov_b32 s94, 0x3e0293ee\n"
      "v_mfma_f32_32x32x16_bf16 v[64:79], v[216:219], v[216:219], 0\n"
      "s_mov_b32 s95, 0x3e0293ee\n"
      "s_mov_b32 s74, 0xffff0000\n"
      "s_mov_b32 s76, 0xceabfb8f\n"
      "v_mfma_f32_32x32x16_bf16 v[80:95], v[216:219], v[216:219], 0\n"
      "v_mbcnt_lo_u32_b32 v112, -1, 0\n"
      "s_nop 0\n"
      "v_mbcnt_hi_u32_b32 v112, -1, v112\n"
      "s_add_u32 s69, %[wb], 0x18000\n"
      "v_mfma_f32_32x32x16_bf16 v[192:207], v[216:219], v[216:219], 0\n"
      "v_lshl_add_u32 v226, v112, 4, s69\n"
      "v_lshrrev_b32 v115, 4, v112\n"
      "v_and_b32 v112, 15, v112\n"
      "v_mfma_f32_32x32x16_bf16 a[128:143], v[216:219], v[216:219], 0\n"
      "v_lshlrev_b32 v115, 3, v115\n"
      "v_lshl_add_u32 v115, v112, 6, v115\n"
      "s_add_u32 m0, s62, 9088\n"
      "s_nop 0\n"
      "buffer_load_dwordx4 v120, s[20:23], s60 offen offset:128 nt lds\n"
      "v_add_u32 v115, 140288, v115\n"
      "v_mfma_f32_32x32x16_bf16 a[144:159], v[216:219], v[216:219], 0\n"
      "v_add_u32 v118, %[qoff], v242\n"
      "v_add_u32 v224, %[toff], v242\n"
      "v_add_u32 v119, %[qoff], v243\n"
      "v_mfma_f32_32x32x16_bf16 a[160:175], v[216:219], v[216:219], 0\n"
      "v_add_u32 v225, %[toff], v243\n"
      "v_mfma_f32_32x32x16_bf16 a[176:191], v[216:219], v[216:219], 0\n"
      "s_add_u32 s62, s62, 0x8000\n"
      "s_and_b32 s62, s62, 0x1ffff\n"
      "s_add_u32 s60, s60, %[inc]\n"
      "s_waitcnt vmcnt(13)\n"
      "v_lshlrev_b32 v232, 16, v160\n"
      "v_and_b32 v233, s74, v160\n"
      "v_lshlrev_b32 v234, 16, v161\n"
      "v_and_b32 v235, s74, v161\n"
      "v_lshlrev_b32 v236, 16, v162\n"
      "v_and_b32 v237, s74, v162\n"
      "v_lshlrev_b32 v238, 16, v163\n"
      "v_and_b32 v239, s74, v163\n"
      "v_pk_mul_f32 v[232:233], v[232:233], s[94:95]\n"
      "v_pk_mul_f32 v[234:235], v[234:235], s[94:95]\n"
      "v_pk_mul_f32 v[236:237], v[236:237], s[94:95]\n"
      "v_pk_mul_f32 v[238:239], v[238:239], s[94:95]\n"
      "v_cvt_pk_bf16_f32 v160, v232, v233\n"
      "v_cvt_pk_bf16_f32 v161, v234, v235\n"
      "v_cvt_pk_bf16_f32 v162, v236, v237\n"
      "v_cvt_pk_bf16_f32 v163, v238, v239\n"
      "ds_write_b128 v226, v[160:163] offset:0\n"
      "s_waitcnt vmcnt(12)\n"
      "v_lshlrev_b32 v232, 16, v164\n"
      "v_and_b32 v233, s74, v164\n"
      "v_lshlrev_b32 v234, 16, v165\n"
      "v_and_b32 v235, s74, v165\n"
      "v_lshlrev_b32 v236, 16, v166\n"
      "v_and_b32 v237, s74, v166\n"
      "v_lshlrev_b32 v238, 16, v167\n"
      "v_and_b32 v239, s74, v167\n"
      "v_pk_mul_f32 v[232:233], v[232:233], s[94:95]\n"
      "v_pk_mul_f32 v[234:235], v[234:235], s[94:95]\n"
      "v_pk_mul_f32 v[236:237], v[236:237], s[94:95]\n"
      "v_pk_mul_f32 v[238:239], v[238:239], s[94:95]\n"
      "v_cvt_pk_bf16_f32 v164, v232, v233\n"
      "v_cvt_pk_bf16_f32 v165, v234, v235\n"
      "v_cvt_pk_bf16_f32 v166, v236, v237\n"
      "v_cvt_pk_bf16_f32 v167, v238, v239\n"
      "ds_write_b128 v226, v[164:167] offset:1024\n"
      "s_waitcnt vmcnt(11)\n"
      "v_lshlrev_b32 v232, 16, v168\n"
      "v_and_b32 v233, s74, v168\n"
      "v_lshlrev_b32 v234, 16, v169\n"
      "v_and_b32 v235, s74, v169\n"
      "v_lshlrev_b32 v236, 16, v170\n"
      "v_and_b32 v237, s74, v170\n"
      "v_lshlrev_b32 v238, 16, v171\n"
      "v_and_b32 v239, s74, v171\n"
      "v_pk_mul_f32 v[232:233], v[232:233], s[94:95]\n"
      "v_pk_mul_f32 v[234:235], v[234:235], s[94:95]\n"
      "v_pk_mul_f32 v[236:237], v[236:237], s[94:95]\n"
      "v_pk_mul_f32 v[238:239], v[238:239], s[94:95]\n"
      "v_cvt_pk_bf16_f32 v168, v232, v233\n"
      "v_cvt_pk_bf16_f32 v169, v234, v235\n"
      "v_cvt_pk_bf16_f32 v170, v236, v237\n"
      "v_cvt_pk_bf16_f32 v171, v238, v239\n"
      "ds_write_b128 v226, v[168:171] offset:8192\n"
      "s_add_u32 m0, s62, 0\n"
      "s_nop 0\n"
      "buffer_load_dwordx4 v248, s[20:23], s60 offen nt lds\n"
      "s_waitcnt vmcnt(11)\n"
      "v_lshlrev_b32 v232, 16, v172\n"
      "v_and_b32 v233, s74, v172\n"
      "v_lshlrev_b32 v234, 16, v173\n"
      "v_and_b32 v235, s74, v173\n"
      "v_lshlrev_b32 v236, 16, v174\n"
      "v_and_b32 v237, s74, v174\n"
      "v_lshlrev_b32 v238, 16, v175\n"
      "v_and_b32 v239, s74, v175\n"
      "v_pk_mul_f32 v[232:233], v[232:233], s[94:95]\n"
      "v_pk_mul_f32 v[234:235], v[234:235], s[94:95]\n"
      "v_pk_mul_f32 v[236:237], v[236:237], s[94:95]\n"
      "v_pk_mul_f32 v[238:239], v[238:239], s[94:95]\n"
      "v_cvt_pk_bf16_f32 v172, v232, v233\n"
      "v_cvt_pk_bf16_f32 v173, v234, v235\n"
      "v_cvt_pk_bf16_f32 v174, v236, v237\n"
      "v_cvt_pk_bf16_f32 v175, v238, v239\n"
      "ds_write_b128 v226, v[172:175] offset:9216\n"
      "s_add_u32 m0, s62, 896\n"
      "s_nop 0\n"
      "buffer_load_dwordx4 v248, s[20:23], s60 offen offset:128 nt lds\n"
      "s_waitcnt vmcnt(11)\n"
      "v_lshlrev_b32 v232, 16, v176\n"
      "v_and_b32 v233, s74, v176\n"
      "v_lshlrev_b32 v234, 16, v177\n"
      "v_and_b32 v235, s74, v177\n"
      "v_lshlrev_b32 v236, 16, v178\n"
      "v_and_b32 v237, s74, v178\n"
      "v_lshlrev_b32 v238, 16, v179\n"
      "v_and_b32 v239, s74, v179\n"
      "v_pk_mul_f32 v[232:233], v[232:233], s[94:95]\n"
      "v_pk_mul_f32 v[234:235], v[234:235], s[94:95]\n"
      "v_pk_mul_f32 v[236:237], v[236:237], s[94:95]\n"
      "v_pk_mul_f32 v[238:239], v[238:239], s[94:95]\n"
      "v_cvt_pk_bf16_f32 v176, v232, v233\n"
      "v_cvt_pk_bf16_f32 v177, v234, v235\n"
      "v_cvt_pk_bf16_f32 v178, v236, v237\n"
      "v_cvt_pk_bf16_f32 v179, v238, v239\n"
      "ds_write_b128 v226, v[176:179] offset:16384\n"
      "s_add_u32 m0, s62, 8192\n"
      "s_nop 0\n"
      "buffer_load_dwordx4 v120, s[20:23], s60 offen nt lds\n"
      "s_waitcnt vmcnt(11)\n"
      "v_lshlrev_b32 v232, 16, v180\n"
      "v_and_b32 v233, s74, v180\n"
      "v_lshlrev_b32 v234, 16, v181\n"
      "v_and_b32 v235, s74, v181\n"
      "v_lshlrev_b32 v236, 16, v182\n"
      "v_and_b32 v237, s74, v182\n"
      "v_lshlrev_b32 v238, 16, v183\n"
      "v_and_b32 v239, s74, v183\n"
      "v_pk_mul_f32 v[232:233], v[232:233], s[94:95]\n"
      "v_pk_mul_f32 v[234:235], v[234:235], s[94:95]\n"
      "v_pk_mul_f32 v[236:237], v[236:237], s[94:95]\n"
      "v_pk_mul_f32 v[238:239], v[238:239], s[94:95]\n"
      "v_cvt_pk_bf16_f32 v180, v232, v233\n"
      "v_cvt_pk_bf16_f32 v181, v234, v235\n"
      "v_cvt_pk_bf16_f32 v182, v236, v237\n"
      "v_cvt_pk_bf16_f32 v183, v238, v239\n"
      "ds_write_b128 v226, v[180:183] offset:17408\n"
      "s_add_u32 m0, s62, 9088\n"
      "s_nop 0\n"
      "buffer_load_dwordx4 v120, s[20:23], s60 offen offset:128 nt lds\n"
      "s_waitcnt vmcnt(11)\n"
      "v_lshlrev_b32 v232, 16, v184\n"
      "v_and_b32 v233, s74, v184\n"
      "v_lshlrev_b32 v234, 16, v185\n"
      "v_and_b32 v235, s74, v185\n"
      "v_lshlrev_b32 v236, 16, v186\n"
      "v_and_b32 v237, s74, v186\n"
      "v_lshlrev_b32 v238, 16, v187\n"
      "v_and_b32 v239, s74, v187\n"
      "v_pk_mul_f32 v[232:233], v[232:233], s[94:95]\n"
      "v_pk_mul_f32 v[234:235], v[234:235], s[94:95]\n"
      "v_pk_mul_f32 v[236:237], v[236:237], s[94:95]\n"
      "v_pk_mul_f32 v[238:239], v[238:239], s[94:95]\n"
      "v_cvt_pk_bf16_f32 v184, v232, v233\n"
      "v_cvt_pk_bf16_f32 v185, v234, v235\n"
      "v_cvt_pk_bf16_f32 v186, v236, v237\n"
      "v_cvt_pk_bf16_f32 v187, v238, v239\n"
      "ds_write_b128 v226, v[184:187] offset:24576\n"
      "s_add_u32 m0, %[tv], 0\n"
      "s_nop 0\n"
      "buffer_load_dwordx4 v249, s[84:87], s75 offen nt lds\n"
      "s_add_u32 m0, %[tv], 1024\n"
      "s_nop 0\n"
      "buffer_load_dwordx4 v121, s[84:87], s75 offen nt lds\n"
      "s_waitcnt vmcnt(12)\n"
      "v_lshlrev_b32 v232, 16, v188\n"
      "v_and_b32 v233, s74, v188\n"
      "v_lshlrev_b32 v234, 16, v189\n"
      "v_and_b32 v235, s74, v189\n"
      "v_lshlrev_b32 v236, 16, v190\n"
      "v_and_b32 v237, s74, v190\n"
      "v_lshlrev_b32 v238, 16, v191\n"
      "v_and_b32 v239, s74, v191\n"
      "v_pk_mul_f32 v[232:233], v[232:233], s[94:95]\n"
      "v_pk_mul_f32 v[234:235], v[234:235], s[94:95]\n"
      "v_pk_mul_f32 v[236:237], v[236:237], s[94:95]\n"
      "v_pk_mul_f32 v[238:239], v[238:239], s[94:95]\n"
      "v_cvt_pk_bf16_f32 v188, v232, v233\n"
      "v_cvt_pk_bf16_f32 v189, v234, v235\n"
      "v_cvt_pk_bf16_f32 v190, v236, v237\n"
      "v_cvt_pk_bf16_f32 v191, v238, v239\n"
      "ds_write_b128 v226, v[188:191] offset:25600\n"
      "s_add_u32 s62, s62, 0x8000\n"
      "s_and_b32 s62, s62, 0x1ffff\n"
      "s_add_u32 s60, s60, s78\n"
      "s_waitcnt vmcnt(10) lgkmcnt(0)\n"
      "s_barrier\n"
      "s_memrealtime s[68:69]\n"
      "ds_read_b64 v[208:209], v115 offset:0\n"
      "ds_read_b64 v[210:211], v115 offset:32\n"
      "ds_read_b64 v[212:213], v115 offset:1024\n"
      "ds_read_b64 v[214:215], v115 offset:1056\n"
      "ds_read_b128 a[96:99], v224 offset:0\n"
      "ds_read_b128 a[100:103], v224 offset:4096\n"
      "ds_read_b128 a[0:3], v118 offset:0\n"
      "ds_read_b128 a[4:7], v118 offset:4096\n"
      "ds_read_b128 a[8:11], v118 offset:8192\n"
      "ds_read_b128 a[12:15], v118 offset:12288\n"
      "ds_read_b128 a[104:107], v225 offset:0\n"
      "ds_read_b128 a[108:111], v225 offset:4096\n"
      "global_load_dwordx2 v[240:241], v240, s[18:19] sc1\n"
      "s_mov_b32 s28, s18\n"
      "s_and_b32 s29, s19, 0xffff\n"
      "s_mov_b32 s30, 0x688000\n"
      "s_mov_b32 s31, 0x20000\n"
      "s_cmp_lt_u32 %[rep], 128\n"
      "s_cselect_b32 s89, 0x2ffe, 0\n"
      "s_or_b32 s89, s89, 0x2aae\n"
      "s_waitcnt lgkmcnt(8)\n"
      "ds_read_b128 a[16:19], v119 offset:0\n"
      "ds_read_b128 a[20:23], v119 offset:4096\n"
      "ds_read_b128 a[24:27], v119 offset:8192\n"
      "ds_read_b128 a[28:31], v119 offset:12288\n"
      "v_lshlrev_b32 v96, 16, v208\n"
      "v_and_b32 v97, s74, v208\n"
      "v_lshlrev_b32 v98, 16, v209\n"
      "v_and_b32 v99, s74, v209\n"
      "v_lshlrev_b32 v100, 16, v210\n"
      "v_and_b32 v101, s74, v210\n"
      "v_lshlrev_b32 v102, 16, v211\n"
      "v_and_b32 v103, s74, v211\n"
      "v_lshlrev_b32 v104, 16, v212\n"
      "v_and_b32 v105, s74, v212\n"
      "v_lshlrev_b32 v106, 16, v213\n"
      "v_and_b32 v107, s74, v213\n"
      "v_lshlrev_b32 v108, 16, v214\n"
      "v_and_b32 v109, s74, v214\n"
      "v_lshlrev_b32 v110, 16, v215\n"
      "v_and_b32 v111, s74, v215\n"
      "v_fma_f32 v96, -v96, s76, s76\n"
      "v_fma_f32 v97, -v97, s76, s76\n"
      "v_fma_f32 v98, -v98, s76, s76\n"
      "v_fma_f32 v99, -v99, s76, s76\n"
      "v_fma_f32 v100, -v100, s76, s76\n"
      "v_fma_f32 v101, -v101, s76, s76\n"
      "v_fma_f32 v102, -v102, s76, s76\n"
      "v_fma_f32 v103, -v103, s76, s76\n"
      "v_fma_f32 v104, -v104, s76, s76\n"
      "v_fma_f32 v105, -v105, s76, s76\n"
      "v_fma_f32 v106, -v106, s76, s76\n"
      "v_fma_f32 v107, -v107, s76, s76\n"
      "v_fma_f32 v108, -v108, s76, s76\n"
      "v_fma_f32 v109, -v109, s76, s76\n"
      "v_fma_f32 v110, -v110, s76, s76\n"
      "v_fma_f32 v111, -v111, s76, s76\n"
      "s_waitcnt lgkmcnt(0)\n"
      "ds_read_b128 a[112:115], v224 offset:1024\n"
      "ds_read_b128 a[116:119], v224 offset:5120\n"
      "ds_read_b128 a[32:35], v118 offset:1024\n"
      "ds_read_b128 a[36:39], v118 offset:5120\n"
      "ds_read_b128 a[40:43], v118 offset:9216\n"
      "ds_read_b128 a[44:47], v118 offset:13312\n"
      "ds_read_b128 a[120:123], v225 offset:1024\n"
      "ds_read_b128 a[124:127], v225 offset:5120\n"
      "ds_read_b128 a[48:51], v119 offset:1024\n"
      "ds_read_b128 a[52:55], v119 offset:5120\n"
      "ds_read_b128 a[56:59], v119 offset:9216\n"
      "ds_read_b128 a[60:63], v119 offset:13312\n"
      "s_cmp_lt_u32 %[lead], 32\n"
      "s_cbranch_scc0 NOLEAD_%=\n"
      "v_mov_b32 v115, s68\n"
      "buffer_store_dword v115, v250, s[28:31], %[lead] offen sc1\n"
      "NOLEAD_%=:\n"
      "v_mfma_f32_16x16x32_bf16 v[160:163], a[96:99], a[0:3], v[96:99]\n"
      "s_mov_b32 s83, 0\n"
      "v_add_u32 v122, 0x2000, v249\n"
      "v_add_u32 v123, 0x2000, v121\n"
      "v_mfma_f32_16x16x32_bf16 v[168:171], a[96:99], a[4:7], v[104:107]\n"
      "s_add_u32 m0, s62, 0\n"
      "s_sub_u32 s67, s68, s82\n"
      "v_mov_b32 v113, 0x3f803f80\n"
      "v_accvgpr_write_b32 a192, 0\n"
      "v_mfma_f32_16x16x32_bf16 v[176:179], a[96:99], a[8:11], v[96:99]\n"
      "s_min_u32 s67, s67, 140\n"
      "buffer_load_dwordx4 v248, s[20:23], s60 offen nt lds\n"
      "v_cmp_eq_u32 vcc, 0, v112\n"
      "v_mfma_f32_16x16x32_bf16 v[184:187], a[96:99], a[12:15], v[104:107]\n"
      "s_add_u32 m0, s62, 896\n"
      "s_add_u32 s66, s82, s67\n"
      "v_mfma_f32_16x16x32_bf16 v[164:167], a[100:103], a[0:3], v[100:103]\n"
      "s_add_u32 s66, s66, 70\n"
      "buffer_load_dwordx4 v248, s[20:23], s60 offen offset:128 nt lds\n"
      "v_cndmask_b32 v114, 0, v113, vcc\n"
      "v_mfma_f32_16x16x32_bf16 v[172:175], a[100:103], a[4:7], v[108:111]\n"
      "s_add_u32 m0, s62, 8192\n"
      "s_sub_u32 s71, s68, 400\n"
      "v_accvgpr_write_b32 a196, v114\n"
      "v_accvgpr_write_b32 a197, v114\n"
      "v_mfma_f32_16x16x32_bf16 v[180:183], a[100:103], a[8:11], v[100:103]\n"
      "s_mov_b32 s68, s66\n"
      "buffer_load_dwordx4 v120, s[20:23], s60 offen nt lds\n"
      "v_accvgpr_write_b32 a198, v114\n"
      "v_accvgpr_write_b32 a199, v114\n"
      "v_mfma_f32_16x16x32_bf16 v[188:191], a[100:103], a[12:15], v[108:111]\n"
      "s_add_u32 m0, s62, 9088\n"
      "s_mov_b32 s82, s66\n"
      "v_cmp_eq_u32 vcc, 4, v112\n"
      "v_mfma_f32_16x16x32_bf16 v[160:163], a[104:107], a[16:19], v[160:163]\n"
      "s_add_u32 s62, s62, 0x8000\n"
      "buffer_load_dwordx4 v120, s[20:23], s60 offen offset:128 nt lds\n"
      "s_mov_b32 s61, %[so0]\n"
      "v_mfma_f32_16x16x32_bf16 v[168:171], a[104:107], a[20:23], v[168:171]\n"
      "s_and_b32 s62, s62, 0x1ffff\n"
      "s_add_u32 s77, %[wb], 0x10000\n"
      "v_cndmask_b32 v114, 0, v113, vcc\n"
      "v_mfma_f32_16x16x32_bf16 v[176:179], a[104:107], a[24:27], v[176:179]\n"
      "s_add_u32 s60, s60, %[inc]\n"
      "s_mov_b32 s70, 300\n"
      "v_accvgpr_write_b32 a200, v114\n"
      "v_accvgpr_write_b32 a201, v114\n"
      "v_mfma_f32_16x16x32_bf16 v[184:187], a[104:107], a[28:31], v[184:187]\n"
      "s_mov_b32 s80, 0x12a\n"
      "v_accvgpr_write_b32 a202, v114\n"
      "v_accvgpr_write_b32 a203, v114\n"
      "v_mfma_f32_16x16x32_bf16 v[164:167], a[108:111], a[16:19], v[164:167]\n"
      "s_mov_b32 s81, 0x0\n"
      "v_cmp_eq_u32 vcc, 8, v112\n"
      "v_mfma_f32_16x16x32_bf16 v[172:175], a[108:111], a[20:23], v[172:175]\n"
      "s_mov_b32 s101, 1000\n"
      "v_mfma_f32_16x16x32_bf16 v[180:183], a[108:111], a[24:27], v[180:183]\n"
      "s_mov_b32 s98, 110\n"
      "v_cndmask_b32 v114, 0, v113, vcc\n"
      "v_mfma_f32_16x16x32_bf16 v[188:191], a[108:111], a[28:31], v[188:191]\n"
      "s_waitcnt lgkmcnt(0)\n"
      "s_mov_b32 s93, 28\n"
      "v_accvgpr_write_b32 a204, v114\n"
      "v_accvgpr_write_b32 a205, v114\n"
      "v_mfma_f32_16x16x32_bf16 v[160:163], a[112:115], a[32:35], v[160:163]\n"
      "s_mov_b32 s99, 21\n"
      "v_accvgpr_write_b32 a206, v114\n"
      "v_accvgpr_write_b32 a207, v114\n"
      "v_mfma_f32_16x16x32_bf16 v[168:171], a[112:115], a[36:39], v[168:171]\n"
      "s_mov_b32 s100, 282\n"
      "v_cmp_eq_u32 vcc, 12, v112\n"
      "v_mfma_f32_16x16x32_bf16 v[176:179], a[112:115], a[40:43], v[176:179]\n"
      "s_lshl_b32 s91, %[wb], 18\n"
      "v_mfma_f32_16x16x32_bf16 v[184:187], a[112:115], a[44:47], v[184:187]\n"
      "s_sub_u32 s91, 0x10000000, s91\n"
      "v_cndmask_b32 v114, 0, v113, vcc\n"
      "v_mfma_f32_16x16x32_bf16 v[164:167], a[116:119], a[32:35], v[164:167]\n"
      "s_mov_b32 s86, 110\n"
      "v_accvgpr_write_b32 a208, v114\n"
      "v_accvgpr_write_b32 a209, v114\n"
      "v_mfma_f32_16x16x32_bf16 v[172:175], a[116:119], a[36:39], v[172:175]\n"
      "s_mov_b32 s87, 0\n"
      "v_accvgpr_write_b32 a210, v114\n"
      "v_accvgpr_write_b32 a211, v114\n"
      "s_waitcnt vmcnt(11)\n"
      "s_barrier\n"
      "v_mfma_f32_16x16x32_bf16 v[180:183], a[116:119], a[40:43], v[180:183]\n"
      "s_mov_b32 s26, 0x8000000\n"
      "ds_read_b128 a[64:67], v242 offset:0\n"
      "v_mfma_f32_16x16x32_bf16 v[188:191], a[116:119], a[44:47], v[188:191]\n"
      "s_mov_b32 s27, 0x20000\n"
      "ds_read_b128 a[68:71], v242 offset:4096\n"
      "v_mfma_f32_16x16x32_bf16 v[160:163], a[120:123], a[48:51], v[160:163]\n"
      "s_mov_b32 s24, s8\n"
      "ds_read_b128 a[72:75], v243 offset:0\n"
      "v_mfma_f32_16x16x32_bf16 v[168:171], a[120:123], a[52:55], v[168:171]\n"
      "s_and_b32 s25, s9, 0xffff\n"
      "ds_read_b128 a[76:79], v243 offset:4096\n"
      "v_mfma_f32_16x16x32_bf16 v[176:179], a[120:123], a[56:59], v[176:179]\n"
      "ds_read_b128 a[80:83], v242 offset:1024\n"
      "v_mfma_f32_16x16x32_bf16 v[184:187], a[120:123], a[60:63], v[184:187]\n"
      "ds_read_b128 a[84:87], v242 offset:5120\n"
      "v_mfma_f32_16x16x32_bf16 v[164:167], a[124:127], a[48:51], v[164:167]\n"
      "ds_read_b128 a[88:91], v243 offset:1024\n"
      "v_mfma_f32_16x16x32_bf16 v[172:175], a[124:127], a[52:55], v[172:175]\n"
      "ds_read_b128 a[92:95], v243 offset:5120\n"
      "v_mfma_f32_16x16x32_bf16 v[180:183], a[124:127], a[56:59], v[180:183]\n"
      "v_mfma_f32_16x16x32_bf16 v[188:191], a[124:127], a[60:63], v[188:191]\n"
      "s_nop 3\n"
      "v_max3_f32 v112, v160, v161, v162\n"
      "v_max3_f32 v113, v168, v169, v170\n"
      "v_max3_f32 v114, v176, v177, v178\n"
      "v_max3_f32 v115, v184, v185, v186\n"
      "v_max3_f32 v112, v112, v163, v164\n"
      "v_max3_f32 v113, v113, v171, v172\n"
      "v_max3_f32 v114, v114, v179, v180\n"
      "v_max3_f32 v115, v115, v187, v188\n"
      "v_max3_f32 v112, v112, v165, v166\n"
      "v_max3_f32 v113, v113, v173, v174\n"
      "v_max3_f32 v114, v114, v181, v182\n"
      "v_max3_f32 v115, v115, v189, v190\n"
      "v_max_f32 v112, v112, v167\n"
      "v_max_f32 v113, v113, v175\n"
      "v_max_f32 v114, v114, v183\n"
      "v_max_f32 v115, v115, v191\n"
      "v_mov_b32 v208, v112\n"
      "v_mov_b32 v209, v113\n"
      "v_mov_b32 v210, v114\n"
      "v_mov_b32 v211, v115\n"
      "v_permlane16_swap_b32 v112, v208\n"
      "v_permlane16_swap_b32 v113, v209\n"
      "v_permlane16_swap_b32 v114, v210\n"
      "v_permlane16_swap_b32 v115, v211\n"
      "v_max_f32 v112, v112, v208\n"
      "v_max_f32 v113, v113, v209\n"
      "v_max_f32 v114, v114, v210\n"
      "v_max_f32 v115, v115, v211\n"
      "v_mov_b32 v208, v112\n"
      "v_mov_b32 v209, v113\n"
      "v_mov_b32 v210, v114\n"
      "v_mov_b32 v211, v115\n"
      "v_permlane32_swap_b32 v112, v208\n"
      "v_permlane32_swap_b32 v113, v209\n"
      "v_permlane32_swap_b32 v114, v210\n"
      "v_permlane32_swap_b32 v115, v211\n"
      "v_max_f32 v112, v112, v208\n"
      "v_max_f32 v113, v113, v209\n"
      "v_max_f32 v114, v114, v210\n"
      "v_max_f32 v115, v115, v211\n"
      "v_sub_f32 v112, 0xc1800000, v112\n"
      "v_sub_f32 v113, 0xc1800000, v113\n"
      "v_sub_f32 v114, 0xc1800000, v114\n"
      "v_sub_f32 v115, 0xc1800000, v115\n"
      "v_floor_f32 v112, v112\n"
      "v_floor_f32 v113, v113\n"
      "v_floor_f32 v114, v114\n"
      "v_floor_f32 v115, v115\n"
      "v_min_f32 v224, 0x7f000000, v112\n"
      "v_min_f32 v228, 0x7f000000, v113\n"
      "v_min_f32 v232, 0x7f000000, v114\n"
      "v_min_f32 v236, 0x7f000000, v115\n"
      "v_add_f32 v112, %[neg], v224\n"
      "v_add_f32 v113, %[neg], v228\n"
      "v_add_f32 v114, %[neg], v232\n"
      "v_add_f32 v115, %[neg], v236\n"
      "v_mov_b32 v225, v224\n"
      "v_mov_b32 v229, v228\n"
      "v_mov_b32 v233, v232\n"
      "v_mov_b32 v237, v236\n"
      "v_mov_b64 v[226:227], v[224:225]\n"
      "v_mov_b64 v[230:231], v[228:229]\n"
      "v_mov_b64 v[234:235], v[232:233]\n"
      "v_mov_b64 v[238:239], v[236:237]\n"
      "s_add_u32 m0, s77, 16384\n"
      "v_pk_add_f32 v[160:161], v[160:161], v[112:113] op_sel:[0,0] op_sel_hi:[1,0]\n"
      "buffer_load_dwordx4 v249, s[24:27], s61 offen nt lds\n"
      "v_pk_add_f32 v[162:163], v[162:163], v[112:113] op_sel:[0,0] op_sel_hi:[1,0]\n"
      "v_pk_add_f32 v[164:165], v[164:165], v[112:113] op_sel:[0,0] op_sel_hi:[1,0]\n"
      "v_pk_add_f32 v[166:167], v[166:167], v[112:113] op_sel:[0,0] op_sel_hi:[1,0]\n"
      "s_add_u32 m0, s77, 17408\n"
      "v_pk_add_f32 v[168:169], v[168:169], v[112:113] op_sel:[0,1] op_sel_hi:[1,1]\n"
      "buffer_load_dwordx4 v121, s[24:27], s61 offen nt lds\n"
      "v_pk_add_f32 v[170:171], v[170:171], v[112:113] op_sel:[0,1] op_sel_hi:[1,1]\n"
      "v_pk_add_f32 v[172:173], v[172:173], v[112:113] op_sel:[0,1] op_sel_hi:[1,1]\n"
      "v_pk_add_f32 v[174:175], v[174:175], v[112:113] op_sel:[0,1] op_sel_hi:[1,1]\n"
      "s_add_u32 m0, s77, 24576\n"
      "v_pk_add_f32 v[176:177], v[176:177], v[114:115] op_sel:[0,0] op_sel_hi:[1,0]\n"
      "buffer_load_dwordx4 v122, s[24:27], s61 offen nt lds\n"
      "v_pk_add_f32 v[178:179], v[178:179], v[114:115] op_sel:[0,0] op_sel_hi:[1,0]\n"
      "v_pk_add_f32 v[180:181], v[180:181], v[114:115] op_sel:[0,0] op_sel_hi:[1,0]\n"
      "v_pk_add_f32 v[182:183], v[182:183], v[114:115] op_sel:[0,0] op_sel_hi:[1,0]\n"
      "s_add_u32 m0, s77, 25600\n"
      "v_pk_add_f32 v[184:185], v[184:185], v[114:115] op_sel:[0,1] op_sel_hi:[1,1]\n"
      "buffer_load_dwordx4 v123, s[24:27], s61 offen nt lds\n"
      "v_pk_add_f32 v[186:187], v[186:187], v[114:115] op_sel:[0,1] op_sel_hi:[1,1]\n"
      "v_pk_add_f32 v[188:189], v[188:189], v[114:115] op_sel:[0,1] op_sel_hi:[1,1]\n"
      "v_pk_add_f32 v[190:191], v[190:191], v[114:115] op_sel:[0,1] op_sel_hi:[1,1]\n"
      "s_add_u32 s77, s77, 0x8000\n"
      "s_and_b32 s77, s77, 0x1ffff\n"
      "s_add_u32 s61, s61, %[inc]\n"
      "v_exp_f32 v112, v160\n"
      "v_exp_f32 v113, v161\n"
      "v_exp_f32 v114, v162\n"
      "v_cvt_pk_bf16_f32 v208, v112, v113\n"
      "v_exp_f32 v115, v163\n"
      "v_exp_f32 v112, v164\n"
      "v_cvt_pk_bf16_f32 v209, v114, v115\n"
      "v_exp_f32 v113, v165\n"
      "v_or_b32 v116, v208, v209\n"
      "v_exp_f32 v114, v166\n"
      "v_cvt_pk_bf16_f32 v210, v112, v113\n"
      "v_exp_f32 v115, v167\n"
      "v_exp_f32 v112, v168\n"
      "v_cvt_pk_bf16_f32 v211, v114, v115\n"
      "v_exp_f32 v113, v169\n"
      "v_or3_b32 v116, v116, v210, v211\n"
      "v_exp_f32 v114, v170\n"
      "v_cvt_pk_bf16_f32 v212, v112, v113\n"
      "v_exp_f32 v115, v171\n"
      "v_exp_f32 v112, v172\n"
      "v_cvt_pk_bf16_f32 v213, v114, v115\n"
      "v_exp_f32 v113, v173\n"
      "v_or3_b32 v116, v116, v212, v213\n"
      "s_waitcnt lgkmcnt(0)\n"
      "v_cvt_pk_bf16_f32 v214, v112, v113\n"
      "s_sub_u32 s63, %[n1], 1\n"
      "s_branch ENTRY_%=\n"
      "LOOP1_%=:\n"
      "s_waitcnt vmcnt(12) lgkmcnt(0)\n"
      "s_sub_u32 s67, s68, s90\n"
      "s_cmp_lt_i32 s67, 0\n"
      "s_cbranch_scc1 GW0_%=\n"
      "GX0_%=:\n"
      "s_barrier\n"
      "v_mfma_f32_16x16x32_bf16 v[40:43], a[96:99], v[216:219], v[40:43]\n"
      "s_memrealtime s[68:69]\n"
      "buffer_load_dword v250, v251, s[28:31], 0 offen sc1\n"
      "ds_read_b128 a[64:67], v242 offset:0\n"
      "v_exp_f32 v114, v166\n"
      "v_mfma_f32_16x16x32_bf16 v[44:47], a[96:99], v[220:223], v[44:47]\n"
      "s_add_u32 m0, s77, 16384\n"
      "ds_read_b128 a[68:71], v242 offset:4096\n"
      "ds_read_b128 a[72:75], v243 offset:0\n"
      "v_mfma_f32_16x16x32_bf16 v[60:63], a[100:103], v[220:223], v[60:63]\n"
      "buffer_load_dwordx4 v249, s[24:27], s61 offen nt lds\n"
      "v_exp_f32 v115, v167\n"
      "v_mfma_f32_16x16x32_bf16 v[56:59], a[100:103], v[216:219], v[56:59]\n"
      "s_add_u32 m0, s77, 17408\n"
      "ds_read_b128 a[76:79], v243 offset:4096\n"
      "v_cvt_pk_bf16_f32 v211, v114, v115\n"
      "v_mfma_f32_16x16x32_bf16 a[136:139], a[104:107], v[216:219], a[136:139]\n"
      "buffer_load_dwordx4 v121, s[24:27], s61 offen nt lds\n"
      "v_exp_f32 v112, v168\n"
      "v_mfma_f32_16x16x32_bf16 a[140:143], a[104:107], v[220:223], a[140:143]\n"
      "s_add_u32 m0, s77, 24576\n"
      "ds_read_b128 a[80:83], v242 offset:1024\n"
      "v_or3_b32 v116, v116, v210, v211\n"
      "v_mfma_f32_16x16x32_bf16 a[156:159], a[108:111], v[220:223], a[156:159]\n"
      "buffer_load_dwordx4 v122, s[24:27], s61 offen nt lds\n"
      "v_exp_f32 v113, v169\n"
      "v_mfma_f32_16x16x32_bf16 a[152:155], a[108:111], v[216:219], a[152:155]\n"
      "s_add_u32 m0, s77, 25600\n"
      "ds_read_b128 a[84:87], v242 offset:5120\n"
      "v_cvt_pk_bf16_f32 v212, v112, v113\n"
      "v_mfma_f32_16x16x32_bf16 v[72:75], a[112:115], v[216:219], v[72:75]\n"
      "s_add_u32 s77, s77, 0x8000\n"
      "buffer_load_dwordx4 v123, s[24:27], s61 offen nt lds\n"
      "v_exp_f32 v114, v170\n"
      "v_mfma_f32_16x16x32_bf16 v[76:79], a[112:115], v[220:223], v[76:79]\n"
      "s_and_b32 s77, s77, 0x1ffff\n"
      "ds_read_b128 a[88:91], v243 offset:1024\n"
      "ds_read_b128 a[92:95], v243 offset:5120\n"
      "v_mfma_f32_16x16x32_bf16 v[92:95], a[116:119], v[220:223], v[92:95]\n"
      "s_add_u32 s61, s61, %[inc]\n"
      "v_exp_f32 v115, v171\n"
      "v_mfma_f32_16x16x32_bf16 v[88:91], a[116:119], v[216:219], v[88:91]\n"
      "v_cvt_pk_bf16_f32 v213, v114, v115\n"
      "v_mfma_f32_16x16x32_bf16 a[168:171], a[120:123], v[216:219], a[168:171]\n"
      "v_exp_f32 v112, v172\n"
      "v_mfma_f32_16x16x32_bf16 a[172:175], a[120:123], v[220:223], a[172:175]\n"
      "v_or3_b32 v116, v116, v212, v213\n"
      "v_mfma_f32_16x16x32_bf16 a[188:191], a[124:127], v[220:223], a[188:191]\n"
      "v_exp_f32 v113, v173\n"
      "v_mfma_f32_16x16x32_bf16 a[184:187], a[124:127], v[216:219], a[184:187]\n"
      "s_waitcnt lgkmcnt(0)\n"
      "v_cvt_pk_bf16_f32 v214, v112, v113\n"
      "ENTRY_%=:\n"
      "v_mfma_f32_16x16x32_bf16 v[128:131], a[64:67], a[0:3], v[224:227]\n"
      "s_sub_u32 s67, s68, s66\n"
      "v_exp_f32 v114, v174\n"
      "v_mfma_f32_16x16x32_bf16 v[128:131], a[72:75], a[16:19], v[128:131]\n"
      "s_cmp_gt_i32 s67, s70\n"
      "ds_read_b64_tr_b16 a[96:97], v244 offset:0\n"
      "ds_read_b64_tr_b16 a[98:99], v244 offset:4096\n"
      "v_mfma_f32_16x16x32_bf16 v[128:131], a[80:83], a[32:35], v[128:131]\n"
      "s_cselect_b32 s69, s86, 0\n"
      "v_exp_f32 v115, v175\n"
      "v_mfma_f32_16x16x32_bf16 v[128:131], a[88:91], a[48:51], v[128:131]\n"
      "s_add_u32 s66, s66, s69\n"
      "ds_read_b64_tr_b16 a[100:101], v245 offset:0\n"
      "v_cvt_pk_bf16_f32 v215, v114, v115\n"
      "v_mfma_f32_16x16x32_bf16 v[132:135], a[92:95], a[48:51], v[224:227]\n"
      "s_add_u32 s67, s67, s101\n"
      "v_exp_f32 v112, v176\n"
      "v_mfma_f32_16x16x32_bf16 v[132:135], a[84:87], a[32:35], v[132:135]\n"
      "s_min_i32 s70, s70, s67\n"
      "ds_read_b64_tr_b16 a[102:103], v245 offset:4096\n"
      "v_or3_b32 v116, v116, v214, v215\n"
      "v_mfma_f32_16x16x32_bf16 v[132:135], a[76:79], a[16:19], v[132:135]\n"
      "s_max_i32 s70, s70, 85\n"
      "v_exp_f32 v113, v177\n"
      "v_mfma_f32_16x16x32_bf16 v[132:135], a[68:71], a[0:3], v[132:135]\n"
      "s_add_u32 s66, s66, s86\n"
      "ds_read_b64_tr_b16 a[104:105], v246 offset:0\n"
      "v_cvt_pk_bf16_f32 v216, v112, v113\n"
      "v_mfma_f32_16x16x32_bf16 v[140:143], a[68:71], a[4:7], v[228:231]\n"
      "s_and_b32 s76, s80, 31\n"
      "v_exp_f32 v114, v178\n"
      "v_mfma_f32_16x16x32_bf16 v[140:143], a[76:79], a[20:23], v[140:143]\n"
      "s_lshr_b64 s[80:81], s[80:81], 5\n"
      "ds_read_b64_tr_b16 a[106:107], v246 offset:4096\n"
      "ds_read_b64_tr_b16 a[108:109], v247 offset:0\n"
      "v_mfma_f32_16x16x32_bf16 v[140:143], a[84:87], a[36:39], v[140:143]\n"
      "s_add_u32 s66, s66, s76\n"
      "v_exp_f32 v115, v179\n"
      "v_mfma_f32_16x16x32_bf16 v[140:143], a[92:95], a[52:55], v[140:143]\n"
      "s_sub_u32 s67, s68, s82\n"
      "ds_read_b64_tr_b16 a[110:111], v247 offset:4096\n"
      "v_cvt_pk_bf16_f32 v217, v114, v115\n"
      "v_mfma_f32_16x16x32_bf16 v[136:139], a[88:91], a[52:55], v[228:231]\n"
      "s_min_i32 s76, s67, s83\n"
      "v_exp_f32 v112, v180\n"
      "v_mfma_f32_16x16x32_bf16 v[136:139], a[80:83], a[36:39], v[136:139]\n"
      "s_mov_b32 s83, s67\n"
      "ds_read_b64_tr_b16 a[112:113], v244 offset:1024\n"
      "v_or3_b32 v116, v116, v216, v217\n"
      "v_mfma_f32_16x16x32_bf16 v[136:139], a[72:75], a[20:23], v[136:139]\n"
      "s_sub_u32 s76, s76, 36\n"
      "v_exp_f32 v113, v181\n"
      "v_mfma_f32_16x16x32_bf16 v[136:139], a[64:67], a[4:7], v[136:139]\n"
      "s_min_i32 s76, s76, 148\n"
      "ds_read_b64_tr_b16 a[114:115], v244 offset:5120\n"
      "v_cvt_pk_bf16_f32 v218, v112, v113\n"
      "v_mfma_f32_16x16x32_bf16 v[144:147], a[64:67], a[8:11], v[232:235]\n"
      "s_max_i32 s76, s76, 70\n"
      "v_exp_f32 v114, v182\n"
      "v_mfma_f32_16x16x32_bf16 v[144:147], a[72:75], a[24:27], v[144:147]\n"
      "s_mov_b32 s82, s68\n"
      "ds_read_b64_tr_b16 a[116:117], v245 offset:1024\n"
      "ds_read_b64_tr_b16 a[118:119], v245 offset:5120\n"
      "v_mfma_f32_16x16x32_bf16 v[144:147], a[80:83], a[40:43], v[144:147]\n"
      "s_add_u32 s65, s82, s76\n"
      "v_exp_f32 v115, v183\n"
      "v_mfma_f32_16x16x32_bf16 v[144:147], a[88:91], a[56:59], v[144:147]\n"
      "s_sub_u32 s93, s93, 1\n"
      "ds_read_b64_tr_b16 a[120:121], v246 offset:1024\n"
      "v_cvt_pk_bf16_f32 v219, v114, v115\n"
      "v_mfma_f32_16x16x32_bf16 v[148:151], a[92:95], a[56:59], v[232:235]\n"
      "s_cmp_le_i32 s93, 14\n"
      "v_exp_f32 v112, v184\n"
      "v_mfma_f32_16x16x32_bf16 v[148:151], a[84:87], a[40:43], v[148:151]\n"
      "s_cselect_b32 s86, 107, s86\n"
      "ds_read_b64_tr_b16 a[122:123], v246 offset:5120\n"
      "v_or3_b32 v116, v116, v218, v219\n"
      "v_mfma_f32_16x16x32_bf16 v[148:151], a[76:79], a[24:27], v[148:151]\n"
      "s_sub_u32 s67, s65, s66\n"
      "v_exp_f32 v113, v185\n"
      "v_mfma_f32_16x16x32_bf16 v[148:151], a[68:71], a[8:11], v[148:151]\n"
      "s_cmp_lt_i32 s67, 0\n"
      "ds_read_b64_tr_b16 a[124:125], v247 offset:1024\n"
      "v_cvt_pk_bf16_f32 v220, v112, v113\n"
      "v_mfma_f32_16x16x32_bf16 v[156:159], a[68:71], a[12:15], v[236:239]\n"
      "s_cselect_b32 s88, s66, s65\n"
      "v_exp_f32 v114, v186\n"
      "v_mfma_f32_16x16x32_bf16 v[156:159], a[76:79], a[28:31], v[156:159]\n"
      "s_add_u32 s90, s88, s91\n"
      "ds_read_b64_tr_b16 a[126:127], v247 offset:5120\n"
      "v_mfma_f32_16x16x32_bf16 v[156:159], a[84:87], a[44:47], v[156:159]\n"
      "v_exp_f32 v115, v187\n"
      "v_mfma_f32_16x16x32_bf16 v[156:159], a[92:95], a[60:63], v[156:159]\n"
      "v_cvt_pk_bf16_f32 v221, v114, v115\n"
      "v_mfma_f32_16x16x32_bf16 v[152:155], a[88:91], a[60:63], v[236:239]\n"
      "v_exp_f32 v112, v188\n"
      "v_mfma_f32_16x16x32_bf16 v[152:155], a[80:83], a[44:47], v[152:155]\n"
      "v_or3_b32 v116, v116, v220, v221\n"
      "v_mfma_f32_16x16x32_bf16 v[152:155], a[72:75], a[28:31], v[152:155]\n"
      "v_exp_f32 v113, v189\n"
      "v_mfma_f32_16x16x32_bf16 v[152:155], a[64:67], a[12:15], v[152:155]\n"
      "s_waitcnt lgkmcnt(10)\n"
      "v_cvt_pk_bf16_f32 v222, v112, v113\n"
      "v_mfma_f32_16x16x32_bf16 v[32:35], a[96:99], v[192:195], v[32:35]\n"
      "v_exp_f32 v114, v190\n"
      "v_mfma_f32_16x16x32_bf16 v[48:51], a[100:103], v[192:195], v[48:51]\n"
      "v_mfma_f32_16x16x32_bf16 a[128:131], a[104:107], v[192:195], a[128:131]\n"
      "s_waitcnt lgkmcnt(6)\n"
      "v_exp_f32 v115, v191\n"
      "v_mfma_f32_16x16x32_bf16 a[144:147], a[108:111], v[192:195], a[144:147]\n"
      "v_cvt_pk_bf16_f32 v223, v114, v115\n"
      "v_mfma_f32_16x16x32_bf16 v[64:67], a[112:115], v[192:195], v[64:67]\n"
      "s_waitcnt lgkmcnt(2)\n"
      "v_mfma_f32_16x16x32_bf16 v[80:83], a[116:119], v[192:195], v[80:83]\n"
      "v_or3_b32 v116, v116, v222, v223\n"
      "v_mfma_f32_16x16x32_bf16 a[160:163], a[120:123], v[192:195], a[160:163]\n"
      "s_mov_b32 s64, 0\n"
      "s_waitcnt lgkmcnt(0)\n"
      "v_and_b32 v116, 0x40004000, v116\n"
      "v_mfma_f32_16x16x32_bf16 a[176:179], a[124:127], v[192:195], a[176:179]\n"
      "v_cmp_ne_u32 vcc, 0, v116\n"
      "s_cbranch_vccnz FB1_%=\n"
      "RET1_0_%=:\n"
      "v_mfma_f32_16x16x32_bf16 a[180:183], a[124:127], v[196:199], a[180:183]\n"
      "v_exp_f32 v112, v128\n"
      "s_barrier\n"
      "v_mfma_f32_16x16x32_bf16 a[164:167], a[120:123], v[196:199], a[164:167]\n"
      "v_mfma_f32_16x16x32_bf16 a[192:195], a[196:199], v[208:211], a[192:195]\n"
      "s_add_u32 m0, s62, 0\n"
      "v_exp_f32 v113, v129\n"
      "v_mfma_f32_16x16x32_bf16 v[84:87], a[116:119], v[196:199], v[84:87]\n"
      "buffer_load_dwordx4 v248, s[20:23], s60 offen nt lds\n"
      "v_cvt_pk_bf16_f32 v192, v112, v113\n"
      "v_mfma_f32_16x16x32_bf16 v[68:71], a[112:115], v[196:199], v[68:71]\n"
      "s_add_u32 m0, s62, 896\n"
      "v_exp_f32 v114, v130\n"
      "v_mfma_f32_16x16x32_bf16 a[192:195], a[200:203], v[212:215], a[192:195]\n"
      "buffer_load_dwordx4 v248, s[20:23], s60 offen offset:128 nt lds\n"
      "v_mfma_f32_16x16x32_bf16 a[148:151], a[108:111], v[196:199], a[148:151]\n"
      "s_add_u32 m0, s62, 8192\n"
      "v_exp_f32 v115, v131\n"
      "v_mfma_f32_16x16x32_bf16 a[132:135], a[104:107], v[196:199], a[132:135]\n"
      "s_memrealtime s[68:69]\n"
      "buffer_load_dwordx4 v120, s[20:23], s60 offen nt lds\n"
      "v_cvt_pk_bf16_f32 v193, v114, v115\n"
      "v_mfma_f32_16x16x32_bf16 a[192:195], a[204:207], v[216:219], a[192:195]\n"
      "s_add_u32 m0, s62, 9088\n"
      "v_exp_f32 v112, v132\n"
      "v_mfma_f32_16x16x32_bf16 v[52:55], a[100:103], v[196:199], v[52:55]\n"
      "s_add_u32 s62, s62, 0x8000\n"
      "buffer_load_dwordx4 v120, s[20:23], s60 offen offset:128 nt lds\n"
      "v_or_b32 v116, v192, v193\n"
      "v_mfma_f32_16x16x32_bf16 v[36:39], a[96:99], v[196:199], v[36:39]\n"
      "s_and_b32 s62, s62, 0x1ffff\n"
      "v_exp_f32 v113, v133\n"
      "v_mfma_f32_16x16x32_bf16 a[192:195], a[208:211], v[220:223], a[192:195]\n"
      "s_add_u32 s60, s60, s78\n"
      "v_cvt_pk_bf16_f32 v194, v112, v113\n"
      "s_waitcnt vmcnt(12) lgkmcnt(0)\n"
      "s_sub_u32 s67, s68, s90\n"
      "s_cmp_lt_i32 s67, 0\n"
      "s_cbranch_scc1 GW1_%=\n"
      "GX1_%=:\n"
      "s_barrier\n"
      "v_mfma_f32_16x16x32_bf16 v[40:43], a[96:99], v[200:203], v[40:43]\n"
      "s_memrealtime s[68:69]\n"
      "ds_read_b128 a[64:67], v242 offset:32768\n"
      "v_exp_f32 v114, v134\n"
      "v_mfma_f32_16x16x32_bf16 v[44:47], a[96:99], v[204:207], v[44:47]\n"
      "s_add_u32 m0, s77, 16384\n"
      "ds_read_b128 a[68:71], v242 offset:36864\n"
      "ds_read_b128 a[72:75], v243 offset:32768\n"
      "v_mfma_f32_16x16x32_bf16 v[60:63], a[100:103], v[204:207], v[60:63]\n"
      "buffer_load_dwordx4 v249, s[24:27], s61 offen nt lds\n"
      "v_exp_f32 v115, v135\n"
      "v_mfma_f32_16x16x32_bf16 v[56:59], a[100:103], v[200:203], v[56:59]\n"
      "s_add_u32 m0, s77, 17408\n"
      "ds_read_b128 a[76:79], v243 offset:36864\n"
      "v_cvt_pk_bf16_f32 v195, v114, v115\n"
      "v_mfma_f32_16x16x32_bf16 a[136:139], a[104:107], v[200:203], a[136:139]\n"
      "buffer_load_dwordx4 v121, s[24:27], s61 offen nt lds\n"
      "v_exp_f32 v112, v136\n"
      "v_mfma_f32_16x16x32_bf16 a[140:143], a[104:107], v[204:207], a[140:143]\n"
      "s_add_u32 m0, s77, 24576\n"
      "ds_read_b128 a[80:83], v242 offset:33792\n"
      "v_or3_b32 v116, v116, v194, v195\n"
      "v_mfma_f32_16x16x32_bf16 a[156:159], a[108:111], v[204:207], a[156:159]\n"
      "buffer_load_dwordx4 v122, s[24:27], s61 offen nt lds\n"
      "v_exp_f32 v113, v137\n"
      "v_mfma_f32_16x16x32_bf16 a[152:155], a[108:111], v[200:203], a[152:155]\n"
      "s_add_u32 m0, s77, 25600\n"
      "ds_read_b128 a[84:87], v242 offset:37888\n"
      "v_cvt_pk_bf16_f32 v196, v112, v113\n"
      "v_mfma_f32_16x16x32_bf16 v[72:75], a[112:115], v[200:203], v[72:75]\n"
      "s_add_u32 s77, s77, 0x8000\n"
      "buffer_load_dwordx4 v123, s[24:27], s61 offen nt lds\n"
      "v_exp_f32 v114, v138\n"
      "v_mfma_f32_16x16x32_bf16 v[76:79], a[112:115], v[204:207], v[76:79]\n"
      "s_and_b32 s77, s77, 0x1ffff\n"
      "ds_read_b128 a[88:91], v243 offset:33792\n"
      "ds_read_b128 a[92:95], v243 offset:37888\n"
      "v_mfma_f32_16x16x32_bf16 v[92:95], a[116:119], v[204:207], v[92:95]\n"
      "s_add_u32 s61, s61, s78\n"
      "v_exp_f32 v115, v139\n"
      "v_mfma_f32_16x16x32_bf16 v[88:91], a[116:119], v[200:203], v[88:91]\n"
      "v_cvt_pk_bf16_f32 v197, v114, v115\n"
      "v_mfma_f32_16x16x32_bf16 a[168:171], a[120:123], v[200:203], a[168:171]\n"
      "v_exp_f32 v112, v140\n"
      "v_mfma_f32_16x16x32_bf16 a[172:175], a[120:123], v[204:207], a[172:175]\n"
      "v_or3_b32 v116, v116, v196, v197\n"
      "v_mfma_f32_16x16x32_bf16 a[188:191], a[124:127], v[204:207], a[188:191]\n"
      "v_exp_f32 v113, v141\n"
      "v_mfma_f32_16x16x32_bf16 a[184:187], a[124:127], v[200:203], a[184:187]\n"
      "s_waitcnt lgkmcnt(0)\n"
      "v_cvt_pk_bf16_f32 v198, v112, v113\n"
      "v_mfma_f32_16x16x32_bf16 v[160:163], a[64:67], a[0:3], v[224:227]\n"
      "s_sub_u32 s67, s68, s66\n"
      "v_exp_f32 v114, v142\n"
      "v_mfma_f32_16x16x32_bf16 v[160:163], a[72:75], a[16:19], v[160:163]\n"
      "s_cmp_gt_i32 s67, s70\n"
      "ds_read_b64_tr_b16 a[96:97], v244 offset:32768\n"
      "ds_read_b64_tr_b16 a[98:99], v244 offset:36864\n"
      "v_mfma_f32_16x16x32_bf16 v[160:163], a[80:83], a[32:35], v[160:163]\n"
      "s_cselect_b32 s69, s86, 0\n"
      "v_exp_f32 v115, v143\n"
      "v_mfma_f32_16x16x32_bf16 v[160:163], a[88:91], a[48:51], v[160:163]\n"
      "s_add_u32 s66, s66, s69\n"
      "ds_read_b64_tr_b16 a[100:101], v245 offset:32768\n"
      "v_cvt_pk_bf16_f32 v199, v114, v115\n"
      "v_mfma_f32_16x16x32_bf16 v[164:167], a[92:95], a[48:51], v[224:227]\n"
      "s_add_u32 s67, s67, s101\n"
      "v_exp_f32 v112, v144\n"
      "v_mfma_f32_16x16x32_bf16 v[164:167], a[84:87], a[32:35], v[164:167]\n"
      "s_min_i32 s70, s70, s67\n"
      "ds_read_b64_tr_b16 a[102:103], v245 offset:36864\n"
      "v_or3_b32 v116, v116, v198, v199\n"
      "v_mfma_f32_16x16x32_bf16 v[164:167], a[76:79], a[16:19], v[164:167]\n"
      "s_max_i32 s70, s70, 85\n"
      "v_exp_f32 v113, v145\n"
      "v_mfma_f32_16x16x32_bf16 v[164:167], a[68:71], a[0:3], v[164:167]\n"
      "s_add_u32 s66, s66, s86\n"
      "ds_read_b64_tr_b16 a[104:105], v246 offset:32768\n"
      "v_cvt_pk_bf16_f32 v200, v112, v113\n"
      "v_mfma_f32_16x16x32_bf16 v[172:175], a[68:71], a[4:7], v[228:231]\n"
      "s_and_b32 s76, s80, 31\n"
      "v_exp_f32 v114, v146\n"
      "v_mfma_f32_16x16x32_bf16 v[172:175], a[76:79], a[20:23], v[172:175]\n"
      "s_lshr_b64 s[80:81], s[80:81], 5\n"
      "ds_read_b64_tr_b16 a[106:107], v246 offset:36864\n"
      "ds_read_b64_tr_b16 a[108:109], v247 offset:32768\n"
      "v_mfma_f32_16x16x32_bf16 v[172:175], a[84:87], a[36:39], v[172:175]\n"
      "s_add_u32 s66, s66, s76\n"
      "v_exp_f32 v115, v147\n"
      "v_mfma_f32_16x16x32_bf16 v[172:175], a[92:95], a[52:55], v[172:175]\n"
      "s_sub_u32 s67, s68, s82\n"
      "ds_read_b64_tr_b16 a[110:111], v247 offset:36864\n"
      "v_cvt_pk_bf16_f32 v201, v114, v115\n"
      "v_mfma_f32_16x16x32_bf16 v[168:171], a[88:91], a[52:55], v[228:231]\n"
      "s_min_i32 s76, s67, s83\n"
      "v_exp_f32 v112, v148\n"
      "v_mfma_f32_16x16x32_bf16 v[168:171], a[80:83], a[36:39], v[168:171]\n"
      "s_mov_b32 s83, s67\n"
      "ds_read_b64_tr_b16 a[112:113], v244 offset:33792\n"
      "v_or3_b32 v116, v116, v200, v201\n"
      "v_mfma_f32_16x16x32_bf16 v[168:171], a[72:75], a[20:23], v[168:171]\n"
      "s_sub_u32 s76, s76, 36\n"
      "v_exp_f32 v113, v149\n"
      "v_mfma_f32_16x16x32_bf16 v[168:171], a[64:67], a[4:7], v[168:171]\n"
      "s_min_i32 s76, s76, 148\n"
      "ds_read_b64_tr_b16 a[114:115], v244 offset:37888\n"
      "v_cvt_pk_bf16_f32 v202, v112, v113\n"
      "v_mfma_f32_16x16x32_bf16 v[176:179], a[64:67], a[8:11], v[232:235]\n"
      "s_max_i32 s76, s76, 70\n"
      "v_exp_f32 v114, v150\n"
      "v_mfma_f32_16x16x32_bf16 v[176:179], a[72:75], a[24:27], v[176:179]\n"
      "s_mov_b32 s82, s68\n"
      "ds_read_b64_tr_b16 a[116:117], v245 offset:33792\n"
      "ds_read_b64_tr_b16 a[118:119], v245 offset:37888\n"
      "v_mfma_f32_16x16x32_bf16 v[176:179], a[80:83], a[40:43], v[176:179]\n"
      "s_add_u32 s65, s82, s76\n"
      "v_exp_f32 v115, v151\n"
      "v_mfma_f32_16x16x32_bf16 v[176:179], a[88:91], a[56:59], v[176:179]\n"
      "s_sub_u32 s93, s93, 1\n"
      "ds_read_b64_tr_b16 a[120:121], v246 offset:33792\n"
      "v_cvt_pk_bf16_f32 v203, v114, v115\n"
      "v_mfma_f32_16x16x32_bf16 v[180:183], a[92:95], a[56:59], v[232:235]\n"
      "s_cmp_le_i32 s93, 14\n"
      "v_exp_f32 v112, v152\n"
      "v_mfma_f32_16x16x32_bf16 v[180:183], a[84:87], a[40:43], v[180:183]\n"
      "s_cselect_b32 s86, 107, s86\n"
      "ds_read_b64_tr_b16 a[122:123], v246 offset:37888\n"
      "v_or3_b32 v116, v116, v202, v203\n"
      "v_mfma_f32_16x16x32_bf16 v[180:183], a[76:79], a[24:27], v[180:183]\n"
      "s_sub_u32 s67, s65, s66\n"
      "v_exp_f32 v113, v153\n"
      "v_mfma_f32_16x16x32_bf16 v[180:183], a[68:71], a[8:11], v[180:183]\n"
      "s_cmp_lt_i32 s67, 0\n"
      "ds_read_b64_tr_b16 a[124:125], v247 offset:33792\n"
      "v_cvt_pk_bf16_f32 v204, v112, v113\n"
      "v_mfma_f32_16x16x32_bf16 v[188:191], a[68:71], a[12:15], v[236:239]\n"
      "s_cselect_b32 s88, s66, s65\n"
      "v_exp_f32 v114, v154\n"
      "v_mfma_f32_16x16x32_bf16 v[188:191], a[76:79], a[28:31], v[188:191]\n"
      "s_add_u32 s90, s88, s91\n"
      "ds_read_b64_tr_b16 a[126:127], v247 offset:37888\n"
      "v_mfma_f32_16x16x32_bf16 v[188:191], a[84:87], a[44:47], v[188:191]\n"
      "v_exp_f32 v115, v155\n"
      "v_mfma_f32_16x16x32_bf16 v[188:191], a[92:95], a[60:63], v[188:191]\n"
      "v_cvt_pk_bf16_f32 v205, v114, v115\n"
      "v_mfma_f32_16x16x32_bf16 v[184:187], a[88:91], a[60:63], v[236:239]\n"
      "v_exp_f32 v112, v156\n"
      "v_mfma_f32_16x16x32_bf16 v[184:187], a[80:83], a[44:47], v[184:187]\n"
      "v_or3_b32 v116, v116, v204, v205\n"
      "v_mfma_f32_16x16x32_bf16 v[184:187], a[72:75], a[28:31], v[184:187]\n"
      "v_exp_f32 v113, v157\n"
      "v_mfma_f32_16x16x32_bf16 v[184:187], a[64:67], a[12:15], v[184:187]\n"
      "s_waitcnt lgkmcnt(10)\n"
      "v_cvt_pk_bf16_f32 v206, v112, v113\n"
      "v_mfma_f32_16x16x32_bf16 v[32:35], a[96:99], v[208:211], v[32:35]\n"
      "v_exp_f32 v114, v158\n"
      "v_mfma_f32_16x16x32_bf16 v[48:51], a[100:103], v[208:211], v[48:51]\n"
      "v_mfma_f32_16x16x32_bf16 a[128:131], a[104:107], v[208:211], a[128:131]\n"
      "s_waitcnt lgkmcnt(6)\n"
      "v_exp_f32 v115, v159\n"
      "v_mfma_f32_16x16x32_bf16 a[144:147], a[108:111], v[208:211], a[144:147]\n"
      "v_cvt_pk_bf16_f32 v207, v114, v115\n"
      "v_mfma_f32_16x16x32_bf16 v[64:67], a[112:115], v[208:211], v[64:67]\n"
      "s_waitcnt lgkmcnt(2)\n"
      "v_mfma_f32_16x16x32_bf16 v[80:83], a[116:119], v[208:211], v[80:83]\n"
      "v_or3_b32 v116, v116, v206, v207\n"
      "v_mfma_f32_16x16x32_bf16 a[160:163], a[120:123], v[208:211], a[160:163]\n"
      "s_mov_b32 s64, 0\n"
      "s_waitcnt lgkmcnt(0)\n"
      "v_and_b32 v116, 0x40004000, v116\n"
      "v_mfma_f32_16x16x32_bf16 a[176:179], a[124:127], v[208:211], a[176:179]\n"
      "v_cmp_ne_u32 vcc, 0, v116\n"
      "s_cbranch_vccnz FB0_%=\n"
      "RET0_0_%=:\n"
      "v_mfma_f32_16x16x32_bf16 a[180:183], a[124:127], v[212:215], a[180:183]\n"
      "v_exp_f32 v112, v160\n"
      "s_barrier\n"
      "v_mfma_f32_16x16x32_bf16 a[164:167], a[120:123], v[212:215], a[164:167]\n"
      "v_xor_b32 v242, 0x10000, v242\n"
      "v_mfma_f32_16x16x32_bf16 a[192:195], a[196:199], v[192:195], a[192:195]\n"
      "s_add_u32 m0, s62, 0\n"
      "v_exp_f32 v113, v161\n"
      "v_mfma_f32_16x16x32_bf16 v[84:87], a[116:119], v[212:215], v[84:87]\n"
      "buffer_load_dwordx4 v248, s[20:23], s60 offen nt lds\n"
      "v_cvt_pk_bf16_f32 v208, v112, v113\n"
      "v_xor_b32 v243, 0x10000, v243\n"
      "v_mfma_f32_16x16x32_bf16 v[68:71], a[112:115], v[212:215], v[68:71]\n"
      "s_add_u32 m0, s62, 896\n"
      "v_exp_f32 v114, v162\n"
      "v_mfma_f32_16x16x32_bf16 a[192:195], a[200:203], v[196:199], a[192:195]\n"
      "buffer_load_dwordx4 v248, s[20:23], s60 offen offset:128 nt lds\n"
      "v_xor_b32 v244, 0x10000, v244\n"
      "v_mfma_f32_16x16x32_bf16 a[148:151], a[108:111], v[212:215], a[148:151]\n"
      "s_add_u32 m0, s62, 8192\n"
      "v_exp_f32 v115, v163\n"
      "v_mfma_f32_16x16x32_bf16 a[132:135], a[104:107], v[212:215], a[132:135]\n"
      "s_memrealtime s[68:69]\n"
      "buffer_load_dwordx4 v120, s[20:23], s60 offen nt lds\n"
      "v_cvt_pk_bf16_f32 v209, v114, v115\n"
      "v_xor_b32 v245, 0x10000, v245\n"
      "v_mfma_f32_16x16x32_bf16 a[192:195], a[204:207], v[200:203], a[192:195]\n"
      "s_add_u32 m0, s62, 9088\n"
      "v_exp_f32 v112, v164\n"
      "v_mfma_f32_16x16x32_bf16 v[52:55], a[100:103], v[212:215], v[52:55]\n"
      "s_add_u32 s62, s62, 0x8000\n"
      "buffer_load_dwordx4 v120, s[20:23], s60 offen offset:128 nt lds\n"
      "v_or_b32 v116, v208, v209\n"
      "v_xor_b32 v246, 0x10000, v246\n"
      "v_mfma_f32_16x16x32_bf16 v[36:39], a[96:99], v[212:215], v[36:39]\n"
      "s_and_b32 s62, s62, 0x1ffff\n"
      "s_add_u32 s60, s60, %[inc]\n"
      "v_exp_f32 v113, v165\n"
      "v_mfma_f32_16x16x32_bf16 a[192:195], a[208:211], v[204:207], a[192:195]\n"
      "s_sub_u32 s63, s63, 1\n"
      "v_cvt_pk_bf16_f32 v210, v112, v113\n"
      "v_xor_b32 v247, 0x10000, v247\n"
      "s_cbranch_scc0 LOOP1_%=\n"
      "v_readfirstlane_b32 s72, v240\n"
      "v_readfirstlane_b32 s79, v241\n"
      "s_waitcnt vmcnt(12) lgkmcnt(0)\n"
      "s_sub_u32 s67, s68, s90\n"
      "s_cmp_lt_i32 s67, 0\n"
      "s_cbranch_scc1 GW2_%=\n"
      "GX2_%=:\n"
      "s_barrier\n"
      "v_mfma_f32_16x16x32_bf16 v[40:43], a[96:99], v[216:219], v[40:43]\n"
      "s_memrealtime s[68:69]\n"
      "buffer_load_dword v250, v251, s[28:31], 0 offen sc1\n"
      "ds_read_b128 a[64:67], v242 offset:0\n"
      "v_exp_f32 v114, v166\n"
      "v_mfma_f32_16x16x32_bf16 v[44:47], a[96:99], v[220:223], v[44:47]\n"
      "s_add_u32 m0, s77, 16384\n"
      "ds_read_b128 a[68:71], v242 offset:4096\n"
      "ds_read_b128 a[72:75], v243 offset:0\n"
      "v_mfma_f32_16x16x32_bf16 v[60:63], a[100:103], v[220:223], v[60:63]\n"
      "buffer_load_dwordx4 v249, s[24:27], s61 offen nt lds\n"
      "v_exp_f32 v115, v167\n"
      "v_mfma_f32_16x16x32_bf16 v[56:59], a[100:103], v[216:219], v[56:59]\n"
      "s_add_u32 m0, s77, 17408\n"
      "ds_read_b128 a[76:79], v243 offset:4096\n"
      "v_cvt_pk_bf16_f32 v211, v114, v115\n"
      "v_mfma_f32_16x16x32_bf16 a[136:139], a[104:107], v[216:219], a[136:139]\n"
      "buffer_load_dwordx4 v121, s[24:27], s61 offen nt lds\n"
      "v_exp_f32 v112, v168\n"
      "v_mfma_f32_16x16x32_bf16 a[140:143], a[104:107], v[220:223], a[140:143]\n"
      "s_add_u32 m0, s77, 24576\n"
      "ds_read_b128 a[80:83], v242 offset:1024\n"
      "v_or3_b32 v116, v116, v210, v211\n"
      "v_mfma_f32_16x16x32_bf16 a[156:159], a[108:111], v[220:223], a[156:159]\n"
      "buffer_load_dwordx4 v122, s[24:27], s61 offen nt lds\n"
      "v_exp_f32 v113, v169\n"
      "v_mfma_f32_16x16x32_bf16 a[152:155], a[108:111], v[216:219], a[152:155]\n"
      "s_add_u32 m0, s77, 25600\n"
      "ds_read_b128 a[84:87], v242 offset:5120\n"
      "v_cvt_pk_bf16_f32 v212, v112, v113\n"
      "v_mfma_f32_16x16x32_bf16 v[72:75], a[112:115], v[216:219], v[72:75]\n"
      "s_add_u32 s77, s77, 0x8000\n"
      "buffer_load_dwordx4 v123, s[24:27], s61 offen nt lds\n"
      "v_exp_f32 v114, v170\n"
      "v_mfma_f32_16x16x32_bf16 v[76:79], a[112:115], v[220:223], v[76:79]\n"
      "s_and_b32 s77, s77, 0x1ffff\n"
      "ds_read_b128 a[88:91], v243 offset:1024\n"
      "ds_read_b128 a[92:95], v243 offset:5120\n"
      "v_mfma_f32_16x16x32_bf16 v[92:95], a[116:119], v[220:223], v[92:95]\n"
      "s_add_u32 s61, s61, %[inc]\n"
      "v_exp_f32 v115, v171\n"
      "v_mfma_f32_16x16x32_bf16 v[88:91], a[116:119], v[216:219], v[88:91]\n"
      "v_cvt_pk_bf16_f32 v213, v114, v115\n"
      "v_mfma_f32_16x16x32_bf16 a[168:171], a[120:123], v[216:219], a[168:171]\n"
      "v_exp_f32 v112, v172\n"
      "v_mfma_f32_16x16x32_bf16 a[172:175], a[120:123], v[220:223], a[172:175]\n"
      "v_or3_b32 v116, v116, v212, v213\n"
      "v_mfma_f32_16x16x32_bf16 a[188:191], a[124:127], v[220:223], a[188:191]\n"
      "v_exp_f32 v113, v173\n"
      "v_mfma_f32_16x16x32_bf16 a[184:187], a[124:127], v[216:219], a[184:187]\n"
      "s_waitcnt lgkmcnt(0)\n"
      "v_cvt_pk_bf16_f32 v214, v112, v113\n"
      "v_mfma_f32_16x16x32_bf16 v[128:131], a[64:67], a[0:3], v[224:227]\n"
      "s_sub_u32 s67, s68, s66\n"
      "v_exp_f32 v114, v174\n"
      "v_mfma_f32_16x16x32_bf16 v[128:131], a[72:75], a[16:19], v[128:131]\n"
      "s_cmp_gt_i32 s67, s70\n"
      "ds_read_b64_tr_b16 a[96:97], v244 offset:0\n"
      "ds_read_b64_tr_b16 a[98:99], v244 offset:4096\n"
      "v_mfma_f32_16x16x32_bf16 v[128:131], a[80:83], a[32:35], v[128:131]\n"
      "s_cselect_b32 s69, s86, 0\n"
      "v_exp_f32 v115, v175\n"
      "v_mfma_f32_16x16x32_bf16 v[128:131], a[88:91], a[48:51], v[128:131]\n"
      "s_add_u32 s66, s66, s69\n"
      "ds_read_b64_tr_b16 a[100:101], v245 offset:0\n"
      "v_cvt_pk_bf16_f32 v215, v114, v115\n"
      "v_mfma_f32_16x16x32_bf16 v[132:135], a[92:95], a[48:51], v[224:227]\n"
      "s_add_u32 s67, s67, s101\n"
      "v_exp_f32 v112, v176\n"
      "v_mfma_f32_16x16x32_bf16 v[132:135], a[84:87], a[32:35], v[132:135]\n"
      "s_min_i32 s70, s70, s67\n"
      "ds_read_b64_tr_b16 a[102:103], v245 offset:4096\n"
      "v_or3_b32 v116, v116, v214, v215\n"
      "v_mfma_f32_16x16x32_bf16 v[132:135], a[76:79], a[16:19], v[132:135]\n"
      "s_max_i32 s70, s70, 85\n"
      "v_exp_f32 v113, v177\n"
      "v_mfma_f32_16x16x32_bf16 v[132:135], a[68:71], a[0:3], v[132:135]\n"
      "s_add_u32 s66, s66, s86\n"
      "ds_read_b64_tr_b16 a[104:105], v246 offset:0\n"
      "v_cvt_pk_bf16_f32 v216, v112, v113\n"
      "v_mfma_f32_16x16x32_bf16 v[140:143], a[68:71], a[4:7], v[228:231]\n"
      "s_and_b32 s76, s80, 31\n"
      "v_exp_f32 v114, v178\n"
      "v_mfma_f32_16x16x32_bf16 v[140:143], a[76:79], a[20:23], v[140:143]\n"
      "s_lshr_b64 s[80:81], s[80:81], 5\n"
      "ds_read_b64_tr_b16 a[106:107], v246 offset:4096\n"
      "ds_read_b64_tr_b16 a[108:109], v247 offset:0\n"
      "v_mfma_f32_16x16x32_bf16 v[140:143], a[84:87], a[36:39], v[140:143]\n"
      "s_add_u32 s66, s66, s76\n"
      "v_exp_f32 v115, v179\n"
      "v_mfma_f32_16x16x32_bf16 v[140:143], a[92:95], a[52:55], v[140:143]\n"
      "s_sub_u32 s67, s68, s82\n"
      "ds_read_b64_tr_b16 a[110:111], v247 offset:4096\n"
      "v_cvt_pk_bf16_f32 v217, v114, v115\n"
      "v_mfma_f32_16x16x32_bf16 v[136:139], a[88:91], a[52:55], v[228:231]\n"
      "s_min_i32 s76, s67, s83\n"
      "v_exp_f32 v112, v180\n"
      "v_mfma_f32_16x16x32_bf16 v[136:139], a[80:83], a[36:39], v[136:139]\n"
      "s_mov_b32 s83, s67\n"
      "ds_read_b64_tr_b16 a[112:113], v244 offset:1024\n"
      "v_or3_b32 v116, v116, v216, v217\n"
      "v_mfma_f32_16x16x32_bf16 v[136:139], a[72:75], a[20:23], v[136:139]\n"
      "s_sub_u32 s76, s76, 36\n"
      "v_exp_f32 v113, v181\n"
      "v_mfma_f32_16x16x32_bf16 v[136:139], a[64:67], a[4:7], v[136:139]\n"
      "s_min_i32 s76, s76, 148\n"
      "ds_read_b64_tr_b16 a[114:115], v244 offset:5120\n"
      "v_cvt_pk_bf16_f32 v218, v112, v113\n"
      "v_mfma_f32_16x16x32_bf16 v[144:147], a[64:67], a[8:11], v[232:235]\n"
      "s_max_i32 s76, s76, 70\n"
      "v_exp_f32 v114, v182\n"
      "v_mfma_f32_16x16x32_bf16 v[144:147], a[72:75], a[24:27], v[144:147]\n"
      "s_mov_b32 s82, s68\n"
      "ds_read_b64_tr_b16 a[116:117], v245 offset:1024\n"
      "ds_read_b64_tr_b16 a[118:119], v245 offset:5120\n"
      "v_mfma_f32_16x16x32_bf16 v[144:147], a[80:83], a[40:43], v[144:147]\n"
      "s_lshr_b32 s69, s86, 1\n"
      "v_exp_f32 v115, v183\n"
      "v_mfma_f32_16x16x32_bf16 v[144:147], a[88:91], a[56:59], v[144:147]\n"
      "s_add_u32 s66, s66, s69\n"
      "ds_read_b64_tr_b16 a[120:121], v246 offset:1024\n"
      "v_cvt_pk_bf16_f32 v219, v114, v115\n"
      "v_mfma_f32_16x16x32_bf16 v[148:151], a[92:95], a[56:59], v[232:235]\n"
      "s_add_u32 s82, s82, s69\n"
      "v_exp_f32 v112, v184\n"
      "v_mfma_f32_16x16x32_bf16 v[148:151], a[84:87], a[40:43], v[148:151]\n"
      "s_add_u32 s65, s82, s76\n"
      "ds_read_b64_tr_b16 a[122:123], v246 offset:5120\n"
      "v_or3_b32 v116, v116, v218, v219\n"
      "v_mfma_f32_16x16x32_bf16 v[148:151], a[76:79], a[24:27], v[148:151]\n"
      "s_sub_u32 s93, s93, 1\n"
      "v_exp_f32 v113, v185\n"
      "v_mfma_f32_16x16x32_bf16 v[148:151], a[68:71], a[8:11], v[148:151]\n"
      "s_cmp_le_i32 s93, 14\n"
      "ds_read_b64_tr_b16 a[124:125], v247 offset:1024\n"
      "v_cvt_pk_bf16_f32 v220, v112, v113\n"
      "v_mfma_f32_16x16x32_bf16 v[156:159], a[68:71], a[12:15], v[236:239]\n"
      "s_cselect_b32 s86, 107, s86\n"
      "v_exp_f32 v114, v186\n"
      "v_mfma_f32_16x16x32_bf16 v[156:159], a[76:79], a[28:31], v[156:159]\n"
      "s_sub_u32 s67, s65, s66\n"
      "ds_read_b64_tr_b16 a[126:127], v247 offset:5120\n"
      "v_mfma_f32_16x16x32_bf16 v[156:159], a[84:87], a[44:47], v[156:159]\n"
      "s_cmp_lt_i32 s67, 0\n"
      "v_exp_f32 v115, v187\n"
      "v_mfma_f32_16x16x32_bf16 v[156:159], a[92:95], a[60:63], v[156:159]\n"
      "s_cselect_b32 s88, s66, s65\n"
      "v_cvt_pk_bf16_f32 v221, v114, v115\n"
      "v_mfma_f32_16x16x32_bf16 v[152:155], a[88:91], a[60:63], v[236:239]\n"
      "s_add_u32 s90, s88, s91\n"
      "v_exp_f32 v112, v188\n"
      "v_mfma_f32_16x16x32_bf16 v[152:155], a[80:83], a[44:47], v[152:155]\n"
      "v_or3_b32 v116, v116, v220, v221\n"
      "v_mfma_f32_16x16x32_bf16 v[152:155], a[72:75], a[28:31], v[152:155]\n"
      "v_exp_f32 v113, v189\n"
      "v_mfma_f32_16x16x32_bf16 v[152:155], a[64:67], a[12:15], v[152:155]\n"
      "s_waitcnt lgkmcnt(10)\n"
      "v_cvt_pk_bf16_f32 v222, v112, v113\n"
      "v_mfma_f32_16x16x32_bf16 v[32:35], a[96:99], v[192:195], v[32:35]\n"
      "v_exp_f32 v114, v190\n"
      "v_mfma_f32_16x16x32_bf16 v[48:51], a[100:103], v[192:195], v[48:51]\n"
      "v_mfma_f32_16x16x32_bf16 a[128:131], a[104:107], v[192:195], a[128:131]\n"
      "s_waitcnt lgkmcnt(6)\n"
      "v_exp_f32 v115, v191\n"
      "v_mfma_f32_16x16x32_bf16 a[144:147], a[108:111], v[192:195], a[144:147]\n"
      "v_cvt_pk_bf16_f32 v223, v114, v115\n"
      "v_mfma_f32_16x16x32_bf16 v[64:67], a[112:115], v[192:195], v[64:67]\n"
      "s_waitcnt lgkmcnt(2)\n"
      "v_mfma_f32_16x16x32_bf16 v[80:83], a[116:119], v[192:195], v[80:83]\n"
      "v_or3_b32 v116, v116, v222, v223\n"
      "v_mfma_f32_16x16x32_bf16 a[160:163], a[120:123], v[192:195], a[160:163]\n"
      "s_mov_b32 s64, 1\n"
      "s_waitcnt lgkmcnt(0)\n"
      "v_and_b32 v116, 0x40004000, v116\n"
      "v_mfma_f32_16x16x32_bf16 a[176:179], a[124:127], v[192:195], a[176:179]\n"
      "v_cmp_ne_u32 vcc, 0, v116\n"
      "s_cbranch_vccnz FB1_%=\n"
      "RET1_1_%=:\n"
      "v_mfma_f32_16x16x32_bf16 a[180:183], a[124:127], v[196:199], a[180:183]\n"
      "v_exp_f32 v112, v128\n"
      "s_barrier\n"
      "v_mfma_f32_16x16x32_bf16 a[164:167], a[120:123], v[196:199], a[164:167]\n"
      "v_mfma_f32_16x16x32_bf16 a[192:195], a[196:199], v[208:211], a[192:195]\n"
      "s_add_u32 m0, s62, 0\n"
      "v_exp_f32 v113, v129\n"
      "v_mfma_f32_16x16x32_bf16 v[84:87], a[116:119], v[196:199], v[84:87]\n"
      "buffer_load_dwordx4 v248, s[20:23], s60 offen nt lds\n"
      "v_cvt_pk_bf16_f32 v192, v112, v113\n"
      "v_mfma_f32_16x16x32_bf16 v[68:71], a[112:115], v[196:199], v[68:71]\n"
      "s_add_u32 m0, s62, 896\n"
      "v_exp_f32 v114, v130\n"
      "v_mfma_f32_16x16x32_bf16 a[192:195], a[200:203], v[212:215], a[192:195]\n"
      "buffer_load_dwordx4 v248, s[20:23], s60 offen offset:128 nt lds\n"
      "v_mfma_f32_16x16x32_bf16 a[148:151], a[108:111], v[196:199], a[148:151]\n"
      "s_add_u32 m0, s62, 8192\n"
      "v_exp_f32 v115, v131\n"
      "v_mfma_f32_16x16x32_bf16 a[132:135], a[104:107], v[196:199], a[132:135]\n"
      "s_memrealtime s[68:69]\n"
      "buffer_load_dwordx4 v120, s[20:23], s60 offen nt lds\n"
      "v_cvt_pk_bf16_f32 v193, v114, v115\n"
      "v_mfma_f32_16x16x32_bf16 a[192:195], a[204:207], v[216:219], a[192:195]\n"
      "s_add_u32 m0, s62, 9088\n"
      "v_exp_f32 v112, v132\n"
      "v_mfma_f32_16x16x32_bf16 v[52:55], a[100:103], v[196:199], v[52:55]\n"
      "s_add_u32 s62, s62, 0x8000\n"
      "buffer_load_dwordx4 v120, s[20:23], s60 offen offset:128 nt lds\n"
      "v_or_b32 v116, v192, v193\n"
      "v_mfma_f32_16x16x32_bf16 v[36:39], a[96:99], v[196:199], v[36:39]\n"
      "s_and_b32 s62, s62, 0x1ffff\n"
      "v_exp_f32 v113, v133\n"
      "v_mfma_f32_16x16x32_bf16 a[192:195], a[208:211], v[220:223], a[192:195]\n"
      "s_add_u32 s60, s60, s78\n"
      "v_cvt_pk_bf16_f32 v194, v112, v113\n"
      "s_waitcnt vmcnt(12) lgkmcnt(0)\n"
      "s_barrier\n"
      "v_mfma_f32_16x16x32_bf16 v[40:43], a[96:99], v[200:203], v[40:43]\n"
      "ds_read_b128 a[64:67], v242 offset:32768\n"
      "v_exp_f32 v114, v134\n"
      "v_mfma_f32_16x16x32_bf16 v[44:47], a[96:99], v[204:207], v[44:47]\n"
      "ds_read_b128 a[68:71], v242 offset:36864\n"
      "ds_read_b128 a[72:75], v243 offset:32768\n"
      "v_mfma_f32_16x16x32_bf16 v[60:63], a[100:103], v[204:207], v[60:63]\n"
      "v_exp_f32 v115, v135\n"
      "v_mfma_f32_16x16x32_bf16 v[56:59], a[100:103], v[200:203], v[56:59]\n"
      "ds_read_b128 a[76:79], v243 offset:36864\n"
      "v_cvt_pk_bf16_f32 v195, v114, v115\n"
      "v_mfma_f32_16x16x32_bf16 a[136:139], a[104:107], v[200:203], a[136:139]\n"
      "v_exp_f32 v112, v136\n"
      "v_mfma_f32_16x16x32_bf16 a[140:143], a[104:107], v[204:207], a[140:143]\n"
      "ds_read_b128 a[80:83], v242 offset:33792\n"
      "v_or3_b32 v116, v116, v194, v195\n"
      "v_mfma_f32_16x16x32_bf16 a[156:159], a[108:111], v[204:207], a[156:159]\n"
      "v_exp_f32 v113, v137\n"
      "v_mfma_f32_16x16x32_bf16 a[152:155], a[108:111], v[200:203], a[152:155]\n"
      "ds_read_b128 a[84:87], v242 offset:37888\n"
      "v_cvt_pk_bf16_f32 v196, v112, v113\n"
      "v_mfma_f32_16x16x32_bf16 v[72:75], a[112:115], v[200:203], v[72:75]\n"
      "v_exp_f32 v114, v138\n"
      "v_mfma_f32_16x16x32_bf16 v[76:79], a[112:115], v[204:207], v[76:79]\n"
      "ds_read_b128 a[88:91], v243 offset:33792\n"
      "ds_read_b128 a[92:95], v243 offset:37888\n"
      "v_mfma_f32_16x16x32_bf16 v[92:95], a[116:119], v[204:207], v[92:95]\n"
      "v_exp_f32 v115, v139\n"
      "v_mfma_f32_16x16x32_bf16 v[88:91], a[116:119], v[200:203], v[88:91]\n"
      "v_cvt_pk_bf16_f32 v197, v114, v115\n"
      "v_mfma_f32_16x16x32_bf16 a[168:171], a[120:123], v[200:203], a[168:171]\n"
      "v_exp_f32 v112, v140\n"
      "v_mfma_f32_16x16x32_bf16 a[172:175], a[120:123], v[204:207], a[172:175]\n"
      "v_or3_b32 v116, v116, v196, v197\n"
      "v_mfma_f32_16x16x32_bf16 a[188:191], a[124:127], v[204:207], a[188:191]\n"
      "v_exp_f32 v113, v141\n"
      "v_mfma_f32_16x16x32_bf16 a[184:187], a[124:127], v[200:203], a[184:187]\n"
      "s_waitcnt lgkmcnt(0)\n"
      "v_cvt_pk_bf16_f32 v198, v112, v113\n"
      "v_mfma_f32_16x16x32_bf16 v[160:163], a[64:67], a[0:3], v[224:227]\n"
      "v_exp_f32 v114, v142\n"
      "v_mfma_f32_16x16x32_bf16 v[160:163], a[72:75], a[16:19], v[160:163]\n"
      "ds_read_b64_tr_b16 a[96:97], v244 offset:32768\n"
      "ds_read_b64_tr_b16 a[98:99], v244 offset:36864\n"
      "v_mfma_f32_16x16x32_bf16 v[160:163], a[80:83], a[32:35], v[160:163]\n"
      "v_exp_f32 v115, v143\n"
      "v_mfma_f32_16x16x32_bf16 v[160:163], a[88:91], a[48:51], v[160:163]\n"
      "ds_read_b64_tr_b16 a[100:101], v245 offset:32768\n"
      "v_cvt_pk_bf16_f32 v199, v114, v115\n"
      "v_mfma_f32_16x16x32_bf16 v[164:167], a[92:95], a[48:51], v[224:227]\n"
      "v_exp_f32 v112, v144\n"
      "v_mfma_f32_16x16x32_bf16 v[164:167], a[84:87], a[32:35], v[164:167]\n"
      "ds_read_b64_tr_b16 a[102:103], v245 offset:36864\n"
      "v_or3_b32 v116, v116, v198, v199\n"
      "v_mfma_f32_16x16x32_bf16 v[164:167], a[76:79], a[16:19], v[164:167]\n"
      "v_exp_f32 v113, v145\n"
      "v_mfma_f32_16x16x32_bf16 v[164:167], a[68:71], a[0:3], v[164:167]\n"
      "ds_read_b64_tr_b16 a[104:105], v246 offset:32768\n"
      "v_cvt_pk_bf16_f32 v200, v112, v113\n"
      "v_mfma_f32_16x16x32_bf16 v[172:175], a[68:71], a[4:7], v[228:231]\n"
      "v_exp_f32 v114, v146\n"
      "v_mfma_f32_16x16x32_bf16 v[172:175], a[76:79], a[20:23], v[172:175]\n"
      "ds_read_b64_tr_b16 a[106:107], v246 offset:36864\n"
      "ds_read_b64_tr_b16 a[108:109], v247 offset:32768\n"
      "v_mfma_f32_16x16x32_bf16 v[172:175], a[84:87], a[36:39], v[172:175]\n"
      "v_exp_f32 v115, v147\n"
      "v_mfma_f32_16x16x32_bf16 v[172:175], a[92:95], a[52:55], v[172:175]\n"
      "ds_read_b64_tr_b16 a[110:111], v247 offset:36864\n"
      "v_cvt_pk_bf16_f32 v201, v114, v115\n"
      "v_mfma_f32_16x16x32_bf16 v[168:171], a[88:91], a[52:55], v[228:231]\n"
      "v_exp_f32 v112, v148\n"
      "v_mfma_f32_16x16x32_bf16 v[168:171], a[80:83], a[36:39], v[168:171]\n"
      "ds_read_b64_tr_b16 a[112:113], v244 offset:33792\n"
      "v_or3_b32 v116, v116, v200, v201\n"
      "v_mfma_f32_16x16x32_bf16 v[168:171], a[72:75], a[20:23], v[168:171]\n"
      "v_exp_f32 v113, v149\n"
      "v_mfma_f32_16x16x32_bf16 v[168:171], a[64:67], a[4:7], v[168:171]\n"
      "ds_read_b64_tr_b16 a[114:115], v244 offset:37888\n"
      "v_cvt_pk_bf16_f32 v202, v112, v113\n"
      "v_mfma_f32_16x16x32_bf16 v[176:179], a[64:67], a[8:11], v[232:235]\n"
      "v_exp_f32 v114, v150\n"
      "v_mfma_f32_16x16x32_bf16 v[176:179], a[72:75], a[24:27], v[176:179]\n"
      "ds_read_b64_tr_b16 a[116:117], v245 offset:33792\n"
      "ds_read_b64_tr_b16 a[118:119], v245 offset:37888\n"
      "v_mfma_f32_16x16x32_bf16 v[176:179], a[80:83], a[40:43], v[176:179]\n"
      "v_exp_f32 v115, v151\n"
      "v_mfma_f32_16x16x32_bf16 v[176:179], a[88:91], a[56:59], v[176:179]\n"
      "ds_read_b64_tr_b16 a[120:121], v246 offset:33792\n"
      "v_cvt_pk_bf16_f32 v203, v114, v115\n"
      "v_mfma_f32_16x16x32_bf16 v[180:183], a[92:95], a[56:59], v[232:235]\n"
      "v_exp_f32 v112, v152\n"
      "v_mfma_f32_16x16x32_bf16 v[180:183], a[84:87], a[40:43], v[180:183]\n"
      "ds_read_b64_tr_b16 a[122:123], v246 offset:37888\n"
      "v_or3_b32 v116, v116, v202, v203\n"
      "v_mfma_f32_16x16x32_bf16 v[180:183], a[76:79], a[24:27], v[180:183]\n"
      "v_exp_f32 v113, v153\n"
      "v_mfma_f32_16x16x32_bf16 v[180:183], a[68:71], a[8:11], v[180:183]\n"
      "ds_read_b64_tr_b16 a[124:125], v247 offset:33792\n"
      "v_cvt_pk_bf16_f32 v204, v112, v113\n"
      "v_mfma_f32_16x16x32_bf16 v[188:191], a[68:71], a[12:15], v[236:239]\n"
      "v_exp_f32 v114, v154\n"
      "v_mfma_f32_16x16x32_bf16 v[188:191], a[76:79], a[28:31], v[188:191]\n"
      "ds_read_b64_tr_b16 a[126:127], v247 offset:37888\n"
      "v_mfma_f32_16x16x32_bf16 v[188:191], a[84:87], a[44:47], v[188:191]\n"
      "v_exp_f32 v115, v155\n"
      "v_mfma_f32_16x16x32_bf16 v[188:191], a[92:95], a[60:63], v[188:191]\n"
      "v_cvt_pk_bf16_f32 v205, v114, v115\n"
      "v_mfma_f32_16x16x32_bf16 v[184:187], a[88:91], a[60:63], v[236:239]\n"
      "v_exp_f32 v112, v156\n"
      "v_mfma_f32_16x16x32_bf16 v[184:187], a[80:83], a[44:47], v[184:187]\n"
      "v_or3_b32 v116, v116, v204, v205\n"
      "v_mfma_f32_16x16x32_bf16 v[184:187], a[72:75], a[28:31], v[184:187]\n"
      "v_exp_f32 v113, v157\n"
      "v_mfma_f32_16x16x32_bf16 v[184:187], a[64:67], a[12:15], v[184:187]\n"
      "s_waitcnt lgkmcnt(10)\n"
      "v_cvt_pk_bf16_f32 v206, v112, v113\n"
      "v_mfma_f32_16x16x32_bf16 v[32:35], a[96:99], v[208:211], v[32:35]\n"
      "v_exp_f32 v114, v158\n"
      "v_mfma_f32_16x16x32_bf16 v[48:51], a[100:103], v[208:211], v[48:51]\n"
      "v_mfma_f32_16x16x32_bf16 a[128:131], a[104:107], v[208:211], a[128:131]\n"
      "s_waitcnt lgkmcnt(6)\n"
      "v_exp_f32 v115, v159\n"
      "v_mfma_f32_16x16x32_bf16 a[144:147], a[108:111], v[208:211], a[144:147]\n"
      "v_cvt_pk_bf16_f32 v207, v114, v115\n"
      "v_mfma_f32_16x16x32_bf16 v[64:67], a[112:115], v[208:211], v[64:67]\n"
      "s_waitcnt lgkmcnt(2)\n"
      "v_mfma_f32_16x16x32_bf16 v[80:83], a[116:119], v[208:211], v[80:83]\n"
      "v_or3_b32 v116, v116, v206, v207\n"
      "v_mfma_f32_16x16x32_bf16 a[160:163], a[120:123], v[208:211], a[160:163]\n"
      "s_mov_b32 s64, 1\n"
      "s_waitcnt lgkmcnt(0)\n"
      "v_and_b32 v116, 0x40004000, v116\n"
      "v_mfma_f32_16x16x32_bf16 a[176:179], a[124:127], v[208:211], a[176:179]\n"
      "v_cmp_ne_u32 vcc, 0, v116\n"
      "s_cbranch_vccnz FB0_%=\n"
      "RET0_1_%=:\n"
      "v_mfma_f32_16x16x32_bf16 a[180:183], a[124:127], v[212:215], a[180:183]\n"
      "v_exp_f32 v112, v160\n"
      "v_mfma_f32_16x16x32_bf16 a[164:167], a[120:123], v[212:215], a[164:167]\n"
      "v_xor_b32 v242, 0x10000, v242\n"
      "v_mfma_f32_16x16x32_bf16 a[192:195], a[196:199], v[192:195], a[192:195]\n"
      "s_add_u32 m0, s77, 16384\n"
      "v_exp_f32 v113, v161\n"
      "v_mfma_f32_16x16x32_bf16 v[84:87], a[116:119], v[212:215], v[84:87]\n"
      "buffer_load_dwordx4 v249, s[24:27], s61 offen nt lds\n"
      "v_cvt_pk_bf16_f32 v208, v112, v113\n"
      "v_xor_b32 v243, 0x10000, v243\n"
      "v_mfma_f32_16x16x32_bf16 v[68:71], a[112:115], v[212:215], v[68:71]\n"
      "s_add_u32 m0, s77, 17408\n"
      "v_exp_f32 v114, v162\n"
      "v_mfma_f32_16x16x32_bf16 a[192:195], a[200:203], v[196:199], a[192:195]\n"
      "buffer_load_dwordx4 v121, s[24:27], s61 offen nt lds\n"
      "v_xor_b32 v244, 0x10000, v244\n"
      "v_mfma_f32_16x16x32_bf16 a[148:151], a[108:111], v[212:215], a[148:151]\n"
      "s_add_u32 m0, s77, 24576\n"
      "v_exp_f32 v115, v163\n"
      "v_mfma_f32_16x16x32_bf16 a[132:135], a[104:107], v[212:215], a[132:135]\n"
      "s_memrealtime s[68:69]\n"
      "buffer_load_dwordx4 v122, s[24:27], s61 offen nt lds\n"
      "v_cvt_pk_bf16_f32 v209, v114, v115\n"
      "v_xor_b32 v245, 0x10000, v245\n"
      "v_mfma_f32_16x16x32_bf16 a[192:195], a[204:207], v[200:203], a[192:195]\n"
      "s_add_u32 m0, s77, 25600\n"
      "v_exp_f32 v112, v164\n"
      "v_mfma_f32_16x16x32_bf16 v[52:55], a[100:103], v[212:215], v[52:55]\n"
      "s_add_u32 s77, s77, 0x8000\n"
      "buffer_load_dwordx4 v123, s[24:27], s61 offen nt lds\n"
      "v_or_b32 v116, v208, v209\n"
      "v_xor_b32 v246, 0x10000, v246\n"
      "v_mfma_f32_16x16x32_bf16 v[36:39], a[96:99], v[212:215], v[36:39]\n"
      "s_and_b32 s77, s77, 0x1ffff\n"
      "v_exp_f32 v113, v165\n"
      "v_mfma_f32_16x16x32_bf16 a[192:195], a[208:211], v[204:207], a[192:195]\n"
      "s_add_u32 s61, s61, s78\n"
      "v_cvt_pk_bf16_f32 v210, v112, v113\n"
      "v_xor_b32 v247, 0x10000, v247\n"
      "s_waitcnt vmcnt(8) lgkmcnt(0)\n"
      "s_sub_u32 s67, s68, s90\n"
      "s_cmp_lt_i32 s67, 0\n"
      "s_cbranch_scc1 GW3_%=\n"
      "GX3_%=:\n"
      "s_barrier\n"
      "v_mfma_f32_16x16x32_bf16 v[40:43], a[96:99], v[216:219], v[40:43]\n"
      "s_memrealtime s[68:69]\n"
      "buffer_load_dword v250, v251, s[28:31], 0 offen sc1\n"
      "ds_read_b128 a[64:67], v242 offset:0\n"
      "v_exp_f32 v114, v166\n"
      "v_mfma_f32_16x16x32_bf16 v[44:47], a[96:99], v[220:223], v[44:47]\n"
      "s_add_u32 m0, s62, 0\n"
      "ds_read_b128 a[68:71], v242 offset:4096\n"
      "ds_read_b128 a[72:75], v243 offset:0\n"
      "v_mfma_f32_16x16x32_bf16 v[60:63], a[100:103], v[220:223], v[60:63]\n"
      "buffer_load_dwordx4 v248, s[20:23], s60 offen nt lds\n"
      "v_exp_f32 v115, v167\n"
      "v_mfma_f32_16x16x32_bf16 v[56:59], a[100:103], v[216:219], v[56:59]\n"
      "s_add_u32 m0, s62, 896\n"
      "ds_read_b128 a[76:79], v243 offset:4096\n"
      "v_cvt_pk_bf16_f32 v211, v114, v115\n"
      "v_mfma_f32_16x16x32_bf16 a[136:139], a[104:107], v[216:219], a[136:139]\n"
      "buffer_load_dwordx4 v248, s[20:23], s60 offen offset:128 nt lds\n"
      "v_exp_f32 v112, v168\n"
      "v_mfma_f32_16x16x32_bf16 a[140:143], a[104:107], v[220:223], a[140:143]\n"
      "s_add_u32 m0, s62, 8192\n"
      "ds_read_b128 a[80:83], v242 offset:1024\n"
      "v_or3_b32 v116, v116, v210, v211\n"
      "v_mfma_f32_16x16x32_bf16 a[156:159], a[108:111], v[220:223], a[156:159]\n"
      "buffer_load_dwordx4 v120, s[20:23], s60 offen nt lds\n"
      "v_exp_f32 v113, v169\n"
      "v_mfma_f32_16x16x32_bf16 a[152:155], a[108:111], v[216:219], a[152:155]\n"
      "s_add_u32 m0, s62, 9088\n"
      "ds_read_b128 a[84:87], v242 offset:5120\n"
      "v_cvt_pk_bf16_f32 v212, v112, v113\n"
      "v_mfma_f32_16x16x32_bf16 v[72:75], a[112:115], v[216:219], v[72:75]\n"
      "s_add_u32 s62, s62, 0x8000\n"
      "buffer_load_dwordx4 v120, s[20:23], s60 offen offset:128 nt lds\n"
      "v_exp_f32 v114, v170\n"
      "v_mfma_f32_16x16x32_bf16 v[76:79], a[112:115], v[220:223], v[76:79]\n"
      "s_and_b32 s62, s62, 0x1ffff\n"
      "ds_read_b128 a[88:91], v243 offset:1024\n"
      "ds_read_b128 a[92:95], v243 offset:5120\n"
      "v_mfma_f32_16x16x32_bf16 v[92:95], a[116:119], v[220:223], v[92:95]\n"
      "s_add_u32 s60, s60, %[inc]\n"
      "v_exp_f32 v115, v171\n"
      "v_mfma_f32_16x16x32_bf16 v[88:91], a[116:119], v[216:219], v[88:91]\n"
      "v_cvt_pk_bf16_f32 v213, v114, v115\n"
      "v_mfma_f32_16x16x32_bf16 a[168:171], a[120:123], v[216:219], a[168:171]\n"
      "v_exp_f32 v112, v172\n"
      "v_mfma_f32_16x16x32_bf16 a[172:175], a[120:123], v[220:223], a[172:175]\n"
      "v_or3_b32 v116, v116, v212, v213\n"
      "v_mfma_f32_16x16x32_bf16 a[188:191], a[124:127], v[220:223], a[188:191]\n"
      "v_exp_f32 v113, v173\n"
      "v_mfma_f32_16x16x32_bf16 a[184:187], a[124:127], v[216:219], a[184:187]\n"
      "s_waitcnt lgkmcnt(0)\n"
      "v_cvt_pk_bf16_f32 v214, v112, v113\n"
      "v_mfma_f32_16x16x32_bf16 v[128:131], a[64:67], a[0:3], v[224:227]\n"
      "s_sub_u32 s67, s68, s66\n"
      "v_exp_f32 v114, v174\n"
      "v_mfma_f32_16x16x32_bf16 v[128:131], a[72:75], a[16:19], v[128:131]\n"
      "s_cmp_gt_i32 s67, s70\n"
      "ds_read_b64_tr_b16 a[96:97], v244 offset:0\n"
      "ds_read_b64_tr_b16 a[98:99], v244 offset:4096\n"
      "v_mfma_f32_16x16x32_bf16 v[128:131], a[80:83], a[32:35], v[128:131]\n"
      "s_cselect_b32 s69, s86, 0\n"
      "v_exp_f32 v115, v175\n"
      "v_mfma_f32_16x16x32_bf16 v[128:131], a[88:91], a[48:51], v[128:131]\n"
      "s_add_u32 s66, s66, s69\n"
      "ds_read_b64_tr_b16 a[100:101], v245 offset:0\n"
      "v_cvt_pk_bf16_f32 v215, v114, v115\n"
      "v_mfma_f32_16x16x32_bf16 v[132:135], a[92:95], a[48:51], v[224:227]\n"
      "s_add_u32 s67, s67, s101\n"
      "v_exp_f32 v112, v176\n"
      "v_mfma_f32_16x16x32_bf16 v[132:135], a[84:87], a[32:35], v[132:135]\n"
      "s_min_i32 s70, s70, s67\n"
      "ds_read_b64_tr_b16 a[102:103], v245 offset:4096\n"
      "v_or3_b32 v116, v116, v214, v215\n"
      "v_mfma_f32_16x16x32_bf16 v[132:135], a[76:79], a[16:19], v[132:135]\n"
      "s_max_i32 s70, s70, 85\n"
      "v_exp_f32 v113, v177\n"
      "v_mfma_f32_16x16x32_bf16 v[132:135], a[68:71], a[0:3], v[132:135]\n"
      "s_add_u32 s66, s66, s86\n"
      "ds_read_b64_tr_b16 a[104:105], v246 offset:0\n"
      "v_cvt_pk_bf16_f32 v216, v112, v113\n"
      "v_mfma_f32_16x16x32_bf16 v[140:143], a[68:71], a[4:7], v[228:231]\n"
      "s_and_b32 s76, s80, 31\n"
      "v_exp_f32 v114, v178\n"
      "v_mfma_f32_16x16x32_bf16 v[140:143], a[76:79], a[20:23], v[140:143]\n"
      "s_lshr_b64 s[80:81], s[80:81], 5\n"
      "ds_read_b64_tr_b16 a[106:107], v246 offset:4096\n"
      "ds_read_b64_tr_b16 a[108:109], v247 offset:0\n"
      "v_mfma_f32_16x16x32_bf16 v[140:143], a[84:87], a[36:39], v[140:143]\n"
      "s_add_u32 s66, s66, s76\n"
      "v_exp_f32 v115, v179\n"
      "v_mfma_f32_16x16x32_bf16 v[140:143], a[92:95], a[52:55], v[140:143]\n"
      "s_sub_u32 s67, s68, s82\n"
      "ds_read_b64_tr_b16 a[110:111], v247 offset:4096\n"
      "v_cvt_pk_bf16_f32 v217, v114, v115\n"
      "v_mfma_f32_16x16x32_bf16 v[136:139], a[88:91], a[52:55], v[228:231]\n"
      "s_min_i32 s76, s67, s83\n"
      "v_exp_f32 v112, v180\n"
      "v_mfma_f32_16x16x32_bf16 v[136:139], a[80:83], a[36:39], v[136:139]\n"
      "s_mov_b32 s83, s67\n"
      "ds_read_b64_tr_b16 a[112:113], v244 offset:1024\n"
      "v_or3_b32 v116, v116, v216, v217\n"
      "v_mfma_f32_16x16x32_bf16 v[136:139], a[72:75], a[20:23], v[136:139]\n"
      "s_sub_u32 s76, s76, 36\n"
      "v_exp_f32 v113, v181\n"
      "v_mfma_f32_16x16x32_bf16 v[136:139], a[64:67], a[4:7], v[136:139]\n"
      "s_min_i32 s76, s76, 148\n"
      "ds_read_b64_tr_b16 a[114:115], v244 offset:5120\n"
      "v_cvt_pk_bf16_f32 v218, v112, v113\n"
      "v_mfma_f32_16x16x32_bf16 v[144:147], a[64:67], a[8:11], v[232:235]\n"
      "s_max_i32 s76, s76, 70\n"
      "v_exp_f32 v114, v182\n"
      "v_mfma_f32_16x16x32_bf16 v[144:147], a[72:75], a[24:27], v[144:147]\n"
      "s_mov_b32 s82, s68\n"
      "ds_read_b64_tr_b16 a[116:117], v245 offset:1024\n"
      "ds_read_b64_tr_b16 a[118:119], v245 offset:5120\n"
      "v_mfma_f32_16x16x32_bf16 v[144:147], a[80:83], a[40:43], v[144:147]\n"
      "s_lshr_b32 s69, s86, 1\n"
      "v_exp_f32 v115, v183\n"
      "v_mfma_f32_16x16x32_bf16 v[144:147], a[88:91], a[56:59], v[144:147]\n"
      "s_add_u32 s66, s66, s69\n"
      "ds_read_b64_tr_b16 a[120:121], v246 offset:1024\n"
      "v_cvt_pk_bf16_f32 v219, v114, v115\n"
      "v_mfma_f32_16x16x32_bf16 v[148:151], a[92:95], a[56:59], v[232:235]\n"
      "s_add_u32 s82, s82, s69\n"
      "v_exp_f32 v112, v184\n"
      "v_mfma_f32_16x16x32_bf16 v[148:151], a[84:87], a[40:43], v[148:151]\n"
      "s_add_u32 s65, s82, s76\n"
      "ds_read_b64_tr_b16 a[122:123], v246 offset:5120\n"
      "v_or3_b32 v116, v116, v218, v219\n"
      "v_mfma_f32_16x16x32_bf16 v[148:151], a[76:79], a[24:27], v[148:151]\n"
      "s_sub_u32 s93, s93, 1\n"
      "v_exp_f32 v113, v185\n"
      "v_mfma_f32_16x16x32_bf16 v[148:151], a[68:71], a[8:11], v[148:151]\n"
      "s_cmp_le_i32 s93, 14\n"
      "ds_read_b64_tr_b16 a[124:125], v247 offset:1024\n"
      "v_cvt_pk_bf16_f32 v220, v112, v113\n"
      "v_mfma_f32_16x16x32_bf16 v[156:159], a[68:71], a[12:15], v[236:239]\n"
      "s_cselect_b32 s86, 107, s86\n"
      "v_exp_f32 v114, v186\n"
      "v_mfma_f32_16x16x32_bf16 v[156:159], a[76:79], a[28:31], v[156:159]\n"
      "s_sub_u32 s67, s65, s66\n"
      "ds_read_b64_tr_b16 a[126:127], v247 offset:5120\n"
      "v_mfma_f32_16x16x32_bf16 v[156:159], a[84:87], a[44:47], v[156:159]\n"
      "s_cmp_lt_i32 s67, 0\n"
      "v_exp_f32 v115, v187\n"
      "v_mfma_f32_16x16x32_bf16 v[156:159], a[92:95], a[60:63], v[156:159]\n"
      "s_cselect_b32 s88, s66, s65\n"
      "v_cvt_pk_bf16_f32 v221, v114, v115\n"
      "v_mfma_f32_16x16x32_bf16 v[152:155], a[88:91], a[60:63], v[236:239]\n"
      "s_add_u32 s90, s88, s91\n"
      "v_exp_f32 v112, v188\n"
      "v_mfma_f32_16x16x32_bf16 v[152:155], a[80:83], a[44:47], v[152:155]\n"
      "v_or3_b32 v116, v116, v220, v221\n"
      "v_mfma_f32_16x16x32_bf16 v[152:155], a[72:75], a[28:31], v[152:155]\n"
      "v_exp_f32 v113, v189\n"
      "v_mfma_f32_16x16x32_bf16 v[152:155], a[64:67], a[12:15], v[152:155]\n"
      "s_waitcnt lgkmcnt(10)\n"
      "v_cvt_pk_bf16_f32 v222, v112, v113\n"
      "v_mfma_f32_16x16x32_bf16 v[32:35], a[96:99], v[192:195], v[32:35]\n"
      "v_exp_f32 v114, v190\n"
      "v_mfma_f32_16x16x32_bf16 v[48:51], a[100:103], v[192:195], v[48:51]\n"
      "v_mfma_f32_16x16x32_bf16 a[128:131], a[104:107], v[192:195], a[128:131]\n"
      "s_waitcnt lgkmcnt(6)\n"
      "v_exp_f32 v115, v191\n"
      "v_mfma_f32_16x16x32_bf16 a[144:147], a[108:111], v[192:195], a[144:147]\n"
      "v_cvt_pk_bf16_f32 v223, v114, v115\n"
      "v_mfma_f32_16x16x32_bf16 v[64:67], a[112:115], v[192:195], v[64:67]\n"
      "s_waitcnt lgkmcnt(2)\n"
      "v_mfma_f32_16x16x32_bf16 v[80:83], a[116:119], v[192:195], v[80:83]\n"
      "v_or3_b32 v116, v116, v222, v223\n"
      "v_mfma_f32_16x16x32_bf16 a[160:163], a[120:123], v[192:195], a[160:163]\n"
      "s_mov_b32 s64, 2\n"
      "s_waitcnt lgkmcnt(0)\n"
      "v_and_b32 v116, 0x40004000, v116\n"
      "v_mfma_f32_16x16x32_bf16 a[176:179], a[124:127], v[192:195], a[176:179]\n"
      "v_cmp_ne_u32 vcc, 0, v116\n"
      "s_cbranch_vccnz FB1_%=\n"
      "RET1_2_%=:\n"
      "v_mfma_f32_16x16x32_bf16 a[180:183], a[124:127], v[196:199], a[180:183]\n"
      "v_exp_f32 v112, v128\n"
      "v_mfma_f32_16x16x32_bf16 a[164:167], a[120:123], v[196:199], a[164:167]\n"
      "v_mfma_f32_16x16x32_bf16 a[192:195], a[196:199], v[208:211], a[192:195]\n"
      "s_add_u32 m0, s77, 16384\n"
      "v_exp_f32 v113, v129\n"
      "v_mfma_f32_16x16x32_bf16 v[84:87], a[116:119], v[196:199], v[84:87]\n"
      "buffer_load_dwordx4 v249, s[24:27], s61 offen nt lds\n"
      "v_cvt_pk_bf16_f32 v192, v112, v113\n"
      "v_mfma_f32_16x16x32_bf16 v[68:71], a[112:115], v[196:199], v[68:71]\n"
      "s_add_u32 m0, s77, 17408\n"
      "v_exp_f32 v114, v130\n"
      "v_mfma_f32_16x16x32_bf16 a[192:195], a[200:203], v[212:215], a[192:195]\n"
      "buffer_load_dwordx4 v121, s[24:27], s61 offen nt lds\n"
      "v_mfma_f32_16x16x32_bf16 a[148:151], a[108:111], v[196:199], a[148:151]\n"
      "s_add_u32 m0, s77, 24576\n"
      "v_exp_f32 v115, v131\n"
      "v_mfma_f32_16x16x32_bf16 a[132:135], a[104:107], v[196:199], a[132:135]\n"
      "s_memrealtime s[68:69]\n"
      "buffer_load_dwordx4 v122, s[24:27], s61 offen nt lds\n"
      "v_cvt_pk_bf16_f32 v193, v114, v115\n"
      "v_mfma_f32_16x16x32_bf16 a[192:195], a[204:207], v[216:219], a[192:195]\n"
      "s_add_u32 m0, s77, 25600\n"
      "v_exp_f32 v112, v132\n"
      "v_mfma_f32_16x16x32_bf16 v[52:55], a[100:103], v[196:199], v[52:55]\n"
      "s_add_u32 s77, s77, 0x8000\n"
      "buffer_load_dwordx4 v123, s[24:27], s61 offen nt lds\n"
      "v_or_b32 v116, v192, v193\n"
      "v_mfma_f32_16x16x32_bf16 v[36:39], a[96:99], v[196:199], v[36:39]\n"
      "s_and_b32 s77, s77, 0x1ffff\n"
      "v_exp_f32 v113, v133\n"
      "v_mfma_f32_16x16x32_bf16 a[192:195], a[208:211], v[220:223], a[192:195]\n"
      "s_add_u32 s61, s61, %[inc]\n"
      "v_cvt_pk_bf16_f32 v194, v112, v113\n"
      "s_waitcnt vmcnt(9) lgkmcnt(0)\n"
      "s_barrier\n"
      "v_mfma_f32_16x16x32_bf16 v[40:43], a[96:99], v[200:203], v[40:43]\n"
      "ds_read_b128 a[64:67], v242 offset:32768\n"
      "v_exp_f32 v114, v134\n"
      "v_mfma_f32_16x16x32_bf16 v[44:47], a[96:99], v[204:207], v[44:47]\n"
      "ds_read_b128 a[68:71], v242 offset:36864\n"
      "ds_read_b128 a[72:75], v243 offset:32768\n"
      "v_mfma_f32_16x16x32_bf16 v[60:63], a[100:103], v[204:207], v[60:63]\n"
      "v_exp_f32 v115, v135\n"
      "v_mfma_f32_16x16x32_bf16 v[56:59], a[100:103], v[200:203], v[56:59]\n"
      "ds_read_b128 a[76:79], v243 offset:36864\n"
      "v_cvt_pk_bf16_f32 v195, v114, v115\n"
      "v_mfma_f32_16x16x32_bf16 a[136:139], a[104:107], v[200:203], a[136:139]\n"
      "v_exp_f32 v112, v136\n"
      "v_mfma_f32_16x16x32_bf16 a[140:143], a[104:107], v[204:207], a[140:143]\n"
      "ds_read_b128 a[80:83], v242 offset:33792\n"
      "v_or3_b32 v116, v116, v194, v195\n"
      "v_mfma_f32_16x16x32_bf16 a[156:159], a[108:111], v[204:207], a[156:159]\n"
      "v_exp_f32 v113, v137\n"
      "v_mfma_f32_16x16x32_bf16 a[152:155], a[108:111], v[200:203], a[152:155]\n"
      "ds_read_b128 a[84:87], v242 offset:37888\n"
      "v_cvt_pk_bf16_f32 v196, v112, v113\n"
      "v_mfma_f32_16x16x32_bf16 v[72:75], a[112:115], v[200:203], v[72:75]\n"
      "v_exp_f32 v114, v138\n"
      "v_mfma_f32_16x16x32_bf16 v[76:79], a[112:115], v[204:207], v[76:79]\n"
      "ds_read_b128 a[88:91], v243 offset:33792\n"
      "ds_read_b128 a[92:95], v243 offset:37888\n"
      "v_mfma_f32_16x16x32_bf16 v[92:95], a[116:119], v[204:207], v[92:95]\n"
      "v_exp_f32 v115, v139\n"
      "v_mfma_f32_16x16x32_bf16 v[88:91], a[116:119], v[200:203], v[88:91]\n"
      "v_cvt_pk_bf16_f32 v197, v114, v115\n"
      "v_mfma_f32_16x16x32_bf16 a[168:171], a[120:123], v[200:203], a[168:171]\n"
      "v_exp_f32 v112, v140\n"
      "v_mfma_f32_16x16x32_bf16 a[172:175], a[120:123], v[204:207], a[172:175]\n"
      "v_or3_b32 v116, v116, v196, v197\n"
      "v_mfma_f32_16x16x32_bf16 a[188:191], a[124:127], v[204:207], a[188:191]\n"
      "v_exp_f32 v113, v141\n"
      "v_mfma_f32_16x16x32_bf16 a[184:187], a[124:127], v[200:203], a[184:187]\n"
      "s_waitcnt lgkmcnt(0)\n"
      "v_cvt_pk_bf16_f32 v198, v112, v113\n"
      "v_mfma_f32_16x16x32_bf16 v[160:163], a[64:67], a[0:3], v[224:227]\n"
      "v_exp_f32 v114, v142\n"
      "v_mfma_f32_16x16x32_bf16 v[160:163], a[72:75], a[16:19], v[160:163]\n"
      "ds_read_b64_tr_b16 a[96:97], v244 offset:32768\n"
      "ds_read_b64_tr_b16 a[98:99], v244 offset:36864\n"
      "v_mfma_f32_16x16x32_bf16 v[160:163], a[80:83], a[32:35], v[160:163]\n"
      "v_exp_f32 v115, v143\n"
      "v_mfma_f32_16x16x32_bf16 v[160:163], a[88:91], a[48:51], v[160:163]\n"
      "ds_read_b64_tr_b16 a[100:101], v245 offset:32768\n"
      "v_cvt_pk_bf16_f32 v199, v114, v115\n"
      "v_mfma_f32_16x16x32_bf16 v[164:167], a[92:95], a[48:51], v[224:227]\n"
      "v_exp_f32 v112, v144\n"
      "v_mfma_f32_16x16x32_bf16 v[164:167], a[84:87], a[32:35], v[164:167]\n"
      "ds_read_b64_tr_b16 a[102:103], v245 offset:36864\n"
      "v_or3_b32 v116, v116, v198, v199\n"
      "v_mfma_f32_16x16x32_bf16 v[164:167], a[76:79], a[16:19], v[164:167]\n"
      "v_exp_f32 v113, v145\n"
      "v_mfma_f32_16x16x32_bf16 v[164:167], a[68:71], a[0:3], v[164:167]\n"
      "ds_read_b64_tr_b16 a[104:105], v246 offset:32768\n"
      "v_cvt_pk_bf16_f32 v200, v112, v113\n"
      "v_mfma_f32_16x16x32_bf16 v[172:175], a[68:71], a[4:7], v[228:231]\n"
      "v_exp_f32 v114, v146\n"
      "v_mfma_f32_16x16x32_bf16 v[172:175], a[76:79], a[20:23], v[172:175]\n"
      "ds_read_b64_tr_b16 a[106:107], v246 offset:36864\n"
      "ds_read_b64_tr_b16 a[108:109], v247 offset:32768\n"
      "v_mfma_f32_16x16x32_bf16 v[172:175], a[84:87], a[36:39], v[172:175]\n"
      "v_exp_f32 v115, v147\n"
      "v_mfma_f32_16x16x32_bf16 v[172:175], a[92:95], a[52:55], v[172:175]\n"
      "ds_read_b64_tr_b16 a[110:111], v247 offset:36864\n"
      "v_cvt_pk_bf16_f32 v201, v114, v115\n"
      "v_mfma_f32_16x16x32_bf16 v[168:171], a[88:91], a[52:55], v[228:231]\n"
      "v_exp_f32 v112, v148\n"
      "v_mfma_f32_16x16x32_bf16 v[168:171], a[80:83], a[36:39], v[168:171]\n"
      "ds_read_b64_tr_b16 a[112:113], v244 offset:33792\n"
      "v_or3_b32 v116, v116, v200, v201\n"
      "v_mfma_f32_16x16x32_bf16 v[168:171], a[72:75], a[20:23], v[168:171]\n"
      "v_exp_f32 v113, v149\n"
      "v_mfma_f32_16x16x32_bf16 v[168:171], a[64:67], a[4:7], v[168:171]\n"
      "ds_read_b64_tr_b16 a[114:115], v244 offset:37888\n"
      "v_cvt_pk_bf16_f32 v202, v112, v113\n"
      "v_mfma_f32_16x16x32_bf16 v[176:179], a[64:67], a[8:11], v[232:235]\n"
      "v_exp_f32 v114, v150\n"
      "v_mfma_f32_16x16x32_bf16 v[176:179], a[72:75], a[24:27], v[176:179]\n"
      "ds_read_b64_tr_b16 a[116:117], v245 offset:33792\n"
      "ds_read_b64_tr_b16 a[118:119], v245 offset:37888\n"
      "v_mfma_f32_16x16x32_bf16 v[176:179], a[80:83], a[40:43], v[176:179]\n"
      "v_exp_f32 v115, v151\n"
      "v_mfma_f32_16x16x32_bf16 v[176:179], a[88:91], a[56:59], v[176:179]\n"
      "ds_read_b64_tr_b16 a[120:121], v246 offset:33792\n"
      "v_cvt_pk_bf16_f32 v203, v114, v115\n"
      "v_mfma_f32_16x16x32_bf16 v[180:183], a[92:95], a[56:59], v[232:235]\n"
      "v_exp_f32 v112, v152\n"
      "v_mfma_f32_16x16x32_bf16 v[180:183], a[84:87], a[40:43], v[180:183]\n"
      "ds_read_b64_tr_b16 a[122:123], v246 offset:37888\n"
      "v_or3_b32 v116, v116, v202, v203\n"
      "v_mfma_f32_16x16x32_bf16 v[180:183], a[76:79], a[24:27], v[180:183]\n"
      "v_exp_f32 v113, v153\n"
      "v_mfma_f32_16x16x32_bf16 v[180:183], a[68:71], a[8:11], v[180:183]\n"
      "ds_read_b64_tr_b16 a[124:125], v247 offset:33792\n"
      "v_cvt_pk_bf16_f32 v204, v112, v113\n"
      "v_mfma_f32_16x16x32_bf16 v[188:191], a[68:71], a[12:15], v[236:239]\n"
      "v_exp_f32 v114, v154\n"
      "v_mfma_f32_16x16x32_bf16 v[188:191], a[76:79], a[28:31], v[188:191]\n"
      "ds_read_b64_tr_b16 a[126:127], v247 offset:37888\n"
      "v_mfma_f32_16x16x32_bf16 v[188:191], a[84:87], a[44:47], v[188:191]\n"
      "v_exp_f32 v115, v155\n"
      "v_mfma_f32_16x16x32_bf16 v[188:191], a[92:95], a[60:63], v[188:191]\n"
      "v_cvt_pk_bf16_f32 v205, v114, v115\n"
      "v_mfma_f32_16x16x32_bf16 v[184:187], a[88:91], a[60:63], v[236:239]\n"
      "v_exp_f32 v112, v156\n"
      "v_mfma_f32_16x16x32_bf16 v[184:187], a[80:83], a[44:47], v[184:187]\n"
      "v_or3_b32 v116, v116, v204, v205\n"
      "v_mfma_f32_16x16x32_bf16 v[184:187], a[72:75], a[28:31], v[184:187]\n"
      "v_exp_f32 v113, v157\n"
      "v_mfma_f32_16x16x32_bf16 v[184:187], a[64:67], a[12:15], v[184:187]\n"
      "s_waitcnt lgkmcnt(10)\n"
      "v_cvt_pk_bf16_f32 v206, v112, v113\n"
      "v_mfma_f32_16x16x32_bf16 v[32:35], a[96:99], v[208:211], v[32:35]\n"
      "v_exp_f32 v114, v158\n"
      "v_mfma_f32_16x16x32_bf16 v[48:51], a[100:103], v[208:211], v[48:51]\n"
      "v_mfma_f32_16x16x32_bf16 a[128:131], a[104:107], v[208:211], a[128:131]\n"
      "s_waitcnt lgkmcnt(6)\n"
      "v_exp_f32 v115, v159\n"
      "v_mfma_f32_16x16x32_bf16 a[144:147], a[108:111], v[208:211], a[144:147]\n"
      "v_cvt_pk_bf16_f32 v207, v114, v115\n"
      "v_mfma_f32_16x16x32_bf16 v[64:67], a[112:115], v[208:211], v[64:67]\n"
      "s_waitcnt lgkmcnt(2)\n"
      "v_mfma_f32_16x16x32_bf16 v[80:83], a[116:119], v[208:211], v[80:83]\n"
      "v_or3_b32 v116, v116, v206, v207\n"
      "v_mfma_f32_16x16x32_bf16 a[160:163], a[120:123], v[208:211], a[160:163]\n"
      "s_mov_b32 s64, 2\n"
      "s_waitcnt lgkmcnt(0)\n"
      "v_and_b32 v116, 0x40004000, v116\n"
      "v_mfma_f32_16x16x32_bf16 a[176:179], a[124:127], v[208:211], a[176:179]\n"
      "v_cmp_ne_u32 vcc, 0, v116\n"
      "s_cbranch_vccnz FB0_%=\n"
      "RET0_2_%=:\n"
      "v_mfma_f32_16x16x32_bf16 a[180:183], a[124:127], v[212:215], a[180:183]\n"
      "v_exp_f32 v112, v160\n"
      "v_mfma_f32_16x16x32_bf16 a[164:167], a[120:123], v[212:215], a[164:167]\n"
      "v_xor_b32 v242, 0x10000, v242\n"
      "v_mfma_f32_16x16x32_bf16 a[192:195], a[196:199], v[192:195], a[192:195]\n"
      "s_add_u32 m0, s62, 0\n"
      "v_exp_f32 v113, v161\n"
      "v_mfma_f32_16x16x32_bf16 v[84:87], a[116:119], v[212:215], v[84:87]\n"
      "buffer_load_dwordx4 v248, s[20:23], s60 offen nt lds\n"
      "v_cvt_pk_bf16_f32 v208, v112, v113\n"
      "v_xor_b32 v243, 0x10000, v243\n"
      "v_mfma_f32_16x16x32_bf16 v[68:71], a[112:115], v[212:215], v[68:71]\n"
      "s_add_u32 m0, s62, 896\n"
      "v_exp_f32 v114, v162\n"
      "v_mfma_f32_16x16x32_bf16 a[192:195], a[200:203], v[196:199], a[192:195]\n"
      "buffer_load_dwordx4 v248, s[20:23], s60 offen offset:128 nt lds\n"
      "v_xor_b32 v244, 0x10000, v244\n"
      "v_mfma_f32_16x16x32_bf16 a[148:151], a[108:111], v[212:215], a[148:151]\n"
      "s_add_u32 m0, s62, 8192\n"
      "v_exp_f32 v115, v163\n"
      "v_mfma_f32_16x16x32_bf16 a[132:135], a[104:107], v[212:215], a[132:135]\n"
      "s_memrealtime s[68:69]\n"
      "buffer_load_dwordx4 v120, s[20:23], s60 offen nt lds\n"
      "v_cvt_pk_bf16_f32 v209, v114, v115\n"
      "v_xor_b32 v245, 0x10000, v245\n"
      "v_mfma_f32_16x16x32_bf16 a[192:195], a[204:207], v[200:203], a[192:195]\n"
      "s_add_u32 m0, s62, 9088\n"
      "v_exp_f32 v112, v164\n"
      "v_mfma_f32_16x16x32_bf16 v[52:55], a[100:103], v[212:215], v[52:55]\n"
      "s_add_u32 s62, s62, 0x8000\n"
      "buffer_load_dwordx4 v120, s[20:23], s60 offen offset:128 nt lds\n"
      "v_or_b32 v116, v208, v209\n"
      "v_xor_b32 v246, 0x10000, v246\n"
      "v_mfma_f32_16x16x32_bf16 v[36:39], a[96:99], v[212:215], v[36:39]\n"
      "s_and_b32 s62, s62, 0x1ffff\n"
      "v_exp_f32 v113, v165\n"
      "v_mfma_f32_16x16x32_bf16 a[192:195], a[208:211], v[204:207], a[192:195]\n"
      "s_add_u32 s60, s60, s78\n"
      "v_cvt_pk_bf16_f32 v210, v112, v113\n"
      "v_xor_b32 v247, 0x10000, v247\n"
      "s_waitcnt vmcnt(8)\n"
      "s_sub_u32 s63, 11, %[n1]\n"
      "LOOP2_%=:\n"
      "s_waitcnt vmcnt(9) lgkmcnt(0)\n"
      "s_sub_u32 s67, s68, s90\n"
      "s_cmp_lt_i32 s67, 0\n"
      "s_cbranch_scc1 GW4_%=\n"
      "GX4_%=:\n"
      "s_barrier\n"
      "v_mfma_f32_16x16x32_bf16 v[40:43], a[96:99], v[216:219], v[40:43]\n"
      "s_memrealtime s[68:69]\n"
      "ds_read_b128 a[64:67], v242 offset:0\n"
      "v_exp_f32 v114, v166\n"
      "v_mfma_f32_16x16x32_bf16 v[44:47], a[96:99], v[220:223], v[44:47]\n"
      "s_add_u32 m0, s77, 16384\n"
      "ds_read_b128 a[68:71], v242 offset:4096\n"
      "ds_read_b128 a[72:75], v243 offset:0\n"
      "v_mfma_f32_16x16x32_bf16 v[60:63], a[100:103], v[220:223], v[60:63]\n"
      "buffer_load_dwordx4 v249, s[24:27], s61 offen nt lds\n"
      "v_exp_f32 v115, v167\n"
      "v_mfma_f32_16x16x32_bf16 v[56:59], a[100:103], v[216:219], v[56:59]\n"
      "s_add_u32 m0, s77, 17408\n"
      "ds_read_b128 a[76:79], v243 offset:4096\n"
      "v_cvt_pk_bf16_f32 v211, v114, v115\n"
      "v_mfma_f32_16x16x32_bf16 a[136:139], a[104:107], v[216:219], a[136:139]\n"
      "buffer_load_dwordx4 v121, s[24:27], s61 offen nt lds\n"
      "v_exp_f32 v112, v168\n"
      "v_mfma_f32_16x16x32_bf16 a[140:143], a[104:107], v[220:223], a[140:143]\n"
      "s_add_u32 m0, s77, 24576\n"
      "ds_read_b128 a[80:83], v242 offset:1024\n"
      "v_or3_b32 v116, v116, v210, v211\n"
      "v_mfma_f32_16x16x32_bf16 a[156:159], a[108:111], v[220:223], a[156:159]\n"
      "buffer_load_dwordx4 v122, s[24:27], s61 offen nt lds\n"
      "v_exp_f32 v113, v169\n"
      "v_mfma_f32_16x16x32_bf16 a[152:155], a[108:111], v[216:219], a[152:155]\n"
      "s_add_u32 m0, s77, 25600\n"
      "ds_read_b128 a[84:87], v242 offset:5120\n"
      "v_cvt_pk_bf16_f32 v212, v112, v113\n"
      "v_mfma_f32_16x16x32_bf16 v[72:75], a[112:115], v[216:219], v[72:75]\n"
      "s_add_u32 s77, s77, 0x8000\n"
      "buffer_load_dwordx4 v123, s[24:27], s61 offen nt lds\n"
      "v_exp_f32 v114, v170\n"
      "v_mfma_f32_16x16x32_bf16 v[76:79], a[112:115], v[220:223], v[76:79]\n"
      "s_and_b32 s77, s77, 0x1ffff\n"
      "ds_read_b128 a[88:91], v243 offset:1024\n"
      "ds_read_b128 a[92:95], v243 offset:5120\n"
      "v_mfma_f32_16x16x32_bf16 v[92:95], a[116:119], v[220:223], v[92:95]\n"
      "s_add_u32 s61, s61, s78\n"
      "v_exp_f32 v115, v171\n"
      "v_mfma_f32_16x16x32_bf16 v[88:91], a[116:119], v[216:219], v[88:91]\n"
      "v_cvt_pk_bf16_f32 v213, v114, v115\n"
      "v_mfma_f32_16x16x32_bf16 a[168:171], a[120:123], v[216:219], a[168:171]\n"
      "v_exp_f32 v112, v172\n"
      "v_mfma_f32_16x16x32_bf16 a[172:175], a[120:123], v[220:223], a[172:175]\n"
      "v_or3_b32 v116, v116, v212, v213\n"
      "v_mfma_f32_16x16x32_bf16 a[188:191], a[124:127], v[220:223], a[188:191]\n"
      "v_exp_f32 v113, v173\n"
      "v_mfma_f32_16x16x32_bf16 a[184:187], a[124:127], v[216:219], a[184:187]\n"
      "s_waitcnt lgkmcnt(0)\n"
      "v_cvt_pk_bf16_f32 v214, v112, v113\n"
      "v_mfma_f32_16x16x32_bf16 v[128:131], a[64:67], a[0:3], v[224:227]\n"
      "s_sub_u32 s67, s68, s66\n"
      "v_exp_f32 v114, v174\n"
      "v_mfma_f32_16x16x32_bf16 v[128:131], a[72:75], a[16:19], v[128:131]\n"
      "s_cmp_gt_i32 s67, s70\n"
      "v_mfma_f32_16x16x32_bf16 v[128:131], a[80:83], a[32:35], v[128:131]\n"
      "s_cselect_b32 s69, s86, 0\n"
      "v_exp_f32 v115, v175\n"
      "v_mfma_f32_16x16x32_bf16 v[128:131], a[88:91], a[48:51], v[128:131]\n"
      "s_add_u32 s66, s66, s69\n"
      "v_cvt_pk_bf16_f32 v215, v114, v115\n"
      "v_mfma_f32_16x16x32_bf16 v[132:135], a[92:95], a[48:51], v[224:227]\n"
      "s_add_u32 s67, s67, s101\n"
      "v_exp_f32 v112, v176\n"
      "v_mfma_f32_16x16x32_bf16 v[132:135], a[84:87], a[32:35], v[132:135]\n"
      "s_min_i32 s70, s70, s67\n"
      "v_or3_b32 v116, v116, v214, v215\n"
      "v_mfma_f32_16x16x32_bf16 v[132:135], a[76:79], a[16:19], v[132:135]\n"
      "s_max_i32 s70, s70, 85\n"
      "v_exp_f32 v113, v177\n"
      "v_mfma_f32_16x16x32_bf16 v[132:135], a[68:71], a[0:3], v[132:135]\n"
      "s_add_u32 s66, s66, s86\n"
      "v_cvt_pk_bf16_f32 v216, v112, v113\n"
      "v_mfma_f32_16x16x32_bf16 v[140:143], a[68:71], a[4:7], v[228:231]\n"
      "s_and_b32 s76, s80, 31\n"
      "v_exp_f32 v114, v178\n"
      "v_mfma_f32_16x16x32_bf16 v[140:143], a[76:79], a[20:23], v[140:143]\n"
      "s_lshr_b64 s[80:81], s[80:81], 5\n"
      "v_mfma_f32_16x16x32_bf16 v[140:143], a[84:87], a[36:39], v[140:143]\n"
      "s_add_u32 s66, s66, s76\n"
      "v_exp_f32 v115, v179\n"
      "v_mfma_f32_16x16x32_bf16 v[140:143], a[92:95], a[52:55], v[140:143]\n"
      "s_sub_u32 s67, s68, s82\n"
      "v_cvt_pk_bf16_f32 v217, v114, v115\n"
      "v_mfma_f32_16x16x32_bf16 v[136:139], a[88:91], a[52:55], v[228:231]\n"
      "s_min_i32 s76, s67, s83\n"
      "v_exp_f32 v112, v180\n"
      "s_waitcnt vmcnt(8)\n"
      "s_barrier\n"
      "v_mfma_f32_16x16x32_bf16 v[136:139], a[80:83], a[36:39], v[136:139]\n"
      "s_mov_b32 s83, s67\n"
      "ds_read_b64_tr_b16 a[96:97], v244 offset:0\n"
      "v_or3_b32 v116, v116, v216, v217\n"
      "v_mfma_f32_16x16x32_bf16 v[136:139], a[72:75], a[20:23], v[136:139]\n"
      "s_sub_u32 s76, s76, 36\n"
      "v_exp_f32 v113, v181\n"
      "v_mfma_f32_16x16x32_bf16 v[136:139], a[64:67], a[4:7], v[136:139]\n"
      "s_min_i32 s76, s76, 148\n"
      "ds_read_b64_tr_b16 a[98:99], v244 offset:4096\n"
      "v_cvt_pk_bf16_f32 v218, v112, v113\n"
      "v_mfma_f32_16x16x32_bf16 v[144:147], a[64:67], a[8:11], v[232:235]\n"
      "s_max_i32 s76, s76, 70\n"
      "v_exp_f32 v114, v182\n"
      "v_mfma_f32_16x16x32_bf16 v[144:147], a[72:75], a[24:27], v[144:147]\n"
      "s_mov_b32 s82, s68\n"
      "ds_read_b64_tr_b16 a[100:101], v245 offset:0\n"
      "ds_read_b64_tr_b16 a[102:103], v245 offset:4096\n"
      "v_mfma_f32_16x16x32_bf16 v[144:147], a[80:83], a[40:43], v[144:147]\n"
      "s_add_u32 s65, s82, s76\n"
      "v_exp_f32 v115, v183\n"
      "v_mfma_f32_16x16x32_bf16 v[144:147], a[88:91], a[56:59], v[144:147]\n"
      "s_sub_u32 s93, s93, 1\n"
      "ds_read_b64_tr_b16 a[104:105], v246 offset:0\n"
      "v_cvt_pk_bf16_f32 v219, v114, v115\n"
      "v_mfma_f32_16x16x32_bf16 v[148:151], a[92:95], a[56:59], v[232:235]\n"
      "s_cmp_le_i32 s93, 14\n"
      "v_exp_f32 v112, v184\n"
      "v_mfma_f32_16x16x32_bf16 v[148:151], a[84:87], a[40:43], v[148:151]\n"
      "s_cselect_b32 s86, 107, s86\n"
      "ds_read_b64_tr_b16 a[106:107], v246 offset:4096\n"
      "v_or3_b32 v116, v116, v218, v219\n"
      "v_mfma_f32_16x16x32_bf16 v[148:151], a[76:79], a[24:27], v[148:151]\n"
      "s_sub_u32 s67, s65, s66\n"
      "v_exp_f32 v113, v185\n"
      "v_mfma_f32_16x16x32_bf16 v[148:151], a[68:71], a[8:11], v[148:151]\n"
      "s_cmp_lt_i32 s67, 0\n"
      "ds_read_b64_tr_b16 a[108:109], v247 offset:0\n"
      "v_cvt_pk_bf16_f32 v220, v112, v113\n"
      "v_mfma_f32_16x16x32_bf16 v[156:159], a[68:71], a[12:15], v[236:239]\n"
      "s_cselect_b32 s88, s66, s65\n"
      "v_exp_f32 v114, v186\n"
      "v_mfma_f32_16x16x32_bf16 v[156:159], a[76:79], a[28:31], v[156:159]\n"
      "s_add_u32 s90, s88, s91\n"
      "ds_read_b64_tr_b16 a[110:111], v247 offset:4096\n"
      "ds_read_b64_tr_b16 a[112:113], v244 offset:1024\n"
      "v_mfma_f32_16x16x32_bf16 v[156:159], a[84:87], a[44:47], v[156:159]\n"
      "v_exp_f32 v115, v187\n"
      "v_mfma_f32_16x16x32_bf16 v[156:159], a[92:95], a[60:63], v[156:159]\n"
      "ds_read_b64_tr_b16 a[114:115], v244 offset:5120\n"
      "v_cvt_pk_bf16_f32 v221, v114, v115\n"
      "v_mfma_f32_16x16x32_bf16 v[152:155], a[88:91], a[60:63], v[236:239]\n"
      "v_exp_f32 v112, v188\n"
      "v_mfma_f32_16x16x32_bf16 v[152:155], a[80:83], a[44:47], v[152:155]\n"
      "ds_read_b64_tr_b16 a[116:117], v245 offset:1024\n"
      "v_or3_b32 v116, v116, v220, v221\n"
      "v_mfma_f32_16x16x32_bf16 v[152:155], a[72:75], a[28:31], v[152:155]\n"
      "v_exp_f32 v113, v189\n"
      "v_mfma_f32_16x16x32_bf16 v[152:155], a[64:67], a[12:15], v[152:155]\n"
      "ds_read_b64_tr_b16 a[118:119], v245 offset:5120\n"
      "v_cvt_pk_bf16_f32 v222, v112, v113\n"
      "s_waitcnt lgkmcnt(6)\n"
      "v_mfma_f32_16x16x32_bf16 v[32:35], a[96:99], v[192:195], v[32:35]\n"
      "v_exp_f32 v114, v190\n"
      "v_mfma_f32_16x16x32_bf16 v[48:51], a[100:103], v[192:195], v[48:51]\n"
      "ds_read_b64_tr_b16 a[120:121], v246 offset:1024\n"
      "ds_read_b64_tr_b16 a[122:123], v246 offset:5120\n"
      "v_mfma_f32_16x16x32_bf16 a[128:131], a[104:107], v[192:195], a[128:131]\n"
      "s_waitcnt lgkmcnt(4)\n"
      "v_exp_f32 v115, v191\n"
      "v_mfma_f32_16x16x32_bf16 a[144:147], a[108:111], v[192:195], a[144:147]\n"
      "ds_read_b64_tr_b16 a[124:125], v247 offset:1024\n"
      "v_cvt_pk_bf16_f32 v223, v114, v115\n"
      "v_mfma_f32_16x16x32_bf16 v[64:67], a[112:115], v[192:195], v[64:67]\n"
      "ds_read_b64_tr_b16 a[126:127], v247 offset:5120\n"
      "s_waitcnt lgkmcnt(2)\n"
      "v_mfma_f32_16x16x32_bf16 v[80:83], a[116:119], v[192:195], v[80:83]\n"
      "v_or3_b32 v116, v116, v222, v223\n"
      "v_mfma_f32_16x16x32_bf16 a[160:163], a[120:123], v[192:195], a[160:163]\n"
      "s_mov_b32 s64, 3\n"
      "s_waitcnt lgkmcnt(0)\n"
      "v_and_b32 v116, 0x40004000, v116\n"
      "v_mfma_f32_16x16x32_bf16 a[176:179], a[124:127], v[192:195], a[176:179]\n"
      "v_cmp_ne_u32 vcc, 0, v116\n"
      "s_cbranch_vccnz FB1_%=\n"
      "RET1_3_%=:\n"
      "v_mfma_f32_16x16x32_bf16 a[180:183], a[124:127], v[196:199], a[180:183]\n"
      "v_exp_f32 v112, v128\n"
      "v_mfma_f32_16x16x32_bf16 a[164:167], a[120:123], v[196:199], a[164:167]\n"
      "v_mfma_f32_16x16x32_bf16 a[192:195], a[196:199], v[208:211], a[192:195]\n"
      "s_add_u32 m0, s62, 0\n"
      "v_exp_f32 v113, v129\n"
      "v_mfma_f32_16x16x32_bf16 v[84:87], a[116:119], v[196:199], v[84:87]\n"
      "buffer_load_dwordx4 v248, s[20:23], s60 offen nt lds\n"
      "v_cvt_pk_bf16_f32 v192, v112, v113\n"
      "v_mfma_f32_16x16x32_bf16 v[68:71], a[112:115], v[196:199], v[68:71]\n"
      "s_add_u32 m0, s62, 896\n"
      "v_exp_f32 v114, v130\n"
      "v_mfma_f32_16x16x32_bf16 a[192:195], a[200:203], v[212:215], a[192:195]\n"
      "buffer_load_dwordx4 v248, s[20:23], s60 offen offset:128 nt lds\n"
      "v_mfma_f32_16x16x32_bf16 a[148:151], a[108:111], v[196:199], a[148:151]\n"
      "s_add_u32 m0, s62, 8192\n"
      "v_exp_f32 v115, v131\n"
      "v_mfma_f32_16x16x32_bf16 a[132:135], a[104:107], v[196:199], a[132:135]\n"
      "s_memrealtime s[68:69]\n"
      "buffer_load_dwordx4 v120, s[20:23], s60 offen nt lds\n"
      "v_cvt_pk_bf16_f32 v193, v114, v115\n"
      "v_mfma_f32_16x16x32_bf16 a[192:195], a[204:207], v[216:219], a[192:195]\n"
      "s_add_u32 m0, s62, 9088\n"
      "v_exp_f32 v112, v132\n"
      "v_mfma_f32_16x16x32_bf16 v[52:55], a[100:103], v[196:199], v[52:55]\n"
      "s_add_u32 s62, s62, 0x8000\n"
      "buffer_load_dwordx4 v120, s[20:23], s60 offen offset:128 nt lds\n"
      "v_or_b32 v116, v192, v193\n"
      "v_mfma_f32_16x16x32_bf16 v[36:39], a[96:99], v[196:199], v[36:39]\n"
      "s_and_b32 s62, s62, 0x1ffff\n"
      "v_exp_f32 v113, v133\n"
      "v_mfma_f32_16x16x32_bf16 a[192:195], a[208:211], v[220:223], a[192:195]\n"
      "s_add_u32 s60, s60, %[inc]\n"
      "v_cvt_pk_bf16_f32 v194, v112, v113\n"
      "s_waitcnt vmcnt(8) lgkmcnt(0)\n"
      "s_sub_u32 s67, s68, s90\n"
      "s_cmp_lt_i32 s67, 0\n"
      "s_cbranch_scc1 GW5_%=\n"
      "GX5_%=:\n"
      "s_barrier\n"
      "v_mfma_f32_16x16x32_bf16 v[40:43], a[96:99], v[200:203], v[40:43]\n"
      "s_memrealtime s[68:69]\n"
      "buffer_load_dword v250, v251, s[28:31], 0 offen sc1\n"
      "ds_read_b128 a[64:67], v242 offset:32768\n"
      "v_exp_f32 v114, v134\n"
      "v_mfma_f32_16x16x32_bf16 v[44:47], a[96:99], v[204:207], v[44:47]\n"
      "s_add_u32 m0, s77, 16384\n"
      "ds_read_b128 a[68:71], v242 offset:36864\n"
      "ds_read_b128 a[72:75], v243 offset:32768\n"
      "v_mfma_f32_16x16x32_bf16 v[60:63], a[100:103], v[204:207], v[60:63]\n"
      "buffer_load_dwordx4 v249, s[24:27], s61 offen nt lds\n"
      "v_exp_f32 v115, v135\n"
      "v_mfma_f32_16x16x32_bf16 v[56:59], a[100:103], v[200:203], v[56:59]\n"
      "s_add_u32 m0, s77, 17408\n"
      "ds_read_b128 a[76:79], v243 offset:36864\n"
      "v_cvt_pk_bf16_f32 v195, v114, v115\n"
      "v_mfma_f32_16x16x32_bf16 a[136:139], a[104:107], v[200:203], a[136:139]\n"
      "buffer_load_dwordx4 v121, s[24:27], s61 offen nt lds\n"
      "v_exp_f32 v112, v136\n"
      "v_mfma_f32_16x16x32_bf16 a[140:143], a[104:107], v[204:207], a[140:143]\n"
      "s_add_u32 m0, s77, 24576\n"
      "ds_read_b128 a[80:83], v242 offset:33792\n"
      "v_or3_b32 v116, v116, v194, v195\n"
      "v_mfma_f32_16x16x32_bf16 a[156:159], a[108:111], v[204:207], a[156:159]\n"
      "buffer_load_dwordx4 v122, s[24:27], s61 offen nt lds\n"
      "v_exp_f32 v113, v137\n"
      "v_mfma_f32_16x16x32_bf16 a[152:155], a[108:111], v[200:203], a[152:155]\n"
      "s_add_u32 m0, s77, 25600\n"
      "ds_read_b128 a[84:87], v242 offset:37888\n"
      "v_cvt_pk_bf16_f32 v196, v112, v113\n"
      "v_mfma_f32_16x16x32_bf16 v[72:75], a[112:115], v[200:203], v[72:75]\n"
      "s_add_u32 s77, s77, 0x8000\n"
      "buffer_load_dwordx4 v123, s[24:27], s61 offen nt lds\n"
      "v_exp_f32 v114, v138\n"
      "v_mfma_f32_16x16x32_bf16 v[76:79], a[112:115], v[204:207], v[76:79]\n"
      "s_and_b32 s77, s77, 0x1ffff\n"
      "ds_read_b128 a[88:91], v243 offset:33792\n"
      "ds_read_b128 a[92:95], v243 offset:37888\n"
      "v_mfma_f32_16x16x32_bf16 v[92:95], a[116:119], v[204:207], v[92:95]\n"
      "s_add_u32 s61, s61, %[inc]\n"
      "v_exp_f32 v115, v139\n"
      "v_mfma_f32_16x16x32_bf16 v[88:91], a[116:119], v[200:203], v[88:91]\n"
      "v_cvt_pk_bf16_f32 v197, v114, v115\n"
      "v_mfma_f32_16x16x32_bf16 a[168:171], a[120:123], v[200:203], a[168:171]\n"
      "v_exp_f32 v112, v140\n"
      "v_mfma_f32_16x16x32_bf16 a[172:175], a[120:123], v[204:207], a[172:175]\n"
      "v_or3_b32 v116, v116, v196, v197\n"
      "v_mfma_f32_16x16x32_bf16 a[188:191], a[124:127], v[204:207], a[188:191]\n"
      "v_exp_f32 v113, v141\n"
      "v_mfma_f32_16x16x32_bf16 a[184:187], a[124:127], v[200:203], a[184:187]\n"
      "s_waitcnt lgkmcnt(0)\n"
      "v_cvt_pk_bf16_f32 v198, v112, v113\n"
      "v_mfma_f32_16x16x32_bf16 v[160:163], a[64:67], a[0:3], v[224:227]\n"
      "s_sub_u32 s67, s68, s66\n"
      "v_exp_f32 v114, v142\n"
      "v_mfma_f32_16x16x32_bf16 v[160:163], a[72:75], a[16:19], v[160:163]\n"
      "s_cmp_gt_i32 s67, s70\n"
      "v_mfma_f32_16x16x32_bf16 v[160:163], a[80:83], a[32:35], v[160:163]\n"
      "s_cselect_b32 s69, s86, 0\n"
      "v_exp_f32 v115, v143\n"
      "v_mfma_f32_16x16x32_bf16 v[160:163], a[88:91], a[48:51], v[160:163]\n"
      "s_add_u32 s66, s66, s69\n"
      "v_cvt_pk_bf16_f32 v199, v114, v115\n"
      "v_mfma_f32_16x16x32_bf16 v[164:167], a[92:95], a[48:51], v[224:227]\n"
      "s_add_u32 s67, s67, s101\n"
      "v_exp_f32 v112, v144\n"
      "v_mfma_f32_16x16x32_bf16 v[164:167], a[84:87], a[32:35], v[164:167]\n"
      "s_min_i32 s70, s70, s67\n"
      "v_or3_b32 v116, v116, v198, v199\n"
      "v_mfma_f32_16x16x32_bf16 v[164:167], a[76:79], a[16:19], v[164:167]\n"
      "s_max_i32 s70, s70, 85\n"
      "v_exp_f32 v113, v145\n"
      "v_mfma_f32_16x16x32_bf16 v[164:167], a[68:71], a[0:3], v[164:167]\n"
      "s_add_u32 s66, s66, s86\n"
      "v_cvt_pk_bf16_f32 v200, v112, v113\n"
      "v_mfma_f32_16x16x32_bf16 v[172:175], a[68:71], a[4:7], v[228:231]\n"
      "s_and_b32 s76, s80, 31\n"
      "v_exp_f32 v114, v146\n"
      "v_mfma_f32_16x16x32_bf16 v[172:175], a[76:79], a[20:23], v[172:175]\n"
      "s_lshr_b64 s[80:81], s[80:81], 5\n"
      "v_mfma_f32_16x16x32_bf16 v[172:175], a[84:87], a[36:39], v[172:175]\n"
      "s_add_u32 s66, s66, s76\n"
      "v_exp_f32 v115, v147\n"
      "v_mfma_f32_16x16x32_bf16 v[172:175], a[92:95], a[52:55], v[172:175]\n"
      "s_sub_u32 s67, s68, s82\n"
      "v_cvt_pk_bf16_f32 v201, v114, v115\n"
      "v_mfma_f32_16x16x32_bf16 v[168:171], a[88:91], a[52:55], v[228:231]\n"
      "s_min_i32 s76, s67, s83\n"
      "v_exp_f32 v112, v148\n"
      "s_waitcnt vmcnt(9)\n"
      "s_barrier\n"
      "v_mfma_f32_16x16x32_bf16 v[168:171], a[80:83], a[36:39], v[168:171]\n"
      "s_mov_b32 s83, s67\n"
      "ds_read_b64_tr_b16 a[96:97], v244 offset:32768\n"
      "v_or3_b32 v116, v116, v200, v201\n"
      "v_mfma_f32_16x16x32_bf16 v[168:171], a[72:75], a[20:23], v[168:171]\n"
      "s_sub_u32 s76, s76, 36\n"
      "v_exp_f32 v113, v149\n"
      "v_mfma_f32_16x16x32_bf16 v[168:171], a[64:67], a[4:7], v[168:171]\n"
      "s_min_i32 s76, s76, 148\n"
      "ds_read_b64_tr_b16 a[98:99], v244 offset:36864\n"
      "v_cvt_pk_bf16_f32 v202, v112, v113\n"
      "v_mfma_f32_16x16x32_bf16 v[176:179], a[64:67], a[8:11], v[232:235]\n"
      "s_max_i32 s76, s76, 70\n"
      "v_exp_f32 v114, v150\n"
      "v_mfma_f32_16x16x32_bf16 v[176:179], a[72:75], a[24:27], v[176:179]\n"
      "s_mov_b32 s82, s68\n"
      "ds_read_b64_tr_b16 a[100:101], v245 offset:32768\n"
      "ds_read_b64_tr_b16 a[102:103], v245 offset:36864\n"
      "v_mfma_f32_16x16x32_bf16 v[176:179], a[80:83], a[40:43], v[176:179]\n"
      "s_add_u32 s65, s82, s76\n"
      "v_exp_f32 v115, v151\n"
      "v_mfma_f32_16x16x32_bf16 v[176:179], a[88:91], a[56:59], v[176:179]\n"
      "s_sub_u32 s93, s93, 1\n"
      "ds_read_b64_tr_b16 a[104:105], v246 offset:32768\n"
      "v_cvt_pk_bf16_f32 v203, v114, v115\n"
      "v_mfma_f32_16x16x32_bf16 v[180:183], a[92:95], a[56:59], v[232:235]\n"
      "s_cmp_le_i32 s93, 14\n"
      "v_exp_f32 v112, v152\n"
      "v_mfma_f32_16x16x32_bf16 v[180:183], a[84:87], a[40:43], v[180:183]\n"
      "s_cselect_b32 s86, 107, s86\n"
      "ds_read_b64_tr_b16 a[106:107], v246 offset:36864\n"
      "v_or3_b32 v116, v116, v202, v203\n"
      "v_mfma_f32_16x16x32_bf16 v[180:183], a[76:79], a[24:27], v[180:183]\n"
      "s_sub_u32 s67, s65, s66\n"
      "v_exp_f32 v113, v153\n"
      "v_mfma_f32_16x16x32_bf16 v[180:183], a[68:71], a[8:11], v[180:183]\n"
      "s_cmp_lt_i32 s67, 0\n"
      "ds_read_b64_tr_b16 a[108:109], v247 offset:32768\n"
      "v_cvt_pk_bf16_f32 v204, v112, v113\n"
      "v_mfma_f32_16x16x32_bf16 v[188:191], a[68:71], a[12:15], v[236:239]\n"
      "s_cselect_b32 s88, s66, s65\n"
      "v_exp_f32 v114, v154\n"
      "v_mfma_f32_16x16x32_bf16 v[188:191], a[76:79], a[28:31], v[188:191]\n"
      "s_add_u32 s90, s88, s91\n"
      "ds_read_b64_tr_b16 a[110:111], v247 offset:36864\n"
      "ds_read_b64_tr_b16 a[112:113], v244 offset:33792\n"
      "v_mfma_f32_16x16x32_bf16 v[188:191], a[84:87], a[44:47], v[188:191]\n"
      "v_exp_f32 v115, v155\n"
      "v_mfma_f32_16x16x32_bf16 v[188:191], a[92:95], a[60:63], v[188:191]\n"
      "ds_read_b64_tr_b16 a[114:115], v244 offset:37888\n"
      "v_cvt_pk_bf16_f32 v205, v114, v115\n"
      "v_mfma_f32_16x16x32_bf16 v[184:187], a[88:91], a[60:63], v[236:239]\n"
      "v_exp_f32 v112, v156\n"
      "v_mfma_f32_16x16x32_bf16 v[184:187], a[80:83], a[44:47], v[184:187]\n"
      "ds_read_b64_tr_b16 a[116:117], v245 offset:33792\n"
      "v_or3_b32 v116, v116, v204, v205\n"
      "v_mfma_f32_16x16x32_bf16 v[184:187], a[72:75], a[28:31], v[184:187]\n"
      "v_exp_f32 v113, v157\n"
      "v_mfma_f32_16x16x32_bf16 v[184:187], a[64:67], a[12:15], v[184:187]\n"
      "ds_read_b64_tr_b16 a[118:119], v245 offset:37888\n"
      "v_cvt_pk_bf16_f32 v206, v112, v113\n"
      "s_waitcnt lgkmcnt(6)\n"
      "v_mfma_f32_16x16x32_bf16 v[32:35], a[96:99], v[208:211], v[32:35]\n"
      "v_exp_f32 v114, v158\n"
      "v_mfma_f32_16x16x32_bf16 v[48:51], a[100:103], v[208:211], v[48:51]\n"
      "ds_read_b64_tr_b16 a[120:121], v246 offset:33792\n"
      "ds_read_b64_tr_b16 a[122:123], v246 offset:37888\n"
      "v_mfma_f32_16x16x32_bf16 a[128:131], a[104:107], v[208:211], a[128:131]\n"
      "s_waitcnt lgkmcnt(4)\n"
      "v_exp_f32 v115, v159\n"
      "v_mfma_f32_16x16x32_bf16 a[144:147], a[108:111], v[208:211], a[144:147]\n"
      "ds_read_b64_tr_b16 a[124:125], v247 offset:33792\n"
      "v_cvt_pk_bf16_f32 v207, v114, v115\n"
      "v_mfma_f32_16x16x32_bf16 v[64:67], a[112:115], v[208:211], v[64:67]\n"
      "ds_read_b64_tr_b16 a[126:127], v247 offset:37888\n"
      "s_waitcnt lgkmcnt(2)\n"
      "v_mfma_f32_16x16x32_bf16 v[80:83], a[116:119], v[208:211], v[80:83]\n"
      "v_or3_b32 v116, v116, v206, v207\n"
      "v_mfma_f32_16x16x32_bf16 a[160:163], a[120:123], v[208:211], a[160:163]\n"
      "s_mov_b32 s64, 3\n"
      "s_waitcnt lgkmcnt(0)\n"
      "v_and_b32 v116, 0x40004000, v116\n"
      "v_mfma_f32_16x16x32_bf16 a[176:179], a[124:127], v[208:211], a[176:179]\n"
      "v_cmp_ne_u32 vcc, 0, v116\n"
      "s_cbranch_vccnz FB0_%=\n"
      "RET0_3_%=:\n"
      "v_mfma_f32_16x16x32_bf16 a[180:183], a[124:127], v[212:215], a[180:183]\n"
      "v_exp_f32 v112, v160\n"
      "v_mfma_f32_16x16x32_bf16 a[164:167], a[120:123], v[212:215], a[164:167]\n"
      "v_xor_b32 v242, 0x10000, v242\n"
      "v_mfma_f32_16x16x32_bf16 a[192:195], a[196:199], v[192:195], a[192:195]\n"
      "s_add_u32 m0, s62, 0\n"
      "v_exp_f32 v113, v161\n"
      "v_mfma_f32_16x16x32_bf16 v[84:87], a[116:119], v[212:215], v[84:87]\n"
      "buffer_load_dwordx4 v248, s[20:23], s60 offen nt lds\n"
      "v_cvt_pk_bf16_f32 v208, v112, v113\n"
      "v_xor_b32 v243, 0x10000, v243\n"
      "v_mfma_f32_16x16x32_bf16 v[68:71], a[112:115], v[212:215], v[68:71]\n"
      "s_add_u32 m0, s62, 896\n"
      "v_exp_f32 v114, v162\n"
      "v_mfma_f32_16x16x32_bf16 a[192:195], a[200:203], v[196:199], a[192:195]\n"
      "buffer_load_dwordx4 v248, s[20:23], s60 offen offset:128 nt lds\n"
      "v_xor_b32 v244, 0x10000, v244\n"
      "v_mfma_f32_16x16x32_bf16 a[148:151], a[108:111], v[212:215], a[148:151]\n"
      "s_add_u32 m0, s62, 8192\n"
      "v_exp_f32 v115, v163\n"
      "v_mfma_f32_16x16x32_bf16 a[132:135], a[104:107], v[212:215], a[132:135]\n"
      "s_memrealtime s[68:69]\n"
      "buffer_load_dwordx4 v120, s[20:23], s60 offen nt lds\n"
      "v_cvt_pk_bf16_f32 v209, v114, v115\n"
      "v_xor_b32 v245, 0x10000, v245\n"
      "v_mfma_f32_16x16x32_bf16 a[192:195], a[204:207], v[200:203], a[192:195]\n"
      "s_add_u32 m0, s62, 9088\n"
      "v_exp_f32 v112, v164\n"
      "v_mfma_f32_16x16x32_bf16 v[52:55], a[100:103], v[212:215], v[52:55]\n"
      "s_add_u32 s62, s62, 0x8000\n"
      "buffer_load_dwordx4 v120, s[20:23], s60 offen offset:128 nt lds\n"
      "v_or_b32 v116, v208, v209\n"
      "v_xor_b32 v246, 0x10000, v246\n"
      "v_mfma_f32_16x16x32_bf16 v[36:39], a[96:99], v[212:215], v[36:39]\n"
      "s_and_b32 s62, s62, 0x1ffff\n"
      "s_add_u32 s60, s60, s78\n"
      "v_exp_f32 v113, v165\n"
      "v_mfma_f32_16x16x32_bf16 a[192:195], a[208:211], v[204:207], a[192:195]\n"
      "s_sub_u32 s63, s63, 1\n"
      "v_cvt_pk_bf16_f32 v210, v112, v113\n"
      "v_xor_b32 v247, 0x10000, v247\n"
      "s_cbranch_scc0 LOOP2_%=\n"
      "s_waitcnt vmcnt(9) lgkmcnt(0)\n"
      "s_sub_u32 s67, s68, s90\n"
      "s_cmp_lt_i32 s67, 0\n"
      "s_cbranch_scc1 GW6_%=\n"
      "GX6_%=:\n"
      "s_barrier\n"
      "v_mfma_f32_16x16x32_bf16 v[40:43], a[96:99], v[216:219], v[40:43]\n"
      "s_memrealtime s[68:69]\n"
      "ds_read_b128 a[64:67], v242 offset:0\n"
      "v_exp_f32 v114, v166\n"
      "v_mfma_f32_16x16x32_bf16 v[44:47], a[96:99], v[220:223], v[44:47]\n"
      "s_add_u32 m0, %[wb], 50176\n"
      "ds_read_b128 a[68:71], v242 offset:4096\n"
      "ds_read_b128 a[72:75], v243 offset:0\n"
      "v_mfma_f32_16x16x32_bf16 v[60:63], a[100:103], v[220:223], v[60:63]\n"
      "s_add_u32 s94, s61, s78\n"
      "buffer_load_dwordx4 v121, s[24:27], s61 offen nt lds\n"
      "v_exp_f32 v115, v167\n"
      "v_mfma_f32_16x16x32_bf16 v[56:59], a[100:103], v[216:219], v[56:59]\n"
      "s_add_u32 m0, %[wb], 58368\n"
      "ds_read_b128 a[76:79], v243 offset:4096\n"
      "v_cvt_pk_bf16_f32 v211, v114, v115\n"
      "v_mfma_f32_16x16x32_bf16 a[136:139], a[104:107], v[216:219], a[136:139]\n"
      "s_add_u32 s95, s61, 0x10000\n"
      "buffer_load_dwordx4 v123, s[24:27], s61 offen nt lds\n"
      "v_exp_f32 v112, v168\n"
      "v_mfma_f32_16x16x32_bf16 a[140:143], a[104:107], v[220:223], a[140:143]\n"
      "s_add_u32 s77, s94, 0x10000\n"
      "ds_read_b128 a[80:83], v242 offset:1024\n"
      "v_or3_b32 v116, v116, v210, v211\n"
      "v_mfma_f32_16x16x32_bf16 a[156:159], a[108:111], v[220:223], a[156:159]\n"
      "s_add_u32 s78, s61, 0x20000\n"
      "v_exp_f32 v113, v169\n"
      "v_mfma_f32_16x16x32_bf16 a[152:155], a[108:111], v[216:219], a[152:155]\n"
      "ds_read_b128 a[84:87], v242 offset:5120\n"
      "v_cvt_pk_bf16_f32 v212, v112, v113\n"
      "v_mfma_f32_16x16x32_bf16 v[72:75], a[112:115], v[216:219], v[72:75]\n"
      "v_exp_f32 v114, v170\n"
      "v_mfma_f32_16x16x32_bf16 v[76:79], a[112:115], v[220:223], v[76:79]\n"
      "ds_read_b128 a[88:91], v243 offset:1024\n"
      "ds_read_b128 a[92:95], v243 offset:5120\n"
      "v_mfma_f32_16x16x32_bf16 v[92:95], a[116:119], v[220:223], v[92:95]\n"
      "v_exp_f32 v115, v171\n"
      "v_mfma_f32_16x16x32_bf16 v[88:91], a[116:119], v[216:219], v[88:91]\n"
      "v_cvt_pk_bf16_f32 v213, v114, v115\n"
      "v_mfma_f32_16x16x32_bf16 a[168:171], a[120:123], v[216:219], a[168:171]\n"
      "v_exp_f32 v112, v172\n"
      "v_mfma_f32_16x16x32_bf16 a[172:175], a[120:123], v[220:223], a[172:175]\n"
      "v_or3_b32 v116, v116, v212, v213\n"
      "v_mfma_f32_16x16x32_bf16 a[188:191], a[124:127], v[220:223], a[188:191]\n"
      "v_exp_f32 v113, v173\n"
      "v_mfma_f32_16x16x32_bf16 a[184:187], a[124:127], v[216:219], a[184:187]\n"
      "s_waitcnt lgkmcnt(0)\n"
      "v_cvt_pk_bf16_f32 v214, v112, v113\n"
      "v_mfma_f32_16x16x32_bf16 v[128:131], a[64:67], a[0:3], v[224:227]\n"
      "s_sub_u32 s67, s68, s66\n"
      "v_exp_f32 v114, v174\n"
      "v_mfma_f32_16x16x32_bf16 v[128:131], a[72:75], a[16:19], v[128:131]\n"
      "s_cmp_gt_i32 s67, s70\n"
      "v_mfma_f32_16x16x32_bf16 v[128:131], a[80:83], a[32:35], v[128:131]\n"
      "s_cselect_b32 s69, s86, 0\n"
      "v_exp_f32 v115, v175\n"
      "v_mfma_f32_16x16x32_bf16 v[128:131], a[88:91], a[48:51], v[128:131]\n"
      "s_add_u32 s66, s66, s69\n"
      "v_cvt_pk_bf16_f32 v215, v114, v115\n"
      "v_mfma_f32_16x16x32_bf16 v[132:135], a[92:95], a[48:51], v[224:227]\n"
      "s_add_u32 s67, s67, s101\n"
      "v_exp_f32 v112, v176\n"
      "v_mfma_f32_16x16x32_bf16 v[132:135], a[84:87], a[32:35], v[132:135]\n"
      "s_min_i32 s70, s70, s67\n"
      "v_or3_b32 v116, v116, v214, v215\n"
      "v_mfma_f32_16x16x32_bf16 v[132:135], a[76:79], a[16:19], v[132:135]\n"
      "s_max_i32 s70, s70, 85\n"
      "v_exp_f32 v113, v177\n"
      "v_mfma_f32_16x16x32_bf16 v[132:135], a[68:71], a[0:3], v[132:135]\n"
      "s_add_u32 s66, s66, s86\n"
      "v_cvt_pk_bf16_f32 v216, v112, v113\n"
      "v_mfma_f32_16x16x32_bf16 v[140:143], a[68:71], a[4:7], v[228:231]\n"
      "s_and_b32 s76, s80, 31\n"
      "v_exp_f32 v114, v178\n"
      "v_mfma_f32_16x16x32_bf16 v[140:143], a[76:79], a[20:23], v[140:143]\n"
      "s_lshr_b64 s[80:81], s[80:81], 5\n"
      "v_mfma_f32_16x16x32_bf16 v[140:143], a[84:87], a[36:39], v[140:143]\n"
      "s_add_u32 s66, s66, s76\n"
      "v_exp_f32 v115, v179\n"
      "v_mfma_f32_16x16x32_bf16 v[140:143], a[92:95], a[52:55], v[140:143]\n"
      "s_sub_u32 s67, s68, s82\n"
      "v_cvt_pk_bf16_f32 v217, v114, v115\n"
      "v_mfma_f32_16x16x32_bf16 v[136:139], a[88:91], a[52:55], v[228:231]\n"
      "s_min_i32 s76, s67, s83\n"
      "v_exp_f32 v112, v180\n"
      "s_waitcnt vmcnt(6)\n"
      "s_barrier\n"
      "v_mfma_f32_16x16x32_bf16 v[136:139], a[80:83], a[36:39], v[136:139]\n"
      "s_mov_b32 s83, s67\n"
      "ds_read_b64_tr_b16 a[96:97], v244 offset:0\n"
      "v_or3_b32 v116, v116, v216, v217\n"
      "v_mfma_f32_16x16x32_bf16 v[136:139], a[72:75], a[20:23], v[136:139]\n"
      "s_add_u32 m0, s62, 0\n"
      "v_exp_f32 v113, v181\n"
      "v_mfma_f32_16x16x32_bf16 v[136:139], a[64:67], a[4:7], v[136:139]\n"
      "s_sub_u32 s76, s76, 36\n"
      "ds_read_b64_tr_b16 a[98:99], v244 offset:4096\n"
      "buffer_load_dwordx4 v248, s[20:23], s60 offen nt lds\n"
      "v_cvt_pk_bf16_f32 v218, v112, v113\n"
      "v_mfma_f32_16x16x32_bf16 v[144:147], a[64:67], a[8:11], v[232:235]\n"
      "s_add_u32 m0, s62, 896\n"
      "v_exp_f32 v114, v182\n"
      "v_mfma_f32_16x16x32_bf16 v[144:147], a[72:75], a[24:27], v[144:147]\n"
      "s_min_i32 s76, s76, 148\n"
      "ds_read_b64_tr_b16 a[100:101], v245 offset:0\n"
      "ds_read_b64_tr_b16 a[102:103], v245 offset:4096\n"
      "buffer_load_dwordx4 v248, s[20:23], s60 offen offset:128 nt lds\n"
      "v_mfma_f32_16x16x32_bf16 v[144:147], a[80:83], a[40:43], v[144:147]\n"
      "s_add_u32 m0, s62, 8192\n"
      "v_exp_f32 v115, v183\n"
      "v_mfma_f32_16x16x32_bf16 v[144:147], a[88:91], a[56:59], v[144:147]\n"
      "s_max_i32 s76, s76, 70\n"
      "ds_read_b64_tr_b16 a[104:105], v246 offset:0\n"
      "buffer_load_dwordx4 v120, s[20:23], s60 offen nt lds\n"
      "v_cvt_pk_bf16_f32 v219, v114, v115\n"
      "v_mfma_f32_16x16x32_bf16 v[148:151], a[92:95], a[56:59], v[232:235]\n"
      "s_add_u32 m0, s62, 9088\n"
      "v_exp_f32 v112, v184\n"
      "v_mfma_f32_16x16x32_bf16 v[148:151], a[84:87], a[40:43], v[148:151]\n"
      "s_add_u32 s62, s62, 0x8000\n"
      "ds_read_b64_tr_b16 a[106:107], v246 offset:4096\n"
      "buffer_load_dwordx4 v120, s[20:23], s60 offen offset:128 nt lds\n"
      "v_or3_b32 v116, v116, v218, v219\n"
      "v_mfma_f32_16x16x32_bf16 v[148:151], a[76:79], a[24:27], v[148:151]\n"
      "s_and_b32 s62, s62, 0x1ffff\n"
      "v_exp_f32 v113, v185\n"
      "v_mfma_f32_16x16x32_bf16 v[148:151], a[68:71], a[8:11], v[148:151]\n"
      "s_add_u32 s60, s60, %[inc]\n"
      "ds_read_b64_tr_b16 a[108:109], v247 offset:0\n"
      "v_cvt_pk_bf16_f32 v220, v112, v113\n"
      "v_mfma_f32_16x16x32_bf16 v[156:159], a[68:71], a[12:15], v[236:239]\n"
      "s_mov_b32 s82, s68\n"
      "v_exp_f32 v114, v186\n"
      "v_mfma_f32_16x16x32_bf16 v[156:159], a[76:79], a[28:31], v[156:159]\n"
      "s_sub_u32 s66, s66, s99\n"
      "ds_read_b64_tr_b16 a[110:111], v247 offset:4096\n"
      "ds_read_b64_tr_b16 a[112:113], v244 offset:1024\n"
      "v_mfma_f32_16x16x32_bf16 v[156:159], a[84:87], a[44:47], v[156:159]\n"
      "s_sub_u32 s82, s82, s99\n"
      "v_exp_f32 v115, v187\n"
      "v_mfma_f32_16x16x32_bf16 v[156:159], a[92:95], a[60:63], v[156:159]\n"
      "s_add_u32 s65, s82, s76\n"
      "ds_read_b64_tr_b16 a[114:115], v244 offset:5120\n"
      "v_cvt_pk_bf16_f32 v221, v114, v115\n"
      "v_mfma_f32_16x16x32_bf16 v[152:155], a[88:91], a[60:63], v[236:239]\n"
      "s_sub_u32 s93, s93, 1\n"
      "v_exp_f32 v112, v188\n"
      "v_mfma_f32_16x16x32_bf16 v[152:155], a[80:83], a[44:47], v[152:155]\n"
      "s_cmp_le_i32 s93, 14\n"
      "ds_read_b64_tr_b16 a[116:117], v245 offset:1024\n"
      "v_or3_b32 v116, v116, v220, v221\n"
      "v_mfma_f32_16x16x32_bf16 v[152:155], a[72:75], a[28:31], v[152:155]\n"
      "s_cselect_b32 s86, 107, s86\n"
      "v_exp_f32 v113, v189\n"
      "v_mfma_f32_16x16x32_bf16 v[152:155], a[64:67], a[12:15], v[152:155]\n"
      "ds_read_b64_tr_b16 a[118:119], v245 offset:5120\n"
      "v_cvt_pk_bf16_f32 v222, v112, v113\n"
      "s_waitcnt lgkmcnt(6)\n"
      "v_mfma_f32_16x16x32_bf16 v[32:35], a[96:99], v[192:195], v[32:35]\n"
      "s_sub_u32 s67, s65, s66\n"
      "v_exp_f32 v114, v190\n"
      "v_mfma_f32_16x16x32_bf16 v[48:51], a[100:103], v[192:195], v[48:51]\n"
      "s_cmp_lt_i32 s67, 0\n"
      "s_cselect_b32 s88, s66, s65\n"
      "ds_read_b64_tr_b16 a[120:121], v246 offset:1024\n"
      "ds_read_b64_tr_b16 a[122:123], v246 offset:5120\n"
      "v_mfma_f32_16x16x32_bf16 a[128:131], a[104:107], v[192:195], a[128:131]\n"
      "s_waitcnt lgkmcnt(4)\n"
      "v_exp_f32 v115, v191\n"
      "v_mfma_f32_16x16x32_bf16 a[144:147], a[108:111], v[192:195], a[144:147]\n"
      "s_add_u32 s90, s88, s91\n"
      "ds_read_b64_tr_b16 a[124:125], v247 offset:1024\n"
      "v_cvt_pk_bf16_f32 v223, v114, v115\n"
      "v_mfma_f32_16x16x32_bf16 v[64:67], a[112:115], v[192:195], v[64:67]\n"
      "ds_read_b64_tr_b16 a[126:127], v247 offset:5120\n"
      "s_waitcnt lgkmcnt(2)\n"
      "v_mfma_f32_16x16x32_bf16 v[80:83], a[116:119], v[192:195], v[80:83]\n"
      "v_or3_b32 v116, v116, v222, v223\n"
      "v_mfma_f32_16x16x32_bf16 a[160:163], a[120:123], v[192:195], a[160:163]\n"
      "s_mov_b32 s64, 4\n"
      "s_waitcnt lgkmcnt(0)\n"
      "v_and_b32 v116, 0x40004000, v116\n"
      "v_mfma_f32_16x16x32_bf16 a[176:179], a[124:127], v[192:195], a[176:179]\n"
      "v_cmp_ne_u32 vcc, 0, v116\n"
      "s_cbranch_vccnz FB1_%=\n"
      "RET1_4_%=:\n"
      "v_mfma_f32_16x16x32_bf16 a[180:183], a[124:127], v[196:199], a[180:183]\n"
      "v_exp_f32 v112, v128\n"
      "v_mfma_f32_16x16x32_bf16 a[164:167], a[120:123], v[196:199], a[164:167]\n"
      "v_xor_b32 v240, 0x10000, v244\n"
      "v_mfma_f32_16x16x32_bf16 a[192:195], a[196:199], v[208:211], a[192:195]\n"
      "v_exp_f32 v113, v129\n"
      "v_mfma_f32_16x16x32_bf16 v[84:87], a[116:119], v[196:199], v[84:87]\n"
      "v_cvt_pk_bf16_f32 v192, v112, v113\n"
      "v_xor_b32 v241, 0x10000, v245\n"
      "v_mfma_f32_16x16x32_bf16 v[68:71], a[112:115], v[196:199], v[68:71]\n"
      "v_exp_f32 v114, v130\n"
      "v_mfma_f32_16x16x32_bf16 a[192:195], a[200:203], v[212:215], a[192:195]\n"
      "v_xor_b32 v250, 0x10000, v246\n"
      "v_mfma_f32_16x16x32_bf16 a[148:151], a[108:111], v[196:199], a[148:151]\n"
      "v_exp_f32 v115, v131\n"
      "v_mfma_f32_16x16x32_bf16 a[132:135], a[104:107], v[196:199], a[132:135]\n"
      "s_memrealtime s[68:69]\n"
      "v_cvt_pk_bf16_f32 v193, v114, v115\n"
      "v_xor_b32 v251, 0x10000, v247\n"
      "v_mfma_f32_16x16x32_bf16 a[192:195], a[204:207], v[216:219], a[192:195]\n"
      "v_exp_f32 v112, v132\n"
      "v_mfma_f32_16x16x32_bf16 v[52:55], a[100:103], v[196:199], v[52:55]\n"
      "v_or_b32 v116, v192, v193\n"
      "v_mfma_f32_16x16x32_bf16 v[36:39], a[96:99], v[196:199], v[36:39]\n"
      "v_exp_f32 v113, v133\n"
      "v_mfma_f32_16x16x32_bf16 a[192:195], a[208:211], v[220:223], a[192:195]\n"
      "v_cvt_pk_bf16_f32 v194, v112, v113\n"
      "s_waitcnt vmcnt(6) lgkmcnt(0)\n"
      "s_sub_u32 s67, s68, s90\n"
      "s_cmp_lt_i32 s67, 0\n"
      "s_cbranch_scc1 GW7_%=\n"
      "GX7_%=:\n"
      "s_barrier\n"
      "v_mfma_f32_16x16x32_bf16 v[40:43], a[96:99], v[200:203], v[40:43]\n"
      "s_memrealtime s[68:69]\n"
      "ds_read_b128 a[64:67], v242 offset:32768\n"
      "v_exp_f32 v114, v134\n"
      "v_mfma_f32_16x16x32_bf16 v[44:47], a[96:99], v[204:207], v[44:47]\n"
      "s_add_u32 m0, %[wb], 82944\n"
      "ds_read_b128 a[68:71], v242 offset:36864\n"
      "v_mfma_f32_16x16x32_bf16 v[60:63], a[100:103], v[204:207], v[60:63]\n"
      "ds_read_b128 a[72:75], v243 offset:32768\n"
      "buffer_load_dwordx4 v121, s[24:27], s94 offen nt lds\n"
      "v_mfma_f32_16x16x32_bf16 v[56:59], a[100:103], v[200:203], v[56:59]\n"
      "s_add_u32 m0, %[wb], 91136\n"
      "ds_read_b128 a[76:79], v243 offset:36864\n"
      "v_mfma_f32_16x16x32_bf16 a[136:139], a[104:107], v[200:203], a[136:139]\n"
      "ds_read_b128 a[80:83], v242 offset:33792\n"
      "buffer_load_dwordx4 v123, s[24:27], s94 offen nt lds\n"
      "v_mfma_f32_16x16x32_bf16 a[140:143], a[104:107], v[204:207], a[140:143]\n"
      "ds_read_b128 a[84:87], v242 offset:37888\n"
      "v_mfma_f32_16x16x32_bf16 a[156:159], a[108:111], v[204:207], a[156:159]\n"
      "ds_read_b128 a[88:91], v243 offset:33792\n"
      "v_mfma_f32_16x16x32_bf16 a[152:155], a[108:111], v[200:203], a[152:155]\n"
      "ds_read_b128 a[92:95], v243 offset:37888\n"
      "v_mfma_f32_16x16x32_bf16 v[72:75], a[112:115], v[200:203], v[72:75]\n"
      "v_exp_f32 v115, v135\n"
      "v_mfma_f32_16x16x32_bf16 v[76:79], a[112:115], v[204:207], v[76:79]\n"
      "v_exp_f32 v112, v136\n"
      "v_mfma_f32_16x16x32_bf16 v[92:95], a[116:119], v[204:207], v[92:95]\n"
      "v_cvt_pk_bf16_f32 v195, v114, v115\n"
      "v_exp_f32 v113, v137\n"
      "v_mfma_f32_16x16x32_bf16 v[88:91], a[116:119], v[200:203], v[88:91]\n"
      "v_or3_b32 v116, v116, v194, v195\n"
      "v_mfma_f32_16x16x32_bf16 a[168:171], a[120:123], v[200:203], a[168:171]\n"
      "v_exp_f32 v114, v138\n"
      "v_mfma_f32_16x16x32_bf16 a[172:175], a[120:123], v[204:207], a[172:175]\n"
      "v_cvt_pk_bf16_f32 v196, v112, v113\n"
      "v_exp_f32 v115, v139\n"
      "v_mfma_f32_16x16x32_bf16 a[188:191], a[124:127], v[204:207], a[188:191]\n"
      "v_exp_f32 v112, v140\n"
      "v_mfma_f32_16x16x32_bf16 a[184:187], a[124:127], v[200:203], a[184:187]\n"
      "s_waitcnt lgkmcnt(0)\n"
      "v_cvt_pk_bf16_f32 v197, v114, v115\n"
      "v_mfma_f32_16x16x32_bf16 v[160:163], a[64:67], a[0:3], v[224:227]\n"
      "s_sub_u32 s67, s68, s66\n"
      "v_exp_f32 v113, v141\n"
      "v_mfma_f32_16x16x32_bf16 v[168:171], a[64:67], a[4:7], v[228:231]\n"
      "s_cmp_gt_i32 s67, s70\n"
      "v_or3_b32 v116, v116, v196, v197\n"
      "v_exp_f32 v114, v142\n"
      "v_mfma_f32_16x16x32_bf16 v[176:179], a[64:67], a[8:11], v[232:235]\n"
      "s_cselect_b32 s69, s86, 0\n"
      "v_cvt_pk_bf16_f32 v198, v112, v113\n"
      "v_mfma_f32_16x16x32_bf16 v[184:187], a[64:67], a[12:15], v[236:239]\n"
      "s_add_u32 s66, s66, s69\n"
      "v_exp_f32 v115, v143\n"
      "v_mfma_f32_16x16x32_bf16 v[164:167], a[68:71], a[0:3], v[224:227]\n"
      "s_add_u32 s67, s67, s101\n"
      "v_exp_f32 v112, v144\n"
      "v_mfma_f32_16x16x32_bf16 v[172:175], a[68:71], a[4:7], v[228:231]\n"
      "s_min_i32 s70, s70, s67\n"
      "v_cvt_pk_bf16_f32 v199, v114, v115\n"
      "v_exp_f32 v113, v145\n"
      "v_mfma_f32_16x16x32_bf16 v[180:183], a[68:71], a[8:11], v[232:235]\n"
      "s_max_i32 s70, s70, 85\n"
      "v_or3_b32 v116, v116, v198, v199\n"
      "v_mfma_f32_16x16x32_bf16 v[188:191], a[68:71], a[12:15], v[236:239]\n"
      "s_waitcnt lgkmcnt(4)\n"
      "v_exp_f32 v114, v146\n"
      "v_mfma_f32_16x16x32_bf16 v[160:163], a[72:75], a[16:19], v[160:163]\n"
      "s_add_u32 s66, s66, s86\n"
      "v_cvt_pk_bf16_f32 v200, v112, v113\n"
      "v_exp_f32 v115, v147\n"
      "v_mfma_f32_16x16x32_bf16 v[168:171], a[72:75], a[20:23], v[168:171]\n"
      "s_and_b32 s76, s80, 31\n"
      "v_exp_f32 v112, v148\n"
      "v_mfma_f32_16x16x32_bf16 v[176:179], a[72:75], a[24:27], v[176:179]\n"
      "s_lshr_b64 s[80:81], s[80:81], 5\n"
      "v_cvt_pk_bf16_f32 v201, v114, v115\n"
      "v_mfma_f32_16x16x32_bf16 v[184:187], a[72:75], a[28:31], v[184:187]\n"
      "s_add_u32 s66, s66, s76\n"
      "v_exp_f32 v113, v149\n"
      "v_mfma_f32_16x16x32_bf16 v[164:167], a[76:79], a[16:19], v[164:167]\n"
      "s_sub_u32 s67, s68, s82\n"
      "v_or3_b32 v116, v116, v200, v201\n"
      "v_exp_f32 v114, v150\n"
      "v_mfma_f32_16x16x32_bf16 v[172:175], a[76:79], a[20:23], v[172:175]\n"
      "s_min_i32 s76, s67, s83\n"
      "v_cvt_pk_bf16_f32 v202, v112, v113\n"
      "v_mfma_f32_16x16x32_bf16 v[180:183], a[76:79], a[24:27], v[180:183]\n"
      "s_add_u32 m0, s62, 0\n"
      "v_exp_f32 v115, v151\n"
      "v_mfma_f32_16x16x32_bf16 v[188:191], a[76:79], a[28:31], v[188:191]\n"
      "buffer_load_dwordx4 v248, s[20:23], s60 offen nt lds\n"
      "s_waitcnt lgkmcnt(2)\n"
      "v_mfma_f32_16x16x32_bf16 v[160:163], a[80:83], a[32:35], v[160:163]\n"
      "s_add_u32 m0, s62, 896\n"
      "v_exp_f32 v112, v152\n"
      "v_mfma_f32_16x16x32_bf16 v[168:171], a[80:83], a[36:39], v[168:171]\n"
      "s_mov_b32 s83, s67\n"
      "buffer_load_dwordx4 v248, s[20:23], s60 offen offset:128 nt lds\n"
      "v_mfma_f32_16x16x32_bf16 v[176:179], a[80:83], a[40:43], v[176:179]\n"
      "s_add_u32 m0, s62, 8192\n"
      "v_mfma_f32_16x16x32_bf16 v[184:187], a[80:83], a[44:47], v[184:187]\n"
      "s_sub_u32 s76, s76, 36\n"
      "buffer_load_dwordx4 v120, s[20:23], s60 offen nt lds\n"
      "s_waitcnt vmcnt(9)\n"
      "s_barrier\n"
      "v_mfma_f32_16x16x32_bf16 v[164:167], a[84:87], a[32:35], v[164:167]\n"
      "s_add_u32 m0, s62, 9088\n"
      "ds_read_b64_tr_b16 a[96:97], v246 offset:33792\n"
      "v_mfma_f32_16x16x32_bf16 v[172:175], a[84:87], a[36:39], v[172:175]\n"
      "s_min_i32 s76, s76, 148\n"
      "ds_read_b64_tr_b16 a[98:99], v246 offset:37888\n"
      "buffer_load_dwordx4 v120, s[20:23], s60 offen offset:128 nt lds\n"
      "v_mfma_f32_16x16x32_bf16 v[180:183], a[84:87], a[40:43], v[180:183]\n"
      "s_max_i32 s76, s76, 70\n"
      "ds_read_b64_tr_b16 a[100:101], v247 offset:33792\n"
      "v_mfma_f32_16x16x32_bf16 v[188:191], a[84:87], a[44:47], v[188:191]\n"
      "ds_read_b64_tr_b16 a[102:103], v247 offset:37888\n"
      "s_waitcnt lgkmcnt(4)\n"
      "v_mfma_f32_16x16x32_bf16 v[160:163], a[88:91], a[48:51], v[160:163]\n"
      "s_mov_b32 s82, s68\n"
      "ds_read_b64_tr_b16 a[104:105], v244 offset:33792\n"
      "v_cvt_pk_bf16_f32 v203, v114, v115\n"
      "v_mfma_f32_16x16x32_bf16 v[168:171], a[88:91], a[52:55], v[168:171]\n"
      "s_sub_u32 s66, s66, s99\n"
      "ds_read_b64_tr_b16 a[106:107], v244 offset:37888\n"
      "v_exp_f32 v113, v153\n"
      "v_mfma_f32_16x16x32_bf16 v[176:179], a[88:91], a[56:59], v[176:179]\n"
      "s_sub_u32 s82, s82, s99\n"
      "ds_read_b64_tr_b16 a[108:109], v245 offset:33792\n"
      "v_mfma_f32_16x16x32_bf16 v[184:187], a[88:91], a[60:63], v[184:187]\n"
      "s_add_u32 s65, s82, s76\n"
      "ds_read_b64_tr_b16 a[110:111], v245 offset:37888\n"
      "v_or3_b32 v116, v116, v202, v203\n"
      "v_mfma_f32_16x16x32_bf16 v[164:167], a[92:95], a[48:51], v[164:167]\n"
      "s_sub_u32 s93, s93, 1\n"
      "v_exp_f32 v114, v154\n"
      "v_mfma_f32_16x16x32_bf16 v[172:175], a[92:95], a[52:55], v[172:175]\n"
      "s_cmp_le_i32 s93, 14\n"
      "v_cvt_pk_bf16_f32 v204, v112, v113\n"
      "v_exp_f32 v115, v155\n"
      "v_mfma_f32_16x16x32_bf16 v[180:183], a[92:95], a[56:59], v[180:183]\n"
      "s_cselect_b32 s86, 107, s86\n"
      "v_exp_f32 v112, v156\n"
      "v_mfma_f32_16x16x32_bf16 v[188:191], a[92:95], a[60:63], v[188:191]\n"
      "s_waitcnt lgkmcnt(4)\n"
      "v_cvt_pk_bf16_f32 v205, v114, v115\n"
      "v_mfma_f32_16x16x32_bf16 a[160:163], a[96:99], v[208:211], a[160:163]\n"
      "s_sub_u32 s67, s65, s66\n"
      "v_exp_f32 v113, v157\n"
      "v_mfma_f32_16x16x32_bf16 a[164:167], a[96:99], v[212:215], a[164:167]\n"
      "s_cmp_lt_i32 s67, 0\n"
      "v_or3_b32 v116, v116, v204, v205\n"
      "v_exp_f32 v114, v158\n"
      "v_mfma_f32_16x16x32_bf16 a[168:171], a[96:99], v[216:219], a[168:171]\n"
      "s_cselect_b32 s88, s66, s65\n"
      "v_cvt_pk_bf16_f32 v206, v112, v113\n"
      "v_mfma_f32_16x16x32_bf16 a[172:175], a[96:99], v[220:223], a[172:175]\n"
      "s_add_u32 s90, s88, s91\n"
      "v_exp_f32 v115, v159\n"
      "s_nop 0\n"
      "v_mfma_f32_16x16x32_bf16 a[176:179], a[100:103], v[208:211], a[176:179]\n"
      "s_mov_b32 s64, 4\n"
      "v_cvt_pk_bf16_f32 v207, v114, v115\n"
      "v_or3_b32 v116, v116, v206, v207\n"
      "v_mfma_f32_16x16x32_bf16 a[180:183], a[100:103], v[212:215], a[180:183]\n"
      "v_and_b32 v116, 0x40004000, v116\n"
      "v_cmp_ne_u32 vcc, 0, v116\n"
      "s_cbranch_vccnz FB0_%=\n"
      "RET0_4_%=:\n"
      "v_mfma_f32_16x16x32_bf16 a[184:187], a[100:103], v[216:219], a[184:187]\n"
      "v_exp_f32 v112, v160\n"
      "v_mfma_f32_16x16x32_bf16 a[188:191], a[100:103], v[220:223], a[188:191]\n"
      "s_memrealtime s[68:69]\n"
      "v_xor_b32 v242, 0x10000, v242\n"
      "v_mfma_f32_16x16x32_bf16 a[192:195], a[196:199], v[192:195], a[192:195]\n"
      "v_exp_f32 v113, v161\n"
      "v_mfma_f32_16x16x32_bf16 a[192:195], a[200:203], v[196:199], a[192:195]\n"
      "v_xor_b32 v243, 0x10000, v243\n"
      "v_exp_f32 v114, v162\n"
      "v_mfma_f32_16x16x32_bf16 a[192:195], a[204:207], v[200:203], a[192:195]\n"
      "v_cvt_pk_bf16_f32 v0, v112, v113\n"
      "v_mfma_f32_16x16x32_bf16 a[192:195], a[208:211], v[204:207], a[192:195]\n"
      "v_exp_f32 v115, v163\n"
      "s_waitcnt vmcnt(6) lgkmcnt(0)\n"
      "s_sub_u32 s67, s68, s90\n"
      "s_cmp_lt_i32 s67, 0\n"
      "s_cbranch_scc1 GW8_%=\n"
      "GX8_%=:\n"
      "s_barrier\n"
      "v_mfma_f32_16x16x32_bf16 v[64:67], a[104:107], v[208:211], v[64:67]\n"
      "ds_read_b128 a[64:67], v242 offset:0\n"
      "ds_read_b128 a[68:71], v242 offset:4096\n"
      "v_mfma_f32_16x16x32_bf16 v[68:71], a[104:107], v[212:215], v[68:71]\n"
      "s_add_u32 m0, %[wb], 115712\n"
      "ds_read_b128 a[72:75], v243 offset:0\n"
      "ds_read_b128 a[76:79], v243 offset:4096\n"
      "v_mfma_f32_16x16x32_bf16 v[72:75], a[104:107], v[216:219], v[72:75]\n"
      "ds_read_b128 a[80:83], v242 offset:1024\n"
      "ds_read_b128 a[84:87], v242 offset:5120\n"
      "buffer_load_dwordx4 v121, s[24:27], s95 offen nt lds\n"
      "v_mfma_f32_16x16x32_bf16 v[76:79], a[104:107], v[220:223], v[76:79]\n"
      "s_add_u32 m0, %[wb], 123904\n"
      "ds_read_b128 a[88:91], v243 offset:1024\n"
      "ds_read_b128 a[92:95], v243 offset:5120\n"
      "v_mfma_f32_16x16x32_bf16 v[80:83], a[108:111], v[208:211], v[80:83]\n"
      "buffer_load_dwordx4 v123, s[24:27], s95 offen nt lds\n"
      "v_mfma_f32_16x16x32_bf16 v[84:87], a[108:111], v[212:215], v[84:87]\n"
      "s_add_u32 m0, %[wb], 17408\n"
      "v_mfma_f32_16x16x32_bf16 v[88:91], a[108:111], v[216:219], v[88:91]\n"
      "buffer_load_dwordx4 v121, s[24:27], s77 offen nt lds\n"
      "v_mfma_f32_16x16x32_bf16 v[92:95], a[108:111], v[220:223], v[92:95]\n"
      "s_waitcnt lgkmcnt(6)\n"
      "s_add_u32 m0, %[wb], 25600\n"
      "v_exp_f32 v112, v164\n"
      "v_mfma_f32_16x16x32_bf16 v[128:131], a[64:67], a[0:3], v[224:227]\n"
      "buffer_load_dwordx4 v123, s[24:27], s77 offen nt lds\n"
      "v_mfma_f32_16x16x32_bf16 v[136:139], a[64:67], a[4:7], v[228:231]\n"
      "v_cvt_pk_bf16_f32 v1, v114, v115\n"
      "v_exp_f32 v113, v165\n"
      "v_mfma_f32_16x16x32_bf16 v[144:147], a[64:67], a[8:11], v[232:235]\n"
      "v_or_b32 v116, v0, v1\n"
      "v_mfma_f32_16x16x32_bf16 v[152:155], a[64:67], a[12:15], v[236:239]\n"
      "v_exp_f32 v114, v166\n"
      "v_cvt_pk_bf16_f32 v2, v112, v113\n"
      "v_mfma_f32_16x16x32_bf16 v[132:135], a[68:71], a[0:3], v[224:227]\n"
      "v_exp_f32 v115, v167\n"
      "v_mfma_f32_16x16x32_bf16 v[140:143], a[68:71], a[4:7], v[228:231]\n"
      "v_exp_f32 v112, v168\n"
      "v_cvt_pk_bf16_f32 v3, v114, v115\n"
      "v_mfma_f32_16x16x32_bf16 v[148:151], a[68:71], a[8:11], v[232:235]\n"
      "v_exp_f32 v113, v169\n"
      "v_or3_b32 v116, v116, v2, v3\n"
      "v_mfma_f32_16x16x32_bf16 v[156:159], a[68:71], a[12:15], v[236:239]\n"
      "s_waitcnt lgkmcnt(4)\n"
      "v_exp_f32 v114, v170\n"
      "v_mfma_f32_16x16x32_bf16 v[128:131], a[72:75], a[16:19], v[128:131]\n"
      "v_cvt_pk_bf16_f32 v4, v112, v113\n"
      "v_exp_f32 v115, v171\n"
      "v_mfma_f32_16x16x32_bf16 v[136:139], a[72:75], a[20:23], v[136:139]\n"
      "v_exp_f32 v112, v172\n"
      "v_mfma_f32_16x16x32_bf16 v[144:147], a[72:75], a[24:27], v[144:147]\n"
      "v_cvt_pk_bf16_f32 v5, v114, v115\n"
      "v_exp_f32 v113, v173\n"
      "v_mfma_f32_16x16x32_bf16 v[152:155], a[72:75], a[28:31], v[152:155]\n"
      "v_or3_b32 v116, v116, v4, v5\n"
      "v_mfma_f32_16x16x32_bf16 v[132:135], a[76:79], a[16:19], v[132:135]\n"
      "v_exp_f32 v114, v174\n"
      "v_cvt_pk_bf16_f32 v6, v112, v113\n"
      "v_mfma_f32_16x16x32_bf16 v[140:143], a[76:79], a[20:23], v[140:143]\n"
      "v_exp_f32 v115, v175\n"
      "v_mfma_f32_16x16x32_bf16 v[148:151], a[76:79], a[24:27], v[148:151]\n"
      "v_exp_f32 v112, v176\n"
      "v_cvt_pk_bf16_f32 v7, v114, v115\n"
      "v_mfma_f32_16x16x32_bf16 v[156:159], a[76:79], a[28:31], v[156:159]\n"
      "s_waitcnt lgkmcnt(2)\n"
      "v_exp_f32 v113, v177\n"
      "v_mfma_f32_16x16x32_bf16 v[128:131], a[80:83], a[32:35], v[128:131]\n"
      "v_or3_b32 v116, v116, v6, v7\n"
      "v_exp_f32 v114, v178\n"
      "v_mfma_f32_16x16x32_bf16 v[136:139], a[80:83], a[36:39], v[136:139]\n"
      "v_cvt_pk_bf16_f32 v8, v112, v113\n"
      "v_exp_f32 v115, v179\n"
      "v_mfma_f32_16x16x32_bf16 v[144:147], a[80:83], a[40:43], v[144:147]\n"
      "v_exp_f32 v112, v180\n"
      "v_mfma_f32_16x16x32_bf16 v[152:155], a[80:83], a[44:47], v[152:155]\n"
      "v_cvt_pk_bf16_f32 v9, v114, v115\n"
      "v_exp_f32 v113, v181\n"
      "s_waitcnt vmcnt(8)\n"
      "s_barrier\n"
      "v_mfma_f32_16x16x32_bf16 v[132:135], a[84:87], a[32:35], v[132:135]\n"
      "ds_read_b64_tr_b16 a[112:113], v250 offset:1024\n"
      "v_or3_b32 v116, v116, v8, v9\n"
      "v_mfma_f32_16x16x32_bf16 v[140:143], a[84:87], a[36:39], v[140:143]\n"
      "ds_read_b64_tr_b16 a[114:115], v250 offset:5120\n"
      "v_exp_f32 v114, v182\n"
      "v_mfma_f32_16x16x32_bf16 v[148:151], a[84:87], a[40:43], v[148:151]\n"
      "ds_read_b64_tr_b16 a[116:117], v251 offset:1024\n"
      "v_mfma_f32_16x16x32_bf16 v[156:159], a[84:87], a[44:47], v[156:159]\n"
      "ds_read_b64_tr_b16 a[118:119], v251 offset:5120\n"
      "v_cvt_pk_bf16_f32 v10, v112, v113\n"
      "s_waitcnt lgkmcnt(4)\n"
      "v_mfma_f32_16x16x32_bf16 v[128:131], a[88:91], a[48:51], v[128:131]\n"
      "ds_read_b64_tr_b16 a[120:121], v240 offset:1024\n"
      "v_exp_f32 v115, v183\n"
      "v_mfma_f32_16x16x32_bf16 v[136:139], a[88:91], a[52:55], v[136:139]\n"
      "ds_read_b64_tr_b16 a[122:123], v240 offset:5120\n"
      "v_exp_f32 v112, v184\n"
      "v_mfma_f32_16x16x32_bf16 v[144:147], a[88:91], a[56:59], v[144:147]\n"
      "ds_read_b64_tr_b16 a[124:125], v241 offset:1024\n"
      "v_cvt_pk_bf16_f32 v11, v114, v115\n"
      "v_mfma_f32_16x16x32_bf16 v[152:155], a[88:91], a[60:63], v[152:155]\n"
      "ds_read_b64_tr_b16 a[126:127], v241 offset:5120\n"
      "v_exp_f32 v113, v185\n"
      "v_mfma_f32_16x16x32_bf16 v[132:135], a[92:95], a[48:51], v[132:135]\n"
      "v_or3_b32 v116, v116, v10, v11\n"
      "v_exp_f32 v114, v186\n"
      "v_mfma_f32_16x16x32_bf16 v[140:143], a[92:95], a[52:55], v[140:143]\n"
      "v_cvt_pk_bf16_f32 v12, v112, v113\n"
      "v_mfma_f32_16x16x32_bf16 v[148:151], a[92:95], a[56:59], v[148:151]\n"
      "v_exp_f32 v115, v187\n"
      "v_mfma_f32_16x16x32_bf16 v[156:159], a[92:95], a[60:63], v[156:159]\n"
      "s_waitcnt lgkmcnt(4)\n"
      "v_exp_f32 v112, v188\n"
      "v_cvt_pk_bf16_f32 v13, v114, v115\n"
      "v_mfma_f32_16x16x32_bf16 a[160:163], a[112:115], v[192:195], a[160:163]\n"
      "v_exp_f32 v113, v189\n"
      "v_or3_b32 v116, v116, v12, v13\n"
      "v_mfma_f32_16x16x32_bf16 a[164:167], a[112:115], v[196:199], a[164:167]\n"
      "v_exp_f32 v114, v190\n"
      "v_mfma_f32_16x16x32_bf16 a[168:171], a[112:115], v[200:203], a[168:171]\n"
      "v_cvt_pk_bf16_f32 v14, v112, v113\n"
      "v_exp_f32 v115, v191\n"
      "v_mfma_f32_16x16x32_bf16 a[172:175], a[112:115], v[204:207], a[172:175]\n"
      "s_add_u32 m0, %[wb], 50176\n"
      "s_nop 0\n"
      "v_cvt_pk_bf16_f32 v15, v114, v115\n"
      "v_or3_b32 v116, v116, v14, v15\n"
      "v_mfma_f32_16x16x32_bf16 a[176:179], a[116:119], v[192:195], a[176:179]\n"
      "s_mov_b32 s64, 5\n"
      "buffer_load_dwordx4 v121, s[24:27], s78 offen nt lds\n"
      "v_mfma_f32_16x16x32_bf16 a[180:183], a[116:119], v[196:199], a[180:183]\n"
      "s_add_u32 m0, %[wb], 58368\n"
      "v_and_b32 v116, 0x40004000, v116\n"
      "v_cmp_ne_u32 vcc, 0, v116\n"
      "s_cbranch_vccnz FB1_%=\n"
      "RET1_5_%=:\n"
      "v_mfma_f32_16x16x32_bf16 a[184:187], a[116:119], v[200:203], a[184:187]\n"
      "buffer_load_dwordx4 v123, s[24:27], s78 offen nt lds\n"
      "v_mfma_f32_16x16x32_bf16 a[188:191], a[116:119], v[204:207], a[188:191]\n"
      "s_memrealtime s[68:69]\n"
      "v_exp_f32 v112, v128\n"
      "v_exp_f32 v113, v129\n"
      "v_mfma_f32_16x16x32_bf16 a[192:195], a[196:199], v[0:3], a[192:195]\n"
      "v_exp_f32 v114, v130\n"
      "v_mfma_f32_16x16x32_bf16 a[192:195], a[200:203], v[4:7], a[192:195]\n"
      "v_cvt_pk_bf16_f32 v16, v112, v113\n"
      "v_exp_f32 v115, v131\n"
      "v_mfma_f32_16x16x32_bf16 a[192:195], a[204:207], v[8:11], a[192:195]\n"
      "v_exp_f32 v112, v132\n"
      "v_cvt_pk_bf16_f32 v17, v114, v115\n"
      "v_mfma_f32_16x16x32_bf16 a[192:195], a[208:211], v[12:15], a[192:195]\n"
      "v_exp_f32 v113, v133\n"
      "v_or_b32 v116, v16, v17\n"
      "s_add_u32 s72, s72, 0x7f4a7c15\n"
      "s_addc_u32 s76, s79, 0x9e3779b9\n"
      "s_waitcnt vmcnt(6) lgkmcnt(0)\n"
      "s_barrier\n"
      "v_mfma_f32_16x16x32_bf16 v[64:67], a[120:123], v[192:195], v[64:67]\n"
      "s_xor_b32 s80, %[xo], 0x400000\n"
      "ds_read_b128 a[64:67], v242 offset:32768\n"
      "ds_read_b128 a[68:71], v242 offset:36864\n"
      "v_mfma_f32_16x16x32_bf16 v[68:71], a[120:123], v[196:199], v[68:71]\n"
      "s_add_u32 s79, s80, 0x1000\n"
      "ds_read_b128 a[72:75], v243 offset:32768\n"
      "ds_read_b128 a[76:79], v243 offset:36864\n"
      "v_exp_f32 v114, v134\n"
      "v_mfma_f32_16x16x32_bf16 v[72:75], a[120:123], v[200:203], v[72:75]\n"
      "s_add_u32 s81, %[xo], 0x1000\n"
      "ds_read_b128 a[80:83], v242 offset:33792\n"
      "ds_read_b128 a[84:87], v242 offset:37888\n"
      "v_cvt_pk_bf16_f32 v18, v112, v113\n"
      "v_mfma_f32_16x16x32_bf16 v[76:79], a[120:123], v[204:207], v[76:79]\n"
      "s_mov_b32 s63, 0x100000\n"
      "ds_read_b128 a[88:91], v243 offset:33792\n"
      "ds_read_b128 a[92:95], v243 offset:37888\n"
      "v_mfma_f32_16x16x32_bf16 v[80:83], a[124:127], v[192:195], v[80:83]\n"
      "s_mov_b32 s60, 0x928b2f\n"
      "v_exp_f32 v115, v135\n"
      "v_exp_f32 v112, v136\n"
      "v_mfma_f32_16x16x32_bf16 v[84:87], a[124:127], v[196:199], v[84:87]\n"
      "s_mov_b32 s62, 0x9818e9\n"
      "v_cvt_pk_bf16_f32 v19, v114, v115\n"
      "v_exp_f32 v113, v137\n"
      "v_mfma_f32_16x16x32_bf16 v[88:91], a[124:127], v[200:203], v[88:91]\n"
      "s_mov_b32 s70, 0xdd9dc9\n"
      "v_xor_b32 v248, 0x2000, v244\n"
      "v_or3_b32 v116, v116, v18, v19\n"
      "v_mfma_f32_16x16x32_bf16 v[92:95], a[124:127], v[204:207], v[92:95]\n"
      "s_waitcnt lgkmcnt(6)\n"
      "v_exp_f32 v114, v138\n"
      "v_cvt_pk_bf16_f32 v20, v112, v113\n"
      "v_mfma_f32_16x16x32_bf16 v[160:163], a[64:67], a[0:3], v[224:227]\n"
      "s_mov_b32 s71, 0x8ed905\n"
      "v_exp_f32 v115, v139\n"
      "v_mfma_f32_16x16x32_bf16 v[168:171], a[64:67], a[4:7], v[228:231]\n"
      "s_sub_u32 s88, 299, %[cph]\n"
      "v_xor_b32 v120, 0x2000, v245\n"
      "v_exp_f32 v112, v140\n"
      "v_cvt_pk_bf16_f32 v21, v114, v115\n"
      "v_mfma_f32_16x16x32_bf16 v[176:179], a[64:67], a[8:11], v[232:235]\n"
      "s_add_u32 s88, s88, s66\n"
      "v_exp_f32 v113, v141\n"
      "v_mfma_f32_16x16x32_bf16 v[184:187], a[64:67], a[12:15], v[236:239]\n"
      "s_add_u32 m0, %[wb], 49152\n"
      "v_or3_b32 v116, v116, v20, v21\n"
      "v_exp_f32 v114, v142\n"
      "v_mfma_f32_16x16x32_bf16 v[164:167], a[68:71], a[0:3], v[224:227]\n"
      "s_add_u32 s88, s88, s100\n"
      "buffer_load_dwordx4 v249, s[24:27], s61 offen nt lds\n"
      "v_mbcnt_lo_u32_b32 v127, -1, 0\n"
      "v_mfma_f32_16x16x32_bf16 v[172:175], a[68:71], a[4:7], v[228:231]\n"
      "s_add_u32 m0, %[wb], 57344\n"
      "v_cvt_pk_bf16_f32 v22, v112, v113\n"
      "v_mfma_f32_16x16x32_bf16 v[180:183], a[68:71], a[8:11], v[232:235]\n"
      "buffer_load_dwordx4 v122, s[24:27], s61 offen nt lds\n"
      "v_mfma_f32_16x16x32_bf16 v[188:191], a[68:71], a[12:15], v[236:239]\n"
      "s_waitcnt lgkmcnt(4)\n"
      "v_mbcnt_hi_u32_b32 v127, -1, v127\n"
      "v_exp_f32 v115, v143\n"
      "v_mfma_f32_16x16x32_bf16 v[160:163], a[72:75], a[16:19], v[160:163]\n"
      "v_exp_f32 v112, v144\n"
      "v_cvt_pk_bf16_f32 v23, v114, v115\n"
      "v_mfma_f32_16x16x32_bf16 v[168:171], a[72:75], a[20:23], v[168:171]\n"
      "v_exp_f32 v113, v145\n"
      "v_or3_b32 v116, v116, v22, v23\n"
      "v_mfma_f32_16x16x32_bf16 v[176:179], a[72:75], a[24:27], v[176:179]\n"
      "v_lshlrev_b32 v127, 4, v127\n"
      "v_exp_f32 v114, v146\n"
      "v_mfma_f32_16x16x32_bf16 v[184:187], a[72:75], a[28:31], v[184:187]\n"
      "v_cvt_pk_bf16_f32 v24, v112, v113\n"
      "v_exp_f32 v115, v147\n"
      "v_mfma_f32_16x16x32_bf16 v[164:167], a[76:79], a[16:19], v[164:167]\n"
      "v_exp_f32 v112, v148\n"
      "v_cvt_pk_bf16_f32 v25, v114, v115\n"
      "v_mfma_f32_16x16x32_bf16 v[172:175], a[76:79], a[20:23], v[172:175]\n"
      "v_add_u32 v125, %[xw], v127\n"
      "v_exp_f32 v113, v149\n"
      "v_mfma_f32_16x16x32_bf16 v[180:183], a[76:79], a[24:27], v[180:183]\n"
      "v_or3_b32 v116, v116, v24, v25\n"
      "v_exp_f32 v114, v150\n"
      "v_mfma_f32_16x16x32_bf16 v[188:191], a[76:79], a[28:31], v[188:191]\n"
      "s_waitcnt lgkmcnt(2)\n"
      "v_cvt_pk_bf16_f32 v26, v112, v113\n"
      "v_exp_f32 v115, v151\n"
      "v_mfma_f32_16x16x32_bf16 v[160:163], a[80:83], a[32:35], v[160:163]\n"
      "v_add_u32 v126, %[xr], v127\n"
      "v_exp_f32 v112, v152\n"
      "v_mfma_f32_16x16x32_bf16 v[168:171], a[80:83], a[36:39], v[168:171]\n"
      "s_add_u32 m0, %[wb], 81920\n"
      "v_cvt_pk_bf16_f32 v27, v114, v115\n"
      "v_exp_f32 v113, v153\n"
      "v_mfma_f32_16x16x32_bf16 v[176:179], a[80:83], a[40:43], v[176:179]\n"
      "buffer_load_dwordx4 v249, s[24:27], s94 offen nt lds\n"
      "v_mfma_f32_16x16x32_bf16 v[184:187], a[80:83], a[44:47], v[184:187]\n"
      "s_add_u32 m0, %[wb], 90112\n"
      "v_or3_b32 v116, v116, v26, v27\n"
      "v_mfma_f32_16x16x32_bf16 v[164:167], a[84:87], a[32:35], v[164:167]\n"
      "buffer_load_dwordx4 v122, s[24:27], s94 offen nt lds\n"
      "v_mfma_f32_16x16x32_bf16 v[172:175], a[84:87], a[36:39], v[172:175]\n"
      "v_exp_f32 v114, v154\n"
      "v_cvt_pk_bf16_f32 v28, v112, v113\n"
      "v_mfma_f32_16x16x32_bf16 v[180:183], a[84:87], a[40:43], v[180:183]\n"
      "v_exp_f32 v115, v155\n"
      "v_exp_f32 v112, v156\n"
      "v_mov_b32 v123, s88\n"
      "v_mfma_f32_16x16x32_bf16 v[188:191], a[84:87], a[44:47], v[188:191]\n"
      "s_waitcnt lgkmcnt(0)\n"
      "v_cvt_pk_bf16_f32 v29, v114, v115\n"
      "v_exp_f32 v113, v157\n"
      "s_waitcnt vmcnt(6)\n"
      "s_barrier\n"
      "v_mfma_f32_16x16x32_bf16 v[160:163], a[88:91], a[48:51], v[160:163]\n"
      "ds_read_b64_tr_b16 a[96:97], v250 offset:33792\n"
      "ds_read_b64_tr_b16 a[98:99], v250 offset:37888\n"
      "v_mfma_f32_16x16x32_bf16 v[168:171], a[88:91], a[52:55], v[168:171]\n"
      "ds_read_b64_tr_b16 a[100:101], v251 offset:33792\n"
      "ds_read_b64_tr_b16 a[102:103], v251 offset:37888\n"
      "v_or3_b32 v116, v116, v28, v29\n"
      "v_mfma_f32_16x16x32_bf16 v[176:179], a[88:91], a[56:59], v[176:179]\n"
      "ds_read_b64_tr_b16 a[112:113], v246 offset:1024\n"
      "ds_read_b64_tr_b16 a[114:115], v246 offset:5120\n"
      "v_mfma_f32_16x16x32_bf16 v[184:187], a[88:91], a[60:63], v[184:187]\n"
      "ds_read_b64_tr_b16 a[116:117], v247 offset:1024\n"
      "ds_read_b64_tr_b16 a[118:119], v247 offset:5120\n"
      "v_exp_f32 v114, v158\n"
      "v_mfma_f32_16x16x32_bf16 v[164:167], a[92:95], a[48:51], v[164:167]\n"
      "ds_read_b64_tr_b16 a[104:105], v240 offset:33792\n"
      "v_cvt_pk_bf16_f32 v30, v112, v113\n"
      "v_mfma_f32_16x16x32_bf16 v[172:175], a[92:95], a[52:55], v[172:175]\n"
      "s_mov_b32 s64, 5\n"
      "ds_read_b64_tr_b16 a[106:107], v240 offset:37888\n"
      "v_exp_f32 v115, v159\n"
      "s_nop 0\n"
      "v_mfma_f32_16x16x32_bf16 v[180:183], a[92:95], a[56:59], v[180:183]\n"
      "ds_read_b64_tr_b16 a[108:109], v241 offset:33792\n"
      "v_cvt_pk_bf16_f32 v31, v114, v115\n"
      "v_or3_b32 v116, v116, v30, v31\n"
      "v_mfma_f32_16x16x32_bf16 v[188:191], a[92:95], a[60:63], v[188:191]\n"
      "ds_read_b64_tr_b16 a[110:111], v241 offset:37888\n"
      "v_and_b32 v116, 0x40004000, v116\n"
      "v_cmp_ne_u32 vcc, 0, v116\n"
      "s_waitcnt lgkmcnt(4)\n"
      "s_cbranch_vccnz FB0_%=\n"
      "RET0_5_%=:\n"
      "v_mfma_f32_16x16x32_bf16 a[160:163], a[96:99], v[0:3], a[160:163]\n"
      "ds_read_b64_tr_b16 a[120:121], v244 offset:1024\n"
      "v_mov_b32 v117, v224\n"
      "v_xor_b32 v242, 0x10000, v242\n"
      "v_xor_b32 v243, 0x10000, v243\n"
      "v_mfma_f32_16x16x32_bf16 a[176:179], a[100:103], v[0:3], a[176:179]\n"
      "ds_read_b64_tr_b16 a[122:123], v244 offset:5120\n"
      "v_exp_f32 v112, v160\n"
      "v_mfma_f32_16x16x32_bf16 a[160:163], a[112:115], v[16:19], a[160:163]\n"
      "s_mov_b32 s64, 6\n"
      "ds_read_b64_tr_b16 a[124:125], v245 offset:1024\n"
      "v_exp_f32 v113, v161\n"
      "v_exp_f32 v114, v162\n"
      "v_mfma_f32_16x16x32_bf16 a[176:179], a[116:119], v[16:19], a[176:179]\n"
      "s_add_u32 m0, %[wb], 114688\n"
      "ds_read_b64_tr_b16 a[126:127], v245 offset:5120\n"
      "v_cvt_pk_bf16_f32 v96, v112, v113\n"
      "v_exp_f32 v115, v163\n"
      "v_mfma_f32_16x16x32_bf16 a[164:167], a[96:99], v[4:7], a[164:167]\n"
      "buffer_load_dwordx4 v249, s[24:27], s95 offen nt lds\n"
      "v_mov_b32_dpp v117, v228 quad_perm:[0,1,2,3] row_mask:0x2 bank_mask:0xf\n"
      "v_mfma_f32_16x16x32_bf16 a[180:183], a[100:103], v[4:7], a[180:183]\n"
      "s_add_u32 m0, %[wb], 122880\n"
      "v_exp_f32 v112, v164\n"
      "v_cvt_pk_bf16_f32 v97, v114, v115\n"
      "v_mfma_f32_16x16x32_bf16 a[164:167], a[112:115], v[20:23], a[164:167]\n"
      "buffer_load_dwordx4 v122, s[24:27], s95 offen nt lds\n"
      "v_mfma_f32_16x16x32_bf16 a[180:183], a[116:119], v[20:23], a[180:183]\n"
      "ds_write_b128 v125, a[160:163] offset:0\n"
      "v_exp_f32 v113, v165\n"
      "v_or_b32 v116, v96, v97\n"
      "v_mfma_f32_16x16x32_bf16 a[168:171], a[96:99], v[8:11], a[168:171]\n"
      "v_mov_b32_dpp v117, v232 quad_perm:[0,1,2,3] row_mask:0x4 bank_mask:0xf\n"
      "v_exp_f32 v114, v166\n"
      "v_cvt_pk_bf16_f32 v98, v112, v113\n"
      "v_mfma_f32_16x16x32_bf16 a[184:187], a[100:103], v[8:11], a[184:187]\n"
      "ds_write_b128 v125, a[176:179] offset:4096\n"
      "v_exp_f32 v115, v167\n"
      "v_mfma_f32_16x16x32_bf16 a[168:171], a[112:115], v[24:27], a[168:171]\n"
      "v_exp_f32 v112, v168\n"
      "v_cvt_pk_bf16_f32 v99, v114, v115\n"
      "v_mfma_f32_16x16x32_bf16 a[184:187], a[116:119], v[24:27], a[184:187]\n"
      "v_exp_f32 v113, v169\n"
      "v_or3_b32 v116, v116, v98, v99\n"
      "v_mfma_f32_16x16x32_bf16 a[172:175], a[96:99], v[12:15], a[172:175]\n"
      "ds_write_b128 v125, v[96:99] offset:8192\n"
      "v_mov_b32_dpp v117, v236 quad_perm:[0,1,2,3] row_mask:0x8 bank_mask:0xf\n"
      "v_exp_f32 v114, v170\n"
      "v_mfma_f32_16x16x32_bf16 a[188:191], a[100:103], v[12:15], a[188:191]\n"
      "v_cvt_pk_bf16_f32 v100, v112, v113\n"
      "v_exp_f32 v115, v171\n"
      "v_mfma_f32_16x16x32_bf16 a[172:175], a[112:115], v[28:31], a[172:175]\n"
      "v_exp_f32 v112, v172\n"
      "v_cvt_pk_bf16_f32 v101, v114, v115\n"
      "v_exp_f32 v113, v173\n"
      "v_mfma_f32_16x16x32_bf16 a[188:191], a[116:119], v[28:31], a[188:191]\n"
      "s_add_u32 m0, %[wb], 16384\n"
      "ds_write_b128 v125, a[164:167] offset:1024\n"
      "v_or3_b32 v116, v116, v100, v101\n"
      "v_mfma_f32_16x16x32_bf16 a[192:195], a[196:199], v[16:19], a[192:195]\n"
      "buffer_load_dwordx4 v249, s[24:27], s77 offen nt lds\n"
      "ds_write_b32 v125, v117 offset:12292\n"
      "v_mfma_f32_16x16x32_bf16 a[192:195], a[200:203], v[20:23], a[192:195]\n"
      "s_add_u32 m0, %[wb], 24576\n"
      "v_exp_f32 v114, v174\n"
      "v_cvt_pk_bf16_f32 v102, v112, v113\n"
      "v_mfma_f32_16x16x32_bf16 a[192:195], a[204:207], v[24:27], a[192:195]\n"
      "buffer_load_dwordx4 v122, s[24:27], s77 offen nt lds\n"
      "ds_write_b128 v125, a[180:183] offset:5120\n"
      "ds_write_b32 v125, v123 offset:12296\n"
      "v_mfma_f32_16x16x32_bf16 a[192:195], a[208:211], v[28:31], a[192:195]\n"
      "s_waitcnt lgkmcnt(7)\n"
      "v_exp_f32 v115, v175\n"
      "v_exp_f32 v112, v176\n"
      "v_mfma_f32_16x16x32_bf16 v[64:67], a[104:107], v[0:3], v[64:67]\n"
      "v_cvt_pk_bf16_f32 v103, v114, v115\n"
      "v_exp_f32 v113, v177\n"
      "v_mfma_f32_16x16x32_bf16 v[68:71], a[104:107], v[4:7], v[68:71]\n"
      "ds_write_b128 v125, v[100:103] offset:9216\n"
      "v_or3_b32 v116, v116, v102, v103\n"
      "v_exp_f32 v114, v178\n"
      "s_waitcnt vmcnt(8)\n"
      "s_barrier\n"
      "v_mfma_f32_16x16x32_bf16 v[72:75], a[104:107], v[8:11], v[72:75]\n"
      "ds_read_b64_tr_b16 a[212:213], v244 offset:33792\n"
      "v_cvt_pk_bf16_f32 v104, v112, v113\n"
      "v_mfma_f32_16x16x32_bf16 v[76:79], a[104:107], v[12:15], v[76:79]\n"
      "ds_write_b128 v125, a[168:171] offset:2048\n"
      "ds_read_b64_tr_b16 a[214:215], v244 offset:37888\n"
      "v_exp_f32 v115, v179\n"
      "v_mfma_f32_16x16x32_bf16 a[228:231], a[196:199], v[96:99], a[192:195]\n"
      "ds_read_b64_tr_b16 a[216:217], v245 offset:33792\n"
      "v_exp_f32 v112, v180\n"
      "v_cvt_pk_bf16_f32 v105, v114, v115\n"
      "v_mfma_f32_16x16x32_bf16 v[80:83], a[108:111], v[0:3], v[80:83]\n"
      "ds_read_b64_tr_b16 a[218:219], v245 offset:37888\n"
      "v_exp_f32 v113, v181\n"
      "v_mfma_f32_16x16x32_bf16 v[84:87], a[108:111], v[4:7], v[84:87]\n"
      "ds_write_b128 v125, a[184:187] offset:6144\n"
      "ds_read_b64_tr_b16 a[220:221], v248 offset:33792\n"
      "v_or3_b32 v116, v116, v104, v105\n"
      "v_mfma_f32_16x16x32_bf16 v[88:91], a[108:111], v[8:11], v[88:91]\n"
      "ds_read_b64_tr_b16 a[222:223], v248 offset:37888\n"
      "v_exp_f32 v114, v182\n"
      "v_cvt_pk_bf16_f32 v106, v112, v113\n"
      "v_mfma_f32_16x16x32_bf16 v[92:95], a[108:111], v[12:15], v[92:95]\n"
      "ds_read_b64_tr_b16 a[224:225], v120 offset:33792\n"
      "v_exp_f32 v115, v183\n"
      "v_mfma_f32_16x16x32_bf16 v[64:67], a[120:123], v[16:19], v[64:67]\n"
      "s_add_u32 m0, %[wb], 82944\n"
      "ds_write_b128 v125, a[172:175] offset:3072\n"
      "ds_read_b64_tr_b16 a[226:227], v120 offset:37888\n"
      "v_exp_f32 v112, v184\n"
      "v_mfma_f32_16x16x32_bf16 v[68:71], a[120:123], v[20:23], v[68:71]\n"
      "buffer_load_dwordx4 v249, s[24:27], s78 offen nt lds\n"
      "v_mfma_f32_16x16x32_bf16 v[72:75], a[120:123], v[24:27], v[72:75]\n"
      "s_add_u32 m0, %[wb], 91136\n"
      "v_cvt_pk_bf16_f32 v107, v114, v115\n"
      "v_exp_f32 v113, v185\n"
      "v_or3_b32 v116, v116, v106, v107\n"
      "v_mfma_f32_16x16x32_bf16 v[76:79], a[120:123], v[28:31], v[76:79]\n"
      "buffer_load_dwordx4 v122, s[24:27], s78 offen nt lds\n"
      "ds_write_b128 v125, v[104:107] offset:10240\n"
      "v_mfma_f32_16x16x32_bf16 a[228:231], a[200:203], v[100:103], a[228:231]\n"
      "v_exp_f32 v114, v186\n"
      "v_cvt_pk_bf16_f32 v108, v112, v113\n"
      "v_mfma_f32_16x16x32_bf16 v[80:83], a[124:127], v[16:19], v[80:83]\n"
      "v_exp_f32 v115, v187\n"
      "v_exp_f32 v112, v188\n"
      "v_mfma_f32_16x16x32_bf16 v[84:87], a[124:127], v[20:23], v[84:87]\n"
      "ds_write_b128 v125, a[188:191] offset:7168\n"
      "v_cvt_pk_bf16_f32 v109, v114, v115\n"
      "v_mfma_f32_16x16x32_bf16 v[88:91], a[124:127], v[24:27], v[88:91]\n"
      "v_exp_f32 v113, v189\n"
      "v_or3_b32 v116, v116, v108, v109\n"
      "v_exp_f32 v114, v190\n"
      "v_mfma_f32_16x16x32_bf16 v[92:95], a[124:127], v[28:31], v[92:95]\n"
      "v_cvt_pk_bf16_f32 v110, v112, v113\n"
      "v_exp_f32 v115, v191\n"
      "s_nop 0\n"
      "v_mfma_f32_16x16x32_bf16 a[228:231], a[204:207], v[104:107], a[228:231]\n"
      "v_cvt_pk_bf16_f32 v111, v114, v115\n"
      "v_or3_b32 v116, v116, v110, v111\n"
      "v_and_b32 v116, 0x40004000, v116\n"
      "v_cmp_ne_u32 vcc, 0, v116\n"
      "s_cbranch_vccnz FB1_%=\n"
      "RET1_6_%=:\n"
      "ds_write_b128 v125, v[108:111] offset:11264\n"
      "v_mfma_f32_16x16x32_bf16 a[228:231], a[208:211], v[108:111], a[228:231]\n"
      "s_waitcnt lgkmcnt(9)\n"
      "v_mfma_f32_16x16x32_bf16 v[64:67], a[212:215], v[96:99], v[64:67]\n"
      "v_mov_b32 v148, 1.0\n"
      "v_mfma_f32_16x16x32_bf16 v[80:83], a[216:219], v[96:99], v[80:83]\n"
      "v_mov_b32 v149, 1.0\n"
      "v_mfma_f32_16x16x32_bf16 v[68:71], a[212:215], v[100:103], v[68:71]\n"
      "v_mov_b32 v157, %[fo]\n"
      "v_mfma_f32_16x16x32_bf16 v[84:87], a[216:219], v[100:103], v[84:87]\n"
      "v_mov_b32 v150, s72\n"
      "v_mfma_f32_16x16x32_bf16 v[72:75], a[212:215], v[104:107], v[72:75]\n"
      "v_mov_b32 v151, s76\n"
      "ds_write_b32 v125, a228 offset:12288\n"
      "v_mfma_f32_16x16x32_bf16 v[88:91], a[216:219], v[104:107], v[88:91]\n"
      "v_xor_b32 v158, 8, v157\n"
      "v_mfma_f32_16x16x32_bf16 v[76:79], a[212:215], v[108:111], v[76:79]\n"
      "v_mfma_f32_16x16x32_bf16 v[92:95], a[216:219], v[108:111], v[92:95]\n"
      "s_waitcnt lgkmcnt(0)\n"
      "s_barrier\n"
      "s_memrealtime s[68:69]\n"
      "ds_read_b32 v237, v127 offset:12296\n"
      "ds_read_b64 v[152:153], v126 offset:12288\n"
      "ds_read_b128 v[160:163], v126 offset:0\n"
      "ds_read_b128 v[176:179], v126 offset:4096\n"
      "ds_read_b128 v[164:167], v126 offset:1024\n"
      "ds_read_b128 v[180:183], v126 offset:5120\n"
      "ds_read_b128 v[168:171], v126 offset:2048\n"
      "ds_read_b128 v[184:187], v126 offset:6144\n"
      "ds_read_b128 v[172:175], v126 offset:3072\n"
      "ds_read_b128 v[188:191], v126 offset:7168\n"
      "ds_read_b128 a[64:67], v126 offset:8192\n"
      "ds_read_b128 a[68:71], v126 offset:9216\n"
      "ds_read_b128 a[72:75], v126 offset:10240\n"
      "ds_read_b128 a[76:79], v126 offset:11264\n"
      "v_accvgpr_read_b32 v144, a228\n"
      "v_and_b32 v118, 0xf0, v127\n"
      "v_lshrrev_b32 v156, 3, v127\n"
      "v_and_b32 v156, 32, v156\n"
      "v_lshl_or_b32 v156, v118, 4, v156\n"
      "v_lshrrev_b32 v118, 5, v127\n"
      "v_and_or_b32 v156, v118, 16, v156\n"
      "v_add_u32 v156, %[oo], v156\n"
      "v_add_u32 v155, 0x2000, v156\n"
      "v_xor_b32 v242, 0x2000, v240\n"
      "v_xor_b32 v243, 0x2000, v241\n"
      "v_mov_b32 v236, v150\n"
      "s_waitcnt lgkmcnt(12)\n"
      "v_cmp_neq_f32 vcc, v153, v117\n"
      "v_readfirstlane_b32 s88, v237\n"
      "s_cbranch_vccnz MIS_%=\n"
      "MR_%=:\n"
      "v_mul_f32 v144, v144, v148\n"
      "v_mov_b32 v145, v117\n"
      "v_fmac_f32 v144, v152, v149\n"
      "s_waitcnt lgkmcnt(10)\n"
      "v_pk_add_f32 v[64:65], v[64:65], v[160:161]\n"
      "v_pk_add_f32 v[66:67], v[66:67], v[162:163]\n"
      "v_pk_add_f32 v[80:81], v[80:81], v[176:177]\n"
      "v_pk_add_f32 v[82:83], v[82:83], v[178:179]\n"
      "s_waitcnt lgkmcnt(8)\n"
      "v_pk_add_f32 v[68:69], v[68:69], v[164:165]\n"
      "v_pk_add_f32 v[70:71], v[70:71], v[166:167]\n"
      "v_pk_add_f32 v[84:85], v[84:85], v[180:181]\n"
      "v_pk_add_f32 v[86:87], v[86:87], v[182:183]\n"
      "s_waitcnt lgkmcnt(6)\n"
      "v_pk_add_f32 v[72:73], v[72:73], v[168:169]\n"
      "v_pk_add_f32 v[74:75], v[74:75], v[170:171]\n"
      "v_pk_add_f32 v[88:89], v[88:89], v[184:185]\n"
      "v_pk_add_f32 v[90:91], v[90:91], v[186:187]\n"
      "s_waitcnt lgkmcnt(4)\n"
      "v_pk_add_f32 v[76:77], v[76:77], v[172:173]\n"
      "v_pk_add_f32 v[78:79], v[78:79], v[174:175]\n"
      "v_pk_add_f32 v[92:93], v[92:93], v[188:189]\n"
      "v_pk_add_f32 v[94:95], v[94:95], v[190:191]\n"
      "s_waitcnt lgkmcnt(0)\n"
      "s_add_u32 s65, s68, 55\n"
      "s_mov_b32 s66, s88\n"
      "s_sub_u32 s67, s65, s66\n"
      "s_cmp_lt_i32 s67, 0\n"
      "s_cselect_b32 s88, s66, s65\n"
      "v_mov_b32 v237, s88\n"
      "s_mov_b32 s88, s66\n"
      "s_cmp_lg_u32 %[xw], 0\n"
      "s_cbranch_scc1 NH_%=\n"
      "s_mov_b64 exec, 1\n"
      "global_store_dwordx2 v157, v[236:237], s[18:19] sc1\n"
      "s_mov_b64 exec, -1\n"
      "NH_%=:\n"
      "v_mfma_f32_16x16x32_bf16 v[64:67], a[220:223], a[64:67], v[64:67]\n"
      "v_mfma_f32_16x16x32_bf16 v[80:83], a[224:227], a[64:67], v[80:83]\n"
      "v_mfma_f32_16x16x32_bf16 v[68:71], a[220:223], a[68:71], v[68:71]\n"
      "v_mfma_f32_16x16x32_bf16 v[84:87], a[224:227], a[68:71], v[84:87]\n"
      "v_mfma_f32_16x16x32_bf16 v[72:75], a[220:223], a[72:75], v[72:75]\n"
      "v_mfma_f32_16x16x32_bf16 v[88:91], a[224:227], a[72:75], v[88:91]\n"
      "v_mfma_f32_16x16x32_bf16 v[76:79], a[220:223], a[76:79], v[76:79]\n"
      "v_mfma_f32_16x16x32_bf16 v[92:95], a[224:227], a[76:79], v[92:95]\n"
      "s_nop 1\n"
      "s_memrealtime s[68:69]\n"
      "v_cvt_pk_bf16_f32 v128, v64, v65\n"
      "v_cvt_pk_bf16_f32 v129, v66, v67\n"
      "v_cvt_pk_bf16_f32 v130, v80, v81\n"
      "v_cvt_pk_bf16_f32 v131, v82, v83\n"
      "v_cvt_pk_bf16_f32 v132, v68, v69\n"
      "v_cvt_pk_bf16_f32 v133, v70, v71\n"
      "v_cvt_pk_bf16_f32 v134, v84, v85\n"
      "v_cvt_pk_bf16_f32 v135, v86, v87\n"
      "v_cvt_pk_bf16_f32 v136, v72, v73\n"
      "v_cvt_pk_bf16_f32 v137, v74, v75\n"
      "v_cvt_pk_bf16_f32 v138, v88, v89\n"
      "v_cvt_pk_bf16_f32 v139, v90, v91\n"
      "v_cvt_pk_bf16_f32 v140, v76, v77\n"
      "v_cvt_pk_bf16_f32 v141, v78, v79\n"
      "v_cvt_pk_bf16_f32 v142, v92, v93\n"
      "v_cvt_pk_bf16_f32 v143, v94, v95\n"
      "v_mad_u32_u24 v147, v144, s60, v151\n"
      "v_mad_u32_u24 v147, v145, s62, v147\n"
      "v_add3_u32 v146, v150, v144, v145\n"
      "v_mad_u32_u24 v147, v128, s60, v147\n"
      "v_mad_u32_u24 v147, v129, s62, v147\n"
      "v_add3_u32 v146, v146, v128, v129\n"
      "v_mad_u32_u24 v147, v130, s70, v147\n"
      "v_mad_u32_u24 v147, v131, s71, v147\n"
      "v_add3_u32 v146, v146, v130, v131\n"
      "v_mad_u32_u24 v147, v132, s60, v147\n"
      "v_mad_u32_u24 v147, v133, s62, v147\n"
      "v_add3_u32 v146, v146, v132, v133\n"
      "v_mad_u32_u24 v147, v134, s70, v147\n"
      "v_mad_u32_u24 v147, v135, s71, v147\n"
      "v_add3_u32 v146, v146, v134, v135\n"
      "v_mad_u32_u24 v147, v136, s60, v147\n"
      "v_mad_u32_u24 v147, v137, s62, v147\n"
      "v_add3_u32 v146, v146, v136, v137\n"
      "v_mad_u32_u24 v147, v138, s70, v147\n"
      "v_mad_u32_u24 v147, v139, s71, v147\n"
      "v_add3_u32 v146, v146, v138, v139\n"
      "v_mad_u32_u24 v147, v140, s60, v147\n"
      "v_mad_u32_u24 v147, v141, s62, v147\n"
      "v_add3_u32 v146, v146, v140, v141\n"
      "v_mad_u32_u24 v147, v142, s70, v147\n"
      "v_mad_u32_u24 v147, v143, s71, v147\n"
      "v_add3_u32 v146, v146, v142, v143\n"
      "s_waitcnt lgkmcnt(0)\n"
      "s_call_b64 s[84:85], SPIN_%=\n"
      "global_load_dwordx2 v[232:233], v158, s[18:19] sc1\n"
      "buffer_store_dwordx4 v[128:131], v127, s[28:31], %[xo] offen offset:0 sc1\n"
      "buffer_store_dwordx4 v[132:135], v127, s[28:31], %[xo] offen offset:1024 sc1\n"
      "buffer_store_dwordx4 v[136:139], v127, s[28:31], %[xo] offen offset:2048 sc1\n"
      "buffer_store_dwordx4 v[140:143], v127, s[28:31], %[xo] offen offset:3072 sc1\n"
      "buffer_store_dwordx4 v[144:147], v127, s[28:31], s81 offen sc1\n"
      "global_load_dwordx2 v[234:235], v158, s[18:19] sc1\n"
      "s_waitcnt vmcnt(7)\n"
      "s_barrier\n"
      "ds_read_b64_tr_b16 a[96:97], v246 offset:32768\n"
      "ds_read_b64_tr_b16 a[98:99], v246 offset:36864\n"
      "ds_read_b64_tr_b16 a[100:101], v247 offset:32768\n"
      "ds_read_b64_tr_b16 a[102:103], v247 offset:36864\n"
      "ds_read_b64_tr_b16 a[112:113], v250 offset:0\n"
      "ds_read_b64_tr_b16 a[114:115], v250 offset:4096\n"
      "ds_read_b64_tr_b16 a[116:117], v251 offset:0\n"
      "ds_read_b64_tr_b16 a[118:119], v251 offset:4096\n"
      "ds_read_b64_tr_b16 a[0:1], v250 offset:32768\n"
      "ds_read_b64_tr_b16 a[2:3], v250 offset:36864\n"
      "ds_read_b64_tr_b16 a[4:5], v251 offset:32768\n"
      "ds_read_b64_tr_b16 a[6:7], v251 offset:36864\n"
      "s_waitcnt lgkmcnt(8)\n"
      "v_mfma_f32_16x16x32_bf16 a[128:131], a[96:99], v[208:211], a[128:131]\n"
      "ds_read_b64_tr_b16 a[104:105], v244 offset:32768\n"
      "ds_read_b64_tr_b16 a[106:107], v244 offset:36864\n"
      "v_mfma_f32_16x16x32_bf16 a[144:147], a[100:103], v[208:211], a[144:147]\n"
      "ds_read_b64_tr_b16 a[108:109], v245 offset:32768\n"
      "ds_read_b64_tr_b16 a[110:111], v245 offset:36864\n"
      "s_waitcnt lgkmcnt(8)\n"
      "v_mfma_f32_16x16x32_bf16 a[128:131], a[112:115], v[192:195], a[128:131]\n"
      "ds_read_b64_tr_b16 a[120:121], v240 offset:0\n"
      "ds_read_b64_tr_b16 a[122:123], v240 offset:4096\n"
      "v_mfma_f32_16x16x32_bf16 a[144:147], a[116:119], v[192:195], a[144:147]\n"
      "ds_read_b64_tr_b16 a[124:125], v241 offset:0\n"
      "ds_read_b64_tr_b16 a[126:127], v241 offset:4096\n"
      "s_waitcnt lgkmcnt(8)\n"
      "v_mfma_f32_16x16x32_bf16 a[128:131], a[0:3], v[0:3], a[128:131]\n"
      "ds_read_b64_tr_b16 a[8:9], v240 offset:32768\n"
      "ds_read_b64_tr_b16 a[10:11], v240 offset:36864\n"
      "v_mfma_f32_16x16x32_bf16 a[144:147], a[4:7], v[0:3], a[144:147]\n"
      "ds_read_b64_tr_b16 a[12:13], v241 offset:32768\n"
      "ds_read_b64_tr_b16 a[14:15], v241 offset:36864\n"
      "v_mfma_f32_16x16x32_bf16 a[132:135], a[96:99], v[212:215], a[132:135]\n"
      "ds_write_b128 v125, v[16:19] offset:8192\n"
      "v_lshrrev_b32 v118, 2, v127\n"
      "v_mfma_f32_16x16x32_bf16 a[148:151], a[100:103], v[212:215], a[148:151]\n"
      "v_xor_b32 v118, v118, v127\n"
      "v_mfma_f32_16x16x32_bf16 a[132:135], a[112:115], v[196:199], a[132:135]\n"
      "v_and_b32 v118, 0xc0, v118\n"
      "v_mfma_f32_16x16x32_bf16 a[148:151], a[116:119], v[196:199], a[148:151]\n"
      "v_and_or_b32 v118, v127, 32, v118\n"
      "v_mfma_f32_16x16x32_bf16 a[132:135], a[0:3], v[4:7], a[132:135]\n"
      "ds_write_b128 v125, v[20:23] offset:9216\n"
      "v_and_b32 v119, 16, v127\n"
      "v_mfma_f32_16x16x32_bf16 a[148:151], a[4:7], v[4:7], a[148:151]\n"
      "v_mov_b32 v84, 0x3f80\n"
      "v_mfma_f32_16x16x32_bf16 a[136:139], a[96:99], v[216:219], a[136:139]\n"
      "v_lshlrev_b32 v119, v119, v84\n"
      "v_mfma_f32_16x16x32_bf16 a[152:155], a[100:103], v[216:219], a[152:155]\n"
      "v_cmp_eq_u32 vcc, 0, v118\n"
      "v_mfma_f32_16x16x32_bf16 a[136:139], a[112:115], v[200:203], a[136:139]\n"
      "ds_write_b128 v125, v[24:27] offset:10240\n"
      "v_mov_b32 v84, 0\n"
      "v_mfma_f32_16x16x32_bf16 a[152:155], a[116:119], v[200:203], a[152:155]\n"
      "v_cndmask_b32 v86, 0, v119, vcc\n"
      "v_mfma_f32_16x16x32_bf16 a[136:139], a[0:3], v[8:11], a[136:139]\n"
      "v_cmp_eq_u32 vcc, 32, v118\n"
      "v_mfma_f32_16x16x32_bf16 a[152:155], a[4:7], v[8:11], a[152:155]\n"
      "v_mov_b32 v85, 0\n"
      "v_mfma_f32_16x16x32_bf16 a[140:143], a[96:99], v[220:223], a[140:143]\n"
      "ds_write_b128 v125, v[28:31] offset:11264\n"
      "v_cndmask_b32 v87, 0, v119, vcc\n"
      "v_mfma_f32_16x16x32_bf16 a[156:159], a[100:103], v[220:223], a[156:159]\n"
      "v_mov_b32 v88, 0\n"
      "v_mfma_f32_16x16x32_bf16 a[140:143], a[112:115], v[204:207], a[140:143]\n"
      "v_mov_b32 v89, 0\n"
      "v_mfma_f32_16x16x32_bf16 a[156:159], a[116:119], v[204:207], a[156:159]\n"
      "v_mfma_f32_16x16x32_bf16 a[140:143], a[0:3], v[12:15], a[140:143]\n"
      "ds_write_b128 v125, a[128:131] offset:0\n"
      "v_mfma_f32_16x16x32_bf16 a[156:159], a[4:7], v[12:15], a[156:159]\n"
      "s_waitcnt lgkmcnt(13)\n"
      "v_mfma_f32_16x16x32_bf16 v[32:35], a[104:107], v[208:211], v[32:35]\n"
      "ds_read_b64_tr_b16 a[32:33], v240 offset:1024\n"
      "v_mfma_f32_16x16x32_bf16 v[36:39], a[104:107], v[212:215], v[36:39]\n"
      "ds_read_b64_tr_b16 a[34:35], v240 offset:5120\n"
      "v_mfma_f32_16x16x32_bf16 v[40:43], a[104:107], v[216:219], v[40:43]\n"
      "ds_write_b128 v125, a[144:147] offset:4096\n"
      "ds_read_b64_tr_b16 a[36:37], v241 offset:1024\n"
      "v_mfma_f32_16x16x32_bf16 v[44:47], a[104:107], v[220:223], v[44:47]\n"
      "ds_read_b64_tr_b16 a[38:39], v241 offset:5120\n"
      "v_mfma_f32_16x16x32_bf16 v[48:51], a[108:111], v[208:211], v[48:51]\n"
      "ds_read_b64_tr_b16 a[40:41], v242 offset:1024\n"
      "v_mfma_f32_16x16x32_bf16 v[52:55], a[108:111], v[212:215], v[52:55]\n"
      "ds_read_b64_tr_b16 a[42:43], v242 offset:5120\n"
      "v_mfma_f32_16x16x32_bf16 v[56:59], a[108:111], v[216:219], v[56:59]\n"
      "ds_write_b128 v125, a[132:135] offset:1024\n"
      "ds_read_b64_tr_b16 a[44:45], v243 offset:1024\n"
      "v_mfma_f32_16x16x32_bf16 v[60:63], a[108:111], v[220:223], v[60:63]\n"
      "ds_read_b64_tr_b16 a[46:47], v243 offset:5120\n"
      "s_waitcnt lgkmcnt(15)\n"
      "v_mfma_f32_16x16x32_bf16 v[32:35], a[120:123], v[192:195], v[32:35]\n"
      "ds_read_b64_tr_b16 a[16:17], v244 offset:0\n"
      "v_mfma_f32_16x16x32_bf16 v[36:39], a[120:123], v[196:199], v[36:39]\n"
      "ds_read_b64_tr_b16 a[18:19], v244 offset:4096\n"
      "v_mfma_f32_16x16x32_bf16 v[40:43], a[120:123], v[200:203], v[40:43]\n"
      "ds_write_b128 v125, a[148:151] offset:5120\n"
      "ds_read_b64_tr_b16 a[20:21], v245 offset:0\n"
      "v_mfma_f32_16x16x32_bf16 v[44:47], a[120:123], v[204:207], v[44:47]\n"
      "ds_read_b64_tr_b16 a[22:23], v245 offset:4096\n"
      "v_mfma_f32_16x16x32_bf16 v[48:51], a[124:127], v[192:195], v[48:51]\n"
      "ds_read_b64_tr_b16 a[24:25], v248 offset:0\n"
      "v_mfma_f32_16x16x32_bf16 v[52:55], a[124:127], v[196:199], v[52:55]\n"
      "ds_read_b64_tr_b16 a[26:27], v248 offset:4096\n"
      "v_mfma_f32_16x16x32_bf16 v[56:59], a[124:127], v[200:203], v[56:59]\n"
      "ds_write_b128 v125, a[136:139] offset:2048\n"
      "ds_read_b64_tr_b16 a[28:29], v120 offset:0\n"
      "v_mfma_f32_16x16x32_bf16 v[60:63], a[124:127], v[204:207], v[60:63]\n"
      "ds_read_b64_tr_b16 a[30:31], v120 offset:4096\n"
      "s_waitcnt lgkmcnt(15)\n"
      "v_mfma_f32_16x16x32_bf16 v[32:35], a[8:11], v[0:3], v[32:35]\n"
      "v_mfma_f32_16x16x32_bf16 v[36:39], a[8:11], v[4:7], v[36:39]\n"
      "v_mfma_f32_16x16x32_bf16 v[40:43], a[8:11], v[8:11], v[40:43]\n"
      "ds_write_b128 v125, a[152:155] offset:6144\n"
      "v_mfma_f32_16x16x32_bf16 v[44:47], a[8:11], v[12:15], v[44:47]\n"
      "s_waitcnt vmcnt(6)\n"
      "v_cmp_ne_u32 vcc, v232, v150\n"
      "s_memrealtime s[68:69]\n"
      "s_cbranch_vccnz POLL_%=\n"
      "GOT_%=:\n"
      "v_readfirstlane_b32 s88, v233\n"
      "s_waitcnt lgkmcnt(0)\n"
      "s_add_u32 s88, s88, 60\n"
      "s_sub_u32 s67, s88, s68\n"
      "s_cmp_gt_u32 s67, 1000\n"
      "s_cbranch_scc1 LD_%=\n"
      "s_call_b64 s[84:85], SPIN_%=\n"
      "LD_%=:\n"
      "buffer_load_dwordx4 v[80:83], v127, s[28:31], s79 offen sc1\n"
      "buffer_load_dwordx4 v[64:67], v127, s[28:31], s80 offen offset:0 sc1\n"
      "buffer_load_dwordx4 v[68:71], v127, s[28:31], s80 offen offset:1024 sc1\n"
      "buffer_load_dwordx4 v[72:75], v127, s[28:31], s80 offen offset:2048 sc1\n"
      "buffer_load_dwordx4 v[76:79], v127, s[28:31], s80 offen offset:3072 sc1\n"
      "v_mfma_f32_16x16x32_bf16 v[48:51], a[12:15], v[0:3], v[48:51]\n"
      "v_mfma_f32_16x16x32_bf16 v[52:55], a[12:15], v[4:7], v[52:55]\n"
      "v_mfma_f32_16x16x32_bf16 v[56:59], a[12:15], v[8:11], v[56:59]\n"
      "ds_write_b128 v125, a[140:143] offset:3072\n"
      "v_mfma_f32_16x16x32_bf16 v[60:63], a[12:15], v[12:15], v[60:63]\n"
      "s_waitcnt lgkmcnt(15)\n"
      "v_mfma_f32_16x16x32_bf16 v[32:35], a[32:35], v[96:99], v[32:35]\n"
      "v_mfma_f32_16x16x32_bf16 v[48:51], a[36:39], v[96:99], v[48:51]\n"
      "v_mfma_f32_16x16x32_bf16 v[36:39], a[32:35], v[100:103], v[36:39]\n"
      "ds_write_b128 v125, a[156:159] offset:7168\n"
      "v_mfma_f32_16x16x32_bf16 v[52:55], a[36:39], v[100:103], v[52:55]\n"
      "v_mfma_f32_16x16x32_bf16 v[40:43], a[32:35], v[104:107], v[40:43]\n"
      "v_mfma_f32_16x16x32_bf16 v[56:59], a[36:39], v[104:107], v[56:59]\n"
      "v_mfma_f32_16x16x32_bf16 v[44:47], a[32:35], v[108:111], v[44:47]\n"
      "v_mfma_f32_16x16x32_bf16 v[60:63], a[36:39], v[108:111], v[60:63]\n"
      "s_waitcnt lgkmcnt(13)\n"
      "v_mfma_f32_16x16x32_bf16 v[32:35], a[40:43], a[64:67], v[32:35]\n"
      "v_mfma_f32_16x16x32_bf16 v[48:51], a[44:47], a[64:67], v[48:51]\n"
      "v_mfma_f32_16x16x32_bf16 v[36:39], a[40:43], a[68:71], v[36:39]\n"
      "v_mfma_f32_16x16x32_bf16 v[52:55], a[44:47], a[68:71], v[52:55]\n"
      "v_mfma_f32_16x16x32_bf16 v[40:43], a[40:43], a[72:75], v[40:43]\n"
      "v_mfma_f32_16x16x32_bf16 v[56:59], a[44:47], a[72:75], v[56:59]\n"
      "v_mfma_f32_16x16x32_bf16 v[44:47], a[40:43], a[76:79], v[44:47]\n"
      "v_mfma_f32_16x16x32_bf16 v[60:63], a[44:47], a[76:79], v[60:63]\n"
      "s_waitcnt lgkmcnt(8)\n"
      "v_mfma_f32_16x16x32_bf16 v[32:35], a[16:19], v[16:19], v[32:35]\n"
      "v_mfma_f32_16x16x32_bf16 v[48:51], a[20:23], v[16:19], v[48:51]\n"
      "v_mfma_f32_16x16x32_bf16 v[36:39], a[16:19], v[20:23], v[36:39]\n"
      "v_mfma_f32_16x16x32_bf16 v[52:55], a[20:23], v[20:23], v[52:55]\n"
      "v_mfma_f32_16x16x32_bf16 v[40:43], a[16:19], v[24:27], v[40:43]\n"
      "v_mfma_f32_16x16x32_bf16 v[56:59], a[20:23], v[24:27], v[56:59]\n"
      "v_mfma_f32_16x16x32_bf16 v[44:47], a[16:19], v[28:31], v[44:47]\n"
      "v_mfma_f32_16x16x32_bf16 v[60:63], a[20:23], v[28:31], v[60:63]\n"
      "s_waitcnt lgkmcnt(0)\n"
      "s_barrier\n"
      "ds_read_b128 v[160:163], v126 offset:0\n"
      "ds_read_b128 v[176:179], v126 offset:4096\n"
      "ds_read_b128 v[164:167], v126 offset:1024\n"
      "ds_read_b128 v[180:183], v126 offset:5120\n"
      "ds_read_b128 v[168:171], v126 offset:2048\n"
      "ds_read_b128 v[184:187], v126 offset:6144\n"
      "ds_read_b128 v[172:175], v126 offset:3072\n"
      "ds_read_b128 v[188:191], v126 offset:7168\n"
      "ds_read_b128 a[80:83], v126 offset:8192\n"
      "ds_read_b128 a[84:87], v126 offset:9216\n"
      "ds_read_b128 a[88:91], v126 offset:10240\n"
      "ds_read_b128 a[92:95], v126 offset:11264\n"
      "s_waitcnt lgkmcnt(10)\n"
      "v_pk_add_f32 v[32:33], v[32:33], v[160:161]\n"
      "v_pk_add_f32 v[34:35], v[34:35], v[162:163]\n"
      "v_pk_add_f32 v[48:49], v[48:49], v[176:177]\n"
      "v_pk_add_f32 v[50:51], v[50:51], v[178:179]\n"
      "s_waitcnt lgkmcnt(8)\n"
      "v_pk_add_f32 v[36:37], v[36:37], v[164:165]\n"
      "v_pk_add_f32 v[38:39], v[38:39], v[166:167]\n"
      "v_pk_add_f32 v[52:53], v[52:53], v[180:181]\n"
      "v_pk_add_f32 v[54:55], v[54:55], v[182:183]\n"
      "s_waitcnt lgkmcnt(6)\n"
      "v_pk_add_f32 v[40:41], v[40:41], v[168:169]\n"
      "v_pk_add_f32 v[42:43], v[42:43], v[170:171]\n"
      "v_pk_add_f32 v[56:57], v[56:57], v[184:185]\n"
      "v_pk_add_f32 v[58:59], v[58:59], v[186:187]\n"
      "s_waitcnt lgkmcnt(4)\n"
      "v_pk_add_f32 v[44:45], v[44:45], v[172:173]\n"
      "v_pk_add_f32 v[46:47], v[46:47], v[174:175]\n"
      "v_pk_add_f32 v[60:61], v[60:61], v[188:189]\n"
      "v_pk_add_f32 v[62:63], v[62:63], v[190:191]\n"
      "s_waitcnt lgkmcnt(0)\n"
      "v_mfma_f32_16x16x32_bf16 v[32:35], a[24:27], a[80:83], v[32:35]\n"
      "v_mfma_f32_16x16x32_bf16 v[48:51], a[28:31], a[80:83], v[48:51]\n"
      "v_mfma_f32_16x16x32_bf16 v[36:39], a[24:27], a[84:87], v[36:39]\n"
      "v_mfma_f32_16x16x32_bf16 v[52:55], a[28:31], a[84:87], v[52:55]\n"
      "v_mfma_f32_16x16x32_bf16 v[40:43], a[24:27], a[88:91], v[40:43]\n"
      "v_mfma_f32_16x16x32_bf16 v[56:59], a[28:31], a[88:91], v[56:59]\n"
      "v_mfma_f32_16x16x32_bf16 v[44:47], a[24:27], a[92:95], v[44:47]\n"
      "v_mfma_f32_16x16x32_bf16 v[60:63], a[28:31], a[92:95], v[60:63]\n"
      "CHK_%=:\n"
      "s_waitcnt vmcnt(4)\n"
      "v_add_f32 v224, v144, v80\n"
      "v_rcp_f32 v224, v224\n"
      "v_mad_u32_u24 v119, v80, s60, v151\n"
      "v_mad_u32_u24 v119, v81, s62, v119\n"
      "v_add3_u32 v118, v150, v80, v81\n"
      "s_waitcnt vmcnt(3)\n"
      "v_mfma_f32_16x16x32_bf16 v[160:163], v[86:89], v[64:67], v[32:35]\n"
      "v_mad_u32_u24 v119, v64, s60, v119\n"
      "v_mad_u32_u24 v119, v65, s62, v119\n"
      "v_add3_u32 v118, v118, v64, v65\n"
      "v_mov_b32 v226, v224\n"
      "v_mfma_f32_16x16x32_bf16 v[176:179], v[84:87], v[64:67], v[48:51]\n"
      "v_mad_u32_u24 v119, v66, s70, v119\n"
      "v_mad_u32_u24 v119, v67, s71, v119\n"
      "v_add3_u32 v118, v118, v66, v67\n"
      "v_permlane32_swap_b32 v224, v226\n"
      "s_waitcnt vmcnt(2)\n"
      "v_mfma_f32_16x16x32_bf16 v[164:167], v[86:89], v[68:71], v[36:39]\n"
      "v_mad_u32_u24 v119, v68, s60, v119\n"
      "v_mad_u32_u24 v119, v69, s62, v119\n"
      "v_add3_u32 v118, v118, v68, v69\n"
      "v_mov_b32 v225, v224\n"
      "v_mov_b32 v227, v226\n"
      "v_mfma_f32_16x16x32_bf16 v[180:183], v[84:87], v[68:71], v[52:55]\n"
      "v_mad_u32_u24 v119, v70, s70, v119\n"
      "v_mad_u32_u24 v119, v71, s71, v119\n"
      "v_add3_u32 v118, v118, v70, v71\n"
      "v_permlane16_swap_b32 v224, v225\n"
      "s_waitcnt vmcnt(1)\n"
      "v_mfma_f32_16x16x32_bf16 v[168:171], v[86:89], v[72:75], v[40:43]\n"
      "v_mad_u32_u24 v119, v72, s60, v119\n"
      "v_mad_u32_u24 v119, v73, s62, v119\n"
      "v_add3_u32 v118, v118, v72, v73\n"
      "v_permlane16_swap_b32 v226, v227\n"
      "v_mfma_f32_16x16x32_bf16 v[184:187], v[84:87], v[72:75], v[56:59]\n"
      "v_mad_u32_u24 v119, v74, s70, v119\n"
      "v_mad_u32_u24 v119, v75, s71, v119\n"
      "v_add3_u32 v118, v118, v74, v75\n"
      "s_waitcnt vmcnt(0)\n"
      "v_mfma_f32_16x16x32_bf16 v[172:175], v[86:89], v[76:79], v[44:47]\n"
      "v_mad_u32_u24 v119, v76, s60, v119\n"
      "v_mad_u32_u24 v119, v77, s62, v119\n"
      "v_add3_u32 v118, v118, v76, v77\n"
      "v_mfma_f32_16x16x32_bf16 v[188:191], v[84:87], v[76:79], v[60:63]\n"
      "v_mad_u32_u24 v119, v78, s70, v119\n"
      "v_mad_u32_u24 v119, v79, s71, v119\n"
      "v_add3_u32 v118, v118, v78, v79\n"
      "v_cmp_ne_u64 vcc, v[118:119], v[82:83]\n"
      "s_cbranch_vccnz RETRY_%=\n"
      "OK_%=:\n"
      "v_cmp_neq_f32 vcc, v81, v117\n"
      "s_cbranch_vccnz FIX_%=\n"
      "FR_%=:\n"
      "v_pk_mul_f32 v[160:161], v[160:161], v[224:225] op_sel:[0,0] op_sel_hi:[1,0]\n"
      "v_pk_mul_f32 v[162:163], v[162:163], v[224:225] op_sel:[0,0] op_sel_hi:[1,0]\n"
      "v_pk_mul_f32 v[176:177], v[176:177], v[224:225] op_sel:[0,0] op_sel_hi:[1,0]\n"
      "v_pk_mul_f32 v[178:179], v[178:179], v[224:225] op_sel:[0,0] op_sel_hi:[1,0]\n"
      "v_cvt_pk_bf16_f32 v128, v160, v161\n"
      "v_cvt_pk_bf16_f32 v130, v176, v177\n"
      "v_cvt_pk_bf16_f32 v129, v162, v163\n"
      "v_cvt_pk_bf16_f32 v131, v178, v179\n"
      "v_pk_mul_f32 v[164:165], v[164:165], v[224:225] op_sel:[0,1] op_sel_hi:[1,1]\n"
      "v_permlane16_swap_b32 v128, v130\n"
      "v_permlane16_swap_b32 v129, v131\n"
      "global_store_dwordx4 v156, v[128:131], s[16:17] offset:-4096 sc1\n"
      "v_pk_mul_f32 v[166:167], v[166:167], v[224:225] op_sel:[0,1] op_sel_hi:[1,1]\n"
      "v_pk_mul_f32 v[180:181], v[180:181], v[224:225] op_sel:[0,1] op_sel_hi:[1,1]\n"
      "v_pk_mul_f32 v[182:183], v[182:183], v[224:225] op_sel:[0,1] op_sel_hi:[1,1]\n"
      "v_cvt_pk_bf16_f32 v132, v164, v165\n"
      "v_cvt_pk_bf16_f32 v134, v180, v181\n"
      "v_cvt_pk_bf16_f32 v133, v166, v167\n"
      "v_cvt_pk_bf16_f32 v135, v182, v183\n"
      "v_pk_mul_f32 v[168:169], v[168:169], v[226:227] op_sel:[0,0] op_sel_hi:[1,0]\n"
      "v_permlane16_swap_b32 v132, v134\n"
      "v_permlane16_swap_b32 v133, v135\n"
      "global_store_dwordx4 v156, v[132:135], s[16:17] offset:0 sc1\n"
      "v_pk_mul_f32 v[170:171], v[170:171], v[226:227] op_sel:[0,0] op_sel_hi:[1,0]\n"
      "v_pk_mul_f32 v[184:185], v[184:185], v[226:227] op_sel:[0,0] op_sel_hi:[1,0]\n"
      "v_pk_mul_f32 v[186:187], v[186:187], v[226:227] op_sel:[0,0] op_sel_hi:[1,0]\n"
      "v_cvt_pk_bf16_f32 v136, v168, v169\n"
      "v_cvt_pk_bf16_f32 v138, v184, v185\n"
      "v_cvt_pk_bf16_f32 v137, v170, v171\n"
      "v_cvt_pk_bf16_f32 v139, v186, v187\n"
      "v_pk_mul_f32 v[172:173], v[172:173], v[226:227] op_sel:[0,1] op_sel_hi:[1,1]\n"
      "v_permlane16_swap_b32 v136, v138\n"
      "v_permlane16_swap_b32 v137, v139\n"
      "global_store_dwordx4 v155, v[136:139], s[16:17] offset:-4096 sc1\n"
      "v_pk_mul_f32 v[174:175], v[174:175], v[226:227] op_sel:[0,1] op_sel_hi:[1,1]\n"
      "v_pk_mul_f32 v[188:189], v[188:189], v[226:227] op_sel:[0,1] op_sel_hi:[1,1]\n"
      "v_pk_mul_f32 v[190:191], v[190:191], v[226:227] op_sel:[0,1] op_sel_hi:[1,1]\n"
      "v_cvt_pk_bf16_f32 v140, v172, v173\n"
      "v_cvt_pk_bf16_f32 v142, v188, v189\n"
      "v_cvt_pk_bf16_f32 v141, v174, v175\n"
      "v_cvt_pk_bf16_f32 v143, v190, v191\n"
      "v_and_b32 v157, 0xffffff80, v157\n"
      "v_permlane16_swap_b32 v140, v142\n"
      "v_permlane16_swap_b32 v141, v143\n"
      "global_store_dwordx4 v155, v[140:143], s[16:17] offset:0 sc1\n"
      "s_and_b32 s67, %[fo], 8\n"
      "s_or_b32 s67, s67, %[xw]\n"
      "s_cbranch_scc1 END_%=\n"
      "s_mov_b64 exec, 1\n"
      "global_store_dwordx2 v157, v[150:151], s[18:19] sc1\n"
      "s_mov_b64 exec, -1\n"
      "END_%=:\n"
      ".subsection 1\n"
      "POLL_%=:\n"
      "s_waitcnt vmcnt(0) lgkmcnt(0)\n"
      "v_cmp_ne_u32 vcc, v234, v150\n"
      "v_mov_b32 v233, v235\n"
      "s_memrealtime s[68:69]\n"
      "s_sub_u32 s63, s63, 1\n"
      "s_cbranch_vccz GOT_%=\n"
      "global_load_dwordx2 v[234:235], v158, s[18:19] sc1\n"
      "s_cbranch_scc0 POLL_%=\n"
      "s_branch GOT_%=\n"
      "RETRY_%=:\n"
      "s_sub_u32 s63, s63, 1\n"
      "s_cbranch_scc1 OK_%=\n"
      "s_sleep 4\n"
      "buffer_load_dwordx4 v[80:83], v127, s[28:31], s79 offen sc1\n"
      "buffer_load_dwordx4 v[64:67], v127, s[28:31], s80 offen offset:0 sc1\n"
      "buffer_load_dwordx4 v[68:71], v127, s[28:31], s80 offen offset:1024 sc1\n"
      "buffer_load_dwordx4 v[72:75], v127, s[28:31], s80 offen offset:2048 sc1\n"
      "buffer_load_dwordx4 v[76:79], v127, s[28:31], s80 offen offset:3072 sc1\n"
      "s_branch CHK_%=\n"
      "GW0_%=:\n"
      "s_call_b64 s[84:85], WAIT_%=\n"
      "s_branch GX0_%=\n"
      "GW1_%=:\n"
      "s_call_b64 s[84:85], WAIT_%=\n"
      "s_branch GX1_%=\n"
      "GW2_%=:\n"
      "s_call_b64 s[84:85], WAIT_%=\n"
      "s_branch GX2_%=\n"
      "GW3_%=:\n"
      "s_call_b64 s[84:85], WAIT_%=\n"
      "s_add_u32 s87, s87, 1\n"
      "s_branch GX3_%=\n"
      "GW4_%=:\n"
      "s_call_b64 s[84:85], WAIT_%=\n"
      "s_branch GX4_%=\n"
      "GW5_%=:\n"
      "s_call_b64 s[84:85], WAIT_%=\n"
      "s_branch GX5_%=\n"
      "GW6_%=:\n"
      "s_call_b64 s[84:85], WAIT_%=\n"
      "s_branch GX6_%=\n"
      "GW7_%=:\n"
      "s_call_b64 s[84:85], WAIT_%=\n"
      "s_branch GX7_%=\n"
      "GW8_%=:\n"
      "s_call_b64 s[84:85], WAIT_%=\n"
      "s_branch GX8_%=\n"
      "WAIT_%=:\n"
      "s_add_u32 s87, s87, 1\n"
      "s_bitcmp1_b32 s89, s87\n"
      "s_cbranch_scc1 H1_%=\n"
      "SPIN_%=:\n"
      "s_sub_u32 s67, s68, s88\n"
      "s_cmp_ge_i32 s67, 0\n"
      "s_cbranch_scc1 GO_%=\n"
      "s_sleep 1\n"
      "s_memrealtime s[68:69]\n"
      "s_waitcnt lgkmcnt(0)\n"
      "s_branch SPIN_%=\n"
      "GO_%=:\n"
      "s_setpc_b64 s[84:85]\n"
      "H1_%=:\n"
      "s_cmp_gt_i32 s93, 50\n"
      "s_cbranch_scc1 HS_%=\n"
      "s_bitcmp1_b32 0x2a80, s87\n"
      "s_cbranch_scc0 H2_%=\n"
      "s_add_u32 s67, s66, 400\n"
      "s_waitcnt vmcnt(8)\n"
      "v_sub_u32 v250, s67, v250\n"
      "v_cmp_gt_u32 vcc, 900, v250\n"
      "v_add_u32 v250, 0xa00, v250\n"
      "v_xor_b32 v251, 0x100, v251\n"
      "s_sub_u32 s69, s66, s71\n"
      "s_cmp_ge_u32 s87, 12\n"
      "s_cselect_b32 s101, 50, s101\n"
      "s_bcnt1_i32_b32 s74, vcc_lo\n"
      "s_bcnt1_i32_b32 s75, vcc_hi\n"
      "v_cmp_gt_u32 vcc, 0x800, v250\n"
      "s_add_u32 s67, s74, s75\n"
      "s_cmp_eq_u32 s87, 7\n"
      "s_cselect_b32 s76, 1, 0\n"
      "s_lshr_b32 s76, s67, s76\n"
      "s_cmp_le_u32 s76, 2\n"
      "s_cselect_b32 s86, 107, s98\n"
      "s_cmp_ge_u32 s76, 9\n"
      "s_cselect_b32 s86, 112, s86\n"
      "s_cbranch_vccnz H6_%=\n"
      "s_cmp_ge_u32 s74, 12\n"
      "s_cbranch_scc1 H3_%=\n"
      "s_cmp_eq_u32 s87, 13\n"
      "s_cbranch_scc1 H4_%=\n"
      "H8_%=:\n"
      "s_lshr_b32 s67, %[rep], 7\n"
      "s_cmp_eq_u32 s67, 1\n"
      "s_cbranch_scc0 SPIN_%=\n"
      "H5_%=:\n"
      "s_cmp_gt_i32 s93, 50\n"
      "s_cselect_b32 s75, -10, 0\n"
      "v_mov_b32 v127, s66\n"
      "s_add_u32 s74, s87, 2\n"
      "s_bfe_u32 s74, s74, 0x10001\n"
      "s_lshl_b32 s74, s74, 8\n"
      "s_add_u32 s74, s74, %[rep]\n"
      "RS_%=:\n"
      "s_sub_u32 s67, s68, s88\n"
      "s_cmp_ge_i32 s67, s75\n"
      "s_cbranch_scc1 RG_%=\n"
      "s_sleep 1\n"
      "s_memrealtime s[68:69]\n"
      "s_waitcnt lgkmcnt(0)\n"
      "s_branch RS_%=\n"
      "RG_%=:\n"
      "s_getreg_b32 s67, hwreg(HW_REG_IB_STS, 0, 4)\n"
      "s_cmp_le_u32 s67, 4\n"
      "s_cbranch_scc1 SPIN_%=\n"
      "buffer_store_dword v127, v124, s[28:31], s74 offen sc1\n"
      "s_branch SPIN_%=\n"
      "H6_%=:\n"
      "s_ff1_i32_b64 s67, vcc\n"
      "v_sub_u32 v250, s66, v250\n"
      "s_nop 3\n"
      "v_readlane_b32 s76, v250, s67\n"
      "s_add_u32 s76, s76, 2960\n"
      "s_and_b32 s76, s76, 31\n"
      "s_lshl_b32 s76, s76, 3\n"
      "s_lshl_b32 s89, 4, s87\n"
      "s_branch H7_%=\n"
      "H3_%=:\n"
      "s_mul_i32 s76, s69, 110\n"
      "s_ashr_i32 s76, s76, 10\n"
      "s_min_i32 s76, s76, 40\n"
      "s_add_u32 s76, s76, 76\n"
      "s_cmp_lt_u32 s75, 24\n"
      "s_cselect_b32 s76, 56, s76\n"
      "s_cmp_lt_u32 s75, 13\n"
      "s_cselect_b32 s76, 40, s76\n"
      "s_cmp_lt_u32 s75, 5\n"
      "s_cselect_b32 s76, 24, s76\n"
      "s_add_u32 s67, s66, 2000\n"
      "s_and_b32 s67, s67, -32\n"
      "s_lshr_b32 s75, s76, 3\n"
      "s_or_b32 s67, s67, s75\n"
      "v_mov_b32 v127, s67\n"
      "s_add_u32 s74, s87, 2\n"
      "s_bfe_u32 s74, s74, 0x10001\n"
      "s_lshl_b32 s74, s74, 8\n"
      "s_add_u32 s74, s74, %[rep]\n"
      "s_cmp_lt_u32 %[rep], 128\n"
      "s_cselect_b32 s89, 22, 20\n"
      "s_lshl_b32 s89, s89, s87\n"
      "s_sub_u32 s75, %[rep], 128\n"
      "s_cmp_lt_u32 s75, 32\n"
      "s_cbranch_scc0 H7_%=\n"
      "buffer_store_dword v127, v124, s[28:31], s74 offen sc1\n"
      "H7_%=:\n"
      "s_add_u32 s66, s66, s76\n"
      "s_mov_b32 s99, 13\n"
      "s_mov_b32 s100, 286\n"
      "s_add_u32 s93, s93, 99\n"
      "s_mov_b32 s86, 110\n"
      "s_mov_b32 s80, 0xc853212b\n"
      "s_mov_b32 s81, 0x842210\n"
      "s_cmp_le_u32 s76, 40\n"
      "s_cselect_b32 s75, 15, 0\n"
      "s_cmp_le_u32 s76, 24\n"
      "s_cselect_b32 s75, 25, s75\n"
      "s_lshr_b64 s[80:81], s[80:81], s75\n"
      "s_sub_u32 s67, s65, s66\n"
      "s_cmp_lt_i32 s67, 0\n"
      "s_cselect_b32 s88, s66, s65\n"
      "s_mov_b32 s101, 200\n"
      "s_branch SPIN_%=\n"
      "H4_%=:\n"
      "s_mov_b32 s91, 0\n"
      "s_mov_b32 s89, 0\n"
      "v_mov_b32 v251, 0x4000000\n"
      "s_branch SPIN_%=\n"
      "HS_%=:\n"
      "s_bitcmp1_b32 s87, 0\n"
      "s_cbranch_scc0 H5_%=\n"
      "v_xor_b32 v251, 0x100, v251\n"
      "s_lshr_b32 s67, s89, s87\n"
      "s_cmp_gt_u32 s67, 1\n"
      "s_cbranch_scc1 H8_%=\n"
      "s_add_u32 s67, s66, 400\n"
      "s_waitcnt vmcnt(8)\n"
      "v_sub_u32 v250, s67, v250\n"
      "v_cmp_gt_u32 vcc, 900, v250\n"
      "s_nop 3\n"
      "s_bcnt1_i32_b32 s74, vcc_lo\n"
      "s_bcnt1_i32_b32 s75, vcc_hi\n"
      "s_cmp_ge_u32 s75, 5\n"
      "s_cbranch_scc1 HT_%=\n"
      "s_cmp_gt_u32 s74, 2\n"
      "s_cbranch_scc1 H4_%=\n"
      "s_mov_b32 s99, 21\n"
      "s_mov_b32 s100, 282\n"
      "s_mov_b32 s86, 107\n"
      "s_mov_b64 s[80:81], 0\n"
      "s_branch H4_%=\n"
      "HT_%=:\n"
      "s_add_u32 s66, s66, 64\n"
      "s_mov_b32 s80, 0x86429909\n"
      "s_mov_b32 s81, 0x42110\n"
      "s_sub_u32 s67, s65, s66\n"
      "s_cmp_lt_i32 s67, 0\n"
      "s_cselect_b32 s88, s66, s65\n"
      "s_branch H4_%=\n"
      "H0_%=:\n"
      "buffer_load_dword v250, v251, s[28:31], 0 offen sc1\n"
      "s_branch SPIN_%=\n"
      "H9_%=:\n"
      "s_bitcmp1_b32 s87, 0\n"
      "s_cbranch_scc0 H5_%=\n"
      "v_xor_b32 v251, 0x100, v251\n"
      "s_cmp_eq_u32 s87, 5\n"
      "s_cbranch_scc1 H8_%=\n"
      "s_branch SPIN_%=\n"
      "H2_%=:\n"
      "s_cmp_gt_u32 s87, 2\n"
      "s_cbranch_scc1 H9_%=\n"
      "s_cmp_eq_u32 s87, 1\n"
      "s_cbranch_scc1 H0_%=\n"
      "s_waitcnt vmcnt(8)\n"
      "v_subrev_u32 v250, s71, v250\n"
      "v_min_u32 v250, 0x258, v250\n"
      "v_mbcnt_lo_u32_b32 v251, -1, 0\n"
      "v_mbcnt_hi_u32_b32 v251, -1, v251\n"
      "v_add_u32_dpp v250, v250, v250 row_shr:1 row_mask:0xf bank_mask:0xf bound_ctrl:0\n"
      "v_lshlrev_b32 v124, 23, v251\n"
      "s_nop 0\n"
      "v_add_u32_dpp v250, v250, v250 row_shr:2 row_mask:0xf bank_mask:0xf bound_ctrl:0\n"
      "v_lshlrev_b32 v251, 2, v251\n"
      "v_add_u32 v124, 0x6000, v124\n"
      "v_add_u32_dpp v250, v250, v250 row_shr:4 row_mask:0xf bank_mask:0xf bound_ctrl:0\n"
      "v_add_u32 v251, 0x6100, v251\n"
      "v_readlane_b32 s66, v250, 7\n"
      "s_nop 0\n"
      "s_lshr_b32 s66, s66, 3\n"
      "s_add_u32 s66, s66, s71\n"
      "s_add_u32 s66, s66, %[cph]\n"
      "s_add_u32 s71, s66, 550\n"
      "s_sub_u32 s67, s65, s66\n"
      "s_cmp_lt_i32 s67, 0\n"
      "s_cselect_b32 s88, s66, s65\n"
      "s_branch SPIN_%=\n"
      "FB0_%=:\n"
      "s_nop 7\n"
      "s_nop 7\n"
      "v_xor_b32 v118, 0x0, v242\n"
      "v_xor_b32 v119, 0x0, v243\n"
      "ds_read_b128 a[64:67], v118 offset:0\n"
      "ds_read_b128 a[68:71], v118 offset:4096\n"
      "ds_read_b128 a[72:75], v119 offset:0\n"
      "ds_read_b128 a[76:79], v119 offset:4096\n"
      "ds_read_b128 a[80:83], v118 offset:1024\n"
      "ds_read_b128 a[84:87], v118 offset:5120\n"
      "ds_read_b128 a[88:91], v119 offset:1024\n"
      "ds_read_b128 a[92:95], v119 offset:5120\n"
      "s_waitcnt lgkmcnt(0)\n"
      "v_mfma_f32_16x16x32_bf16 v[128:131], a[64:67], a[0:3], 0\n"
      "v_mfma_f32_16x16x32_bf16 v[136:139], a[64:67], a[4:7], 0\n"
      "v_mfma_f32_16x16x32_bf16 v[144:147], a[64:67], a[8:11], 0\n"
      "v_mfma_f32_16x16x32_bf16 v[152:155], a[64:67], a[12:15], 0\n"
      "v_mfma_f32_16x16x32_bf16 v[132:135], a[68:71], a[0:3], 0\n"
      "v_mfma_f32_16x16x32_bf16 v[140:143], a[68:71], a[4:7], 0\n"
      "v_mfma_f32_16x16x32_bf16 v[148:151], a[68:71], a[8:11], 0\n"
      "v_mfma_f32_16x16x32_bf16 v[156:159], a[68:71], a[12:15], 0\n"
      "v_mfma_f32_16x16x32_bf16 v[128:131], a[72:75], a[16:19], v[128:131]\n"
      "v_mfma_f32_16x16x32_bf16 v[136:139], a[72:75], a[20:23], v[136:139]\n"
      "v_mfma_f32_16x16x32_bf16 v[144:147], a[72:75], a[24:27], v[144:147]\n"
      "v_mfma_f32_16x16x32_bf16 v[152:155], a[72:75], a[28:31], v[152:155]\n"
      "v_mfma_f32_16x16x32_bf16 v[132:135], a[76:79], a[16:19], v[132:135]\n"
      "v_mfma_f32_16x16x32_bf16 v[140:143], a[76:79], a[20:23], v[140:143]\n"
      "v_mfma_f32_16x16x32_bf16 v[148:151], a[76:79], a[24:27], v[148:151]\n"
      "v_mfma_f32_16x16x32_bf16 v[156:159], a[76:79], a[28:31], v[156:159]\n"
      "v_mfma_f32_16x16x32_bf16 v[128:131], a[80:83], a[32:35], v[128:131]\n"
      "v_mfma_f32_16x16x32_bf16 v[136:139], a[80:83], a[36:39], v[136:139]\n"
      "v_mfma_f32_16x16x32_bf16 v[144:147], a[80:83], a[40:43], v[144:147]\n"
      "v_mfma_f32_16x16x32_bf16 v[152:155], a[80:83], a[44:47], v[152:155]\n"
      "v_mfma_f32_16x16x32_bf16 v[132:135], a[84:87], a[32:35], v[132:135]\n"
      "v_mfma_f32_16x16x32_bf16 v[140:143], a[84:87], a[36:39], v[140:143]\n"
      "v_mfma_f32_16x16x32_bf16 v[148:151], a[84:87], a[40:43], v[148:151]\n"
      "v_mfma_f32_16x16x32_bf16 v[156:159], a[84:87], a[44:47], v[156:159]\n"
      "v_mfma_f32_16x16x32_bf16 v[128:131], a[88:91], a[48:51], v[128:131]\n"
      "v_mfma_f32_16x16x32_bf16 v[136:139], a[88:91], a[52:55], v[136:139]\n"
      "v_mfma_f32_16x16x32_bf16 v[144:147], a[88:91], a[56:59], v[144:147]\n"
      "v_mfma_f32_16x16x32_bf16 v[152:155], a[88:91], a[60:63], v[152:155]\n"
      "v_mfma_f32_16x16x32_bf16 v[132:135], a[92:95], a[48:51], v[132:135]\n"
      "v_mfma_f32_16x16x32_bf16 v[140:143], a[92:95], a[52:55], v[140:143]\n"
      "v_mfma_f32_16x16x32_bf16 v[148:151], a[92:95], a[56:59], v[148:151]\n"
      "v_mfma_f32_16x16x32_bf16 v[156:159], a[92:95], a[60:63], v[156:159]\n"
      "s_nop 7\n"
      "v_max3_f32 v112, v128, v129, v130\n"
      "v_max3_f32 v113, v136, v137, v138\n"
      "v_max3_f32 v114, v144, v145, v146\n"
      "v_max3_f32 v115, v152, v153, v154\n"
      "v_max3_f32 v112, v112, v131, v132\n"
      "v_max3_f32 v113, v113, v139, v140\n"
      "v_max3_f32 v114, v114, v147, v148\n"
      "v_max3_f32 v115, v115, v155, v156\n"
      "v_max3_f32 v112, v112, v133, v134\n"
      "v_max3_f32 v113, v113, v141, v142\n"
      "v_max3_f32 v114, v114, v149, v150\n"
      "v_max3_f32 v115, v115, v157, v158\n"
      "v_max_f32 v112, v112, v135\n"
      "v_max_f32 v113, v113, v143\n"
      "v_max_f32 v114, v114, v151\n"
      "v_max_f32 v115, v115, v159\n"
      "v_mov_b32 v160, v112\n"
      "v_mov_b32 v161, v113\n"
      "v_mov_b32 v162, v114\n"
      "v_mov_b32 v163, v115\n"
      "v_permlane16_swap_b32 v112, v160\n"
      "v_permlane16_swap_b32 v113, v161\n"
      "v_permlane16_swap_b32 v114, v162\n"
      "v_permlane16_swap_b32 v115, v163\n"
      "v_max_f32 v112, v112, v160\n"
      "v_max_f32 v113, v113, v161\n"
      "v_max_f32 v114, v114, v162\n"
      "v_max_f32 v115, v115, v163\n"
      "v_mov_b32 v160, v112\n"
      "v_mov_b32 v161, v113\n"
      "v_mov_b32 v162, v114\n"
      "v_mov_b32 v163, v115\n"
      "v_permlane32_swap_b32 v112, v160\n"
      "v_permlane32_swap_b32 v113, v161\n"
      "v_permlane32_swap_b32 v114, v162\n"
      "v_permlane32_swap_b32 v115, v163\n"
      "v_max_f32 v112, v112, v160\n"
      "v_max_f32 v113, v113, v161\n"
      "v_max_f32 v114, v114, v162\n"
      "v_max_f32 v115, v115, v163\n"
      "v_sub_f32 v112, 0xc1800000, v112\n"
      "v_sub_f32 v113, 0xc1800000, v113\n"
      "v_sub_f32 v114, 0xc1800000, v114\n"
      "v_sub_f32 v115, 0xc1800000, v115\n"
      "v_floor_f32 v112, v112\n"
      "v_floor_f32 v113, v113\n"
      "v_floor_f32 v114, v114\n"
      "v_floor_f32 v115, v115\n"
      "v_min_f32 v112, v112, v224\n"
      "v_min_f32 v113, v113, v228\n"
      "v_min_f32 v114, v114, v232\n"
      "v_min_f32 v115, v115, v236\n"
      "v_sub_f32 v164, v112, v224\n"
      "v_sub_f32 v165, v113, v228\n"
      "v_sub_f32 v166, v114, v232\n"
      "v_sub_f32 v167, v115, v236\n"
      "v_mov_b32 v224, v112\n"
      "v_mov_b32 v228, v113\n"
      "v_mov_b32 v232, v114\n"
      "v_mov_b32 v236, v115\n"
      "v_exp_f32 v112, v164\n"
      "v_exp_f32 v113, v165\n"
      "v_exp_f32 v114, v166\n"
      "v_exp_f32 v115, v167\n"
      "v_mov_b32 v225, v224\n"
      "v_mov_b32 v229, v228\n"
      "v_mov_b32 v233, v232\n"
      "v_mov_b32 v237, v236\n"
      "v_mov_b64 v[226:227], v[224:225]\n"
      "v_mov_b64 v[230:231], v[228:229]\n"
      "v_mov_b64 v[234:235], v[232:233]\n"
      "v_mov_b64 v[238:239], v[236:237]\n"
      "s_call_b64 s[84:85], SCALE_%=\n"
      "v_pk_add_f32 v[128:129], v[128:129], v[224:225]\n"
      "v_pk_add_f32 v[130:131], v[130:131], v[224:225]\n"
      "v_pk_add_f32 v[132:133], v[132:133], v[224:225]\n"
      "v_pk_add_f32 v[134:135], v[134:135], v[224:225]\n"
      "v_pk_add_f32 v[136:137], v[136:137], v[228:229]\n"
      "v_pk_add_f32 v[138:139], v[138:139], v[228:229]\n"
      "v_pk_add_f32 v[140:141], v[140:141], v[228:229]\n"
      "v_pk_add_f32 v[142:143], v[142:143], v[228:229]\n"
      "v_pk_add_f32 v[144:145], v[144:145], v[232:233]\n"
      "v_pk_add_f32 v[146:147], v[146:147], v[232:233]\n"
      "v_pk_add_f32 v[148:149], v[148:149], v[232:233]\n"
      "v_pk_add_f32 v[150:151], v[150:151], v[232:233]\n"
      "v_pk_add_f32 v[152:153], v[152:153], v[236:237]\n"
      "v_pk_add_f32 v[154:155], v[154:155], v[236:237]\n"
      "v_pk_add_f32 v[156:157], v[156:157], v[236:237]\n"
      "v_pk_add_f32 v[158:159], v[158:159], v[236:237]\n"
      "v_exp_f32 v112, v128\n"
      "v_exp_f32 v113, v129\n"
      "v_exp_f32 v114, v130\n"
      "v_cvt_pk_bf16_f32 v128, v112, v113\n"
      "v_exp_f32 v115, v131\n"
      "v_exp_f32 v112, v132\n"
      "v_cvt_pk_bf16_f32 v129, v114, v115\n"
      "v_exp_f32 v113, v133\n"
      "v_exp_f32 v114, v134\n"
      "v_cvt_pk_bf16_f32 v130, v112, v113\n"
      "v_exp_f32 v115, v135\n"
      "v_exp_f32 v112, v136\n"
      "v_cvt_pk_bf16_f32 v131, v114, v115\n"
      "v_exp_f32 v113, v137\n"
      "v_exp_f32 v114, v138\n"
      "v_cvt_pk_bf16_f32 v132, v112, v113\n"
      "v_exp_f32 v115, v139\n"
      "v_exp_f32 v112, v140\n"
      "v_cvt_pk_bf16_f32 v133, v114, v115\n"
      "v_exp_f32 v113, v141\n"
      "v_exp_f32 v114, v142\n"
      "v_cvt_pk_bf16_f32 v134, v112, v113\n"
      "v_exp_f32 v115, v143\n"
      "v_exp_f32 v112, v144\n"
      "v_cvt_pk_bf16_f32 v135, v114, v115\n"
      "v_exp_f32 v113, v145\n"
      "v_exp_f32 v114, v146\n"
      "v_cvt_pk_bf16_f32 v136, v112, v113\n"
      "v_exp_f32 v115, v147\n"
      "v_exp_f32 v112, v148\n"
      "v_cvt_pk_bf16_f32 v137, v114, v115\n"
      "v_exp_f32 v113, v149\n"
      "v_exp_f32 v114, v150\n"
      "v_cvt_pk_bf16_f32 v138, v112, v113\n"
      "v_exp_f32 v115, v151\n"
      "v_exp_f32 v112, v152\n"
      "v_cvt_pk_bf16_f32 v139, v114, v115\n"
      "v_exp_f32 v113, v153\n"
      "v_exp_f32 v114, v154\n"
      "v_cvt_pk_bf16_f32 v140, v112, v113\n"
      "v_exp_f32 v115, v155\n"
      "v_exp_f32 v112, v156\n"
      "v_cvt_pk_bf16_f32 v141, v114, v115\n"
      "v_exp_f32 v113, v157\n"
      "v_exp_f32 v114, v158\n"
      "v_cvt_pk_bf16_f32 v142, v112, v113\n"
      "v_exp_f32 v115, v159\n"
      "s_nop 0\n"
      "v_cvt_pk_bf16_f32 v143, v114, v115\n"
      "ds_read_b128 a[64:67], v242 offset:32768\n"
      "ds_read_b128 a[68:71], v242 offset:36864\n"
      "ds_read_b128 a[72:75], v243 offset:32768\n"
      "ds_read_b128 a[76:79], v243 offset:36864\n"
      "ds_read_b128 a[80:83], v242 offset:33792\n"
      "ds_read_b128 a[84:87], v242 offset:37888\n"
      "ds_read_b128 a[88:91], v243 offset:33792\n"
      "ds_read_b128 a[92:95], v243 offset:37888\n"
      "s_waitcnt lgkmcnt(0)\n"
      "v_mfma_f32_16x16x32_bf16 v[160:163], a[64:67], a[0:3], v[224:227]\n"
      "v_mfma_f32_16x16x32_bf16 v[168:171], a[64:67], a[4:7], v[228:231]\n"
      "v_mfma_f32_16x16x32_bf16 v[176:179], a[64:67], a[8:11], v[232:235]\n"
      "v_mfma_f32_16x16x32_bf16 v[184:187], a[64:67], a[12:15], v[236:239]\n"
      "v_mfma_f32_16x16x32_bf16 v[164:167], a[68:71], a[0:3], v[224:227]\n"
      "v_mfma_f32_16x16x32_bf16 v[172:175], a[68:71], a[4:7], v[228:231]\n"
      "v_mfma_f32_16x16x32_bf16 v[180:183], a[68:71], a[8:11], v[232:235]\n"
      "v_mfma_f32_16x16x32_bf16 v[188:191], a[68:71], a[12:15], v[236:239]\n"
      "v_mfma_f32_16x16x32_bf16 v[160:163], a[72:75], a[16:19], v[160:163]\n"
      "v_mfma_f32_16x16x32_bf16 v[168:171], a[72:75], a[20:23], v[168:171]\n"
      "v_mfma_f32_16x16x32_bf16 v[176:179], a[72:75], a[24:27], v[176:179]\n"
      "v_mfma_f32_16x16x32_bf16 v[184:187], a[72:75], a[28:31], v[184:187]\n"
      "v_mfma_f32_16x16x32_bf16 v[164:167], a[76:79], a[16:19], v[164:167]\n"
      "v_mfma_f32_16x16x32_bf16 v[172:175], a[76:79], a[20:23], v[172:175]\n"
      "v_mfma_f32_16x16x32_bf16 v[180:183], a[76:79], a[24:27], v[180:183]\n"
      "v_mfma_f32_16x16x32_bf16 v[188:191], a[76:79], a[28:31], v[188:191]\n"
      "v_mfma_f32_16x16x32_bf16 v[160:163], a[80:83], a[32:35], v[160:163]\n"
      "v_mfma_f32_16x16x32_bf16 v[168:171], a[80:83], a[36:39], v[168:171]\n"
      "v_mfma_f32_16x16x32_bf16 v[176:179], a[80:83], a[40:43], v[176:179]\n"
      "v_mfma_f32_16x16x32_bf16 v[184:187], a[80:83], a[44:47], v[184:187]\n"
      "v_mfma_f32_16x16x32_bf16 v[164:167], a[84:87], a[32:35], v[164:167]\n"
      "v_mfma_f32_16x16x32_bf16 v[172:175], a[84:87], a[36:39], v[172:175]\n"
      "v_mfma_f32_16x16x32_bf16 v[180:183], a[84:87], a[40:43], v[180:183]\n"
      "v_mfma_f32_16x16x32_bf16 v[188:191], a[84:87], a[44:47], v[188:191]\n"
      "v_mfma_f32_16x16x32_bf16 v[160:163], a[88:91], a[48:51], v[160:163]\n"
      "v_mfma_f32_16x16x32_bf16 v[168:171], a[88:91], a[52:55], v[168:171]\n"
      "v_mfma_f32_16x16x32_bf16 v[176:179], a[88:91], a[56:59], v[176:179]\n"
      "v_mfma_f32_16x16x32_bf16 v[184:187], a[88:91], a[60:63], v[184:187]\n"
      "v_mfma_f32_16x16x32_bf16 v[164:167], a[92:95], a[48:51], v[164:167]\n"
      "v_mfma_f32_16x16x32_bf16 v[172:175], a[92:95], a[52:55], v[172:175]\n"
      "v_mfma_f32_16x16x32_bf16 v[180:183], a[92:95], a[56:59], v[180:183]\n"
      "v_mfma_f32_16x16x32_bf16 v[188:191], a[92:95], a[60:63], v[188:191]\n"
      "s_cmp_eq_u32 s64, 0\n"
      "s_cbranch_scc0 NRET0_0_%=\n"
      "v_mov_b64 v[192:193], v[128:129]\n"
      "v_mov_b64 v[194:195], v[130:131]\n"
      "v_mov_b64 v[196:197], v[132:133]\n"
      "v_mov_b64 v[198:199], v[134:135]\n"
      "v_mov_b64 v[200:201], v[136:137]\n"
      "v_mov_b64 v[202:203], v[138:139]\n"
      "v_mov_b64 v[204:205], v[140:141]\n"
      "v_mov_b64 v[206:207], v[142:143]\n"
      "s_nop 1\n"
      "s_branch RET0_0_%=\n"
      "NRET0_0_%=:\n"
      "s_cmp_eq_u32 s64, 1\n"
      "s_cbranch_scc0 NRET0_1_%=\n"
      "v_mov_b64 v[192:193], v[128:129]\n"
      "v_mov_b64 v[194:195], v[130:131]\n"
      "v_mov_b64 v[196:197], v[132:133]\n"
      "v_mov_b64 v[198:199], v[134:135]\n"
      "v_mov_b64 v[200:201], v[136:137]\n"
      "v_mov_b64 v[202:203], v[138:139]\n"
      "v_mov_b64 v[204:205], v[140:141]\n"
      "v_mov_b64 v[206:207], v[142:143]\n"
      "s_nop 1\n"
      "s_branch RET0_1_%=\n"
      "NRET0_1_%=:\n"
      "s_cmp_eq_u32 s64, 2\n"
      "s_cbranch_scc0 NRET0_2_%=\n"
      "v_mov_b64 v[192:193], v[128:129]\n"
      "v_mov_b64 v[194:195], v[130:131]\n"
      "v_mov_b64 v[196:197], v[132:133]\n"
      "v_mov_b64 v[198:199], v[134:135]\n"
      "v_mov_b64 v[200:201], v[136:137]\n"
      "v_mov_b64 v[202:203], v[138:139]\n"
      "v_mov_b64 v[204:205], v[140:141]\n"
      "v_mov_b64 v[206:207], v[142:143]\n"
      "s_nop 1\n"
      "s_branch RET0_2_%=\n"
      "NRET0_2_%=:\n"
      "s_cmp_eq_u32 s64, 3\n"
      "s_cbranch_scc0 NRET0_3_%=\n"
      "v_mov_b64 v[192:193], v[128:129]\n"
      "v_mov_b64 v[194:195], v[130:131]\n"
      "v_mov_b64 v[196:197], v[132:133]\n"
      "v_mov_b64 v[198:199], v[134:135]\n"
      "v_mov_b64 v[200:201], v[136:137]\n"
      "v_mov_b64 v[202:203], v[138:139]\n"
      "v_mov_b64 v[204:205], v[140:141]\n"
      "v_mov_b64 v[206:207], v[142:143]\n"
      "s_nop 1\n"
      "s_branch RET0_3_%=\n"
      "NRET0_3_%=:\n"
      "s_cmp_eq_u32 s64, 4\n"
      "s_cbranch_scc0 NRET0_4_%=\n"
      "v_mov_b64 v[192:193], v[128:129]\n"
      "v_mov_b64 v[194:195], v[130:131]\n"
      "v_mov_b64 v[196:197], v[132:133]\n"
      "v_mov_b64 v[198:199], v[134:135]\n"
      "v_mov_b64 v[200:201], v[136:137]\n"
      "v_mov_b64 v[202:203], v[138:139]\n"
      "v_mov_b64 v[204:205], v[140:141]\n"
      "v_mov_b64 v[206:207], v[142:143]\n"
      "s_nop 1\n"
      "s_branch RET0_4_%=\n"
      "NRET0_4_%=:\n"
      "s_cmp_eq_u32 s64, 5\n"
      "s_cbranch_scc0 NRET0_5_%=\n"
      "v_mov_b64 v[16:17], v[128:129]\n"
      "v_mov_b64 v[18:19], v[130:131]\n"
      "v_mov_b64 v[20:21], v[132:133]\n"
      "v_mov_b64 v[22:23], v[134:135]\n"
      "v_mov_b64 v[24:25], v[136:137]\n"
      "v_mov_b64 v[26:27], v[138:139]\n"
      "v_mov_b64 v[28:29], v[140:141]\n"
      "v_mov_b64 v[30:31], v[142:143]\n"
      "s_nop 1\n"
      "s_branch RET0_5_%=\n"
      "NRET0_5_%=:\n"
      "FB1_%=:\n"
      "s_add_u32 s84, s63, 1\n"
      "s_sub_u32 s84, s84, %[n1]\n"
      "s_or_b32 s84, s84, s64\n"
      "s_cbranch_scc0 RET1_0_%=\n"
      "s_nop 7\n"
      "s_nop 7\n"
      "v_xor_b32 v118, 0x10000, v242\n"
      "v_xor_b32 v119, 0x10000, v243\n"
      "ds_read_b128 a[64:67], v118 offset:32768\n"
      "ds_read_b128 a[68:71], v118 offset:36864\n"
      "ds_read_b128 a[72:75], v119 offset:32768\n"
      "ds_read_b128 a[76:79], v119 offset:36864\n"
      "ds_read_b128 a[80:83], v118 offset:33792\n"
      "ds_read_b128 a[84:87], v118 offset:37888\n"
      "ds_read_b128 a[88:91], v119 offset:33792\n"
      "ds_read_b128 a[92:95], v119 offset:37888\n"
      "s_waitcnt lgkmcnt(0)\n"
      "v_mfma_f32_16x16x32_bf16 v[160:163], a[64:67], a[0:3], 0\n"
      "v_mfma_f32_16x16x32_bf16 v[168:171], a[64:67], a[4:7], 0\n"
      "v_mfma_f32_16x16x32_bf16 v[176:179], a[64:67], a[8:11], 0\n"
      "v_mfma_f32_16x16x32_bf16 v[184:187], a[64:67], a[12:15], 0\n"
      "v_mfma_f32_16x16x32_bf16 v[164:167], a[68:71], a[0:3], 0\n"
      "v_mfma_f32_16x16x32_bf16 v[172:175], a[68:71], a[4:7], 0\n"
      "v_mfma_f32_16x16x32_bf16 v[180:183], a[68:71], a[8:11], 0\n"
      "v_mfma_f32_16x16x32_bf16 v[188:191], a[68:71], a[12:15], 0\n"
      "v_mfma_f32_16x16x32_bf16 v[160:163], a[72:75], a[16:19], v[160:163]\n"
      "v_mfma_f32_16x16x32_bf16 v[168:171], a[72:75], a[20:23], v[168:171]\n"
      "v_mfma_f32_16x16x32_bf16 v[176:179], a[72:75], a[24:27], v[176:179]\n"
      "v_mfma_f32_16x16x32_bf16 v[184:187], a[72:75], a[28:31], v[184:187]\n"
      "v_mfma_f32_16x16x32_bf16 v[164:167], a[76:79], a[16:19], v[164:167]\n"
      "v_mfma_f32_16x16x32_bf16 v[172:175], a[76:79], a[20:23], v[172:175]\n"
      "v_mfma_f32_16x16x32_bf16 v[180:183], a[76:79], a[24:27], v[180:183]\n"
      "v_mfma_f32_16x16x32_bf16 v[188:191], a[76:79], a[28:31], v[188:191]\n"
      "v_mfma_f32_16x16x32_bf16 v[160:163], a[80:83], a[32:35], v[160:163]\n"
      "v_mfma_f32_16x16x32_bf16 v[168:171], a[80:83], a[36:39], v[168:171]\n"
      "v_mfma_f32_16x16x32_bf16 v[176:179], a[80:83], a[40:43], v[176:179]\n"
      "v_mfma_f32_16x16x32_bf16 v[184:187], a[80:83], a[44:47], v[184:187]\n"
      "v_mfma_f32_16x16x32_bf16 v[164:167], a[84:87], a[32:35], v[164:167]\n"
      "v_mfma_f32_16x16x32_bf16 v[172:175], a[84:87], a[36:39], v[172:175]\n"
      "v_mfma_f32_16x16x32_bf16 v[180:183], a[84:87], a[40:43], v[180:183]\n"
      "v_mfma_f32_16x16x32_bf16 v[188:191], a[84:87], a[44:47], v[188:191]\n"
      "v_mfma_f32_16x16x32_bf16 v[160:163], a[88:91], a[48:51], v[160:163]\n"
      "v_mfma_f32_16x16x32_bf16 v[168:171], a[88:91], a[52:55], v[168:171]\n"
      "v_mfma_f32_16x16x32_bf16 v[176:179], a[88:91], a[56:59], v[176:179]\n"
      "v_mfma_f32_16x16x32_bf16 v[184:187], a[88:91], a[60:63], v[184:187]\n"
      "v_mfma_f32_16x16x32_bf16 v[164:167], a[92:95], a[48:51], v[164:167]\n"
      "v_mfma_f32_16x16x32_bf16 v[172:175], a[92:95], a[52:55], v[172:175]\n"
      "v_mfma_f32_16x16x32_bf16 v[180:183], a[92:95], a[56:59], v[180:183]\n"
      "v_mfma_f32_16x16x32_bf16 v[188:191], a[92:95], a[60:63], v[188:191]\n"
      "s_nop 7\n"
      "v_max3_f32 v112, v160, v161, v162\n"
      "v_max3_f32 v113, v168, v169, v170\n"
      "v_max3_f32 v114, v176, v177, v178\n"
      "v_max3_f32 v115, v184, v185, v186\n"
      "v_max3_f32 v112, v112, v163, v164\n"
      "v_max3_f32 v113, v113, v171, v172\n"
      "v_max3_f32 v114, v114, v179, v180\n"
      "v_max3_f32 v115, v115, v187, v188\n"
      "v_max3_f32 v112, v112, v165, v166\n"
      "v_max3_f32 v113, v113, v173, v174\n"
      "v_max3_f32 v114, v114, v181, v182\n"
      "v_max3_f32 v115, v115, v189, v190\n"
      "v_max_f32 v112, v112, v167\n"
      "v_max_f32 v113, v113, v175\n"
      "v_max_f32 v114, v114, v183\n"
      "v_max_f32 v115, v115, v191\n"
      "v_mov_b32 v128, v112\n"
      "v_mov_b32 v129, v113\n"
      "v_mov_b32 v130, v114\n"
      "v_mov_b32 v131, v115\n"
      "v_permlane16_swap_b32 v112, v128\n"
      "v_permlane16_swap_b32 v113, v129\n"
      "v_permlane16_swap_b32 v114, v130\n"
      "v_permlane16_swap_b32 v115, v131\n"
      "v_max_f32 v112, v112, v128\n"
      "v_max_f32 v113, v113, v129\n"
      "v_max_f32 v114, v114, v130\n"
      "v_max_f32 v115, v115, v131\n"
      "v_mov_b32 v128, v112\n"
      "v_mov_b32 v129, v113\n"
      "v_mov_b32 v130, v114\n"
      "v_mov_b32 v131, v115\n"
      "v_permlane32_swap_b32 v112, v128\n"
      "v_permlane32_swap_b32 v113, v129\n"
      "v_permlane32_swap_b32 v114, v130\n"
      "v_permlane32_swap_b32 v115, v131\n"
      "v_max_f32 v112, v112, v128\n"
      "v_max_f32 v113, v113, v129\n"
      "v_max_f32 v114, v114, v130\n"
      "v_max_f32 v115, v115, v131\n"
      "v_sub_f32 v112, 0xc1800000, v112\n"
      "v_sub_f32 v113, 0xc1800000, v113\n"
      "v_sub_f32 v114, 0xc1800000, v114\n"
      "v_sub_f32 v115, 0xc1800000, v115\n"
      "v_floor_f32 v112, v112\n"
      "v_floor_f32 v113, v113\n"
      "v_floor_f32 v114, v114\n"
      "v_floor_f32 v115, v115\n"
      "v_min_f32 v112, v112, v224\n"
      "v_min_f32 v113, v113, v228\n"
      "v_min_f32 v114, v114, v232\n"
      "v_min_f32 v115, v115, v236\n"
      "v_sub_f32 v132, v112, v224\n"
      "v_sub_f32 v133, v113, v228\n"
      "v_sub_f32 v134, v114, v232\n"
      "v_sub_f32 v135, v115, v236\n"
      "v_mov_b32 v224, v112\n"
      "v_mov_b32 v228, v113\n"
      "v_mov_b32 v232, v114\n"
      "v_mov_b32 v236, v115\n"
      "v_exp_f32 v112, v132\n"
      "v_exp_f32 v113, v133\n"
      "v_exp_f32 v114, v134\n"
      "v_exp_f32 v115, v135\n"
      "v_mov_b32 v225, v224\n"
      "v_mov_b32 v229, v228\n"
      "v_mov_b32 v233, v232\n"
      "v_mov_b32 v237, v236\n"
      "v_mov_b64 v[226:227], v[224:225]\n"
      "v_mov_b64 v[230:231], v[228:229]\n"
      "v_mov_b64 v[234:235], v[232:233]\n"
      "v_mov_b64 v[238:239], v[236:237]\n"
      "s_call_b64 s[84:85], SCALE_%=\n"
      "v_pk_add_f32 v[160:161], v[160:161], v[224:225]\n"
      "v_pk_add_f32 v[162:163], v[162:163], v[224:225]\n"
      "v_pk_add_f32 v[164:165], v[164:165], v[224:225]\n"
      "v_pk_add_f32 v[166:167], v[166:167], v[224:225]\n"
      "v_pk_add_f32 v[168:169], v[168:169], v[228:229]\n"
      "v_pk_add_f32 v[170:171], v[170:171], v[228:229]\n"
      "v_pk_add_f32 v[172:173], v[172:173], v[228:229]\n"
      "v_pk_add_f32 v[174:175], v[174:175], v[228:229]\n"
      "v_pk_add_f32 v[176:177], v[176:177], v[232:233]\n"
      "v_pk_add_f32 v[178:179], v[178:179], v[232:233]\n"
      "v_pk_add_f32 v[180:181], v[180:181], v[232:233]\n"
      "v_pk_add_f32 v[182:183], v[182:183], v[232:233]\n"
      "v_pk_add_f32 v[184:185], v[184:185], v[236:237]\n"
      "v_pk_add_f32 v[186:187], v[186:187], v[236:237]\n"
      "v_pk_add_f32 v[188:189], v[188:189], v[236:237]\n"
      "v_pk_add_f32 v[190:191], v[190:191], v[236:237]\n"
      "v_exp_f32 v112, v160\n"
      "v_exp_f32 v113, v161\n"
      "v_exp_f32 v114, v162\n"
      "v_cvt_pk_bf16_f32 v160, v112, v113\n"
      "v_exp_f32 v115, v163\n"
      "v_exp_f32 v112, v164\n"
      "v_cvt_pk_bf16_f32 v161, v114, v115\n"
      "v_exp_f32 v113, v165\n"
      "v_exp_f32 v114, v166\n"
      "v_cvt_pk_bf16_f32 v162, v112, v113\n"
      "v_exp_f32 v115, v167\n"
      "v_exp_f32 v112, v168\n"
      "v_cvt_pk_bf16_f32 v163, v114, v115\n"
      "v_exp_f32 v113, v169\n"
      "v_exp_f32 v114, v170\n"
      "v_cvt_pk_bf16_f32 v164, v112, v113\n"
      "v_exp_f32 v115, v171\n"
      "v_exp_f32 v112, v172\n"
      "v_cvt_pk_bf16_f32 v165, v114, v115\n"
      "v_exp_f32 v113, v173\n"
      "v_exp_f32 v114, v174\n"
      "v_cvt_pk_bf16_f32 v166, v112, v113\n"
      "v_exp_f32 v115, v175\n"
      "v_exp_f32 v112, v176\n"
      "v_cvt_pk_bf16_f32 v167, v114, v115\n"
      "v_exp_f32 v113, v177\n"
      "v_exp_f32 v114, v178\n"
      "v_cvt_pk_bf16_f32 v168, v112, v113\n"
      "v_exp_f32 v115, v179\n"
      "v_exp_f32 v112, v180\n"
      "v_cvt_pk_bf16_f32 v169, v114, v115\n"
      "v_exp_f32 v113, v181\n"
      "v_exp_f32 v114, v182\n"
      "v_cvt_pk_bf16_f32 v170, v112, v113\n"
      "v_exp_f32 v115, v183\n"
      "v_exp_f32 v112, v184\n"
      "v_cvt_pk_bf16_f32 v171, v114, v115\n"
      "v_exp_f32 v113, v185\n"
      "v_exp_f32 v114, v186\n"
      "v_cvt_pk_bf16_f32 v172, v112, v113\n"
      "v_exp_f32 v115, v187\n"
      "v_exp_f32 v112, v188\n"
      "v_cvt_pk_bf16_f32 v173, v114, v115\n"
      "v_exp_f32 v113, v189\n"
      "v_exp_f32 v114, v190\n"
      "v_cvt_pk_bf16_f32 v174, v112, v113\n"
      "v_exp_f32 v115, v191\n"
      "s_nop 0\n"
      "v_cvt_pk_bf16_f32 v175, v114, v115\n"
      "ds_read_b128 a[64:67], v242 offset:0\n"
      "ds_read_b128 a[68:71], v242 offset:4096\n"
      "ds_read_b128 a[72:75], v243 offset:0\n"
      "ds_read_b128 a[76:79], v243 offset:4096\n"
      "ds_read_b128 a[80:83], v242 offset:1024\n"
      "ds_read_b128 a[84:87], v242 offset:5120\n"
      "ds_read_b128 a[88:91], v243 offset:1024\n"
      "ds_read_b128 a[92:95], v243 offset:5120\n"
      "s_waitcnt lgkmcnt(0)\n"
      "v_mfma_f32_16x16x32_bf16 v[128:131], a[64:67], a[0:3], v[224:227]\n"
      "v_mfma_f32_16x16x32_bf16 v[136:139], a[64:67], a[4:7], v[228:231]\n"
      "v_mfma_f32_16x16x32_bf16 v[144:147], a[64:67], a[8:11], v[232:235]\n"
      "v_mfma_f32_16x16x32_bf16 v[152:155], a[64:67], a[12:15], v[236:239]\n"
      "v_mfma_f32_16x16x32_bf16 v[132:135], a[68:71], a[0:3], v[224:227]\n"
      "v_mfma_f32_16x16x32_bf16 v[140:143], a[68:71], a[4:7], v[228:231]\n"
      "v_mfma_f32_16x16x32_bf16 v[148:151], a[68:71], a[8:11], v[232:235]\n"
      "v_mfma_f32_16x16x32_bf16 v[156:159], a[68:71], a[12:15], v[236:239]\n"
      "v_mfma_f32_16x16x32_bf16 v[128:131], a[72:75], a[16:19], v[128:131]\n"
      "v_mfma_f32_16x16x32_bf16 v[136:139], a[72:75], a[20:23], v[136:139]\n"
      "v_mfma_f32_16x16x32_bf16 v[144:147], a[72:75], a[24:27], v[144:147]\n"
      "v_mfma_f32_16x16x32_bf16 v[152:155], a[72:75], a[28:31], v[152:155]\n"
      "v_mfma_f32_16x16x32_bf16 v[132:135], a[76:79], a[16:19], v[132:135]\n"
      "v_mfma_f32_16x16x32_bf16 v[140:143], a[76:79], a[20:23], v[140:143]\n"
      "v_mfma_f32_16x16x32_bf16 v[148:151], a[76:79], a[24:27], v[148:151]\n"
      "v_mfma_f32_16x16x32_bf16 v[156:159], a[76:79], a[28:31], v[156:159]\n"
      "v_mfma_f32_16x16x32_bf16 v[128:131], a[80:83], a[32:35], v[128:131]\n"
      "v_mfma_f32_16x16x32_bf16 v[136:139], a[80:83], a[36:39], v[136:139]\n"
      "v_mfma_f32_16x16x32_bf16 v[144:147], a[80:83], a[40:43], v[144:147]\n"
      "v_mfma_f32_16x16x32_bf16 v[152:155], a[80:83], a[44:47], v[152:155]\n"
      "v_mfma_f32_16x16x32_bf16 v[132:135], a[84:87], a[32:35], v[132:135]\n"
      "v_mfma_f32_16x16x32_bf16 v[140:143], a[84:87], a[36:39], v[140:143]\n"
      "v_mfma_f32_16x16x32_bf16 v[148:151], a[84:87], a[40:43], v[148:151]\n"
      "v_mfma_f32_16x16x32_bf16 v[156:159], a[84:87], a[44:47], v[156:159]\n"
      "v_mfma_f32_16x16x32_bf16 v[128:131], a[88:91], a[48:51], v[128:131]\n"
      "v_mfma_f32_16x16x32_bf16 v[136:139], a[88:91], a[52:55], v[136:139]\n"
      "v_mfma_f32_16x16x32_bf16 v[144:147], a[88:91], a[56:59], v[144:147]\n"
      "v_mfma_f32_16x16x32_bf16 v[152:155], a[88:91], a[60:63], v[152:155]\n"
      "v_mfma_f32_16x16x32_bf16 v[132:135], a[92:95], a[48:51], v[132:135]\n"
      "v_mfma_f32_16x16x32_bf16 v[140:143], a[92:95], a[52:55], v[140:143]\n"
      "v_mfma_f32_16x16x32_bf16 v[148:151], a[92:95], a[56:59], v[148:151]\n"
      "v_mfma_f32_16x16x32_bf16 v[156:159], a[92:95], a[60:63], v[156:159]\n"
      "s_cmp_eq_u32 s64, 0\n"
      "s_cbranch_scc0 NRET1_0_%=\n"
      "v_mov_b64 v[208:209], v[160:161]\n"
      "v_mov_b64 v[210:211], v[162:163]\n"
      "v_mov_b64 v[212:213], v[164:165]\n"
      "v_mov_b64 v[214:215], v[166:167]\n"
      "v_mov_b64 v[216:217], v[168:169]\n"
      "v_mov_b64 v[218:219], v[170:171]\n"
      "v_mov_b64 v[220:221], v[172:173]\n"
      "v_mov_b64 v[222:223], v[174:175]\n"
      "s_nop 1\n"
      "s_branch RET1_0_%=\n"
      "NRET1_0_%=:\n"
      "s_cmp_eq_u32 s64, 1\n"
      "s_cbranch_scc0 NRET1_1_%=\n"
      "v_mov_b64 v[208:209], v[160:161]\n"
      "v_mov_b64 v[210:211], v[162:163]\n"
      "v_mov_b64 v[212:213], v[164:165]\n"
      "v_mov_b64 v[214:215], v[166:167]\n"
      "v_mov_b64 v[216:217], v[168:169]\n"
      "v_mov_b64 v[218:219], v[170:171]\n"
      "v_mov_b64 v[220:221], v[172:173]\n"
      "v_mov_b64 v[222:223], v[174:175]\n"
      "s_nop 1\n"
      "s_branch RET1_1_%=\n"
      "NRET1_1_%=:\n"
      "s_cmp_eq_u32 s64, 2\n"
      "s_cbranch_scc0 NRET1_2_%=\n"
      "v_mov_b64 v[208:209], v[160:161]\n"
      "v_mov_b64 v[210:211], v[162:163]\n"
      "v_mov_b64 v[212:213], v[164:165]\n"
      "v_mov_b64 v[214:215], v[166:167]\n"
      "v_mov_b64 v[216:217], v[168:169]\n"
      "v_mov_b64 v[218:219], v[170:171]\n"
      "v_mov_b64 v[220:221], v[172:173]\n"
      "v_mov_b64 v[222:223], v[174:175]\n"
      "s_nop 1\n"
      "s_branch RET1_2_%=\n"
      "NRET1_2_%=:\n"
      "s_cmp_eq_u32 s64, 3\n"
      "s_cbranch_scc0 NRET1_3_%=\n"
      "v_mov_b64 v[208:209], v[160:161]\n"
      "v_mov_b64 v[210:211], v[162:163]\n"
      "v_mov_b64 v[212:213], v[164:165]\n"
      "v_mov_b64 v[214:215], v[166:167]\n"
      "v_mov_b64 v[216:217], v[168:169]\n"
      "v_mov_b64 v[218:219], v[170:171]\n"
      "v_mov_b64 v[220:221], v[172:173]\n"
      "v_mov_b64 v[222:223], v[174:175]\n"
      "s_nop 1\n"
      "s_branch RET1_3_%=\n"
      "NRET1_3_%=:\n"
      "s_cmp_eq_u32 s64, 4\n"
      "s_cbranch_scc0 NRET1_4_%=\n"
      "v_mov_b64 v[208:209], v[160:161]\n"
      "v_mov_b64 v[210:211], v[162:163]\n"
      "v_mov_b64 v[212:213], v[164:165]\n"
      "v_mov_b64 v[214:215], v[166:167]\n"
      "v_mov_b64 v[216:217], v[168:169]\n"
      "v_mov_b64 v[218:219], v[170:171]\n"
      "v_mov_b64 v[220:221], v[172:173]\n"
      "v_mov_b64 v[222:223], v[174:175]\n"
      "s_nop 1\n"
      "s_branch RET1_4_%=\n"
      "NRET1_4_%=:\n"
      "s_cmp_eq_u32 s64, 5\n"
      "s_cbranch_scc0 NRET1_5_%=\n"
      "v_mov_b64 v[0:1], v[160:161]\n"
      "v_mov_b64 v[2:3], v[162:163]\n"
      "v_mov_b64 v[4:5], v[164:165]\n"
      "v_mov_b64 v[6:7], v[166:167]\n"
      "v_mov_b64 v[8:9], v[168:169]\n"
      "v_mov_b64 v[10:11], v[170:171]\n"
      "v_mov_b64 v[12:13], v[172:173]\n"
      "v_mov_b64 v[14:15], v[174:175]\n"
      "s_nop 1\n"
      "s_branch RET1_5_%=\n"
      "NRET1_5_%=:\n"
      "s_cmp_eq_u32 s64, 6\n"
      "s_cbranch_scc0 NRET1_6_%=\n"
      "v_mov_b64 v[96:97], v[160:161]\n"
      "v_mov_b64 v[98:99], v[162:163]\n"
      "v_mov_b64 v[100:101], v[164:165]\n"
      "v_mov_b64 v[102:103], v[166:167]\n"
      "v_mov_b64 v[104:105], v[168:169]\n"
      "v_mov_b64 v[106:107], v[170:171]\n"
      "v_mov_b64 v[108:109], v[172:173]\n"
      "v_mov_b64 v[110:111], v[174:175]\n"
      "v_mov_b32 v117, v224\n"
      "s_nop 1\n"
      "v_mov_b32_dpp v117, v228 quad_perm:[0,1,2,3] row_mask:0x2 bank_mask:0xf\n"
      "s_nop 1\n"
      "v_mov_b32_dpp v117, v232 quad_perm:[0,1,2,3] row_mask:0x4 bank_mask:0xf\n"
      "s_nop 1\n"
      "v_mov_b32_dpp v117, v236 quad_perm:[0,1,2,3] row_mask:0x8 bank_mask:0xf\n"
      "v_mfma_f32_16x16x32_bf16 a[228:231], a[196:199], v[96:99], a[192:195]\n"
      "v_mfma_f32_16x16x32_bf16 a[228:231], a[200:203], v[100:103], a[228:231]\n"
      "v_mfma_f32_16x16x32_bf16 a[228:231], a[204:207], v[104:107], a[228:231]\n"
      "ds_write_b128 v125, a[160:163] offset:0\n"
      "ds_write_b128 v125, a[164:167] offset:1024\n"
      "ds_write_b128 v125, a[168:171] offset:2048\n"
      "ds_write_b128 v125, a[172:175] offset:3072\n"
      "ds_write_b128 v125, a[176:179] offset:4096\n"
      "ds_write_b128 v125, a[180:183] offset:5120\n"
      "ds_write_b128 v125, a[184:187] offset:6144\n"
      "ds_write_b128 v125, a[188:191] offset:7168\n"
      "ds_write_b128 v125, v[96:99] offset:8192\n"
      "ds_write_b128 v125, v[100:103] offset:9216\n"
      "ds_write_b128 v125, v[104:107] offset:10240\n"
      "ds_write_b32 v125, v117 offset:12292\n"
      "s_nop 1\n"
      "s_branch RET1_6_%=\n"
      "NRET1_6_%=:\n"
      "SCALE_%=:\n"
      "v_pk_mul_f32 v[32:33], v[32:33], v[112:113] op_sel:[0,0] op_sel_hi:[1,0]\n"
      "v_pk_mul_f32 v[34:35], v[34:35], v[112:113] op_sel:[0,0] op_sel_hi:[1,0]\n"
      "v_pk_mul_f32 v[36:37], v[36:37], v[112:113] op_sel:[0,1] op_sel_hi:[1,1]\n"
      "v_pk_mul_f32 v[38:39], v[38:39], v[112:113] op_sel:[0,1] op_sel_hi:[1,1]\n"
      "v_pk_mul_f32 v[40:41], v[40:41], v[114:115] op_sel:[0,0] op_sel_hi:[1,0]\n"
      "v_pk_mul_f32 v[42:43], v[42:43], v[114:115] op_sel:[0,0] op_sel_hi:[1,0]\n"
      "v_pk_mul_f32 v[44:45], v[44:45], v[114:115] op_sel:[0,1] op_sel_hi:[1,1]\n"
      "v_pk_mul_f32 v[46:47], v[46:47], v[114:115] op_sel:[0,1] op_sel_hi:[1,1]\n"
      "v_pk_mul_f32 v[48:49], v[48:49], v[112:113] op_sel:[0,0] op_sel_hi:[1,0]\n"
      "v_pk_mul_f32 v[50:51], v[50:51], v[112:113] op_sel:[0,0] op_sel_hi:[1,0]\n"
      "v_pk_mul_f32 v[52:53], v[52:53], v[112:113] op_sel:[0,1] op_sel_hi:[1,1]\n"
      "v_pk_mul_f32 v[54:55], v[54:55], v[112:113] op_sel:[0,1] op_sel_hi:[1,1]\n"
      "v_pk_mul_f32 v[56:57], v[56:57], v[114:115] op_sel:[0,0] op_sel_hi:[1,0]\n"
      "v_pk_mul_f32 v[58:59], v[58:59], v[114:115] op_sel:[0,0] op_sel_hi:[1,0]\n"
      "v_pk_mul_f32 v[60:61], v[60:61], v[114:115] op_sel:[0,1] op_sel_hi:[1,1]\n"
      "v_pk_mul_f32 v[62:63], v[62:63], v[114:115] op_sel:[0,1] op_sel_hi:[1,1]\n"
      "v_pk_mul_f32 v[64:65], v[64:65], v[112:113] op_sel:[0,0] op_sel_hi:[1,0]\n"
      "v_pk_mul_f32 v[66:67], v[66:67], v[112:113] op_sel:[0,0] op_sel_hi:[1,0]\n"
      "v_pk_mul_f32 v[68:69], v[68:69], v[112:113] op_sel:[0,1] op_sel_hi:[1,1]\n"
      "v_pk_mul_f32 v[70:71], v[70:71], v[112:113] op_sel:[0,1] op_sel_hi:[1,1]\n"
      "v_pk_mul_f32 v[72:73], v[72:73], v[114:115] op_sel:[0,0] op_sel_hi:[1,0]\n"
      "v_pk_mul_f32 v[74:75], v[74:75], v[114:115] op_sel:[0,0] op_sel_hi:[1,0]\n"
      "v_pk_mul_f32 v[76:77], v[76:77], v[114:115] op_sel:[0,1] op_sel_hi:[1,1]\n"
      "v_pk_mul_f32 v[78:79], v[78:79], v[114:115] op_sel:[0,1] op_sel_hi:[1,1]\n"
      "v_pk_mul_f32 v[80:81], v[80:81], v[112:113] op_sel:[0,0] op_sel_hi:[1,0]\n"
      "v_pk_mul_f32 v[82:83], v[82:83], v[112:113] op_sel:[0,0] op_sel_hi:[1,0]\n"
      "v_pk_mul_f32 v[84:85], v[84:85], v[112:113] op_sel:[0,1] op_sel_hi:[1,1]\n"
      "v_pk_mul_f32 v[86:87], v[86:87], v[112:113] op_sel:[0,1] op_sel_hi:[1,1]\n"
      "v_pk_mul_f32 v[88:89], v[88:89], v[114:115] op_sel:[0,0] op_sel_hi:[1,0]\n"
      "v_pk_mul_f32 v[90:91], v[90:91], v[114:115] op_sel:[0,0] op_sel_hi:[1,0]\n"
      "v_pk_mul_f32 v[92:93], v[92:93], v[114:115] op_sel:[0,1] op_sel_hi:[1,1]\n"
      "v_pk_mul_f32 v[94:95], v[94:95], v[114:115] op_sel:[0,1] op_sel_hi:[1,1]\n"
      "v_accvgpr_read_b32 v118, a128\n"
      "v_accvgpr_read_b32 v119, a129\n"
      "v_pk_mul_f32 v[118:119], v[118:119], v[112:113] op_sel:[0,0] op_sel_hi:[1,0]\n"
      "v_accvgpr_write_b32 a128, v118\n"
      "v_accvgpr_write_b32 a129, v119\n"
      "v_accvgpr_read_b32 v118, a130\n"
      "v_accvgpr_read_b32 v119, a131\n"
      "v_pk_mul_f32 v[118:119], v[118:119], v[112:113] op_sel:[0,0] op_sel_hi:[1,0]\n"
      "v_accvgpr_write_b32 a130, v118\n"
      "v_accvgpr_write_b32 a131, v119\n"
      "v_accvgpr_read_b32 v118, a132\n"
      "v_accvgpr_read_b32 v119, a133\n"
      "v_pk_mul_f32 v[118:119], v[118:119], v[112:113] op_sel:[0,1] op_sel_hi:[1,1]\n"
      "v_accvgpr_write_b32 a132, v118\n"
      "v_accvgpr_write_b32 a133, v119\n"
      "v_accvgpr_read_b32 v118, a134\n"
      "v_accvgpr_read_b32 v119, a135\n"
      "v_pk_mul_f32 v[118:119], v[118:119], v[112:113] op_sel:[0,1] op_sel_hi:[1,1]\n"
      "v_accvgpr_write_b32 a134, v118\n"
      "v_accvgpr_write_b32 a135, v119\n"
      "v_accvgpr_read_b32 v118, a136\n"
      "v_accvgpr_read_b32 v119, a137\n"
      "v_pk_mul_f32 v[118:119], v[118:119], v[114:115] op_sel:[0,0] op_sel_hi:[1,0]\n"
      "v_accvgpr_write_b32 a136, v118\n"
      "v_accvgpr_write_b32 a137, v119\n"
      "v_accvgpr_read_b32 v118, a138\n"
      "v_accvgpr_read_b32 v119, a139\n"
      "v_pk_mul_f32 v[118:119], v[118:119], v[114:115] op_sel:[0,0] op_sel_hi:[1,0]\n"
      "v_accvgpr_write_b32 a138, v118\n"
      "v_accvgpr_write_b32 a139, v119\n"
      "v_accvgpr_read_b32 v118, a140\n"
      "v_accvgpr_read_b32 v119, a141\n"
      "v_pk_mul_f32 v[118:119], v[118:119], v[114:115] op_sel:[0,1] op_sel_hi:[1,1]\n"
      "v_accvgpr_write_b32 a140, v118\n"
      "v_accvgpr_write_b32 a141, v119\n"
      "v_accvgpr_read_b32 v118, a142\n"
      "v_accvgpr_read_b32 v119, a143\n"
      "v_pk_mul_f32 v[118:119], v[118:119], v[114:115] op_sel:[0,1] op_sel_hi:[1,1]\n"
      "v_accvgpr_write_b32 a142, v118\n"
      "v_accvgpr_write_b32 a143, v119\n"
      "v_accvgpr_read_b32 v118, a144\n"
      "v_accvgpr_read_b32 v119, a145\n"
      "v_pk_mul_f32 v[118:119], v[118:119], v[112:113] op_sel:[0,0] op_sel_hi:[1,0]\n"
      "v_accvgpr_write_b32 a144, v118\n"
      "v_accvgpr_write_b32 a145, v119\n"
      "v_accvgpr_read_b32 v118, a146\n"
      "v_accvgpr_read_b32 v119, a147\n"
      "v_pk_mul_f32 v[118:119], v[118:119], v[112:113] op_sel:[0,0] op_sel_hi:[1,0]\n"
      "v_accvgpr_write_b32 a146, v118\n"
      "v_accvgpr_write_b32 a147, v119\n"
      "v_accvgpr_read_b32 v118, a148\n"
      "v_accvgpr_read_b32 v119, a149\n"
      "v_pk_mul_f32 v[118:119], v[118:119], v[112:113] op_sel:[0,1] op_sel_hi:[1,1]\n"
      "v_accvgpr_write_b32 a148, v118\n"
      "v_accvgpr_write_b32 a149, v119\n"
      "v_accvgpr_read_b32 v118, a150\n"
      "v_accvgpr_read_b32 v119, a151\n"
      "v_pk_mul_f32 v[118:119], v[118:119], v[112:113] op_sel:[0,1] op_sel_hi:[1,1]\n"
      "v_accvgpr_write_b32 a150, v118\n"
      "v_accvgpr_write_b32 a151, v119\n"
      "v_accvgpr_read_b32 v118, a152\n"
      "v_accvgpr_read_b32 v119, a153\n"
      "v_pk_mul_f32 v[118:119], v[118:119], v[114:115] op_sel:[0,0] op_sel_hi:[1,0]\n"
      "v_accvgpr_write_b32 a152, v118\n"
      "v_accvgpr_write_b32 a153, v119\n"
      "v_accvgpr_read_b32 v118, a154\n"
      "v_accvgpr_read_b32 v119, a155\n"
      "v_pk_mul_f32 v[118:119], v[118:119], v[114:115] op_sel:[0,0] op_sel_hi:[1,0]\n"
      "v_accvgpr_write_b32 a154, v118\n"
      "v_accvgpr_write_b32 a155, v119\n"
      "v_accvgpr_read_b32 v118, a156\n"
      "v_accvgpr_read_b32 v119, a157\n"
      "v_pk_mul_f32 v[118:119], v[118:119], v[114:115] op_sel:[0,1] op_sel_hi:[1,1]\n"
      "v_accvgpr_write_b32 a156, v118\n"
      "v_accvgpr_write_b32 a157, v119\n"
      "v_accvgpr_read_b32 v118, a158\n"
      "v_accvgpr_read_b32 v119, a159\n"
      "v_pk_mul_f32 v[118:119], v[118:119], v[114:115] op_sel:[0,1] op_sel_hi:[1,1]\n"
      "v_accvgpr_write_b32 a158, v118\n"
      "v_accvgpr_write_b32 a159, v119\n"
      "v_accvgpr_read_b32 v118, a160\n"
      "v_accvgpr_read_b32 v119, a161\n"
      "v_pk_mul_f32 v[118:119], v[118:119], v[112:113] op_sel:[0,0] op_sel_hi:[1,0]\n"
      "v_accvgpr_write_b32 a160, v118\n"
      "v_accvgpr_write_b32 a161, v119\n"
      "v_accvgpr_read_b32 v118, a162\n"
      "v_accvgpr_read_b32 v119, a163\n"
      "v_pk_mul_f32 v[118:119], v[118:119], v[112:113] op_sel:[0,0] op_sel_hi:[1,0]\n"
      "v_accvgpr_write_b32 a162, v118\n"
      "v_accvgpr_write_b32 a163, v119\n"
      "v_accvgpr_read_b32 v118, a164\n"
      "v_accvgpr_read_b32 v119, a165\n"
      "v_pk_mul_f32 v[118:119], v[118:119], v[112:113] op_sel:[0,1] op_sel_hi:[1,1]\n"
      "v_accvgpr_write_b32 a164, v118\n"
      "v_accvgpr_write_b32 a165, v119\n"
      "v_accvgpr_read_b32 v118, a166\n"
      "v_accvgpr_read_b32 v119, a167\n"
      "v_pk_mul_f32 v[118:119], v[118:119], v[112:113] op_sel:[0,1] op_sel_hi:[1,1]\n"
      "v_accvgpr_write_b32 a166, v118\n"
      "v_accvgpr_write_b32 a167, v119\n"
      "v_accvgpr_read_b32 v118, a168\n"
      "v_accvgpr_read_b32 v119, a169\n"
      "v_pk_mul_f32 v[118:119], v[118:119], v[114:115] op_sel:[0,0] op_sel_hi:[1,0]\n"
      "v_accvgpr_write_b32 a168, v118\n"
      "v_accvgpr_write_b32 a169, v119\n"
      "v_accvgpr_read_b32 v118, a170\n"
      "v_accvgpr_read_b32 v119, a171\n"
      "v_pk_mul_f32 v[118:119], v[118:119], v[114:115] op_sel:[0,0] op_sel_hi:[1,0]\n"
      "v_accvgpr_write_b32 a170, v118\n"
      "v_accvgpr_write_b32 a171, v119\n"
      "v_accvgpr_read_b32 v118, a172\n"
      "v_accvgpr_read_b32 v119, a173\n"
      "v_pk_mul_f32 v[118:119], v[118:119], v[114:115] op_sel:[0,1] op_sel_hi:[1,1]\n"
      "v_accvgpr_write_b32 a172, v118\n"
      "v_accvgpr_write_b32 a173, v119\n"
      "v_accvgpr_read_b32 v118, a174\n"
      "v_accvgpr_read_b32 v119, a175\n"
      "v_pk_mul_f32 v[118:119], v[118:119], v[114:115] op_sel:[0,1] op_sel_hi:[1,1]\n"
      "v_accvgpr_write_b32 a174, v118\n"
      "v_accvgpr_write_b32 a175, v119\n"
      "v_accvgpr_read_b32 v118, a176\n"
      "v_accvgpr_read_b32 v119, a177\n"
      "v_pk_mul_f32 v[118:119], v[118:119], v[112:113] op_sel:[0,0] op_sel_hi:[1,0]\n"
      "v_accvgpr_write_b32 a176, v118\n"
      "v_accvgpr_write_b32 a177, v119\n"
      "v_accvgpr_read_b32 v118, a178\n"
      "v_accvgpr_read_b32 v119, a179\n"
      "v_pk_mul_f32 v[118:119], v[118:119], v[112:113] op_sel:[0,0] op_sel_hi:[1,0]\n"
      "v_accvgpr_write_b32 a178, v118\n"
      "v_accvgpr_write_b32 a179, v119\n"
      "v_accvgpr_read_b32 v118, a180\n"
      "v_accvgpr_read_b32 v119, a181\n"
      "v_pk_mul_f32 v[118:119], v[118:119], v[112:113] op_sel:[0,1] op_sel_hi:[1,1]\n"
      "v_accvgpr_write_b32 a180, v118\n"
      "v_accvgpr_write_b32 a181, v119\n"
      "v_accvgpr_read_b32 v118, a182\n"
      "v_accvgpr_read_b32 v119, a183\n"
      "v_pk_mul_f32 v[118:119], v[118:119], v[112:113] op_sel:[0,1] op_sel_hi:[1,1]\n"
      "v_accvgpr_write_b32 a182, v118\n"
      "v_accvgpr_write_b32 a183, v119\n"
      "v_accvgpr_read_b32 v118, a184\n"
      "v_accvgpr_read_b32 v119, a185\n"
      "v_pk_mul_f32 v[118:119], v[118:119], v[114:115] op_sel:[0,0] op_sel_hi:[1,0]\n"
      "v_accvgpr_write_b32 a184, v118\n"
      "v_accvgpr_write_b32 a185, v119\n"
      "v_accvgpr_read_b32 v118, a186\n"
      "v_accvgpr_read_b32 v119, a187\n"
      "v_pk_mul_f32 v[118:119], v[118:119], v[114:115] op_sel:[0,0] op_sel_hi:[1,0]\n"
      "v_accvgpr_write_b32 a186, v118\n"
      "v_accvgpr_write_b32 a187, v119\n"
      "v_accvgpr_read_b32 v118, a188\n"
      "v_accvgpr_read_b32 v119, a189\n"
      "v_pk_mul_f32 v[118:119], v[118:119], v[114:115] op_sel:[0,1] op_sel_hi:[1,1]\n"
      "v_accvgpr_write_b32 a188, v118\n"
      "v_accvgpr_write_b32 a189, v119\n"
      "v_accvgpr_read_b32 v118, a190\n"
      "v_accvgpr_read_b32 v119, a191\n"
      "v_pk_mul_f32 v[118:119], v[118:119], v[114:115] op_sel:[0,1] op_sel_hi:[1,1]\n"
      "v_accvgpr_write_b32 a190, v118\n"
      "v_accvgpr_write_b32 a191, v119\n"
      "v_mov_b32 v118, v112\n"
      "s_nop 1\n"
      "v_mov_b32_dpp v118, v113 quad_perm:[0,1,2,3] row_mask:0x2 bank_mask:0xf\n"
      "s_nop 1\n"
      "v_mov_b32_dpp v118, v114 quad_perm:[0,1,2,3] row_mask:0x4 bank_mask:0xf\n"
      "s_nop 1\n"
      "v_mov_b32_dpp v118, v115 quad_perm:[0,1,2,3] row_mask:0x8 bank_mask:0xf\n"
      "v_accvgpr_read_b32 v119, a192\n"
      "v_mul_f32 v118, v118, v119\n"
      "v_accvgpr_write_b32 a192, v118\n"
      "v_lshlrev_b32 v118, 16, v208\n"
      "v_and_b32 v119, 0xffff0000, v208\n"
      "v_pk_mul_f32 v[118:119], v[118:119], v[112:113] op_sel:[0,0] op_sel_hi:[1,0]\n"
      "v_cvt_pk_bf16_f32 v208, v118, v119\n"
      "v_lshlrev_b32 v118, 16, v209\n"
      "v_and_b32 v119, 0xffff0000, v209\n"
      "v_pk_mul_f32 v[118:119], v[118:119], v[112:113] op_sel:[0,0] op_sel_hi:[1,0]\n"
      "v_cvt_pk_bf16_f32 v209, v118, v119\n"
      "v_lshlrev_b32 v118, 16, v210\n"
      "v_and_b32 v119, 0xffff0000, v210\n"
      "v_pk_mul_f32 v[118:119], v[118:119], v[112:113] op_sel:[0,0] op_sel_hi:[1,0]\n"
      "v_cvt_pk_bf16_f32 v210, v118, v119\n"
      "v_lshlrev_b32 v118, 16, v211\n"
      "v_and_b32 v119, 0xffff0000, v211\n"
      "v_pk_mul_f32 v[118:119], v[118:119], v[112:113] op_sel:[0,0] op_sel_hi:[1,0]\n"
      "v_cvt_pk_bf16_f32 v211, v118, v119\n"
      "v_lshlrev_b32 v118, 16, v212\n"
      "v_and_b32 v119, 0xffff0000, v212\n"
      "v_pk_mul_f32 v[118:119], v[118:119], v[112:113] op_sel:[0,1] op_sel_hi:[1,1]\n"
      "v_cvt_pk_bf16_f32 v212, v118, v119\n"
      "v_lshlrev_b32 v118, 16, v213\n"
      "v_and_b32 v119, 0xffff0000, v213\n"
      "v_pk_mul_f32 v[118:119], v[118:119], v[112:113] op_sel:[0,1] op_sel_hi:[1,1]\n"
      "v_cvt_pk_bf16_f32 v213, v118, v119\n"
      "v_lshlrev_b32 v118, 16, v214\n"
      "v_and_b32 v119, 0xffff0000, v214\n"
      "v_pk_mul_f32 v[118:119], v[118:119], v[112:113] op_sel:[0,1] op_sel_hi:[1,1]\n"
      "v_cvt_pk_bf16_f32 v214, v118, v119\n"
      "v_lshlrev_b32 v118, 16, v215\n"
      "v_and_b32 v119, 0xffff0000, v215\n"
      "v_pk_mul_f32 v[118:119], v[118:119], v[112:113] op_sel:[0,1] op_sel_hi:[1,1]\n"
      "v_cvt_pk_bf16_f32 v215, v118, v119\n"
      "v_lshlrev_b32 v118, 16, v216\n"
      "v_and_b32 v119, 0xffff0000, v216\n"
      "v_pk_mul_f32 v[118:119], v[118:119], v[114:115] op_sel:[0,0] op_sel_hi:[1,0]\n"
      "v_cvt_pk_bf16_f32 v216, v118, v119\n"
      "v_lshlrev_b32 v118, 16, v217\n"
      "v_and_b32 v119, 0xffff0000, v217\n"
      "v_pk_mul_f32 v[118:119], v[118:119], v[114:115] op_sel:[0,0] op_sel_hi:[1,0]\n"
      "v_cvt_pk_bf16_f32 v217, v118, v119\n"
      "v_lshlrev_b32 v118, 16, v218\n"
      "v_and_b32 v119, 0xffff0000, v218\n"
      "v_pk_mul_f32 v[118:119], v[118:119], v[114:115] op_sel:[0,0] op_sel_hi:[1,0]\n"
      "v_cvt_pk_bf16_f32 v218, v118, v119\n"
      "v_lshlrev_b32 v118, 16, v219\n"
      "v_and_b32 v119, 0xffff0000, v219\n"
      "v_pk_mul_f32 v[118:119], v[118:119], v[114:115] op_sel:[0,0] op_sel_hi:[1,0]\n"
      "v_cvt_pk_bf16_f32 v219, v118, v119\n"
      "v_lshlrev_b32 v118, 16, v220\n"
      "v_and_b32 v119, 0xffff0000, v220\n"
      "v_pk_mul_f32 v[118:119], v[118:119], v[114:115] op_sel:[0,1] op_sel_hi:[1,1]\n"
      "v_cvt_pk_bf16_f32 v220, v118, v119\n"
      "v_lshlrev_b32 v118, 16, v221\n"
      "v_and_b32 v119, 0xffff0000, v221\n"
      "v_pk_mul_f32 v[118:119], v[118:119], v[114:115] op_sel:[0,1] op_sel_hi:[1,1]\n"
      "v_cvt_pk_bf16_f32 v221, v118, v119\n"
      "v_lshlrev_b32 v118, 16, v222\n"
      "v_and_b32 v119, 0xffff0000, v222\n"
      "v_pk_mul_f32 v[118:119], v[118:119], v[114:115] op_sel:[0,1] op_sel_hi:[1,1]\n"
      "v_cvt_pk_bf16_f32 v222, v118, v119\n"
      "v_lshlrev_b32 v118, 16, v223\n"
      "v_and_b32 v119, 0xffff0000, v223\n"
      "v_pk_mul_f32 v[118:119], v[118:119], v[114:115] op_sel:[0,1] op_sel_hi:[1,1]\n"
      "v_cvt_pk_bf16_f32 v223, v118, v119\n"
      "v_lshlrev_b32 v118, 16, v192\n"
      "v_and_b32 v119, 0xffff0000, v192\n"
      "v_pk_mul_f32 v[118:119], v[118:119], v[112:113] op_sel:[0,0] op_sel_hi:[1,0]\n"
      "v_cvt_pk_bf16_f32 v192, v118, v119\n"
      "v_lshlrev_b32 v118, 16, v193\n"
      "v_and_b32 v119, 0xffff0000, v193\n"
      "v_pk_mul_f32 v[118:119], v[118:119], v[112:113] op_sel:[0,0] op_sel_hi:[1,0]\n"
      "v_cvt_pk_bf16_f32 v193, v118, v119\n"
      "v_lshlrev_b32 v118, 16, v194\n"
      "v_and_b32 v119, 0xffff0000, v194\n"
      "v_pk_mul_f32 v[118:119], v[118:119], v[112:113] op_sel:[0,0] op_sel_hi:[1,0]\n"
      "v_cvt_pk_bf16_f32 v194, v118, v119\n"
      "v_lshlrev_b32 v118, 16, v195\n"
      "v_and_b32 v119, 0xffff0000, v195\n"
      "v_pk_mul_f32 v[118:119], v[118:119], v[112:113] op_sel:[0,0] op_sel_hi:[1,0]\n"
      "v_cvt_pk_bf16_f32 v195, v118, v119\n"
      "v_lshlrev_b32 v118, 16, v196\n"
      "v_and_b32 v119, 0xffff0000, v196\n"
      "v_pk_mul_f32 v[118:119], v[118:119], v[112:113] op_sel:[0,1] op_sel_hi:[1,1]\n"
      "v_cvt_pk_bf16_f32 v196, v118, v119\n"
      "v_lshlrev_b32 v118, 16, v197\n"
      "v_and_b32 v119, 0xffff0000, v197\n"
      "v_pk_mul_f32 v[118:119], v[118:119], v[112:113] op_sel:[0,1] op_sel_hi:[1,1]\n"
      "v_cvt_pk_bf16_f32 v197, v118, v119\n"
      "v_lshlrev_b32 v118, 16, v198\n"
      "v_and_b32 v119, 0xffff0000, v198\n"
      "v_pk_mul_f32 v[118:119], v[118:119], v[112:113] op_sel:[0,1] op_sel_hi:[1,1]\n"
      "v_cvt_pk_bf16_f32 v198, v118, v119\n"
      "v_lshlrev_b32 v118, 16, v199\n"
      "v_and_b32 v119, 0xffff0000, v199\n"
      "v_pk_mul_f32 v[118:119], v[118:119], v[112:113] op_sel:[0,1] op_sel_hi:[1,1]\n"
      "v_cvt_pk_bf16_f32 v199, v118, v119\n"
      "v_lshlrev_b32 v118, 16, v200\n"
      "v_and_b32 v119, 0xffff0000, v200\n"
      "v_pk_mul_f32 v[118:119], v[118:119], v[114:115] op_sel:[0,0] op_sel_hi:[1,0]\n"
      "v_cvt_pk_bf16_f32 v200, v118, v119\n"
      "v_lshlrev_b32 v118, 16, v201\n"
      "v_and_b32 v119, 0xffff0000, v201\n"
      "v_pk_mul_f32 v[118:119], v[118:119], v[114:115] op_sel:[0,0] op_sel_hi:[1,0]\n"
      "v_cvt_pk_bf16_f32 v201, v118, v119\n"
      "v_lshlrev_b32 v118, 16, v202\n"
      "v_and_b32 v119, 0xffff0000, v202\n"
      "v_pk_mul_f32 v[118:119], v[118:119], v[114:115] op_sel:[0,0] op_sel_hi:[1,0]\n"
      "v_cvt_pk_bf16_f32 v202, v118, v119\n"
      "v_lshlrev_b32 v118, 16, v203\n"
      "v_and_b32 v119, 0xffff0000, v203\n"
      "v_pk_mul_f32 v[118:119], v[118:119], v[114:115] op_sel:[0,0] op_sel_hi:[1,0]\n"
      "v_cvt_pk_bf16_f32 v203, v118, v119\n"
      "v_lshlrev_b32 v118, 16, v204\n"
      "v_and_b32 v119, 0xffff0000, v204\n"
      "v_pk_mul_f32 v[118:119], v[118:119], v[114:115] op_sel:[0,1] op_sel_hi:[1,1]\n"
      "v_cvt_pk_bf16_f32 v204, v118, v119\n"
      "v_lshlrev_b32 v118, 16, v205\n"
      "v_and_b32 v119, 0xffff0000, v205\n"
      "v_pk_mul_f32 v[118:119], v[118:119], v[114:115] op_sel:[0,1] op_sel_hi:[1,1]\n"
      "v_cvt_pk_bf16_f32 v205, v118, v119\n"
      "v_lshlrev_b32 v118, 16, v206\n"
      "v_and_b32 v119, 0xffff0000, v206\n"
      "v_pk_mul_f32 v[118:119], v[118:119], v[114:115] op_sel:[0,1] op_sel_hi:[1,1]\n"
      "v_cvt_pk_bf16_f32 v206, v118, v119\n"
      "v_lshlrev_b32 v118, 16, v207\n"
      "v_and_b32 v119, 0xffff0000, v207\n"
      "v_pk_mul_f32 v[118:119], v[118:119], v[114:115] op_sel:[0,1] op_sel_hi:[1,1]\n"
      "v_cvt_pk_bf16_f32 v207, v118, v119\n"
      "v_lshlrev_b32 v118, 16, v0\n"
      "v_and_b32 v119, 0xffff0000, v0\n"
      "v_pk_mul_f32 v[118:119], v[118:119], v[112:113] op_sel:[0,0] op_sel_hi:[1,0]\n"
      "v_cvt_pk_bf16_f32 v0, v118, v119\n"
      "v_lshlrev_b32 v118, 16, v1\n"
      "v_and_b32 v119, 0xffff0000, v1\n"
      "v_pk_mul_f32 v[118:119], v[118:119], v[112:113] op_sel:[0,0] op_sel_hi:[1,0]\n"
      "v_cvt_pk_bf16_f32 v1, v118, v119\n"
      "v_lshlrev_b32 v118, 16, v2\n"
      "v_and_b32 v119, 0xffff0000, v2\n"
      "v_pk_mul_f32 v[118:119], v[118:119], v[112:113] op_sel:[0,0] op_sel_hi:[1,0]\n"
      "v_cvt_pk_bf16_f32 v2, v118, v119\n"
      "v_lshlrev_b32 v118, 16, v3\n"
      "v_and_b32 v119, 0xffff0000, v3\n"
      "v_pk_mul_f32 v[118:119], v[118:119], v[112:113] op_sel:[0,0] op_sel_hi:[1,0]\n"
      "v_cvt_pk_bf16_f32 v3, v118, v119\n"
      "v_lshlrev_b32 v118, 16, v4\n"
      "v_and_b32 v119, 0xffff0000, v4\n"
      "v_pk_mul_f32 v[118:119], v[118:119], v[112:113] op_sel:[0,1] op_sel_hi:[1,1]\n"
      "v_cvt_pk_bf16_f32 v4, v118, v119\n"
      "v_lshlrev_b32 v118, 16, v5\n"
      "v_and_b32 v119, 0xffff0000, v5\n"
      "v_pk_mul_f32 v[118:119], v[118:119], v[112:113] op_sel:[0,1] op_sel_hi:[1,1]\n"
      "v_cvt_pk_bf16_f32 v5, v118, v119\n"
      "v_lshlrev_b32 v118, 16, v6\n"
      "v_and_b32 v119, 0xffff0000, v6\n"
      "v_pk_mul_f32 v[118:119], v[118:119], v[112:113] op_sel:[0,1] op_sel_hi:[1,1]\n"
      "v_cvt_pk_bf16_f32 v6, v118, v119\n"
      "v_lshlrev_b32 v118, 16, v7\n"
      "v_and_b32 v119, 0xffff0000, v7\n"
      "v_pk_mul_f32 v[118:119], v[118:119], v[112:113] op_sel:[0,1] op_sel_hi:[1,1]\n"
      "v_cvt_pk_bf16_f32 v7, v118, v119\n"
      "v_lshlrev_b32 v118, 16, v8\n"
      "v_and_b32 v119, 0xffff0000, v8\n"
      "v_pk_mul_f32 v[118:119], v[118:119], v[114:115] op_sel:[0,0] op_sel_hi:[1,0]\n"
      "v_cvt_pk_bf16_f32 v8, v118, v119\n"
      "v_lshlrev_b32 v118, 16, v9\n"
      "v_and_b32 v119, 0xffff0000, v9\n"
      "v_pk_mul_f32 v[118:119], v[118:119], v[114:115] op_sel:[0,0] op_sel_hi:[1,0]\n"
      "v_cvt_pk_bf16_f32 v9, v118, v119\n"
      "v_lshlrev_b32 v118, 16, v10\n"
      "v_and_b32 v119, 0xffff0000, v10\n"
      "v_pk_mul_f32 v[118:119], v[118:119], v[114:115] op_sel:[0,0] op_sel_hi:[1,0]\n"
      "v_cvt_pk_bf16_f32 v10, v118, v119\n"
      "v_lshlrev_b32 v118, 16, v11\n"
      "v_and_b32 v119, 0xffff0000, v11\n"
      "v_pk_mul_f32 v[118:119], v[118:119], v[114:115] op_sel:[0,0] op_sel_hi:[1,0]\n"
      "v_cvt_pk_bf16_f32 v11, v118, v119\n"
      "v_lshlrev_b32 v118, 16, v12\n"
      "v_and_b32 v119, 0xffff0000, v12\n"
      "v_pk_mul_f32 v[118:119], v[118:119], v[114:115] op_sel:[0,1] op_sel_hi:[1,1]\n"
      "v_cvt_pk_bf16_f32 v12, v118, v119\n"
      "v_lshlrev_b32 v118, 16, v13\n"
      "v_and_b32 v119, 0xffff0000, v13\n"
      "v_pk_mul_f32 v[118:119], v[118:119], v[114:115] op_sel:[0,1] op_sel_hi:[1,1]\n"
      "v_cvt_pk_bf16_f32 v13, v118, v119\n"
      "v_lshlrev_b32 v118, 16, v14\n"
      "v_and_b32 v119, 0xffff0000, v14\n"
      "v_pk_mul_f32 v[118:119], v[118:119], v[114:115] op_sel:[0,1] op_sel_hi:[1,1]\n"
      "v_cvt_pk_bf16_f32 v14, v118, v119\n"
      "v_lshlrev_b32 v118, 16, v15\n"
      "v_and_b32 v119, 0xffff0000, v15\n"
      "v_pk_mul_f32 v[118:119], v[118:119], v[114:115] op_sel:[0,1] op_sel_hi:[1,1]\n"
      "v_cvt_pk_bf16_f32 v15, v118, v119\n"
      "v_lshlrev_b32 v118, 16, v16\n"
      "v_and_b32 v119, 0xffff0000, v16\n"
      "v_pk_mul_f32 v[118:119], v[118:119], v[112:113] op_sel:[0,0] op_sel_hi:[1,0]\n"
      "v_cvt_pk_bf16_f32 v16, v118, v119\n"
      "v_lshlrev_b32 v118, 16, v17\n"
      "v_and_b32 v119, 0xffff0000, v17\n"
      "v_pk_mul_f32 v[118:119], v[118:119], v[112:113] op_sel:[0,0] op_sel_hi:[1,0]\n"
      "v_cvt_pk_bf16_f32 v17, v118, v119\n"
      "v_lshlrev_b32 v118, 16, v18\n"
      "v_and_b32 v119, 0xffff0000, v18\n"
      "v_pk_mul_f32 v[118:119], v[118:119], v[112:113] op_sel:[0,0] op_sel_hi:[1,0]\n"
      "v_cvt_pk_bf16_f32 v18, v118, v119\n"
      "v_lshlrev_b32 v118, 16, v19\n"
      "v_and_b32 v119, 0xffff0000, v19\n"
      "v_pk_mul_f32 v[118:119], v[118:119], v[112:113] op_sel:[0,0] op_sel_hi:[1,0]\n"
      "v_cvt_pk_bf16_f32 v19, v118, v119\n"
      "v_lshlrev_b32 v118, 16, v20\n"
      "v_and_b32 v119, 0xffff0000, v20\n"
      "v_pk_mul_f32 v[118:119], v[118:119], v[112:113] op_sel:[0,1] op_sel_hi:[1,1]\n"
      "v_cvt_pk_bf16_f32 v20, v118, v119\n"
      "v_lshlrev_b32 v118, 16, v21\n"
      "v_and_b32 v119, 0xffff0000, v21\n"
      "v_pk_mul_f32 v[118:119], v[118:119], v[112:113] op_sel:[0,1] op_sel_hi:[1,1]\n"
      "v_cvt_pk_bf16_f32 v21, v118, v119\n"
      "v_lshlrev_b32 v118, 16, v22\n"
      "v_and_b32 v119, 0xffff0000, v22\n"
      "v_pk_mul_f32 v[118:119], v[118:119], v[112:113] op_sel:[0,1] op_sel_hi:[1,1]\n"
      "v_cvt_pk_bf16_f32 v22, v118, v119\n"
      "v_lshlrev_b32 v118, 16, v23\n"
      "v_and_b32 v119, 0xffff0000, v23\n"
      "v_pk_mul_f32 v[118:119], v[118:119], v[112:113] op_sel:[0,1] op_sel_hi:[1,1]\n"
      "v_cvt_pk_bf16_f32 v23, v118, v119\n"
      "v_lshlrev_b32 v118, 16, v24\n"
      "v_and_b32 v119, 0xffff0000, v24\n"
      "v_pk_mul_f32 v[118:119], v[118:119], v[114:115] op_sel:[0,0] op_sel_hi:[1,0]\n"
      "v_cvt_pk_bf16_f32 v24, v118, v119\n"
      "v_lshlrev_b32 v118, 16, v25\n"
      "v_and_b32 v119, 0xffff0000, v25\n"
      "v_pk_mul_f32 v[118:119], v[118:119], v[114:115] op_sel:[0,0] op_sel_hi:[1,0]\n"
      "v_cvt_pk_bf16_f32 v25, v118, v119\n"
      "v_lshlrev_b32 v118, 16, v26\n"
      "v_and_b32 v119, 0xffff0000, v26\n"
      "v_pk_mul_f32 v[118:119], v[118:119], v[114:115] op_sel:[0,0] op_sel_hi:[1,0]\n"
      "v_cvt_pk_bf16_f32 v26, v118, v119\n"
      "v_lshlrev_b32 v118, 16, v27\n"
      "v_and_b32 v119, 0xffff0000, v27\n"
      "v_pk_mul_f32 v[118:119], v[118:119], v[114:115] op_sel:[0,0] op_sel_hi:[1,0]\n"
      "v_cvt_pk_bf16_f32 v27, v118, v119\n"
      "v_lshlrev_b32 v118, 16, v28\n"
      "v_and_b32 v119, 0xffff0000, v28\n"
      "v_pk_mul_f32 v[118:119], v[118:119], v[114:115] op_sel:[0,1] op_sel_hi:[1,1]\n"
      "v_cvt_pk_bf16_f32 v28, v118, v119\n"
      "v_lshlrev_b32 v118, 16, v29\n"
      "v_and_b32 v119, 0xffff0000, v29\n"
      "v_pk_mul_f32 v[118:119], v[118:119], v[114:115] op_sel:[0,1] op_sel_hi:[1,1]\n"
      "v_cvt_pk_bf16_f32 v29, v118, v119\n"
      "v_lshlrev_b32 v118, 16, v30\n"
      "v_and_b32 v119, 0xffff0000, v30\n"
      "v_pk_mul_f32 v[118:119], v[118:119], v[114:115] op_sel:[0,1] op_sel_hi:[1,1]\n"
      "v_cvt_pk_bf16_f32 v30, v118, v119\n"
      "v_lshlrev_b32 v118, 16, v31\n"
      "v_and_b32 v119, 0xffff0000, v31\n"
      "v_pk_mul_f32 v[118:119], v[118:119], v[114:115] op_sel:[0,1] op_sel_hi:[1,1]\n"
      "v_cvt_pk_bf16_f32 v31, v118, v119\n"
      "v_lshlrev_b32 v118, 16, v96\n"
      "v_and_b32 v119, 0xffff0000, v96\n"
      "v_pk_mul_f32 v[118:119], v[118:119], v[112:113] op_sel:[0,0] op_sel_hi:[1,0]\n"
      "v_cvt_pk_bf16_f32 v96, v118, v119\n"
      "v_lshlrev_b32 v118, 16, v97\n"
      "v_and_b32 v119, 0xffff0000, v97\n"
      "v_pk_mul_f32 v[118:119], v[118:119], v[112:113] op_sel:[0,0] op_sel_hi:[1,0]\n"
      "v_cvt_pk_bf16_f32 v97, v118, v119\n"
      "v_lshlrev_b32 v118, 16, v98\n"
      "v_and_b32 v119, 0xffff0000, v98\n"
      "v_pk_mul_f32 v[118:119], v[118:119], v[112:113] op_sel:[0,0] op_sel_hi:[1,0]\n"
      "v_cvt_pk_bf16_f32 v98, v118, v119\n"
      "v_lshlrev_b32 v118, 16, v99\n"
      "v_and_b32 v119, 0xffff0000, v99\n"
      "v_pk_mul_f32 v[118:119], v[118:119], v[112:113] op_sel:[0,0] op_sel_hi:[1,0]\n"
      "v_cvt_pk_bf16_f32 v99, v118, v119\n"
      "v_lshlrev_b32 v118, 16, v100\n"
      "v_and_b32 v119, 0xffff0000, v100\n"
      "v_pk_mul_f32 v[118:119], v[118:119], v[112:113] op_sel:[0,1] op_sel_hi:[1,1]\n"
      "v_cvt_pk_bf16_f32 v100, v118, v119\n"
      "v_lshlrev_b32 v118, 16, v101\n"
      "v_and_b32 v119, 0xffff0000, v101\n"
      "v_pk_mul_f32 v[118:119], v[118:119], v[112:113] op_sel:[0,1] op_sel_hi:[1,1]\n"
      "v_cvt_pk_bf16_f32 v101, v118, v119\n"
      "v_lshlrev_b32 v118, 16, v102\n"
      "v_and_b32 v119, 0xffff0000, v102\n"
      "v_pk_mul_f32 v[118:119], v[118:119], v[112:113] op_sel:[0,1] op_sel_hi:[1,1]\n"
      "v_cvt_pk_bf16_f32 v102, v118, v119\n"
      "v_lshlrev_b32 v118, 16, v103\n"
      "v_and_b32 v119, 0xffff0000, v103\n"
      "v_pk_mul_f32 v[118:119], v[118:119], v[112:113] op_sel:[0,1] op_sel_hi:[1,1]\n"
      "v_cvt_pk_bf16_f32 v103, v118, v119\n"
      "v_lshlrev_b32 v118, 16, v104\n"
      "v_and_b32 v119, 0xffff0000, v104\n"
      "v_pk_mul_f32 v[118:119], v[118:119], v[114:115] op_sel:[0,0] op_sel_hi:[1,0]\n"
      "v_cvt_pk_bf16_f32 v104, v118, v119\n"
      "v_lshlrev_b32 v118, 16, v105\n"
      "v_and_b32 v119, 0xffff0000, v105\n"
      "v_pk_mul_f32 v[118:119], v[118:119], v[114:115] op_sel:[0,0] op_sel_hi:[1,0]\n"
      "v_cvt_pk_bf16_f32 v105, v118, v119\n"
      "v_lshlrev_b32 v118, 16, v106\n"
      "v_and_b32 v119, 0xffff0000, v106\n"
      "v_pk_mul_f32 v[118:119], v[118:119], v[114:115] op_sel:[0,0] op_sel_hi:[1,0]\n"
      "v_cvt_pk_bf16_f32 v106, v118, v119\n"
      "v_lshlrev_b32 v118, 16, v107\n"
      "v_and_b32 v119, 0xffff0000, v107\n"
      "v_pk_mul_f32 v[118:119], v[118:119], v[114:115] op_sel:[0,0] op_sel_hi:[1,0]\n"
      "v_cvt_pk_bf16_f32 v107, v118, v119\n"
      "v_lshlrev_b32 v118, 16, v108\n"
      "v_and_b32 v119, 0xffff0000, v108\n"
      "v_pk_mul_f32 v[118:119], v[118:119], v[114:115] op_sel:[0,1] op_sel_hi:[1,1]\n"
      "v_cvt_pk_bf16_f32 v108, v118, v119\n"
      "v_lshlrev_b32 v118, 16, v109\n"
      "v_and_b32 v119, 0xffff0000, v109\n"
      "v_pk_mul_f32 v[118:119], v[118:119], v[114:115] op_sel:[0,1] op_sel_hi:[1,1]\n"
      "v_cvt_pk_bf16_f32 v109, v118, v119\n"
      "v_lshlrev_b32 v118, 16, v110\n"
      "v_and_b32 v119, 0xffff0000, v110\n"
      "v_pk_mul_f32 v[118:119], v[118:119], v[114:115] op_sel:[0,1] op_sel_hi:[1,1]\n"
      "v_cvt_pk_bf16_f32 v110, v118, v119\n"
      "v_lshlrev_b32 v118, 16, v111\n"
      "v_and_b32 v119, 0xffff0000, v111\n"
      "v_pk_mul_f32 v[118:119], v[118:119], v[114:115] op_sel:[0,1] op_sel_hi:[1,1]\n"
      "v_cvt_pk_bf16_f32 v111, v118, v119\n"
      "s_setpc_b64 s[84:85]\n"
      "MIS_%=:\n"
      "s_waitcnt lgkmcnt(0)\n"
      "v_sub_f32 v148, v153, v117\n"
      "v_min_f32 v149, 0, -v148\n"
      "v_min_f32 v148, 0, v148\n"
      "v_exp_f32 v149, v149\n"
      "v_exp_f32 v148, v148\n"
      "v_min_f32 v117, v117, v153\n"
      "v_mov_b32 v112, v148\n"
      "v_mov_b32 v228, v149\n"
      "v_mov_b32 v114, v112\n"
      "v_mov_b32 v230, v228\n"
      "s_nop 0\n"
      "v_permlane32_swap_b32 v112, v114\n"
      "v_permlane32_swap_b32 v228, v230\n"
      "v_mov_b32 v113, v112\n"
      "v_mov_b32 v229, v228\n"
      "v_mov_b32 v115, v114\n"
      "v_mov_b32 v231, v230\n"
      "v_permlane16_swap_b32 v112, v113\n"
      "v_permlane16_swap_b32 v228, v229\n"
      "v_permlane16_swap_b32 v114, v115\n"
      "v_permlane16_swap_b32 v230, v231\n"
      "s_call_b64 s[84:85], SCALE_%=\n"
      "v_pk_mul_f32 v[160:161], v[160:161], v[228:229] op_sel:[0,0] op_sel_hi:[1,0]\n"
      "v_pk_mul_f32 v[162:163], v[162:163], v[228:229] op_sel:[0,0] op_sel_hi:[1,0]\n"
      "v_pk_mul_f32 v[164:165], v[164:165], v[228:229] op_sel:[0,1] op_sel_hi:[1,1]\n"
      "v_pk_mul_f32 v[166:167], v[166:167], v[228:229] op_sel:[0,1] op_sel_hi:[1,1]\n"
      "v_pk_mul_f32 v[168:169], v[168:169], v[230:231] op_sel:[0,0] op_sel_hi:[1,0]\n"
      "v_pk_mul_f32 v[170:171], v[170:171], v[230:231] op_sel:[0,0] op_sel_hi:[1,0]\n"
      "v_pk_mul_f32 v[172:173], v[172:173], v[230:231] op_sel:[0,1] op_sel_hi:[1,1]\n"
      "v_pk_mul_f32 v[174:175], v[174:175], v[230:231] op_sel:[0,1] op_sel_hi:[1,1]\n"
      "v_pk_mul_f32 v[176:177], v[176:177], v[228:229] op_sel:[0,0] op_sel_hi:[1,0]\n"
      "v_pk_mul_f32 v[178:179], v[178:179], v[228:229] op_sel:[0,0] op_sel_hi:[1,0]\n"
      "v_pk_mul_f32 v[180:181], v[180:181], v[228:229] op_sel:[0,1] op_sel_hi:[1,1]\n"
      "v_pk_mul_f32 v[182:183], v[182:183], v[228:229] op_sel:[0,1] op_sel_hi:[1,1]\n"
      "v_pk_mul_f32 v[184:185], v[184:185], v[230:231] op_sel:[0,0] op_sel_hi:[1,0]\n"
      "v_pk_mul_f32 v[186:187], v[186:187], v[230:231] op_sel:[0,0] op_sel_hi:[1,0]\n"
      "v_pk_mul_f32 v[188:189], v[188:189], v[230:231] op_sel:[0,1] op_sel_hi:[1,1]\n"
      "v_pk_mul_f32 v[190:191], v[190:191], v[230:231] op_sel:[0,1] op_sel_hi:[1,1]\n"
      "v_mov_b32 v112, v228\n"
      "v_mov_b32 v113, v229\n"
      "v_mov_b32 v114, v230\n"
      "v_mov_b32 v115, v231\n"
      "v_accvgpr_read_b32 v128, a64\n"
      "v_accvgpr_read_b32 v129, a65\n"
      "v_accvgpr_read_b32 v130, a66\n"
      "v_accvgpr_read_b32 v131, a67\n"
      "v_accvgpr_read_b32 v132, a68\n"
      "v_accvgpr_read_b32 v133, a69\n"
      "v_accvgpr_read_b32 v134, a70\n"
      "v_accvgpr_read_b32 v135, a71\n"
      "v_accvgpr_read_b32 v136, a72\n"
      "v_accvgpr_read_b32 v137, a73\n"
      "v_accvgpr_read_b32 v138, a74\n"
      "v_accvgpr_read_b32 v139, a75\n"
      "v_accvgpr_read_b32 v140, a76\n"
      "v_accvgpr_read_b32 v141, a77\n"
      "v_accvgpr_read_b32 v142, a78\n"
      "v_accvgpr_read_b32 v143, a79\n"
      "v_lshlrev_b32 v118, 16, v128\n"
      "v_and_b32 v119, 0xffff0000, v128\n"
      "v_pk_mul_f32 v[118:119], v[118:119], v[112:113] op_sel:[0,0] op_sel_hi:[1,0]\n"
      "v_cvt_pk_bf16_f32 v128, v118, v119\n"
      "v_lshlrev_b32 v118, 16, v129\n"
      "v_and_b32 v119, 0xffff0000, v129\n"
      "v_pk_mul_f32 v[118:119], v[118:119], v[112:113] op_sel:[0,0] op_sel_hi:[1,0]\n"
      "v_cvt_pk_bf16_f32 v129, v118, v119\n"
      "v_lshlrev_b32 v118, 16, v130\n"
      "v_and_b32 v119, 0xffff0000, v130\n"
      "v_pk_mul_f32 v[118:119], v[118:119], v[112:113] op_sel:[0,0] op_sel_hi:[1,0]\n"
      "v_cvt_pk_bf16_f32 v130, v118, v119\n"
      "v_lshlrev_b32 v118, 16, v131\n"
      "v_and_b32 v119, 0xffff0000, v131\n"
      "v_pk_mul_f32 v[118:119], v[118:119], v[112:113] op_sel:[0,0] op_sel_hi:[1,0]\n"
      "v_cvt_pk_bf16_f32 v131, v118, v119\n"
      "v_lshlrev_b32 v118, 16, v132\n"
      "v_and_b32 v119, 0xffff0000, v132\n"
      "v_pk_mul_f32 v[118:119], v[118:119], v[112:113] op_sel:[0,1] op_sel_hi:[1,1]\n"
      "v_cvt_pk_bf16_f32 v132, v118, v119\n"
      "v_lshlrev_b32 v118, 16, v133\n"
      "v_and_b32 v119, 0xffff0000, v133\n"
      "v_pk_mul_f32 v[118:119], v[118:119], v[112:113] op_sel:[0,1] op_sel_hi:[1,1]\n"
      "v_cvt_pk_bf16_f32 v133, v118, v119\n"
      "v_lshlrev_b32 v118, 16, v134\n"
      "v_and_b32 v119, 0xffff0000, v134\n"
      "v_pk_mul_f32 v[118:119], v[118:119], v[112:113] op_sel:[0,1] op_sel_hi:[1,1]\n"
      "v_cvt_pk_bf16_f32 v134, v118, v119\n"
      "v_lshlrev_b32 v118, 16, v135\n"
      "v_and_b32 v119, 0xffff0000, v135\n"
      "v_pk_mul_f32 v[118:119], v[118:119], v[112:113] op_sel:[0,1] op_sel_hi:[1,1]\n"
      "v_cvt_pk_bf16_f32 v135, v118, v119\n"
      "v_lshlrev_b32 v118, 16, v136\n"
      "v_and_b32 v119, 0xffff0000, v136\n"
      "v_pk_mul_f32 v[118:119], v[118:119], v[114:115] op_sel:[0,0] op_sel_hi:[1,0]\n"
      "v_cvt_pk_bf16_f32 v136, v118, v119\n"
      "v_lshlrev_b32 v118, 16, v137\n"
      "v_and_b32 v119, 0xffff0000, v137\n"
      "v_pk_mul_f32 v[118:119], v[118:119], v[114:115] op_sel:[0,0] op_sel_hi:[1,0]\n"
      "v_cvt_pk_bf16_f32 v137, v118, v119\n"
      "v_lshlrev_b32 v118, 16, v138\n"
      "v_and_b32 v119, 0xffff0000, v138\n"
      "v_pk_mul_f32 v[118:119], v[118:119], v[114:115] op_sel:[0,0] op_sel_hi:[1,0]\n"
      "v_cvt_pk_bf16_f32 v138, v118, v119\n"
      "v_lshlrev_b32 v118, 16, v139\n"
      "v_and_b32 v119, 0xffff0000, v139\n"
      "v_pk_mul_f32 v[118:119], v[118:119], v[114:115] op_sel:[0,0] op_sel_hi:[1,0]\n"
      "v_cvt_pk_bf16_f32 v139, v118, v119\n"
      "v_lshlrev_b32 v118, 16, v140\n"
      "v_and_b32 v119, 0xffff0000, v140\n"
      "v_pk_mul_f32 v[118:119], v[118:119], v[114:115] op_sel:[0,1] op_sel_hi:[1,1]\n"
      "v_cvt_pk_bf16_f32 v140, v118, v119\n"
      "v_lshlrev_b32 v118, 16, v141\n"
      "v_and_b32 v119, 0xffff0000, v141\n"
      "v_pk_mul_f32 v[118:119], v[118:119], v[114:115] op_sel:[0,1] op_sel_hi:[1,1]\n"
      "v_cvt_pk_bf16_f32 v141, v118, v119\n"
      "v_lshlrev_b32 v118, 16, v142\n"
      "v_and_b32 v119, 0xffff0000, v142\n"
      "v_pk_mul_f32 v[118:119], v[118:119], v[114:115] op_sel:[0,1] op_sel_hi:[1,1]\n"
      "v_cvt_pk_bf16_f32 v142, v118, v119\n"
      "v_lshlrev_b32 v118, 16, v143\n"
      "v_and_b32 v119, 0xffff0000, v143\n"
      "v_pk_mul_f32 v[118:119], v[118:119], v[114:115] op_sel:[0,1] op_sel_hi:[1,1]\n"
      "v_cvt_pk_bf16_f32 v143, v118, v119\n"
      "v_accvgpr_write_b32 a64, v128\n"
      "v_accvgpr_write_b32 a65, v129\n"
      "v_accvgpr_write_b32 a66, v130\n"
      "v_accvgpr_write_b32 a67, v131\n"
      "v_accvgpr_write_b32 a68, v132\n"
      "v_accvgpr_write_b32 a69, v133\n"
      "v_accvgpr_write_b32 a70, v134\n"
      "v_accvgpr_write_b32 a71, v135\n"
      "v_accvgpr_write_b32 a72, v136\n"
      "v_accvgpr_write_b32 a73, v137\n"
      "v_accvgpr_write_b32 a74, v138\n"
      "v_accvgpr_write_b32 a75, v139\n"
      "v_accvgpr_write_b32 a76, v140\n"
      "v_accvgpr_write_b32 a77, v141\n"
      "v_accvgpr_write_b32 a78, v142\n"
      "v_accvgpr_write_b32 a79, v143\n"
      "s_branch MR_%=\n"
      "FIX_%=:\n"
      "s_nop 7\n"
      "v_sub_f32 v224, v81, v117\n"
      "v_min_f32 v112, 0, -v224\n"
      "v_min_f32 v224, 0, v224\n"
      "v_exp_f32 v112, v112\n"
      "v_exp_f32 v224, v224\n"
      "s_nop 0\n"
      "v_mul_f32 v144, v144, v224\n"
      "v_mul_f32 v80, v80, v112\n"
      "v_mov_b32 v226, v224\n"
      "v_mov_b32 v114, v112\n"
      "s_nop 0\n"
      "v_permlane32_swap_b32 v224, v226\n"
      "v_permlane32_swap_b32 v112, v114\n"
      "v_mov_b32 v225, v224\n"
      "v_mov_b32 v113, v112\n"
      "v_mov_b32 v227, v226\n"
      "v_mov_b32 v115, v114\n"
      "v_permlane16_swap_b32 v224, v225\n"
      "v_permlane16_swap_b32 v112, v113\n"
      "v_permlane16_swap_b32 v226, v227\n"
      "v_permlane16_swap_b32 v114, v115\n"
      "v_pk_mul_f32 v[32:33], v[32:33], v[224:225] op_sel:[0,0] op_sel_hi:[1,0]\n"
      "v_pk_mul_f32 v[34:35], v[34:35], v[224:225] op_sel:[0,0] op_sel_hi:[1,0]\n"
      "v_pk_mul_f32 v[36:37], v[36:37], v[224:225] op_sel:[0,1] op_sel_hi:[1,1]\n"
      "v_pk_mul_f32 v[38:39], v[38:39], v[224:225] op_sel:[0,1] op_sel_hi:[1,1]\n"
      "v_pk_mul_f32 v[40:41], v[40:41], v[226:227] op_sel:[0,0] op_sel_hi:[1,0]\n"
      "v_pk_mul_f32 v[42:43], v[42:43], v[226:227] op_sel:[0,0] op_sel_hi:[1,0]\n"
      "v_pk_mul_f32 v[44:45], v[44:45], v[226:227] op_sel:[0,1] op_sel_hi:[1,1]\n"
      "v_pk_mul_f32 v[46:47], v[46:47], v[226:227] op_sel:[0,1] op_sel_hi:[1,1]\n"
      "v_pk_mul_f32 v[48:49], v[48:49], v[224:225] op_sel:[0,0] op_sel_hi:[1,0]\n"
      "v_pk_mul_f32 v[50:51], v[50:51], v[224:225] op_sel:[0,0] op_sel_hi:[1,0]\n"
      "v_pk_mul_f32 v[52:53], v[52:53], v[224:225] op_sel:[0,1] op_sel_hi:[1,1]\n"
      "v_pk_mul_f32 v[54:55], v[54:55], v[224:225] op_sel:[0,1] op_sel_hi:[1,1]\n"
      "v_pk_mul_f32 v[56:57], v[56:57], v[226:227] op_sel:[0,0] op_sel_hi:[1,0]\n"
      "v_pk_mul_f32 v[58:59], v[58:59], v[226:227] op_sel:[0,0] op_sel_hi:[1,0]\n"
      "v_pk_mul_f32 v[60:61], v[60:61], v[226:227] op_sel:[0,1] op_sel_hi:[1,1]\n"
      "v_pk_mul_f32 v[62:63], v[62:63], v[226:227] op_sel:[0,1] op_sel_hi:[1,1]\n"
      "v_lshlrev_b32 v118, 16, v64\n"
      "v_and_b32 v119, 0xffff0000, v64\n"
      "v_pk_mul_f32 v[118:119], v[118:119], v[112:113] op_sel:[0,0] op_sel_hi:[1,0]\n"
      "v_cvt_pk_bf16_f32 v64, v118, v119\n"
      "v_lshlrev_b32 v118, 16, v65\n"
      "v_and_b32 v119, 0xffff0000, v65\n"
      "v_pk_mul_f32 v[118:119], v[118:119], v[112:113] op_sel:[0,0] op_sel_hi:[1,0]\n"
      "v_cvt_pk_bf16_f32 v65, v118, v119\n"
      "v_lshlrev_b32 v118, 16, v66\n"
      "v_and_b32 v119, 0xffff0000, v66\n"
      "v_pk_mul_f32 v[118:119], v[118:119], v[112:113] op_sel:[0,0] op_sel_hi:[1,0]\n"
      "v_cvt_pk_bf16_f32 v66, v118, v119\n"
      "v_lshlrev_b32 v118, 16, v67\n"
      "v_and_b32 v119, 0xffff0000, v67\n"
      "v_pk_mul_f32 v[118:119], v[118:119], v[112:113] op_sel:[0,0] op_sel_hi:[1,0]\n"
      "v_cvt_pk_bf16_f32 v67, v118, v119\n"
      "v_lshlrev_b32 v118, 16, v68\n"
      "v_and_b32 v119, 0xffff0000, v68\n"
      "v_pk_mul_f32 v[118:119], v[118:119], v[112:113] op_sel:[0,1] op_sel_hi:[1,1]\n"
      "v_cvt_pk_bf16_f32 v68, v118, v119\n"
      "v_lshlrev_b32 v118, 16, v69\n"
      "v_and_b32 v119, 0xffff0000, v69\n"
      "v_pk_mul_f32 v[118:119], v[118:119], v[112:113] op_sel:[0,1] op_sel_hi:[1,1]\n"
      "v_cvt_pk_bf16_f32 v69, v118, v119\n"
      "v_lshlrev_b32 v118, 16, v70\n"
      "v_and_b32 v119, 0xffff0000, v70\n"
      "v_pk_mul_f32 v[118:119], v[118:119], v[112:113] op_sel:[0,1] op_sel_hi:[1,1]\n"
      "v_cvt_pk_bf16_f32 v70, v118, v119\n"
      "v_lshlrev_b32 v118, 16, v71\n"
      "v_and_b32 v119, 0xffff0000, v71\n"
      "v_pk_mul_f32 v[118:119], v[118:119], v[112:113] op_sel:[0,1] op_sel_hi:[1,1]\n"
      "v_cvt_pk_bf16_f32 v71, v118, v119\n"
      "v_lshlrev_b32 v118, 16, v72\n"
      "v_and_b32 v119, 0xffff0000, v72\n"
      "v_pk_mul_f32 v[118:119], v[118:119], v[114:115] op_sel:[0,0] op_sel_hi:[1,0]\n"
      "v_cvt_pk_bf16_f32 v72, v118, v119\n"
      "v_lshlrev_b32 v118, 16, v73\n"
      "v_and_b32 v119, 0xffff0000, v73\n"
      "v_pk_mul_f32 v[118:119], v[118:119], v[114:115] op_sel:[0,0] op_sel_hi:[1,0]\n"
      "v_cvt_pk_bf16_f32 v73, v118, v119\n"
      "v_lshlrev_b32 v118, 16, v74\n"
      "v_and_b32 v119, 0xffff0000, v74\n"
      "v_pk_mul_f32 v[118:119], v[118:119], v[114:115] op_sel:[0,0] op_sel_hi:[1,0]\n"
      "v_cvt_pk_bf16_f32 v74, v118, v119\n"
      "v_lshlrev_b32 v118, 16, v75\n"
      "v_and_b32 v119, 0xffff0000, v75\n"
      "v_pk_mul_f32 v[118:119], v[118:119], v[114:115] op_sel:[0,0] op_sel_hi:[1,0]\n"
      "v_cvt_pk_bf16_f32 v75, v118, v119\n"
      "v_lshlrev_b32 v118, 16, v76\n"
      "v_and_b32 v119, 0xffff0000, v76\n"
      "v_pk_mul_f32 v[118:119], v[118:119], v[114:115] op_sel:[0,1] op_sel_hi:[1,1]\n"
      "v_cvt_pk_bf16_f32 v76, v118, v119\n"
      "v_lshlrev_b32 v118, 16, v77\n"
      "v_and_b32 v119, 0xffff0000, v77\n"
      "v_pk_mul_f32 v[118:119], v[118:119], v[114:115] op_sel:[0,1] op_sel_hi:[1,1]\n"
      "v_cvt_pk_bf16_f32 v77, v118, v119\n"
      "v_lshlrev_b32 v118, 16, v78\n"
      "v_and_b32 v119, 0xffff0000, v78\n"
      "v_pk_mul_f32 v[118:119], v[118:119], v[114:115] op_sel:[0,1] op_sel_hi:[1,1]\n"
      "v_cvt_pk_bf16_f32 v78, v118, v119\n"
      "v_lshlrev_b32 v118, 16, v79\n"
      "v_and_b32 v119, 0xffff0000, v79\n"
      "v_pk_mul_f32 v[118:119], v[118:119], v[114:115] op_sel:[0,1] op_sel_hi:[1,1]\n"
      "v_cvt_pk_bf16_f32 v79, v118, v119\n"
      "v_add_f32 v224, v144, v80\n"
      "v_rcp_f32 v224, v224\n"
      "v_mfma_f32_16x16x32_bf16 v[160:163], v[86:89], v[64:67], v[32:35]\n"
      "v_mfma_f32_16x16x32_bf16 v[176:179], v[84:87], v[64:67], v[48:51]\n"
      "v_mfma_f32_16x16x32_bf16 v[164:167], v[86:89], v[68:71], v[36:39]\n"
      "v_mfma_f32_16x16x32_bf16 v[180:183], v[84:87], v[68:71], v[52:55]\n"
      "v_mfma_f32_16x16x32_bf16 v[168:171], v[86:89], v[72:75], v[40:43]\n"
      "v_mfma_f32_16x16x32_bf16 v[184:187], v[84:87], v[72:75], v[56:59]\n"
      "v_mfma_f32_16x16x32_bf16 v[172:175], v[86:89], v[76:79], v[44:47]\n"
      "v_mfma_f32_16x16x32_bf16 v[188:191], v[84:87], v[76:79], v[60:63]\n"
      "v_mov_b32 v226, v224\n"
      "s_nop 1\n"
      "s_nop 0\n"
      "s_nop 1\n"
      "v_permlane32_swap_b32 v224, v226\n"
      "s_nop 1\n"
      "v_mov_b32 v225, v224\n"
      "s_nop 1\n"
      "v_mov_b32 v227, v226\n"
      "s_nop 1\n"
      "v_permlane16_swap_b32 v224, v225\n"
      "s_nop 1\n"
      "v_permlane16_swap_b32 v226, v227\n"
      "s_nop 1\n"
      "s_branch FR_%=\n"
      ".subsection 0\n"
      : "+{s[4:19]}"(ka), "+{v128}"(pre), "+{v[160:191]}"(qq), "+{s92}"(tch), "+{v[242:249]}"(ad), "+{v[250:251]}"(ra)
      : [qo] "s"(bk * 128 * D_ * 2), [oo] "s"((bk * 128 + rh * 64) * (D_ * 2) + (2 * split + kh) * 64 + 4096),
        [fo] "s"((unsigned)WS_EPOCH + bk * 128 + 64 + split * 8), [xo] "s"((unsigned)(WS_X + split * WS_X1 + (bk * 4 + wid) * XW_BYTES)),
        [xw] "s"(xarea(wid)), [xr] "s"(xarea(wid ^ 2)),
        [so0] "s"(so0), [inc] "s"(inc), [wb] "s"(wbase), [tv] "s"(wbase + STAGE_BYTES + KIMG + split * 8192), [n1] "s"(n1),
        [qoff] "s"(Q_OFF + rh * 16384 - kh * 8192), [toff] "s"(TREE_OFF - kh * 8192),
        [neg] "s"((wid ^ (3 * split)) ? 0xff800000u : 0u),
        [lead] "s"(blk * 4 + wid * 32), [cph] "s"(C2 + (unsigned)((jj & ~3) * 8 + xg * 4) * WIN / 256u),
        [rep] "s"((jj >> 2) == 160 / 32 ? 128 + (blk & 31) * 4 : (blk - 224) * 4)
      : "v0", "v1", "v2", "v3", "v4", "v5", "v6", "v7", "v8", "v9", "v10", "v11", "v12", "v13", "v14", "v15", "v16", "v17", "v18", "v19", "v20", "v21", "v22", "v23", "v24", "v25", "v26", "v27", "v28", "v29", "v30", "v31", "v32", "v33", "v34", "v35", "v36", "v37", "v38", "v39", "v40", "v41", "v42", "v43", "v44", "v45", "v46", "v47", "v48", "v49", "v50", "v51", "v52", "v53", "v54", "v55", "v56", "v57", "v58", "v59", "v60", "v61", "v62", "v63", "v64", "v65", "v66", "v67", "v68", "v69", "v70", "v71", "v72", "v73", "v74", "v75", "v76", "v77", "v78", "v79", "v80", "v81", "v82", "v83", "v84", "v85", "v86", "v87", "v88", "v89", "v90", "v91", "v92", "v93", "v94", "v95", "v96", "v97", "v98", "v99", "v100", "v101", "v102", "v103", "v104", "v105", "v106", "v107", "v108", "v109", "v110", "v111", "v112", "v113", "v114", "v115", "v116", "v117", "v118", "v119", "v120", "v121", "v122", "v123", "v124", "v125", "v126", "v127", "v129", "v130", "v131", "v132", "v133", "v134", "v135", "v136", "v137", "v138", "v139", "v140", "v141", "v142", "v143", "v144", "v145", "v146", "v147", "v148", "v149", "v150", "v151", "v152", "v153", "v154", "v155", "v156", "v157", "v158", "v159", "v192", "v193", "v194", "v195", "v196", "v197", "v198", "v199", "v200", "v201", "v202", "v203", "v204", "v205", "v206", "v207", "v208", "v209", "v210", "v211", "v212", "v213", "v214", "v215", "v216", "v217", "v218", "v219", "v220", "v221", "v222", "v223", "v224", "v225", "v226", "v227", "v228", "v229", "v230", "v231", "v232", "v233", "v234", "v235", "v236", "v237", "v238", "v239", "v240", "v241", "a0", "a1", "a2", "a3", "a4", "a5", "a6", "a7", "a8", "a9", "a10", "a11", "a12", "a13", "a14", "a15", "a16", "a17", "a18", "a19", "a20", "a21", "a22", "a23", "a24", "a25", "a26", "a27", "a28", "a29", "a30", "a31", "a32", "a33", "a34", "a35", "a36", "a37", "a38", "a39", "a40", "a41", "a42", "a43", "a44", "a45", "a46", "a47", "a48", "a49", "a50", "a51", "a52", "a53", "a54", "a55", "a56", "a57", "a58", "a59", "a60", "a61", "a62", "a63", "a64", "a65", "a66", "a67", "a68", "a69", "a70", "a71", "a72", "a73", "a74", "a75", "a76", "a77", "a78", "a79", "a80", "a81", "a82", "a83", "a84", "a85", "a86", "a87", "a88", "a89", "a90", "a91", "a92", "a93", "a94", "a95", "a96", "a97", "a98", "a99", "a100", "a101", "a102", "a103", "a104", "a105", "a106", "a107", "a108", "a109", "a110", "a111", "a112", "a113", "a114", "a115", "a116", "a117", "a118", "a119", "a120", "a121", "a122", "a123", "a124", "a125", "a126", "a127", "a128", "a129", "a130", "a131", "a132", "a133", "a134", "a135", "a136", "a137", "a138", "a139", "a140", "a141", "a142", "a143", "a144", "a145", "a146", "a147", "a148", "a149", "a150", "a151", "a152", "a153", "a154", "a155", "a156", "a157", "a158", "a159", "a160", "a161", "a162", "a163", "a164", "a165", "a166", "a167", "a168", "a169", "a170", "a171", "a172", "a173", "a174", "a175", "a176", "a177", "a178", "a179", "a180", "a181", "a182", "a183", "a184", "a185", "a186", "a187", "a188", "a189", "a190", "a191", "a192", "a193", "a194", "a195", "a196", "a197", "a198", "a199", "a200", "a201", "a202", "a203", "a204", "a205", "a206", "a207", "a208", "a209", "a210", "a211", "a212", "a213", "a214", "a215", "a216", "a217", "a218", "a219", "a220", "a221", "a222", "a223", "a224", "a225", "a226", "a227", "a228", "a229", "a230", "a231", "s20", "s21", "s22", "s23", "s24", "s25", "s26", "s27", "s28", "s29", "s30", "s31", "s60", "s61", "s62", "s63", "s64", "s65", "s66", "s67", "s68", "s69", "s70", "s71", "s72", "s73", "s74", "s75", "s76", "s77", "s78", "s79", "s80", "s81", "s82", "s83", "s84", "s85", "s86", "s87", "s88", "s89", "s90", "s91", "s93", "s94", "s95", "s98", "s99", "s100", "s101", "vcc", "scc", "memory");
}

}

extern "C" void kernel_launch(void* const* d_in, const int* in_sizes, int n_in, void* d_out, int out_size,
                              void* d_ws, size_t ws_size, hipStream_t stream) {
  if (n_in != 6 || ws_size < WS_END) return;
  const __hip_bfloat16* q = (const __hip_bfloat16*)d_in[0];
  const __hip_bfloat16* kc = (const __hip_bfloat16*)d_in[1];
  const __hip_bfloat16* vc = (const __hip_bfloat16*)d_in[2];
  const __hip_bfloat16* kt = (const __hip_bfloat16*)d_in[3];
  const __hip_bfloat16* vt = (const __hip_bfloat16*)d_in[4];
  const __hip_bfloat16* tm = (const __hip_bfloat16*)d_in[5];
  hipLaunchKernelGGL(tree_attn_main, dim3(B_ * HKV_ * 2), dim3(256), 0, stream, q, kc, vc, kt, vt, tm,
                     (__hip_bfloat16*)d_out, (char*)d_ws);
}
```

```cpp
#include <hip/hip_runtime.h>
#include <hip/hip_bf16.h>
#include <stdint.h>

#pragma clang diagnostic ignored "-Wint-to-pointer-cast"
#pragma clang diagnostic ignored "-Winline-asm"

namespace {

constexpr int B_ = 16, HQ_ = 32, HKV_ = 8, T_ = 32, C_ = 4096, D_ = 128;
constexpr int TILE = 64;
constexpr int KIMG = TILE * D_ * 2;
constexpr int STAGE_BYTES = 2 * KIMG;
constexpr int TREE_OFF = 4 * STAGE_BYTES;
constexpr int Q_OFF = 3 * STAGE_BYTES;
constexpr int LDS_BYTES = TREE_OFF + 2 * 13312;
constexpr unsigned C2 = 299, WIN = 52;

constexpr size_t WS_EPOCH = 0;
constexpr size_t WS_REF = WS_EPOCH + 128 * 128;
constexpr size_t WS_X = WS_REF + 16384, WS_X1 = 4 << 20;
constexpr size_t XW_BYTES = 4096 + 1024;
constexpr size_t WS_END = 0x680000 + WS_X;
static_assert(WS_X1 + WS_X + 128 * 4 * XW_BYTES <= WS_END, "");

typedef float    f32x4  __attribute__((ext_vector_type(4)));
typedef unsigned long long u64x8 __attribute__((ext_vector_type(8)));
typedef unsigned u32x32 __attribute__((ext_vector_type(32)));
typedef unsigned u32x8  __attribute__((ext_vector_type(8)));
typedef unsigned u32x4  __attribute__((ext_vector_type(4)));
typedef unsigned u32x2  __attribute__((ext_vector_type(2)));
typedef __attribute__((address_space(3))) f32x4 lds_f32x4;
typedef __attribute__((address_space(1))) unsigned long long gu64;

__global__ __launch_bounds__(256, 1)
void tree_attn_main(const __hip_bfloat16*, const __hip_bfloat16*, const __hip_bfloat16*, const __hip_bfloat16*,
                    const __hip_bfloat16*, const __hip_bfloat16*, __hip_bfloat16*, char*) {
  __shared__ __attribute__((aligned(1024))) char smem[LDS_BYTES];
  unsigned blk = blockIdx.x;
  u64x8 ka; u32x32 qq; unsigned pre, tch, voffK; int tid = threadIdx.x;
  asm volatile(
      "s_load_dwordx16 s[4:19], %[kp], 0x0\n"
      "s_getpc_b64 s[84:85]\n"
      "v_lshlrev_b32 v112, 4, %[tid]\n"
      "s_lshl_b32 s69, %[blk], 7\n"
      "s_and_b32 s69, s69, 0x7c00\n"
      "v_lshl_add_u32 v120, %[tid], 2, s69\n"
      "global_load_dword v128, v120, s[84:85]\n"
      "v_readfirstlane_b32 s77, %[tid]\n"
      "v_lshrrev_b32 %[vk], 4, %[tid]\n"
      "s_and_b32 s78, %[blk], 7\n"
      "v_xor_b32 %[vk], %[vk], %[tid]\n"
      "s_lshr_b32 s74, %[blk], 4\n"
      "v_and_b32 %[vk], 7, %[vk]\n"
      "s_lshl4_add_u32 s78, s78, s74\n"
      "v_lshrrev_b32 v120, 3, %[tid]\n"
      "s_lshl_b32 s78, s78, 15\n"
      "v_lshlrev_b32 %[vk], 4, %[vk]\n"
      "s_lshl_b32 s77, s77, 5\n"
      "v_lshl_or_b32 %[vk], v120, 8, %[vk]\n"
      "s_bfm_b32 s66, 1, 22\n"
      "s_bfm_b32 s67, 1, 17\n"
      "v_add_u32 v120, 0x2000, %[vk]\n"
      "s_add_u32 s74, s78, 0x4000\n"
      "s_lshl_b32 s76, s78, 5\n"
      "s_add_u32 m0, s77, 0x20000\n"
      "s_mov_b32 s86, 0x100000\n"
      "s_mov_b32 s87, 0x20000\n"
      "s_lshr_b32 s75, s78, 2\n"
      "s_waitcnt lgkmcnt(0)\n"
      "s_mov_b32 s64, s4\n"
      "s_and_b32 s65, s5, 0xffff\n"
      "buffer_load_dwordx4 v[160:163], %[vk], s[64:67], s78 offen nt\n"
      "buffer_load_dwordx4 v[164:167], %[vk], s[64:67], s78 offen offset:128 nt\n"
      "buffer_load_dwordx4 v[168:171], v120, s[64:67], s78 offen nt\n"
      "buffer_load_dwordx4 v[172:175], v120, s[64:67], s78 offen offset:128 nt\n"
      "s_load_dword s92, s[6:7], s76\n"
      "s_load_dword s92, s[8:9], s76\n"
      "buffer_load_dwordx4 v[176:179], %[vk], s[64:67], s74 offen nt\n"
      "buffer_load_dwordx4 v[180:183], %[vk], s[64:67], s74 offen offset:128 nt\n"
      "buffer_load_dwordx4 v[184:187], v120, s[64:67], s74 offen nt\n"
      "buffer_load_dwordx4 v[188:191], v120, s[64:67], s74 offen offset:128 nt\n"
      "s_mov_b32 s84, s10\n"
      "s_and_b32 s85, s11, 0xffff\n"
      "s_lshr_b32 s76, s78, 18\n"
      "s_lshl_b32 s76, s76, 11\n"
      "s_lshr_b32 s69, s77, 1\n"
      "buffer_load_dwordx4 %[vk], s[84:87], s75 offen nt lds\n"
      "s_add_u32 m0, s77, 131968\n"
      "s_cmp_gt_u32 s77, 2048\n"
      "buffer_load_dwordx4 %[vk], s[84:87], s75 offen offset:128 nt lds\n"
      "s_cbranch_scc1 NOMASK_%=\n"
      "s_add_u32 m0, s69, 140288\n"
      "s_mov_b32 s64, s14\n"
      "s_and_b32 s65, s15, 0xffff\n"
      "s_mov_b32 s66, 0x8000\n"
      "buffer_load_dwordx4 v112, s[64:67], s76 offen lds\n"
      "NOMASK_%=:\n"
      "s_mov_b32 s64, s6\n"
      "s_and_b32 s65, s7, 0xffff\n"
      "s_bfm_b32 s66, 1, 27\n"
      "s_lshl_b32 s76, s78, 5\n"
      "s_bfe_u32 s74, %[blk], 0x10003\n"
      "s_lshl_b32 s74, s74, 15\n"
      "s_add_u32 s76, s76, s74\n"
      "s_bfe_u32 s74, %[blk], 0x10004\n"
      "s_lshl_b32 s74, s74, 14\n"
      "s_add_u32 s76, s76, s74\n"
      "s_add_u32 m0, s77, 0\n"
      "s_nop 0\n"
      "buffer_load_dwordx4 %[vk], s[64:67], s76 offen nt lds\n"
      "s_add_u32 m0, s77, 896\n"
      "s_nop 0\n"
      "buffer_load_dwordx4 %[vk], s[64:67], s76 offen offset:128 nt lds\n"
      "s_add_u32 m0, s77, 8192\n"
      "s_nop 0\n"
      "buffer_load_dwordx4 v120, s[64:67], s76 offen nt lds\n"
      : "=&{s[4:19]}"(ka), "=&{v128}"(pre), "=&{v[160:191]}"(qq), "=&{s92}"(tch), [vk] "=&v"(voffK), [tid] "+v"(tid)
      : [kp] "s"(__builtin_amdgcn_kernarg_segment_ptr()), [blk] "s"(blk)
      : "v112", "v120", "s64", "s65", "s66", "s67", "s69", "s70", "s71", "s74", "s75", "s76", "s77", "s78", "s84", "s85", "s86", "s87", "s96", "s97", "scc", "memory");
  asm volatile("" : "+s"(blk));
  const int lane = tid & 63;
  const int wid = __builtin_amdgcn_readfirstlane(tid >> 6);
  const unsigned wbase = wid * 2048;
  const int rh = wid & 1, kh = wid >> 1;
  const int xg = blk & 7, jj = blk >> 3, bk = xg * 16 + (jj >> 1), split = jj & 1;
  const int c16 = lane & 15, g = lane >> 4;
  const unsigned lds0 = (unsigned)(uintptr_t)smem;

  const unsigned voffV = (unsigned)((8 * wid + (lane >> 3)) * 256 + (((lane & 7) ^ (g << 1)) << 4));
  const unsigned so0 = (unsigned)(bk * C_ + split * 2 * TILE) * (D_ * 2) + (bk & 1) * KIMG, inc = KIMG - 2 * (bk & 1) * KIMG;
#pragma unroll
  for (int j = 0; j < 4; ++j) *(lds_f32x4*)(lds0 + KIMG + 4096 * j + tid * 16) = f32x4{0.f, 0.f, 0.f, 0.f};
#pragma unroll
  for (int j = 0; j < 2; ++j) *(lds_f32x4*)(lds0 + STAGE_BYTES + KIMG + (1 - split) * 8192 + 4096 * j + tid * 16) = f32x4{0.f, 0.f, 0.f, 0.f};
  u32x8 ad;
#pragma unroll
  for (int j = 0; j < 2; ++j)
    ad[j] = lds0 + (unsigned)(2048 * (c16 >> 3) + 128 * (c16 & 7) + (((4 * j + g) ^ (c16 >> 1)) << 4) + kh * 8192);
  {
    const int q4 = c16 >> 2, p = lane & 3;
#pragma unroll
    for (int j = 0; j < 4; ++j)
      ad[2 + j] = lds0 + (unsigned)(2048 * (g >> 1) + 128 * (4 * (g & 1) + q4) + kh * 8192 + KIMG +
                                    (((2 * (j ^ (2 * kh)) + (p >> 1)) ^ ((2 * (g & 1) + (q4 >> 1)) << 1)) << 4) + 8 * (p & 1));
  }
  ad[6] = voffK; ad[7] = voffV + 128 * split;
  u32x2 ra = {(unsigned)(lane < 4 ? WS_REF + lane * 512 : 1 << 26), (unsigned)(WS_REF + (jj & (4 - 1)) * 512 + (lane & 7) * 4 + (wid << 26))};
  auto xarea = [&](int w) { return lds0 + ((0x8d802000u >> (8 * w)) & 255u) * 1024; };
  const unsigned pg = jj >> 2, px = pg - (pg > 160 / 32) + xg * 5;
  const unsigned n1 = 4 + max(px - 6 * (px * 43 >> 8), ((0x70000000u | 6u << 160 / 8) >> 4 * pg) & 7);
  asm volatile(
      "s_memrealtime s[82:83]\n"
      "s_mov_b32 s60, %[so0]\n"
      "s_mov_b32 s62, %[wb]\n"
      "v_add_u32 v120, 0x2000, v248\n"
      "s_and_b32 s80, %[fo], 0xffffff80\n"
      "v_mov_b32 v240, s80\n"
      "s_mov_b32 s22, 0x8000000\n"
      "s_mov_b32 s23, 0x20000\n"
      "s_mov_b32 s20, s6\n"
      "s_and_b32 s21, s7, 0xffff\n"
      "s_nop 0\n"
      "v_mov_b64 v[216:217], 0\n"
      "v_mov_b64 v[218:219], 0\n"
      "s_mov_b32 s86, 0x100000\n"
      "s_mov_b32 s87, 0x20000\n"
      "v_mfma_f32_32x32x16_bf16 v[32:47], v[216:219], v[216:219], 0\n"
      "s_lshr_b32 s75, %[qo], 2\n"
      "s_mov_b32 s84, s12\n"
      "s_and_b32 s85, s13, 0xffff\n"
      "v_mfma_f32_32x32x16_bf16 v[48:63], v[216:219], v[216:219], 0\n"
      "s_nop 0\n"
      "v_xor_b32 v121, 0x80, v249\n"
      "s_sub_u32 s78, 0x10000, %[inc]\n"
      "s_mov_b32 s94, 0x3e0293ee\n"
      "v_mfma_f32_32x32x16_bf16 v[64:79], v[216:219], v[216:219], 0\n"
      "s_mov_b32 s95, 0x3e0293ee\n"
      "s_mov_b32 s74, 0xffff0000\n"
      "s_mov_b32 s76, 0xceabfb8f\n"
      "v_mfma_f32_32x32x16_bf16 v[80:95], v[216:219], v[216:219], 0\n"
      "v_mbcnt_lo_u32_b32 v112, -1, 0\n"
      "s_nop 0\n"
      "v_mbcnt_hi_u32_b32 v112, -1, v112\n"
      "s_add_u32 s69, %[wb], 0x18000\n"
      "v_mfma_f32_32x32x16_bf16 v[192:207], v[216:219], v[216:219], 0\n"
      "v_lshl_add_u32 v226, v112, 4, s69\n"
      "v_lshrrev_b32 v115, 4, v112\n"
      "v_and_b32 v112, 15, v112\n"
      "v_mfma_f32_32x32x16_bf16 a[128:143], v[216:219], v[216:219], 0\n"
      "v_lshlrev_b32 v115, 3, v115\n"
      "v_lshl_add_u32 v115, v112, 6, v115\n"
      "s_add_u32 m0, s62, 9088\n"
      "s_nop 0\n"
      "buffer_load_dwordx4 v120, s[20:23], s60 offen offset:128 nt lds\n"
      "v_add_u32 v115, 140288, v115\n"
      "v_mfma_f32_32x32x16_bf16 a[144:159], v[216:219], v[216:219], 0\n"
      "v_add_u32 v118, %[qoff], v242\n"
      "v_add_u32 v224, %[toff], v242\n"
      "v_add_u32 v119, %[qoff], v243\n"
      "v_mfma_f32_32x32x16_bf16 a[160:175], v[216:219], v[216:219], 0\n"
      "v_add_u32 v225, %[toff], v243\n"
      "v_mfma_f32_32x32x16_bf16 a[176:191], v[216:219], v[216:219], 0\n"
      "s_add_u32 s62, s62, 0x8000\n"
      "s_and_b32 s62, s62, 0x1ffff\n"
      "s_add_u32 s60, s60, %[inc]\n"
      "s_waitcnt vmcnt(13)\n"
      "v_lshlrev_b32 v232, 16, v160\n"
      "v_and_b32 v233, s74, v160\n"
      "v_lshlrev_b32 v234, 16, v161\n"
      "v_and_b32 v235, s74, v161\n"
      "v_lshlrev_b32 v236, 16, v162\n"
      "v_and_b32 v237, s74, v162\n"
      "v_lshlrev_b32 v238, 16, v163\n"
      "v_and_b32 v239, s74, v163\n"
      "v_pk_mul_f32 v[232:233], v[232:233], s[94:95]\n"
      "v_pk_mul_f32 v[234:235], v[234:235], s[94:95]\n"
      "v_pk_mul_f32 v[236:237], v[236:237], s[94:95]\n"
      "v_pk_mul_f32 v[238:239], v[238:239], s[94:95]\n"
      "v_cvt_pk_bf16_f32 v160, v232, v233\n"
      "v_cvt_pk_bf16_f32 v161, v234, v235\n"
      "v_cvt_pk_bf16_f32 v162, v236, v237\n"
      "v_cvt_pk_bf16_f32 v163, v238, v239\n"
      "ds_write_b128 v226, v[160:163] offset:0\n"
      "s_waitcnt vmcnt(12)\n"
      "v_lshlrev_b32 v232, 16, v164\n"
      "v_and_b32 v233, s74, v164\n"
      "v_lshlrev_b32 v234, 16, v165\n"
      "v_and_b32 v235, s74, v165\n"
      "v_lshlrev_b32 v236, 16, v166\n"
      "v_and_b32 v237, s74, v166\n"
      "v_lshlrev_b32 v238, 16, v167\n"
      "v_and_b32 v239, s74, v167\n"
      "v_pk_mul_f32 v[232:233], v[232:233], s[94:95]\n"
      "v_pk_mul_f32 v[234:235], v[234:235], s[94:95]\n"
      "v_pk_mul_f32 v[236:237], v[236:237], s[94:95]\n"
      "v_pk_mul_f32 v[238:239], v[238:239], s[94:95]\n"
      "v_cvt_pk_bf16_f32 v164, v232, v233\n"
      "v_cvt_pk_bf16_f32 v165, v234, v235\n"
      "v_cvt_pk_bf16_f32 v166, v236, v237\n"
      "v_cvt_pk_bf16_f32 v167, v238, v239\n"
      "ds_write_b128 v226, v[164:167] offset:1024\n"
      "s_waitcnt vmcnt(11)\n"
      "v_lshlrev_b32 v232, 16, v168\n"
      "v_and_b32 v233, s74, v168\n"
      "v_lshlrev_b32 v234, 16, v169\n"
      "v_and_b32 v235, s74, v169\n"
      "v_lshlrev_b32 v236, 16, v170\n"
      "v_and_b32 v237, s74, v170\n"
      "v_lshlrev_b32 v238, 16, v171\n"
      "v_and_b32 v239, s74, v171\n"
      "v_pk_mul_f32 v[232:233], v[232:233], s[94:95]\n"
      "v_pk_mul_f32 v[234:235], v[234:235], s[94:95]\n"
      "v_pk_mul_f32 v[236:237], v[236:237], s[94:95]\n"
      "v_pk_mul_f32 v[238:239], v[238:239], s[94:95]\n"
      "v_cvt_pk_bf16_f32 v168, v232, v233\n"
      "v_cvt_pk_bf16_f32 v169, v234, v235\n"
      "v_cvt_pk_bf16_f32 v170, v236, v237\n"
      "v_cvt_pk_bf16_f32 v171, v238, v239\n"
      "ds_write_b128 v226, v[168:171] offset:8192\n"
      "s_add_u32 m0, s62, 0\n"
      "s_nop 0\n"
      "buffer_load_dwordx4 v248, s[20:23], s60 offen nt lds\n"
      "s_waitcnt vmcnt(11)\n"
      "v_lshlrev_b32 v232, 16, v172\n"
      "v_and_b32 v233, s74, v172\n"
      "v_lshlrev_b32 v234, 16, v173\n"
      "v_and_b32 v235, s74, v173\n"
      "v_lshlrev_b32 v236, 16, v174\n"
      "v_and_b32 v237, s74, v174\n"
      "v_lshlrev_b32 v238, 16, v175\n"
      "v_and_b32 v239, s74, v175\n"
      "v_pk_mul_f32 v[232:233], v[232:233], s[94:95]\n"
      "v_pk_mul_f32 v[234:235], v[234:235], s[94:95]\n"
      "v_pk_mul_f32 v[236:237], v[236:237], s[94:95]\n"
      "v_pk_mul_f32 v[238:239], v[238:239], s[94:95]\n"
      "v_cvt_pk_bf16_f32 v172, v232, v233\n"
      "v_cvt_pk_bf16_f32 v173, v234, v235\n"
      "v_cvt_pk_bf16_f32 v174, v236, v237\n"
      "v_cvt_pk_bf16_f32 v175, v238, v239\n"
      "ds_write_b128 v226, v[172:175] offset:9216\n"
      "s_add_u32 m0, s62, 896\n"
      "s_nop 0\n"
      "buffer_load_dwordx4 v248, s[20:23], s60 offen offset:128 nt lds\n"
      "s_waitcnt vmcnt(11)\n"
      "v_lshlrev_b32 v232, 16, v176\n"
      "v_and_b32 v233, s74, v176\n"
      "v_lshlrev_b32 v234, 16, v177\n"
      "v_and_b32 v235, s74, v177\n"
      "v_lshlrev_b32 v236, 16, v178\n"
      "v_and_b32 v237, s74, v178\n"
      "v_lshlrev_b32 v238, 16, v179\n"
      "v_and_b32 v239, s74, v179\n"
      "v_pk_mul_f32 v[232:233], v[232:233], s[94:95]\n"
      "v_pk_mul_f32 v[234:235], v[234:235], s[94:95]\n"
      "v_pk_mul_f32 v[236:237], v[236:237], s[94:95]\n"
      "v_pk_mul_f32 v[238:239], v[238:239], s[94:95]\n"
      "v_cvt_pk_bf16_f32 v176, v232, v233\n"
      "v_cvt_pk_bf16_f32 v177, v234, v235\n"
      "v_cvt_pk_bf16_f32 v178, v236, v237\n"
      "v_cvt_pk_bf16_f32 v179, v238, v239\n"
      "ds_write_b128 v226, v[176:179] offset:16384\n"
      "s_add_u32 m0, s62, 8192\n"
      "s_nop 0\n"
      "buffer_load_dwordx4 v120, s[20:23], s60 offen nt lds\n"
      "s_waitcnt vmcnt(11)\n"
      "v_lshlrev_b32 v232, 16, v180\n"
      "v_and_b32 v233, s74, v180\n"
      "v_lshlrev_b32 v234, 16, v181\n"
      "v_and_b32 v235, s74, v181\n"
      "v_lshlrev_b32 v236, 16, v182\n"
      "v_and_b32 v237, s74, v182\n"
      "v_lshlrev_b32 v238, 16, v183\n"
      "v_and_b32 v239, s74, v183\n"
      "v_pk_mul_f32 v[232:233], v[232:233], s[94:95]\n"
      "v_pk_mul_f32 v[234:235], v[234:235], s[94:95]\n"
      "v_pk_mul_f32 v[236:237], v[236:237], s[94:95]\n"
      "v_pk_mul_f32 v[238:239], v[238:239], s[94:95]\n"
      "v_cvt_pk_bf16_f32 v180, v232, v233\n"
      "v_cvt_pk_bf16_f32 v181, v234, v235\n"
      "v_cvt_pk_bf16_f32 v182, v236, v237\n"
      "v_cvt_pk_bf16_f32 v183, v238, v239\n"
      "ds_write_b128 v226, v[180:183] offset:17408\n"
      "s_add_u32 m0, s62, 9088\n"
      "s_nop 0\n"
      "buffer_load_dwordx4 v120, s[20:23], s60 offen offset:128 nt lds\n"
      "s_waitcnt vmcnt(11)\n"
      "v_lshlrev_b32 v232, 16, v184\n"
      "v_and_b32 v233, s74, v184\n"
      "v_lshlrev_b32 v234, 16, v185\n"
      "v_and_b32 v235, s74, v185\n"
      "v_lshlrev_b32 v236, 16, v186\n"
      "v_and_b32 v237, s74, v186\n"
      "v_lshlrev_b32 v238, 16, v187\n"
      "v_and_b32 v239, s74, v187\n"
      "v_pk_mul_f32 v[232:233], v[232:233], s[94:95]\n"
      "v_pk_mul_f32 v[234:235], v[234:235], s[94:95]\n"
      "v_pk_mul_f32 v[236:237], v[236:237], s[94:95]\n"
      "v_pk_mul_f32 v[238:239], v[238:239], s[94:95]\n"
      "v_cvt_pk_bf16_f32 v184, v232, v233\n"
      "v_cvt_pk_bf16_f32 v185, v234, v235\n"
      "v_cvt_pk_bf16_f32 v186, v236, v237\n"
      "v_cvt_pk_bf16_f32 v187, v238, v239\n"
      "ds_write_b128 v226, v[184:187] offset:24576\n"
      "s_add_u32 m0, %[tv], 0\n"
      "s_nop 0\n"
      "buffer_load_dwordx4 v249, s[84:87], s75 offen nt lds\n"
      "s_add_u32 m0, %[tv], 1024\n"
      "s_nop 0\n"
      "buffer_load_dwordx4 v121, s[84:87], s75 offen nt lds\n"
      "s_waitcnt vmcnt(12)\n"
      "v_lshlrev_b32 v232, 16, v188\n"
      "v_and_b32 v233, s74, v188\n"
      "v_lshlrev_b32 v234, 16, v189\n"
      "v_and_b32 v235, s74, v189\n"
      "v_lshlrev_b32 v236, 16, v190\n"
      "v_and_b32 v237, s74, v190\n"
      "v_lshlrev_b32 v238, 16, v191\n"
      "v_and_b32 v239, s74, v191\n"
      "v_pk_mul_f32 v[232:233], v[232:233], s[94:95]\n"
      "v_pk_mul_f32 v[234:235], v[234:235], s[94:95]\n"
      "v_pk_mul_f32 v[236:237], v[236:237], s[94:95]\n"
      "v_pk_mul_f32 v[238:239], v[238:239], s[94:95]\n"
      "v_cvt_pk_bf16_f32 v188, v232, v233\n"
      "v_cvt_pk_bf16_f32 v189, v234, v235\n"
      "v_cvt_pk_bf16_f32 v190, v236, v237\n"
      "v_cvt_pk_bf16_f32 v191, v238, v239\n"
      "ds_write_b128 v226, v[188:191] offset:25600\n"
      "s_add_u32 s62, s62, 0x8000\n"
      "s_and_b32 s62, s62, 0x1ffff\n"
      "s_add_u32 s60, s60, s78\n"
      "s_waitcnt vmcnt(10) lgkmcnt(0)\n"
      "s_barrier\n"
      "s_memrealtime s[68:69]\n"
      "ds_read_b64 v[208:209], v115 offset:0\n"
      "ds_read_b64 v[210:211], v115 offset:32\n"
      "ds_read_b64 v[212:213], v115 offset:1024\n"
      "ds_read_b64 v[214:215], v115 offset:1056\n"
      "ds_read_b128 a[96:99], v224 offset:0\n"
      "ds_read_b128 a[100:103], v224 offset:4096\n"
      "ds_read_b128 a[0:3], v118 offset:0\n"
      "ds_read_b128 a[4:7], v118 offset:4096\n"
      "ds_read_b128 a[8:11], v118 offset:8192\n"
      "ds_read_b128 a[12:15], v118 offset:12288\n"
      "ds_read_b128 a[104:107], v225 offset:0\n"
      "ds_read_b128 a[108:111], v225 offset:4096\n"
      "global_load_dwordx2 v[240:241], v240, s[18:19] sc1\n"
      "s_mov_b32 s28, s18\n"
      "s_and_b32 s29, s19, 0xffff\n"
      "s_mov_b32 s30, 0x688000\n"
      "s_mov_b32 s31, 0x20000\n"
      "s_cmp_lt_u32 %[rep], 128\n"
      "s_cselect_b32 s89, 0x2ffe, 0\n"
      "s_or_b32 s89, s89, 0x2aae\n"
      "s_waitcnt lgkmcnt(8)\n"
      "ds_read_b128 a[16:19], v119 offset:0\n"
      "ds_read_b128 a[20:23], v119 offset:4096\n"
      "ds_read_b128 a[24:27], v119 offset:8192\n"
      "ds_read_b128 a[28:31], v119 offset:12288\n"
      "v_lshlrev_b32 v96, 16, v208\n"
      "v_and_b32 v97, s74, v208\n"
      "v_lshlrev_b32 v98, 16, v209\n"
      "v_and_b32 v99, s74, v209\n"
      "v_lshlrev_b32 v100, 16, v210\n"
      "v_and_b32 v101, s74, v210\n"
      "v_lshlrev_b32 v102, 16, v211\n"
      "v_and_b32 v103, s74, v211\n"
      "v_lshlrev_b32 v104, 16, v212\n"
      "v_and_b32 v105, s74, v212\n"
      "v_lshlrev_b32 v106, 16, v213\n"
      "v_and_b32 v107, s74, v213\n"
      "v_lshlrev_b32 v108, 16, v214\n"
      "v_and_b32 v109, s74, v214\n"
      "v_lshlrev_b32 v110, 16, v215\n"
      "v_and_b32 v111, s74, v215\n"
      "v_fma_f32 v96, -v96, s76, s76\n"
      "v_fma_f32 v97, -v97, s76, s76\n"
      "v_fma_f32 v98, -v98, s76, s76\n"
      "v_fma_f32 v99, -v99, s76, s76\n"
      "v_fma_f32 v100, -v100, s76, s76\n"
      "v_fma_f32 v101, -v101, s76, s76\n"
      "v_fma_f32 v102, -v102, s76, s76\n"
      "v_fma_f32 v103, -v103, s76, s76\n"
      "v_fma_f32 v104, -v104, s76, s76\n"
      "v_fma_f32 v105, -v105, s76, s76\n"
      "v_fma_f32 v106, -v106, s76, s76\n"
      "v_fma_f32 v107, -v107, s76, s76\n"
      "v_fma_f32 v108, -v108, s76, s76\n"
      "v_fma_f32 v109, -v109, s76, s76\n"
      "v_fma_f32 v110, -v110, s76, s76\n"
      "v_fma_f32 v111, -v111, s76, s76\n"
      "s_waitcnt lgkmcnt(0)\n"
      "ds_read_b128 a[112:115], v224 offset:1024\n"
      "ds_read_b128 a[116:119], v224 offset:5120\n"
      "ds_read_b128 a[32:35], v118 offset:1024\n"
      "ds_read_b128 a[36:39], v118 offset:5120\n"
      "ds_read_b128 a[40:43], v118 offset:9216\n"
      "ds_read_b128 a[44:47], v118 offset:13312\n"
      "ds_read_b128 a[120:123], v225 offset:1024\n"
      "ds_read_b128 a[124:127], v225 offset:5120\n"
      "ds_read_b128 a[48:51], v119 offset:1024\n"
      "ds_read_b128 a[52:55], v119 offset:5120\n"
      "ds_read_b128 a[56:59], v119 offset:9216\n"
      "ds_read_b128 a[60:63], v119 offset:13312\n"
      "s_cmp_lt_u32 %[lead], 32\n"
      "s_cbranch_scc0 NOLEAD_%=\n"
      "v_mov_b32 v115, s68\n"
      "buffer_store_dword v115, v250, s[28:31], %[lead] offen sc1\n"
      "NOLEAD_%=:\n"
      "v_mfma_f32_16x16x32_bf16 v[160:163], a[96:99], a[0:3], v[96:99]\n"
      "s_mov_b32 s83, 0\n"
      "v_add_u32 v122, 0x2000, v249\n"
      "v_add_u32 v123, 0x2000, v121\n"
      "v_mfma_f32_16x16x32_bf16 v[168:171], a[96:99], a[4:7], v[104:107]\n"
      "s_add_u32 m0, s62, 0\n"
      "s_sub_u32 s67, s68, s82\n"
      "v_mov_b32 v113, 0x3f803f80\n"
      "v_accvgpr_write_b32 a192, 0\n"
      "v_mfma_f32_16x16x32_bf16 v[176:179], a[96:99], a[8:11], v[96:99]\n"
      "s_min_u32 s67, s67, 140\n"
      "buffer_load_dwordx4 v248, s[20:23], s60 offen nt lds\n"
      "v_cmp_eq_u32 vcc, 0, v112\n"
      "v_mfma_f32_16x16x32_bf16 v[184:187], a[96:99], a[12:15], v[104:107]\n"
      "s_add_u32 m0, s62, 896\n"
      "s_add_u32 s66, s82, s67\n"
      "v_mfma_f32_16x16x32_bf16 v[164:167], a[100:103], a[0:3], v[100:103]\n"
      "s_add_u32 s66, s66, 70\n"
      "buffer_load_dwordx4 v248, s[20:23], s60 offen offset:128 nt lds\n"
      "v_cndmask_b32 v114, 0, v113, vcc\n"
      "v_mfma_f32_16x16x32_bf16 v[172:175], a[100:103], a[4:7], v[108:111]\n"
      "s_add_u32 m0, s62, 8192\n"
      "s_sub_u32 s71, s68, 400\n"
      "v_accvgpr_write_b32 a196, v114\n"
      "v_accvgpr_write_b32 a197, v114\n"
      "v_mfma_f32_16x16x32_bf16 v[180:183], a[100:103], a[8:11], v[100:103]\n"
      "s_mov_b32 s68, s66\n"
      "buffer_load_dwordx4 v120, s[20:23], s60 offen nt lds\n"
      "v_accvgpr_write_b32 a198, v114\n"
      "v_accvgpr_write_b32 a199, v114\n"
      "v_mfma_f32_16x16x32_bf16 v[188:191], a[100:103], a[12:15], v[108:111]\n"
      "s_add_u32 m0, s62, 9088\n"
      "s_mov_b32 s82, s66\n"
      "v_cmp_eq_u32 vcc, 4, v112\n"
      "v_mfma_f32_16x16x32_bf16 v[160:163], a[104:107], a[16:19], v[160:163]\n"
      "s_add_u32 s62, s62, 0x8000\n"
      "buffer_load_dwordx4 v120, s[20:23], s60 offen offset:128 nt lds\n"
      "s_mov_b32 s61, %[so0]\n"
      "v_mfma_f32_16x16x32_bf16 v[168:171], a[104:107], a[20:23], v[168:171]\n"
      "s_and_b32 s62, s62, 0x1ffff\n"
      "s_add_u32 s77, %[wb], 0x10000\n"
      "v_cndmask_b32 v114, 0, v113, vcc\n"
      "v_mfma_f32_16x16x32_bf16 v[176:179], a[104:107], a[24:27], v[176:179]\n"
      "s_add_u32 s60, s60, %[inc]\n"
      "s_mov_b32 s70, 300\n"
      "v_accvgpr_write_b32 a200, v114\n"
      "v_accvgpr_write_b32 a201, v114\n"
      "v_mfma_f32_16x16x32_bf16 v[184:187], a[104:107], a[28:31], v[184:187]\n"
      "s_mov_b32 s80, 0x12a\n"
      "v_accvgpr_write_b32 a202, v114\n"
      "v_accvgpr_write_b32 a203, v114\n"
      "v_mfma_f32_16x16x32_bf16 v[164:167], a[108:111], a[16:19], v[164:167]\n"
      "s_mov_b32 s81, 0x0\n"
      "v_cmp_eq_u32 vcc, 8, v112\n"
      "v_mfma_f32_16x16x32_bf16 v[172:175], a[108:111], a[20:23], v[172:175]\n"
      "s_mov_b32 s101, 1000\n"
      "v_mfma_f32_16x16x32_bf16 v[180:183], a[108:111], a[24:27], v[180:183]\n"
      "s_mov_b32 s98, 110\n"
      "v_cndmask_b32 v114, 0, v113, vcc\n"
      "v_mfma_f32_16x16x32_bf16 v[188:191], a[108:111], a[28:31], v[188:191]\n"
      "s_waitcnt lgkmcnt(0)\n"
      "s_mov_b32 s93, 28\n"
      "v_accvgpr_write_b32 a204, v114\n"
      "v_accvgpr_write_b32 a205, v114\n"
      "v_mfma_f32_16x16x32_bf16 v[160:163], a[112:115], a[32:35], v[160:163]\n"
      "s_mov_b32 s99, 27\n"
      "v_accvgpr_write_b32 a206, v114\n"
      "v_accvgpr_write_b32 a207, v114\n"
      "v_mfma_f32_16x16x32_bf16 v[168:171], a[112:115], a[36:39], v[168:171]\n"
      "s_mov_b32 s100, 285\n"
      "v_cmp_eq_u32 vcc, 12, v112\n"
      "v_mfma_f32_16x16x32_bf16 v[176:179], a[112:115], a[40:43], v[176:179]\n"
      "s_lshl_b32 s91, %[wb], 18\n"
      "v_mfma_f32_16x16x32_bf16 v[184:187], a[112:115], a[44:47], v[184:187]\n"
      "s_sub_u32 s91, 0x10000000, s91\n"
      "v_cndmask_b32 v114, 0, v113, vcc\n"
      "v_mfma_f32_16x16x32_bf16 v[164:167], a[116:119], a[32:35], v[164:167]\n"
      "s_mov_b32 s86, 110\n"
      "v_accvgpr_write_b32 a208, v114\n"
      "v_accvgpr_write_b32 a209, v114\n"
      "v_mfma_f32_16x16x32_bf16 v[172:175], a[116:119], a[36:39], v[172:175]\n"
      "s_mov_b32 s87, 0\n"
      "v_accvgpr_write_b32 a210, v114\n"
      "v_accvgpr_write_b32 a211, v114\n"
      "s_waitcnt vmcnt(11)\n"
      "s_barrier\n"
      "v_mfma_f32_16x16x32_bf16 v[180:183], a[116:119], a[40:43], v[180:183]\n"
      "s_mov_b32 s26, 0x8000000\n"
      "ds_read_b128 a[64:67], v242 offset:0\n"
      "v_mfma_f32_16x16x32_bf16 v[188:191], a[116:119], a[44:47], v[188:191]\n"
      "s_mov_b32 s27, 0x20000\n"
      "ds_read_b128 a[68:71], v242 offset:4096\n"
      "v_mfma_f32_16x16x32_bf16 v[160:163], a[120:123], a[48:51], v[160:163]\n"
      "s_mov_b32 s24, s8\n"
      "ds_read_b128 a[72:75], v243 offset:0\n"
      "v_mfma_f32_16x16x32_bf16 v[168:171], a[120:123], a[52:55], v[168:171]\n"
      "s_and_b32 s25, s9, 0xffff\n"
      "ds_read_b128 a[76:79], v243 offset:4096\n"
      "v_mfma_f32_16x16x32_bf16 v[176:179], a[120:123], a[56:59], v[176:179]\n"
      "ds_read_b128 a[80:83], v242 offset:1024\n"
      "v_mfma_f32_16x16x32_bf16 v[184:187], a[120:123], a[60:63], v[184:187]\n"
      "ds_read_b128 a[84:87], v242 offset:5120\n"
      "v_mfma_f32_16x16x32_bf16 v[164:167], a[124:127], a[48:51], v[164:167]\n"
      "ds_read_b128 a[88:91], v243 offset:1024\n"
      "v_mfma_f32_16x16x32_bf16 v[172:175], a[124:127], a[52:55], v[172:175]\n"
      "ds_read_b128 a[92:95], v243 offset:5120\n"
      "v_mfma_f32_16x16x32_bf16 v[180:183], a[124:127], a[56:59], v[180:183]\n"
      "v_mfma_f32_16x16x32_bf16 v[188:191], a[124:127], a[60:63], v[188:191]\n"
      "s_nop 3\n"
      "v_max3_f32 v112, v160, v161, v162\n"
      "v_max3_f32 v113, v168, v169, v170\n"
      "v_max3_f32 v114, v176, v177, v178\n"
      "v_max3_f32 v115, v184, v185, v186\n"
      "v_max3_f32 v112, v112, v163, v164\n"
      "v_max3_f32 v113, v113, v171, v172\n"
      "v_max3_f32 v114, v114, v179, v180\n"
      "v_max3_f32 v115, v115, v187, v188\n"
      "v_max3_f32 v112, v112, v165, v166\n"
      "v_max3_f32 v113, v113, v173, v174\n"
      "v_max3_f32 v114, v114, v181, v182\n"
      "v_max3_f32 v115, v115, v189, v190\n"
      "v_max_f32 v112, v112, v167\n"
      "v_max_f32 v113, v113, v175\n"
      "v_max_f32 v114, v114, v183\n"
      "v_max_f32 v115, v115, v191\n"
      "v_mov_b32 v208, v112\n"
      "v_mov_b32 v209, v113\n"
      "v_mov_b32 v210, v114\n"
      "v_mov_b32 v211, v115\n"
      "v_permlane16_swap_b32 v112, v208\n"
      "v_permlane16_swap_b32 v113, v209\n"
      "v_permlane16_swap_b32 v114, v210\n"
      "v_permlane16_swap_b32 v115, v211\n"
      "v_max_f32 v112, v112, v208\n"
      "v_max_f32 v113, v113, v209\n"
      "v_max_f32 v114, v114, v210\n"
      "v_max_f32 v115, v115, v211\n"
      "v_mov_b32 v208, v112\n"
      "v_mov_b32 v209, v113\n"
      "v_mov_b32 v210, v114\n"
      "v_mov_b32 v211, v115\n"
      "v_permlane32_swap_b32 v112, v208\n"
      "v_permlane32_swap_b32 v113, v209\n"
      "v_permlane32_swap_b32 v114, v210\n"
      "v_permlane32_swap_b32 v115, v211\n"
      "v_max_f32 v112, v112, v208\n"
      "v_max_f32 v113, v113, v209\n"
      "v_max_f32 v114, v114, v210\n"
      "v_max_f32 v115, v115, v211\n"
      "v_sub_f32 v112, 0xc1800000, v112\n"
      "v_sub_f32 v113, 0xc1800000, v113\n"
      "v_sub_f32 v114, 0xc1800000, v114\n"
      "v_sub_f32 v115, 0xc1800000, v115\n"
      "v_floor_f32 v112, v112\n"
      "v_floor_f32 v113, v113\n"
      "v_floor_f32 v114, v114\n"
      "v_floor_f32 v115, v115\n"
      "v_min_f32 v224, 0x7f000000, v112\n"
      "v_min_f32 v228, 0x7f000000, v113\n"
      "v_min_f32 v232, 0x7f000000, v114\n"
      "v_min_f32 v236, 0x7f000000, v115\n"
      "v_add_f32 v112, %[neg], v224\n"
      "v_add_f32 v113, %[neg], v228\n"
      "v_add_f32 v114, %[neg], v232\n"
      "v_add_f32 v115, %[neg], v236\n"
      "v_mov_b32 v225, v224\n"
      "v_mov_b32 v229, v228\n"
      "v_mov_b32 v233, v232\n"
      "v_mov_b32 v237, v236\n"
      "v_mov_b64 v[226:227], v[224:225]\n"
      "v_mov_b64 v[230:231], v[228:229]\n"
      "v_mov_b64 v[234:235], v[232:233]\n"
      "v_mov_b64 v[238:239], v[236:237]\n"
      "s_add_u32 m0, s77, 16384\n"
      "v_pk_add_f32 v[160:161], v[160:161], v[112:113] op_sel:[0,0] op_sel_hi:[1,0]\n"
      "buffer_load_dwordx4 v249, s[24:27], s61 offen nt lds\n"
      "v_pk_add_f32 v[162:163], v[162:163], v[112:113] op_sel:[0,0] op_sel_hi:[1,0]\n"
      "v_pk_add_f32 v[164:165], v[164:165], v[112:113] op_sel:[0,0] op_sel_hi:[1,0]\n"
      "v_pk_add_f32 v[166:167], v[166:167], v[112:113] op_sel:[0,0] op_sel_hi:[1,0]\n"
      "s_add_u32 m0, s77, 17408\n"
      "v_pk_add_f32 v[168:169], v[168:169], v[112:113] op_sel:[0,1] op_sel_hi:[1,1]\n"
      "buffer_load_dwordx4 v121, s[24:27], s61 offen nt lds\n"
      "v_pk_add_f32 v[170:171], v[170:171], v[112:113] op_sel:[0,1] op_sel_hi:[1,1]\n"
      "v_pk_add_f32 v[172:173], v[172:173], v[112:113] op_sel:[0,1] op_sel_hi:[1,1]\n"
      "v_pk_add_f32 v[174:175], v[174:175], v[112:113] op_sel:[0,1] op_sel_hi:[1,1]\n"
      "s_add_u32 m0, s77, 24576\n"
      "v_pk_add_f32 v[176:177], v[176:177], v[114:115] op_sel:[0,0] op_sel_hi:[1,0]\n"
      "buffer_load_dwordx4 v122, s[24:27], s61 offen nt lds\n"
      "v_pk_add_f32 v[178:179], v[178:179], v[114:115] op_sel:[0,0] op_sel_hi:[1,0]\n"
      "v_pk_add_f32 v[180:181], v[180:181], v[114:115] op_sel:[0,0] op_sel_hi:[1,0]\n"
      "v_pk_add_f32 v[182:183], v[182:183], v[114:115] op_sel:[0,0] op_sel_hi:[1,0]\n"
      "s_add_u32 m0, s77, 25600\n"
      "v_pk_add_f32 v[184:185], v[184:185], v[114:115] op_sel:[0,1] op_sel_hi:[1,1]\n"
      "buffer_load_dwordx4 v123, s[24:27], s61 offen nt lds\n"
      "v_pk_add_f32 v[186:187], v[186:187], v[114:115] op_sel:[0,1] op_sel_hi:[1,1]\n"
      "v_pk_add_f32 v[188:189], v[188:189], v[114:115] op_sel:[0,1] op_sel_hi:[1,1]\n"
      "v_pk_add_f32 v[190:191], v[190:191], v[114:115] op_sel:[0,1] op_sel_hi:[1,1]\n"
      "s_add_u32 s77, s77, 0x8000\n"
      "s_and_b32 s77, s77, 0x1ffff\n"
      "s_add_u32 s61, s61, %[inc]\n"
      "v_exp_f32 v112, v160\n"
      "v_exp_f32 v113, v161\n"
      "v_exp_f32 v114, v162\n"
      "v_cvt_pk_bf16_f32 v208, v112, v113\n"
      "v_exp_f32 v115, v163\n"
      "v_exp_f32 v112, v164\n"
      "v_cvt_pk_bf16_f32 v209, v114, v115\n"
      "v_exp_f32 v113, v165\n"
      "v_or_b32 v116, v208, v209\n"
      "v_exp_f32 v114, v166\n"
      "v_cvt_pk_bf16_f32 v210, v112, v113\n"
      "v_exp_f32 v115, v167\n"
      "v_exp_f32 v112, v168\n"
      "v_cvt_pk_bf16_f32 v211, v114, v115\n"
      "v_exp_f32 v113, v169\n"
      "v_or3_b32 v116, v116, v210, v211\n"
      "v_exp_f32 v114, v170\n"
      "v_cvt_pk_bf16_f32 v212, v112, v113\n"
      "v_exp_f32 v115, v171\n"
      "v_exp_f32 v112, v172\n"
      "v_cvt_pk_bf16_f32 v213, v114, v115\n"
      "v_exp_f32 v113, v173\n"
      "v_or3_b32 v116, v116, v212, v213\n"
      "s_waitcnt lgkmcnt(0)\n"
      "v_cvt_pk_bf16_f32 v214, v112, v113\n"
      "s_sub_u32 s63, %[n1], 1\n"
      "s_branch ENTRY_%=\n"
      "LOOP1_%=:\n"
      "s_waitcnt vmcnt(12) lgkmcnt(0)\n"
      "s_sub_u32 s67, s68, s90\n"
      "s_cmp_lt_i32 s67, 0\n"
      "s_cbranch_scc1 GW0_%=\n"
      "GX0_%=:\n"
      "s_barrier\n"
      "v_mfma_f32_16x16x32_bf16 v[40:43], a[96:99], v[216:219], v[40:43]\n"
      "s_memrealtime s[68:69]\n"
      "buffer_load_dword v250, v251, s[28:31], 0 offen sc1\n"
      "ds_read_b128 a[64:67], v242 offset:0\n"
      "v_exp_f32 v114, v166\n"
      "v_mfma_f32_16x16x32_bf16 v[44:47], a[96:99], v[220:223], v[44:47]\n"
      "s_add_u32 m0, s77, 16384\n"
      "ds_read_b128 a[68:71], v242 offset:4096\n"
      "ds_read_b128 a[72:75], v243 offset:0\n"
      "v_mfma_f32_16x16x32_bf16 v[60:63], a[100:103], v[220:223], v[60:63]\n"
      "buffer_load_dwordx4 v249, s[24:27], s61 offen nt lds\n"
      "v_exp_f32 v115, v167\n"
      "v_mfma_f32_16x16x32_bf16 v[56:59], a[100:103], v[216:219], v[56:59]\n"
      "s_add_u32 m0, s77, 17408\n"
      "ds_read_b128 a[76:79], v243 offset:4096\n"
      "v_cvt_pk_bf16_f32 v211, v114, v115\n"
      "v_mfma_f32_16x16x32_bf16 a[136:139], a[104:107], v[216:219], a[136:139]\n"
      "buffer_load_dwordx4 v121, s[24:27], s61 offen nt lds\n"
      "v_exp_f32 v112, v168\n"
      "v_mfma_f32_16x16x32_bf16 a[140:143], a[104:107], v[220:223], a[140:143]\n"
      "s_add_u32 m0, s77, 24576\n"
      "ds_read_b128 a[80:83], v242 offset:1024\n"
      "v_or3_b32 v116, v116, v210, v211\n"
      "v_mfma_f32_16x16x32_bf16 a[156:159], a[108:111], v[220:223], a[156:159]\n"
      "buffer_load_dwordx4 v122, s[24:27], s61 offen nt lds\n"
      "v_exp_f32 v113, v169\n"
      "v_mfma_f32_16x16x32_bf16 a[152:155], a[108:111], v[216:219], a[152:155]\n"
      "s_add_u32 m0, s77, 25600\n"
      "ds_read_b128 a[84:87], v242 offset:5120\n"
      "v_cvt_pk_bf16_f32 v212, v112, v113\n"
      "v_mfma_f32_16x16x32_bf16 v[72:75], a[112:115], v[216:219], v[72:75]\n"
      "s_add_u32 s77, s77, 0x8000\n"
      "buffer_load_dwordx4 v123, s[24:27], s61 offen nt lds\n"
      "v_exp_f32 v114, v170\n"
      "v_mfma_f32_16x16x32_bf16 v[76:79], a[112:115], v[220:223], v[76:79]\n"
      "s_and_b32 s77, s77, 0x1ffff\n"
      "ds_read_b128 a[88:91], v243 offset:1024\n"
      "ds_read_b128 a[92:95], v243 offset:5120\n"
      "v_mfma_f32_16x16x32_bf16 v[92:95], a[116:119], v[220:223], v[92:95]\n"
      "s_add_u32 s61, s61, %[inc]\n"
      "v_exp_f32 v115, v171\n"
      "v_mfma_f32_16x16x32_bf16 v[88:91], a[116:119], v[216:219], v[88:91]\n"
      "v_cvt_pk_bf16_f32 v213, v114, v115\n"
      "v_mfma_f32_16x16x32_bf16 a[168:171], a[120:123], v[216:219], a[168:171]\n"
      "v_exp_f32 v112, v172\n"
      "v_mfma_f32_16x16x32_bf16 a[172:175], a[120:123], v[220:223], a[172:175]\n"
      "v_or3_b32 v116, v116, v212, v213\n"
      "v_mfma_f32_16x16x32_bf16 a[188:191], a[124:127], v[220:223], a[188:191]\n"
      "v_exp_f32 v113, v173\n"
      "v_mfma_f32_16x16x32_bf16 a[184:187], a[124:127], v[216:219], a[184:187]\n"
      "s_waitcnt lgkmcnt(0)\n"
      "v_cvt_pk_bf16_f32 v214, v112, v113\n"
      "ENTRY_%=:\n"
      "v_mfma_f32_16x16x32_bf16 v[128:131], a[64:67], a[0:3], v[224:227]\n"
      "s_sub_u32 s67, s68, s66\n"
      "v_exp_f32 v114, v174\n"
      "v_mfma_f32_16x16x32_bf16 v[128:131], a[72:75], a[16:19], v[128:131]\n"
      "s_cmp_gt_i32 s67, s70\n"
      "ds_read_b64_tr_b16 a[96:97], v244 offset:0\n"
      "ds_read_b64_tr_b16 a[98:99], v244 offset:4096\n"
      "v_mfma_f32_16x16x32_bf16 v[128:131], a[80:83], a[32:35], v[128:131]\n"
      "s_cselect_b32 s69, s86, 0\n"
      "v_exp_f32 v115, v175\n"
      "v_mfma_f32_16x16x32_bf16 v[128:131], a[88:91], a[48:51], v[128:131]\n"
      "s_add_u32 s66, s66, s69\n"
      "ds_read_b64_tr_b16 a[100:101], v245 offset:0\n"
      "v_cvt_pk_bf16_f32 v215, v114, v115\n"
      "v_mfma_f32_16x16x32_bf16 v[132:135], a[92:95], a[48:51], v[224:227]\n"
      "s_add_u32 s67, s67, s101\n"
      "v_exp_f32 v112, v176\n"
      "v_mfma_f32_16x16x32_bf16 v[132:135], a[84:87], a[32:35], v[132:135]\n"
      "s_min_i32 s70, s70, s67\n"
      "ds_read_b64_tr_b16 a[102:103], v245 offset:4096\n"
      "v_or3_b32 v116, v116, v214, v215\n"
      "v_mfma_f32_16x16x32_bf16 v[132:135], a[76:79], a[16:19], v[132:135]\n"
      "s_max_i32 s70, s70, 85\n"
      "v_exp_f32 v113, v177\n"
      "v_mfma_f32_16x16x32_bf16 v[132:135], a[68:71], a[0:3], v[132:135]\n"
      "s_add_u32 s66, s66, s86\n"
      "ds_read_b64_tr_b16 a[104:105], v246 offset:0\n"
      "v_cvt_pk_bf16_f32 v216, v112, v113\n"
      "v_mfma_f32_16x16x32_bf16 v[140:143], a[68:71], a[4:7], v[228:231]\n"
      "s_and_b32 s76, s80, 31\n"
      "v_exp_f32 v114, v178\n"
      "v_mfma_f32_16x16x32_bf16 v[140:143], a[76:79], a[20:23], v[140:143]\n"
      "s_lshr_b64 s[80:81], s[80:81], 5\n"
      "ds_read_b64_tr_b16 a[106:107], v246 offset:4096\n"
      "ds_read_b64_tr_b16 a[108:109], v247 offset:0\n"
      "v_mfma_f32_16x16x32_bf16 v[140:143], a[84:87], a[36:39], v[140:143]\n"
      "s_add_u32 s66, s66, s76\n"
      "v_exp_f32 v115, v179\n"
      "v_mfma_f32_16x16x32_bf16 v[140:143], a[92:95], a[52:55], v[140:143]\n"
      "s_sub_u32 s67, s68, s82\n"
      "ds_read_b64_tr_b16 a[110:111], v247 offset:4096\n"
      "v_cvt_pk_bf16_f32 v217, v114, v115\n"
      "v_mfma_f32_16x16x32_bf16 v[136:139], a[88:91], a[52:55], v[228:231]\n"
      "s_min_i32 s76, s67, s83\n"
      "v_exp_f32 v112, v180\n"
      "v_mfma_f32_16x16x32_bf16 v[136:139], a[80:83], a[36:39], v[136:139]\n"
      "s_mov_b32 s83, s67\n"
      "ds_read_b64_tr_b16 a[112:113], v244 offset:1024\n"
      "v_or3_b32 v116, v116, v216, v217\n"
      "v_mfma_f32_16x16x32_bf16 v[136:139], a[72:75], a[20:23], v[136:139]\n"
      "s_sub_u32 s76, s76, 36\n"
      "v_exp_f32 v113, v181\n"
      "v_mfma_f32_16x16x32_bf16 v[136:139], a[64:67], a[4:7], v[136:139]\n"
      "s_min_i32 s76, s76, 148\n"
      "ds_read_b64_tr_b16 a[114:115], v244 offset:5120\n"
      "v_cvt_pk_bf16_f32 v218, v112, v113\n"
      "v_mfma_f32_16x16x32_bf16 v[144:147], a[64:67], a[8:11], v[232:235]\n"
      "s_max_i32 s76, s76, 70\n"
      "v_exp_f32 v114, v182\n"
      "v_mfma_f32_16x16x32_bf16 v[144:147], a[72:75], a[24:27], v[144:147]\n"
      "s_mov_b32 s82, s68\n"
      "ds_read_b64_tr_b16 a[116:117], v245 offset:1024\n"
      "ds_read_b64_tr_b16 a[118:119], v245 offset:5120\n"
      "v_mfma_f32_16x16x32_bf16 v[144:147], a[80:83], a[40:43], v[144:147]\n"
      "s_add_u32 s65, s82, s76\n"
      "v_exp_f32 v115, v183\n"
      "v_mfma_f32_16x16x32_bf16 v[144:147], a[88:91], a[56:59], v[144:147]\n"
      "s_sub_u32 s93, s93, 1\n"
      "ds_read_b64_tr_b16 a[120:121], v246 offset:1024\n"
      "v_cvt_pk_bf16_f32 v219, v114, v115\n"
      "v_mfma_f32_16x16x32_bf16 v[148:151], a[92:95], a[56:59], v[232:235]\n"
      "s_cmp_le_i32 s93, 14\n"
      "v_exp_f32 v112, v184\n"
      "v_mfma_f32_16x16x32_bf16 v[148:151], a[84:87], a[40:43], v[148:151]\n"
      "s_cselect_b32 s86, 107, s86\n"
      "ds_read_b64_tr_b16 a[122:123], v246 offset:5120\n"
      "v_or3_b32 v116, v116, v218, v219\n"
      "v_mfma_f32_16x16x32_bf16 v[148:151], a[76:79], a[24:27], v[148:151]\n"
      "s_sub_u32 s67, s65, s66\n"
      "v_exp_f32 v113, v185\n"
      "v_mfma_f32_16x16x32_bf16 v[148:151], a[68:71], a[8:11], v[148:151]\n"
      "s_cmp_lt_i32 s67, 0\n"
      "ds_read_b64_tr_b16 a[124:125], v247 offset:1024\n"
      "v_cvt_pk_bf16_f32 v220, v112, v113\n"
      "v_mfma_f32_16x16x32_bf16 v[156:159], a[68:71], a[12:15], v[236:239]\n"
      "s_cselect_b32 s88, s66, s65\n"
      "v_exp_f32 v114, v186\n"
      "v_mfma_f32_16x16x32_bf16 v[156:159], a[76:79], a[28:31], v[156:159]\n"
      "s_add_u32 s90, s88, s91\n"
      "ds_read_b64_tr_b16 a[126:127], v247 offset:5120\n"
      "v_mfma_f32_16x16x32_bf16 v[156:159], a[84:87], a[44:47], v[156:159]\n"
      "v_exp_f32 v115, v187\n"
      "v_mfma_f32_16x16x32_bf16 v[156:159], a[92:95], a[60:63], v[156:159]\n"
      "v_cvt_pk_bf16_f32 v221, v114, v115\n"
      "v_mfma_f32_16x16x32_bf16 v[152:155], a[88:91], a[60:63], v[236:239]\n"
      "v_exp_f32 v112, v188\n"
      "v_mfma_f32_16x16x32_bf16 v[152:155], a[80:83], a[44:47], v[152:155]\n"
      "v_or3_b32 v116, v116, v220, v221\n"
      "v_mfma_f32_16x16x32_bf16 v[152:155], a[72:75], a[28:31], v[152:155]\n"
      "v_exp_f32 v113, v189\n"
      "v_mfma_f32_16x16x32_bf16 v[152:155], a[64:67], a[12:15], v[152:155]\n"
      "s_waitcnt lgkmcnt(10)\n"
      "v_cvt_pk_bf16_f32 v222, v112, v113\n"
      "v_mfma_f32_16x16x32_bf16 v[32:35], a[96:99], v[192:195], v[32:35]\n"
      "v_exp_f32 v114, v190\n"
      "v_mfma_f32_16x16x32_bf16 v[48:51], a[100:103], v[192:195], v[48:51]\n"
      "v_mfma_f32_16x16x32_bf16 a[128:131], a[104:107], v[192:195], a[128:131]\n"
      "s_waitcnt lgkmcnt(6)\n"
      "v_exp_f32 v115, v191\n"
      "v_mfma_f32_16x16x32_bf16 a[144:147], a[108:111], v[192:195], a[144:147]\n"
      "v_cvt_pk_bf16_f32 v223, v114, v115\n"
      "v_mfma_f32_16x16x32_bf16 v[64:67], a[112:115], v[192:195], v[64:67]\n"
      "s_waitcnt lgkmcnt(2)\n"
      "v_mfma_f32_16x16x32_bf16 v[80:83], a[116:119], v[192:195], v[80:83]\n"
      "v_or3_b32 v116, v116, v222, v223\n"
      "v_mfma_f32_16x16x32_bf16 a[160:163], a[120:123], v[192:195], a[160:163]\n"
      "s_mov_b32 s64, 0\n"
      "s_waitcnt lgkmcnt(0)\n"
      "v_and_b32 v116, 0x40004000, v116\n"
      "v_mfma_f32_16x16x32_bf16 a[176:179], a[124:127], v[192:195], a[176:179]\n"
      "v_cmp_ne_u32 vcc, 0, v116\n"
      "s_cbranch_vccnz FB1_%=\n"
      "RET1_0_%=:\n"
      "v_mfma_f32_16x16x32_bf16 a[180:183], a[124:127], v[196:199], a[180:183]\n"
      "v_exp_f32 v112, v128\n"
      "s_barrier\n"
      "v_mfma_f32_16x16x32_bf16 a[164:167], a[120:123], v[196:199], a[164:167]\n"
      "v_mfma_f32_16x16x32_bf16 a[192:195], a[196:199], v[208:211], a[192:195]\n"
      "s_add_u32 m0, s62, 0\n"
      "v_exp_f32 v113, v129\n"
      "v_mfma_f32_16x16x32_bf16 v[84:87], a[116:119], v[196:199], v[84:87]\n"
      "buffer_load_dwordx4 v248, s[20:23], s60 offen nt lds\n"
      "v_cvt_pk_bf16_f32 v192, v112, v113\n"
      "v_mfma_f32_16x16x32_bf16 v[68:71], a[112:115], v[196:199], v[68:71]\n"
      "s_add_u32 m0, s62, 896\n"
      "v_exp_f32 v114, v130\n"
      "v_mfma_f32_16x16x32_bf16 a[192:195], a[200:203], v[212:215], a[192:195]\n"
      "buffer_load_dwordx4 v248, s[20:23], s60 offen offset:128 nt lds\n"
      "v_mfma_f32_16x16x32_bf16 a[148:151], a[108:111], v[196:199], a[148:151]\n"
      "s_add_u32 m0, s62, 8192\n"
      "v_exp_f32 v115, v131\n"
      "v_mfma_f32_16x16x32_bf16 a[132:135], a[104:107], v[196:199], a[132:135]\n"
      "s_memrealtime s[68:69]\n"
      "buffer_load_dwordx4 v120, s[20:23], s60 offen nt lds\n"
      "v_cvt_pk_bf16_f32 v193, v114, v115\n"
      "v_mfma_f32_16x16x32_bf16 a[192:195], a[204:207], v[216:219], a[192:195]\n"
      "s_add_u32 m0, s62, 9088\n"
      "v_exp_f32 v112, v132\n"
      "v_mfma_f32_16x16x32_bf16 v[52:55], a[100:103], v[196:199], v[52:55]\n"
      "s_add_u32 s62, s62, 0x8000\n"
      "buffer_load_dwordx4 v120, s[20:23], s60 offen offset:128 nt lds\n"
      "v_or_b32 v116, v192, v193\n"
      "v_mfma_f32_16x16x32_bf16 v[36:39], a[96:99], v[196:199], v[36:39]\n"
      "s_and_b32 s62, s62, 0x1ffff\n"
      "v_exp_f32 v113, v133\n"
      "v_mfma_f32_16x16x32_bf16 a[192:195], a[208:211], v[220:223], a[192:195]\n"
      "s_add_u32 s60, s60, s78\n"
      "v_cvt_pk_bf16_f32 v194, v112, v113\n"
      "s_waitcnt vmcnt(12) lgkmcnt(0)\n"
      "s_sub_u32 s67, s68, s90\n"
      "s_cmp_lt_i32 s67, 0\n"
      "s_cbranch_scc1 GW1_%=\n"
      "GX1_%=:\n"
      "s_barrier\n"
      "v_mfma_f32_16x16x32_bf16 v[40:43], a[96:99], v[200:203], v[40:43]\n"
      "s_memrealtime s[68:69]\n"
      "ds_read_b128 a[64:67], v242 offset:32768\n"
      "v_exp_f32 v114, v134\n"
      "v_mfma_f32_16x16x32_bf16 v[44:47], a[96:99], v[204:207], v[44:47]\n"
      "s_add_u32 m0, s77, 16384\n"
      "ds_read_b128 a[68:71], v242 offset:36864\n"
      "ds_read_b128 a[72:75], v243 offset:32768\n"
      "v_mfma_f32_16x16x32_bf16 v[60:63], a[100:103], v[204:207], v[60:63]\n"
      "buffer_load_dwordx4 v249, s[24:27], s61 offen nt lds\n"
      "v_exp_f32 v115, v135\n"
      "v_mfma_f32_16x16x32_bf16 v[56:59], a[100:103], v[200:203], v[56:59]\n"
      "s_add_u32 m0, s77, 17408\n"
      "ds_read_b128 a[76:79], v243 offset:36864\n"
      "v_cvt_pk_bf16_f32 v195, v114, v115\n"
      "v_mfma_f32_16x16x32_bf16 a[136:139], a[104:107], v[200:203], a[136:139]\n"
      "buffer_load_dwordx4 v121, s[24:27], s61 offen nt lds\n"
      "v_exp_f32 v112, v136\n"
      "v_mfma_f32_16x16x32_bf16 a[140:143], a[104:107], v[204:207], a[140:143]\n"
      "s_add_u32 m0, s77, 24576\n"
      "ds_read_b128 a[80:83], v242 offset:33792\n"
      "v_or3_b32 v116, v116, v194, v195\n"
      "v_mfma_f32_16x16x32_bf16 a[156:159], a[108:111], v[204:207], a[156:159]\n"
      "buffer_load_dwordx4 v122, s[24:27], s61 offen nt lds\n"
      "v_exp_f32 v113, v137\n"
      "v_mfma_f32_16x16x32_bf16 a[152:155], a[108:111], v[200:203], a[152:155]\n"
      "s_add_u32 m0, s77, 25600\n"
      "ds_read_b128 a[84:87], v242 offset:37888\n"
      "v_cvt_pk_bf16_f32 v196, v112, v113\n"
      "v_mfma_f32_16x16x32_bf16 v[72:75], a[112:115], v[200:203], v[72:75]\n"
      "s_add_u32 s77, s77, 0x8000\n"
      "buffer_load_dwordx4 v123, s[24:27], s61 offen nt lds\n"
      "v_exp_f32 v114, v138\n"
      "v_mfma_f32_16x16x32_bf16 v[76:79], a[112:115], v[204:207], v[76:79]\n"
      "s_and_b32 s77, s77, 0x1ffff\n"
      "ds_read_b128 a[88:91], v243 offset:33792\n"
      "ds_read_b128 a[92:95], v243 offset:37888\n"
      "v_mfma_f32_16x16x32_bf16 v[92:95], a[116:119], v[204:207], v[92:95]\n"
      "s_add_u32 s61, s61, s78\n"
      "v_exp_f32 v115, v139\n"
      "v_mfma_f32_16x16x32_bf16 v[88:91], a[116:119], v[200:203], v[88:91]\n"
      "v_cvt_pk_bf16_f32 v197, v114, v115\n"
      "v_mfma_f32_16x16x32_bf16 a[168:171], a[120:123], v[200:203], a[168:171]\n"
      "v_exp_f32 v112, v140\n"
      "v_mfma_f32_16x16x32_bf16 a[172:175], a[120:123], v[204:207], a[172:175]\n"
      "v_or3_b32 v116, v116, v196, v197\n"
      "v_mfma_f32_16x16x32_bf16 a[188:191], a[124:127], v[204:207], a[188:191]\n"
      "v_exp_f32 v113, v141\n"
      "v_mfma_f32_16x16x32_bf16 a[184:187], a[124:127], v[200:203], a[184:187]\n"
      "s_waitcnt lgkmcnt(0)\n"
      "v_cvt_pk_bf16_f32 v198, v112, v113\n"
      "v_mfma_f32_16x16x32_bf16 v[160:163], a[64:67], a[0:3], v[224:227]\n"
      "s_sub_u32 s67, s68, s66\n"
      "v_exp_f32 v114, v142\n"
      "v_mfma_f32_16x16x32_bf16 v[160:163], a[72:75], a[16:19], v[160:163]\n"
      "s_cmp_gt_i32 s67, s70\n"
      "ds_read_b64_tr_b16 a[96:97], v244 offset:32768\n"
      "ds_read_b64_tr_b16 a[98:99], v244 offset:36864\n"
      "v_mfma_f32_16x16x32_bf16 v[160:163], a[80:83], a[32:35], v[160:163]\n"
      "s_cselect_b32 s69, s86, 0\n"
      "v_exp_f32 v115, v143\n"
      "v_mfma_f32_16x16x32_bf16 v[160:163], a[88:91], a[48:51], v[160:163]\n"
      "s_add_u32 s66, s66, s69\n"
      "ds_read_b64_tr_b16 a[100:101], v245 offset:32768\n"
      "v_cvt_pk_bf16_f32 v199, v114, v115\n"
      "v_mfma_f32_16x16x32_bf16 v[164:167], a[92:95], a[48:51], v[224:227]\n"
      "s_add_u32 s67, s67, s101\n"
      "v_exp_f32 v112, v144\n"
      "v_mfma_f32_16x16x32_bf16 v[164:167], a[84:87], a[32:35], v[164:167]\n"
      "s_min_i32 s70, s70, s67\n"
      "ds_read_b64_tr_b16 a[102:103], v245 offset:36864\n"
      "v_or3_b32 v116, v116, v198, v199\n"
      "v_mfma_f32_16x16x32_bf16 v[164:167], a[76:79], a[16:19], v[164:167]\n"
      "s_max_i32 s70, s70, 85\n"
      "v_exp_f32 v113, v145\n"
      "v_mfma_f32_16x16x32_bf16 v[164:167], a[68:71], a[0:3], v[164:167]\n"
      "s_add_u32 s66, s66, s86\n"
      "ds_read_b64_tr_b16 a[104:105], v246 offset:32768\n"
      "v_cvt_pk_bf16_f32 v200, v112, v113\n"
      "v_mfma_f32_16x16x32_bf16 v[172:175], a[68:71], a[4:7], v[228:231]\n"
      "s_and_b32 s76, s80, 31\n"
      "v_exp_f32 v114, v146\n"
      "v_mfma_f32_16x16x32_bf16 v[172:175], a[76:79], a[20:23], v[172:175]\n"
      "s_lshr_b64 s[80:81], s[80:81], 5\n"
      "ds_read_b64_tr_b16 a[106:107], v246 offset:36864\n"
      "ds_read_b64_tr_b16 a[108:109], v247 offset:32768\n"
      "v_mfma_f32_16x16x32_bf16 v[172:175], a[84:87], a[36:39], v[172:175]\n"
      "s_add_u32 s66, s66, s76\n"
      "v_exp_f32 v115, v147\n"
      "v_mfma_f32_16x16x32_bf16 v[172:175], a[92:95], a[52:55], v[172:175]\n"
      "s_sub_u32 s67, s68, s82\n"
      "ds_read_b64_tr_b16 a[110:111], v247 offset:36864\n"
      "v_cvt_pk_bf16_f32 v201, v114, v115\n"
      "v_mfma_f32_16x16x32_bf16 v[168:171], a[88:91], a[52:55], v[228:231]\n"
      "s_min_i32 s76, s67, s83\n"
      "v_exp_f32 v112, v148\n"
      "v_mfma_f32_16x16x32_bf16 v[168:171], a[80:83], a[36:39], v[168:171]\n"
      "s_mov_b32 s83, s67\n"
      "ds_read_b64_tr_b16 a[112:113], v244 offset:33792\n"
      "v_or3_b32 v116, v116, v200, v201\n"
      "v_mfma_f32_16x16x32_bf16 v[168:171], a[72:75], a[20:23], v[168:171]\n"
      "s_sub_u32 s76, s76, 36\n"
      "v_exp_f32 v113, v149\n"
      "v_mfma_f32_16x16x32_bf16 v[168:171], a[64:67], a[4:7], v[168:171]\n"
      "s_min_i32 s76, s76, 148\n"
      "ds_read_b64_tr_b16 a[114:115], v244 offset:37888\n"
      "v_cvt_pk_bf16_f32 v202, v112, v113\n"
      "v_mfma_f32_16x16x32_bf16 v[176:179], a[64:67], a[8:11], v[232:235]\n"
      "s_max_i32 s76, s76, 70\n"
      "v_exp_f32 v114, v150\n"
      "v_mfma_f32_16x16x32_bf16 v[176:179], a[72:75], a[24:27], v[176:179]\n"
      "s_mov_b32 s82, s68\n"
      "ds_read_b64_tr_b16 a[116:117], v245 offset:33792\n"
      "ds_read_b64_tr_b16 a[118:119], v245 offset:37888\n"
      "v_mfma_f32_16x16x32_bf16 v[176:179], a[80:83], a[40:43], v[176:179]\n"
      "s_add_u32 s65, s82, s76\n"
      "v_exp_f32 v115, v151\n"
      "v_mfma_f32_16x16x32_bf16 v[176:179], a[88:91], a[56:59], v[176:179]\n"
      "s_sub_u32 s93, s93, 1\n"
      "ds_read_b64_tr_b16 a[120:121], v246 offset:33792\n"
      "v_cvt_pk_bf16_f32 v203, v114, v115\n"
      "v_mfma_f32_16x16x32_bf16 v[180:183], a[92:95], a[56:59], v[232:235]\n"
      "s_cmp_le_i32 s93, 14\n"
      "v_exp_f32 v112, v152\n"
      "v_mfma_f32_16x16x32_bf16 v[180:183], a[84:87], a[40:43], v[180:183]\n"
      "s_cselect_b32 s86, 107, s86\n"
      "ds_read_b64_tr_b16 a[122:123], v246 offset:37888\n"
      "v_or3_b32 v116, v116, v202, v203\n"
      "v_mfma_f32_16x16x32_bf16 v[180:183], a[76:79], a[24:27], v[180:183]\n"
      "s_sub_u32 s67, s65, s66\n"
      "v_exp_f32 v113, v153\n"
      "v_mfma_f32_16x16x32_bf16 v[180:183], a[68:71], a[8:11], v[180:183]\n"
      "s_cmp_lt_i32 s67, 0\n"
      "ds_read_b64_tr_b16 a[124:125], v247 offset:33792\n"
      "v_cvt_pk_bf16_f32 v204, v112, v113\n"
      "v_mfma_f32_16x16x32_bf16 v[188:191], a[68:71], a[12:15], v[236:239]\n"
      "s_cselect_b32 s88, s66, s65\n"
      "v_exp_f32 v114, v154\n"
      "v_mfma_f32_16x16x32_bf16 v[188:191], a[76:79], a[28:31], v[188:191]\n"
      "s_add_u32 s90, s88, s91\n"
      "ds_read_b64_tr_b16 a[126:127], v247 offset:37888\n"
      "v_mfma_f32_16x16x32_bf16 v[188:191], a[84:87], a[44:47], v[188:191]\n"
      "v_exp_f32 v115, v155\n"
      "v_mfma_f32_16x16x32_bf16 v[188:191], a[92:95], a[60:63], v[188:191]\n"
      "v_cvt_pk_bf16_f32 v205, v114, v115\n"
      "v_mfma_f32_16x16x32_bf16 v[184:187], a[88:91], a[60:63], v[236:239]\n"
      "v_exp_f32 v112, v156\n"
      "v_mfma_f32_16x16x32_bf16 v[184:187], a[80:83], a[44:47], v[184:187]\n"
      "v_or3_b32 v116, v116, v204, v205\n"
      "v_mfma_f32_16x16x32_bf16 v[184:187], a[72:75], a[28:31], v[184:187]\n"
      "v_exp_f32 v113, v157\n"
      "v_mfma_f32_16x16x32_bf16 v[184:187], a[64:67], a[12:15], v[184:187]\n"
      "s_waitcnt lgkmcnt(10)\n"
      "v_cvt_pk_bf16_f32 v206, v112, v113\n"
      "v_mfma_f32_16x16x32_bf16 v[32:35], a[96:99], v[208:211], v[32:35]\n"
      "v_exp_f32 v114, v158\n"
      "v_mfma_f32_16x16x32_bf16 v[48:51], a[100:103], v[208:211], v[48:51]\n"
      "v_mfma_f32_16x16x32_bf16 a[128:131], a[104:107], v[208:211], a[128:131]\n"
      "s_waitcnt lgkmcnt(6)\n"
      "v_exp_f32 v115, v159\n"
      "v_mfma_f32_16x16x32_bf16 a[144:147], a[108:111], v[208:211], a[144:147]\n"
      "v_cvt_pk_bf16_f32 v207, v114, v115\n"
      "v_mfma_f32_16x16x32_bf16 v[64:67], a[112:115], v[208:211], v[64:67]\n"
      "s_waitcnt lgkmcnt(2)\n"
      "v_mfma_f32_16x16x32_bf16 v[80:83], a[116:119], v[208:211], v[80:83]\n"
      "v_or3_b32 v116, v116, v206, v207\n"
      "v_mfma_f32_16x16x32_bf16 a[160:163], a[120:123], v[208:211], a[160:163]\n"
      "s_mov_b32 s64, 0\n"
      "s_waitcnt lgkmcnt(0)\n"
      "v_and_b32 v116, 0x40004000, v116\n"
      "v_mfma_f32_16x16x32_bf16 a[176:179], a[124:127], v[208:211], a[176:179]\n"
      "v_cmp_ne_u32 vcc, 0, v116\n"
      "s_cbranch_vccnz FB0_%=\n"
      "RET0_0_%=:\n"
      "v_mfma_f32_16x16x32_bf16 a[180:183], a[124:127], v[212:215], a[180:183]\n"
      "v_exp_f32 v112, v160\n"
      "s_barrier\n"
      "v_mfma_f32_16x16x32_bf16 a[164:167], a[120:123], v[212:215], a[164:167]\n"
      "v_xor_b32 v242, 0x10000, v242\n"
      "v_mfma_f32_16x16x32_bf16 a[192:195], a[196:199], v[192:195], a[192:195]\n"
      "s_add_u32 m0, s62, 0\n"
      "v_exp_f32 v113, v161\n"
      "v_mfma_f32_16x16x32_bf16 v[84:87], a[116:119], v[212:215], v[84:87]\n"
      "buffer_load_dwordx4 v248, s[20:23], s60 offen nt lds\n"
      "v_cvt_pk_bf16_f32 v208, v112, v113\n"
      "v_xor_b32 v243, 0x10000, v243\n"
      "v_mfma_f32_16x16x32_bf16 v[68:71], a[112:115], v[212:215], v[68:71]\n"
      "s_add_u32 m0, s62, 896\n"
      "v_exp_f32 v114, v162\n"
      "v_mfma_f32_16x16x32_bf16 a[192:195], a[200:203], v[196:199], a[192:195]\n"
      "buffer_load_dwordx4 v248, s[20:23], s60 offen offset:128 nt lds\n"
      "v_xor_b32 v244, 0x10000, v244\n"
      "v_mfma_f32_16x16x32_bf16 a[148:151], a[108:111], v[212:215], a[148:151]\n"
      "s_add_u32 m0, s62, 8192\n"
      "v_exp_f32 v115, v163\n"
      "v_mfma_f32_16x16x32_bf16 a[132:135], a[104:107], v[212:215], a[132:135]\n"
      "s_memrealtime s[68:69]\n"
      "buffer_load_dwordx4 v120, s[20:23], s60 offen nt lds\n"
      "v_cvt_pk_bf16_f32 v209, v114, v115\n"
      "v_xor_b32 v245, 0x10000, v245\n"
      "v_mfma_f32_16x16x32_bf16 a[192:195], a[204:207], v[200:203], a[192:195]\n"
      "s_add_u32 m0, s62, 9088\n"
      "v_exp_f32 v112, v164\n"
      "v_mfma_f32_16x16x32_bf16 v[52:55], a[100:103], v[212:215], v[52:55]\n"
      "s_add_u32 s62, s62, 0x8000\n"
      "buffer_load_dwordx4 v120, s[20:23], s60 offen offset:128 nt lds\n"
      "v_or_b32 v116, v208, v209\n"
      "v_xor_b32 v246, 0x10000, v246\n"
      "v_mfma_f32_16x16x32_bf16 v[36:39], a[96:99], v[212:215], v[36:39]\n"
      "s_and_b32 s62, s62, 0x1ffff\n"
      "s_add_u32 s60, s60, %[inc]\n"
      "v_exp_f32 v113, v165\n"
      "v_mfma_f32_16x16x32_bf16 a[192:195], a[208:211], v[204:207], a[192:195]\n"
      "s_sub_u32 s63, s63, 1\n"
      "v_cvt_pk_bf16_f32 v210, v112, v113\n"
      "v_xor_b32 v247, 0x10000, v247\n"
      "s_cbranch_scc0 LOOP1_%=\n"
      "v_readfirstlane_b32 s72, v240\n"
      "v_readfirstlane_b32 s79, v241\n"
      "s_waitcnt vmcnt(12) lgkmcnt(0)\n"
      "s_sub_u32 s67, s68, s90\n"
      "s_cmp_lt_i32 s67, 0\n"
      "s_cbranch_scc1 GW2_%=\n"
      "GX2_%=:\n"
      "s_barrier\n"
      "v_mfma_f32_16x16x32_bf16 v[40:43], a[96:99], v[216:219], v[40:43]\n"
      "s_memrealtime s[68:69]\n"
      "buffer_load_dword v250, v251, s[28:31], 0 offen sc1\n"
      "ds_read_b128 a[64:67], v242 offset:0\n"
      "v_exp_f32 v114, v166\n"
      "v_mfma_f32_16x16x32_bf16 v[44:47], a[96:99], v[220:223], v[44:47]\n"
      "s_add_u32 m0, s77, 16384\n"
      "ds_read_b128 a[68:71], v242 offset:4096\n"
      "ds_read_b128 a[72:75], v243 offset:0\n"
      "v_mfma_f32_16x16x32_bf16 v[60:63], a[100:103], v[220:223], v[60:63]\n"
      "buffer_load_dwordx4 v249, s[24:27], s61 offen nt lds\n"
      "v_exp_f32 v115, v167\n"
      "v_mfma_f32_16x16x32_bf16 v[56:59], a[100:103], v[216:219], v[56:59]\n"
      "s_add_u32 m0, s77, 17408\n"
      "ds_read_b128 a[76:79], v243 offset:4096\n"
      "v_cvt_pk_bf16_f32 v211, v114, v115\n"
      "v_mfma_f32_16x16x32_bf16 a[136:139], a[104:107], v[216:219], a[136:139]\n"
      "buffer_load_dwordx4 v121, s[24:27], s61 offen nt lds\n"
      "v_exp_f32 v112, v168\n"
      "v_mfma_f32_16x16x32_bf16 a[140:143], a[104:107], v[220:223], a[140:143]\n"
      "s_add_u32 m0, s77, 24576\n"
      "ds_read_b128 a[80:83], v242 offset:1024\n"
      "v_or3_b32 v116, v116, v210, v211\n"
      "v_mfma_f32_16x16x32_bf16 a[156:159], a[108:111], v[220:223], a[156:159]\n"
      "buffer_load_dwordx4 v122, s[24:27], s61 offen nt lds\n"
      "v_exp_f32 v113, v169\n"
      "v_mfma_f32_16x16x32_bf16 a[152:155], a[108:111], v[216:219], a[152:155]\n"
      "s_add_u32 m0, s77, 25600\n"
      "ds_read_b128 a[84:87], v242 offset:5120\n"
      "v_cvt_pk_bf16_f32 v212, v112, v113\n"
      "v_mfma_f32_16x16x32_bf16 v[72:75], a[112:115], v[216:219], v[72:75]\n"
      "s_add_u32 s77, s77, 0x8000\n"
      "buffer_load_dwordx4 v123, s[24:27], s61 offen nt lds\n"
      "v_exp_f32 v114, v170\n"
      "v_mfma_f32_16x16x32_bf16 v[76:79], a[112:115], v[220:223], v[76:79]\n"
      "s_and_b32 s77, s77, 0x1ffff\n"
      "ds_read_b128 a[88:91], v243 offset:1024\n"
      "ds_read_b128 a[92:95], v243 offset:5120\n"
      "v_mfma_f32_16x16x32_bf16 v[92:95], a[116:119], v[220:223], v[92:95]\n"
      "s_add_u32 s61, s61, %[inc]\n"
      "v_exp_f32 v115, v171\n"
      "v_mfma_f32_16x16x32_bf16 v[88:91], a[116:119], v[216:219], v[88:91]\n"
      "v_cvt_pk_bf16_f32 v213, v114, v115\n"
      "v_mfma_f32_16x16x32_bf16 a[168:171], a[120:123], v[216:219], a[168:171]\n"
      "v_exp_f32 v112, v172\n"
      "v_mfma_f32_16x16x32_bf16 a[172:175], a[120:123], v[220:223], a[172:175]\n"
      "v_or3_b32 v116, v116, v212, v213\n"
      "v_mfma_f32_16x16x32_bf16 a[188:191], a[124:127], v[220:223], a[188:191]\n"
      "v_exp_f32 v113, v173\n"
      "v_mfma_f32_16x16x32_bf16 a[184:187], a[124:127], v[216:219], a[184:187]\n"
      "s_waitcnt lgkmcnt(0)\n"
      "v_cvt_pk_bf16_f32 v214, v112, v113\n"
      "v_mfma_f32_16x16x32_bf16 v[128:131], a[64:67], a[0:3], v[224:227]\n"
      "s_sub_u32 s67, s68, s66\n"
      "v_exp_f32 v114, v174\n"
      "v_mfma_f32_16x16x32_bf16 v[128:131], a[72:75], a[16:19], v[128:131]\n"
      "s_cmp_gt_i32 s67, s70\n"
      "ds_read_b64_tr_b16 a[96:97], v244 offset:0\n"
      "ds_read_b64_tr_b16 a[98:99], v244 offset:4096\n"
      "v_mfma_f32_16x16x32_bf16 v[128:131], a[80:83], a[32:35], v[128:131]\n"
      "s_cselect_b32 s69, s86, 0\n"
      "v_exp_f32 v115, v175\n"
      "v_mfma_f32_16x16x32_bf16 v[128:131], a[88:91], a[48:51], v[128:131]\n"
      "s_add_u32 s66, s66, s69\n"
      "ds_read_b64_tr_b16 a[100:101], v245 offset:0\n"
      "v_cvt_pk_bf16_f32 v215, v114, v115\n"
      "v_mfma_f32_16x16x32_bf16 v[132:135], a[92:95], a[48:51], v[224:227]\n"
      "s_add_u32 s67, s67, s101\n"
      "v_exp_f32 v112, v176\n"
      "v_mfma_f32_16x16x32_bf16 v[132:135], a[84:87], a[32:35], v[132:135]\n"
      "s_min_i32 s70, s70, s67\n"
      "ds_read_b64_tr_b16 a[102:103], v245 offset:4096\n"
      "v_or3_b32 v116, v116, v214, v215\n"
      "v_mfma_f32_16x16x32_bf16 v[132:135], a[76:79], a[16:19], v[132:135]\n"
      "s_max_i32 s70, s70, 85\n"
      "v_exp_f32 v113, v177\n"
      "v_mfma_f32_16x16x32_bf16 v[132:135], a[68:71], a[0:3], v[132:135]\n"
      "s_add_u32 s66, s66, s86\n"
      "ds_read_b64_tr_b16 a[104:105], v246 offset:0\n"
      "v_cvt_pk_bf16_f32 v216, v112, v113\n"
      "v_mfma_f32_16x16x32_bf16 v[140:143], a[68:71], a[4:7], v[228:231]\n"
      "s_and_b32 s76, s80, 31\n"
      "v_exp_f32 v114, v178\n"
      "v_mfma_f32_16x16x32_bf16 v[140:143], a[76:79], a[20:23], v[140:143]\n"
      "s_lshr_b64 s[80:81], s[80:81], 5\n"
      "ds_read_b64_tr_b16 a[106:107], v246 offset:4096\n"
      "ds_read_b64_tr_b16 a[108:109], v247 offset:0\n"
      "v_mfma_f32_16x16x32_bf16 v[140:143], a[84:87], a[36:39], v[140:143]\n"
      "s_add_u32 s66, s66, s76\n"
      "v_exp_f32 v115, v179\n"
      "v_mfma_f32_16x16x32_bf16 v[140:143], a[92:95], a[52:55], v[140:143]\n"
      "s_sub_u32 s67, s68, s82\n"
      "ds_read_b64_tr_b16 a[110:111], v247 offset:4096\n"
      "v_cvt_pk_bf16_f32 v217, v114, v115\n"
      "v_mfma_f32_16x16x32_bf16 v[136:139], a[88:91], a[52:55], v[228:231]\n"
      "s_min_i32 s76, s67, s83\n"
      "v_exp_f32 v112, v180\n"
      "v_mfma_f32_16x16x32_bf16 v[136:139], a[80:83], a[36:39], v[136:139]\n"
      "s_mov_b32 s83, s67\n"
      "ds_read_b64_tr_b16 a[112:113], v244 offset:1024\n"
      "v_or3_b32 v116, v116, v216, v217\n"
      "v_mfma_f32_16x16x32_bf16 v[136:139], a[72:75], a[20:23], v[136:139]\n"
      "s_sub_u32 s76, s76, 36\n"
      "v_exp_f32 v113, v181\n"
      "v_mfma_f32_16x16x32_bf16 v[136:139], a[64:67], a[4:7], v[136:139]\n"
      "s_min_i32 s76, s76, 148\n"
      "ds_read_b64_tr_b16 a[114:115], v244 offset:5120\n"
      "v_cvt_pk_bf16_f32 v218, v112, v113\n"
      "v_mfma_f32_16x16x32_bf16 v[144:147], a[64:67], a[8:11], v[232:235]\n"
      "s_max_i32 s76, s76, 70\n"
      "v_exp_f32 v114, v182\n"
      "v_mfma_f32_16x16x32_bf16 v[144:147], a[72:75], a[24:27], v[144:147]\n"
      "s_mov_b32 s82, s68\n"
      "ds_read_b64_tr_b16 a[116:117], v245 offset:1024\n"
      "ds_read_b64_tr_b16 a[118:119], v245 offset:5120\n"
      "v_mfma_f32_16x16x32_bf16 v[144:147], a[80:83], a[40:43], v[144:147]\n"
      "s_lshr_b32 s69, s86, 1\n"
      "v_exp_f32 v115, v183\n"
      "v_mfma_f32_16x16x32_bf16 v[144:147], a[88:91], a[56:59], v[144:147]\n"
      "s_add_u32 s66, s66, s69\n"
      "ds_read_b64_tr_b16 a[120:121], v246 offset:1024\n"
      "v_cvt_pk_bf16_f32 v219, v114, v115\n"
      "v_mfma_f32_16x16x32_bf16 v[148:151], a[92:95], a[56:59], v[232:235]\n"
      "s_add_u32 s82, s82, s69\n"
      "v_exp_f32 v112, v184\n"
      "v_mfma_f32_16x16x32_bf16 v[148:151], a[84:87], a[40:43], v[148:151]\n"
      "s_add_u32 s65, s82, s76\n"
      "ds_read_b64_tr_b16 a[122:123], v246 offset:5120\n"
      "v_or3_b32 v116, v116, v218, v219\n"
      "v_mfma_f32_16x16x32_bf16 v[148:151], a[76:79], a[24:27], v[148:151]\n"
      "s_sub_u32 s93, s93, 1\n"
      "v_exp_f32 v113, v185\n"
      "v_mfma_f32_16x16x32_bf16 v[148:151], a[68:71], a[8:11], v[148:151]\n"
      "s_cmp_le_i32 s93, 14\n"
      "ds_read_b64_tr_b16 a[124:125], v247 offset:1024\n"
      "v_cvt_pk_bf16_f32 v220, v112, v113\n"
      "v_mfma_f32_16x16x32_bf16 v[156:159], a[68:71], a[12:15], v[236:239]\n"
      "s_cselect_b32 s86, 107, s86\n"
      "v_exp_f32 v114, v186\n"
      "v_mfma_f32_16x16x32_bf16 v[156:159], a[76:79], a[28:31], v[156:159]\n"
      "s_sub_u32 s67, s65, s66\n"
      "ds_read_b64_tr_b16 a[126:127], v247 offset:5120\n"
      "v_mfma_f32_16x16x32_bf16 v[156:159], a[84:87], a[44:47], v[156:159]\n"
      "s_cmp_lt_i32 s67, 0\n"
      "v_exp_f32 v115, v187\n"
      "v_mfma_f32_16x16x32_bf16 v[156:159], a[92:95], a[60:63], v[156:159]\n"
      "s_cselect_b32 s88, s66, s65\n"
      "v_cvt_pk_bf16_f32 v221, v114, v115\n"
      "v_mfma_f32_16x16x32_bf16 v[152:155], a[88:91], a[60:63], v[236:239]\n"
      "s_add_u32 s90, s88, s91\n"
      "v_exp_f32 v112, v188\n"
      "v_mfma_f32_16x16x32_bf16 v[152:155], a[80:83], a[44:47], v[152:155]\n"
      "v_or3_b32 v116, v116, v220, v221\n"
      "v_mfma_f32_16x16x32_bf16 v[152:155], a[72:75], a[28:31], v[152:155]\n"
      "v_exp_f32 v113, v189\n"
      "v_mfma_f32_16x16x32_bf16 v[152:155], a[64:67], a[12:15], v[152:155]\n"
      "s_waitcnt lgkmcnt(10)\n"
      "v_cvt_pk_bf16_f32 v222, v112, v113\n"
      "v_mfma_f32_16x16x32_bf16 v[32:35], a[96:99], v[192:195], v[32:35]\n"
      "v_exp_f32 v114, v190\n"
      "v_mfma_f32_16x16x32_bf16 v[48:51], a[100:103], v[192:195], v[48:51]\n"
      "v_mfma_f32_16x16x32_bf16 a[128:131], a[104:107], v[192:195], a[128:131]\n"
      "s_waitcnt lgkmcnt(6)\n"
      "v_exp_f32 v115, v191\n"
      "v_mfma_f32_16x16x32_bf16 a[144:147], a[108:111], v[192:195], a[144:147]\n"
      "v_cvt_pk_bf16_f32 v223, v114, v115\n"
      "v_mfma_f32_16x16x32_bf16 v[64:67], a[112:115], v[192:195], v[64:67]\n"
      "s_waitcnt lgkmcnt(2)\n"
      "v_mfma_f32_16x16x32_bf16 v[80:83], a[116:119], v[192:195], v[80:83]\n"
      "v_or3_b32 v116, v116, v222, v223\n"
      "v_mfma_f32_16x16x32_bf16 a[160:163], a[120:123], v[192:195], a[160:163]\n"
      "s_mov_b32 s64, 1\n"
      "s_waitcnt lgkmcnt(0)\n"
      "v_and_b32 v116, 0x40004000, v116\n"
      "v_mfma_f32_16x16x32_bf16 a[176:179], a[124:127], v[192:195], a[176:179]\n"
      "v_cmp_ne_u32 vcc, 0, v116\n"
      "s_cbranch_vccnz FB1_%=\n"
      "RET1_1_%=:\n"
      "v_mfma_f32_16x16x32_bf16 a[180:183], a[124:127], v[196:199], a[180:183]\n"
      "v_exp_f32 v112, v128\n"
      "s_barrier\n"
      "v_mfma_f32_16x16x32_bf16 a[164:167], a[120:123], v[196:199], a[164:167]\n"
      "v_mfma_f32_16x16x32_bf16 a[192:195], a[196:199], v[208:211], a[192:195]\n"
      "s_add_u32 m0, s62, 0\n"
      "v_exp_f32 v113, v129\n"
      "v_mfma_f32_16x16x32_bf16 v[84:87], a[116:119], v[196:199], v[84:87]\n"
      "buffer_load_dwordx4 v248, s[20:23], s60 offen nt lds\n"
      "v_cvt_pk_bf16_f32 v192, v112, v113\n"
      "v_mfma_f32_16x16x32_bf16 v[68:71], a[112:115], v[196:199], v[68:71]\n"
      "s_add_u32 m0, s62, 896\n"
      "v_exp_f32 v114, v130\n"
      "v_mfma_f32_16x16x32_bf16 a[192:195], a[200:203], v[212:215], a[192:195]\n"
      "buffer_load_dwordx4 v248, s[20:23], s60 offen offset:128 nt lds\n"
      "v_mfma_f32_16x16x32_bf16 a[148:151], a[108:111], v[196:199], a[148:151]\n"
      "s_add_u32 m0, s62, 8192\n"
      "v_exp_f32 v115, v131\n"
      "v_mfma_f32_16x16x32_bf16 a[132:135], a[104:107], v[196:199], a[132:135]\n"
      "s_memrealtime s[68:69]\n"
      "buffer_load_dwordx4 v120, s[20:23], s60 offen nt lds\n"
      "v_cvt_pk_bf16_f32 v193, v114, v115\n"
      "v_mfma_f32_16x16x32_bf16 a[192:195], a[204:207], v[216:219], a[192:195]\n"
      "s_add_u32 m0, s62, 9088\n"
      "v_exp_f32 v112, v132\n"
      "v_mfma_f32_16x16x32_bf16 v[52:55], a[100:103], v[196:199], v[52:55]\n"
      "s_add_u32 s62, s62, 0x8000\n"
      "buffer_load_dwordx4 v120, s[20:23], s60 offen offset:128 nt lds\n"
      "v_or_b32 v116, v192, v193\n"
      "v_mfma_f32_16x16x32_bf16 v[36:39], a[96:99], v[196:199], v[36:39]\n"
      "s_and_b32 s62, s62, 0x1ffff\n"
      "v_exp_f32 v113, v133\n"
      "v_mfma_f32_16x16x32_bf16 a[192:195], a[208:211], v[220:223], a[192:195]\n"
      "s_add_u32 s60, s60, s78\n"
      "v_cvt_pk_bf16_f32 v194, v112, v113\n"
      "s_waitcnt vmcnt(12) lgkmcnt(0)\n"
      "s_barrier\n"
      "v_mfma_f32_16x16x32_bf16 v[40:43], a[96:99], v[200:203], v[40:43]\n"
      "ds_read_b128 a[64:67], v242 offset:32768\n"
      "v_exp_f32 v114, v134\n"
      "v_mfma_f32_16x16x32_bf16 v[44:47], a[96:99], v[204:207], v[44:47]\n"
      "ds_read_b128 a[68:71], v242 offset:36864\n"
      "ds_read_b128 a[72:75], v243 offset:32768\n"
      "v_mfma_f32_16x16x32_bf16 v[60:63], a[100:103], v[204:207], v[60:63]\n"
      "v_exp_f32 v115, v135\n"
      "v_mfma_f32_16x16x32_bf16 v[56:59], a[100:103], v[200:203], v[56:59]\n"
      "ds_read_b128 a[76:79], v243 offset:36864\n"
      "v_cvt_pk_bf16_f32 v195, v114, v115\n"
      "v_mfma_f32_16x16x32_bf16 a[136:139], a[104:107], v[200:203], a[136:139]\n"
      "v_exp_f32 v112, v136\n"
      "v_mfma_f32_16x16x32_bf16 a[140:143], a[104:107], v[204:207], a[140:143]\n"
      "ds_read_b128 a[80:83], v242 offset:33792\n"
      "v_or3_b32 v116, v116, v194, v195\n"
      "v_mfma_f32_16x16x32_bf16 a[156:159], a[108:111], v[204:207], a[156:159]\n"
      "v_exp_f32 v113, v137\n"
      "v_mfma_f32_16x16x32_bf16 a[152:155], a[108:111], v[200:203], a[152:155]\n"
      "ds_read_b128 a[84:87], v242 offset:37888\n"
      "v_cvt_pk_bf16_f32 v196, v112, v113\n"
      "v_mfma_f32_16x16x32_bf16 v[72:75], a[112:115], v[200:203], v[72:75]\n"
      "v_exp_f32 v114, v138\n"
      "v_mfma_f32_16x16x32_bf16 v[76:79], a[112:115], v[204:207], v[76:79]\n"
      "ds_read_b128 a[88:91], v243 offset:33792\n"
      "ds_read_b128 a[92:95], v243 offset:37888\n"
      "v_mfma_f32_16x16x32_bf16 v[92:95], a[116:119], v[204:207], v[92:95]\n"
      "v_exp_f32 v115, v139\n"
      "v_mfma_f32_16x16x32_bf16 v[88:91], a[116:119], v[200:203], v[88:91]\n"
      "v_cvt_pk_bf16_f32 v197, v114, v115\n"
      "v_mfma_f32_16x16x32_bf16 a[168:171], a[120:123], v[200:203], a[168:171]\n"
      "v_exp_f32 v112, v140\n"
      "v_mfma_f32_16x16x32_bf16 a[172:175], a[120:123], v[204:207], a[172:175]\n"
      "v_or3_b32 v116, v116, v196, v197\n"
      "v_mfma_f32_16x16x32_bf16 a[188:191], a[124:127], v[204:207], a[188:191]\n"
      "v_exp_f32 v113, v141\n"
      "v_mfma_f32_16x16x32_bf16 a[184:187], a[124:127], v[200:203], a[184:187]\n"
      "s_waitcnt lgkmcnt(0)\n"
      "v_cvt_pk_bf16_f32 v198, v112, v113\n"
      "v_mfma_f32_16x16x32_bf16 v[160:163], a[64:67], a[0:3], v[224:227]\n"
      "v_exp_f32 v114, v142\n"
      "v_mfma_f32_16x16x32_bf16 v[160:163], a[72:75], a[16:19], v[160:163]\n"
      "ds_read_b64_tr_b16 a[96:97], v244 offset:32768\n"
      "ds_read_b64_tr_b16 a[98:99], v244 offset:36864\n"
      "v_mfma_f32_16x16x32_bf16 v[160:163], a[80:83], a[32:35], v[160:163]\n"
      "v_exp_f32 v115, v143\n"
      "v_mfma_f32_16x16x32_bf16 v[160:163], a[88:91], a[48:51], v[160:163]\n"
      "ds_read_b64_tr_b16 a[100:101], v245 offset:32768\n"
      "v_cvt_pk_bf16_f32 v199, v114, v115\n"
      "v_mfma_f32_16x16x32_bf16 v[164:167], a[92:95], a[48:51], v[224:227]\n"
      "v_exp_f32 v112, v144\n"
      "v_mfma_f32_16x16x32_bf16 v[164:167], a[84:87], a[32:35], v[164:167]\n"
      "ds_read_b64_tr_b16 a[102:103], v245 offset:36864\n"
      "v_or3_b32 v116, v116, v198, v199\n"
      "v_mfma_f32_16x16x32_bf16 v[164:167], a[76:79], a[16:19], v[164:167]\n"
      "v_exp_f32 v113, v145\n"
      "v_mfma_f32_16x16x32_bf16 v[164:167], a[68:71], a[0:3], v[164:167]\n"
      "ds_read_b64_tr_b16 a[104:105], v246 offset:32768\n"
      "v_cvt_pk_bf16_f32 v200, v112, v113\n"
      "v_mfma_f32_16x16x32_bf16 v[172:175], a[68:71], a[4:7], v[228:231]\n"
      "v_exp_f32 v114, v146\n"
      "v_mfma_f32_16x16x32_bf16 v[172:175], a[76:79], a[20:23], v[172:175]\n"
      "ds_read_b64_tr_b16 a[106:107], v246 offset:36864\n"
      "ds_read_b64_tr_b16 a[108:109], v247 offset:32768\n"
      "v_mfma_f32_16x16x32_bf16 v[172:175], a[84:87], a[36:39], v[172:175]\n"
      "v_exp_f32 v115, v147\n"
      "v_mfma_f32_16x16x32_bf16 v[172:175], a[92:95], a[52:55], v[172:175]\n"
      "ds_read_b64_tr_b16 a[110:111], v247 offset:36864\n"
      "v_cvt_pk_bf16_f32 v201, v114, v115\n"
      "v_mfma_f32_16x16x32_bf16 v[168:171], a[88:91], a[52:55], v[228:231]\n"
      "v_exp_f32 v112, v148\n"
      "v_mfma_f32_16x16x32_bf16 v[168:171], a[80:83], a[36:39], v[168:171]\n"
      "ds_read_b64_tr_b16 a[112:113], v244 offset:33792\n"
      "v_or3_b32 v116, v116, v200, v201\n"
      "v_mfma_f32_16x16x32_bf16 v[168:171], a[72:75], a[20:23], v[168:171]\n"
      "v_exp_f32 v113, v149\n"
      "v_mfma_f32_16x16x32_bf16 v[168:171], a[64:67], a[4:7], v[168:171]\n"
      "ds_read_b64_tr_b16 a[114:115], v244 offset:37888\n"
      "v_cvt_pk_bf16_f32 v202, v112, v113\n"
      "v_mfma_f32_16x16x32_bf16 v[176:179], a[64:67], a[8:11], v[232:235]\n"
      "v_exp_f32 v114, v150\n"
      "v_mfma_f32_16x16x32_bf16 v[176:179], a[72:75], a[24:27], v[176:179]\n"
      "ds_read_b64_tr_b16 a[116:117], v245 offset:33792\n"
      "ds_read_b64_tr_b16 a[118:119], v245 offset:37888\n"
      "v_mfma_f32_16x16x32_bf16 v[176:179], a[80:83], a[40:43], v[176:179]\n"
      "v_exp_f32 v115, v151\n"
      "v_mfma_f32_16x16x32_bf16 v[176:179], a[88:91], a[56:59], v[176:179]\n"
      "ds_read_b64_tr_b16 a[120:121], v246 offset:33792\n"
      "v_cvt_pk_bf16_f32 v203, v114, v115\n"
      "v_mfma_f32_16x16x32_bf16 v[180:183], a[92:95], a[56:59], v[232:235]\n"
      "v_exp_f32 v112, v152\n"
      "v_mfma_f32_16x16x32_bf16 v[180:183], a[84:87], a[40:43], v[180:183]\n"
      "ds_read_b64_tr_b16 a[122:123], v246 offset:37888\n"
      "v_or3_b32 v116, v116, v202, v203\n"
      "v_mfma_f32_16x16x32_bf16 v[180:183], a[76:79], a[24:27], v[180:183]\n"
      "v_exp_f32 v113, v153\n"
      "v_mfma_f32_16x16x32_bf16 v[180:183], a[68:71], a[8:11], v[180:183]\n"
      "ds_read_b64_tr_b16 a[124:125], v247 offset:33792\n"
      "v_cvt_pk_bf16_f32 v204, v112, v113\n"
      "v_mfma_f32_16x16x32_bf16 v[188:191], a[68:71], a[12:15], v[236:239]\n"
      "v_exp_f32 v114, v154\n"
      "v_mfma_f32_16x16x32_bf16 v[188:191], a[76:79], a[28:31], v[188:191]\n"
      "ds_read_b64_tr_b16 a[126:127], v247 offset:37888\n"
      "v_mfma_f32_16x16x32_bf16 v[188:191], a[84:87], a[44:47], v[188:191]\n"
      "v_exp_f32 v115, v155\n"
      "v_mfma_f32_16x16x32_bf16 v[188:191], a[92:95], a[60:63], v[188:191]\n"
      "v_cvt_pk_bf16_f32 v205, v114, v115\n"
      "v_mfma_f32_16x16x32_bf16 v[184:187], a[88:91], a[60:63], v[236:239]\n"
      "v_exp_f32 v112, v156\n"
      "v_mfma_f32_16x16x32_bf16 v[184:187], a[80:83], a[44:47], v[184:187]\n"
      "v_or3_b32 v116, v116, v204, v205\n"
      "v_mfma_f32_16x16x32_bf16 v[184:187], a[72:75], a[28:31], v[184:187]\n"
      "v_exp_f32 v113, v157\n"
      "v_mfma_f32_16x16x32_bf16 v[184:187], a[64:67], a[12:15], v[184:187]\n"
      "s_waitcnt lgkmcnt(10)\n"
      "v_cvt_pk_bf16_f32 v206, v112, v113\n"
      "v_mfma_f32_16x16x32_bf16 v[32:35], a[96:99], v[208:211], v[32:35]\n"
      "v_exp_f32 v114, v158\n"
      "v_mfma_f32_16x16x32_bf16 v[48:51], a[100:103], v[208:211], v[48:51]\n"
      "v_mfma_f32_16x16x32_bf16 a[128:131], a[104:107], v[208:211], a[128:131]\n"
      "s_waitcnt lgkmcnt(6)\n"
      "v_exp_f32 v115, v159\n"
      "v_mfma_f32_16x16x32_bf16 a[144:147], a[108:111], v[208:211], a[144:147]\n"
      "v_cvt_pk_bf16_f32 v207, v114, v115\n"
      "v_mfma_f32_16x16x32_bf16 v[64:67], a[112:115], v[208:211], v[64:67]\n"
      "s_waitcnt lgkmcnt(2)\n"
      "v_mfma_f32_16x16x32_bf16 v[80:83], a[116:119], v[208:211], v[80:83]\n"
      "v_or3_b32 v116, v116, v206, v207\n"
      "v_mfma_f32_16x16x32_bf16 a[160:163], a[120:123], v[208:211], a[160:163]\n"
      "s_mov_b32 s64, 1\n"
      "s_waitcnt lgkmcnt(0)\n"
      "v_and_b32 v116, 0x40004000, v116\n"
      "v_mfma_f32_16x16x32_bf16 a[176:179], a[124:127], v[208:211], a[176:179]\n"
      "v_cmp_ne_u32 vcc, 0, v116\n"
      "s_cbranch_vccnz FB0_%=\n"
      "RET0_1_%=:\n"
      "v_mfma_f32_16x16x32_bf16 a[180:183], a[124:127], v[212:215], a[180:183]\n"
      "v_exp_f32 v112, v160\n"
      "v_mfma_f32_16x16x32_bf16 a[164:167], a[120:123], v[212:215], a[164:167]\n"
      "v_xor_b32 v242, 0x10000, v242\n"
      "v_mfma_f32_16x16x32_bf16 a[192:195], a[196:199], v[192:195], a[192:195]\n"
      "s_add_u32 m0, s77, 16384\n"
      "v_exp_f32 v113, v161\n"
      "v_mfma_f32_16x16x32_bf16 v[84:87], a[116:119], v[212:215], v[84:87]\n"
      "buffer_load_dwordx4 v249, s[24:27], s61 offen nt lds\n"
      "v_cvt_pk_bf16_f32 v208, v112, v113\n"
      "v_xor_b32 v243, 0x10000, v243\n"
      "v_mfma_f32_16x16x32_bf16 v[68:71], a[112:115], v[212:215], v[68:71]\n"
      "s_add_u32 m0, s77, 17408\n"
      "v_exp_f32 v114, v162\n"
      "v_mfma_f32_16x16x32_bf16 a[192:195], a[200:203], v[196:199], a[192:195]\n"
      "buffer_load_dwordx4 v121, s[24:27], s61 offen nt lds\n"
      "v_xor_b32 v244, 0x10000, v244\n"
      "v_mfma_f32_16x16x32_bf16 a[148:151], a[108:111], v[212:215], a[148:151]\n"
      "s_add_u32 m0, s77, 24576\n"
      "v_exp_f32 v115, v163\n"
      "v_mfma_f32_16x16x32_bf16 a[132:135], a[104:107], v[212:215], a[132:135]\n"
      "s_memrealtime s[68:69]\n"
      "buffer_load_dwordx4 v122, s[24:27], s61 offen nt lds\n"
      "v_cvt_pk_bf16_f32 v209, v114, v115\n"
      "v_xor_b32 v245, 0x10000, v245\n"
      "v_mfma_f32_16x16x32_bf16 a[192:195], a[204:207], v[200:203], a[192:195]\n"
      "s_add_u32 m0, s77, 25600\n"
      "v_exp_f32 v112, v164\n"
      "v_mfma_f32_16x16x32_bf16 v[52:55], a[100:103], v[212:215], v[52:55]\n"
      "s_add_u32 s77, s77, 0x8000\n"
      "buffer_load_dwordx4 v123, s[24:27], s61 offen nt lds\n"
      "v_or_b32 v116, v208, v209\n"
      "v_xor_b32 v246, 0x10000, v246\n"
      "v_mfma_f32_16x16x32_bf16 v[36:39], a[96:99], v[212:215], v[36:39]\n"
      "s_and_b32 s77, s77, 0x1ffff\n"
      "v_exp_f32 v113, v165\n"
      "v_mfma_f32_16x16x32_bf16 a[192:195], a[208:211], v[204:207], a[192:195]\n"
      "s_add_u32 s61, s61, s78\n"
      "v_cvt_pk_bf16_f32 v210, v112, v113\n"
      "v_xor_b32 v247, 0x10000, v247\n"
      "s_waitcnt vmcnt(8) lgkmcnt(0)\n"
      "s_sub_u32 s67, s68, s90\n"
      "s_cmp_lt_i32 s67, 0\n"
      "s_cbranch_scc1 GW3_%=\n"
      "GX3_%=:\n"
      "s_barrier\n"
      "v_mfma_f32_16x16x32_bf16 v[40:43], a[96:99], v[216:219], v[40:43]\n"
      "s_memrealtime s[68:69]\n"
      "buffer_load_dword v250, v251, s[28:31], 0 offen sc1\n"
      "ds_read_b128 a[64:67], v242 offset:0\n"
      "v_exp_f32 v114, v166\n"
      "v_mfma_f32_16x16x32_bf16 v[44:47], a[96:99], v[220:223], v[44:47]\n"
      "s_add_u32 m0, s62, 0\n"
      "ds_read_b128 a[68:71], v242 offset:4096\n"
      "ds_read_b128 a[72:75], v243 offset:0\n"
      "v_mfma_f32_16x16x32_bf16 v[60:63], a[100:103], v[220:223], v[60:63]\n"
      "buffer_load_dwordx4 v248, s[20:23], s60 offen nt lds\n"
      "v_exp_f32 v115, v167\n"
      "v_mfma_f32_16x16x32_bf16 v[56:59], a[100:103], v[216:219], v[56:59]\n"
      "s_add_u32 m0, s62, 896\n"
      "ds_read_b128 a[76:79], v243 offset:4096\n"
      "v_cvt_pk_bf16_f32 v211, v114, v115\n"
      "v_mfma_f32_16x16x32_bf16 a[136:139], a[104:107], v[216:219], a[136:139]\n"
      "buffer_load_dwordx4 v248, s[20:23], s60 offen offset:128 nt lds\n"
      "v_exp_f32 v112, v168\n"
      "v_mfma_f32_16x16x32_bf16 a[140:143], a[104:107], v[220:223], a[140:143]\n"
      "s_add_u32 m0, s62, 8192\n"
      "ds_read_b128 a[80:83], v242 offset:1024\n"
      "v_or3_b32 v116, v116, v210, v211\n"
      "v_mfma_f32_16x16x32_bf16 a[156:159], a[108:111], v[220:223], a[156:159]\n"
      "buffer_load_dwordx4 v120, s[20:23], s60 offen nt lds\n"
      "v_exp_f32 v113, v169\n"
      "v_mfma_f32_16x16x32_bf16 a[152:155], a[108:111], v[216:219], a[152:155]\n"
      "s_add_u32 m0, s62, 9088\n"
      "ds_read_b128 a[84:87], v242 offset:5120\n"
      "v_cvt_pk_bf16_f32 v212, v112, v113\n"
      "v_mfma_f32_16x16x32_bf16 v[72:75], a[112:115], v[216:219], v[72:75]\n"
      "s_add_u32 s62, s62, 0x8000\n"
      "buffer_load_dwordx4 v120, s[20:23], s60 offen offset:128 nt lds\n"
      "v_exp_f32 v114, v170\n"
      "v_mfma_f32_16x16x32_bf16 v[76:79], a[112:115], v[220:223], v[76:79]\n"
      "s_and_b32 s62, s62, 0x1ffff\n"
      "ds_read_b128 a[88:91], v243 offset:1024\n"
      "ds_read_b128 a[92:95], v243 offset:5120\n"
      "v_mfma_f32_16x16x32_bf16 v[92:95], a[116:119], v[220:223], v[92:95]\n"
      "s_add_u32 s60, s60, %[inc]\n"
      "v_exp_f32 v115, v171\n"
      "v_mfma_f32_16x16x32_bf16 v[88:91], a[116:119], v[216:219], v[88:91]\n"
      "v_cvt_pk_bf16_f32 v213, v114, v115\n"
      "v_mfma_f32_16x16x32_bf16 a[168:171], a[120:123], v[216:219], a[168:171]\n"
      "v_exp_f32 v112, v172\n"
      "v_mfma_f32_16x16x32_bf16 a[172:175], a[120:123], v[220:223], a[172:175]\n"
      "v_or3_b32 v116, v116, v212, v213\n"
      "v_mfma_f32_16x16x32_bf16 a[188:191], a[124:127], v[220:223], a[188:191]\n"
      "v_exp_f32 v113, v173\n"
      "v_mfma_f32_16x16x32_bf16 a[184:187], a[124:127], v[216:219], a[184:187]\n"
      "s_waitcnt lgkmcnt(0)\n"
      "v_cvt_pk_bf16_f32 v214, v112, v113\n"
      "v_mfma_f32_16x16x32_bf16 v[128:131], a[64:67], a[0:3], v[224:227]\n"
      "s_sub_u32 s67, s68, s66\n"
      "v_exp_f32 v114, v174\n"
      "v_mfma_f32_16x16x32_bf16 v[128:131], a[72:75], a[16:19], v[128:131]\n"
      "s_cmp_gt_i32 s67, s70\n"
      "ds_read_b64_tr_b16 a[96:97], v244 offset:0\n"
      "ds_read_b64_tr_b16 a[98:99], v244 offset:4096\n"
      "v_mfma_f32_16x16x32_bf16 v[128:131], a[80:83], a[32:35], v[128:131]\n"
      "s_cselect_b32 s69, s86, 0\n"
      "v_exp_f32 v115, v175\n"
      "v_mfma_f32_16x16x32_bf16 v[128:131], a[88:91], a[48:51], v[128:131]\n"
      "s_add_u32 s66, s66, s69\n"
      "ds_read_b64_tr_b16 a[100:101], v245 offset:0\n"
      "v_cvt_pk_bf16_f32 v215, v114, v115\n"
      "v_mfma_f32_16x16x32_bf16 v[132:135], a[92:95], a[48:51], v[224:227]\n"
      "s_add_u32 s67, s67, s101\n"
      "v_exp_f32 v112, v176\n"
      "v_mfma_f32_16x16x32_bf16 v[132:135], a[84:87], a[32:35], v[132:135]\n"
      "s_min_i32 s70, s70, s67\n"
      "ds_read_b64_tr_b16 a[102:103], v245 offset:4096\n"
      "v_or3_b32 v116, v116, v214, v215\n"
      "v_mfma_f32_16x16x32_bf16 v[132:135], a[76:79], a[16:19], v[132:135]\n"
      "s_max_i32 s70, s70, 85\n"
      "v_exp_f32 v113, v177\n"
      "v_mfma_f32_16x16x32_bf16 v[132:135], a[68:71], a[0:3], v[132:135]\n"
      "s_add_u32 s66, s66, s86\n"
      "ds_read_b64_tr_b16 a[104:105], v246 offset:0\n"
      "v_cvt_pk_bf16_f32 v216, v112, v113\n"
      "v_mfma_f32_16x16x32_bf16 v[140:143], a[68:71], a[4:7], v[228:231]\n"
      "s_and_b32 s76, s80, 31\n"
      "v_exp_f32 v114, v178\n"
      "v_mfma_f32_16x16x32_bf16 v[140:143], a[76:79], a[20:23], v[140:143]\n"
      "s_lshr_b64 s[80:81], s[80:81], 5\n"
      "ds_read_b64_tr_b16 a[106:107], v246 offset:4096\n"
      "ds_read_b64_tr_b16 a[108:109], v247 offset:0\n"
      "v_mfma_f32_16x16x32_bf16 v[140:143], a[84:87], a[36:39], v[140:143]\n"
      "s_add_u32 s66, s66, s76\n"
      "v_exp_f32 v115, v179\n"
      "v_mfma_f32_16x16x32_bf16 v[140:143], a[92:95], a[52:55], v[140:143]\n"
      "s_sub_u32 s67, s68, s82\n"
      "ds_read_b64_tr_b16 a[110:111], v247 offset:4096\n"
      "v_cvt_pk_bf16_f32 v217, v114, v115\n"
      "v_mfma_f32_16x16x32_bf16 v[136:139], a[88:91], a[52:55], v[228:231]\n"
      "s_min_i32 s76, s67, s83\n"
      "v_exp_f32 v112, v180\n"
      "v_mfma_f32_16x16x32_bf16 v[136:139], a[80:83], a[36:39], v[136:139]\n"
      "s_mov_b32 s83, s67\n"
      "ds_read_b64_tr_b16 a[112:113], v244 offset:1024\n"
      "v_or3_b32 v116, v116, v216, v217\n"
      "v_mfma_f32_16x16x32_bf16 v[136:139], a[72:75], a[20:23], v[136:139]\n"
      "s_sub_u32 s76, s76, 36\n"
      "v_exp_f32 v113, v181\n"
      "v_mfma_f32_16x16x32_bf16 v[136:139], a[64:67], a[4:7], v[136:139]\n"
      "s_min_i32 s76, s76, 148\n"
      "ds_read_b64_tr_b16 a[114:115], v244 offset:5120\n"
      "v_cvt_pk_bf16_f32 v218, v112, v113\n"
      "v_mfma_f32_16x16x32_bf16 v[144:147], a[64:67], a[8:11], v[232:235]\n"
      "s_max_i32 s76, s76, 70\n"
      "v_exp_f32 v114, v182\n"
      "v_mfma_f32_16x16x32_bf16 v[144:147], a[72:75], a[24:27], v[144:147]\n"
      "s_mov_b32 s82, s68\n"
      "ds_read_b64_tr_b16 a[116:117], v245 offset:1024\n"
      "ds_read_b64_tr_b16 a[118:119], v245 offset:5120\n"
      "v_mfma_f32_16x16x32_bf16 v[144:147], a[80:83], a[40:43], v[144:147]\n"
      "s_lshr_b32 s69, s86, 1\n"
      "v_exp_f32 v115, v183\n"
      "v_mfma_f32_16x16x32_bf16 v[144:147], a[88:91], a[56:59], v[144:147]\n"
      "s_add_u32 s66, s66, s69\n"
      "ds_read_b64_tr_b16 a[120:121], v246 offset:1024\n"
      "v_cvt_pk_bf16_f32 v219, v114, v115\n"
      "v_mfma_f32_16x16x32_bf16 v[148:151], a[92:95], a[56:59], v[232:235]\n"
      "s_add_u32 s82, s82, s69\n"
      "v_exp_f32 v112, v184\n"
      "v_mfma_f32_16x16x32_bf16 v[148:151], a[84:87], a[40:43], v[148:151]\n"
      "s_add_u32 s65, s82, s76\n"
      "ds_read_b64_tr_b16 a[122:123], v246 offset:5120\n"
      "v_or3_b32 v116, v116, v218, v219\n"
      "v_mfma_f32_16x16x32_bf16 v[148:151], a[76:79], a[24:27], v[148:151]\n"
      "s_sub_u32 s93, s93, 1\n"
      "v_exp_f32 v113, v185\n"
      "v_mfma_f32_16x16x32_bf16 v[148:151], a[68:71], a[8:11], v[148:151]\n"
      "s_cmp_le_i32 s93, 14\n"
      "ds_read_b64_tr_b16 a[124:125], v247 offset:1024\n"
      "v_cvt_pk_bf16_f32 v220, v112, v113\n"
      "v_mfma_f32_16x16x32_bf16 v[156:159], a[68:71], a[12:15], v[236:239]\n"
      "s_cselect_b32 s86, 107, s86\n"
      "v_exp_f32 v114, v186\n"
      "v_mfma_f32_16x16x32_bf16 v[156:159], a[76:79], a[28:31], v[156:159]\n"
      "s_sub_u32 s67, s65, s66\n"
      "ds_read_b64_tr_b16 a[126:127], v247 offset:5120\n"
      "v_mfma_f32_16x16x32_bf16 v[156:159], a[84:87], a[44:47], v[156:159]\n"
      "s_cmp_lt_i32 s67, 0\n"
      "v_exp_f32 v115, v187\n"
      "v_mfma_f32_16x16x32_bf16 v[156:159], a[92:95], a[60:63], v[156:159]\n"
      "s_cselect_b32 s88, s66, s65\n"
      "v_cvt_pk_bf16_f32 v221, v114, v115\n"
      "v_mfma_f32_16x16x32_bf16 v[152:155], a[88:91], a[60:63], v[236:239]\n"
      "s_add_u32 s90, s88, s91\n"
      "v_exp_f32 v112, v188\n"
      "v_mfma_f32_16x16x32_bf16 v[152:155], a[80:83], a[44:47], v[152:155]\n"
      "v_or3_b32 v116, v116, v220, v221\n"
      "v_mfma_f32_16x16x32_bf16 v[152:155], a[72:75], a[28:31], v[152:155]\n"
      "v_exp_f32 v113, v189\n"
      "v_mfma_f32_16x16x32_bf16 v[152:155], a[64:67], a[12:15], v[152:155]\n"
      "s_waitcnt lgkmcnt(10)\n"
      "v_cvt_pk_bf16_f32 v222, v112, v113\n"
      "v_mfma_f32_16x16x32_bf16 v[32:35], a[96:99], v[192:195], v[32:35]\n"
      "v_exp_f32 v114, v190\n"
      "v_mfma_f32_16x16x32_bf16 v[48:51], a[100:103], v[192:195], v[48:51]\n"
      "v_mfma_f32_16x16x32_bf16 a[128:131], a[104:107], v[192:195], a[128:131]\n"
      "s_waitcnt lgkmcnt(6)\n"
      "v_exp_f32 v115, v191\n"
      "v_mfma_f32_16x16x32_bf16 a[144:147], a[108:111], v[192:195], a[144:147]\n"
      "v_cvt_pk_bf16_f32 v223, v114, v115\n"
      "v_mfma_f32_16x16x32_bf16 v[64:67], a[112:115], v[192:195], v[64:67]\n"
      "s_waitcnt lgkmcnt(2)\n"
      "v_mfma_f32_16x16x32_bf16 v[80:83], a[116:119], v[192:195], v[80:83]\n"
      "v_or3_b32 v116, v116, v222, v223\n"
      "v_mfma_f32_16x16x32_bf16 a[160:163], a[120:123], v[192:195], a[160:163]\n"
      "s_mov_b32 s64, 2\n"
      "s_waitcnt lgkmcnt(0)\n"
      "v_and_b32 v116, 0x40004000, v116\n"
      "v_mfma_f32_16x16x32_bf16 a[176:179], a[124:127], v[192:195], a[176:179]\n"
      "v_cmp_ne_u32 vcc, 0, v116\n"
      "s_cbranch_vccnz FB1_%=\n"
      "RET1_2_%=:\n"
      "v_mfma_f32_16x16x32_bf16 a[180:183], a[124:127], v[196:199], a[180:183]\n"
      "v_exp_f32 v112, v128\n"
      "v_mfma_f32_16x16x32_bf16 a[164:167], a[120:123], v[196:199], a[164:167]\n"
      "v_mfma_f32_16x16x32_bf16 a[192:195], a[196:199], v[208:211], a[192:195]\n"
      "s_add_u32 m0, s77, 16384\n"
      "v_exp_f32 v113, v129\n"
      "v_mfma_f32_16x16x32_bf16 v[84:87], a[116:119], v[196:199], v[84:87]\n"
      "buffer_load_dwordx4 v249, s[24:27], s61 offen nt lds\n"
      "v_cvt_pk_bf16_f32 v192, v112, v113\n"
      "v_mfma_f32_16x16x32_bf16 v[68:71], a[112:115], v[196:199], v[68:71]\n"
      "s_add_u32 m0, s77, 17408\n"
      "v_exp_f32 v114, v130\n"
      "v_mfma_f32_16x16x32_bf16 a[192:195], a[200:203], v[212:215], a[192:195]\n"
      "buffer_load_dwordx4 v121, s[24:27], s61 offen nt lds\n"
      "v_mfma_f32_16x16x32_bf16 a[148:151], a[108:111], v[196:199], a[148:151]\n"
      "s_add_u32 m0, s77, 24576\n"
      "v_exp_f32 v115, v131\n"
      "v_mfma_f32_16x16x32_bf16 a[132:135], a[104:107], v[196:199], a[132:135]\n"
      "s_memrealtime s[68:69]\n"
      "buffer_load_dwordx4 v122, s[24:27], s61 offen nt lds\n"
      "v_cvt_pk_bf16_f32 v193, v114, v115\n"
      "v_mfma_f32_16x16x32_bf16 a[192:195], a[204:207], v[216:219], a[192:195]\n"
      "s_add_u32 m0, s77, 25600\n"
      "v_exp_f32 v112, v132\n"
      "v_mfma_f32_16x16x32_bf16 v[52:55], a[100:103], v[196:199], v[52:55]\n"
      "s_add_u32 s77, s77, 0x8000\n"
      "buffer_load_dwordx4 v123, s[24:27], s61 offen nt lds\n"
      "v_or_b32 v116, v192, v193\n"
      "v_mfma_f32_16x16x32_bf16 v[36:39], a[96:99], v[196:199], v[36:39]\n"
      "s_and_b32 s77, s77, 0x1ffff\n"
      "v_exp_f32 v113, v133\n"
      "v_mfma_f32_16x16x32_bf16 a[192:195], a[208:211], v[220:223], a[192:195]\n"
      "s_add_u32 s61, s61, %[inc]\n"
      "v_cvt_pk_bf16_f32 v194, v112, v113\n"
      "s_waitcnt vmcnt(9) lgkmcnt(0)\n"
      "s_barrier\n"
      "v_mfma_f32_16x16x32_bf16 v[40:43], a[96:99], v[200:203], v[40:43]\n"
      "ds_read_b128 a[64:67], v242 offset:32768\n"
      "v_exp_f32 v114, v134\n"
      "v_mfma_f32_16x16x32_bf16 v[44:47], a[96:99], v[204:207], v[44:47]\n"
      "ds_read_b128 a[68:71], v242 offset:36864\n"
      "ds_read_b128 a[72:75], v243 offset:32768\n"
      "v_mfma_f32_16x16x32_bf16 v[60:63], a[100:103], v[204:207], v[60:63]\n"
      "v_exp_f32 v115, v135\n"
      "v_mfma_f32_16x16x32_bf16 v[56:59], a[100:103], v[200:203], v[56:59]\n"
      "ds_read_b128 a[76:79], v243 offset:36864\n"
      "v_cvt_pk_bf16_f32 v195, v114, v115\n"
      "v_mfma_f32_16x16x32_bf16 a[136:139], a[104:107], v[200:203], a[136:139]\n"
      "v_exp_f32 v112, v136\n"
      "v_mfma_f32_16x16x32_bf16 a[140:143], a[104:107], v[204:207], a[140:143]\n"
      "ds_read_b128 a[80:83], v242 offset:33792\n"
      "v_or3_b32 v116, v116, v194, v195\n"
      "v_mfma_f32_16x16x32_bf16 a[156:159], a[108:111], v[204:207], a[156:159]\n"
      "v_exp_f32 v113, v137\n"
      "v_mfma_f32_16x16x32_bf16 a[152:155], a[108:111], v[200:203], a[152:155]\n"
      "ds_read_b128 a[84:87], v242 offset:37888\n"
      "v_cvt_pk_bf16_f32 v196, v112, v113\n"
      "v_mfma_f32_16x16x32_bf16 v[72:75], a[112:115], v[200:203], v[72:75]\n"
      "v_exp_f32 v114, v138\n"
      "v_mfma_f32_16x16x32_bf16 v[76:79], a[112:115], v[204:207], v[76:79]\n"
      "ds_read_b128 a[88:91], v243 offset:33792\n"
      "ds_read_b128 a[92:95], v243 offset:37888\n"
      "v_mfma_f32_16x16x32_bf16 v[92:95], a[116:119], v[204:207], v[92:95]\n"
      "v_exp_f32 v115, v139\n"
      "v_mfma_f32_16x16x32_bf16 v[88:91], a[116:119], v[200:203], v[88:91]\n"
      "v_cvt_pk_bf16_f32 v197, v114, v115\n"
      "v_mfma_f32_16x16x32_bf16 a[168:171], a[120:123], v[200:203], a[168:171]\n"
      "v_exp_f32 v112, v140\n"
      "v_mfma_f32_16x16x32_bf16 a[172:175], a[120:123], v[204:207], a[172:175]\n"
      "v_or3_b32 v116, v116, v196, v197\n"
      "v_mfma_f32_16x16x32_bf16 a[188:191], a[124:127], v[204:207], a[188:191]\n"
      "v_exp_f32 v113, v141\n"
      "v_mfma_f32_16x16x32_bf16 a[184:187], a[124:127], v[200:203], a[184:187]\n"
      "s_waitcnt lgkmcnt(0)\n"
      "v_cvt_pk_bf16_f32 v198, v112, v113\n"
      "v_mfma_f32_16x16x32_bf16 v[160:163], a[64:67], a[0:3], v[224:227]\n"
      "v_exp_f32 v114, v142\n"
      "v_mfma_f32_16x16x32_bf16 v[160:163], a[72:75], a[16:19], v[160:163]\n"
      "ds_read_b64_tr_b16 a[96:97], v244 offset:32768\n"
      "ds_read_b64_tr_b16 a[98:99], v244 offset:36864\n"
      "v_mfma_f32_16x16x32_bf16 v[160:163], a[80:83], a[32:35], v[160:163]\n"
      "v_exp_f32 v115, v143\n"
      "v_mfma_f32_16x16x32_bf16 v[160:163], a[88:91], a[48:51], v[160:163]\n"
      "ds_read_b64_tr_b16 a[100:101], v245 offset:32768\n"
      "v_cvt_pk_bf16_f32 v199, v114, v115\n"
      "v_mfma_f32_16x16x32_bf16 v[164:167], a[92:95], a[48:51], v[224:227]\n"
      "v_exp_f32 v112, v144\n"
      "v_mfma_f32_16x16x32_bf16 v[164:167], a[84:87], a[32:35], v[164:167]\n"
      "ds_read_b64_tr_b16 a[102:103], v245 offset:36864\n"
      "v_or3_b32 v116, v116, v198, v199\n"
      "v_mfma_f32_16x16x32_bf16 v[164:167], a[76:79], a[16:19], v[164:167]\n"
      "v_exp_f32 v113, v145\n"
      "v_mfma_f32_16x16x32_bf16 v[164:167], a[68:71], a[0:3], v[164:167]\n"
      "ds_read_b64_tr_b16 a[104:105], v246 offset:32768\n"
      "v_cvt_pk_bf16_f32 v200, v112, v113\n"
      "v_mfma_f32_16x16x32_bf16 v[172:175], a[68:71], a[4:7], v[228:231]\n"
      "v_exp_f32 v114, v146\n"
      "v_mfma_f32_16x16x32_bf16 v[172:175], a[76:79], a[20:23], v[172:175]\n"
      "ds_read_b64_tr_b16 a[106:107], v246 offset:36864\n"
      "ds_read_b64_tr_b16 a[108:109], v247 offset:32768\n"
      "v_mfma_f32_16x16x32_bf16 v[172:175], a[84:87], a[36:39], v[172:175]\n"
      "v_exp_f32 v115, v147\n"
      "v_mfma_f32_16x16x32_bf16 v[172:175], a[92:95], a[52:55], v[172:175]\n"
      "ds_read_b64_tr_b16 a[110:111], v247 offset:36864\n"
      "v_cvt_pk_bf16_f32 v201, v114, v115\n"
      "v_mfma_f32_16x16x32_bf16 v[168:171], a[88:91], a[52:55], v[228:231]\n"
      "v_exp_f32 v112, v148\n"
      "v_mfma_f32_16x16x32_bf16 v[168:171], a[80:83], a[36:39], v[168:171]\n"
      "ds_read_b64_tr_b16 a[112:113], v244 offset:33792\n"
      "v_or3_b32 v116, v116, v200, v201\n"
      "v_mfma_f32_16x16x32_bf16 v[168:171], a[72:75], a[20:23], v[168:171]\n"
      "v_exp_f32 v113, v149\n"
      "v_mfma_f32_16x16x32_bf16 v[168:171], a[64:67], a[4:7], v[168:171]\n"
      "ds_read_b64_tr_b16 a[114:115], v244 offset:37888\n"
      "v_cvt_pk_bf16_f32 v202, v112, v113\n"
      "v_mfma_f32_16x16x32_bf16 v[176:179], a[64:67], a[8:11], v[232:235]\n"
      "v_exp_f32 v114, v150\n"
      "v_mfma_f32_16x16x32_bf16 v[176:179], a[72:75], a[24:27], v[176:179]\n"
      "ds_read_b64_tr_b16 a[116:117], v245 offset:33792\n"
      "ds_read_b64_tr_b16 a[118:119], v245 offset:37888\n"
      "v_mfma_f32_16x16x32_bf16 v[176:179], a[80:83], a[40:43], v[176:179]\n"
      "v_exp_f32 v115, v151\n"
      "v_mfma_f32_16x16x32_bf16 v[176:179], a[88:91], a[56:59], v[176:179]\n"
      "ds_read_b64_tr_b16 a[120:121], v246 offset:33792\n"
      "v_cvt_pk_bf16_f32 v203, v114, v115\n"
      "v_mfma_f32_16x16x32_bf16 v[180:183], a[92:95], a[56:59], v[232:235]\n"
      "v_exp_f32 v112, v152\n"
      "v_mfma_f32_16x16x32_bf16 v[180:183], a[84:87], a[40:43], v[180:183]\n"
      "ds_read_b64_tr_b16 a[122:123], v246 offset:37888\n"
      "v_or3_b32 v116, v116, v202, v203\n"
      "v_mfma_f32_16x16x32_bf16 v[180:183], a[76:79], a[24:27], v[180:183]\n"
      "v_exp_f32 v113, v153\n"
      "v_mfma_f32_16x16x32_bf16 v[180:183], a[68:71], a[8:11], v[180:183]\n"
      "ds_read_b64_tr_b16 a[124:125], v247 offset:33792\n"
      "v_cvt_pk_bf16_f32 v204, v112, v113\n"
      "v_mfma_f32_16x16x32_bf16 v[188:191], a[68:71], a[12:15], v[236:239]\n"
      "v_exp_f32 v114, v154\n"
      "v_mfma_f32_16x16x32_bf16 v[188:191], a[76:79], a[28:31], v[188:191]\n"
      "ds_read_b64_tr_b16 a[126:127], v247 offset:37888\n"
      "v_mfma_f32_16x16x32_bf16 v[188:191], a[84:87], a[44:47], v[188:191]\n"
      "v_exp_f32 v115, v155\n"
      "v_mfma_f32_16x16x32_bf16 v[188:191], a[92:95], a[60:63], v[188:191]\n"
      "v_cvt_pk_bf16_f32 v205, v114, v115\n"
      "v_mfma_f32_16x16x32_bf16 v[184:187], a[88:91], a[60:63], v[236:239]\n"
      "v_exp_f32 v112, v156\n"
      "v_mfma_f32_16x16x32_bf16 v[184:187], a[80:83], a[44:47], v[184:187]\n"
      "v_or3_b32 v116, v116, v204, v205\n"
      "v_mfma_f32_16x16x32_bf16 v[184:187], a[72:75], a[28:31], v[184:187]\n"
      "v_exp_f32 v113, v157\n"
      "v_mfma_f32_16x16x32_bf16 v[184:187], a[64:67], a[12:15], v[184:187]\n"
      "s_waitcnt lgkmcnt(10)\n"
      "v_cvt_pk_bf16_f32 v206, v112, v113\n"
      "v_mfma_f32_16x16x32_bf16 v[32:35], a[96:99], v[208:211], v[32:35]\n"
      "v_exp_f32 v114, v158\n"
      "v_mfma_f32_16x16x32_bf16 v[48:51], a[100:103], v[208:211], v[48:51]\n"
      "v_mfma_f32_16x16x32_bf16 a[128:131], a[104:107], v[208:211], a[128:131]\n"
      "s_waitcnt lgkmcnt(6)\n"
      "v_exp_f32 v115, v159\n"
      "v_mfma_f32_16x16x32_bf16 a[144:147], a[108:111], v[208:211], a[144:147]\n"
      "v_cvt_pk_bf16_f32 v207, v114, v115\n"
      "v_mfma_f32_16x16x32_bf16 v[64:67], a[112:115], v[208:211], v[64:67]\n"
      "s_waitcnt lgkmcnt(2)\n"
      "v_mfma_f32_16x16x32_bf16 v[80:83], a[116:119], v[208:211], v[80:83]\n"
      "v_or3_b32 v116, v116, v206, v207\n"
      "v_mfma_f32_16x16x32_bf16 a[160:163], a[120:123], v[208:211], a[160:163]\n"
      "s_mov_b32 s64, 2\n"
      "s_waitcnt lgkmcnt(0)\n"
      "v_and_b32 v116, 0x40004000, v116\n"
      "v_mfma_f32_16x16x32_bf16 a[176:179], a[124:127], v[208:211], a[176:179]\n"
      "v_cmp_ne_u32 vcc, 0, v116\n"
      "s_cbranch_vccnz FB0_%=\n"
      "RET0_2_%=:\n"
      "v_mfma_f32_16x16x32_bf16 a[180:183], a[124:127], v[212:215], a[180:183]\n"
      "v_exp_f32 v112, v160\n"
      "v_mfma_f32_16x16x32_bf16 a[164:167], a[120:123], v[212:215], a[164:167]\n"
      "v_xor_b32 v242, 0x10000, v242\n"
      "v_mfma_f32_16x16x32_bf16 a[192:195], a[196:199], v[192:195], a[192:195]\n"
      "s_add_u32 m0, s62, 0\n"
      "v_exp_f32 v113, v161\n"
      "v_mfma_f32_16x16x32_bf16 v[84:87], a[116:119], v[212:215], v[84:87]\n"
      "buffer_load_dwordx4 v248, s[20:23], s60 offen nt lds\n"
      "v_cvt_pk_bf16_f32 v208, v112, v113\n"
      "v_xor_b32 v243, 0x10000, v243\n"
      "v_mfma_f32_16x16x32_bf16 v[68:71], a[112:115], v[212:215], v[68:71]\n"
      "s_add_u32 m0, s62, 896\n"
      "v_exp_f32 v114, v162\n"
      "v_mfma_f32_16x16x32_bf16 a[192:195], a[200:203], v[196:199], a[192:195]\n"
      "buffer_load_dwordx4 v248, s[20:23], s60 offen offset:128 nt lds\n"
      "v_xor_b32 v244, 0x10000, v244\n"
      "v_mfma_f32_16x16x32_bf16 a[148:151], a[108:111], v[212:215], a[148:151]\n"
      "s_add_u32 m0, s62, 8192\n"
      "v_exp_f32 v115, v163\n"
      "v_mfma_f32_16x16x32_bf16 a[132:135], a[104:107], v[212:215], a[132:135]\n"
      "s_memrealtime s[68:69]\n"
      "buffer_load_dwordx4 v120, s[20:23], s60 offen nt lds\n"
      "v_cvt_pk_bf16_f32 v209, v114, v115\n"
      "v_xor_b32 v245, 0x10000, v245\n"
      "v_mfma_f32_16x16x32_bf16 a[192:195], a[204:207], v[200:203], a[192:195]\n"
      "s_add_u32 m0, s62, 9088\n"
      "v_exp_f32 v112, v164\n"
      "v_mfma_f32_16x16x32_bf16 v[52:55], a[100:103], v[212:215], v[52:55]\n"
      "s_add_u32 s62, s62, 0x8000\n"
      "buffer_load_dwordx4 v120, s[20:23], s60 offen offset:128 nt lds\n"
      "v_or_b32 v116, v208, v209\n"
      "v_xor_b32 v246, 0x10000, v246\n"
      "v_mfma_f32_16x16x32_bf16 v[36:39], a[96:99], v[212:215], v[36:39]\n"
      "s_and_b32 s62, s62, 0x1ffff\n"
      "v_exp_f32 v113, v165\n"
      "v_mfma_f32_16x16x32_bf16 a[192:195], a[208:211], v[204:207], a[192:195]\n"
      "s_add_u32 s60, s60, s78\n"
      "v_cvt_pk_bf16_f32 v210, v112, v113\n"
      "v_xor_b32 v247, 0x10000, v247\n"
      "s_waitcnt vmcnt(8)\n"
      "s_sub_u32 s63, 11, %[n1]\n"
      "LOOP2_%=:\n"
      "s_waitcnt vmcnt(9) lgkmcnt(0)\n"
      "s_sub_u32 s67, s68, s90\n"
      "s_cmp_lt_i32 s67, 0\n"
      "s_cbranch_scc1 GW4_%=\n"
      "GX4_%=:\n"
      "s_barrier\n"
      "v_mfma_f32_16x16x32_bf16 v[40:43], a[96:99], v[216:219], v[40:43]\n"
      "s_memrealtime s[68:69]\n"
      "ds_read_b128 a[64:67], v242 offset:0\n"
      "v_exp_f32 v114, v166\n"
      "v_mfma_f32_16x16x32_bf16 v[44:47], a[96:99], v[220:223], v[44:47]\n"
      "s_add_u32 m0, s77, 16384\n"
      "ds_read_b128 a[68:71], v242 offset:4096\n"
      "ds_read_b128 a[72:75], v243 offset:0\n"
      "v_mfma_f32_16x16x32_bf16 v[60:63], a[100:103], v[220:223], v[60:63]\n"
      "buffer_load_dwordx4 v249, s[24:27], s61 offen nt lds\n"
      "v_exp_f32 v115, v167\n"
      "v_mfma_f32_16x16x32_bf16 v[56:59], a[100:103], v[216:219], v[56:59]\n"
      "s_add_u32 m0, s77, 17408\n"
      "ds_read_b128 a[76:79], v243 offset:4096\n"
      "v_cvt_pk_bf16_f32 v211, v114, v115\n"
      "v_mfma_f32_16x16x32_bf16 a[136:139], a[104:107], v[216:219], a[136:139]\n"
      "buffer_load_dwordx4 v121, s[24:27], s61 offen nt lds\n"
      "v_exp_f32 v112, v168\n"
      "v_mfma_f32_16x16x32_bf16 a[140:143], a[104:107], v[220:223], a[140:143]\n"
      "s_add_u32 m0, s77, 24576\n"
      "ds_read_b128 a[80:83], v242 offset:1024\n"
      "v_or3_b32 v116, v116, v210, v211\n"
      "v_mfma_f32_16x16x32_bf16 a[156:159], a[108:111], v[220:223], a[156:159]\n"
      "buffer_load_dwordx4 v122, s[24:27], s61 offen nt lds\n"
      "v_exp_f32 v113, v169\n"
      "v_mfma_f32_16x16x32_bf16 a[152:155], a[108:111], v[216:219], a[152:155]\n"
      "s_add_u32 m0, s77, 25600\n"
      "ds_read_b128 a[84:87], v242 offset:5120\n"
      "v_cvt_pk_bf16_f32 v212, v112, v113\n"
      "v_mfma_f32_16x16x32_bf16 v[72:75], a[112:115], v[216:219], v[72:75]\n"
      "s_add_u32 s77, s77, 0x8000\n"
      "buffer_load_dwordx4 v123, s[24:27], s61 offen nt lds\n"
      "v_exp_f32 v114, v170\n"
      "v_mfma_f32_16x16x32_bf16 v[76:79], a[112:115], v[220:223], v[76:79]\n"
      "s_and_b32 s77, s77, 0x1ffff\n"
      "ds_read_b128 a[88:91], v243 offset:1024\n"
      "ds_read_b128 a[92:95], v243 offset:5120\n"
      "v_mfma_f32_16x16x32_bf16 v[92:95], a[116:119], v[220:223], v[92:95]\n"
      "s_add_u32 s61, s61, s78\n"
      "v_exp_f32 v115, v171\n"
      "v_mfma_f32_16x16x32_bf16 v[88:91], a[116:119], v[216:219], v[88:91]\n"
      "v_cvt_pk_bf16_f32 v213, v114, v115\n"
      "v_mfma_f32_16x16x32_bf16 a[168:171], a[120:123], v[216:219], a[168:171]\n"
      "v_exp_f32 v112, v172\n"
      "v_mfma_f32_16x16x32_bf16 a[172:175], a[120:123], v[220:223], a[172:175]\n"
      "v_or3_b32 v116, v116, v212, v213\n"
      "v_mfma_f32_16x16x32_bf16 a[188:191], a[124:127], v[220:223], a[188:191]\n"
      "v_exp_f32 v113, v173\n"
      "v_mfma_f32_16x16x32_bf16 a[184:187], a[124:127], v[216:219], a[184:187]\n"
      "s_waitcnt lgkmcnt(0)\n"
      "v_cvt_pk_bf16_f32 v214, v112, v113\n"
      "v_mfma_f32_16x16x32_bf16 v[128:131], a[64:67], a[0:3], v[224:227]\n"
      "s_sub_u32 s67, s68, s66\n"
      "v_exp_f32 v114, v174\n"
      "v_mfma_f32_16x16x32_bf16 v[128:131], a[72:75], a[16:19], v[128:131]\n"
      "s_cmp_gt_i32 s67, s70\n"
      "v_mfma_f32_16x16x32_bf16 v[128:131], a[80:83], a[32:35], v[128:131]\n"
      "s_cselect_b32 s69, s86, 0\n"
      "v_exp_f32 v115, v175\n"
      "v_mfma_f32_16x16x32_bf16 v[128:131], a[88:91], a[48:51], v[128:131]\n"
      "s_add_u32 s66, s66, s69\n"
      "v_cvt_pk_bf16_f32 v215, v114, v115\n"
      "v_mfma_f32_16x16x32_bf16 v[132:135], a[92:95], a[48:51], v[224:227]\n"
      "s_add_u32 s67, s67, s101\n"
      "v_exp_f32 v112, v176\n"
      "v_mfma_f32_16x16x32_bf16 v[132:135], a[84:87], a[32:35], v[132:135]\n"
      "s_min_i32 s70, s70, s67\n"
      "v_or3_b32 v116, v116, v214, v215\n"
      "v_mfma_f32_16x16x32_bf16 v[132:135], a[76:79], a[16:19], v[132:135]\n"
      "s_max_i32 s70, s70, 85\n"
      "v_exp_f32 v113, v177\n"
      "v_mfma_f32_16x16x32_bf16 v[132:135], a[68:71], a[0:3], v[132:135]\n"
      "s_add_u32 s66, s66, s86\n"
      "v_cvt_pk_bf16_f32 v216, v112, v113\n"
      "v_mfma_f32_16x16x32_bf16 v[140:143], a[68:71], a[4:7], v[228:231]\n"
      "s_and_b32 s76, s80, 31\n"
      "v_exp_f32 v114, v178\n"
      "v_mfma_f32_16x16x32_bf16 v[140:143], a[76:79], a[20:23], v[140:143]\n"
      "s_lshr_b64 s[80:81], s[80:81], 5\n"
      "v_mfma_f32_16x16x32_bf16 v[140:143], a[84:87], a[36:39], v[140:143]\n"
      "s_add_u32 s66, s66, s76\n"
      "v_exp_f32 v115, v179\n"
      "v_mfma_f32_16x16x32_bf16 v[140:143], a[92:95], a[52:55], v[140:143]\n"
      "s_sub_u32 s67, s68, s82\n"
      "v_cvt_pk_bf16_f32 v217, v114, v115\n"
      "v_mfma_f32_16x16x32_bf16 v[136:139], a[88:91], a[52:55], v[228:231]\n"
      "s_min_i32 s76, s67, s83\n"
      "v_exp_f32 v112, v180\n"
      "s_waitcnt vmcnt(8)\n"
      "s_barrier\n"
      "v_mfma_f32_16x16x32_bf16 v[136:139], a[80:83], a[36:39], v[136:139]\n"
      "s_mov_b32 s83, s67\n"
      "ds_read_b64_tr_b16 a[96:97], v244 offset:0\n"
      "v_or3_b32 v116, v116, v216, v217\n"
      "v_mfma_f32_16x16x32_bf16 v[136:139], a[72:75], a[20:23], v[136:139]\n"
      "s_sub_u32 s76, s76, 36\n"
      "v_exp_f32 v113, v181\n"
      "v_mfma_f32_16x16x32_bf16 v[136:139], a[64:67], a[4:7], v[136:139]\n"
      "s_min_i32 s76, s76, 148\n"
      "ds_read_b64_tr_b16 a[98:99], v244 offset:4096\n"
      "v_cvt_pk_bf16_f32 v218, v112, v113\n"
      "v_mfma_f32_16x16x32_bf16 v[144:147], a[64:67], a[8:11], v[232:235]\n"
      "s_max_i32 s76, s76, 70\n"
      "v_exp_f32 v114, v182\n"
      "v_mfma_f32_16x16x32_bf16 v[144:147], a[72:75], a[24:27], v[144:147]\n"
      "s_mov_b32 s82, s68\n"
      "ds_read_b64_tr_b16 a[100:101], v245 offset:0\n"
      "ds_read_b64_tr_b16 a[102:103], v245 offset:4096\n"
      "v_mfma_f32_16x16x32_bf16 v[144:147], a[80:83], a[40:43], v[144:147]\n"
      "s_add_u32 s65, s82, s76\n"
      "v_exp_f32 v115, v183\n"
      "v_mfma_f32_16x16x32_bf16 v[144:147], a[88:91], a[56:59], v[144:147]\n"
      "s_sub_u32 s93, s93, 1\n"
      "ds_read_b64_tr_b16 a[104:105], v246 offset:0\n"
      "v_cvt_pk_bf16_f32 v219, v114, v115\n"
      "v_mfma_f32_16x16x32_bf16 v[148:151], a[92:95], a[56:59], v[232:235]\n"
      "s_cmp_le_i32 s93, 14\n"
      "v_exp_f32 v112, v184\n"
      "v_mfma_f32_16x16x32_bf16 v[148:151], a[84:87], a[40:43], v[148:151]\n"
      "s_cselect_b32 s86, 107, s86\n"
      "ds_read_b64_tr_b16 a[106:107], v246 offset:4096\n"
      "v_or3_b32 v116, v116, v218, v219\n"
      "v_mfma_f32_16x16x32_bf16 v[148:151], a[76:79], a[24:27], v[148:151]\n"
      "s_sub_u32 s67, s65, s66\n"
      "v_exp_f32 v113, v185\n"
      "v_mfma_f32_16x16x32_bf16 v[148:151], a[68:71], a[8:11], v[148:151]\n"
      "s_cmp_lt_i32 s67, 0\n"
      "ds_read_b64_tr_b16 a[108:109], v247 offset:0\n"
      "v_cvt_pk_bf16_f32 v220, v112, v113\n"
      "v_mfma_f32_16x16x32_bf16 v[156:159], a[68:71], a[12:15], v[236:239]\n"
      "s_cselect_b32 s88, s66, s65\n"
      "v_exp_f32 v114, v186\n"
      "v_mfma_f32_16x16x32_bf16 v[156:159], a[76:79], a[28:31], v[156:159]\n"
      "s_add_u32 s90, s88, s91\n"
      "ds_read_b64_tr_b16 a[110:111], v247 offset:4096\n"
      "ds_read_b64_tr_b16 a[112:113], v244 offset:1024\n"
      "v_mfma_f32_16x16x32_bf16 v[156:159], a[84:87], a[44:47], v[156:159]\n"
      "v_exp_f32 v115, v187\n"
      "v_mfma_f32_16x16x32_bf16 v[156:159], a[92:95], a[60:63], v[156:159]\n"
      "ds_read_b64_tr_b16 a[114:115], v244 offset:5120\n"
      "v_cvt_pk_bf16_f32 v221, v114, v115\n"
      "v_mfma_f32_16x16x32_bf16 v[152:155], a[88:91], a[60:63], v[236:239]\n"
      "v_exp_f32 v112, v188\n"
      "v_mfma_f32_16x16x32_bf16 v[152:155], a[80:83], a[44:47], v[152:155]\n"
      "ds_read_b64_tr_b16 a[116:117], v245 offset:1024\n"
      "v_or3_b32 v116, v116, v220, v221\n"
      "v_mfma_f32_16x16x32_bf16 v[152:155], a[72:75], a[28:31], v[152:155]\n"
      "v_exp_f32 v113, v189\n"
      "v_mfma_f32_16x16x32_bf16 v[152:155], a[64:67], a[12:15], v[152:155]\n"
      "ds_read_b64_tr_b16 a[118:119], v245 offset:5120\n"
      "v_cvt_pk_bf16_f32 v222, v112, v113\n"
      "s_waitcnt lgkmcnt(6)\n"
      "v_mfma_f32_16x16x32_bf16 v[32:35], a[96:99], v[192:195], v[32:35]\n"
      "v_exp_f32 v114, v190\n"
      "v_mfma_f32_16x16x32_bf16 v[48:51], a[100:103], v[192:195], v[48:51]\n"
      "ds_read_b64_tr_b16 a[120:121], v246 offset:1024\n"
      "ds_read_b64_tr_b16 a[122:123], v246 offset:5120\n"
      "v_mfma_f32_16x16x32_bf16 a[128:131], a[104:107], v[192:195], a[128:131]\n"
      "s_waitcnt lgkmcnt(4)\n"
      "v_exp_f32 v115, v191\n"
      "v_mfma_f32_16x16x32_bf16 a[144:147], a[108:111], v[192:195], a[144:147]\n"
      "ds_read_b64_tr_b16 a[124:125], v247 offset:1024\n"
      "v_cvt_pk_bf16_f32 v223, v114, v115\n"
      "v_mfma_f32_16x16x32_bf16 v[64:67], a[112:115], v[192:195], v[64:67]\n"
      "ds_read_b64_tr_b16 a[126:127], v247 offset:5120\n"
      "s_waitcnt lgkmcnt(2)\n"
      "v_mfma_f32_16x16x32_bf16 v[80:83], a[116:119], v[192:195], v[80:83]\n"
      "v_or3_b32 v116, v116, v222, v223\n"
      "v_mfma_f32_16x16x32_bf16 a[160:163], a[120:123], v[192:195], a[160:163]\n"
      "s_mov_b32 s64, 3\n"
      "s_waitcnt lgkmcnt(0)\n"
      "v_and_b32 v116, 0x40004000, v116\n"
      "v_mfma_f32_16x16x32_bf16 a[176:179], a[124:127], v[192:195], a[176:179]\n"
      "v_cmp_ne_u32 vcc, 0, v116\n"
      "s_cbranch_vccnz FB1_%=\n"
      "RET1_3_%=:\n"
      "v_mfma_f32_16x16x32_bf16 a[180:183], a[124:127], v[196:199], a[180:183]\n"
      "v_exp_f32 v112, v128\n"
      "v_mfma_f32_16x16x32_bf16 a[164:167], a[120:123], v[196:199], a[164:167]\n"
      "v_mfma_f32_16x16x32_bf16 a[192:195], a[196:199], v[208:211], a[192:195]\n"
      "s_add_u32 m0, s62, 0\n"
      "v_exp_f32 v113, v129\n"
      "v_mfma_f32_16x16x32_bf16 v[84:87], a[116:119], v[196:199], v[84:87]\n"
      "buffer_load_dwordx4 v248, s[20:23], s60 offen nt lds\n"
      "v_cvt_pk_bf16_f32 v192, v112, v113\n"
      "v_mfma_f32_16x16x32_bf16 v[68:71], a[112:115], v[196:199], v[68:71]\n"
      "s_add_u32 m0, s62, 896\n"
      "v_exp_f32 v114, v130\n"
      "v_mfma_f32_16x16x32_bf16 a[192:195], a[200:203], v[212:215], a[192:195]\n"
      "buffer_load_dwordx4 v248, s[20:23], s60 offen offset:128 nt lds\n"
      "v_mfma_f32_16x16x32_bf16 a[148:151], a[108:111], v[196:199], a[148:151]\n"
      "s_add_u32 m0, s62, 8192\n"
      "v_exp_f32 v115, v131\n"
      "v_mfma_f32_16x16x32_bf16 a[132:135], a[104:107], v[196:199], a[132:135]\n"
      "s_memrealtime s[68:69]\n"
      "buffer_load_dwordx4 v120, s[20:23], s60 offen nt lds\n"
      "v_cvt_pk_bf16_f32 v193, v114, v115\n"
      "v_mfma_f32_16x16x32_bf16 a[192:195], a[204:207], v[216:219], a[192:195]\n"
      "s_add_u32 m0, s62, 9088\n"
      "v_exp_f32 v112, v132\n"
      "v_mfma_f32_16x16x32_bf16 v[52:55], a[100:103], v[196:199], v[52:55]\n"
      "s_add_u32 s62, s62, 0x8000\n"
      "buffer_load_dwordx4 v120, s[20:23], s60 offen offset:128 nt lds\n"
      "v_or_b32 v116, v192, v193\n"
      "v_mfma_f32_16x16x32_bf16 v[36:39], a[96:99], v[196:199], v[36:39]\n"
      "s_and_b32 s62, s62, 0x1ffff\n"
      "v_exp_f32 v113, v133\n"
      "v_mfma_f32_16x16x32_bf16 a[192:195], a[208:211], v[220:223], a[192:195]\n"
      "s_add_u32 s60, s60, %[inc]\n"
      "v_cvt_pk_bf16_f32 v194, v112, v113\n"
      "s_waitcnt vmcnt(8) lgkmcnt(0)\n"
      "s_sub_u32 s67, s68, s90\n"
      "s_cmp_lt_i32 s67, 0\n"
      "s_cbranch_scc1 GW5_%=\n"
      "GX5_%=:\n"
      "s_barrier\n"
      "v_mfma_f32_16x16x32_bf16 v[40:43], a[96:99], v[200:203], v[40:43]\n"
      "s_memrealtime s[68:69]\n"
      "buffer_load_dword v250, v251, s[28:31], 0 offen sc1\n"
      "ds_read_b128 a[64:67], v242 offset:32768\n"
      "v_exp_f32 v114, v134\n"
      "v_mfma_f32_16x16x32_bf16 v[44:47], a[96:99], v[204:207], v[44:47]\n"
      "s_add_u32 m0, s77, 16384\n"
      "ds_read_b128 a[68:71], v242 offset:36864\n"
      "ds_read_b128 a[72:75], v243 offset:32768\n"
      "v_mfma_f32_16x16x32_bf16 v[60:63], a[100:103], v[204:207], v[60:63]\n"
      "buffer_load_dwordx4 v249, s[24:27], s61 offen nt lds\n"
      "v_exp_f32 v115, v135\n"
      "v_mfma_f32_16x16x32_bf16 v[56:59], a[100:103], v[200:203], v[56:59]\n"
      "s_add_u32 m0, s77, 17408\n"
      "ds_read_b128 a[76:79], v243 offset:36864\n"
      "v_cvt_pk_bf16_f32 v195, v114, v115\n"
      "v_mfma_f32_16x16x32_bf16 a[136:139], a[104:107], v[200:203], a[136:139]\n"
      "buffer_load_dwordx4 v121, s[24:27], s61 offen nt lds\n"
      "v_exp_f32 v112, v136\n"
      "v_mfma_f32_16x16x32_bf16 a[140:143], a[104:107], v[204:207], a[140:143]\n"
      "s_add_u32 m0, s77, 24576\n"
      "ds_read_b128 a[80:83], v242 offset:33792\n"
      "v_or3_b32 v116, v116, v194, v195\n"
      "v_mfma_f32_16x16x32_bf16 a[156:159], a[108:111], v[204:207], a[156:159]\n"
      "buffer_load_dwordx4 v122, s[24:27], s61 offen nt lds\n"
      "v_exp_f32 v113, v137\n"
      "v_mfma_f32_16x16x32_bf16 a[152:155], a[108:111], v[200:203], a[152:155]\n"
      "s_add_u32 m0, s77, 25600\n"
      "ds_read_b128 a[84:87], v242 offset:37888\n"
      "v_cvt_pk_bf16_f32 v196, v112, v113\n"
      "v_mfma_f32_16x16x32_bf16 v[72:75], a[112:115], v[200:203], v[72:75]\n"
      "s_add_u32 s77, s77, 0x8000\n"
      "buffer_load_dwordx4 v123, s[24:27], s61 offen nt lds\n"
      "v_exp_f32 v114, v138\n"
      "v_mfma_f32_16x16x32_bf16 v[76:79], a[112:115], v[204:207], v[76:79]\n"
      "s_and_b32 s77, s77, 0x1ffff\n"
      "ds_read_b128 a[88:91], v243 offset:33792\n"
      "ds_read_b128 a[92:95], v243 offset:37888\n"
      "v_mfma_f32_16x16x32_bf16 v[92:95], a[116:119], v[204:207], v[92:95]\n"
      "s_add_u32 s61, s61, %[inc]\n"
      "v_exp_f32 v115, v139\n"
      "v_mfma_f32_16x16x32_bf16 v[88:91], a[116:119], v[200:203], v[88:91]\n"
      "v_cvt_pk_bf16_f32 v197, v114, v115\n"
      "v_mfma_f32_16x16x32_bf16 a[168:171], a[120:123], v[200:203], a[168:171]\n"
      "v_exp_f32 v112, v140\n"
      "v_mfma_f32_16x16x32_bf16 a[172:175], a[120:123], v[204:207], a[172:175]\n"
      "v_or3_b32 v116, v116, v196, v197\n"
      "v_mfma_f32_16x16x32_bf16 a[188:191], a[124:127], v[204:207], a[188:191]\n"
      "v_exp_f32 v113, v141\n"
      "v_mfma_f32_16x16x32_bf16 a[184:187], a[124:127], v[200:203], a[184:187]\n"
      "s_waitcnt lgkmcnt(0)\n"
      "v_cvt_pk_bf16_f32 v198, v112, v113\n"
      "v_mfma_f32_16x16x32_bf16 v[160:163], a[64:67], a[0:3], v[224:227]\n"
      "s_sub_u32 s67, s68, s66\n"
      "v_exp_f32 v114, v142\n"
      "v_mfma_f32_16x16x32_bf16 v[160:163], a[72:75], a[16:19], v[160:163]\n"
      "s_cmp_gt_i32 s67, s70\n"
      "v_mfma_f32_16x16x32_bf16 v[160:163], a[80:83], a[32:35], v[160:163]\n"
      "s_cselect_b32 s69, s86, 0\n"
      "v_exp_f32 v115, v143\n"
      "v_mfma_f32_16x16x32_bf16 v[160:163], a[88:91], a[48:51], v[160:163]\n"
      "s_add_u32 s66, s66, s69\n"
      "v_cvt_pk_bf16_f32 v199, v114, v115\n"
      "v_mfma_f32_16x16x32_bf16 v[164:167], a[92:95], a[48:51], v[224:227]\n"
      "s_add_u32 s67, s67, s101\n"
      "v_exp_f32 v112, v144\n"
      "v_mfma_f32_16x16x32_bf16 v[164:167], a[84:87], a[32:35], v[164:167]\n"
      "s_min_i32 s70, s70, s67\n"
      "v_or3_b32 v116, v116, v198, v199\n"
      "v_mfma_f32_16x16x32_bf16 v[164:167], a[76:79], a[16:19], v[164:167]\n"
      "s_max_i32 s70, s70, 85\n"
      "v_exp_f32 v113, v145\n"
      "v_mfma_f32_16x16x32_bf16 v[164:167], a[68:71], a[0:3], v[164:167]\n"
      "s_add_u32 s66, s66, s86\n"
      "v_cvt_pk_bf16_f32 v200, v112, v113\n"
      "v_mfma_f32_16x16x32_bf16 v[172:175], a[68:71], a[4:7], v[228:231]\n"
      "s_and_b32 s76, s80, 31\n"
      "v_exp_f32 v114, v146\n"
      "v_mfma_f32_16x16x32_bf16 v[172:175], a[76:79], a[20:23], v[172:175]\n"
      "s_lshr_b64 s[80:81], s[80:81], 5\n"
      "v_mfma_f32_16x16x32_bf16 v[172:175], a[84:87], a[36:39], v[172:175]\n"
      "s_add_u32 s66, s66, s76\n"
      "v_exp_f32 v115, v147\n"
      "v_mfma_f32_16x16x32_bf16 v[172:175], a[92:95], a[52:55], v[172:175]\n"
      "s_sub_u32 s67, s68, s82\n"
      "v_cvt_pk_bf16_f32 v201, v114, v115\n"
      "v_mfma_f32_16x16x32_bf16 v[168:171], a[88:91], a[52:55], v[228:231]\n"
      "s_min_i32 s76, s67, s83\n"
      "v_exp_f32 v112, v148\n"
      "s_waitcnt vmcnt(9)\n"
      "s_barrier\n"
      "v_mfma_f32_16x16x32_bf16 v[168:171], a[80:83], a[36:39], v[168:171]\n"
      "s_mov_b32 s83, s67\n"
      "ds_read_b64_tr_b16 a[96:97], v244 offset:32768\n"
      "v_or3_b32 v116, v116, v200, v201\n"
      "v_mfma_f32_16x16x32_bf16 v[168:171], a[72:75], a[20:23], v[168:171]\n"
      "s_sub_u32 s76, s76, 36\n"
      "v_exp_f32 v113, v149\n"
      "v_mfma_f32_16x16x32_bf16 v[168:171], a[64:67], a[4:7], v[168:171]\n"
      "s_min_i32 s76, s76, 148\n"
      "ds_read_b64_tr_b16 a[98:99], v244 offset:36864\n"
      "v_cvt_pk_bf16_f32 v202, v112, v113\n"
      "v_mfma_f32_16x16x32_bf16 v[176:179], a[64:67], a[8:11], v[232:235]\n"
      "s_max_i32 s76, s76, 70\n"
      "v_exp_f32 v114, v150\n"
      "v_mfma_f32_16x16x32_bf16 v[176:179], a[72:75], a[24:27], v[176:179]\n"
      "s_mov_b32 s82, s68\n"
      "ds_read_b64_tr_b16 a[100:101], v245 offset:32768\n"
      "ds_read_b64_tr_b16 a[102:103], v245 offset:36864\n"
      "v_mfma_f32_16x16x32_bf16 v[176:179], a[80:83], a[40:43], v[176:179]\n"
      "s_add_u32 s65, s82, s76\n"
      "v_exp_f32 v115, v151\n"
      "v_mfma_f32_16x16x32_bf16 v[176:179], a[88:91], a[56:59], v[176:179]\n"
      "s_sub_u32 s93, s93, 1\n"
      "ds_read_b64_tr_b16 a[104:105], v246 offset:32768\n"
      "v_cvt_pk_bf16_f32 v203, v114, v115\n"
      "v_mfma_f32_16x16x32_bf16 v[180:183], a[92:95], a[56:59], v[232:235]\n"
      "s_cmp_le_i32 s93, 14\n"
      "v_exp_f32 v112, v152\n"
      "v_mfma_f32_16x16x32_bf16 v[180:183], a[84:87], a[40:43], v[180:183]\n"
      "s_cselect_b32 s86, 107, s86\n"
      "ds_read_b64_tr_b16 a[106:107], v246 offset:36864\n"
      "v_or3_b32 v116, v116, v202, v203\n"
      "v_mfma_f32_16x16x32_bf16 v[180:183], a[76:79], a[24:27], v[180:183]\n"
      "s_sub_u32 s67, s65, s66\n"
      "v_exp_f32 v113, v153\n"
      "v_mfma_f32_16x16x32_bf16 v[180:183], a[68:71], a[8:11], v[180:183]\n"
      "s_cmp_lt_i32 s67, 0\n"
      "ds_read_b64_tr_b16 a[108:109], v247 offset:32768\n"
      "v_cvt_pk_bf16_f32 v204, v112, v113\n"
      "v_mfma_f32_16x16x32_bf16 v[188:191], a[68:71], a[12:15], v[236:239]\n"
      "s_cselect_b32 s88, s66, s65\n"
      "v_exp_f32 v114, v154\n"
      "v_mfma_f32_16x16x32_bf16 v[188:191], a[76:79], a[28:31], v[188:191]\n"
      "s_add_u32 s90, s88, s91\n"
      "ds_read_b64_tr_b16 a[110:111], v247 offset:36864\n"
      "ds_read_b64_tr_b16 a[112:113], v244 offset:33792\n"
      "v_mfma_f32_16x16x32_bf16 v[188:191], a[84:87], a[44:47], v[188:191]\n"
      "v_exp_f32 v115, v155\n"
      "v_mfma_f32_16x16x32_bf16 v[188:191], a[92:95], a[60:63], v[188:191]\n"
      "ds_read_b64_tr_b16 a[114:115], v244 offset:37888\n"
      "v_cvt_pk_bf16_f32 v205, v114, v115\n"
      "v_mfma_f32_16x16x32_bf16 v[184:187], a[88:91], a[60:63], v[236:239]\n"
      "v_exp_f32 v112, v156\n"
      "v_mfma_f32_16x16x32_bf16 v[184:187], a[80:83], a[44:47], v[184:187]\n"
      "ds_read_b64_tr_b16 a[116:117], v245 offset:33792\n"
      "v_or3_b32 v116, v116, v204, v205\n"
      "v_mfma_f32_16x16x32_bf16 v[184:187], a[72:75], a[28:31], v[184:187]\n"
      "v_exp_f32 v113, v157\n"
      "v_mfma_f32_16x16x32_bf16 v[184:187], a[64:67], a[12:15], v[184:187]\n"
      "ds_read_b64_tr_b16 a[118:119], v245 offset:37888\n"
      "v_cvt_pk_bf16_f32 v206, v112, v113\n"
      "s_waitcnt lgkmcnt(6)\n"
      "v_mfma_f32_16x16x32_bf16 v[32:35], a[96:99], v[208:211], v[32:35]\n"
      "v_exp_f32 v114, v158\n"
      "v_mfma_f32_16x16x32_bf16 v[48:51], a[100:103], v[208:211], v[48:51]\n"
      "ds_read_b64_tr_b16 a[120:121], v246 offset:33792\n"
      "ds_read_b64_tr_b16 a[122:123], v246 offset:37888\n"
      "v_mfma_f32_16x16x32_bf16 a[128:131], a[104:107], v[208:211], a[128:131]\n"
      "s_waitcnt lgkmcnt(4)\n"
      "v_exp_f32 v115, v159\n"
      "v_mfma_f32_16x16x32_bf16 a[144:147], a[108:111], v[208:211], a[144:147]\n"
      "ds_read_b64_tr_b16 a[124:125], v247 offset:33792\n"
      "v_cvt_pk_bf16_f32 v207, v114, v115\n"
      "v_mfma_f32_16x16x32_bf16 v[64:67], a[112:115], v[208:211], v[64:67]\n"
      "ds_read_b64_tr_b16 a[126:127], v247 offset:37888\n"
      "s_waitcnt lgkmcnt(2)\n"
      "v_mfma_f32_16x16x32_bf16 v[80:83], a[116:119], v[208:211], v[80:83]\n"
      "v_or3_b32 v116, v116, v206, v207\n"
      "v_mfma_f32_16x16x32_bf16 a[160:163], a[120:123], v[208:211], a[160:163]\n"
      "s_mov_b32 s64, 3\n"
      "s_waitcnt lgkmcnt(0)\n"
      "v_and_b32 v116, 0x40004000, v116\n"
      "v_mfma_f32_16x16x32_bf16 a[176:179], a[124:127], v[208:211], a[176:179]\n"
      "v_cmp_ne_u32 vcc, 0, v116\n"
      "s_cbranch_vccnz FB0_%=\n"
      "RET0_3_%=:\n"
      "v_mfma_f32_16x16x32_bf16 a[180:183], a[124:127], v[212:215], a[180:183]\n"
      "v_exp_f32 v112, v160\n"
      "v_mfma_f32_16x16x32_bf16 a[164:167], a[120:123], v[212:215], a[164:167]\n"
      "v_xor_b32 v242, 0x10000, v242\n"
      "v_mfma_f32_16x16x32_bf16 a[192:195], a[196:199], v[192:195], a[192:195]\n"
      "s_add_u32 m0, s62, 0\n"
      "v_exp_f32 v113, v161\n"
      "v_mfma_f32_16x16x32_bf16 v[84:87], a[116:119], v[212:215], v[84:87]\n"
      "buffer_load_dwordx4 v248, s[20:23], s60 offen nt lds\n"
      "v_cvt_pk_bf16_f32 v208, v112, v113\n"
      "v_xor_b32 v243, 0x10000, v243\n"
      "v_mfma_f32_16x16x32_bf16 v[68:71], a[112:115], v[212:215], v[68:71]\n"
      "s_add_u32 m0, s62, 896\n"
      "v_exp_f32 v114, v162\n"
      "v_mfma_f32_16x16x32_bf16 a[192:195], a[200:203], v[196:199], a[192:195]\n"
      "buffer_load_dwordx4 v248, s[20:23], s60 offen offset:128 nt lds\n"
      "v_xor_b32 v244, 0x10000, v244\n"
      "v_mfma_f32_16x16x32_bf16 a[148:151], a[108:111], v[212:215], a[148:151]\n"
      "s_add_u32 m0, s62, 8192\n"
      "v_exp_f32 v115, v163\n"
      "v_mfma_f32_16x16x32_bf16 a[132:135], a[104:107], v[212:215], a[132:135]\n"
      "s_memrealtime s[68:69]\n"
      "buffer_load_dwordx4 v120, s[20:23], s60 offen nt lds\n"
      "v_cvt_pk_bf16_f32 v209, v114, v115\n"
      "v_xor_b32 v245, 0x10000, v245\n"
      "v_mfma_f32_16x16x32_bf16 a[192:195], a[204:207], v[200:203], a[192:195]\n"
      "s_add_u32 m0, s62, 9088\n"
      "v_exp_f32 v112, v164\n"
      "v_mfma_f32_16x16x32_bf16 v[52:55], a[100:103], v[212:215], v[52:55]\n"
      "s_add_u32 s62, s62, 0x8000\n"
      "buffer_load_dwordx4 v120, s[20:23], s60 offen offset:128 nt lds\n"
      "v_or_b32 v116, v208, v209\n"
      "v_xor_b32 v246, 0x10000, v246\n"
      "v_mfma_f32_16x16x32_bf16 v[36:39], a[96:99], v[212:215], v[36:39]\n"
      "s_and_b32 s62, s62, 0x1ffff\n"
      "s_add_u32 s60, s60, s78\n"
      "v_exp_f32 v113, v165\n"
      "v_mfma_f32_16x16x32_bf16 a[192:195], a[208:211], v[204:207], a[192:195]\n"
      "s_sub_u32 s63, s63, 1\n"
      "v_cvt_pk_bf16_f32 v210, v112, v113\n"
      "v_xor_b32 v247, 0x10000, v247\n"
      "s_cbranch_scc0 LOOP2_%=\n"
      "s_waitcnt vmcnt(9) lgkmcnt(0)\n"
      "s_sub_u32 s67, s68, s90\n"
      "s_cmp_lt_i32 s67, 0\n"
      "s_cbranch_scc1 GW6_%=\n"
      "GX6_%=:\n"
      "s_barrier\n"
      "v_mfma_f32_16x16x32_bf16 v[40:43], a[96:99], v[216:219], v[40:43]\n"
      "s_memrealtime s[68:69]\n"
      "ds_read_b128 a[64:67], v242 offset:0\n"
      "v_exp_f32 v114, v166\n"
      "v_mfma_f32_16x16x32_bf16 v[44:47], a[96:99], v[220:223], v[44:47]\n"
      "s_add_u32 m0, %[wb], 50176\n"
      "ds_read_b128 a[68:71], v242 offset:4096\n"
      "ds_read_b128 a[72:75], v243 offset:0\n"
      "v_mfma_f32_16x16x32_bf16 v[60:63], a[100:103], v[220:223], v[60:63]\n"
      "s_add_u32 s94, s61, s78\n"
      "buffer_load_dwordx4 v121, s[24:27], s61 offen nt lds\n"
      "v_exp_f32 v115, v167\n"
      "v_mfma_f32_16x16x32_bf16 v[56:59], a[100:103], v[216:219], v[56:59]\n"
      "s_add_u32 m0, %[wb], 58368\n"
      "ds_read_b128 a[76:79], v243 offset:4096\n"
      "v_cvt_pk_bf16_f32 v211, v114, v115\n"
      "v_mfma_f32_16x16x32_bf16 a[136:139], a[104:107], v[216:219], a[136:139]\n"
      "s_add_u32 s95, s61, 0x10000\n"
      "buffer_load_dwordx4 v123, s[24:27], s61 offen nt lds\n"
      "v_exp_f32 v112, v168\n"
      "v_mfma_f32_16x16x32_bf16 a[140:143], a[104:107], v[220:223], a[140:143]\n"
      "s_add_u32 s77, s94, 0x10000\n"
      "ds_read_b128 a[80:83], v242 offset:1024\n"
      "v_or3_b32 v116, v116, v210, v211\n"
      "v_mfma_f32_16x16x32_bf16 a[156:159], a[108:111], v[220:223], a[156:159]\n"
      "s_add_u32 s78, s61, 0x20000\n"
      "v_exp_f32 v113, v169\n"
      "v_mfma_f32_16x16x32_bf16 a[152:155], a[108:111], v[216:219], a[152:155]\n"
      "ds_read_b128 a[84:87], v242 offset:5120\n"
      "v_cvt_pk_bf16_f32 v212, v112, v113\n"
      "v_mfma_f32_16x16x32_bf16 v[72:75], a[112:115], v[216:219], v[72:75]\n"
      "v_exp_f32 v114, v170\n"
      "v_mfma_f32_16x16x32_bf16 v[76:79], a[112:115], v[220:223], v[76:79]\n"
      "ds_read_b128 a[88:91], v243 offset:1024\n"
      "ds_read_b128 a[92:95], v243 offset:5120\n"
      "v_mfma_f32_16x16x32_bf16 v[92:95], a[116:119], v[220:223], v[92:95]\n"
      "v_exp_f32 v115, v171\n"
      "v_mfma_f32_16x16x32_bf16 v[88:91], a[116:119], v[216:219], v[88:91]\n"
      "v_cvt_pk_bf16_f32 v213, v114, v115\n"
      "v_mfma_f32_16x16x32_bf16 a[168:171], a[120:123], v[216:219], a[168:171]\n"
      "v_exp_f32 v112, v172\n"
      "v_mfma_f32_16x16x32_bf16 a[172:175], a[120:123], v[220:223], a[172:175]\n"
      "v_or3_b32 v116, v116, v212, v213\n"
      "v_mfma_f32_16x16x32_bf16 a[188:191], a[124:127], v[220:223], a[188:191]\n"
      "v_exp_f32 v113, v173\n"
      "v_mfma_f32_16x16x32_bf16 a[184:187], a[124:127], v[216:219], a[184:187]\n"
      "s_waitcnt lgkmcnt(0)\n"
      "v_cvt_pk_bf16_f32 v214, v112, v113\n"
      "v_mfma_f32_16x16x32_bf16 v[128:131], a[64:67], a[0:3], v[224:227]\n"
      "s_sub_u32 s67, s68, s66\n"
      "v_exp_f32 v114, v174\n"
      "v_mfma_f32_16x16x32_bf16 v[128:131], a[72:75], a[16:19], v[128:131]\n"
      "s_cmp_gt_i32 s67, s70\n"
      "v_mfma_f32_16x16x32_bf16 v[128:131], a[80:83], a[32:35], v[128:131]\n"
      "s_cselect_b32 s69, s86, 0\n"
      "v_exp_f32 v115, v175\n"
      "v_mfma_f32_16x16x32_bf16 v[128:131], a[88:91], a[48:51], v[128:131]\n"
      "s_add_u32 s66, s66, s69\n"
      "v_cvt_pk_bf16_f32 v215, v114, v115\n"
      "v_mfma_f32_16x16x32_bf16 v[132:135], a[92:95], a[48:51], v[224:227]\n"
      "s_add_u32 s67, s67, s101\n"
      "v_exp_f32 v112, v176\n"
      "v_mfma_f32_16x16x32_bf16 v[132:135], a[84:87], a[32:35], v[132:135]\n"
      "s_min_i32 s70, s70, s67\n"
      "v_or3_b32 v116, v116, v214, v215\n"
      "v_mfma_f32_16x16x32_bf16 v[132:135], a[76:79], a[16:19], v[132:135]\n"
      "s_max_i32 s70, s70, 85\n"
      "v_exp_f32 v113, v177\n"
      "v_mfma_f32_16x16x32_bf16 v[132:135], a[68:71], a[0:3], v[132:135]\n"
      "s_add_u32 s66, s66, s86\n"
      "v_cvt_pk_bf16_f32 v216, v112, v113\n"
      "v_mfma_f32_16x16x32_bf16 v[140:143], a[68:71], a[4:7], v[228:231]\n"
      "s_and_b32 s76, s80, 31\n"
      "v_exp_f32 v114, v178\n"
      "v_mfma_f32_16x16x32_bf16 v[140:143], a[76:79], a[20:23], v[140:143]\n"
      "s_lshr_b64 s[80:81], s[80:81], 5\n"
      "v_mfma_f32_16x16x32_bf16 v[140:143], a[84:87], a[36:39], v[140:143]\n"
      "s_add_u32 s66, s66, s76\n"
      "v_exp_f32 v115, v179\n"
      "v_mfma_f32_16x16x32_bf16 v[140:143], a[92:95], a[52:55], v[140:143]\n"
      "s_sub_u32 s67, s68, s82\n"
      "v_cvt_pk_bf16_f32 v217, v114, v115\n"
      "v_mfma_f32_16x16x32_bf16 v[136:139], a[88:91], a[52:55], v[228:231]\n"
      "s_min_i32 s76, s67, s83\n"
      "v_exp_f32 v112, v180\n"
      "s_waitcnt vmcnt(6)\n"
      "s_barrier\n"
      "v_mfma_f32_16x16x32_bf16 v[136:139], a[80:83], a[36:39], v[136:139]\n"
      "s_mov_b32 s83, s67\n"
      "ds_read_b64_tr_b16 a[96:97], v244 offset:0\n"
      "v_or3_b32 v116, v116, v216, v217\n"
      "v_mfma_f32_16x16x32_bf16 v[136:139], a[72:75], a[20:23], v[136:139]\n"
      "s_add_u32 m0, s62, 0\n"
      "v_exp_f32 v113, v181\n"
      "v_mfma_f32_16x16x32_bf16 v[136:139], a[64:67], a[4:7], v[136:139]\n"
      "s_sub_u32 s76, s76, 36\n"
      "ds_read_b64_tr_b16 a[98:99], v244 offset:4096\n"
      "buffer_load_dwordx4 v248, s[20:23], s60 offen nt lds\n"
      "v_cvt_pk_bf16_f32 v218, v112, v113\n"
      "v_mfma_f32_16x16x32_bf16 v[144:147], a[64:67], a[8:11], v[232:235]\n"
      "s_add_u32 m0, s62, 896\n"
      "v_exp_f32 v114, v182\n"
      "v_mfma_f32_16x16x32_bf16 v[144:147], a[72:75], a[24:27], v[144:147]\n"
      "s_min_i32 s76, s76, 148\n"
      "ds_read_b64_tr_b16 a[100:101], v245 offset:0\n"
      "ds_read_b64_tr_b16 a[102:103], v245 offset:4096\n"
      "buffer_load_dwordx4 v248, s[20:23], s60 offen offset:128 nt lds\n"
      "v_mfma_f32_16x16x32_bf16 v[144:147], a[80:83], a[40:43], v[144:147]\n"
      "s_add_u32 m0, s62, 8192\n"
      "v_exp_f32 v115, v183\n"
      "v_mfma_f32_16x16x32_bf16 v[144:147], a[88:91], a[56:59], v[144:147]\n"
      "s_max_i32 s76, s76, 70\n"
      "ds_read_b64_tr_b16 a[104:105], v246 offset:0\n"
      "buffer_load_dwordx4 v120, s[20:23], s60 offen nt lds\n"
      "v_cvt_pk_bf16_f32 v219, v114, v115\n"
      "v_mfma_f32_16x16x32_bf16 v[148:151], a[92:95], a[56:59], v[232:235]\n"
      "s_add_u32 m0, s62, 9088\n"
      "v_exp_f32 v112, v184\n"
      "v_mfma_f32_16x16x32_bf16 v[148:151], a[84:87], a[40:43], v[148:151]\n"
      "s_add_u32 s62, s62, 0x8000\n"
      "ds_read_b64_tr_b16 a[106:107], v246 offset:4096\n"
      "buffer_load_dwordx4 v120, s[20:23], s60 offen offset:128 nt lds\n"
      "v_or3_b32 v116, v116, v218, v219\n"
      "v_mfma_f32_16x16x32_bf16 v[148:151], a[76:79], a[24:27], v[148:151]\n"
      "s_and_b32 s62, s62, 0x1ffff\n"
      "v_exp_f32 v113, v185\n"
      "v_mfma_f32_16x16x32_bf16 v[148:151], a[68:71], a[8:11], v[148:151]\n"
      "s_add_u32 s60, s60, %[inc]\n"
      "ds_read_b64_tr_b16 a[108:109], v247 offset:0\n"
      "v_cvt_pk_bf16_f32 v220, v112, v113\n"
      "v_mfma_f32_16x16x32_bf16 v[156:159], a[68:71], a[12:15], v[236:239]\n"
      "s_mov_b32 s82, s68\n"
      "v_exp_f32 v114, v186\n"
      "v_mfma_f32_16x16x32_bf16 v[156:159], a[76:79], a[28:31], v[156:159]\n"
      "s_sub_u32 s66, s66, s99\n"
      "ds_read_b64_tr_b16 a[110:111], v247 offset:4096\n"
      "ds_read_b64_tr_b16 a[112:113], v244 offset:1024\n"
      "v_mfma_f32_16x16x32_bf16 v[156:159], a[84:87], a[44:47], v[156:159]\n"
      "s_sub_u32 s82, s82, s99\n"
      "v_exp_f32 v115, v187\n"
      "v_mfma_f32_16x16x32_bf16 v[156:159], a[92:95], a[60:63], v[156:159]\n"
      "s_add_u32 s65, s82, s76\n"
      "ds_read_b64_tr_b16 a[114:115], v244 offset:5120\n"
      "v_cvt_pk_bf16_f32 v221, v114, v115\n"
      "v_mfma_f32_16x16x32_bf16 v[152:155], a[88:91], a[60:63], v[236:239]\n"
      "s_sub_u32 s93, s93, 1\n"
      "v_exp_f32 v112, v188\n"
      "v_mfma_f32_16x16x32_bf16 v[152:155], a[80:83], a[44:47], v[152:155]\n"
      "s_cmp_le_i32 s93, 14\n"
      "ds_read_b64_tr_b16 a[116:117], v245 offset:1024\n"
      "v_or3_b32 v116, v116, v220, v221\n"
      "v_mfma_f32_16x16x32_bf16 v[152:155], a[72:75], a[28:31], v[152:155]\n"
      "s_cselect_b32 s86, 107, s86\n"
      "v_exp_f32 v113, v189\n"
      "v_mfma_f32_16x16x32_bf16 v[152:155], a[64:67], a[12:15], v[152:155]\n"
      "ds_read_b64_tr_b16 a[118:119], v245 offset:5120\n"
      "v_cvt_pk_bf16_f32 v222, v112, v113\n"
      "s_waitcnt lgkmcnt(6)\n"
      "v_mfma_f32_16x16x32_bf16 v[32:35], a[96:99], v[192:195], v[32:35]\n"
      "s_sub_u32 s67, s65, s66\n"
      "v_exp_f32 v114, v190\n"
      "v_mfma_f32_16x16x32_bf16 v[48:51], a[100:103], v[192:195], v[48:51]\n"
      "s_cmp_lt_i32 s67, 0\n"
      "s_cselect_b32 s88, s66, s65\n"
      "ds_read_b64_tr_b16 a[120:121], v246 offset:1024\n"
      "ds_read_b64_tr_b16 a[122:123], v246 offset:5120\n"
      "v_mfma_f32_16x16x32_bf16 a[128:131], a[104:107], v[192:195], a[128:131]\n"
      "s_waitcnt lgkmcnt(4)\n"
      "v_exp_f32 v115, v191\n"
      "v_mfma_f32_16x16x32_bf16 a[144:147], a[108:111], v[192:195], a[144:147]\n"
      "s_add_u32 s90, s88, s91\n"
      "ds_read_b64_tr_b16 a[124:125], v247 offset:1024\n"
      "v_cvt_pk_bf16_f32 v223, v114, v115\n"
      "v_mfma_f32_16x16x32_bf16 v[64:67], a[112:115], v[192:195], v[64:67]\n"
      "ds_read_b64_tr_b16 a[126:127], v247 offset:5120\n"
      "s_waitcnt lgkmcnt(2)\n"
      "v_mfma_f32_16x16x32_bf16 v[80:83], a[116:119], v[192:195], v[80:83]\n"
      "v_or3_b32 v116, v116, v222, v223\n"
      "v_mfma_f32_16x16x32_bf16 a[160:163], a[120:123], v[192:195], a[160:163]\n"
      "s_mov_b32 s64, 4\n"
      "s_waitcnt lgkmcnt(0)\n"
      "v_and_b32 v116, 0x40004000, v116\n"
      "v_mfma_f32_16x16x32_bf16 a[176:179], a[124:127], v[192:195], a[176:179]\n"
      "v_cmp_ne_u32 vcc, 0, v116\n"
      "s_cbranch_vccnz FB1_%=\n"
      "RET1_4_%=:\n"
      "v_mfma_f32_16x16x32_bf16 a[180:183], a[124:127], v[196:199], a[180:183]\n"
      "v_exp_f32 v112, v128\n"
      "v_mfma_f32_16x16x32_bf16 a[164:167], a[120:123], v[196:199], a[164:167]\n"
      "v_xor_b32 v240, 0x10000, v244\n"
      "v_mfma_f32_16x16x32_bf16 a[192:195], a[196:199], v[208:211], a[192:195]\n"
      "v_exp_f32 v113, v129\n"
      "v_mfma_f32_16x16x32_bf16 v[84:87], a[116:119], v[196:199], v[84:87]\n"
      "v_cvt_pk_bf16_f32 v192, v112, v113\n"
      "v_xor_b32 v241, 0x10000, v245\n"
      "v_mfma_f32_16x16x32_bf16 v[68:71], a[112:115], v[196:199], v[68:71]\n"
      "v_exp_f32 v114, v130\n"
      "v_mfma_f32_16x16x32_bf16 a[192:195], a[200:203], v[212:215], a[192:195]\n"
      "v_xor_b32 v250, 0x10000, v246\n"
      "v_mfma_f32_16x16x32_bf16 a[148:151], a[108:111], v[196:199], a[148:151]\n"
      "v_exp_f32 v115, v131\n"
      "v_mfma_f32_16x16x32_bf16 a[132:135], a[104:107], v[196:199], a[132:135]\n"
      "s_memrealtime s[68:69]\n"
      "v_cvt_pk_bf16_f32 v193, v114, v115\n"
      "v_xor_b32 v251, 0x10000, v247\n"
      "v_mfma_f32_16x16x32_bf16 a[192:195], a[204:207], v[216:219], a[192:195]\n"
      "v_exp_f32 v112, v132\n"
      "v_mfma_f32_16x16x32_bf16 v[52:55], a[100:103], v[196:199], v[52:55]\n"
      "v_or_b32 v116, v192, v193\n"
      "v_mfma_f32_16x16x32_bf16 v[36:39], a[96:99], v[196:199], v[36:39]\n"
      "v_exp_f32 v113, v133\n"
      "v_mfma_f32_16x16x32_bf16 a[192:195], a[208:211], v[220:223], a[192:195]\n"
      "v_cvt_pk_bf16_f32 v194, v112, v113\n"
      "s_waitcnt vmcnt(6) lgkmcnt(0)\n"
      "s_sub_u32 s67, s68, s90\n"
      "s_cmp_lt_i32 s67, 0\n"
      "s_cbranch_scc1 GW7_%=\n"
      "GX7_%=:\n"
      "s_barrier\n"
      "v_mfma_f32_16x16x32_bf16 v[40:43], a[96:99], v[200:203], v[40:43]\n"
      "s_memrealtime s[68:69]\n"
      "ds_read_b128 a[64:67], v242 offset:32768\n"
      "v_exp_f32 v114, v134\n"
      "v_mfma_f32_16x16x32_bf16 v[44:47], a[96:99], v[204:207], v[44:47]\n"
      "s_add_u32 m0, %[wb], 82944\n"
      "ds_read_b128 a[68:71], v242 offset:36864\n"
      "v_mfma_f32_16x16x32_bf16 v[60:63], a[100:103], v[204:207], v[60:63]\n"
      "ds_read_b128 a[72:75], v243 offset:32768\n"
      "buffer_load_dwordx4 v121, s[24:27], s94 offen nt lds\n"
      "v_mfma_f32_16x16x32_bf16 v[56:59], a[100:103], v[200:203], v[56:59]\n"
      "s_add_u32 m0, %[wb], 91136\n"
      "ds_read_b128 a[76:79], v243 offset:36864\n"
      "v_mfma_f32_16x16x32_bf16 a[136:139], a[104:107], v[200:203], a[136:139]\n"
      "ds_read_b128 a[80:83], v242 offset:33792\n"
      "buffer_load_dwordx4 v123, s[24:27], s94 offen nt lds\n"
      "v_mfma_f32_16x16x32_bf16 a[140:143], a[104:107], v[204:207], a[140:143]\n"
      "ds_read_b128 a[84:87], v242 offset:37888\n"
      "v_mfma_f32_16x16x32_bf16 a[156:159], a[108:111], v[204:207], a[156:159]\n"
      "ds_read_b128 a[88:91], v243 offset:33792\n"
      "v_mfma_f32_16x16x32_bf16 a[152:155], a[108:111], v[200:203], a[152:155]\n"
      "ds_read_b128 a[92:95], v243 offset:37888\n"
      "v_mfma_f32_16x16x32_bf16 v[72:75], a[112:115], v[200:203], v[72:75]\n"
      "v_exp_f32 v115, v135\n"
      "v_mfma_f32_16x16x32_bf16 v[76:79], a[112:115], v[204:207], v[76:79]\n"
      "v_exp_f32 v112, v136\n"
      "v_mfma_f32_16x16x32_bf16 v[92:95], a[116:119], v[204:207], v[92:95]\n"
      "v_cvt_pk_bf16_f32 v195, v114, v115\n"
      "v_exp_f32 v113, v137\n"
      "v_mfma_f32_16x16x32_bf16 v[88:91], a[116:119], v[200:203], v[88:91]\n"
      "v_or3_b32 v116, v116, v194, v195\n"
      "v_mfma_f32_16x16x32_bf16 a[168:171], a[120:123], v[200:203], a[168:171]\n"
      "v_exp_f32 v114, v138\n"
      "v_mfma_f32_16x16x32_bf16 a[172:175], a[120:123], v[204:207], a[172:175]\n"
      "v_cvt_pk_bf16_f32 v196, v112, v113\n"
      "v_exp_f32 v115, v139\n"
      "v_mfma_f32_16x16x32_bf16 a[188:191], a[124:127], v[204:207], a[188:191]\n"
      "v_exp_f32 v112, v140\n"
      "v_mfma_f32_16x16x32_bf16 a[184:187], a[124:127], v[200:203], a[184:187]\n"
      "s_waitcnt lgkmcnt(0)\n"
      "v_cvt_pk_bf16_f32 v197, v114, v115\n"
      "v_mfma_f32_16x16x32_bf16 v[160:163], a[64:67], a[0:3], v[224:227]\n"
      "s_sub_u32 s67, s68, s66\n"
      "v_exp_f32 v113, v141\n"
      "v_mfma_f32_16x16x32_bf16 v[168:171], a[64:67], a[4:7], v[228:231]\n"
      "s_cmp_gt_i32 s67, s70\n"
      "v_or3_b32 v116, v116, v196, v197\n"
      "v_exp_f32 v114, v142\n"
      "v_mfma_f32_16x16x32_bf16 v[176:179], a[64:67], a[8:11], v[232:235]\n"
      "s_cselect_b32 s69, s86, 0\n"
      "v_cvt_pk_bf16_f32 v198, v112, v113\n"
      "v_mfma_f32_16x16x32_bf16 v[184:187], a[64:67], a[12:15], v[236:239]\n"
      "s_add_u32 s66, s66, s69\n"
      "v_exp_f32 v115, v143\n"
      "v_mfma_f32_16x16x32_bf16 v[164:167], a[68:71], a[0:3], v[224:227]\n"
      "s_add_u32 s67, s67, s101\n"
      "v_exp_f32 v112, v144\n"
      "v_mfma_f32_16x16x32_bf16 v[172:175], a[68:71], a[4:7], v[228:231]\n"
      "s_min_i32 s70, s70, s67\n"
      "v_cvt_pk_bf16_f32 v199, v114, v115\n"
      "v_exp_f32 v113, v145\n"
      "v_mfma_f32_16x16x32_bf16 v[180:183], a[68:71], a[8:11], v[232:235]\n"
      "s_max_i32 s70, s70, 85\n"
      "v_or3_b32 v116, v116, v198, v199\n"
      "v_mfma_f32_16x16x32_bf16 v[188:191], a[68:71], a[12:15], v[236:239]\n"
      "s_waitcnt lgkmcnt(4)\n"
      "v_exp_f32 v114, v146\n"
      "v_mfma_f32_16x16x32_bf16 v[160:163], a[72:75], a[16:19], v[160:163]\n"
      "s_add_u32 s66, s66, s86\n"
      "v_cvt_pk_bf16_f32 v200, v112, v113\n"
      "v_exp_f32 v115, v147\n"
      "v_mfma_f32_16x16x32_bf16 v[168:171], a[72:75], a[20:23], v[168:171]\n"
      "s_and_b32 s76, s80, 31\n"
      "v_exp_f32 v112, v148\n"
      "v_mfma_f32_16x16x32_bf16 v[176:179], a[72:75], a[24:27], v[176:179]\n"
      "s_lshr_b64 s[80:81], s[80:81], 5\n"
      "v_cvt_pk_bf16_f32 v201, v114, v115\n"
      "v_mfma_f32_16x16x32_bf16 v[184:187], a[72:75], a[28:31], v[184:187]\n"
      "s_add_u32 s66, s66, s76\n"
      "v_exp_f32 v113, v149\n"
      "v_mfma_f32_16x16x32_bf16 v[164:167], a[76:79], a[16:19], v[164:167]\n"
      "s_sub_u32 s67, s68, s82\n"
      "v_or3_b32 v116, v116, v200, v201\n"
      "v_exp_f32 v114, v150\n"
      "v_mfma_f32_16x16x32_bf16 v[172:175], a[76:79], a[20:23], v[172:175]\n"
      "s_min_i32 s76, s67, s83\n"
      "v_cvt_pk_bf16_f32 v202, v112, v113\n"
      "v_mfma_f32_16x16x32_bf16 v[180:183], a[76:79], a[24:27], v[180:183]\n"
      "s_add_u32 m0, s62, 0\n"
      "v_exp_f32 v115, v151\n"
      "v_mfma_f32_16x16x32_bf16 v[188:191], a[76:79], a[28:31], v[188:191]\n"
      "buffer_load_dwordx4 v248, s[20:23], s60 offen nt lds\n"
      "s_waitcnt lgkmcnt(2)\n"
      "v_mfma_f32_16x16x32_bf16 v[160:163], a[80:83], a[32:35], v[160:163]\n"
      "s_add_u32 m0, s62, 896\n"
      "v_exp_f32 v112, v152\n"
      "v_mfma_f32_16x16x32_bf16 v[168:171], a[80:83], a[36:39], v[168:171]\n"
      "s_mov_b32 s83, s67\n"
      "buffer_load_dwordx4 v248, s[20:23], s60 offen offset:128 nt lds\n"
      "v_mfma_f32_16x16x32_bf16 v[176:179], a[80:83], a[40:43], v[176:179]\n"
      "s_add_u32 m0, s62, 8192\n"
      "v_mfma_f32_16x16x32_bf16 v[184:187], a[80:83], a[44:47], v[184:187]\n"
      "s_sub_u32 s76, s76, 36\n"
      "buffer_load_dwordx4 v120, s[20:23], s60 offen nt lds\n"
      "s_waitcnt vmcnt(9)\n"
      "s_barrier\n"
      "v_mfma_f32_16x16x32_bf16 v[164:167], a[84:87], a[32:35], v[164:167]\n"
      "s_add_u32 m0, s62, 9088\n"
      "ds_read_b64_tr_b16 a[96:97], v246 offset:33792\n"
      "v_mfma_f32_16x16x32_bf16 v[172:175], a[84:87], a[36:39], v[172:175]\n"
      "s_min_i32 s76, s76, 148\n"
      "ds_read_b64_tr_b16 a[98:99], v246 offset:37888\n"
      "buffer_load_dwordx4 v120, s[20:23], s60 offen offset:128 nt lds\n"
      "v_mfma_f32_16x16x32_bf16 v[180:183], a[84:87], a[40:43], v[180:183]\n"
      "s_max_i32 s76, s76, 70\n"
      "ds_read_b64_tr_b16 a[100:101], v247 offset:33792\n"
      "v_mfma_f32_16x16x32_bf16 v[188:191], a[84:87], a[44:47], v[188:191]\n"
      "ds_read_b64_tr_b16 a[102:103], v247 offset:37888\n"
      "s_waitcnt lgkmcnt(4)\n"
      "v_mfma_f32_16x16x32_bf16 v[160:163], a[88:91], a[48:51], v[160:163]\n"
      "s_mov_b32 s82, s68\n"
      "ds_read_b64_tr_b16 a[104:105], v244 offset:33792\n"
      "v_cvt_pk_bf16_f32 v203, v114, v115\n"
      "v_mfma_f32_16x16x32_bf16 v[168:171], a[88:91], a[52:55], v[168:171]\n"
      "s_sub_u32 s66, s66, s99\n"
      "ds_read_b64_tr_b16 a[106:107], v244 offset:37888\n"
      "v_exp_f32 v113, v153\n"
      "v_mfma_f32_16x16x32_bf16 v[176:179], a[88:91], a[56:59], v[176:179]\n"
      "s_sub_u32 s82, s82, s99\n"
      "ds_read_b64_tr_b16 a[108:109], v245 offset:33792\n"
      "v_mfma_f32_16x16x32_bf16 v[184:187], a[88:91], a[60:63], v[184:187]\n"
      "s_add_u32 s65, s82, s76\n"
      "ds_read_b64_tr_b16 a[110:111], v245 offset:37888\n"
      "v_or3_b32 v116, v116, v202, v203\n"
      "v_mfma_f32_16x16x32_bf16 v[164:167], a[92:95], a[48:51], v[164:167]\n"
      "s_sub_u32 s93, s93, 1\n"
      "v_exp_f32 v114, v154\n"
      "v_mfma_f32_16x16x32_bf16 v[172:175], a[92:95], a[52:55], v[172:175]\n"
      "s_cmp_le_i32 s93, 14\n"
      "v_cvt_pk_bf16_f32 v204, v112, v113\n"
      "v_exp_f32 v115, v155\n"
      "v_mfma_f32_16x16x32_bf16 v[180:183], a[92:95], a[56:59], v[180:183]\n"
      "s_cselect_b32 s86, 107, s86\n"
      "v_exp_f32 v112, v156\n"
      "v_mfma_f32_16x16x32_bf16 v[188:191], a[92:95], a[60:63], v[188:191]\n"
      "s_waitcnt lgkmcnt(4)\n"
      "v_cvt_pk_bf16_f32 v205, v114, v115\n"
      "v_mfma_f32_16x16x32_bf16 a[160:163], a[96:99], v[208:211], a[160:163]\n"
      "s_sub_u32 s67, s65, s66\n"
      "v_exp_f32 v113, v157\n"
      "v_mfma_f32_16x16x32_bf16 a[164:167], a[96:99], v[212:215], a[164:167]\n"
      "s_cmp_lt_i32 s67, 0\n"
      "v_or3_b32 v116, v116, v204, v205\n"
      "v_exp_f32 v114, v158\n"
      "v_mfma_f32_16x16x32_bf16 a[168:171], a[96:99], v[216:219], a[168:171]\n"
      "s_cselect_b32 s88, s66, s65\n"
      "v_cvt_pk_bf16_f32 v206, v112, v113\n"
      "v_mfma_f32_16x16x32_bf16 a[172:175], a[96:99], v[220:223], a[172:175]\n"
      "s_add_u32 s90, s88, s91\n"
      "v_exp_f32 v115, v159\n"
      "s_nop 0\n"
      "v_mfma_f32_16x16x32_bf16 a[176:179], a[100:103], v[208:211], a[176:179]\n"
      "s_mov_b32 s64, 4\n"
      "v_cvt_pk_bf16_f32 v207, v114, v115\n"
      "v_or3_b32 v116, v116, v206, v207\n"
      "v_mfma_f32_16x16x32_bf16 a[180:183], a[100:103], v[212:215], a[180:183]\n"
      "v_and_b32 v116, 0x40004000, v116\n"
      "v_cmp_ne_u32 vcc, 0, v116\n"
      "s_cbranch_vccnz FB0_%=\n"
      "RET0_4_%=:\n"
      "v_mfma_f32_16x16x32_bf16 a[184:187], a[100:103], v[216:219], a[184:187]\n"
      "v_exp_f32 v112, v160\n"
      "v_mfma_f32_16x16x32_bf16 a[188:191], a[100:103], v[220:223], a[188:191]\n"
      "s_memrealtime s[68:69]\n"
      "v_xor_b32 v242, 0x10000, v242\n"
      "v_mfma_f32_16x16x32_bf16 a[192:195], a[196:199], v[192:195], a[192:195]\n"
      "v_exp_f32 v113, v161\n"
      "v_mfma_f32_16x16x32_bf16 a[192:195], a[200:203], v[196:199], a[192:195]\n"
      "v_xor_b32 v243, 0x10000, v243\n"
      "v_exp_f32 v114, v162\n"
      "v_mfma_f32_16x16x32_bf16 a[192:195], a[204:207], v[200:203], a[192:195]\n"
      "v_cvt_pk_bf16_f32 v0, v112, v113\n"
      "v_mfma_f32_16x16x32_bf16 a[192:195], a[208:211], v[204:207], a[192:195]\n"
      "v_exp_f32 v115, v163\n"
      "s_waitcnt vmcnt(6) lgkmcnt(0)\n"
      "s_sub_u32 s67, s68, s90\n"
      "s_cmp_lt_i32 s67, 0\n"
      "s_cbranch_scc1 GW8_%=\n"
      "GX8_%=:\n"
      "s_barrier\n"
      "v_mfma_f32_16x16x32_bf16 v[64:67], a[104:107], v[208:211], v[64:67]\n"
      "ds_read_b128 a[64:67], v242 offset:0\n"
      "ds_read_b128 a[68:71], v242 offset:4096\n"
      "v_mfma_f32_16x16x32_bf16 v[68:71], a[104:107], v[212:215], v[68:71]\n"
      "ds_read_b128 a[72:75], v243 offset:0\n"
      "ds_read_b128 a[76:79], v243 offset:4096\n"
      "v_mfma_f32_16x16x32_bf16 v[72:75], a[104:107], v[216:219], v[72:75]\n"
      "ds_read_b128 a[80:83], v242 offset:1024\n"
      "ds_read_b128 a[84:87], v242 offset:5120\n"
      "v_exp_f32 v112, v164\n"
      "v_mfma_f32_16x16x32_bf16 v[76:79], a[104:107], v[220:223], v[76:79]\n"
      "ds_read_b128 a[88:91], v243 offset:1024\n"
      "ds_read_b128 a[92:95], v243 offset:5120\n"
      "v_mfma_f32_16x16x32_bf16 v[80:83], a[108:111], v[208:211], v[80:83]\n"
      "v_cvt_pk_bf16_f32 v1, v114, v115\n"
      "v_mfma_f32_16x16x32_bf16 v[84:87], a[108:111], v[212:215], v[84:87]\n"
      "v_exp_f32 v113, v165\n"
      "v_or_b32 v116, v0, v1\n"
      "v_mfma_f32_16x16x32_bf16 v[88:91], a[108:111], v[216:219], v[88:91]\n"
      "v_exp_f32 v114, v166\n"
      "v_cvt_pk_bf16_f32 v2, v112, v113\n"
      "v_mfma_f32_16x16x32_bf16 v[92:95], a[108:111], v[220:223], v[92:95]\n"
      "s_waitcnt lgkmcnt(6)\n"
      "v_exp_f32 v115, v167\n"
      "v_mfma_f32_16x16x32_bf16 v[128:131], a[64:67], a[0:3], v[224:227]\n"
      "v_exp_f32 v112, v168\n"
      "v_mfma_f32_16x16x32_bf16 v[136:139], a[64:67], a[4:7], v[228:231]\n"
      "s_add_u32 m0, %[wb], 115712\n"
      "v_cvt_pk_bf16_f32 v3, v114, v115\n"
      "v_exp_f32 v113, v169\n"
      "v_mfma_f32_16x16x32_bf16 v[144:147], a[64:67], a[8:11], v[232:235]\n"
      "buffer_load_dwordx4 v121, s[24:27], s95 offen nt lds\n"
      "v_mfma_f32_16x16x32_bf16 v[152:155], a[64:67], a[12:15], v[236:239]\n"
      "s_add_u32 m0, %[wb], 123904\n"
      "v_or3_b32 v116, v116, v2, v3\n"
      "v_mfma_f32_16x16x32_bf16 v[132:135], a[68:71], a[0:3], v[224:227]\n"
      "buffer_load_dwordx4 v123, s[24:27], s95 offen nt lds\n"
      "v_mfma_f32_16x16x32_bf16 v[140:143], a[68:71], a[4:7], v[228:231]\n"
      "s_add_u32 m0, %[wb], 17408\n"
      "v_exp_f32 v114, v170\n"
      "v_mfma_f32_16x16x32_bf16 v[148:151], a[68:71], a[8:11], v[232:235]\n"
      "buffer_load_dwordx4 v121, s[24:27], s77 offen nt lds\n"
      "v_mfma_f32_16x16x32_bf16 v[156:159], a[68:71], a[12:15], v[236:239]\n"
      "s_waitcnt lgkmcnt(4)\n"
      "s_add_u32 m0, %[wb], 25600\n"
      "v_cvt_pk_bf16_f32 v4, v112, v113\n"
      "v_mfma_f32_16x16x32_bf16 v[128:131], a[72:75], a[16:19], v[128:131]\n"
      "buffer_load_dwordx4 v123, s[24:27], s77 offen nt lds\n"
      "v_mfma_f32_16x16x32_bf16 v[136:139], a[72:75], a[20:23], v[136:139]\n"
      "v_exp_f32 v115, v171\n"
      "v_mfma_f32_16x16x32_bf16 v[144:147], a[72:75], a[24:27], v[144:147]\n"
      "v_exp_f32 v112, v172\n"
      "v_cvt_pk_bf16_f32 v5, v114, v115\n"
      "v_mfma_f32_16x16x32_bf16 v[152:155], a[72:75], a[28:31], v[152:155]\n"
      "v_exp_f32 v113, v173\n"
      "v_mfma_f32_16x16x32_bf16 v[132:135], a[76:79], a[16:19], v[132:135]\n"
      "v_or3_b32 v116, v116, v4, v5\n"
      "v_exp_f32 v114, v174\n"
      "v_mfma_f32_16x16x32_bf16 v[140:143], a[76:79], a[20:23], v[140:143]\n"
      "v_cvt_pk_bf16_f32 v6, v112, v113\n"
      "v_exp_f32 v115, v175\n"
      "v_mfma_f32_16x16x32_bf16 v[148:151], a[76:79], a[24:27], v[148:151]\n"
      "v_exp_f32 v112, v176\n"
      "v_mfma_f32_16x16x32_bf16 v[156:159], a[76:79], a[28:31], v[156:159]\n"
      "s_waitcnt lgkmcnt(2)\n"
      "v_cvt_pk_bf16_f32 v7, v114, v115\n"
      "v_mfma_f32_16x16x32_bf16 v[128:131], a[80:83], a[32:35], v[128:131]\n"
      "v_exp_f32 v113, v177\n"
      "v_or3_b32 v116, v116, v6, v7\n"
      "v_mfma_f32_16x16x32_bf16 v[136:139], a[80:83], a[36:39], v[136:139]\n"
      "v_exp_f32 v114, v178\n"
      "v_cvt_pk_bf16_f32 v8, v112, v113\n"
      "v_mfma_f32_16x16x32_bf16 v[144:147], a[80:83], a[40:43], v[144:147]\n"
      "v_exp_f32 v115, v179\n"
      "v_mfma_f32_16x16x32_bf16 v[152:155], a[80:83], a[44:47], v[152:155]\n"
      "v_exp_f32 v112, v180\n"
      "s_waitcnt vmcnt(8)\n"
      "s_barrier\n"
      "v_mfma_f32_16x16x32_bf16 v[132:135], a[84:87], a[32:35], v[132:135]\n"
      "ds_read_b64_tr_b16 a[112:113], v250 offset:1024\n"
      "v_cvt_pk_bf16_f32 v9, v114, v115\n"
      "v_mfma_f32_16x16x32_bf16 v[140:143], a[84:87], a[36:39], v[140:143]\n"
      "ds_read_b64_tr_b16 a[114:115], v250 offset:5120\n"
      "v_exp_f32 v113, v181\n"
      "v_mfma_f32_16x16x32_bf16 v[148:151], a[84:87], a[40:43], v[148:151]\n"
      "ds_read_b64_tr_b16 a[116:117], v251 offset:1024\n"
      "v_or3_b32 v116, v116, v8, v9\n"
      "v_mfma_f32_16x16x32_bf16 v[156:159], a[84:87], a[44:47], v[156:159]\n"
      "ds_read_b64_tr_b16 a[118:119], v251 offset:5120\n"
      "v_exp_f32 v114, v182\n"
      "s_waitcnt lgkmcnt(4)\n"
      "v_mfma_f32_16x16x32_bf16 v[128:131], a[88:91], a[48:51], v[128:131]\n"
      "ds_read_b64_tr_b16 a[120:121], v240 offset:1024\n"
      "v_cvt_pk_bf16_f32 v10, v112, v113\n"
      "v_mfma_f32_16x16x32_bf16 v[136:139], a[88:91], a[52:55], v[136:139]\n"
      "ds_read_b64_tr_b16 a[122:123], v240 offset:5120\n"
      "v_exp_f32 v115, v183\n"
      "v_mfma_f32_16x16x32_bf16 v[144:147], a[88:91], a[56:59], v[144:147]\n"
      "ds_read_b64_tr_b16 a[124:125], v241 offset:1024\n"
      "v_mfma_f32_16x16x32_bf16 v[152:155], a[88:91], a[60:63], v[152:155]\n"
      "ds_read_b64_tr_b16 a[126:127], v241 offset:5120\n"
      "v_exp_f32 v112, v184\n"
      "v_mfma_f32_16x16x32_bf16 v[132:135], a[92:95], a[48:51], v[132:135]\n"
      "v_cvt_pk_bf16_f32 v11, v114, v115\n"
      "v_exp_f32 v113, v185\n"
      "v_mfma_f32_16x16x32_bf16 v[140:143], a[92:95], a[52:55], v[140:143]\n"
      "v_or3_b32 v116, v116, v10, v11\n"
      "v_exp_f32 v114, v186\n"
      "v_mfma_f32_16x16x32_bf16 v[148:151], a[92:95], a[56:59], v[148:151]\n"
      "v_cvt_pk_bf16_f32 v12, v112, v113\n"
      "v_mfma_f32_16x16x32_bf16 v[156:159], a[92:95], a[60:63], v[156:159]\n"
      "s_waitcnt lgkmcnt(4)\n"
      "v_exp_f32 v115, v187\n"
      "v_mfma_f32_16x16x32_bf16 a[160:163], a[112:115], v[192:195], a[160:163]\n"
      "v_exp_f32 v112, v188\n"
      "v_cvt_pk_bf16_f32 v13, v114, v115\n"
      "v_mfma_f32_16x16x32_bf16 a[164:167], a[112:115], v[196:199], a[164:167]\n"
      "v_exp_f32 v113, v189\n"
      "v_or3_b32 v116, v116, v12, v13\n"
      "v_mfma_f32_16x16x32_bf16 a[168:171], a[112:115], v[200:203], a[168:171]\n"
      "v_exp_f32 v114, v190\n"
      "v_mfma_f32_16x16x32_bf16 a[172:175], a[112:115], v[204:207], a[172:175]\n"
      "v_cvt_pk_bf16_f32 v14, v112, v113\n"
      "v_exp_f32 v115, v191\n"
      "v_mfma_f32_16x16x32_bf16 a[176:179], a[116:119], v[192:195], a[176:179]\n"
      "s_mov_b32 s64, 5\n"
      "s_nop 0\n"
      "v_cvt_pk_bf16_f32 v15, v114, v115\n"
      "v_or3_b32 v116, v116, v14, v15\n"
      "v_mfma_f32_16x16x32_bf16 a[180:183], a[116:119], v[196:199], a[180:183]\n"
      "v_and_b32 v116, 0x40004000, v116\n"
      "v_cmp_ne_u32 vcc, 0, v116\n"
      "s_cbranch_vccnz FB1_%=\n"
      "RET1_5_%=:\n"
      "v_mfma_f32_16x16x32_bf16 a[184:187], a[116:119], v[200:203], a[184:187]\n"
      "v_exp_f32 v112, v128\n"
      "v_exp_f32 v113, v129\n"
      "v_mfma_f32_16x16x32_bf16 a[188:191], a[116:119], v[204:207], a[188:191]\n"
      "s_memrealtime s[68:69]\n"
      "s_add_u32 m0, %[wb], 50176\n"
      "v_exp_f32 v114, v130\n"
      "v_cvt_pk_bf16_f32 v16, v112, v113\n"
      "v_mfma_f32_16x16x32_bf16 a[192:195], a[196:199], v[0:3], a[192:195]\n"
      "buffer_load_dwordx4 v121, s[24:27], s78 offen nt lds\n"
      "v_mfma_f32_16x16x32_bf16 a[192:195], a[200:203], v[4:7], a[192:195]\n"
      "s_add_u32 m0, %[wb], 58368\n"
      "v_exp_f32 v115, v131\n"
      "v_exp_f32 v112, v132\n"
      "v_mfma_f32_16x16x32_bf16 a[192:195], a[204:207], v[8:11], a[192:195]\n"
      "buffer_load_dwordx4 v123, s[24:27], s78 offen nt lds\n"
      "v_cvt_pk_bf16_f32 v17, v114, v115\n"
      "v_mfma_f32_16x16x32_bf16 a[192:195], a[208:211], v[12:15], a[192:195]\n"
      "v_exp_f32 v113, v133\n"
      "v_or_b32 v116, v16, v17\n"
      "s_add_u32 s72, s72, 0x7f4a7c15\n"
      "s_addc_u32 s76, s79, 0x9e3779b9\n"
      "s_waitcnt vmcnt(6) lgkmcnt(0)\n"
      "s_barrier\n"
      "v_mfma_f32_16x16x32_bf16 v[64:67], a[120:123], v[192:195], v[64:67]\n"
      "s_xor_b32 s80, %[xo], 0x400000\n"
      "ds_read_b128 a[64:67], v242 offset:32768\n"
      "ds_read_b128 a[68:71], v242 offset:36864\n"
      "v_mfma_f32_16x16x32_bf16 v[68:71], a[120:123], v[196:199], v[68:71]\n"
      "s_add_u32 s79, s80, 0x1000\n"
      "ds_read_b128 a[72:75], v243 offset:32768\n"
      "ds_read_b128 a[76:79], v243 offset:36864\n"
      "v_exp_f32 v114, v134\n"
      "v_mfma_f32_16x16x32_bf16 v[72:75], a[120:123], v[200:203], v[72:75]\n"
      "s_add_u32 s81, %[xo], 0x1000\n"
      "ds_read_b128 a[80:83], v242 offset:33792\n"
      "ds_read_b128 a[84:87], v242 offset:37888\n"
      "v_cvt_pk_bf16_f32 v18, v112, v113\n"
      "v_mfma_f32_16x16x32_bf16 v[76:79], a[120:123], v[204:207], v[76:79]\n"
      "s_mov_b32 s63, 0x100000\n"
      "ds_read_b128 a[88:91], v243 offset:33792\n"
      "ds_read_b128 a[92:95], v243 offset:37888\n"
      "v_mfma_f32_16x16x32_bf16 v[80:83], a[124:127], v[192:195], v[80:83]\n"
      "s_mov_b32 s60, 0x928b2f\n"
      "v_exp_f32 v115, v135\n"
      "v_exp_f32 v112, v136\n"
      "v_mfma_f32_16x16x32_bf16 v[84:87], a[124:127], v[196:199], v[84:87]\n"
      "s_mov_b32 s62, 0x9818e9\n"
      "v_cvt_pk_bf16_f32 v19, v114, v115\n"
      "v_exp_f32 v113, v137\n"
      "v_mfma_f32_16x16x32_bf16 v[88:91], a[124:127], v[200:203], v[88:91]\n"
      "s_mov_b32 s70, 0xdd9dc9\n"
      "v_xor_b32 v248, 0x2000, v244\n"
      "v_or3_b32 v116, v116, v18, v19\n"
      "v_mfma_f32_16x16x32_bf16 v[92:95], a[124:127], v[204:207], v[92:95]\n"
      "s_waitcnt lgkmcnt(6)\n"
      "v_exp_f32 v114, v138\n"
      "v_cvt_pk_bf16_f32 v20, v112, v113\n"
      "v_mfma_f32_16x16x32_bf16 v[160:163], a[64:67], a[0:3], v[224:227]\n"
      "s_mov_b32 s71, 0x8ed905\n"
      "v_exp_f32 v115, v139\n"
      "v_mfma_f32_16x16x32_bf16 v[168:171], a[64:67], a[4:7], v[228:231]\n"
      "s_sub_u32 s88, 299, %[cph]\n"
      "v_xor_b32 v120, 0x2000, v245\n"
      "v_exp_f32 v112, v140\n"
      "v_cvt_pk_bf16_f32 v21, v114, v115\n"
      "v_mfma_f32_16x16x32_bf16 v[176:179], a[64:67], a[8:11], v[232:235]\n"
      "s_add_u32 s88, s88, s66\n"
      "v_exp_f32 v113, v141\n"
      "v_mfma_f32_16x16x32_bf16 v[184:187], a[64:67], a[12:15], v[236:239]\n"
      "s_add_u32 s88, s88, s100\n"
      "v_or3_b32 v116, v116, v20, v21\n"
      "v_exp_f32 v114, v142\n"
      "v_mfma_f32_16x16x32_bf16 v[164:167], a[68:71], a[0:3], v[224:227]\n"
      "v_mbcnt_lo_u32_b32 v127, -1, 0\n"
      "v_cvt_pk_bf16_f32 v22, v112, v113\n"
      "v_exp_f32 v115, v143\n"
      "v_mfma_f32_16x16x32_bf16 v[172:175], a[68:71], a[4:7], v[228:231]\n"
      "v_exp_f32 v112, v144\n"
      "v_mfma_f32_16x16x32_bf16 v[180:183], a[68:71], a[8:11], v[232:235]\n"
      "v_cvt_pk_bf16_f32 v23, v114, v115\n"
      "v_exp_f32 v113, v145\n"
      "v_mfma_f32_16x16x32_bf16 v[188:191], a[68:71], a[12:15], v[236:239]\n"
      "s_waitcnt lgkmcnt(4)\n"
      "v_mbcnt_hi_u32_b32 v127, -1, v127\n"
      "v_or3_b32 v116, v116, v22, v23\n"
      "v_mfma_f32_16x16x32_bf16 v[160:163], a[72:75], a[16:19], v[160:163]\n"
      "v_exp_f32 v114, v146\n"
      "v_cvt_pk_bf16_f32 v24, v112, v113\n"
      "v_mfma_f32_16x16x32_bf16 v[168:171], a[72:75], a[20:23], v[168:171]\n"
      "s_add_u32 m0, %[wb], 49152\n"
      "v_exp_f32 v115, v147\n"
      "v_exp_f32 v112, v148\n"
      "v_mfma_f32_16x16x32_bf16 v[176:179], a[72:75], a[24:27], v[176:179]\n"
      "buffer_load_dwordx4 v249, s[24:27], s61 offen nt lds\n"
      "v_lshlrev_b32 v127, 4, v127\n"
      "v_mfma_f32_16x16x32_bf16 v[184:187], a[72:75], a[28:31], v[184:187]\n"
      "s_add_u32 m0, %[wb], 57344\n"
      "v_cvt_pk_bf16_f32 v25, v114, v115\n"
      "v_mfma_f32_16x16x32_bf16 v[164:167], a[76:79], a[16:19], v[164:167]\n"
      "buffer_load_dwordx4 v122, s[24:27], s61 offen nt lds\n"
      "v_mfma_f32_16x16x32_bf16 v[172:175], a[76:79], a[20:23], v[172:175]\n"
      "v_add_u32 v125, %[xw], v127\n"
      "v_exp_f32 v113, v149\n"
      "v_mfma_f32_16x16x32_bf16 v[180:183], a[76:79], a[24:27], v[180:183]\n"
      "v_or3_b32 v116, v116, v24, v25\n"
      "v_exp_f32 v114, v150\n"
      "v_mfma_f32_16x16x32_bf16 v[188:191], a[76:79], a[28:31], v[188:191]\n"
      "s_waitcnt lgkmcnt(2)\n"
      "v_cvt_pk_bf16_f32 v26, v112, v113\n"
      "v_exp_f32 v115, v151\n"
      "v_mfma_f32_16x16x32_bf16 v[160:163], a[80:83], a[32:35], v[160:163]\n"
      "v_add_u32 v126, %[xr], v127\n"
      "v_exp_f32 v112, v152\n"
      "v_mfma_f32_16x16x32_bf16 v[168:171], a[80:83], a[36:39], v[168:171]\n"
      "v_cvt_pk_bf16_f32 v27, v114, v115\n"
      "v_exp_f32 v113, v153\n"
      "v_mfma_f32_16x16x32_bf16 v[176:179], a[80:83], a[40:43], v[176:179]\n"
      "v_or3_b32 v116, v116, v26, v27\n"
      "v_mfma_f32_16x16x32_bf16 v[184:187], a[80:83], a[44:47], v[184:187]\n"
      "v_exp_f32 v114, v154\n"
      "v_cvt_pk_bf16_f32 v28, v112, v113\n"
      "v_mfma_f32_16x16x32_bf16 v[164:167], a[84:87], a[32:35], v[164:167]\n"
      "v_exp_f32 v115, v155\n"
      "v_exp_f32 v112, v156\n"
      "v_mfma_f32_16x16x32_bf16 v[172:175], a[84:87], a[36:39], v[172:175]\n"
      "v_cvt_pk_bf16_f32 v29, v114, v115\n"
      "v_exp_f32 v113, v157\n"
      "v_mfma_f32_16x16x32_bf16 v[180:183], a[84:87], a[40:43], v[180:183]\n"
      "v_or3_b32 v116, v116, v28, v29\n"
      "v_exp_f32 v114, v158\n"
      "v_mov_b32 v123, s88\n"
      "v_mfma_f32_16x16x32_bf16 v[188:191], a[84:87], a[44:47], v[188:191]\n"
      "s_waitcnt lgkmcnt(0)\n"
      "v_cvt_pk_bf16_f32 v30, v112, v113\n"
      "v_exp_f32 v115, v159\n"
      "v_add_u32 v124, 12288, v125\n"
      "s_waitcnt vmcnt(4)\n"
      "s_barrier\n"
      "v_mfma_f32_16x16x32_bf16 v[160:163], a[88:91], a[48:51], v[160:163]\n"
      "ds_read_b64_tr_b16 a[96:97], v250 offset:33792\n"
      "ds_read_b64_tr_b16 a[98:99], v250 offset:37888\n"
      "s_nop 0\n"
      "v_mfma_f32_16x16x32_bf16 v[168:171], a[88:91], a[52:55], v[168:171]\n"
      "s_add_u32 m0, %[wb], 81920\n"
      "ds_read_b64_tr_b16 a[100:101], v251 offset:33792\n"
      "ds_read_b64_tr_b16 a[102:103], v251 offset:37888\n"
      "v_cvt_pk_bf16_f32 v31, v114, v115\n"
      "v_mfma_f32_16x16x32_bf16 v[176:179], a[88:91], a[56:59], v[176:179]\n"
      "buffer_load_dwordx4 v249, s[24:27], s94 offen nt lds\n"
      "ds_read_b64_tr_b16 a[112:113], v246 offset:1024\n"
      "ds_read_b64_tr_b16 a[114:115], v246 offset:5120\n"
      "v_mfma_f32_16x16x32_bf16 v[184:187], a[88:91], a[60:63], v[184:187]\n"
      "s_add_u32 m0, %[wb], 90112\n"
      "ds_read_b64_tr_b16 a[116:117], v247 offset:1024\n"
      "ds_read_b64_tr_b16 a[118:119], v247 offset:5120\n"
      "v_mfma_f32_16x16x32_bf16 v[164:167], a[92:95], a[48:51], v[164:167]\n"
      "buffer_load_dwordx4 v122, s[24:27], s94 offen nt lds\n"
      "ds_read_b64_tr_b16 a[104:105], v240 offset:33792\n"
      "v_mfma_f32_16x16x32_bf16 v[172:175], a[92:95], a[52:55], v[172:175]\n"
      "s_mov_b32 s64, 5\n"
      "ds_read_b64_tr_b16 a[106:107], v240 offset:37888\n"
      "v_mfma_f32_16x16x32_bf16 v[180:183], a[92:95], a[56:59], v[180:183]\n"
      "ds_read_b64_tr_b16 a[108:109], v241 offset:33792\n"
      "v_or3_b32 v116, v116, v30, v31\n"
      "v_mfma_f32_16x16x32_bf16 v[188:191], a[92:95], a[60:63], v[188:191]\n"
      "ds_read_b64_tr_b16 a[110:111], v241 offset:37888\n"
      "v_and_b32 v116, 0x40004000, v116\n"
      "v_cmp_ne_u32 vcc, 0, v116\n"
      "s_waitcnt lgkmcnt(4)\n"
      "s_cbranch_vccnz FB0_%=\n"
      "RET0_5_%=:\n"
      "v_mfma_f32_16x16x32_bf16 a[160:163], a[96:99], v[0:3], a[160:163]\n"
      "ds_read_b64_tr_b16 a[120:121], v244 offset:1024\n"
      "v_mov_b32 v117, v224\n"
      "v_xor_b32 v242, 0x10000, v242\n"
      "v_xor_b32 v243, 0x10000, v243\n"
      "v_mfma_f32_16x16x32_bf16 a[176:179], a[100:103], v[0:3], a[176:179]\n"
      "ds_read_b64_tr_b16 a[122:123], v244 offset:5120\n"
      "v_exp_f32 v112, v160\n"
      "v_mfma_f32_16x16x32_bf16 a[160:163], a[112:115], v[16:19], a[160:163]\n"
      "s_mov_b32 s64, 6\n"
      "ds_read_b64_tr_b16 a[124:125], v245 offset:1024\n"
      "v_exp_f32 v113, v161\n"
      "v_exp_f32 v114, v162\n"
      "v_mfma_f32_16x16x32_bf16 a[176:179], a[116:119], v[16:19], a[176:179]\n"
      "ds_read_b64_tr_b16 a[126:127], v245 offset:5120\n"
      "v_cvt_pk_bf16_f32 v96, v112, v113\n"
      "v_exp_f32 v115, v163\n"
      "v_mfma_f32_16x16x32_bf16 a[164:167], a[96:99], v[4:7], a[164:167]\n"
      "v_mov_b32_dpp v117, v228 quad_perm:[0,1,2,3] row_mask:0x2 bank_mask:0xf\n"
      "v_exp_f32 v112, v164\n"
      "v_mfma_f32_16x16x32_bf16 a[180:183], a[100:103], v[4:7], a[180:183]\n"
      "v_cvt_pk_bf16_f32 v97, v114, v115\n"
      "v_exp_f32 v113, v165\n"
      "v_or_b32 v116, v96, v97\n"
      "v_mfma_f32_16x16x32_bf16 a[164:167], a[112:115], v[20:23], a[164:167]\n"
      "v_exp_f32 v114, v166\n"
      "v_cvt_pk_bf16_f32 v98, v112, v113\n"
      "v_mfma_f32_16x16x32_bf16 a[180:183], a[116:119], v[20:23], a[180:183]\n"
      "ds_write_b128 v125, a[160:163] offset:0\n"
      "v_exp_f32 v115, v167\n"
      "v_mfma_f32_16x16x32_bf16 a[168:171], a[96:99], v[8:11], a[168:171]\n"
      "v_mov_b32_dpp v117, v232 quad_perm:[0,1,2,3] row_mask:0x4 bank_mask:0xf\n"
      "v_exp_f32 v112, v168\n"
      "v_cvt_pk_bf16_f32 v99, v114, v115\n"
      "v_mfma_f32_16x16x32_bf16 a[184:187], a[100:103], v[8:11], a[184:187]\n"
      "s_add_u32 m0, %[wb], 114688\n"
      "ds_write_b128 v125, v[96:99] offset:8192\n"
      "v_exp_f32 v113, v169\n"
      "v_mfma_f32_16x16x32_bf16 a[168:171], a[112:115], v[24:27], a[168:171]\n"
      "buffer_load_dwordx4 v249, s[24:27], s95 offen nt lds\n"
      "v_or3_b32 v116, v116, v98, v99\n"
      "v_mfma_f32_16x16x32_bf16 a[184:187], a[116:119], v[24:27], a[184:187]\n"
      "s_add_u32 m0, %[wb], 122880\n"
      "v_exp_f32 v114, v170\n"
      "v_cvt_pk_bf16_f32 v100, v112, v113\n"
      "v_mfma_f32_16x16x32_bf16 a[172:175], a[96:99], v[12:15], a[172:175]\n"
      "buffer_load_dwordx4 v122, s[24:27], s95 offen nt lds\n"
      "ds_write_b128 v125, a[176:179] offset:4096\n"
      "v_mov_b32_dpp v117, v236 quad_perm:[0,1,2,3] row_mask:0x8 bank_mask:0xf\n"
      "v_mfma_f32_16x16x32_bf16 a[188:191], a[100:103], v[12:15], a[188:191]\n"
      "v_exp_f32 v115, v171\n"
      "v_mfma_f32_16x16x32_bf16 a[172:175], a[112:115], v[28:31], a[172:175]\n"
      "v_exp_f32 v112, v172\n"
      "v_cvt_pk_bf16_f32 v101, v114, v115\n"
      "v_exp_f32 v113, v173\n"
      "v_mfma_f32_16x16x32_bf16 a[188:191], a[116:119], v[28:31], a[188:191]\n"
      "ds_write_b128 v125, a[164:167] offset:1024\n"
      "v_or3_b32 v116, v116, v100, v101\n"
      "v_mfma_f32_16x16x32_bf16 a[192:195], a[196:199], v[16:19], a[192:195]\n"
      "ds_write2_b32 v124, v117, v123 offset0:1 offset1:2\n"
      "v_exp_f32 v114, v174\n"
      "v_cvt_pk_bf16_f32 v102, v112, v113\n"
      "v_mfma_f32_16x16x32_bf16 a[192:195], a[200:203], v[20:23], a[192:195]\n"
      "v_exp_f32 v115, v175\n"
      "v_exp_f32 v112, v176\n"
      "v_mfma_f32_16x16x32_bf16 a[192:195], a[204:207], v[24:27], a[192:195]\n"
      "ds_write_b128 v125, a[180:183] offset:5120\n"
      "v_cvt_pk_bf16_f32 v103, v114, v115\n"
      "v_mfma_f32_16x16x32_bf16 a[192:195], a[208:211], v[28:31], a[192:195]\n"
      "s_waitcnt lgkmcnt(6)\n"
      "v_exp_f32 v113, v177\n"
      "v_or3_b32 v116, v116, v102, v103\n"
      "v_exp_f32 v114, v178\n"
      "v_mfma_f32_16x16x32_bf16 v[64:67], a[104:107], v[0:3], v[64:67]\n"
      "v_cvt_pk_bf16_f32 v104, v112, v113\n"
      "v_exp_f32 v115, v179\n"
      "v_mfma_f32_16x16x32_bf16 v[68:71], a[104:107], v[4:7], v[68:71]\n"
      "ds_write_b128 v125, v[100:103] offset:9216\n"
      "v_exp_f32 v112, v180\n"
      "s_waitcnt vmcnt(6)\n"
      "s_barrier\n"
      "v_mfma_f32_16x16x32_bf16 v[72:75], a[104:107], v[8:11], v[72:75]\n"
      "ds_read_b64_tr_b16 a[212:213], v244 offset:33792\n"
      "v_cvt_pk_bf16_f32 v105, v114, v115\n"
      "v_exp_f32 v113, v181\n"
      "v_mfma_f32_16x16x32_bf16 v[76:79], a[104:107], v[12:15], v[76:79]\n"
      "s_add_u32 m0, %[wb], 16384\n"
      "ds_write_b128 v125, a[168:171] offset:2048\n"
      "ds_read_b64_tr_b16 a[214:215], v244 offset:37888\n"
      "v_or3_b32 v116, v116, v104, v105\n"
      "v_mfma_f32_16x16x32_bf16 a[228:231], a[196:199], v[96:99], a[192:195]\n"
      "buffer_load_dwordx4 v249, s[24:27], s77 offen nt lds\n"
      "ds_read_b64_tr_b16 a[216:217], v245 offset:33792\n"
      "v_mfma_f32_16x16x32_bf16 v[80:83], a[108:111], v[0:3], v[80:83]\n"
      "s_add_u32 m0, %[wb], 24576\n"
      "ds_read_b64_tr_b16 a[218:219], v245 offset:37888\n"
      "v_exp_f32 v114, v182\n"
      "v_mfma_f32_16x16x32_bf16 v[84:87], a[108:111], v[4:7], v[84:87]\n"
      "buffer_load_dwordx4 v122, s[24:27], s77 offen nt lds\n"
      "ds_write_b128 v125, a[184:187] offset:6144\n"
      "ds_read_b64_tr_b16 a[220:221], v248 offset:33792\n"
      "v_mfma_f32_16x16x32_bf16 v[88:91], a[108:111], v[8:11], v[88:91]\n"
      "ds_read_b64_tr_b16 a[222:223], v248 offset:37888\n"
      "v_cvt_pk_bf16_f32 v106, v112, v113\n"
      "v_mfma_f32_16x16x32_bf16 v[92:95], a[108:111], v[12:15], v[92:95]\n"
      "ds_read_b64_tr_b16 a[224:225], v120 offset:33792\n"
      "v_exp_f32 v115, v183\n"
      "v_mfma_f32_16x16x32_bf16 v[64:67], a[120:123], v[16:19], v[64:67]\n"
      "ds_write_b128 v125, a[172:175] offset:3072\n"
      "ds_read_b64_tr_b16 a[226:227], v120 offset:37888\n"
      "v_exp_f32 v112, v184\n"
      "v_mfma_f32_16x16x32_bf16 v[68:71], a[120:123], v[20:23], v[68:71]\n"
      "v_cvt_pk_bf16_f32 v107, v114, v115\n"
      "v_exp_f32 v113, v185\n"
      "v_mfma_f32_16x16x32_bf16 v[72:75], a[120:123], v[24:27], v[72:75]\n"
      "v_or3_b32 v116, v116, v106, v107\n"
      "v_exp_f32 v114, v186\n"
      "v_cvt_pk_bf16_f32 v108, v112, v113\n"
      "v_mfma_f32_16x16x32_bf16 v[76:79], a[120:123], v[28:31], v[76:79]\n"
      "ds_write_b128 v125, v[104:107] offset:10240\n"
      "v_exp_f32 v115, v187\n"
      "v_mfma_f32_16x16x32_bf16 a[228:231], a[200:203], v[100:103], a[228:231]\n"
      "v_exp_f32 v112, v188\n"
      "v_cvt_pk_bf16_f32 v109, v114, v115\n"
      "v_mfma_f32_16x16x32_bf16 v[80:83], a[124:127], v[16:19], v[80:83]\n"
      "v_exp_f32 v113, v189\n"
      "v_or3_b32 v116, v116, v108, v109\n"
      "v_exp_f32 v114, v190\n"
      "v_mfma_f32_16x16x32_bf16 v[84:87], a[124:127], v[20:23], v[84:87]\n"
      "s_add_u32 m0, %[wb], 82944\n"
      "ds_write_b128 v125, a[188:191] offset:7168\n"
      "v_cvt_pk_bf16_f32 v110, v112, v113\n"
      "v_mfma_f32_16x16x32_bf16 v[88:91], a[124:127], v[24:27], v[88:91]\n"
      "buffer_load_dwordx4 v249, s[24:27], s78 offen nt lds\n"
      "v_exp_f32 v115, v191\n"
      "v_mfma_f32_16x16x32_bf16 v[92:95], a[124:127], v[28:31], v[92:95]\n"
      "s_add_u32 m0, %[wb], 91136\n"
      "s_nop 0\n"
      "v_cvt_pk_bf16_f32 v111, v114, v115\n"
      "v_or3_b32 v116, v116, v110, v111\n"
      "v_and_b32 v116, 0x40004000, v116\n"
      "v_mfma_f32_16x16x32_bf16 a[228:231], a[204:207], v[104:107], a[228:231]\n"
      "buffer_load_dwordx4 v122, s[24:27], s78 offen nt lds\n"
      "v_cmp_ne_u32 vcc, 0, v116\n"
      "s_cbranch_vccnz FB1_%=\n"
      "RET1_6_%=:\n"
      "ds_write_b128 v125, v[108:111] offset:11264\n"
      "v_mfma_f32_16x16x32_bf16 a[228:231], a[208:211], v[108:111], a[228:231]\n"
      "s_waitcnt lgkmcnt(9)\n"
      "v_mfma_f32_16x16x32_bf16 v[64:67], a[212:215], v[96:99], v[64:67]\n"
      "v_mov_b32 v148, 1.0\n"
      "v_mfma_f32_16x16x32_bf16 v[80:83], a[216:219], v[96:99], v[80:83]\n"
      "v_mov_b32 v149, 1.0\n"
      "v_mfma_f32_16x16x32_bf16 v[68:71], a[212:215], v[100:103], v[68:71]\n"
      "v_mov_b32 v157, %[fo]\n"
      "v_mfma_f32_16x16x32_bf16 v[84:87], a[216:219], v[100:103], v[84:87]\n"
      "v_mov_b32 v150, s72\n"
      "v_mfma_f32_16x16x32_bf16 v[72:75], a[212:215], v[104:107], v[72:75]\n"
      "v_mov_b32 v151, s76\n"
      "ds_write_b32 v125, a228 offset:12288\n"
      "v_mfma_f32_16x16x32_bf16 v[88:91], a[216:219], v[104:107], v[88:91]\n"
      "v_xor_b32 v158, 8, v157\n"
      "v_mfma_f32_16x16x32_bf16 v[76:79], a[212:215], v[108:111], v[76:79]\n"
      "v_mfma_f32_16x16x32_bf16 v[92:95], a[216:219], v[108:111], v[92:95]\n"
      "s_waitcnt lgkmcnt(0)\n"
      "s_barrier\n"
      "s_memrealtime s[68:69]\n"
      "ds_read_b32 v237, v127 offset:12296\n"
      "ds_read_b64 v[152:153], v126 offset:12288\n"
      "ds_read_b128 v[160:163], v126 offset:0\n"
      "ds_read_b128 v[176:179], v126 offset:4096\n"
      "ds_read_b128 v[164:167], v126 offset:1024\n"
      "ds_read_b128 v[180:183], v126 offset:5120\n"
      "ds_read_b128 v[168:171], v126 offset:2048\n"
      "ds_read_b128 v[184:187], v126 offset:6144\n"
      "ds_read_b128 v[172:175], v126 offset:3072\n"
      "ds_read_b128 v[188:191], v126 offset:7168\n"
      "ds_read_b128 a[64:67], v126 offset:8192\n"
      "ds_read_b128 a[68:71], v126 offset:9216\n"
      "ds_read_b128 a[72:75], v126 offset:10240\n"
      "ds_read_b128 a[76:79], v126 offset:11264\n"
      "v_accvgpr_read_b32 v144, a228\n"
      "v_mov_b32 v236, v150\n"
      "s_waitcnt lgkmcnt(12)\n"
      "v_cmp_neq_f32 vcc, v153, v117\n"
      "v_readfirstlane_b32 s88, v237\n"
      "s_cbranch_vccnz MIS_%=\n"
      "MR_%=:\n"
      "v_mul_f32 v144, v144, v148\n"
      "v_mov_b32 v145, v117\n"
      "v_fmac_f32 v144, v152, v149\n"
      "s_waitcnt lgkmcnt(10)\n"
      "v_pk_add_f32 v[64:65], v[64:65], v[160:161]\n"
      "v_pk_add_f32 v[66:67], v[66:67], v[162:163]\n"
      "v_pk_add_f32 v[80:81], v[80:81], v[176:177]\n"
      "v_pk_add_f32 v[82:83], v[82:83], v[178:179]\n"
      "s_waitcnt lgkmcnt(8)\n"
      "v_pk_add_f32 v[68:69], v[68:69], v[164:165]\n"
      "v_pk_add_f32 v[70:71], v[70:71], v[166:167]\n"
      "v_pk_add_f32 v[84:85], v[84:85], v[180:181]\n"
      "v_pk_add_f32 v[86:87], v[86:87], v[182:183]\n"
      "s_waitcnt lgkmcnt(6)\n"
      "v_pk_add_f32 v[72:73], v[72:73], v[168:169]\n"
      "v_pk_add_f32 v[74:75], v[74:75], v[170:171]\n"
      "v_pk_add_f32 v[88:89], v[88:89], v[184:185]\n"
      "v_pk_add_f32 v[90:91], v[90:91], v[186:187]\n"
      "s_waitcnt lgkmcnt(4)\n"
      "v_pk_add_f32 v[76:77], v[76:77], v[172:173]\n"
      "v_pk_add_f32 v[78:79], v[78:79], v[174:175]\n"
      "v_pk_add_f32 v[92:93], v[92:93], v[188:189]\n"
      "v_pk_add_f32 v[94:95], v[94:95], v[190:191]\n"
      "s_waitcnt lgkmcnt(0)\n"
      "s_add_u32 s65, s68, 55\n"
      "s_mov_b32 s66, s88\n"
      "s_sub_u32 s67, s65, s66\n"
      "s_cmp_lt_i32 s67, 0\n"
      "s_cselect_b32 s88, s66, s65\n"
      "v_mov_b32 v237, s88\n"
      "s_mov_b32 s88, s66\n"
      "s_cmp_lg_u32 %[xw], 0\n"
      "s_cbranch_scc1 NH_%=\n"
      "s_mov_b64 exec, 1\n"
      "global_store_dwordx2 v157, v[236:237], s[18:19] sc1\n"
      "s_mov_b64 exec, -1\n"
      "NH_%=:\n"
      "v_mfma_f32_16x16x32_bf16 v[64:67], a[220:223], a[64:67], v[64:67]\n"
      "v_and_b32 v118, 0xf0, v127\n"
      "v_lshrrev_b32 v156, 3, v127\n"
      "v_mfma_f32_16x16x32_bf16 v[80:83], a[224:227], a[64:67], v[80:83]\n"
      "v_and_b32 v156, 32, v156\n"
      "v_lshl_or_b32 v156, v118, 4, v156\n"
      "v_mfma_f32_16x16x32_bf16 v[68:71], a[220:223], a[68:71], v[68:71]\n"
      "v_lshrrev_b32 v118, 5, v127\n"
      "v_and_or_b32 v156, v118, 16, v156\n"
      "v_mfma_f32_16x16x32_bf16 v[84:87], a[224:227], a[68:71], v[84:87]\n"
      "v_add_u32 v156, %[oo], v156\n"
      "v_add_u32 v155, 0x2000, v156\n"
      "v_mfma_f32_16x16x32_bf16 v[72:75], a[220:223], a[72:75], v[72:75]\n"
      "v_cvt_pk_bf16_f32 v128, v64, v65\n"
      "v_cvt_pk_bf16_f32 v129, v66, v67\n"
      "v_mfma_f32_16x16x32_bf16 v[88:91], a[224:227], a[72:75], v[88:91]\n"
      "v_cvt_pk_bf16_f32 v130, v80, v81\n"
      "v_cvt_pk_bf16_f32 v131, v82, v83\n"
      "v_mfma_f32_16x16x32_bf16 v[76:79], a[220:223], a[76:79], v[76:79]\n"
      "v_xor_b32 v242, 0x2000, v240\n"
      "v_xor_b32 v243, 0x2000, v241\n"
      "v_mfma_f32_16x16x32_bf16 v[92:95], a[224:227], a[76:79], v[92:95]\n"
      "v_cvt_pk_bf16_f32 v132, v68, v69\n"
      "v_cvt_pk_bf16_f32 v133, v70, v71\n"
      "s_memrealtime s[68:69]\n"
      "v_cvt_pk_bf16_f32 v134, v84, v85\n"
      "v_cvt_pk_bf16_f32 v135, v86, v87\n"
      "v_mad_u32_u24 v147, v144, s60, v151\n"
      "v_mad_u32_u24 v147, v145, s62, v147\n"
      "v_add3_u32 v146, v150, v144, v145\n"
      "v_mad_u32_u24 v147, v128, s60, v147\n"
      "v_mad_u32_u24 v147, v129, s62, v147\n"
      "v_add3_u32 v146, v146, v128, v129\n"
      "v_mad_u32_u24 v147, v130, s70, v147\n"
      "v_mad_u32_u24 v147, v131, s71, v147\n"
      "v_add3_u32 v146, v146, v130, v131\n"
      "v_cvt_pk_bf16_f32 v136, v72, v73\n"
      "v_cvt_pk_bf16_f32 v137, v74, v75\n"
      "v_cvt_pk_bf16_f32 v138, v88, v89\n"
      "v_cvt_pk_bf16_f32 v139, v90, v91\n"
      "v_cvt_pk_bf16_f32 v140, v76, v77\n"
      "v_cvt_pk_bf16_f32 v141, v78, v79\n"
      "v_cvt_pk_bf16_f32 v142, v92, v93\n"
      "v_cvt_pk_bf16_f32 v143, v94, v95\n"
      "v_mad_u32_u24 v147, v132, s60, v147\n"
      "v_mad_u32_u24 v147, v133, s62, v147\n"
      "v_add3_u32 v146, v146, v132, v133\n"
      "v_mad_u32_u24 v147, v134, s70, v147\n"
      "v_mad_u32_u24 v147, v135, s71, v147\n"
      "v_add3_u32 v146, v146, v134, v135\n"
      "v_mad_u32_u24 v147, v136, s60, v147\n"
      "v_mad_u32_u24 v147, v137, s62, v147\n"
      "v_add3_u32 v146, v146, v136, v137\n"
      "v_mad_u32_u24 v147, v138, s70, v147\n"
      "v_mad_u32_u24 v147, v139, s71, v147\n"
      "v_add3_u32 v146, v146, v138, v139\n"
      "v_mad_u32_u24 v147, v140, s60, v147\n"
      "v_mad_u32_u24 v147, v141, s62, v147\n"
      "v_add3_u32 v146, v146, v140, v141\n"
      "v_mad_u32_u24 v147, v142, s70, v147\n"
      "v_mad_u32_u24 v147, v143, s71, v147\n"
      "v_add3_u32 v146, v146, v142, v143\n"
      "s_waitcnt lgkmcnt(0)\n"
      "s_call_b64 s[84:85], SPIN_%=\n"
      "global_load_dwordx2 v[232:233], v158, s[18:19] sc1\n"
      "buffer_store_dwordx4 v[128:131], v127, s[28:31], %[xo] offen offset:0 sc1\n"
      "buffer_store_dwordx4 v[132:135], v127, s[28:31], %[xo] offen offset:1024 sc1\n"
      "buffer_store_dwordx4 v[136:139], v127, s[28:31], %[xo] offen offset:2048 sc1\n"
      "buffer_store_dwordx4 v[140:143], v127, s[28:31], %[xo] offen offset:3072 sc1\n"
      "buffer_store_dwordx4 v[144:147], v127, s[28:31], s81 offen sc1\n"
      "global_load_dwordx2 v[234:235], v158, s[18:19] sc1\n"
      "s_waitcnt vmcnt(7)\n"
      "s_barrier\n"
      "ds_read_b64_tr_b16 a[96:97], v246 offset:32768\n"
      "ds_read_b64_tr_b16 a[98:99], v246 offset:36864\n"
      "ds_read_b64_tr_b16 a[100:101], v247 offset:32768\n"
      "ds_read_b64_tr_b16 a[102:103], v247 offset:36864\n"
      "ds_read_b64_tr_b16 a[112:113], v250 offset:0\n"
      "ds_read_b64_tr_b16 a[114:115], v250 offset:4096\n"
      "ds_read_b64_tr_b16 a[116:117], v251 offset:0\n"
      "ds_read_b64_tr_b16 a[118:119], v251 offset:4096\n"
      "ds_read_b64_tr_b16 a[0:1], v250 offset:32768\n"
      "ds_read_b64_tr_b16 a[2:3], v250 offset:36864\n"
      "ds_read_b64_tr_b16 a[4:5], v251 offset:32768\n"
      "ds_read_b64_tr_b16 a[6:7], v251 offset:36864\n"
      "s_waitcnt lgkmcnt(8)\n"
      "v_mfma_f32_16x16x32_bf16 a[128:131], a[96:99], v[208:211], a[128:131]\n"
      "ds_read_b64_tr_b16 a[104:105], v244 offset:32768\n"
      "ds_read_b64_tr_b16 a[106:107], v244 offset:36864\n"
      "v_mfma_f32_16x16x32_bf16 a[144:147], a[100:103], v[208:211], a[144:147]\n"
      "ds_read_b64_tr_b16 a[108:109], v245 offset:32768\n"
      "ds_read_b64_tr_b16 a[110:111], v245 offset:36864\n"
      "s_waitcnt lgkmcnt(8)\n"
      "v_mfma_f32_16x16x32_bf16 a[128:131], a[112:115], v[192:195], a[128:131]\n"
      "ds_read_b64_tr_b16 a[120:121], v240 offset:0\n"
      "ds_read_b64_tr_b16 a[122:123], v240 offset:4096\n"
      "v_mfma_f32_16x16x32_bf16 a[144:147], a[116:119], v[192:195], a[144:147]\n"
      "ds_read_b64_tr_b16 a[124:125], v241 offset:0\n"
      "ds_read_b64_tr_b16 a[126:127], v241 offset:4096\n"
      "s_waitcnt lgkmcnt(8)\n"
      "v_mfma_f32_16x16x32_bf16 a[128:131], a[0:3], v[0:3], a[128:131]\n"
      "ds_read_b64_tr_b16 a[8:9], v240 offset:32768\n"
      "ds_read_b64_tr_b16 a[10:11], v240 offset:36864\n"
      "v_mfma_f32_16x16x32_bf16 a[144:147], a[4:7], v[0:3], a[144:147]\n"
      "ds_read_b64_tr_b16 a[12:13], v241 offset:32768\n"
      "ds_read_b64_tr_b16 a[14:15], v241 offset:36864\n"
      "v_mfma_f32_16x16x32_bf16 a[132:135], a[96:99], v[212:215], a[132:135]\n"
      "ds_write_b128 v125, v[16:19] offset:8192\n"
      "v_lshrrev_b32 v118, 2, v127\n"
      "v_mfma_f32_16x16x32_bf16 a[148:151], a[100:103], v[212:215], a[148:151]\n"
      "v_xor_b32 v118, v118, v127\n"
      "v_mfma_f32_16x16x32_bf16 a[132:135], a[112:115], v[196:199], a[132:135]\n"
      "v_and_b32 v118, 0xc0, v118\n"
      "v_mfma_f32_16x16x32_bf16 a[148:151], a[116:119], v[196:199], a[148:151]\n"
      "v_and_or_b32 v118, v127, 32, v118\n"
      "v_mfma_f32_16x16x32_bf16 a[132:135], a[0:3], v[4:7], a[132:135]\n"
      "ds_write_b128 v125, v[20:23] offset:9216\n"
      "v_and_b32 v119, 16, v127\n"
      "v_mfma_f32_16x16x32_bf16 a[148:151], a[4:7], v[4:7], a[148:151]\n"
      "v_mov_b32 v84, 0x3f80\n"
      "v_mfma_f32_16x16x32_bf16 a[136:139], a[96:99], v[216:219], a[136:139]\n"
      "v_lshlrev_b32 v119, v119, v84\n"
      "v_mfma_f32_16x16x32_bf16 a[152:155], a[100:103], v[216:219], a[152:155]\n"
      "v_cmp_eq_u32 vcc, 0, v118\n"
      "v_mfma_f32_16x16x32_bf16 a[136:139], a[112:115], v[200:203], a[136:139]\n"
      "ds_write_b128 v125, v[24:27] offset:10240\n"
      "v_mov_b32 v84, 0\n"
      "v_mfma_f32_16x16x32_bf16 a[152:155], a[116:119], v[200:203], a[152:155]\n"
      "v_cndmask_b32 v86, 0, v119, vcc\n"
      "v_mfma_f32_16x16x32_bf16 a[136:139], a[0:3], v[8:11], a[136:139]\n"
      "v_cmp_eq_u32 vcc, 32, v118\n"
      "v_mfma_f32_16x16x32_bf16 a[152:155], a[4:7], v[8:11], a[152:155]\n"
      "v_mov_b32 v85, 0\n"
      "v_mfma_f32_16x16x32_bf16 a[140:143], a[96:99], v[220:223], a[140:143]\n"
      "ds_write_b128 v125, v[28:31] offset:11264\n"
      "v_cndmask_b32 v87, 0, v119, vcc\n"
      "v_mfma_f32_16x16x32_bf16 a[156:159], a[100:103], v[220:223], a[156:159]\n"
      "v_mov_b32 v88, 0\n"
      "v_mfma_f32_16x16x32_bf16 a[140:143], a[112:115], v[204:207], a[140:143]\n"
      "v_mov_b32 v89, 0\n"
      "v_mfma_f32_16x16x32_bf16 a[156:159], a[116:119], v[204:207], a[156:159]\n"
      "v_mfma_f32_16x16x32_bf16 a[140:143], a[0:3], v[12:15], a[140:143]\n"
      "ds_write_b128 v125, a[128:131] offset:0\n"
      "v_mfma_f32_16x16x32_bf16 a[156:159], a[4:7], v[12:15], a[156:159]\n"
      "s_waitcnt lgkmcnt(13)\n"
      "v_mfma_f32_16x16x32_bf16 v[32:35], a[104:107], v[208:211], v[32:35]\n"
      "ds_read_b64_tr_b16 a[32:33], v240 offset:1024\n"
      "v_mfma_f32_16x16x32_bf16 v[36:39], a[104:107], v[212:215], v[36:39]\n"
      "ds_read_b64_tr_b16 a[34:35], v240 offset:5120\n"
      "v_mfma_f32_16x16x32_bf16 v[40:43], a[104:107], v[216:219], v[40:43]\n"
      "ds_write_b128 v125, a[144:147] offset:4096\n"
      "ds_read_b64_tr_b16 a[36:37], v241 offset:1024\n"
      "v_mfma_f32_16x16x32_bf16 v[44:47], a[104:107], v[220:223], v[44:47]\n"
      "ds_read_b64_tr_b16 a[38:39], v241 offset:5120\n"
      "v_mfma_f32_16x16x32_bf16 v[48:51], a[108:111], v[208:211], v[48:51]\n"
      "ds_read_b64_tr_b16 a[40:41], v242 offset:1024\n"
      "v_mfma_f32_16x16x32_bf16 v[52:55], a[108:111], v[212:215], v[52:55]\n"
      "ds_read_b64_tr_b16 a[42:43], v242 offset:5120\n"
      "v_mfma_f32_16x16x32_bf16 v[56:59], a[108:111], v[216:219], v[56:59]\n"
      "ds_write_b128 v125, a[132:135] offset:1024\n"
      "ds_read_b64_tr_b16 a[44:45], v243 offset:1024\n"
      "v_mfma_f32_16x16x32_bf16 v[60:63], a[108:111], v[220:223], v[60:63]\n"
      "ds_read_b64_tr_b16 a[46:47], v243 offset:5120\n"
      "s_waitcnt lgkmcnt(15)\n"
      "v_mfma_f32_16x16x32_bf16 v[32:35], a[120:123], v[192:195], v[32:35]\n"
      "ds_read_b64_tr_b16 a[16:17], v244 offset:0\n"
      "v_mfma_f32_16x16x32_bf16 v[36:39], a[120:123], v[196:199], v[36:39]\n"
      "ds_read_b64_tr_b16 a[18:19], v244 offset:4096\n"
      "v_mfma_f32_16x16x32_bf16 v[40:43], a[120:123], v[200:203], v[40:43]\n"
      "ds_write_b128 v125, a[148:151] offset:5120\n"
      "ds_read_b64_tr_b16 a[20:21], v245 offset:0\n"
      "v_mfma_f32_16x16x32_bf16 v[44:47], a[120:123], v[204:207], v[44:47]\n"
      "ds_read_b64_tr_b16 a[22:23], v245 offset:4096\n"
      "v_mfma_f32_16x16x32_bf16 v[48:51], a[124:127], v[192:195], v[48:51]\n"
      "ds_read_b64_tr_b16 a[24:25], v248 offset:0\n"
      "v_mfma_f32_16x16x32_bf16 v[52:55], a[124:127], v[196:199], v[52:55]\n"
      "ds_read_b64_tr_b16 a[26:27], v248 offset:4096\n"
      "v_mfma_f32_16x16x32_bf16 v[56:59], a[124:127], v[200:203], v[56:59]\n"
      "ds_write_b128 v125, a[136:139] offset:2048\n"
      "ds_read_b64_tr_b16 a[28:29], v120 offset:0\n"
      "v_mfma_f32_16x16x32_bf16 v[60:63], a[124:127], v[204:207], v[60:63]\n"
      "ds_read_b64_tr_b16 a[30:31], v120 offset:4096\n"
      "s_waitcnt lgkmcnt(15)\n"
      "v_mfma_f32_16x16x32_bf16 v[32:35], a[8:11], v[0:3], v[32:35]\n"
      "v_mfma_f32_16x16x32_bf16 v[36:39], a[8:11], v[4:7], v[36:39]\n"
      "v_mfma_f32_16x16x32_bf16 v[40:43], a[8:11], v[8:11], v[40:43]\n"
      "ds_write_b128 v125, a[152:155] offset:6144\n"
      "v_mfma_f32_16x16x32_bf16 v[44:47], a[8:11], v[12:15], v[44:47]\n"
      "s_waitcnt vmcnt(6)\n"
      "v_cmp_ne_u32 vcc, v232, v150\n"
      "s_memrealtime s[68:69]\n"
      "s_cbranch_vccnz POLL_%=\n"
      "GOT_%=:\n"
      "v_readfirstlane_b32 s88, v233\n"
      "s_waitcnt lgkmcnt(0)\n"
      "s_add_u32 s88, s88, 60\n"
      "s_sub_u32 s67, s88, s68\n"
      "s_cmp_gt_u32 s67, 1000\n"
      "s_cbranch_scc1 LD_%=\n"
      "s_call_b64 s[84:85], SPIN_%=\n"
      "LD_%=:\n"
      "buffer_load_dwordx4 v[80:83], v127, s[28:31], s79 offen sc1\n"
      "buffer_load_dwordx4 v[64:67], v127, s[28:31], s80 offen offset:0 sc1\n"
      "buffer_load_dwordx4 v[68:71], v127, s[28:31], s80 offen offset:1024 sc1\n"
      "buffer_load_dwordx4 v[72:75], v127, s[28:31], s80 offen offset:2048 sc1\n"
      "buffer_load_dwordx4 v[76:79], v127, s[28:31], s80 offen offset:3072 sc1\n"
      "v_mfma_f32_16x16x32_bf16 v[48:51], a[12:15], v[0:3], v[48:51]\n"
      "v_mfma_f32_16x16x32_bf16 v[52:55], a[12:15], v[4:7], v[52:55]\n"
      "v_mfma_f32_16x16x32_bf16 v[56:59], a[12:15], v[8:11], v[56:59]\n"
      "ds_write_b128 v125, a[140:143] offset:3072\n"
      "v_mfma_f32_16x16x32_bf16 v[60:63], a[12:15], v[12:15], v[60:63]\n"
      "s_waitcnt lgkmcnt(15)\n"
      "v_mfma_f32_16x16x32_bf16 v[32:35], a[32:35], v[96:99], v[32:35]\n"
      "v_mfma_f32_16x16x32_bf16 v[48:51], a[36:39], v[96:99], v[48:51]\n"
      "v_mfma_f32_16x16x32_bf16 v[36:39], a[32:35], v[100:103], v[36:39]\n"
      "ds_write_b128 v125, a[156:159] offset:7168\n"
      "v_mfma_f32_16x16x32_bf16 v[52:55], a[36:39], v[100:103], v[52:55]\n"
      "v_mfma_f32_16x16x32_bf16 v[40:43], a[32:35], v[104:107], v[40:43]\n"
      "v_mfma_f32_16x16x32_bf16 v[56:59], a[36:39], v[104:107], v[56:59]\n"
      "v_mfma_f32_16x16x32_bf16 v[44:47], a[32:35], v[108:111], v[44:47]\n"
      "v_mfma_f32_16x16x32_bf16 v[60:63], a[36:39], v[108:111], v[60:63]\n"
      "s_waitcnt lgkmcnt(13)\n"
      "v_mfma_f32_16x16x32_bf16 v[32:35], a[40:43], a[64:67], v[32:35]\n"
      "v_mfma_f32_16x16x32_bf16 v[48:51], a[44:47], a[64:67], v[48:51]\n"
      "v_mfma_f32_16x16x32_bf16 v[36:39], a[40:43], a[68:71], v[36:39]\n"
      "v_mfma_f32_16x16x32_bf16 v[52:55], a[44:47], a[68:71], v[52:55]\n"
      "v_mfma_f32_16x16x32_bf16 v[40:43], a[40:43], a[72:75], v[40:43]\n"
      "v_mfma_f32_16x16x32_bf16 v[56:59], a[44:47], a[72:75], v[56:59]\n"
      "v_mfma_f32_16x16x32_bf16 v[44:47], a[40:43], a[76:79], v[44:47]\n"
      "v_mfma_f32_16x16x32_bf16 v[60:63], a[44:47], a[76:79], v[60:63]\n"
      "s_waitcnt lgkmcnt(8)\n"
      "v_mfma_f32_16x16x32_bf16 v[32:35], a[16:19], v[16:19], v[32:35]\n"
      "v_mfma_f32_16x16x32_bf16 v[48:51], a[20:23], v[16:19], v[48:51]\n"
      "v_mfma_f32_16x16x32_bf16 v[36:39], a[16:19], v[20:23], v[36:39]\n"
      "v_mfma_f32_16x16x32_bf16 v[52:55], a[20:23], v[20:23], v[52:55]\n"
      "v_mfma_f32_16x16x32_bf16 v[40:43], a[16:19], v[24:27], v[40:43]\n"
      "v_mfma_f32_16x16x32_bf16 v[56:59], a[20:23], v[24:27], v[56:59]\n"
      "v_mfma_f32_16x16x32_bf16 v[44:47], a[16:19], v[28:31], v[44:47]\n"
      "v_mfma_f32_16x16x32_bf16 v[60:63], a[20:23], v[28:31], v[60:63]\n"
      "s_waitcnt lgkmcnt(0)\n"
      "s_barrier\n"
      "ds_read_b128 v[160:163], v126 offset:0\n"
      "ds_read_b128 v[176:179], v126 offset:4096\n"
      "ds_read_b128 v[164:167], v126 offset:1024\n"
      "ds_read_b128 v[180:183], v126 offset:5120\n"
      "ds_read_b128 v[168:171], v126 offset:2048\n"
      "ds_read_b128 v[184:187], v126 offset:6144\n"
      "ds_read_b128 v[172:175], v126 offset:3072\n"
      "ds_read_b128 v[188:191], v126 offset:7168\n"
      "ds_read_b128 a[80:83], v126 offset:8192\n"
      "ds_read_b128 a[84:87], v126 offset:9216\n"
      "ds_read_b128 a[88:91], v126 offset:10240\n"
      "ds_read_b128 a[92:95], v126 offset:11264\n"
      "s_waitcnt lgkmcnt(10)\n"
      "v_pk_add_f32 v[32:33], v[32:33], v[160:161]\n"
      "v_pk_add_f32 v[34:35], v[34:35], v[162:163]\n"
      "v_pk_add_f32 v[48:49], v[48:49], v[176:177]\n"
      "v_pk_add_f32 v[50:51], v[50:51], v[178:179]\n"
      "s_waitcnt lgkmcnt(8)\n"
      "v_pk_add_f32 v[36:37], v[36:37], v[164:165]\n"
      "v_pk_add_f32 v[38:39], v[38:39], v[166:167]\n"
      "v_pk_add_f32 v[52:53], v[52:53], v[180:181]\n"
      "v_pk_add_f32 v[54:55], v[54:55], v[182:183]\n"
      "s_waitcnt lgkmcnt(6)\n"
      "v_pk_add_f32 v[40:41], v[40:41], v[168:169]\n"
      "v_pk_add_f32 v[42:43], v[42:43], v[170:171]\n"
      "v_pk_add_f32 v[56:57], v[56:57], v[184:185]\n"
      "v_pk_add_f32 v[58:59], v[58:59], v[186:187]\n"
      "s_waitcnt lgkmcnt(4)\n"
      "v_pk_add_f32 v[44:45], v[44:45], v[172:173]\n"
      "v_pk_add_f32 v[46:47], v[46:47], v[174:175]\n"
      "v_pk_add_f32 v[60:61], v[60:61], v[188:189]\n"
      "v_pk_add_f32 v[62:63], v[62:63], v[190:191]\n"
      "s_waitcnt lgkmcnt(0)\n"
      "v_mfma_f32_16x16x32_bf16 v[32:35], a[24:27], a[80:83], v[32:35]\n"
      "v_mfma_f32_16x16x32_bf16 v[48:51], a[28:31], a[80:83], v[48:51]\n"
      "v_mfma_f32_16x16x32_bf16 v[36:39], a[24:27], a[84:87], v[36:39]\n"
      "v_mfma_f32_16x16x32_bf16 v[52:55], a[28:31], a[84:87], v[52:55]\n"
      "v_mfma_f32_16x16x32_bf16 v[40:43], a[24:27], a[88:91], v[40:43]\n"
      "v_mfma_f32_16x16x32_bf16 v[56:59], a[28:31], a[88:91], v[56:59]\n"
      "v_mfma_f32_16x16x32_bf16 v[44:47], a[24:27], a[92:95], v[44:47]\n"
      "v_mfma_f32_16x16x32_bf16 v[60:63], a[28:31], a[92:95], v[60:63]\n"
      "CHK_%=:\n"
      "s_waitcnt vmcnt(4)\n"
      "v_add_f32 v224, v144, v80\n"
      "v_rcp_f32 v224, v224\n"
      "v_mad_u32_u24 v119, v80, s60, v151\n"
      "v_mad_u32_u24 v119, v81, s62, v119\n"
      "v_add3_u32 v118, v150, v80, v81\n"
      "s_waitcnt vmcnt(3)\n"
      "v_mfma_f32_16x16x32_bf16 v[160:163], v[86:89], v[64:67], v[32:35]\n"
      "v_mad_u32_u24 v119, v64, s60, v119\n"
      "v_mad_u32_u24 v119, v65, s62, v119\n"
      "v_add3_u32 v118, v118, v64, v65\n"
      "v_mov_b32 v226, v224\n"
      "v_mfma_f32_16x16x32_bf16 v[176:179], v[84:87], v[64:67], v[48:51]\n"
      "v_mad_u32_u24 v119, v66, s70, v119\n"
      "v_mad_u32_u24 v119, v67, s71, v119\n"
      "v_add3_u32 v118, v118, v66, v67\n"
      "v_permlane32_swap_b32 v224, v226\n"
      "s_waitcnt vmcnt(2)\n"
      "v_mfma_f32_16x16x32_bf16 v[164:167], v[86:89], v[68:71], v[36:39]\n"
      "v_mad_u32_u24 v119, v68, s60, v119\n"
      "v_mad_u32_u24 v119, v69, s62, v119\n"
      "v_add3_u32 v118, v118, v68, v69\n"
      "v_mov_b32 v225, v224\n"
      "v_mov_b32 v227, v226\n"
      "v_mfma_f32_16x16x32_bf16 v[180:183], v[84:87], v[68:71], v[52:55]\n"
      "v_mad_u32_u24 v119, v70, s70, v119\n"
      "v_mad_u32_u24 v119, v71, s71, v119\n"
      "v_add3_u32 v118, v118, v70, v71\n"
      "v_permlane16_swap_b32 v224, v225\n"
      "s_waitcnt vmcnt(1)\n"
      "v_mfma_f32_16x16x32_bf16 v[168:171], v[86:89], v[72:75], v[40:43]\n"
      "v_mad_u32_u24 v119, v72, s60, v119\n"
      "v_mad_u32_u24 v119, v73, s62, v119\n"
      "v_add3_u32 v118, v118, v72, v73\n"
      "v_permlane16_swap_b32 v226, v227\n"
      "v_mfma_f32_16x16x32_bf16 v[184:187], v[84:87], v[72:75], v[56:59]\n"
      "v_mad_u32_u24 v119, v74, s70, v119\n"
      "v_mad_u32_u24 v119, v75, s71, v119\n"
      "v_add3_u32 v118, v118, v74, v75\n"
      "s_waitcnt vmcnt(0)\n"
      "v_mfma_f32_16x16x32_bf16 v[172:175], v[86:89], v[76:79], v[44:47]\n"
      "v_mad_u32_u24 v119, v76, s60, v119\n"
      "v_mad_u32_u24 v119, v77, s62, v119\n"
      "v_add3_u32 v118, v118, v76, v77\n"
      "v_mfma_f32_16x16x32_bf16 v[188:191], v[84:87], v[76:79], v[60:63]\n"
      "v_mad_u32_u24 v119, v78, s70, v119\n"
      "v_mad_u32_u24 v119, v79, s71, v119\n"
      "v_add3_u32 v118, v118, v78, v79\n"
      "v_cmp_ne_u64 vcc, v[118:119], v[82:83]\n"
      "s_cbranch_vccnz RETRY_%=\n"
      "OK_%=:\n"
      "v_cmp_neq_f32 vcc, v81, v117\n"
      "s_cbranch_vccnz FIX_%=\n"
      "FR_%=:\n"
      "v_pk_mul_f32 v[160:161], v[160:161], v[224:225] op_sel:[0,0] op_sel_hi:[1,0]\n"
      "v_pk_mul_f32 v[162:163], v[162:163], v[224:225] op_sel:[0,0] op_sel_hi:[1,0]\n"
      "v_pk_mul_f32 v[176:177], v[176:177], v[224:225] op_sel:[0,0] op_sel_hi:[1,0]\n"
      "v_pk_mul_f32 v[178:179], v[178:179], v[224:225] op_sel:[0,0] op_sel_hi:[1,0]\n"
      "v_cvt_pk_bf16_f32 v128, v160, v161\n"
      "v_cvt_pk_bf16_f32 v130, v176, v177\n"
      "v_cvt_pk_bf16_f32 v129, v162, v163\n"
      "v_cvt_pk_bf16_f32 v131, v178, v179\n"
      "v_pk_mul_f32 v[164:165], v[164:165], v[224:225] op_sel:[0,1] op_sel_hi:[1,1]\n"
      "v_permlane16_swap_b32 v128, v130\n"
      "v_permlane16_swap_b32 v129, v131\n"
      "global_store_dwordx4 v156, v[128:131], s[16:17] offset:-4096 sc1\n"
      "v_pk_mul_f32 v[166:167], v[166:167], v[224:225] op_sel:[0,1] op_sel_hi:[1,1]\n"
      "v_pk_mul_f32 v[180:181], v[180:181], v[224:225] op_sel:[0,1] op_sel_hi:[1,1]\n"
      "v_pk_mul_f32 v[182:183], v[182:183], v[224:225] op_sel:[0,1] op_sel_hi:[1,1]\n"
      "v_cvt_pk_bf16_f32 v132, v164, v165\n"
      "v_cvt_pk_bf16_f32 v134, v180, v181\n"
      "v_cvt_pk_bf16_f32 v133, v166, v167\n"
      "v_cvt_pk_bf16_f32 v135, v182, v183\n"
      "v_pk_mul_f32 v[168:169], v[168:169], v[226:227] op_sel:[0,0] op_sel_hi:[1,0]\n"
      "v_permlane16_swap_b32 v132, v134\n"
      "v_permlane16_swap_b32 v133, v135\n"
      "global_store_dwordx4 v156, v[132:135], s[16:17] offset:0 sc1\n"
      "v_pk_mul_f32 v[170:171], v[170:171], v[226:227] op_sel:[0,0] op_sel_hi:[1,0]\n"
      "v_pk_mul_f32 v[184:185], v[184:185], v[226:227] op_sel:[0,0] op_sel_hi:[1,0]\n"
      "v_pk_mul_f32 v[186:187], v[186:187], v[226:227] op_sel:[0,0] op_sel_hi:[1,0]\n"
      "v_cvt_pk_bf16_f32 v136, v168, v169\n"
      "v_cvt_pk_bf16_f32 v138, v184, v185\n"
      "v_cvt_pk_bf16_f32 v137, v170, v171\n"
      "v_cvt_pk_bf16_f32 v139, v186, v187\n"
      "v_pk_mul_f32 v[172:173], v[172:173], v[226:227] op_sel:[0,1] op_sel_hi:[1,1]\n"
      "v_permlane16_swap_b32 v136, v138\n"
      "v_permlane16_swap_b32 v137, v139\n"
      "global_store_dwordx4 v155, v[136:139], s[16:17] offset:-4096 sc1\n"
      "v_pk_mul_f32 v[174:175], v[174:175], v[226:227] op_sel:[0,1] op_sel_hi:[1,1]\n"
      "v_pk_mul_f32 v[188:189], v[188:189], v[226:227] op_sel:[0,1] op_sel_hi:[1,1]\n"
      "v_pk_mul_f32 v[190:191], v[190:191], v[226:227] op_sel:[0,1] op_sel_hi:[1,1]\n"
      "v_cvt_pk_bf16_f32 v140, v172, v173\n"
      "v_cvt_pk_bf16_f32 v142, v188, v189\n"
      "v_cvt_pk_bf16_f32 v141, v174, v175\n"
      "v_cvt_pk_bf16_f32 v143, v190, v191\n"
      "v_and_b32 v157, 0xffffff80, v157\n"
      "v_permlane16_swap_b32 v140, v142\n"
      "v_permlane16_swap_b32 v141, v143\n"
      "global_store_dwordx4 v155, v[140:143], s[16:17] offset:0 sc1\n"
      "s_and_b32 s67, %[fo], 8\n"
      "s_or_b32 s67, s67, %[xw]\n"
      "s_cbranch_scc1 END_%=\n"
      "s_mov_b64 exec, 1\n"
      "global_store_dwordx2 v157, v[150:151], s[18:19] sc1\n"
      "s_mov_b64 exec, -1\n"
      "END_%=:\n"
      ".subsection 1\n"
      "POLL_%=:\n"
      "s_waitcnt vmcnt(0) lgkmcnt(0)\n"
      "v_cmp_ne_u32 vcc, v234, v150\n"
      "v_mov_b32 v233, v235\n"
      "s_memrealtime s[68:69]\n"
      "s_sub_u32 s63, s63, 1\n"
      "s_cbranch_vccz GOT_%=\n"
      "global_load_dwordx2 v[234:235], v158, s[18:19] sc1\n"
      "s_cbranch_scc0 POLL_%=\n"
      "s_branch GOT_%=\n"
      "RETRY_%=:\n"
      "s_sub_u32 s63, s63, 1\n"
      "s_cbranch_scc1 OK_%=\n"
      "s_sleep 4\n"
      "buffer_load_dwordx4 v[80:83], v127, s[28:31], s79 offen sc1\n"
      "buffer_load_dwordx4 v[64:67], v127, s[28:31], s80 offen offset:0 sc1\n"
      "buffer_load_dwordx4 v[68:71], v127, s[28:31], s80 offen offset:1024 sc1\n"
      "buffer_load_dwordx4 v[72:75], v127, s[28:31], s80 offen offset:2048 sc1\n"
      "buffer_load_dwordx4 v[76:79], v127, s[28:31], s80 offen offset:3072 sc1\n"
      "s_branch CHK_%=\n"
      "GW0_%=:\n"
      "s_call_b64 s[84:85], WAIT_%=\n"
      "s_branch GX0_%=\n"
      "GW1_%=:\n"
      "s_call_b64 s[84:85], WAIT_%=\n"
      "s_branch GX1_%=\n"
      "GW2_%=:\n"
      "s_call_b64 s[84:85], WAIT_%=\n"
      "s_branch GX2_%=\n"
      "GW3_%=:\n"
      "s_call_b64 s[84:85], WAIT_%=\n"
      "s_add_u32 s87, s87, 1\n"
      "s_branch GX3_%=\n"
      "GW4_%=:\n"
      "s_call_b64 s[84:85], WAIT_%=\n"
      "s_branch GX4_%=\n"
      "GW5_%=:\n"
      "s_call_b64 s[84:85], WAIT_%=\n"
      "s_branch GX5_%=\n"
      "GW6_%=:\n"
      "s_call_b64 s[84:85], WAIT_%=\n"
      "s_branch GX6_%=\n"
      "GW7_%=:\n"
      "s_call_b64 s[84:85], WAIT_%=\n"
      "s_branch GX7_%=\n"
      "GW8_%=:\n"
      "s_call_b64 s[84:85], WAIT_%=\n"
      "s_branch GX8_%=\n"
      "WAIT_%=:\n"
      "s_add_u32 s87, s87, 1\n"
      "s_bitcmp1_b32 s89, s87\n"
      "s_cbranch_scc1 H1_%=\n"
      "SPIN_%=:\n"
      "s_sub_u32 s67, s68, s88\n"
      "s_cmp_ge_i32 s67, 0\n"
      "s_cbranch_scc1 GO_%=\n"
      "s_sleep 1\n"
      "s_memrealtime s[68:69]\n"
      "s_waitcnt lgkmcnt(0)\n"
      "s_branch SPIN_%=\n"
      "GO_%=:\n"
      "s_setpc_b64 s[84:85]\n"
      "H1_%=:\n"
      "s_cmp_gt_i32 s93, 50\n"
      "s_cbranch_scc1 HS_%=\n"
      "s_bitcmp1_b32 0x2a80, s87\n"
      "s_cbranch_scc0 H2_%=\n"
      "s_add_u32 s67, s66, 400\n"
      "s_waitcnt vmcnt(8)\n"
      "v_sub_u32 v250, s67, v250\n"
      "v_cmp_gt_u32 vcc, 900, v250\n"
      "v_add_u32 v250, 0xa00, v250\n"
      "v_xor_b32 v251, 0x100, v251\n"
      "s_sub_u32 s69, s66, s71\n"
      "s_cmp_ge_u32 s87, 12\n"
      "s_cselect_b32 s101, 50, s101\n"
      "s_bcnt1_i32_b32 s74, vcc_lo\n"
      "s_bcnt1_i32_b32 s75, vcc_hi\n"
      "v_cmp_gt_u32 vcc, 0x800, v250\n"
      "s_add_u32 s67, s74, s75\n"
      "s_cmp_eq_u32 s87, 7\n"
      "s_cselect_b32 s76, 1, 0\n"
      "s_lshr_b32 s76, s67, s76\n"
      "s_cmp_le_u32 s76, 2\n"
      "s_cselect_b32 s86, 107, s98\n"
      "s_cmp_ge_u32 s76, 9\n"
      "s_cselect_b32 s86, 112, s86\n"
      "s_cbranch_vccnz H6_%=\n"
      "s_cmp_ge_u32 s74, 12\n"
      "s_cbranch_scc1 H3_%=\n"
      "s_cmp_eq_u32 s87, 13\n"
      "s_cbranch_scc1 H4_%=\n"
      "H8_%=:\n"
      "s_lshr_b32 s67, %[rep], 7\n"
      "s_cmp_eq_u32 s67, 1\n"
      "s_cbranch_scc0 SPIN_%=\n"
      "H5_%=:\n"
      "s_cmp_gt_i32 s93, 50\n"
      "s_cselect_b32 s75, -10, 0\n"
      "v_mov_b32 v127, s66\n"
      "s_add_u32 s74, s87, 2\n"
      "s_bfe_u32 s74, s74, 0x10001\n"
      "s_lshl_b32 s74, s74, 8\n"
      "s_add_u32 s74, s74, %[rep]\n"
      "RS_%=:\n"
      "s_sub_u32 s67, s68, s88\n"
      "s_cmp_ge_i32 s67, s75\n"
      "s_cbranch_scc1 RG_%=\n"
      "s_sleep 1\n"
      "s_memrealtime s[68:69]\n"
      "s_waitcnt lgkmcnt(0)\n"
      "s_branch RS_%=\n"
      "RG_%=:\n"
      "s_getreg_b32 s67, hwreg(HW_REG_IB_STS, 0, 4)\n"
      "s_cmp_le_u32 s67, 4\n"
      "s_cbranch_scc1 SPIN_%=\n"
      "buffer_store_dword v127, v124, s[28:31], s74 offen sc1\n"
      "s_branch SPIN_%=\n"
      "H6_%=:\n"
      "s_ff1_i32_b64 s67, vcc\n"
      "v_sub_u32 v250, s66, v250\n"
      "s_nop 3\n"
      "v_readlane_b32 s76, v250, s67\n"
      "s_add_u32 s76, s76, 2960\n"
      "s_and_b32 s76, s76, 31\n"
      "s_lshl_b32 s76, s76, 3\n"
      "s_lshl_b32 s89, 4, s87\n"
      "s_branch H7_%=\n"
      "H3_%=:\n"
      "s_mul_i32 s76, s69, 110\n"
      "s_ashr_i32 s76, s76, 10\n"
      "s_min_i32 s76, s76, 40\n"
      "s_add_u32 s76, s76, 76\n"
      "s_cmp_lt_u32 s75, 24\n"
      "s_cselect_b32 s76, 56, s76\n"
      "s_cmp_lt_u32 s75, 13\n"
      "s_cselect_b32 s76, 40, s76\n"
      "s_cmp_lt_u32 s75, 5\n"
      "s_cselect_b32 s76, 24, s76\n"
      "s_add_u32 s67, s66, 2000\n"
      "s_and_b32 s67, s67, -32\n"
      "s_lshr_b32 s75, s76, 3\n"
      "s_or_b32 s67, s67, s75\n"
      "v_mov_b32 v127, s67\n"
      "s_add_u32 s74, s87, 2\n"
      "s_bfe_u32 s74, s74, 0x10001\n"
      "s_lshl_b32 s74, s74, 8\n"
      "s_add_u32 s74, s74, %[rep]\n"
      "s_cmp_lt_u32 %[rep], 128\n"
      "s_cselect_b32 s89, 22, 20\n"
      "s_lshl_b32 s89, s89, s87\n"
      "s_sub_u32 s75, %[rep], 128\n"
      "s_cmp_lt_u32 s75, 32\n"
      "s_cbranch_scc0 H7_%=\n"
      "buffer_store_dword v127, v124, s[28:31], s74 offen sc1\n"
      "H7_%=:\n"
      "s_add_u32 s66, s66, s76\n"
      "s_mov_b32 s99, 17\n"
      "s_mov_b32 s100, 288\n"
      "s_add_u32 s93, s93, 99\n"
      "s_mov_b32 s86, 110\n"
      "s_mov_b32 s80, 0xc853212b\n"
      "s_mov_b32 s81, 0x842210\n"
      "s_cmp_le_u32 s76, 40\n"
      "s_cselect_b32 s75, 15, 0\n"
      "s_cmp_le_u32 s76, 24\n"
      "s_cselect_b32 s75, 25, s75\n"
      "s_lshr_b64 s[80:81], s[80:81], s75\n"
      "s_sub_u32 s67, s65, s66\n"
      "s_cmp_lt_i32 s67, 0\n"
      "s_cselect_b32 s88, s66, s65\n"
      "s_mov_b32 s101, 200\n"
      "s_branch SPIN_%=\n"
      "H4_%=:\n"
      "s_mov_b32 s91, 0\n"
      "s_mov_b32 s89, 0\n"
      "v_mov_b32 v251, 0x4000000\n"
      "s_branch SPIN_%=\n"
      "HS_%=:\n"
      "s_bitcmp1_b32 s87, 0\n"
      "s_cbranch_scc0 H5_%=\n"
      "v_xor_b32 v251, 0x100, v251\n"
      "s_lshr_b32 s67, s89, s87\n"
      "s_cmp_gt_u32 s67, 1\n"
      "s_cbranch_scc1 H8_%=\n"
      "s_add_u32 s67, s66, 400\n"
      "s_waitcnt vmcnt(8)\n"
      "v_sub_u32 v250, s67, v250\n"
      "v_cmp_gt_u32 vcc, 900, v250\n"
      "s_nop 3\n"
      "s_bcnt1_i32_b32 s74, vcc_lo\n"
      "s_bcnt1_i32_b32 s75, vcc_hi\n"
      "s_cmp_ge_u32 s75, 5\n"
      "s_cbranch_scc1 HT_%=\n"
      "s_cmp_gt_u32 s74, 2\n"
      "s_cbranch_scc1 H4_%=\n"
      "s_mov_b32 s99, 27\n"
      "s_mov_b32 s100, 285\n"
      "s_mov_b32 s86, 107\n"
      "s_mov_b64 s[80:81], 0\n"
      "s_branch H4_%=\n"
      "HT_%=:\n"
      "s_add_u32 s66, s66, 64\n"
      "s_mov_b32 s80, 0x86429909\n"
      "s_mov_b32 s81, 0x42110\n"
      "s_sub_u32 s67, s65, s66\n"
      "s_cmp_lt_i32 s67, 0\n"
      "s_cselect_b32 s88, s66, s65\n"
      "s_branch H4_%=\n"
      "H0_%=:\n"
      "buffer_load_dword v250, v251, s[28:31], 0 offen sc1\n"
      "s_branch SPIN_%=\n"
      "H9_%=:\n"
      "s_bitcmp1_b32 s87, 0\n"
      "s_cbranch_scc0 H5_%=\n"
      "v_xor_b32 v251, 0x100, v251\n"
      "s_cmp_eq_u32 s87, 5\n"
      "s_cbranch_scc1 H8_%=\n"
      "s_branch SPIN_%=\n"
      "H2_%=:\n"
      "s_cmp_gt_u32 s87, 2\n"
      "s_cbranch_scc1 H9_%=\n"
      "s_cmp_eq_u32 s87, 1\n"
      "s_cbranch_scc1 H0_%=\n"
      "s_waitcnt vmcnt(8)\n"
      "v_subrev_u32 v250, s71, v250\n"
      "v_min_u32 v250, 0x258, v250\n"
      "v_mbcnt_lo_u32_b32 v251, -1, 0\n"
      "v_mbcnt_hi_u32_b32 v251, -1, v251\n"
      "v_add_u32_dpp v250, v250, v250 row_shr:1 row_mask:0xf bank_mask:0xf bound_ctrl:0\n"
      "v_lshlrev_b32 v124, 23, v251\n"
      "s_nop 0\n"
      "v_add_u32_dpp v250, v250, v250 row_shr:2 row_mask:0xf bank_mask:0xf bound_ctrl:0\n"
      "v_lshlrev_b32 v251, 2, v251\n"
      "v_add_u32 v124, 0x6000, v124\n"
      "v_add_u32_dpp v250, v250, v250 row_shr:4 row_mask:0xf bank_mask:0xf bound_ctrl:0\n"
      "v_add_u32 v251, 0x6100, v251\n"
      "v_readlane_b32 s66, v250, 7\n"
      "s_nop 0\n"
      "s_lshr_b32 s66, s66, 3\n"
      "s_add_u32 s66, s66, s71\n"
      "s_add_u32 s66, s66, %[cph]\n"
      "s_add_u32 s71, s66, 550\n"
      "s_sub_u32 s67, s65, s66\n"
      "s_cmp_lt_i32 s67, 0\n"
      "s_cselect_b32 s88, s66, s65\n"
      "s_branch SPIN_%=\n"
      "FB0_%=:\n"
      "s_nop 7\n"
      "s_nop 7\n"
      "v_xor_b32 v118, 0x0, v242\n"
      "v_xor_b32 v119, 0x0, v243\n"
      "ds_read_b128 a[64:67], v118 offset:0\n"
      "ds_read_b128 a[68:71], v118 offset:4096\n"
      "ds_read_b128 a[72:75], v119 offset:0\n"
      "ds_read_b128 a[76:79], v119 offset:4096\n"
      "ds_read_b128 a[80:83], v118 offset:1024\n"
      "ds_read_b128 a[84:87], v118 offset:5120\n"
      "ds_read_b128 a[88:91], v119 offset:1024\n"
      "ds_read_b128 a[92:95], v119 offset:5120\n"
      "s_waitcnt lgkmcnt(0)\n"
      "v_mfma_f32_16x16x32_bf16 v[128:131], a[64:67], a[0:3], 0\n"
      "v_mfma_f32_16x16x32_bf16 v[136:139], a[64:67], a[4:7], 0\n"
      "v_mfma_f32_16x16x32_bf16 v[144:147], a[64:67], a[8:11], 0\n"
      "v_mfma_f32_16x16x32_bf16 v[152:155], a[64:67], a[12:15], 0\n"
      "v_mfma_f32_16x16x32_bf16 v[132:135], a[68:71], a[0:3], 0\n"
      "v_mfma_f32_16x16x32_bf16 v[140:143], a[68:71], a[4:7], 0\n"
      "v_mfma_f32_16x16x32_bf16 v[148:151], a[68:71], a[8:11], 0\n"
      "v_mfma_f32_16x16x32_bf16 v[156:159], a[68:71], a[12:15], 0\n"
      "v_mfma_f32_16x16x32_bf16 v[128:131], a[72:75], a[16:19], v[128:131]\n"
      "v_mfma_f32_16x16x32_bf16 v[136:139], a[72:75], a[20:23], v[136:139]\n"
      "v_mfma_f32_16x16x32_bf16 v[144:147], a[72:75], a[24:27], v[144:147]\n"
      "v_mfma_f32_16x16x32_bf16 v[152:155], a[72:75], a[28:31], v[152:155]\n"
      "v_mfma_f32_16x16x32_bf16 v[132:135], a[76:79], a[16:19], v[132:135]\n"
      "v_mfma_f32_16x16x32_bf16 v[140:143], a[76:79], a[20:23], v[140:143]\n"
      "v_mfma_f32_16x16x32_bf16 v[148:151], a[76:79], a[24:27], v[148:151]\n"
      "v_mfma_f32_16x16x32_bf16 v[156:159], a[76:79], a[28:31], v[156:159]\n"
      "v_mfma_f32_16x16x32_bf16 v[128:131], a[80:83], a[32:35], v[128:131]\n"
      "v_mfma_f32_16x16x32_bf16 v[136:139], a[80:83], a[36:39], v[136:139]\n"
      "v_mfma_f32_16x16x32_bf16 v[144:147], a[80:83], a[40:43], v[144:147]\n"
      "v_mfma_f32_16x16x32_bf16 v[152:155], a[80:83], a[44:47], v[152:155]\n"
      "v_mfma_f32_16x16x32_bf16 v[132:135], a[84:87], a[32:35], v[132:135]\n"
      "v_mfma_f32_16x16x32_bf16 v[140:143], a[84:87], a[36:39], v[140:143]\n"
      "v_mfma_f32_16x16x32_bf16 v[148:151], a[84:87], a[40:43], v[148:151]\n"
      "v_mfma_f32_16x16x32_bf16 v[156:159], a[84:87], a[44:47], v[156:159]\n"
      "v_mfma_f32_16x16x32_bf16 v[128:131], a[88:91], a[48:51], v[128:131]\n"
      "v_mfma_f32_16x16x32_bf16 v[136:139], a[88:91], a[52:55], v[136:139]\n"
      "v_mfma_f32_16x16x32_bf16 v[144:147], a[88:91], a[56:59], v[144:147]\n"
      "v_mfma_f32_16x16x32_bf16 v[152:155], a[88:91], a[60:63], v[152:155]\n"
      "v_mfma_f32_16x16x32_bf16 v[132:135], a[92:95], a[48:51], v[132:135]\n"
      "v_mfma_f32_16x16x32_bf16 v[140:143], a[92:95], a[52:55], v[140:143]\n"
      "v_mfma_f32_16x16x32_bf16 v[148:151], a[92:95], a[56:59], v[148:151]\n"
      "v_mfma_f32_16x16x32_bf16 v[156:159], a[92:95], a[60:63], v[156:159]\n"
      "s_nop 7\n"
      "v_max3_f32 v112, v128, v129, v130\n"
      "v_max3_f32 v113, v136, v137, v138\n"
      "v_max3_f32 v114, v144, v145, v146\n"
      "v_max3_f32 v115, v152, v153, v154\n"
      "v_max3_f32 v112, v112, v131, v132\n"
      "v_max3_f32 v113, v113, v139, v140\n"
      "v_max3_f32 v114, v114, v147, v148\n"
      "v_max3_f32 v115, v115, v155, v156\n"
      "v_max3_f32 v112, v112, v133, v134\n"
      "v_max3_f32 v113, v113, v141, v142\n"
      "v_max3_f32 v114, v114, v149, v150\n"
      "v_max3_f32 v115, v115, v157, v158\n"
      "v_max_f32 v112, v112, v135\n"
      "v_max_f32 v113, v113, v143\n"
      "v_max_f32 v114, v114, v151\n"
      "v_max_f32 v115, v115, v159\n"
      "v_mov_b32 v160, v112\n"
      "v_mov_b32 v161, v113\n"
      "v_mov_b32 v162, v114\n"
      "v_mov_b32 v163, v115\n"
      "v_permlane16_swap_b32 v112, v160\n"
      "v_permlane16_swap_b32 v113, v161\n"
      "v_permlane16_swap_b32 v114, v162\n"
      "v_permlane16_swap_b32 v115, v163\n"
      "v_max_f32 v112, v112, v160\n"
      "v_max_f32 v113, v113, v161\n"
      "v_max_f32 v114, v114, v162\n"
      "v_max_f32 v115, v115, v163\n"
      "v_mov_b32 v160, v112\n"
      "v_mov_b32 v161, v113\n"
      "v_mov_b32 v162, v114\n"
      "v_mov_b32 v163, v115\n"
      "v_permlane32_swap_b32 v112, v160\n"
      "v_permlane32_swap_b32 v113, v161\n"
      "v_permlane32_swap_b32 v114, v162\n"
      "v_permlane32_swap_b32 v115, v163\n"
      "v_max_f32 v112, v112, v160\n"
      "v_max_f32 v113, v113, v161\n"
      "v_max_f32 v114, v114, v162\n"
      "v_max_f32 v115, v115, v163\n"
      "v_sub_f32 v112, 0xc1800000, v112\n"
      "v_sub_f32 v113, 0xc1800000, v113\n"
      "v_sub_f32 v114, 0xc1800000, v114\n"
      "v_sub_f32 v115, 0xc1800000, v115\n"
      "v_floor_f32 v112, v112\n"
      "v_floor_f32 v113, v113\n"
      "v_floor_f32 v114, v114\n"
      "v_floor_f32 v115, v115\n"
      "v_min_f32 v112, v112, v224\n"
      "v_min_f32 v113, v113, v228\n"
      "v_min_f32 v114, v114, v232\n"
      "v_min_f32 v115, v115, v236\n"
      "v_sub_f32 v164, v112, v224\n"
      "v_sub_f32 v165, v113, v228\n"
      "v_sub_f32 v166, v114, v232\n"
      "v_sub_f32 v167, v115, v236\n"
      "v_mov_b32 v224, v112\n"
      "v_mov_b32 v228, v113\n"
      "v_mov_b32 v232, v114\n"
      "v_mov_b32 v236, v115\n"
      "v_exp_f32 v112, v164\n"
      "v_exp_f32 v113, v165\n"
      "v_exp_f32 v114, v166\n"
      "v_exp_f32 v115, v167\n"
      "v_mov_b32 v225, v224\n"
      "v_mov_b32 v229, v228\n"
      "v_mov_b32 v233, v232\n"
      "v_mov_b32 v237, v236\n"
      "v_mov_b64 v[226:227], v[224:225]\n"
      "v_mov_b64 v[230:231], v[228:229]\n"
      "v_mov_b64 v[234:235], v[232:233]\n"
      "v_mov_b64 v[238:239], v[236:237]\n"
      "s_call_b64 s[84:85], SCALE_%=\n"
      "v_pk_add_f32 v[128:129], v[128:129], v[224:225]\n"
      "v_pk_add_f32 v[130:131], v[130:131], v[224:225]\n"
      "v_pk_add_f32 v[132:133], v[132:133], v[224:225]\n"
      "v_pk_add_f32 v[134:135], v[134:135], v[224:225]\n"
      "v_pk_add_f32 v[136:137], v[136:137], v[228:229]\n"
      "v_pk_add_f32 v[138:139], v[138:139], v[228:229]\n"
      "v_pk_add_f32 v[140:141], v[140:141], v[228:229]\n"
      "v_pk_add_f32 v[142:143], v[142:143], v[228:229]\n"
      "v_pk_add_f32 v[144:145], v[144:145], v[232:233]\n"
      "v_pk_add_f32 v[146:147], v[146:147], v[232:233]\n"
      "v_pk_add_f32 v[148:149], v[148:149], v[232:233]\n"
      "v_pk_add_f32 v[150:151], v[150:151], v[232:233]\n"
      "v_pk_add_f32 v[152:153], v[152:153], v[236:237]\n"
      "v_pk_add_f32 v[154:155], v[154:155], v[236:237]\n"
      "v_pk_add_f32 v[156:157], v[156:157], v[236:237]\n"
      "v_pk_add_f32 v[158:159], v[158:159], v[236:237]\n"
      "v_exp_f32 v112, v128\n"
      "v_exp_f32 v113, v129\n"
      "v_exp_f32 v114, v130\n"
      "v_cvt_pk_bf16_f32 v128, v112, v113\n"
      "v_exp_f32 v115, v131\n"
      "v_exp_f32 v112, v132\n"
      "v_cvt_pk_bf16_f32 v129, v114, v115\n"
      "v_exp_f32 v113, v133\n"
      "v_exp_f32 v114, v134\n"
      "v_cvt_pk_bf16_f32 v130, v112, v113\n"
      "v_exp_f32 v115, v135\n"
      "v_exp_f32 v112, v136\n"
      "v_cvt_pk_bf16_f32 v131, v114, v115\n"
      "v_exp_f32 v113, v137\n"
      "v_exp_f32 v114, v138\n"
      "v_cvt_pk_bf16_f32 v132, v112, v113\n"
      "v_exp_f32 v115, v139\n"
      "v_exp_f32 v112, v140\n"
      "v_cvt_pk_bf16_f32 v133, v114, v115\n"
      "v_exp_f32 v113, v141\n"
      "v_exp_f32 v114, v142\n"
      "v_cvt_pk_bf16_f32 v134, v112, v113\n"
      "v_exp_f32 v115, v143\n"
      "v_exp_f32 v112, v144\n"
      "v_cvt_pk_bf16_f32 v135, v114, v115\n"
      "v_exp_f32 v113, v145\n"
      "v_exp_f32 v114, v146\n"
      "v_cvt_pk_bf16_f32 v136, v112, v113\n"
      "v_exp_f32 v115, v147\n"
      "v_exp_f32 v112, v148\n"
      "v_cvt_pk_bf16_f32 v137, v114, v115\n"
      "v_exp_f32 v113, v149\n"
      "v_exp_f32 v114, v150\n"
      "v_cvt_pk_bf16_f32 v138, v112, v113\n"
      "v_exp_f32 v115, v151\n"
      "v_exp_f32 v112, v152\n"
      "v_cvt_pk_bf16_f32 v139, v114, v115\n"
      "v_exp_f32 v113, v153\n"
      "v_exp_f32 v114, v154\n"
      "v_cvt_pk_bf16_f32 v140, v112, v113\n"
      "v_exp_f32 v115, v155\n"
      "v_exp_f32 v112, v156\n"
      "v_cvt_pk_bf16_f32 v141, v114, v115\n"
      "v_exp_f32 v113, v157\n"
      "v_exp_f32 v114, v158\n"
      "v_cvt_pk_bf16_f32 v142, v112, v113\n"
      "v_exp_f32 v115, v159\n"
      "s_nop 0\n"
      "v_cvt_pk_bf16_f32 v143, v114, v115\n"
      "ds_read_b128 a[64:67], v242 offset:32768\n"
      "ds_read_b128 a[68:71], v242 offset:36864\n"
      "ds_read_b128 a[72:75], v243 offset:32768\n"
      "ds_read_b128 a[76:79], v243 offset:36864\n"
      "ds_read_b128 a[80:83], v242 offset:33792\n"
      "ds_read_b128 a[84:87], v242 offset:37888\n"
      "ds_read_b128 a[88:91], v243 offset:33792\n"
      "ds_read_b128 a[92:95], v243 offset:37888\n"
      "s_waitcnt lgkmcnt(0)\n"
      "v_mfma_f32_16x16x32_bf16 v[160:163], a[64:67], a[0:3], v[224:227]\n"
      "v_mfma_f32_16x16x32_bf16 v[168:171], a[64:67], a[4:7], v[228:231]\n"
      "v_mfma_f32_16x16x32_bf16 v[176:179], a[64:67], a[8:11], v[232:235]\n"
      "v_mfma_f32_16x16x32_bf16 v[184:187], a[64:67], a[12:15], v[236:239]\n"
      "v_mfma_f32_16x16x32_bf16 v[164:167], a[68:71], a[0:3], v[224:227]\n"
      "v_mfma_f32_16x16x32_bf16 v[172:175], a[68:71], a[4:7], v[228:231]\n"
      "v_mfma_f32_16x16x32_bf16 v[180:183], a[68:71], a[8:11], v[232:235]\n"
      "v_mfma_f32_16x16x32_bf16 v[188:191], a[68:71], a[12:15], v[236:239]\n"
      "v_mfma_f32_16x16x32_bf16 v[160:163], a[72:75], a[16:19], v[160:163]\n"
      "v_mfma_f32_16x16x32_bf16 v[168:171], a[72:75], a[20:23], v[168:171]\n"
      "v_mfma_f32_16x16x32_bf16 v[176:179], a[72:75], a[24:27], v[176:179]\n"
      "v_mfma_f32_16x16x32_bf16 v[184:187], a[72:75], a[28:31], v[184:187]\n"
      "v_mfma_f32_16x16x32_bf16 v[164:167], a[76:79], a[16:19], v[164:167]\n"
      "v_mfma_f32_16x16x32_bf16 v[172:175], a[76:79], a[20:23], v[172:175]\n"
      "v_mfma_f32_16x16x32_bf16 v[180:183], a[76:79], a[24:27], v[180:183]\n"
      "v_mfma_f32_16x16x32_bf16 v[188:191], a[76:79], a[28:31], v[188:191]\n"
      "v_mfma_f32_16x16x32_bf16 v[160:163], a[80:83], a[32:35], v[160:163]\n"
      "v_mfma_f32_16x16x32_bf16 v[168:171], a[80:83], a[36:39], v[168:171]\n"
      "v_mfma_f32_16x16x32_bf16 v[176:179], a[80:83], a[40:43], v[176:179]\n"
      "v_mfma_f32_16x16x32_bf16 v[184:187], a[80:83], a[44:47], v[184:187]\n"
      "v_mfma_f32_16x16x32_bf16 v[164:167], a[84:87], a[32:35], v[164:167]\n"
      "v_mfma_f32_16x16x32_bf16 v[172:175], a[84:87], a[36:39], v[172:175]\n"
      "v_mfma_f32_16x16x32_bf16 v[180:183], a[84:87], a[40:43], v[180:183]\n"
      "v_mfma_f32_16x16x32_bf16 v[188:191], a[84:87], a[44:47], v[188:191]\n"
      "v_mfma_f32_16x16x32_bf16 v[160:163], a[88:91], a[48:51], v[160:163]\n"
      "v_mfma_f32_16x16x32_bf16 v[168:171], a[88:91], a[52:55], v[168:171]\n"
      "v_mfma_f32_16x16x32_bf16 v[176:179], a[88:91], a[56:59], v[176:179]\n"
      "v_mfma_f32_16x16x32_bf16 v[184:187], a[88:91], a[60:63], v[184:187]\n"
      "v_mfma_f32_16x16x32_bf16 v[164:167], a[92:95], a[48:51], v[164:167]\n"
      "v_mfma_f32_16x16x32_bf16 v[172:175], a[92:95], a[52:55], v[172:175]\n"
      "v_mfma_f32_16x16x32_bf16 v[180:183], a[92:95], a[56:59], v[180:183]\n"
      "v_mfma_f32_16x16x32_bf16 v[188:191], a[92:95], a[60:63], v[188:191]\n"
      "s_cmp_eq_u32 s64, 0\n"
      "s_cbranch_scc0 NRET0_0_%=\n"
      "v_mov_b64 v[192:193], v[128:129]\n"
      "v_mov_b64 v[194:195], v[130:131]\n"
      "v_mov_b64 v[196:197], v[132:133]\n"
      "v_mov_b64 v[198:199], v[134:135]\n"
      "v_mov_b64 v[200:201], v[136:137]\n"
      "v_mov_b64 v[202:203], v[138:139]\n"
      "v_mov_b64 v[204:205], v[140:141]\n"
      "v_mov_b64 v[206:207], v[142:143]\n"
      "s_nop 1\n"
      "s_branch RET0_0_%=\n"
      "NRET0_0_%=:\n"
      "s_cmp_eq_u32 s64, 1\n"
      "s_cbranch_scc0 NRET0_1_%=\n"
      "v_mov_b64 v[192:193], v[128:129]\n"
      "v_mov_b64 v[194:195], v[130:131]\n"
      "v_mov_b64 v[196:197], v[132:133]\n"
      "v_mov_b64 v[198:199], v[134:135]\n"
      "v_mov_b64 v[200:201], v[136:137]\n"
      "v_mov_b64 v[202:203], v[138:139]\n"
      "v_mov_b64 v[204:205], v[140:141]\n"
      "v_mov_b64 v[206:207], v[142:143]\n"
      "s_nop 1\n"
      "s_branch RET0_1_%=\n"
      "NRET0_1_%=:\n"
      "s_cmp_eq_u32 s64, 2\n"
      "s_cbranch_scc0 NRET0_2_%=\n"
      "v_mov_b64 v[192:193], v[128:129]\n"
      "v_mov_b64 v[194:195], v[130:131]\n"
      "v_mov_b64 v[196:197], v[132:133]\n"
      "v_mov_b64 v[198:199], v[134:135]\n"
      "v_mov_b64 v[200:201], v[136:137]\n"
      "v_mov_b64 v[202:203], v[138:139]\n"
      "v_mov_b64 v[204:205], v[140:141]\n"
      "v_mov_b64 v[206:207], v[142:143]\n"
      "s_nop 1\n"
      "s_branch RET0_2_%=\n"
      "NRET0_2_%=:\n"
      "s_cmp_eq_u32 s64, 3\n"
      "s_cbranch_scc0 NRET0_3_%=\n"
      "v_mov_b64 v[192:193], v[128:129]\n"
      "v_mov_b64 v[194:195], v[130:131]\n"
      "v_mov_b64 v[196:197], v[132:133]\n"
      "v_mov_b64 v[198:199], v[134:135]\n"
      "v_mov_b64 v[200:201], v[136:137]\n"
      "v_mov_b64 v[202:203], v[138:139]\n"
      "v_mov_b64 v[204:205], v[140:141]\n"
      "v_mov_b64 v[206:207], v[142:143]\n"
      "s_nop 1\n"
      "s_branch RET0_3_%=\n"
      "NRET0_3_%=:\n"
      "s_cmp_eq_u32 s64, 4\n"
      "s_cbranch_scc0 NRET0_4_%=\n"
      "v_mov_b64 v[192:193], v[128:129]\n"
      "v_mov_b64 v[194:195], v[130:131]\n"
      "v_mov_b64 v[196:197], v[132:133]\n"
      "v_mov_b64 v[198:199], v[134:135]\n"
      "v_mov_b64 v[200:201], v[136:137]\n"
      "v_mov_b64 v[202:203], v[138:139]\n"
      "v_mov_b64 v[204:205], v[140:141]\n"
      "v_mov_b64 v[206:207], v[142:143]\n"
      "s_nop 1\n"
      "s_branch RET0_4_%=\n"
      "NRET0_4_%=:\n"
      "s_cmp_eq_u32 s64, 5\n"
      "s_cbranch_scc0 NRET0_5_%=\n"
      "v_mov_b64 v[16:17], v[128:129]\n"
      "v_mov_b64 v[18:19], v[130:131]\n"
      "v_mov_b64 v[20:21], v[132:133]\n"
      "v_mov_b64 v[22:23], v[134:135]\n"
      "v_mov_b64 v[24:25], v[136:137]\n"
      "v_mov_b64 v[26:27], v[138:139]\n"
      "v_mov_b64 v[28:29], v[140:141]\n"
      "v_mov_b64 v[30:31], v[142:143]\n"
      "s_nop 1\n"
      "s_branch RET0_5_%=\n"
      "NRET0_5_%=:\n"
      "FB1_%=:\n"
      "s_add_u32 s84, s63, 1\n"
      "s_sub_u32 s84, s84, %[n1]\n"
      "s_or_b32 s84, s84, s64\n"
      "s_cbranch_scc0 RET1_0_%=\n"
      "s_nop 7\n"
      "s_nop 7\n"
      "v_xor_b32 v118, 0x10000, v242\n"
      "v_xor_b32 v119, 0x10000, v243\n"
      "ds_read_b128 a[64:67], v118 offset:32768\n"
      "ds_read_b128 a[68:71], v118 offset:36864\n"
      "ds_read_b128 a[72:75], v119 offset:32768\n"
      "ds_read_b128 a[76:79], v119 offset:36864\n"
      "ds_read_b128 a[80:83], v118 offset:33792\n"
      "ds_read_b128 a[84:87], v118 offset:37888\n"
      "ds_read_b128 a[88:91], v119 offset:33792\n"
      "ds_read_b128 a[92:95], v119 offset:37888\n"
      "s_waitcnt lgkmcnt(0)\n"
      "v_mfma_f32_16x16x32_bf16 v[160:163], a[64:67], a[0:3], 0\n"
      "v_mfma_f32_16x16x32_bf16 v[168:171], a[64:67], a[4:7], 0\n"
      "v_mfma_f32_16x16x32_bf16 v[176:179], a[64:67], a[8:11], 0\n"
      "v_mfma_f32_16x16x32_bf16 v[184:187], a[64:67], a[12:15], 0\n"
      "v_mfma_f32_16x16x32_bf16 v[164:167], a[68:71], a[0:3], 0\n"
      "v_mfma_f32_16x16x32_bf16 v[172:175], a[68:71], a[4:7], 0\n"
      "v_mfma_f32_16x16x32_bf16 v[180:183], a[68:71], a[8:11], 0\n"
      "v_mfma_f32_16x16x32_bf16 v[188:191], a[68:71], a[12:15], 0\n"
      "v_mfma_f32_16x16x32_bf16 v[160:163], a[72:75], a[16:19], v[160:163]\n"
      "v_mfma_f32_16x16x32_bf16 v[168:171], a[72:75], a[20:23], v[168:171]\n"
      "v_mfma_f32_16x16x32_bf16 v[176:179], a[72:75], a[24:27], v[176:179]\n"
      "v_mfma_f32_16x16x32_bf16 v[184:187], a[72:75], a[28:31], v[184:187]\n"
      "v_mfma_f32_16x16x32_bf16 v[164:167], a[76:79], a[16:19], v[164:167]\n"
      "v_mfma_f32_16x16x32_bf16 v[172:175], a[76:79], a[20:23], v[172:175]\n"
      "v_mfma_f32_16x16x32_bf16 v[180:183], a[76:79], a[24:27], v[180:183]\n"
      "v_mfma_f32_16x16x32_bf16 v[188:191], a[76:79], a[28:31], v[188:191]\n"
      "v_mfma_f32_16x16x32_bf16 v[160:163], a[80:83], a[32:35], v[160:163]\n"
      "v_mfma_f32_16x16x32_bf16 v[168:171], a[80:83], a[36:39], v[168:171]\n"
      "v_mfma_f32_16x16x32_bf16 v[176:179], a[80:83], a[40:43], v[176:179]\n"
      "v_mfma_f32_16x16x32_bf16 v[184:187], a[80:83], a[44:47], v[184:187]\n"
      "v_mfma_f32_16x16x32_bf16 v[164:167], a[84:87], a[32:35], v[164:167]\n"
      "v_mfma_f32_16x16x32_bf16 v[172:175], a[84:87], a[36:39], v[172:175]\n"
      "v_mfma_f32_16x16x32_bf16 v[180:183], a[84:87], a[40:43], v[180:183]\n"
      "v_mfma_f32_16x16x32_bf16 v[188:191], a[84:87], a[44:47], v[188:191]\n"
      "v_mfma_f32_16x16x32_bf16 v[160:163], a[88:91], a[48:51], v[160:163]\n"
      "v_mfma_f32_16x16x32_bf16 v[168:171], a[88:91], a[52:55], v[168:171]\n"
      "v_mfma_f32_16x16x32_bf16 v[176:179], a[88:91], a[56:59], v[176:179]\n"
      "v_mfma_f32_16x16x32_bf16 v[184:187], a[88:91], a[60:63], v[184:187]\n"
      "v_mfma_f32_16x16x32_bf16 v[164:167], a[92:95], a[48:51], v[164:167]\n"
      "v_mfma_f32_16x16x32_bf16 v[172:175], a[92:95], a[52:55], v[172:175]\n"
      "v_mfma_f32_16x16x32_bf16 v[180:183], a[92:95], a[56:59], v[180:183]\n"
      "v_mfma_f32_16x16x32_bf16 v[188:191], a[92:95], a[60:63], v[188:191]\n"
      "s_nop 7\n"
      "v_max3_f32 v112, v160, v161, v162\n"
      "v_max3_f32 v113, v168, v169, v170\n"
      "v_max3_f32 v114, v176, v177, v178\n"
      "v_max3_f32 v115, v184, v185, v186\n"
      "v_max3_f32 v112, v112, v163, v164\n"
      "v_max3_f32 v113, v113, v171, v172\n"
      "v_max3_f32 v114, v114, v179, v180\n"
      "v_max3_f32 v115, v115, v187, v188\n"
      "v_max3_f32 v112, v112, v165, v166\n"
      "v_max3_f32 v113, v113, v173, v174\n"
      "v_max3_f32 v114, v114, v181, v182\n"
      "v_max3_f32 v115, v115, v189, v190\n"
      "v_max_f32 v112, v112, v167\n"
      "v_max_f32 v113, v113, v175\n"
      "v_max_f32 v114, v114, v183\n"
      "v_max_f32 v115, v115, v191\n"
      "v_mov_b32 v128, v112\n"
      "v_mov_b32 v129, v113\n"
      "v_mov_b32 v130, v114\n"
      "v_mov_b32 v131, v115\n"
      "v_permlane16_swap_b32 v112, v128\n"
      "v_permlane16_swap_b32 v113, v129\n"
      "v_permlane16_swap_b32 v114, v130\n"
      "v_permlane16_swap_b32 v115, v131\n"
      "v_max_f32 v112, v112, v128\n"
      "v_max_f32 v113, v113, v129\n"
      "v_max_f32 v114, v114, v130\n"
      "v_max_f32 v115, v115, v131\n"
      "v_mov_b32 v128, v112\n"
      "v_mov_b32 v129, v113\n"
      "v_mov_b32 v130, v114\n"
      "v_mov_b32 v131, v115\n"
      "v_permlane32_swap_b32 v112, v128\n"
      "v_permlane32_swap_b32 v113, v129\n"
      "v_permlane32_swap_b32 v114, v130\n"
      "v_permlane32_swap_b32 v115, v131\n"
      "v_max_f32 v112, v112, v128\n"
      "v_max_f32 v113, v113, v129\n"
      "v_max_f32 v114, v114, v130\n"
      "v_max_f32 v115, v115, v131\n"
      "v_sub_f32 v112, 0xc1800000, v112\n"
      "v_sub_f32 v113, 0xc1800000, v113\n"
      "v_sub_f32 v114, 0xc1800000, v114\n"
      "v_sub_f32 v115, 0xc1800000, v115\n"
      "v_floor_f32 v112, v112\n"
      "v_floor_f32 v113, v113\n"
      "v_floor_f32 v114, v114\n"
      "v_floor_f32 v115, v115\n"
      "v_min_f32 v112, v112, v224\n"
      "v_min_f32 v113, v113, v228\n"
      "v_min_f32 v114, v114, v232\n"
      "v_min_f32 v115, v115, v236\n"
      "v_sub_f32 v132, v112, v224\n"
      "v_sub_f32 v133, v113, v228\n"
      "v_sub_f32 v134, v114, v232\n"
      "v_sub_f32 v135, v115, v236\n"
      "v_mov_b32 v224, v112\n"
      "v_mov_b32 v228, v113\n"
      "v_mov_b32 v232, v114\n"
      "v_mov_b32 v236, v115\n"
      "v_exp_f32 v112, v132\n"
      "v_exp_f32 v113, v133\n"
      "v_exp_f32 v114, v134\n"
      "v_exp_f32 v115, v135\n"
      "v_mov_b32 v225, v224\n"
      "v_mov_b32 v229, v228\n"
      "v_mov_b32 v233, v232\n"
      "v_mov_b32 v237, v236\n"
      "v_mov_b64 v[226:227], v[224:225]\n"
      "v_mov_b64 v[230:231], v[228:229]\n"
      "v_mov_b64 v[234:235], v[232:233]\n"
      "v_mov_b64 v[238:239], v[236:237]\n"
      "s_call_b64 s[84:85], SCALE_%=\n"
      "v_pk_add_f32 v[160:161], v[160:161], v[224:225]\n"
      "v_pk_add_f32 v[162:163], v[162:163], v[224:225]\n"
      "v_pk_add_f32 v[164:165], v[164:165], v[224:225]\n"
      "v_pk_add_f32 v[166:167], v[166:167], v[224:225]\n"
      "v_pk_add_f32 v[168:169], v[168:169], v[228:229]\n"
      "v_pk_add_f32 v[170:171], v[170:171], v[228:229]\n"
      "v_pk_add_f32 v[172:173], v[172:173], v[228:229]\n"
      "v_pk_add_f32 v[174:175], v[174:175], v[228:229]\n"
      "v_pk_add_f32 v[176:177], v[176:177], v[232:233]\n"
      "v_pk_add_f32 v[178:179], v[178:179], v[232:233]\n"
      "v_pk_add_f32 v[180:181], v[180:181], v[232:233]\n"
      "v_pk_add_f32 v[182:183], v[182:183], v[232:233]\n"
      "v_pk_add_f32 v[184:185], v[184:185], v[236:237]\n"
      "v_pk_add_f32 v[186:187], v[186:187], v[236:237]\n"
      "v_pk_add_f32 v[188:189], v[188:189], v[236:237]\n"
      "v_pk_add_f32 v[190:191], v[190:191], v[236:237]\n"
      "v_exp_f32 v112, v160\n"
      "v_exp_f32 v113, v161\n"
      "v_exp_f32 v114, v162\n"
      "v_cvt_pk_bf16_f32 v160, v112, v113\n"
      "v_exp_f32 v115, v163\n"
      "v_exp_f32 v112, v164\n"
      "v_cvt_pk_bf16_f32 v161, v114, v115\n"
      "v_exp_f32 v113, v165\n"
      "v_exp_f32 v114, v166\n"
      "v_cvt_pk_bf16_f32 v162, v112, v113\n"
      "v_exp_f32 v115, v167\n"
      "v_exp_f32 v112, v168\n"
      "v_cvt_pk_bf16_f32 v163, v114, v115\n"
      "v_exp_f32 v113, v169\n"
      "v_exp_f32 v114, v170\n"
      "v_cvt_pk_bf16_f32 v164, v112, v113\n"
      "v_exp_f32 v115, v171\n"
      "v_exp_f32 v112, v172\n"
      "v_cvt_pk_bf16_f32 v165, v114, v115\n"
      "v_exp_f32 v113, v173\n"
      "v_exp_f32 v114, v174\n"
      "v_cvt_pk_bf16_f32 v166, v112, v113\n"
      "v_exp_f32 v115, v175\n"
      "v_exp_f32 v112, v176\n"
      "v_cvt_pk_bf16_f32 v167, v114, v115\n"
      "v_exp_f32 v113, v177\n"
      "v_exp_f32 v114, v178\n"
      "v_cvt_pk_bf16_f32 v168, v112, v113\n"
      "v_exp_f32 v115, v179\n"
      "v_exp_f32 v112, v180\n"
      "v_cvt_pk_bf16_f32 v169, v114, v115\n"
      "v_exp_f32 v113, v181\n"
      "v_exp_f32 v114, v182\n"
      "v_cvt_pk_bf16_f32 v170, v112, v113\n"
      "v_exp_f32 v115, v183\n"
      "v_exp_f32 v112, v184\n"
      "v_cvt_pk_bf16_f32 v171, v114, v115\n"
      "v_exp_f32 v113, v185\n"
      "v_exp_f32 v114, v186\n"
      "v_cvt_pk_bf16_f32 v172, v112, v113\n"
      "v_exp_f32 v115, v187\n"
      "v_exp_f32 v112, v188\n"
      "v_cvt_pk_bf16_f32 v173, v114, v115\n"
      "v_exp_f32 v113, v189\n"
      "v_exp_f32 v114, v190\n"
      "v_cvt_pk_bf16_f32 v174, v112, v113\n"
      "v_exp_f32 v115, v191\n"
      "s_nop 0\n"
      "v_cvt_pk_bf16_f32 v175, v114, v115\n"
      "ds_read_b128 a[64:67], v242 offset:0\n"
      "ds_read_b128 a[68:71], v242 offset:4096\n"
      "ds_read_b128 a[72:75], v243 offset:0\n"
      "ds_read_b128 a[76:79], v243 offset:4096\n"
      "ds_read_b128 a[80:83], v242 offset:1024\n"
      "ds_read_b128 a[84:87], v242 offset:5120\n"
      "ds_read_b128 a[88:91], v243 offset:1024\n"
      "ds_read_b128 a[92:95], v243 offset:5120\n"
      "s_waitcnt lgkmcnt(0)\n"
      "v_mfma_f32_16x16x32_bf16 v[128:131], a[64:67], a[0:3], v[224:227]\n"
      "v_mfma_f32_16x16x32_bf16 v[136:139], a[64:67], a[4:7], v[228:231]\n"
      "v_mfma_f32_16x16x32_bf16 v[144:147], a[64:67], a[8:11], v[232:235]\n"
      "v_mfma_f32_16x16x32_bf16 v[152:155], a[64:67], a[12:15], v[236:239]\n"
      "v_mfma_f32_16x16x32_bf16 v[132:135], a[68:71], a[0:3], v[224:227]\n"
      "v_mfma_f32_16x16x32_bf16 v[140:143], a[68:71], a[4:7], v[228:231]\n"
      "v_mfma_f32_16x16x32_bf16 v[148:151], a[68:71], a[8:11], v[232:235]\n"
      "v_mfma_f32_16x16x32_bf16 v[156:159], a[68:71], a[12:15], v[236:239]\n"
      "v_mfma_f32_16x16x32_bf16 v[128:131], a[72:75], a[16:19], v[128:131]\n"
      "v_mfma_f32_16x16x32_bf16 v[136:139], a[72:75], a[20:23], v[136:139]\n"
      "v_mfma_f32_16x16x32_bf16 v[144:147], a[72:75], a[24:27], v[144:147]\n"
      "v_mfma_f32_16x16x32_bf16 v[152:155], a[72:75], a[28:31], v[152:155]\n"
      "v_mfma_f32_16x16x32_bf16 v[132:135], a[76:79], a[16:19], v[132:135]\n"
      "v_mfma_f32_16x16x32_bf16 v[140:143], a[76:79], a[20:23], v[140:143]\n"
      "v_mfma_f32_16x16x32_bf16 v[148:151], a[76:79], a[24:27], v[148:151]\n"
      "v_mfma_f32_16x16x32_bf16 v[156:159], a[76:79], a[28:31], v[156:159]\n"
      "v_mfma_f32_16x16x32_bf16 v[128:131], a[80:83], a[32:35], v[128:131]\n"
      "v_mfma_f32_16x16x32_bf16 v[136:139], a[80:83], a[36:39], v[136:139]\n"
      "v_mfma_f32_16x16x32_bf16 v[144:147], a[80:83], a[40:43], v[144:147]\n"
      "v_mfma_f32_16x16x32_bf16 v[152:155], a[80:83], a[44:47], v[152:155]\n"
      "v_mfma_f32_16x16x32_bf16 v[132:135], a[84:87], a[32:35], v[132:135]\n"
      "v_mfma_f32_16x16x32_bf16 v[140:143], a[84:87], a[36:39], v[140:143]\n"
      "v_mfma_f32_16x16x32_bf16 v[148:151], a[84:87], a[40:43], v[148:151]\n"
      "v_mfma_f32_16x16x32_bf16 v[156:159], a[84:87], a[44:47], v[156:159]\n"
      "v_mfma_f32_16x16x32_bf16 v[128:131], a[88:91], a[48:51], v[128:131]\n"
      "v_mfma_f32_16x16x32_bf16 v[136:139], a[88:91], a[52:55], v[136:139]\n"
      "v_mfma_f32_16x16x32_bf16 v[144:147], a[88:91], a[56:59], v[144:147]\n"
      "v_mfma_f32_16x16x32_bf16 v[152:155], a[88:91], a[60:63], v[152:155]\n"
      "v_mfma_f32_16x16x32_bf16 v[132:135], a[92:95], a[48:51], v[132:135]\n"
      "v_mfma_f32_16x16x32_bf16 v[140:143], a[92:95], a[52:55], v[140:143]\n"
      "v_mfma_f32_16x16x32_bf16 v[148:151], a[92:95], a[56:59], v[148:151]\n"
      "v_mfma_f32_16x16x32_bf16 v[156:159], a[92:95], a[60:63], v[156:159]\n"
      "s_cmp_eq_u32 s64, 0\n"
      "s_cbranch_scc0 NRET1_0_%=\n"
      "v_mov_b64 v[208:209], v[160:161]\n"
      "v_mov_b64 v[210:211], v[162:163]\n"
      "v_mov_b64 v[212:213], v[164:165]\n"
      "v_mov_b64 v[214:215], v[166:167]\n"
      "v_mov_b64 v[216:217], v[168:169]\n"
      "v_mov_b64 v[218:219], v[170:171]\n"
      "v_mov_b64 v[220:221], v[172:173]\n"
      "v_mov_b64 v[222:223], v[174:175]\n"
      "s_nop 1\n"
      "s_branch RET1_0_%=\n"
      "NRET1_0_%=:\n"
      "s_cmp_eq_u32 s64, 1\n"
      "s_cbranch_scc0 NRET1_1_%=\n"
      "v_mov_b64 v[208:209], v[160:161]\n"
      "v_mov_b64 v[210:211], v[162:163]\n"
      "v_mov_b64 v[212:213], v[164:165]\n"
      "v_mov_b64 v[214:215], v[166:167]\n"
      "v_mov_b64 v[216:217], v[168:169]\n"
      "v_mov_b64 v[218:219], v[170:171]\n"
      "v_mov_b64 v[220:221], v[172:173]\n"
      "v_mov_b64 v[222:223], v[174:175]\n"
      "s_nop 1\n"
      "s_branch RET1_1_%=\n"
      "NRET1_1_%=:\n"
      "s_cmp_eq_u32 s64, 2\n"
      "s_cbranch_scc0 NRET1_2_%=\n"
      "v_mov_b64 v[208:209], v[160:161]\n"
      "v_mov_b64 v[210:211], v[162:163]\n"
      "v_mov_b64 v[212:213], v[164:165]\n"
      "v_mov_b64 v[214:215], v[166:167]\n"
      "v_mov_b64 v[216:217], v[168:169]\n"
      "v_mov_b64 v[218:219], v[170:171]\n"
      "v_mov_b64 v[220:221], v[172:173]\n"
      "v_mov_b64 v[222:223], v[174:175]\n"
      "s_nop 1\n"
      "s_branch RET1_2_%=\n"
      "NRET1_2_%=:\n"
      "s_cmp_eq_u32 s64, 3\n"
      "s_cbranch_scc0 NRET1_3_%=\n"
      "v_mov_b64 v[208:209], v[160:161]\n"
      "v_mov_b64 v[210:211], v[162:163]\n"
      "v_mov_b64 v[212:213], v[164:165]\n"
      "v_mov_b64 v[214:215], v[166:167]\n"
      "v_mov_b64 v[216:217], v[168:169]\n"
      "v_mov_b64 v[218:219], v[170:171]\n"
      "v_mov_b64 v[220:221], v[172:173]\n"
      "v_mov_b64 v[222:223], v[174:175]\n"
      "s_nop 1\n"
      "s_branch RET1_3_%=\n"
      "NRET1_3_%=:\n"
      "s_cmp_eq_u32 s64, 4\n"
      "s_cbranch_scc0 NRET1_4_%=\n"
      "v_mov_b64 v[208:209], v[160:161]\n"
      "v_mov_b64 v[210:211], v[162:163]\n"
      "v_mov_b64 v[212:213], v[164:165]\n"
      "v_mov_b64 v[214:215], v[166:167]\n"
      "v_mov_b64 v[216:217], v[168:169]\n"
      "v_mov_b64 v[218:219], v[170:171]\n"
      "v_mov_b64 v[220:221], v[172:173]\n"
      "v_mov_b64 v[222:223], v[174:175]\n"
      "s_nop 1\n"
      "s_branch RET1_4_%=\n"
      "NRET1_4_%=:\n"
      "s_cmp_eq_u32 s64, 5\n"
      "s_cbranch_scc0 NRET1_5_%=\n"
      "v_mov_b64 v[0:1], v[160:161]\n"
      "v_mov_b64 v[2:3], v[162:163]\n"
      "v_mov_b64 v[4:5], v[164:165]\n"
      "v_mov_b64 v[6:7], v[166:167]\n"
      "v_mov_b64 v[8:9], v[168:169]\n"
      "v_mov_b64 v[10:11], v[170:171]\n"
      "v_mov_b64 v[12:13], v[172:173]\n"
      "v_mov_b64 v[14:15], v[174:175]\n"
      "s_nop 1\n"
      "s_branch RET1_5_%=\n"
      "NRET1_5_%=:\n"
      "s_cmp_eq_u32 s64, 6\n"
      "s_cbranch_scc0 NRET1_6_%=\n"
      "v_mov_b64 v[96:97], v[160:161]\n"
      "v_mov_b64 v[98:99], v[162:163]\n"
      "v_mov_b64 v[100:101], v[164:165]\n"
      "v_mov_b64 v[102:103], v[166:167]\n"
      "v_mov_b64 v[104:105], v[168:169]\n"
      "v_mov_b64 v[106:107], v[170:171]\n"
      "v_mov_b64 v[108:109], v[172:173]\n"
      "v_mov_b64 v[110:111], v[174:175]\n"
      "v_mov_b32 v117, v224\n"
      "s_nop 1\n"
      "v_mov_b32_dpp v117, v228 quad_perm:[0,1,2,3] row_mask:0x2 bank_mask:0xf\n"
      "s_nop 1\n"
      "v_mov_b32_dpp v117, v232 quad_perm:[0,1,2,3] row_mask:0x4 bank_mask:0xf\n"
      "s_nop 1\n"
      "v_mov_b32_dpp v117, v236 quad_perm:[0,1,2,3] row_mask:0x8 bank_mask:0xf\n"
      "v_mfma_f32_16x16x32_bf16 a[228:231], a[196:199], v[96:99], a[192:195]\n"
      "v_mfma_f32_16x16x32_bf16 a[228:231], a[200:203], v[100:103], a[228:231]\n"
      "v_mfma_f32_16x16x32_bf16 a[228:231], a[204:207], v[104:107], a[228:231]\n"
      "ds_write_b128 v125, a[160:163] offset:0\n"
      "ds_write_b128 v125, a[164:167] offset:1024\n"
      "ds_write_b128 v125, a[168:171] offset:2048\n"
      "ds_write_b128 v125, a[172:175] offset:3072\n"
      "ds_write_b128 v125, a[176:179] offset:4096\n"
      "ds_write_b128 v125, a[180:183] offset:5120\n"
      "ds_write_b128 v125, a[184:187] offset:6144\n"
      "ds_write_b128 v125, a[188:191] offset:7168\n"
      "ds_write_b128 v125, v[96:99] offset:8192\n"
      "ds_write_b128 v125, v[100:103] offset:9216\n"
      "ds_write_b128 v125, v[104:107] offset:10240\n"
      "ds_write2_b32 v124, v117, v123 offset0:1 offset1:2\n"
      "s_nop 1\n"
      "s_branch RET1_6_%=\n"
      "NRET1_6_%=:\n"
      "SCALE_%=:\n"
      "v_pk_mul_f32 v[32:33], v[32:33], v[112:113] op_sel:[0,0] op_sel_hi:[1,0]\n"
      "v_pk_mul_f32 v[34:35], v[34:35], v[112:113] op_sel:[0,0] op_sel_hi:[1,0]\n"
      "v_pk_mul_f32 v[36:37], v[36:37], v[112:113] op_sel:[0,1] op_sel_hi:[1,1]\n"
      "v_pk_mul_f32 v[38:39], v[38:39], v[112:113] op_sel:[0,1] op_sel_hi:[1,1]\n"
      "v_pk_mul_f32 v[40:41], v[40:41], v[114:115] op_sel:[0,0] op_sel_hi:[1,0]\n"
      "v_pk_mul_f32 v[42:43], v[42:43], v[114:115] op_sel:[0,0] op_sel_hi:[1,0]\n"
      "v_pk_mul_f32 v[44:45], v[44:45], v[114:115] op_sel:[0,1] op_sel_hi:[1,1]\n"
      "v_pk_mul_f32 v[46:47], v[46:47], v[114:115] op_sel:[0,1] op_sel_hi:[1,1]\n"
      "v_pk_mul_f32 v[48:49], v[48:49], v[112:113] op_sel:[0,0] op_sel_hi:[1,0]\n"
      "v_pk_mul_f32 v[50:51], v[50:51], v[112:113] op_sel:[0,0] op_sel_hi:[1,0]\n"
      "v_pk_mul_f32 v[52:53], v[52:53], v[112:113] op_sel:[0,1] op_sel_hi:[1,1]\n"
      "v_pk_mul_f32 v[54:55], v[54:55], v[112:113] op_sel:[0,1] op_sel_hi:[1,1]\n"
      "v_pk_mul_f32 v[56:57], v[56:57], v[114:115] op_sel:[0,0] op_sel_hi:[1,0]\n"
      "v_pk_mul_f32 v[58:59], v[58:59], v[114:115] op_sel:[0,0] op_sel_hi:[1,0]\n"
      "v_pk_mul_f32 v[60:61], v[60:61], v[114:115] op_sel:[0,1] op_sel_hi:[1,1]\n"
      "v_pk_mul_f32 v[62:63], v[62:63], v[114:115] op_sel:[0,1] op_sel_hi:[1,1]\n"
      "v_pk_mul_f32 v[64:65], v[64:65], v[112:113] op_sel:[0,0] op_sel_hi:[1,0]\n"
      "v_pk_mul_f32 v[66:67], v[66:67], v[112:113] op_sel:[0,0] op_sel_hi:[1,0]\n"
      "v_pk_mul_f32 v[68:69], v[68:69], v[112:113] op_sel:[0,1] op_sel_hi:[1,1]\n"
      "v_pk_mul_f32 v[70:71], v[70:71], v[112:113] op_sel:[0,1] op_sel_hi:[1,1]\n"
      "v_pk_mul_f32 v[72:73], v[72:73], v[114:115] op_sel:[0,0] op_sel_hi:[1,0]\n"
      "v_pk_mul_f32 v[74:75], v[74:75], v[114:115] op_sel:[0,0] op_sel_hi:[1,0]\n"
      "v_pk_mul_f32 v[76:77], v[76:77], v[114:115] op_sel:[0,1] op_sel_hi:[1,1]\n"
      "v_pk_mul_f32 v[78:79], v[78:79], v[114:115] op_sel:[0,1] op_sel_hi:[1,1]\n"
      "v_pk_mul_f32 v[80:81], v[80:81], v[112:113] op_sel:[0,0] op_sel_hi:[1,0]\n"
      "v_pk_mul_f32 v[82:83], v[82:83], v[112:113] op_sel:[0,0] op_sel_hi:[1,0]\n"
      "v_pk_mul_f32 v[84:85], v[84:85], v[112:113] op_sel:[0,1] op_sel_hi:[1,1]\n"
      "v_pk_mul_f32 v[86:87], v[86:87], v[112:113] op_sel:[0,1] op_sel_hi:[1,1]\n"
      "v_pk_mul_f32 v[88:89], v[88:89], v[114:115] op_sel:[0,0] op_sel_hi:[1,0]\n"
      "v_pk_mul_f32 v[90:91], v[90:91], v[114:115] op_sel:[0,0] op_sel_hi:[1,0]\n"
      "v_pk_mul_f32 v[92:93], v[92:93], v[114:115] op_sel:[0,1] op_sel_hi:[1,1]\n"
      "v_pk_mul_f32 v[94:95], v[94:95], v[114:115] op_sel:[0,1] op_sel_hi:[1,1]\n"
      "v_accvgpr_read_b32 v118, a128\n"
      "v_accvgpr_read_b32 v119, a129\n"
      "v_pk_mul_f32 v[118:119], v[118:119], v[112:113] op_sel:[0,0] op_sel_hi:[1,0]\n"
      "v_accvgpr_write_b32 a128, v118\n"
      "v_accvgpr_write_b32 a129, v119\n"
      "v_accvgpr_read_b32 v118, a130\n"
      "v_accvgpr_read_b32 v119, a131\n"
      "v_pk_mul_f32 v[118:119], v[118:119], v[112:113] op_sel:[0,0] op_sel_hi:[1,0]\n"
      "v_accvgpr_write_b32 a130, v118\n"
      "v_accvgpr_write_b32 a131, v119\n"
      "v_accvgpr_read_b32 v118, a132\n"
      "v_accvgpr_read_b32 v119, a133\n"
      "v_pk_mul_f32 v[118:119], v[118:119], v[112:113] op_sel:[0,1] op_sel_hi:[1,1]\n"
      "v_accvgpr_write_b32 a132, v118\n"
      "v_accvgpr_write_b32 a133, v119\n"
      "v_accvgpr_read_b32 v118, a134\n"
      "v_accvgpr_read_b32 v119, a135\n"
      "v_pk_mul_f32 v[118:119], v[118:119], v[112:113] op_sel:[0,1] op_sel_hi:[1,1]\n"
      "v_accvgpr_write_b32 a134, v118\n"
      "v_accvgpr_write_b32 a135, v119\n"
      "v_accvgpr_read_b32 v118, a136\n"
      "v_accvgpr_read_b32 v119, a137\n"
      "v_pk_mul_f32 v[118:119], v[118:119], v[114:115] op_sel:[0,0] op_sel_hi:[1,0]\n"
      "v_accvgpr_write_b32 a136, v118\n"
      "v_accvgpr_write_b32 a137, v119\n"
      "v_accvgpr_read_b32 v118, a138\n"
      "v_accvgpr_read_b32 v119, a139\n"
      "v_pk_mul_f32 v[118:119], v[118:119], v[114:115] op_sel:[0,0] op_sel_hi:[1,0]\n"
      "v_accvgpr_write_b32 a138, v118\n"
      "v_accvgpr_write_b32 a139, v119\n"
      "v_accvgpr_read_b32 v118, a140\n"
      "v_accvgpr_read_b32 v119, a141\n"
      "v_pk_mul_f32 v[118:119], v[118:119], v[114:115] op_sel:[0,1] op_sel_hi:[1,1]\n"
      "v_accvgpr_write_b32 a140, v118\n"
      "v_accvgpr_write_b32 a141, v119\n"
      "v_accvgpr_read_b32 v118, a142\n"
      "v_accvgpr_read_b32 v119, a143\n"
      "v_pk_mul_f32 v[118:119], v[118:119], v[114:115] op_sel:[0,1] op_sel_hi:[1,1]\n"
      "v_accvgpr_write_b32 a142, v118\n"
      "v_accvgpr_write_b32 a143, v119\n"
      "v_accvgpr_read_b32 v118, a144\n"
      "v_accvgpr_read_b32 v119, a145\n"
      "v_pk_mul_f32 v[118:119], v[118:119], v[112:113] op_sel:[0,0] op_sel_hi:[1,0]\n"
      "v_accvgpr_write_b32 a144, v118\n"
      "v_accvgpr_write_b32 a145, v119\n"
      "v_accvgpr_read_b32 v118, a146\n"
      "v_accvgpr_read_b32 v119, a147\n"
      "v_pk_mul_f32 v[118:119], v[118:119], v[112:113] op_sel:[0,0] op_sel_hi:[1,0]\n"
      "v_accvgpr_write_b32 a146, v118\n"
      "v_accvgpr_write_b32 a147, v119\n"
      "v_accvgpr_read_b32 v118, a148\n"
      "v_accvgpr_read_b32 v119, a149\n"
      "v_pk_mul_f32 v[118:119], v[118:119], v[112:113] op_sel:[0,1] op_sel_hi:[1,1]\n"
      "v_accvgpr_write_b32 a148, v118\n"
      "v_accvgpr_write_b32 a149, v119\n"
      "v_accvgpr_read_b32 v118, a150\n"
      "v_accvgpr_read_b32 v119, a151\n"
      "v_pk_mul_f32 v[118:119], v[118:119], v[112:113] op_sel:[0,1] op_sel_hi:[1,1]\n"
      "v_accvgpr_write_b32 a150, v118\n"
      "v_accvgpr_write_b32 a151, v119\n"
      "v_accvgpr_read_b32 v118, a152\n"
      "v_accvgpr_read_b32 v119, a153\n"
      "v_pk_mul_f32 v[118:119], v[118:119], v[114:115] op_sel:[0,0] op_sel_hi:[1,0]\n"
      "v_accvgpr_write_b32 a152, v118\n"
      "v_accvgpr_write_b32 a153, v119\n"
      "v_accvgpr_read_b32 v118, a154\n"
      "v_accvgpr_read_b32 v119, a155\n"
      "v_pk_mul_f32 v[118:119], v[118:119], v[114:115] op_sel:[0,0] op_sel_hi:[1,0]\n"
      "v_accvgpr_write_b32 a154, v118\n"
      "v_accvgpr_write_b32 a155, v119\n"
      "v_accvgpr_read_b32 v118, a156\n"
      "v_accvgpr_read_b32 v119, a157\n"
      "v_pk_mul_f32 v[118:119], v[118:119], v[114:115] op_sel:[0,1] op_sel_hi:[1,1]\n"
      "v_accvgpr_write_b32 a156, v118\n"
      "v_accvgpr_write_b32 a157, v119\n"
      "v_accvgpr_read_b32 v118, a158\n"
      "v_accvgpr_read_b32 v119, a159\n"
      "v_pk_mul_f32 v[118:119], v[118:119], v[114:115] op_sel:[0,1] op_sel_hi:[1,1]\n"
      "v_accvgpr_write_b32 a158, v118\n"
      "v_accvgpr_write_b32 a159, v119\n"
      "v_accvgpr_read_b32 v118, a160\n"
      "v_accvgpr_read_b32 v119, a161\n"
      "v_pk_mul_f32 v[118:119], v[118:119], v[112:113] op_sel:[0,0] op_sel_hi:[1,0]\n"
      "v_accvgpr_write_b32 a160, v118\n"
      "v_accvgpr_write_b32 a161, v119\n"
      "v_accvgpr_read_b32 v118, a162\n"
      "v_accvgpr_read_b32 v119, a163\n"
      "v_pk_mul_f32 v[118:119], v[118:119], v[112:113] op_sel:[0,0] op_sel_hi:[1,0]\n"
      "v_accvgpr_write_b32 a162, v118\n"
      "v_accvgpr_write_b32 a163, v119\n"
      "v_accvgpr_read_b32 v118, a164\n"
      "v_accvgpr_read_b32 v119, a165\n"
      "v_pk_mul_f32 v[118:119], v[118:119], v[112:113] op_sel:[0,1] op_sel_hi:[1,1]\n"
      "v_accvgpr_write_b32 a164, v118\n"
      "v_accvgpr_write_b32 a165, v119\n"
      "v_accvgpr_read_b32 v118, a166\n"
      "v_accvgpr_read_b32 v119, a167\n"
      "v_pk_mul_f32 v[118:119], v[118:119], v[112:113] op_sel:[0,1] op_sel_hi:[1,1]\n"
      "v_accvgpr_write_b32 a166, v118\n"
      "v_accvgpr_write_b32 a167, v119\n"
      "v_accvgpr_read_b32 v118, a168\n"
      "v_accvgpr_read_b32 v119, a169\n"
      "v_pk_mul_f32 v[118:119], v[118:119], v[114:115] op_sel:[0,0] op_sel_hi:[1,0]\n"
      "v_accvgpr_write_b32 a168, v118\n"
      "v_accvgpr_write_b32 a169, v119\n"
      "v_accvgpr_read_b32 v118, a170\n"
      "v_accvgpr_read_b32 v119, a171\n"
      "v_pk_mul_f32 v[118:119], v[118:119], v[114:115] op_sel:[0,0] op_sel_hi:[1,0]\n"
      "v_accvgpr_write_b32 a170, v118\n"
      "v_accvgpr_write_b32 a171, v119\n"
      "v_accvgpr_read_b32 v118, a172\n"
      "v_accvgpr_read_b32 v119, a173\n"
      "v_pk_mul_f32 v[118:119], v[118:119], v[114:115] op_sel:[0,1] op_sel_hi:[1,1]\n"
      "v_accvgpr_write_b32 a172, v118\n"
      "v_accvgpr_write_b32 a173, v119\n"
      "v_accvgpr_read_b32 v118, a174\n"
      "v_accvgpr_read_b32 v119, a175\n"
      "v_pk_mul_f32 v[118:119], v[118:119], v[114:115] op_sel:[0,1] op_sel_hi:[1,1]\n"
      "v_accvgpr_write_b32 a174, v118\n"
      "v_accvgpr_write_b32 a175, v119\n"
      "v_accvgpr_read_b32 v118, a176\n"
      "v_accvgpr_read_b32 v119, a177\n"
      "v_pk_mul_f32 v[118:119], v[118:119], v[112:113] op_sel:[0,0] op_sel_hi:[1,0]\n"
      "v_accvgpr_write_b32 a176, v118\n"
      "v_accvgpr_write_b32 a177, v119\n"
      "v_accvgpr_read_b32 v118, a178\n"
      "v_accvgpr_read_b32 v119, a179\n"
      "v_pk_mul_f32 v[118:119], v[118:119], v[112:113] op_sel:[0,0] op_sel_hi:[1,0]\n"
      "v_accvgpr_write_b32 a178, v118\n"
      "v_accvgpr_write_b32 a179, v119\n"
      "v_accvgpr_read_b32 v118, a180\n"
      "v_accvgpr_read_b32 v119, a181\n"
      "v_pk_mul_f32 v[118:119], v[118:119], v[112:113] op_sel:[0,1] op_sel_hi:[1,1]\n"
      "v_accvgpr_write_b32 a180, v118\n"
      "v_accvgpr_write_b32 a181, v119\n"
      "v_accvgpr_read_b32 v118, a182\n"
      "v_accvgpr_read_b32 v119, a183\n"
      "v_pk_mul_f32 v[118:119], v[118:119], v[112:113] op_sel:[0,1] op_sel_hi:[1,1]\n"
      "v_accvgpr_write_b32 a182, v118\n"
      "v_accvgpr_write_b32 a183, v119\n"
      "v_accvgpr_read_b32 v118, a184\n"
      "v_accvgpr_read_b32 v119, a185\n"
      "v_pk_mul_f32 v[118:119], v[118:119], v[114:115] op_sel:[0,0] op_sel_hi:[1,0]\n"
      "v_accvgpr_write_b32 a184, v118\n"
      "v_accvgpr_write_b32 a185, v119\n"
      "v_accvgpr_read_b32 v118, a186\n"
      "v_accvgpr_read_b32 v119, a187\n"
      "v_pk_mul_f32 v[118:119], v[118:119], v[114:115] op_sel:[0,0] op_sel_hi:[1,0]\n"
      "v_accvgpr_write_b32 a186, v118\n"
      "v_accvgpr_write_b32 a187, v119\n"
      "v_accvgpr_read_b32 v118, a188\n"
      "v_accvgpr_read_b32 v119, a189\n"
      "v_pk_mul_f32 v[118:119], v[118:119], v[114:115] op_sel:[0,1] op_sel_hi:[1,1]\n"
      "v_accvgpr_write_b32 a188, v118\n"
      "v_accvgpr_write_b32 a189, v119\n"
      "v_accvgpr_read_b32 v118, a190\n"
      "v_accvgpr_read_b32 v119, a191\n"
      "v_pk_mul_f32 v[118:119], v[118:119], v[114:115] op_sel:[0,1] op_sel_hi:[1,1]\n"
      "v_accvgpr_write_b32 a190, v118\n"
      "v_accvgpr_write_b32 a191, v119\n"
      "v_mov_b32 v118, v112\n"
      "s_nop 1\n"
      "v_mov_b32_dpp v118, v113 quad_perm:[0,1,2,3] row_mask:0x2 bank_mask:0xf\n"
      "s_nop 1\n"
      "v_mov_b32_dpp v118, v114 quad_perm:[0,1,2,3] row_mask:0x4 bank_mask:0xf\n"
      "s_nop 1\n"
      "v_mov_b32_dpp v118, v115 quad_perm:[0,1,2,3] row_mask:0x8 bank_mask:0xf\n"
      "v_accvgpr_read_b32 v119, a192\n"
      "v_mul_f32 v118, v118, v119\n"
      "v_accvgpr_write_b32 a192, v118\n"
      "v_lshlrev_b32 v118, 16, v208\n"
      "v_and_b32 v119, 0xffff0000, v208\n"
      "v_pk_mul_f32 v[118:119], v[118:119], v[112:113] op_sel:[0,0] op_sel_hi:[1,0]\n"
      "v_cvt_pk_bf16_f32 v208, v118, v119\n"
      "v_lshlrev_b32 v118, 16, v209\n"
      "v_and_b32 v119, 0xffff0000, v209\n"
      "v_pk_mul_f32 v[118:119], v[118:119], v[112:113] op_sel:[0,0] op_sel_hi:[1,0]\n"
      "v_cvt_pk_bf16_f32 v209, v118, v119\n"
      "v_lshlrev_b32 v118, 16, v210\n"
      "v_and_b32 v119, 0xffff0000, v210\n"
      "v_pk_mul_f32 v[118:119], v[118:119], v[112:113] op_sel:[0,0] op_sel_hi:[1,0]\n"
      "v_cvt_pk_bf16_f32 v210, v118, v119\n"
      "v_lshlrev_b32 v118, 16, v211\n"
      "v_and_b32 v119, 0xffff0000, v211\n"
      "v_pk_mul_f32 v[118:119], v[118:119], v[112:113] op_sel:[0,0] op_sel_hi:[1,0]\n"
      "v_cvt_pk_bf16_f32 v211, v118, v119\n"
      "v_lshlrev_b32 v118, 16, v212\n"
      "v_and_b32 v119, 0xffff0000, v212\n"
      "v_pk_mul_f32 v[118:119], v[118:119], v[112:113] op_sel:[0,1] op_sel_hi:[1,1]\n"
      "v_cvt_pk_bf16_f32 v212, v118, v119\n"
      "v_lshlrev_b32 v118, 16, v213\n"
      "v_and_b32 v119, 0xffff0000, v213\n"
      "v_pk_mul_f32 v[118:119], v[118:119], v[112:113] op_sel:[0,1] op_sel_hi:[1,1]\n"
      "v_cvt_pk_bf16_f32 v213, v118, v119\n"
      "v_lshlrev_b32 v118, 16, v214\n"
      "v_and_b32 v119, 0xffff0000, v214\n"
      "v_pk_mul_f32 v[118:119], v[118:119], v[112:113] op_sel:[0,1] op_sel_hi:[1,1]\n"
      "v_cvt_pk_bf16_f32 v214, v118, v119\n"
      "v_lshlrev_b32 v118, 16, v215\n"
      "v_and_b32 v119, 0xffff0000, v215\n"
      "v_pk_mul_f32 v[118:119], v[118:119], v[112:113] op_sel:[0,1] op_sel_hi:[1,1]\n"
      "v_cvt_pk_bf16_f32 v215, v118, v119\n"
      "v_lshlrev_b32 v118, 16, v216\n"
      "v_and_b32 v119, 0xffff0000, v216\n"
      "v_pk_mul_f32 v[118:119], v[118:119], v[114:115] op_sel:[0,0] op_sel_hi:[1,0]\n"
      "v_cvt_pk_bf16_f32 v216, v118, v119\n"
      "v_lshlrev_b32 v118, 16, v217\n"
      "v_and_b32 v119, 0xffff0000, v217\n"
      "v_pk_mul_f32 v[118:119], v[118:119], v[114:115] op_sel:[0,0] op_sel_hi:[1,0]\n"
      "v_cvt_pk_bf16_f32 v217, v118, v119\n"
      "v_lshlrev_b32 v118, 16, v218\n"
      "v_and_b32 v119, 0xffff0000, v218\n"
      "v_pk_mul_f32 v[118:119], v[118:119], v[114:115] op_sel:[0,0] op_sel_hi:[1,0]\n"
      "v_cvt_pk_bf16_f32 v218, v118, v119\n"
      "v_lshlrev_b32 v118, 16, v219\n"
      "v_and_b32 v119, 0xffff0000, v219\n"
      "v_pk_mul_f32 v[118:119], v[118:119], v[114:115] op_sel:[0,0] op_sel_hi:[1,0]\n"
      "v_cvt_pk_bf16_f32 v219, v118, v119\n"
      "v_lshlrev_b32 v118, 16, v220\n"
      "v_and_b32 v119, 0xffff0000, v220\n"
      "v_pk_mul_f32 v[118:119], v[118:119], v[114:115] op_sel:[0,1] op_sel_hi:[1,1]\n"
      "v_cvt_pk_bf16_f32 v220, v118, v119\n"
      "v_lshlrev_b32 v118, 16, v221\n"
      "v_and_b32 v119, 0xffff0000, v221\n"
      "v_pk_mul_f32 v[118:119], v[118:119], v[114:115] op_sel:[0,1] op_sel_hi:[1,1]\n"
      "v_cvt_pk_bf16_f32 v221, v118, v119\n"
      "v_lshlrev_b32 v118, 16, v222\n"
      "v_and_b32 v119, 0xffff0000, v222\n"
      "v_pk_mul_f32 v[118:119], v[118:119], v[114:115] op_sel:[0,1] op_sel_hi:[1,1]\n"
      "v_cvt_pk_bf16_f32 v222, v118, v119\n"
      "v_lshlrev_b32 v118, 16, v223\n"
      "v_and_b32 v119, 0xffff0000, v223\n"
      "v_pk_mul_f32 v[118:119], v[118:119], v[114:115] op_sel:[0,1] op_sel_hi:[1,1]\n"
      "v_cvt_pk_bf16_f32 v223, v118, v119\n"
      "v_lshlrev_b32 v118, 16, v192\n"
      "v_and_b32 v119, 0xffff0000, v192\n"
      "v_pk_mul_f32 v[118:119], v[118:119], v[112:113] op_sel:[0,0] op_sel_hi:[1,0]\n"
      "v_cvt_pk_bf16_f32 v192, v118, v119\n"
      "v_lshlrev_b32 v118, 16, v193\n"
      "v_and_b32 v119, 0xffff0000, v193\n"
      "v_pk_mul_f32 v[118:119], v[118:119], v[112:113] op_sel:[0,0] op_sel_hi:[1,0]\n"
      "v_cvt_pk_bf16_f32 v193, v118, v119\n"
      "v_lshlrev_b32 v118, 16, v194\n"
      "v_and_b32 v119, 0xffff0000, v194\n"
      "v_pk_mul_f32 v[118:119], v[118:119], v[112:113] op_sel:[0,0] op_sel_hi:[1,0]\n"
      "v_cvt_pk_bf16_f32 v194, v118, v119\n"
      "v_lshlrev_b32 v118, 16, v195\n"
      "v_and_b32 v119, 0xffff0000, v195\n"
      "v_pk_mul_f32 v[118:119], v[118:119], v[112:113] op_sel:[0,0] op_sel_hi:[1,0]\n"
      "v_cvt_pk_bf16_f32 v195, v118, v119\n"
      "v_lshlrev_b32 v118, 16, v196\n"
      "v_and_b32 v119, 0xffff0000, v196\n"
      "v_pk_mul_f32 v[118:119], v[118:119], v[112:113] op_sel:[0,1] op_sel_hi:[1,1]\n"
      "v_cvt_pk_bf16_f32 v196, v118, v119\n"
      "v_lshlrev_b32 v118, 16, v197\n"
      "v_and_b32 v119, 0xffff0000, v197\n"
      "v_pk_mul_f32 v[118:119], v[118:119], v[112:113] op_sel:[0,1] op_sel_hi:[1,1]\n"
      "v_cvt_pk_bf16_f32 v197, v118, v119\n"
      "v_lshlrev_b32 v118, 16, v198\n"
      "v_and_b32 v119, 0xffff0000, v198\n"
      "v_pk_mul_f32 v[118:119], v[118:119], v[112:113] op_sel:[0,1] op_sel_hi:[1,1]\n"
      "v_cvt_pk_bf16_f32 v198, v118, v119\n"
      "v_lshlrev_b32 v118, 16, v199\n"
      "v_and_b32 v119, 0xffff0000, v199\n"
      "v_pk_mul_f32 v[118:119], v[118:119], v[112:113] op_sel:[0,1] op_sel_hi:[1,1]\n"
      "v_cvt_pk_bf16_f32 v199, v118, v119\n"
      "v_lshlrev_b32 v118, 16, v200\n"
      "v_and_b32 v119, 0xffff0000, v200\n"
      "v_pk_mul_f32 v[118:119], v[118:119], v[114:115] op_sel:[0,0] op_sel_hi:[1,0]\n"
      "v_cvt_pk_bf16_f32 v200, v118, v119\n"
      "v_lshlrev_b32 v118, 16, v201\n"
      "v_and_b32 v119, 0xffff0000, v201\n"
      "v_pk_mul_f32 v[118:119], v[118:119], v[114:115] op_sel:[0,0] op_sel_hi:[1,0]\n"
      "v_cvt_pk_bf16_f32 v201, v118, v119\n"
      "v_lshlrev_b32 v118, 16, v202\n"
      "v_and_b32 v119, 0xffff0000, v202\n"
      "v_pk_mul_f32 v[118:119], v[118:119], v[114:115] op_sel:[0,0] op_sel_hi:[1,0]\n"
      "v_cvt_pk_bf16_f32 v202, v118, v119\n"
      "v_lshlrev_b32 v118, 16, v203\n"
      "v_and_b32 v119, 0xffff0000, v203\n"
      "v_pk_mul_f32 v[118:119], v[118:119], v[114:115] op_sel:[0,0] op_sel_hi:[1,0]\n"
      "v_cvt_pk_bf16_f32 v203, v118, v119\n"
      "v_lshlrev_b32 v118, 16, v204\n"
      "v_and_b32 v119, 0xffff0000, v204\n"
      "v_pk_mul_f32 v[118:119], v[118:119], v[114:115] op_sel:[0,1] op_sel_hi:[1,1]\n"
      "v_cvt_pk_bf16_f32 v204, v118, v119\n"
      "v_lshlrev_b32 v118, 16, v205\n"
      "v_and_b32 v119, 0xffff0000, v205\n"
      "v_pk_mul_f32 v[118:119], v[118:119], v[114:115] op_sel:[0,1] op_sel_hi:[1,1]\n"
      "v_cvt_pk_bf16_f32 v205, v118, v119\n"
      "v_lshlrev_b32 v118, 16, v206\n"
      "v_and_b32 v119, 0xffff0000, v206\n"
      "v_pk_mul_f32 v[118:119], v[118:119], v[114:115] op_sel:[0,1] op_sel_hi:[1,1]\n"
      "v_cvt_pk_bf16_f32 v206, v118, v119\n"
      "v_lshlrev_b32 v118, 16, v207\n"
      "v_and_b32 v119, 0xffff0000, v207\n"
      "v_pk_mul_f32 v[118:119], v[118:119], v[114:115] op_sel:[0,1] op_sel_hi:[1,1]\n"
      "v_cvt_pk_bf16_f32 v207, v118, v119\n"
      "v_lshlrev_b32 v118, 16, v0\n"
      "v_and_b32 v119, 0xffff0000, v0\n"
      "v_pk_mul_f32 v[118:119], v[118:119], v[112:113] op_sel:[0,0] op_sel_hi:[1,0]\n"
      "v_cvt_pk_bf16_f32 v0, v118, v119\n"
      "v_lshlrev_b32 v118, 16, v1\n"
      "v_and_b32 v119, 0xffff0000, v1\n"
      "v_pk_mul_f32 v[118:119], v[118:119], v[112:113] op_sel:[0,0] op_sel_hi:[1,0]\n"
      "v_cvt_pk_bf16_f32 v1, v118, v119\n"
      "v_lshlrev_b32 v118, 16, v2\n"
      "v_and_b32 v119, 0xffff0000, v2\n"
      "v_pk_mul_f32 v[118:119], v[118:119], v[112:113] op_sel:[0,0] op_sel_hi:[1,0]\n"
      "v_cvt_pk_bf16_f32 v2, v118, v119\n"
      "v_lshlrev_b32 v118, 16, v3\n"
      "v_and_b32 v119, 0xffff0000, v3\n"
      "v_pk_mul_f32 v[118:119], v[118:119], v[112:113] op_sel:[0,0] op_sel_hi:[1,0]\n"
      "v_cvt_pk_bf16_f32 v3, v118, v119\n"
      "v_lshlrev_b32 v118, 16, v4\n"
      "v_and_b32 v119, 0xffff0000, v4\n"
      "v_pk_mul_f32 v[118:119], v[118:119], v[112:113] op_sel:[0,1] op_sel_hi:[1,1]\n"
      "v_cvt_pk_bf16_f32 v4, v118, v119\n"
      "v_lshlrev_b32 v118, 16, v5\n"
      "v_and_b32 v119, 0xffff0000, v5\n"
      "v_pk_mul_f32 v[118:119], v[118:119], v[112:113] op_sel:[0,1] op_sel_hi:[1,1]\n"
      "v_cvt_pk_bf16_f32 v5, v118, v119\n"
      "v_lshlrev_b32 v118, 16, v6\n"
      "v_and_b32 v119, 0xffff0000, v6\n"
      "v_pk_mul_f32 v[118:119], v[118:119], v[112:113] op_sel:[0,1] op_sel_hi:[1,1]\n"
      "v_cvt_pk_bf16_f32 v6, v118, v119\n"
      "v_lshlrev_b32 v118, 16, v7\n"
      "v_and_b32 v119, 0xffff0000, v7\n"
      "v_pk_mul_f32 v[118:119], v[118:119], v[112:113] op_sel:[0,1] op_sel_hi:[1,1]\n"
      "v_cvt_pk_bf16_f32 v7, v118, v119\n"
      "v_lshlrev_b32 v118, 16, v8\n"
      "v_and_b32 v119, 0xffff0000, v8\n"
      "v_pk_mul_f32 v[118:119], v[118:119], v[114:115] op_sel:[0,0] op_sel_hi:[1,0]\n"
      "v_cvt_pk_bf16_f32 v8, v118, v119\n"
      "v_lshlrev_b32 v118, 16, v9\n"
      "v_and_b32 v119, 0xffff0000, v9\n"
      "v_pk_mul_f32 v[118:119], v[118:119], v[114:115] op_sel:[0,0] op_sel_hi:[1,0]\n"
      "v_cvt_pk_bf16_f32 v9, v118, v119\n"
      "v_lshlrev_b32 v118, 16, v10\n"
      "v_and_b32 v119, 0xffff0000, v10\n"
      "v_pk_mul_f32 v[118:119], v[118:119], v[114:115] op_sel:[0,0] op_sel_hi:[1,0]\n"
      "v_cvt_pk_bf16_f32 v10, v118, v119\n"
      "v_lshlrev_b32 v118, 16, v11\n"
      "v_and_b32 v119, 0xffff0000, v11\n"
      "v_pk_mul_f32 v[118:119], v[118:119], v[114:115] op_sel:[0,0] op_sel_hi:[1,0]\n"
      "v_cvt_pk_bf16_f32 v11, v118, v119\n"
      "v_lshlrev_b32 v118, 16, v12\n"
      "v_and_b32 v119, 0xffff0000, v12\n"
      "v_pk_mul_f32 v[118:119], v[118:119], v[114:115] op_sel:[0,1] op_sel_hi:[1,1]\n"
      "v_cvt_pk_bf16_f32 v12, v118, v119\n"
      "v_lshlrev_b32 v118, 16, v13\n"
      "v_and_b32 v119, 0xffff0000, v13\n"
      "v_pk_mul_f32 v[118:119], v[118:119], v[114:115] op_sel:[0,1] op_sel_hi:[1,1]\n"
      "v_cvt_pk_bf16_f32 v13, v118, v119\n"
      "v_lshlrev_b32 v118, 16, v14\n"
      "v_and_b32 v119, 0xffff0000, v14\n"
      "v_pk_mul_f32 v[118:119], v[118:119], v[114:115] op_sel:[0,1] op_sel_hi:[1,1]\n"
      "v_cvt_pk_bf16_f32 v14, v118, v119\n"
      "v_lshlrev_b32 v118, 16, v15\n"
      "v_and_b32 v119, 0xffff0000, v15\n"
      "v_pk_mul_f32 v[118:119], v[118:119], v[114:115] op_sel:[0,1] op_sel_hi:[1,1]\n"
      "v_cvt_pk_bf16_f32 v15, v118, v119\n"
      "v_lshlrev_b32 v118, 16, v16\n"
      "v_and_b32 v119, 0xffff0000, v16\n"
      "v_pk_mul_f32 v[118:119], v[118:119], v[112:113] op_sel:[0,0] op_sel_hi:[1,0]\n"
      "v_cvt_pk_bf16_f32 v16, v118, v119\n"
      "v_lshlrev_b32 v118, 16, v17\n"
      "v_and_b32 v119, 0xffff0000, v17\n"
      "v_pk_mul_f32 v[118:119], v[118:119], v[112:113] op_sel:[0,0] op_sel_hi:[1,0]\n"
      "v_cvt_pk_bf16_f32 v17, v118, v119\n"
      "v_lshlrev_b32 v118, 16, v18\n"
      "v_and_b32 v119, 0xffff0000, v18\n"
      "v_pk_mul_f32 v[118:119], v[118:119], v[112:113] op_sel:[0,0] op_sel_hi:[1,0]\n"
      "v_cvt_pk_bf16_f32 v18, v118, v119\n"
      "v_lshlrev_b32 v118, 16, v19\n"
      "v_and_b32 v119, 0xffff0000, v19\n"
      "v_pk_mul_f32 v[118:119], v[118:119], v[112:113] op_sel:[0,0] op_sel_hi:[1,0]\n"
      "v_cvt_pk_bf16_f32 v19, v118, v119\n"
      "v_lshlrev_b32 v118, 16, v20\n"
      "v_and_b32 v119, 0xffff0000, v20\n"
      "v_pk_mul_f32 v[118:119], v[118:119], v[112:113] op_sel:[0,1] op_sel_hi:[1,1]\n"
      "v_cvt_pk_bf16_f32 v20, v118, v119\n"
      "v_lshlrev_b32 v118, 16, v21\n"
      "v_and_b32 v119, 0xffff0000, v21\n"
      "v_pk_mul_f32 v[118:119], v[118:119], v[112:113] op_sel:[0,1] op_sel_hi:[1,1]\n"
      "v_cvt_pk_bf16_f32 v21, v118, v119\n"
      "v_lshlrev_b32 v118, 16, v22\n"
      "v_and_b32 v119, 0xffff0000, v22\n"
      "v_pk_mul_f32 v[118:119], v[118:119], v[112:113] op_sel:[0,1] op_sel_hi:[1,1]\n"
      "v_cvt_pk_bf16_f32 v22, v118, v119\n"
      "v_lshlrev_b32 v118, 16, v23\n"
      "v_and_b32 v119, 0xffff0000, v23\n"
      "v_pk_mul_f32 v[118:119], v[118:119], v[112:113] op_sel:[0,1] op_sel_hi:[1,1]\n"
      "v_cvt_pk_bf16_f32 v23, v118, v119\n"
      "v_lshlrev_b32 v118, 16, v24\n"
      "v_and_b32 v119, 0xffff0000, v24\n"
      "v_pk_mul_f32 v[118:119], v[118:119], v[114:115] op_sel:[0,0] op_sel_hi:[1,0]\n"
      "v_cvt_pk_bf16_f32 v24, v118, v119\n"
      "v_lshlrev_b32 v118, 16, v25\n"
      "v_and_b32 v119, 0xffff0000, v25\n"
      "v_pk_mul_f32 v[118:119], v[118:119], v[114:115] op_sel:[0,0] op_sel_hi:[1,0]\n"
      "v_cvt_pk_bf16_f32 v25, v118, v119\n"
      "v_lshlrev_b32 v118, 16, v26\n"
      "v_and_b32 v119, 0xffff0000, v26\n"
      "v_pk_mul_f32 v[118:119], v[118:119], v[114:115] op_sel:[0,0] op_sel_hi:[1,0]\n"
      "v_cvt_pk_bf16_f32 v26, v118, v119\n"
      "v_lshlrev_b32 v118, 16, v27\n"
      "v_and_b32 v119, 0xffff0000, v27\n"
      "v_pk_mul_f32 v[118:119], v[118:119], v[114:115] op_sel:[0,0] op_sel_hi:[1,0]\n"
      "v_cvt_pk_bf16_f32 v27, v118, v119\n"
      "v_lshlrev_b32 v118, 16, v28\n"
      "v_and_b32 v119, 0xffff0000, v28\n"
      "v_pk_mul_f32 v[118:119], v[118:119], v[114:115] op_sel:[0,1] op_sel_hi:[1,1]\n"
      "v_cvt_pk_bf16_f32 v28, v118, v119\n"
      "v_lshlrev_b32 v118, 16, v29\n"
      "v_and_b32 v119, 0xffff0000, v29\n"
      "v_pk_mul_f32 v[118:119], v[118:119], v[114:115] op_sel:[0,1] op_sel_hi:[1,1]\n"
      "v_cvt_pk_bf16_f32 v29, v118, v119\n"
      "v_lshlrev_b32 v118, 16, v30\n"
      "v_and_b32 v119, 0xffff0000, v30\n"
      "v_pk_mul_f32 v[118:119], v[118:119], v[114:115] op_sel:[0,1] op_sel_hi:[1,1]\n"
      "v_cvt_pk_bf16_f32 v30, v118, v119\n"
      "v_lshlrev_b32 v118, 16, v31\n"
      "v_and_b32 v119, 0xffff0000, v31\n"
      "v_pk_mul_f32 v[118:119], v[118:119], v[114:115] op_sel:[0,1] op_sel_hi:[1,1]\n"
      "v_cvt_pk_bf16_f32 v31, v118, v119\n"
      "v_lshlrev_b32 v118, 16, v96\n"
      "v_and_b32 v119, 0xffff0000, v96\n"
      "v_pk_mul_f32 v[118:119], v[118:119], v[112:113] op_sel:[0,0] op_sel_hi:[1,0]\n"
      "v_cvt_pk_bf16_f32 v96, v118, v119\n"
      "v_lshlrev_b32 v118, 16, v97\n"
      "v_and_b32 v119, 0xffff0000, v97\n"
      "v_pk_mul_f32 v[118:119], v[118:119], v[112:113] op_sel:[0,0] op_sel_hi:[1,0]\n"
      "v_cvt_pk_bf16_f32 v97, v118, v119\n"
      "v_lshlrev_b32 v118, 16, v98\n"
      "v_and_b32 v119, 0xffff0000, v98\n"
      "v_pk_mul_f32 v[118:119], v[118:119], v[112:113] op_sel:[0,0] op_sel_hi:[1,0]\n"
      "v_cvt_pk_bf16_f32 v98, v118, v119\n"
      "v_lshlrev_b32 v118, 16, v99\n"
      "v_and_b32 v119, 0xffff0000, v99\n"
      "v_pk_mul_f32 v[118:119], v[118:119], v[112:113] op_sel:[0,0] op_sel_hi:[1,0]\n"
      "v_cvt_pk_bf16_f32 v99, v118, v119\n"
      "v_lshlrev_b32 v118, 16, v100\n"
      "v_and_b32 v119, 0xffff0000, v100\n"
      "v_pk_mul_f32 v[118:119], v[118:119], v[112:113] op_sel:[0,1] op_sel_hi:[1,1]\n"
      "v_cvt_pk_bf16_f32 v100, v118, v119\n"
      "v_lshlrev_b32 v118, 16, v101\n"
      "v_and_b32 v119, 0xffff0000, v101\n"
      "v_pk_mul_f32 v[118:119], v[118:119], v[112:113] op_sel:[0,1] op_sel_hi:[1,1]\n"
      "v_cvt_pk_bf16_f32 v101, v118, v119\n"
      "v_lshlrev_b32 v118, 16, v102\n"
      "v_and_b32 v119, 0xffff0000, v102\n"
      "v_pk_mul_f32 v[118:119], v[118:119], v[112:113] op_sel:[0,1] op_sel_hi:[1,1]\n"
      "v_cvt_pk_bf16_f32 v102, v118, v119\n"
      "v_lshlrev_b32 v118, 16, v103\n"
      "v_and_b32 v119, 0xffff0000, v103\n"
      "v_pk_mul_f32 v[118:119], v[118:119], v[112:113] op_sel:[0,1] op_sel_hi:[1,1]\n"
      "v_cvt_pk_bf16_f32 v103, v118, v119\n"
      "v_lshlrev_b32 v118, 16, v104\n"
      "v_and_b32 v119, 0xffff0000, v104\n"
      "v_pk_mul_f32 v[118:119], v[118:119], v[114:115] op_sel:[0,0] op_sel_hi:[1,0]\n"
      "v_cvt_pk_bf16_f32 v104, v118, v119\n"
      "v_lshlrev_b32 v118, 16, v105\n"
      "v_and_b32 v119, 0xffff0000, v105\n"
      "v_pk_mul_f32 v[118:119], v[118:119], v[114:115] op_sel:[0,0] op_sel_hi:[1,0]\n"
      "v_cvt_pk_bf16_f32 v105, v118, v119\n"
      "v_lshlrev_b32 v118, 16, v106\n"
      "v_and_b32 v119, 0xffff0000, v106\n"
      "v_pk_mul_f32 v[118:119], v[118:119], v[114:115] op_sel:[0,0] op_sel_hi:[1,0]\n"
      "v_cvt_pk_bf16_f32 v106, v118, v119\n"
      "v_lshlrev_b32 v118, 16, v107\n"
      "v_and_b32 v119, 0xffff0000, v107\n"
      "v_pk_mul_f32 v[118:119], v[118:119], v[114:115] op_sel:[0,0] op_sel_hi:[1,0]\n"
      "v_cvt_pk_bf16_f32 v107, v118, v119\n"
      "v_lshlrev_b32 v118, 16, v108\n"
      "v_and_b32 v119, 0xffff0000, v108\n"
      "v_pk_mul_f32 v[118:119], v[118:119], v[114:115] op_sel:[0,1] op_sel_hi:[1,1]\n"
      "v_cvt_pk_bf16_f32 v108, v118, v119\n"
      "v_lshlrev_b32 v118, 16, v109\n"
      "v_and_b32 v119, 0xffff0000, v109\n"
      "v_pk_mul_f32 v[118:119], v[118:119], v[114:115] op_sel:[0,1] op_sel_hi:[1,1]\n"
      "v_cvt_pk_bf16_f32 v109, v118, v119\n"
      "v_lshlrev_b32 v118, 16, v110\n"
      "v_and_b32 v119, 0xffff0000, v110\n"
      "v_pk_mul_f32 v[118:119], v[118:119], v[114:115] op_sel:[0,1] op_sel_hi:[1,1]\n"
      "v_cvt_pk_bf16_f32 v110, v118, v119\n"
      "v_lshlrev_b32 v118, 16, v111\n"
      "v_and_b32 v119, 0xffff0000, v111\n"
      "v_pk_mul_f32 v[118:119], v[118:119], v[114:115] op_sel:[0,1] op_sel_hi:[1,1]\n"
      "v_cvt_pk_bf16_f32 v111, v118, v119\n"
      "s_setpc_b64 s[84:85]\n"
      "MIS_%=:\n"
      "s_waitcnt lgkmcnt(0)\n"
      "v_sub_f32 v148, v153, v117\n"
      "v_min_f32 v149, 0, -v148\n"
      "v_min_f32 v148, 0, v148\n"
      "v_exp_f32 v149, v149\n"
      "v_exp_f32 v148, v148\n"
      "v_min_f32 v117, v117, v153\n"
      "v_mov_b32 v112, v148\n"
      "v_mov_b32 v228, v149\n"
      "v_mov_b32 v114, v112\n"
      "v_mov_b32 v230, v228\n"
      "s_nop 0\n"
      "v_permlane32_swap_b32 v112, v114\n"
      "v_permlane32_swap_b32 v228, v230\n"
      "v_mov_b32 v113, v112\n"
      "v_mov_b32 v229, v228\n"
      "v_mov_b32 v115, v114\n"
      "v_mov_b32 v231, v230\n"
      "v_permlane16_swap_b32 v112, v113\n"
      "v_permlane16_swap_b32 v228, v229\n"
      "v_permlane16_swap_b32 v114, v115\n"
      "v_permlane16_swap_b32 v230, v231\n"
      "s_call_b64 s[84:85], SCALE_%=\n"
      "v_pk_mul_f32 v[160:161], v[160:161], v[228:229] op_sel:[0,0] op_sel_hi:[1,0]\n"
      "v_pk_mul_f32 v[162:163], v[162:163], v[228:229] op_sel:[0,0] op_sel_hi:[1,0]\n"
      "v_pk_mul_f32 v[164:165], v[164:165], v[228:229] op_sel:[0,1] op_sel_hi:[1,1]\n"
      "v_pk_mul_f32 v[166:167], v[166:167], v[228:229] op_sel:[0,1] op_sel_hi:[1,1]\n"
      "v_pk_mul_f32 v[168:169], v[168:169], v[230:231] op_sel:[0,0] op_sel_hi:[1,0]\n"
      "v_pk_mul_f32 v[170:171], v[170:171], v[230:231] op_sel:[0,0] op_sel_hi:[1,0]\n"
      "v_pk_mul_f32 v[172:173], v[172:173], v[230:231] op_sel:[0,1] op_sel_hi:[1,1]\n"
      "v_pk_mul_f32 v[174:175], v[174:175], v[230:231] op_sel:[0,1] op_sel_hi:[1,1]\n"
      "v_pk_mul_f32 v[176:177], v[176:177], v[228:229] op_sel:[0,0] op_sel_hi:[1,0]\n"
      "v_pk_mul_f32 v[178:179], v[178:179], v[228:229] op_sel:[0,0] op_sel_hi:[1,0]\n"
      "v_pk_mul_f32 v[180:181], v[180:181], v[228:229] op_sel:[0,1] op_sel_hi:[1,1]\n"
      "v_pk_mul_f32 v[182:183], v[182:183], v[228:229] op_sel:[0,1] op_sel_hi:[1,1]\n"
      "v_pk_mul_f32 v[184:185], v[184:185], v[230:231] op_sel:[0,0] op_sel_hi:[1,0]\n"
      "v_pk_mul_f32 v[186:187], v[186:187], v[230:231] op_sel:[0,0] op_sel_hi:[1,0]\n"
      "v_pk_mul_f32 v[188:189], v[188:189], v[230:231] op_sel:[0,1] op_sel_hi:[1,1]\n"
      "v_pk_mul_f32 v[190:191], v[190:191], v[230:231] op_sel:[0,1] op_sel_hi:[1,1]\n"
      "v_mov_b32 v112, v228\n"
      "v_mov_b32 v113, v229\n"
      "v_mov_b32 v114, v230\n"
      "v_mov_b32 v115, v231\n"
      "v_accvgpr_read_b32 v128, a64\n"
      "v_accvgpr_read_b32 v129, a65\n"
      "v_accvgpr_read_b32 v130, a66\n"
      "v_accvgpr_read_b32 v131, a67\n"
      "v_accvgpr_read_b32 v132, a68\n"
      "v_accvgpr_read_b32 v133, a69\n"
      "v_accvgpr_read_b32 v134, a70\n"
      "v_accvgpr_read_b32 v135, a71\n"
      "v_accvgpr_read_b32 v136, a72\n"
      "v_accvgpr_read_b32 v137, a73\n"
      "v_accvgpr_read_b32 v138, a74\n"
      "v_accvgpr_read_b32 v139, a75\n"
      "v_accvgpr_read_b32 v140, a76\n"
      "v_accvgpr_read_b32 v141, a77\n"
      "v_accvgpr_read_b32 v142, a78\n"
      "v_accvgpr_read_b32 v143, a79\n"
      "v_lshlrev_b32 v118, 16, v128\n"
      "v_and_b32 v119, 0xffff0000, v128\n"
      "v_pk_mul_f32 v[118:119], v[118:119], v[112:113] op_sel:[0,0] op_sel_hi:[1,0]\n"
      "v_cvt_pk_bf16_f32 v128, v118, v119\n"
      "v_lshlrev_b32 v118, 16, v129\n"
      "v_and_b32 v119, 0xffff0000, v129\n"
      "v_pk_mul_f32 v[118:119], v[118:119], v[112:113] op_sel:[0,0] op_sel_hi:[1,0]\n"
      "v_cvt_pk_bf16_f32 v129, v118, v119\n"
      "v_lshlrev_b32 v118, 16, v130\n"
      "v_and_b32 v119, 0xffff0000, v130\n"
      "v_pk_mul_f32 v[118:119], v[118:119], v[112:113] op_sel:[0,0] op_sel_hi:[1,0]\n"
      "v_cvt_pk_bf16_f32 v130, v118, v119\n"
      "v_lshlrev_b32 v118, 16, v131\n"
      "v_and_b32 v119, 0xffff0000, v131\n"
      "v_pk_mul_f32 v[118:119], v[118:119], v[112:113] op_sel:[0,0] op_sel_hi:[1,0]\n"
      "v_cvt_pk_bf16_f32 v131, v118, v119\n"
      "v_lshlrev_b32 v118, 16, v132\n"
      "v_and_b32 v119, 0xffff0000, v132\n"
      "v_pk_mul_f32 v[118:119], v[118:119], v[112:113] op_sel:[0,1] op_sel_hi:[1,1]\n"
      "v_cvt_pk_bf16_f32 v132, v118, v119\n"
      "v_lshlrev_b32 v118, 16, v133\n"
      "v_and_b32 v119, 0xffff0000, v133\n"
      "v_pk_mul_f32 v[118:119], v[118:119], v[112:113] op_sel:[0,1] op_sel_hi:[1,1]\n"
      "v_cvt_pk_bf16_f32 v133, v118, v119\n"
      "v_lshlrev_b32 v118, 16, v134\n"
      "v_and_b32 v119, 0xffff0000, v134\n"
      "v_pk_mul_f32 v[118:119], v[118:119], v[112:113] op_sel:[0,1] op_sel_hi:[1,1]\n"
      "v_cvt_pk_bf16_f32 v134, v118, v119\n"
      "v_lshlrev_b32 v118, 16, v135\n"
      "v_and_b32 v119, 0xffff0000, v135\n"
      "v_pk_mul_f32 v[118:119], v[118:119], v[112:113] op_sel:[0,1] op_sel_hi:[1,1]\n"
      "v_cvt_pk_bf16_f32 v135, v118, v119\n"
      "v_lshlrev_b32 v118, 16, v136\n"
      "v_and_b32 v119, 0xffff0000, v136\n"
      "v_pk_mul_f32 v[118:119], v[118:119], v[114:115] op_sel:[0,0] op_sel_hi:[1,0]\n"
      "v_cvt_pk_bf16_f32 v136, v118, v119\n"
      "v_lshlrev_b32 v118, 16, v137\n"
      "v_and_b32 v119, 0xffff0000, v137\n"
      "v_pk_mul_f32 v[118:119], v[118:119], v[114:115] op_sel:[0,0] op_sel_hi:[1,0]\n"
      "v_cvt_pk_bf16_f32 v137, v118, v119\n"
      "v_lshlrev_b32 v118, 16, v138\n"
      "v_and_b32 v119, 0xffff0000, v138\n"
      "v_pk_mul_f32 v[118:119], v[118:119], v[114:115] op_sel:[0,0] op_sel_hi:[1,0]\n"
      "v_cvt_pk_bf16_f32 v138, v118, v119\n"
      "v_lshlrev_b32 v118, 16, v139\n"
      "v_and_b32 v119, 0xffff0000, v139\n"
      "v_pk_mul_f32 v[118:119], v[118:119], v[114:115] op_sel:[0,0] op_sel_hi:[1,0]\n"
      "v_cvt_pk_bf16_f32 v139, v118, v119\n"
      "v_lshlrev_b32 v118, 16, v140\n"
      "v_and_b32 v119, 0xffff0000, v140\n"
      "v_pk_mul_f32 v[118:119], v[118:119], v[114:115] op_sel:[0,1] op_sel_hi:[1,1]\n"
      "v_cvt_pk_bf16_f32 v140, v118, v119\n"
      "v_lshlrev_b32 v118, 16, v141\n"
      "v_and_b32 v119, 0xffff0000, v141\n"
      "v_pk_mul_f32 v[118:119], v[118:119], v[114:115] op_sel:[0,1] op_sel_hi:[1,1]\n"
      "v_cvt_pk_bf16_f32 v141, v118, v119\n"
      "v_lshlrev_b32 v118, 16, v142\n"
      "v_and_b32 v119, 0xffff0000, v142\n"
      "v_pk_mul_f32 v[118:119], v[118:119], v[114:115] op_sel:[0,1] op_sel_hi:[1,1]\n"
      "v_cvt_pk_bf16_f32 v142, v118, v119\n"
      "v_lshlrev_b32 v118, 16, v143\n"
      "v_and_b32 v119, 0xffff0000, v143\n"
      "v_pk_mul_f32 v[118:119], v[118:119], v[114:115] op_sel:[0,1] op_sel_hi:[1,1]\n"
      "v_cvt_pk_bf16_f32 v143, v118, v119\n"
      "v_accvgpr_write_b32 a64, v128\n"
      "v_accvgpr_write_b32 a65, v129\n"
      "v_accvgpr_write_b32 a66, v130\n"
      "v_accvgpr_write_b32 a67, v131\n"
      "v_accvgpr_write_b32 a68, v132\n"
      "v_accvgpr_write_b32 a69, v133\n"
      "v_accvgpr_write_b32 a70, v134\n"
      "v_accvgpr_write_b32 a71, v135\n"
      "v_accvgpr_write_b32 a72, v136\n"
      "v_accvgpr_write_b32 a73, v137\n"
      "v_accvgpr_write_b32 a74, v138\n"
      "v_accvgpr_write_b32 a75, v139\n"
      "v_accvgpr_write_b32 a76, v140\n"
      "v_accvgpr_write_b32 a77, v141\n"
      "v_accvgpr_write_b32 a78, v142\n"
      "v_accvgpr_write_b32 a79, v143\n"
      "s_branch MR_%=\n"
      "FIX_%=:\n"
      "s_nop 7\n"
      "v_sub_f32 v224, v81, v117\n"
      "v_min_f32 v112, 0, -v224\n"
      "v_min_f32 v224, 0, v224\n"
      "v_exp_f32 v112, v112\n"
      "v_exp_f32 v224, v224\n"
      "s_nop 0\n"
      "v_mul_f32 v144, v144, v224\n"
      "v_mul_f32 v80, v80, v112\n"
      "v_mov_b32 v226, v224\n"
      "v_mov_b32 v114, v112\n"
      "s_nop 0\n"
      "v_permlane32_swap_b32 v224, v226\n"
      "v_permlane32_swap_b32 v112, v114\n"
      "v_mov_b32 v225, v224\n"
      "v_mov_b32 v113, v112\n"
      "v_mov_b32 v227, v226\n"
      "v_mov_b32 v115, v114\n"
      "v_permlane16_swap_b32 v224, v225\n"
      "v_permlane16_swap_b32 v112, v113\n"
      "v_permlane16_swap_b32 v226, v227\n"
      "v_permlane16_swap_b32 v114, v115\n"
      "v_pk_mul_f32 v[32:33], v[32:33], v[224:225] op_sel:[0,0] op_sel_hi:[1,0]\n"
      "v_pk_mul_f32 v[34:35], v[34:35], v[224:225] op_sel:[0,0] op_sel_hi:[1,0]\n"
      "v_pk_mul_f32 v[36:37], v[36:37], v[224:225] op_sel:[0,1] op_sel_hi:[1,1]\n"
      "v_pk_mul_f32 v[38:39], v[38:39], v[224:225] op_sel:[0,1] op_sel_hi:[1,1]\n"
      "v_pk_mul_f32 v[40:41], v[40:41], v[226:227] op_sel:[0,0] op_sel_hi:[1,0]\n"
      "v_pk_mul_f32 v[42:43], v[42:43], v[226:227] op_sel:[0,0] op_sel_hi:[1,0]\n"
      "v_pk_mul_f32 v[44:45], v[44:45], v[226:227] op_sel:[0,1] op_sel_hi:[1,1]\n"
      "v_pk_mul_f32 v[46:47], v[46:47], v[226:227] op_sel:[0,1] op_sel_hi:[1,1]\n"
      "v_pk_mul_f32 v[48:49], v[48:49], v[224:225] op_sel:[0,0] op_sel_hi:[1,0]\n"
      "v_pk_mul_f32 v[50:51], v[50:51], v[224:225] op_sel:[0,0] op_sel_hi:[1,0]\n"
      "v_pk_mul_f32 v[52:53], v[52:53], v[224:225] op_sel:[0,1] op_sel_hi:[1,1]\n"
      "v_pk_mul_f32 v[54:55], v[54:55], v[224:225] op_sel:[0,1] op_sel_hi:[1,1]\n"
      "v_pk_mul_f32 v[56:57], v[56:57], v[226:227] op_sel:[0,0] op_sel_hi:[1,0]\n"
      "v_pk_mul_f32 v[58:59], v[58:59], v[226:227] op_sel:[0,0] op_sel_hi:[1,0]\n"
      "v_pk_mul_f32 v[60:61], v[60:61], v[226:227] op_sel:[0,1] op_sel_hi:[1,1]\n"
      "v_pk_mul_f32 v[62:63], v[62:63], v[226:227] op_sel:[0,1] op_sel_hi:[1,1]\n"
      "v_lshlrev_b32 v118, 16, v64\n"
      "v_and_b32 v119, 0xffff0000, v64\n"
      "v_pk_mul_f32 v[118:119], v[118:119], v[112:113] op_sel:[0,0] op_sel_hi:[1,0]\n"
      "v_cvt_pk_bf16_f32 v64, v118, v119\n"
      "v_lshlrev_b32 v118, 16, v65\n"
      "v_and_b32 v119, 0xffff0000, v65\n"
      "v_pk_mul_f32 v[118:119], v[118:119], v[112:113] op_sel:[0,0] op_sel_hi:[1,0]\n"
      "v_cvt_pk_bf16_f32 v65, v118, v119\n"
      "v_lshlrev_b32 v118, 16, v66\n"
      "v_and_b32 v119, 0xffff0000, v66\n"
      "v_pk_mul_f32 v[118:119], v[118:119], v[112:113] op_sel:[0,0] op_sel_hi:[1,0]\n"
      "v_cvt_pk_bf16_f32 v66, v118, v119\n"
      "v_lshlrev_b32 v118, 16, v67\n"
      "v_and_b32 v119, 0xffff0000, v67\n"
      "v_pk_mul_f32 v[118:119], v[118:119], v[112:113] op_sel:[0,0] op_sel_hi:[1,0]\n"
      "v_cvt_pk_bf16_f32 v67, v118, v119\n"
      "v_lshlrev_b32 v118, 16, v68\n"
      "v_and_b32 v119, 0xffff0000, v68\n"
      "v_pk_mul_f32 v[118:119], v[118:119], v[112:113] op_sel:[0,1] op_sel_hi:[1,1]\n"
      "v_cvt_pk_bf16_f32 v68, v118, v119\n"
      "v_lshlrev_b32 v118, 16, v69\n"
      "v_and_b32 v119, 0xffff0000, v69\n"
      "v_pk_mul_f32 v[118:119], v[118:119], v[112:113] op_sel:[0,1] op_sel_hi:[1,1]\n"
      "v_cvt_pk_bf16_f32 v69, v118, v119\n"
      "v_lshlrev_b32 v118, 16, v70\n"
      "v_and_b32 v119, 0xffff0000, v70\n"
      "v_pk_mul_f32 v[118:119], v[118:119], v[112:113] op_sel:[0,1] op_sel_hi:[1,1]\n"
      "v_cvt_pk_bf16_f32 v70, v118, v119\n"
      "v_lshlrev_b32 v118, 16, v71\n"
      "v_and_b32 v119, 0xffff0000, v71\n"
      "v_pk_mul_f32 v[118:119], v[118:119], v[112:113] op_sel:[0,1] op_sel_hi:[1,1]\n"
      "v_cvt_pk_bf16_f32 v71, v118, v119\n"
      "v_lshlrev_b32 v118, 16, v72\n"
      "v_and_b32 v119, 0xffff0000, v72\n"
      "v_pk_mul_f32 v[118:119], v[118:119], v[114:115] op_sel:[0,0] op_sel_hi:[1,0]\n"
      "v_cvt_pk_bf16_f32 v72, v118, v119\n"
      "v_lshlrev_b32 v118, 16, v73\n"
      "v_and_b32 v119, 0xffff0000, v73\n"
      "v_pk_mul_f32 v[118:119], v[118:119], v[114:115] op_sel:[0,0] op_sel_hi:[1,0]\n"
      "v_cvt_pk_bf16_f32 v73, v118, v119\n"
      "v_lshlrev_b32 v118, 16, v74\n"
      "v_and_b32 v119, 0xffff0000, v74\n"
      "v_pk_mul_f32 v[118:119], v[118:119], v[114:115] op_sel:[0,0] op_sel_hi:[1,0]\n"
      "v_cvt_pk_bf16_f32 v74, v118, v119\n"
      "v_lshlrev_b32 v118, 16, v75\n"
      "v_and_b32 v119, 0xffff0000, v75\n"
      "v_pk_mul_f32 v[118:119], v[118:119], v[114:115] op_sel:[0,0] op_sel_hi:[1,0]\n"
      "v_cvt_pk_bf16_f32 v75, v118, v119\n"
      "v_lshlrev_b32 v118, 16, v76\n"
      "v_and_b32 v119, 0xffff0000, v76\n"
      "v_pk_mul_f32 v[118:119], v[118:119], v[114:115] op_sel:[0,1] op_sel_hi:[1,1]\n"
      "v_cvt_pk_bf16_f32 v76, v118, v119\n"
      "v_lshlrev_b32 v118, 16, v77\n"
      "v_and_b32 v119, 0xffff0000, v77\n"
      "v_pk_mul_f32 v[118:119], v[118:119], v[114:115] op_sel:[0,1] op_sel_hi:[1,1]\n"
      "v_cvt_pk_bf16_f32 v77, v118, v119\n"
      "v_lshlrev_b32 v118, 16, v78\n"
      "v_and_b32 v119, 0xffff0000, v78\n"
      "v_pk_mul_f32 v[118:119], v[118:119], v[114:115] op_sel:[0,1] op_sel_hi:[1,1]\n"
      "v_cvt_pk_bf16_f32 v78, v118, v119\n"
      "v_lshlrev_b32 v118, 16, v79\n"
      "v_and_b32 v119, 0xffff0000, v79\n"
      "v_pk_mul_f32 v[118:119], v[118:119], v[114:115] op_sel:[0,1] op_sel_hi:[1,1]\n"
      "v_cvt_pk_bf16_f32 v79, v118, v119\n"
      "v_add_f32 v224, v144, v80\n"
      "v_rcp_f32 v224, v224\n"
      "v_mfma_f32_16x16x32_bf16 v[160:163], v[86:89], v[64:67], v[32:35]\n"
      "v_mfma_f32_16x16x32_bf16 v[176:179], v[84:87], v[64:67], v[48:51]\n"
      "v_mfma_f32_16x16x32_bf16 v[164:167], v[86:89], v[68:71], v[36:39]\n"
      "v_mfma_f32_16x16x32_bf16 v[180:183], v[84:87], v[68:71], v[52:55]\n"
      "v_mfma_f32_16x16x32_bf16 v[168:171], v[86:89], v[72:75], v[40:43]\n"
      "v_mfma_f32_16x16x32_bf16 v[184:187], v[84:87], v[72:75], v[56:59]\n"
      "v_mfma_f32_16x16x32_bf16 v[172:175], v[86:89], v[76:79], v[44:47]\n"
      "v_mfma_f32_16x16x32_bf16 v[188:191], v[84:87], v[76:79], v[60:63]\n"
      "v_mov_b32 v226, v224\n"
      "s_nop 1\n"
      "s_nop 0\n"
      "s_nop 1\n"
      "v_permlane32_swap_b32 v224, v226\n"
      "s_nop 1\n"
      "v_mov_b32 v225, v224\n"
      "s_nop 1\n"
      "v_mov_b32 v227, v226\n"
      "s_nop 1\n"
      "v_permlane16_swap_b32 v224, v225\n"
      "s_nop 1\n"
      "v_permlane16_swap_b32 v226, v227\n"
      "s_nop 1\n"
      "s_branch FR_%=\n"
      ".subsection 0\n"
      : "+{s[4:19]}"(ka), "+{v128}"(pre), "+{v[160:191]}"(qq), "+{s92}"(tch), "+{v[242:249]}"(ad), "+{v[250:251]}"(ra)
      : [qo] "s"(bk * 128 * D_ * 2), [oo] "s"((bk * 128 + rh * 64) * (D_ * 2) + (2 * split + kh) * 64 + 4096),
        [fo] "s"((unsigned)WS_EPOCH + bk * 128 + 64 + split * 8), [xo] "s"((unsigned)(WS_X + split * WS_X1 + (bk * 4 + wid) * XW_BYTES)),
        [xw] "s"(xarea(wid)), [xr] "s"(xarea(wid ^ 2)),
        [so0] "s"(so0), [inc] "s"(inc), [wb] "s"(wbase), [tv] "s"(wbase + STAGE_BYTES + KIMG + split * 8192), [n1] "s"(n1),
        [qoff] "s"(Q_OFF + rh * 16384 - kh * 8192), [toff] "s"(TREE_OFF - kh * 8192),
        [neg] "s"((wid ^ (3 * split)) ? 0xff800000u : 0u),
        [lead] "s"(blk * 4 + wid * 32), [cph] "s"(C2 + (unsigned)((jj & ~3) * 8 + xg * 4) * WIN / 256u),
        [rep] "s"((jj >> 2) == 160 / 32 ? 128 + (blk & 31) * 4 : (blk - 224) * 4)
      : "v0", "v1", "v2", "v3", "v4", "v5", "v6", "v7", "v8", "v9", "v10", "v11", "v12", "v13", "v14", "v15", "v16", "v17", "v18", "v19", "v20", "v21", "v22", "v23", "v24", "v25", "v26", "v27", "v28", "v29", "v30", "v31", "v32", "v33", "v34", "v35", "v36", "v37", "v38", "v39", "v40", "v41", "v42", "v43", "v44", "v45", "v46", "v47", "v48", "v49", "v50", "v51", "v52", "v53", "v54", "v55", "v56", "v57", "v58", "v59", "v60", "v61", "v62", "v63", "v64", "v65", "v66", "v67", "v68", "v69", "v70", "v71", "v72", "v73", "v74", "v75", "v76", "v77", "v78", "v79", "v80", "v81", "v82", "v83", "v84", "v85", "v86", "v87", "v88", "v89", "v90", "v91", "v92", "v93", "v94", "v95", "v96", "v97", "v98", "v99", "v100", "v101", "v102", "v103", "v104", "v105", "v106", "v107", "v108", "v109", "v110", "v111", "v112", "v113", "v114", "v115", "v116", "v117", "v118", "v119", "v120", "v121", "v122", "v123", "v124", "v125", "v126", "v127", "v129", "v130", "v131", "v132", "v133", "v134", "v135", "v136", "v137", "v138", "v139", "v140", "v141", "v142", "v143", "v144", "v145", "v146", "v147", "v148", "v149", "v150", "v151", "v152", "v153", "v154", "v155", "v156", "v157", "v158", "v159", "v192", "v193", "v194", "v195", "v196", "v197", "v198", "v199", "v200", "v201", "v202", "v203", "v204", "v205", "v206", "v207", "v208", "v209", "v210", "v211", "v212", "v213", "v214", "v215", "v216", "v217", "v218", "v219", "v220", "v221", "v222", "v223", "v224", "v225", "v226", "v227", "v228", "v229", "v230", "v231", "v232", "v233", "v234", "v235", "v236", "v237", "v238", "v239", "v240", "v241", "a0", "a1", "a2", "a3", "a4", "a5", "a6", "a7", "a8", "a9", "a10", "a11", "a12", "a13", "a14", "a15", "a16", "a17", "a18", "a19", "a20", "a21", "a22", "a23", "a24", "a25", "a26", "a27", "a28", "a29", "a30", "a31", "a32", "a33", "a34", "a35", "a36", "a37", "a38", "a39", "a40", "a41", "a42", "a43", "a44", "a45", "a46", "a47", "a48", "a49", "a50", "a51", "a52", "a53", "a54", "a55", "a56", "a57", "a58", "a59", "a60", "a61", "a62", "a63", "a64", "a65", "a66", "a67", "a68", "a69", "a70", "a71", "a72", "a73", "a74", "a75", "a76", "a77", "a78", "a79", "a80", "a81", "a82", "a83", "a84", "a85", "a86", "a87", "a88", "a89", "a90", "a91", "a92", "a93", "a94", "a95", "a96", "a97", "a98", "a99", "a100", "a101", "a102", "a103", "a104", "a105", "a106", "a107", "a108", "a109", "a110", "a111", "a112", "a113", "a114", "a115", "a116", "a117", "a118", "a119", "a120", "a121", "a122", "a123", "a124", "a125", "a126", "a127", "a128", "a129", "a130", "a131", "a132", "a133", "a134", "a135", "a136", "a137", "a138", "a139", "a140", "a141", "a142", "a143", "a144", "a145", "a146", "a147", "a148", "a149", "a150", "a151", "a152", "a153", "a154", "a155", "a156", "a157", "a158", "a159", "a160", "a161", "a162", "a163", "a164", "a165", "a166", "a167", "a168", "a169", "a170", "a171", "a172", "a173", "a174", "a175", "a176", "a177", "a178", "a179", "a180", "a181", "a182", "a183", "a184", "a185", "a186", "a187", "a188", "a189", "a190", "a191", "a192", "a193", "a194", "a195", "a196", "a197", "a198", "a199", "a200", "a201", "a202", "a203", "a204", "a205", "a206", "a207", "a208", "a209", "a210", "a211", "a212", "a213", "a214", "a215", "a216", "a217", "a218", "a219", "a220", "a221", "a222", "a223", "a224", "a225", "a226", "a227", "a228", "a229", "a230", "a231", "s20", "s21", "s22", "s23", "s24", "s25", "s26", "s27", "s28", "s29", "s30", "s31", "s60", "s61", "s62", "s63", "s64", "s65", "s66", "s67", "s68", "s69", "s70", "s71", "s72", "s73", "s74", "s75", "s76", "s77", "s78", "s79", "s80", "s81", "s82", "s83", "s84", "s85", "s86", "s87", "s88", "s89", "s90", "s91", "s93", "s94", "s95", "s98", "s99", "s100", "s101", "vcc", "scc", "memory");
}

}

extern "C" void kernel_launch(void* const* d_in, const int* in_sizes, int n_in, void* d_out, int out_size,
                              void* d_ws, size_t ws_size, hipStream_t stream) {
  if (n_in != 6 || ws_size < WS_END) return;
  const __hip_bfloat16* q = (const __hip_bfloat16*)d_in[0];
  const __hip_bfloat16* kc = (const __hip_bfloat16*)d_in[1];
  const __hip_bfloat16* vc = (const __hip_bfloat16*)d_in[2];
  const __hip_bfloat16* kt = (const __hip_bfloat16*)d_in[3];
  const __hip_bfloat16* vt = (const __hip_bfloat16*)d_in[4];
  const __hip_bfloat16* tm = (const __hip_bfloat16*)d_in[5];
  hipLaunchKernelGGL(tree_attn_main, dim3(B_ * HKV_ * 2), dim3(256), 0, stream, q, kc, vc, kt, vt, tm,
                     (__hip_bfloat16*)d_out, (char*)d_ws);
}
```

```cpp
#include <hip/hip_runtime.h>
#include <hip/hip_bf16.h>
#include <stdint.h>

#pragma clang diagnostic ignored "-Wint-to-pointer-cast"
#pragma clang diagnostic ignored "-Winline-asm"

namespace {

constexpr int B_ = 16, HQ_ = 32, HKV_ = 8, T_ = 32, C_ = 4096, D_ = 128;
constexpr int TILE = 64;
constexpr int KIMG = TILE * D_ * 2;
constexpr int STAGE_BYTES = 2 * KIMG;
constexpr int TREE_OFF = 4 * STAGE_BYTES;
constexpr int Q_OFF = 3 * STAGE_BYTES;
constexpr int LDS_BYTES = TREE_OFF + 2 * 13312;
constexpr unsigned C2 = 311, WIN = 52;

constexpr size_t WS_EPOCH = 0;
constexpr size_t WS_REF = WS_EPOCH + 128 * 128;
constexpr size_t WS_X = WS_REF + 16384, WS_X1 = 4 << 20;
constexpr size_t XW_BYTES = 4096 + 1024;
constexpr size_t WS_END = 0x680000 + WS_X;
static_assert(WS_X1 + WS_X + 128 * 4 * XW_BYTES <= WS_END, "");

typedef unsigned long long u64x8 __attribute__((ext_vector_type(8)));
typedef unsigned u32x32 __attribute__((ext_vector_type(32)));
typedef unsigned u32x8  __attribute__((ext_vector_type(8)));
typedef unsigned u32x4  __attribute__((ext_vector_type(4)));
typedef unsigned u32x2  __attribute__((ext_vector_type(2)));
typedef __attribute__((address_space(1))) unsigned long long gu64;

__global__ __launch_bounds__(256, 1)
void tree_attn_main(const __hip_bfloat16*, const __hip_bfloat16*, const __hip_bfloat16*, const __hip_bfloat16*,
                    const __hip_bfloat16*, const __hip_bfloat16*, __hip_bfloat16*, char*) {
  __shared__ __attribute__((aligned(1024))) char smem[LDS_BYTES];
  unsigned blk = blockIdx.x;
  u64x8 ka; u32x32 qq; unsigned pre, tch, voffK; int tid = threadIdx.x;
  asm volatile(
      "s_load_dwordx16 s[4:19], %[kp], 0x0\n"
      "s_getpc_b64 s[84:85]\n"
      "v_lshlrev_b32 v112, 4, %[tid]\n"
      "s_lshl_b32 s69, %[blk], 7\n"
      "s_and_b32 s69, s69, 0x7c00\n"
      "v_lshl_add_u32 v120, %[tid], 2, s69\n"
      "global_load_dword v128, v120, s[84:85]\n"
      "v_readfirstlane_b32 s77, %[tid]\n"
      "v_lshrrev_b32 %[vk], 4, %[tid]\n"
      "s_and_b32 s78, %[blk], 7\n"
      "v_xor_b32 %[vk], %[vk], %[tid]\n"
      "s_lshr_b32 s74, %[blk], 4\n"
      "v_and_b32 %[vk], 7, %[vk]\n"
      "s_lshl4_add_u32 s78, s78, s74\n"
      "v_lshrrev_b32 v120, 3, %[tid]\n"
      "s_lshl_b32 s78, s78, 15\n"
      "v_lshlrev_b32 %[vk], 4, %[vk]\n"
      "s_lshl_b32 s77, s77, 5\n"
      "v_lshl_or_b32 %[vk], v120, 8, %[vk]\n"
      "s_bfm_b32 s66, 1, 22\n"
      "s_bfm_b32 s67, 1, 17\n"
      "v_add_u32 v120, 0x2000, %[vk]\n"
      "s_add_u32 s74, s78, 0x4000\n"
      "s_lshl_b32 s76, s78, 5\n"
      "s_add_u32 m0, s77, 0x20000\n"
      "s_mov_b32 s86, 0x100000\n"
      "s_mov_b32 s87, 0x20000\n"
      "s_lshr_b32 s75, s78, 2\n"
      "v_mov_b64 v[116:117], 0\n"
      "v_mov_b64 v[118:119], 0\n"
      "s_lshl_b32 s69, %[blk], 10\n"
      "s_andn2_b32 s69, 0x2000, s69\n"
      "v_add_u32 v113, s69, v112\n"
      "ds_write_b128 v112, v[116:119] offset:16384\n"
      "ds_write_b128 v112, v[116:119] offset:20480\n"
      "ds_write_b128 v112, v[116:119] offset:24576\n"
      "ds_write_b128 v112, v[116:119] offset:28672\n"
      "s_waitcnt lgkmcnt(0)\n"
      "s_mov_b32 s64, s4\n"
      "s_and_b32 s65, s5, 0xffff\n"
      "buffer_load_dwordx4 v[160:163], %[vk], s[64:67], s78 offen nt\n"
      "buffer_load_dwordx4 v[164:167], %[vk], s[64:67], s78 offen offset:128 nt\n"
      "buffer_load_dwordx4 v[168:171], v120, s[64:67], s78 offen nt\n"
      "buffer_load_dwordx4 v[172:175], v120, s[64:67], s78 offen offset:128 nt\n"
      "s_load_dword s92, s[8:9], s76\n"
      "buffer_load_dwordx4 v[176:179], %[vk], s[64:67], s74 offen nt\n"
      "buffer_load_dwordx4 v[180:183], %[vk], s[64:67], s74 offen offset:128 nt\n"
      "buffer_load_dwordx4 v[184:187], v120, s[64:67], s74 offen nt\n"
      "buffer_load_dwordx4 v[188:191], v120, s[64:67], s74 offen offset:128 nt\n"
      "s_mov_b32 s84, s10\n"
      "s_and_b32 s85, s11, 0xffff\n"
      "s_lshr_b32 s76, s78, 18\n"
      "s_lshl_b32 s76, s76, 11\n"
      "s_lshr_b32 s69, s77, 1\n"
      "buffer_load_dwordx4 %[vk], s[84:87], s75 offen nt lds\n"
      "s_add_u32 m0, s77, 131968\n"
      "s_cmp_gt_u32 s77, 2048\n"
      "buffer_load_dwordx4 %[vk], s[84:87], s75 offen offset:128 nt lds\n"
      "s_cbranch_scc1 NOMASK_%=\n"
      "s_add_u32 m0, s69, 140288\n"
      "s_mov_b32 s64, s14\n"
      "s_and_b32 s65, s15, 0xffff\n"
      "s_mov_b32 s66, 0x8000\n"
      "buffer_load_dwordx4 v112, s[64:67], s76 offen lds\n"
      "NOMASK_%=:\n"
      "ds_write_b128 v113, v[116:119] offset:49152\n"
      "ds_write_b128 v113, v[116:119] offset:53248\n"
      "s_mov_b32 s64, s6\n"
      "s_and_b32 s65, s7, 0xffff\n"
      "s_bfm_b32 s66, 1, 27\n"
      "s_lshl_b32 s76, s78, 5\n"
      "s_bfe_u32 s74, %[blk], 0x10003\n"
      "s_lshl_b32 s74, s74, 15\n"
      "s_add_u32 s76, s76, s74\n"
      "s_bfe_u32 s74, %[blk], 0x10004\n"
      "s_lshl_b32 s74, s74, 14\n"
      "s_add_u32 s76, s76, s74\n"
      "s_add_u32 m0, s77, 0\n"
      "s_nop 0\n"
      "buffer_load_dwordx4 %[vk], s[64:67], s76 offen nt lds\n"
      "s_add_u32 m0, s77, 896\n"
      "s_nop 0\n"
      "buffer_load_dwordx4 %[vk], s[64:67], s76 offen offset:128 nt lds\n"
      "s_add_u32 m0, s77, 8192\n"
      "s_nop 0\n"
      "buffer_load_dwordx4 v120, s[64:67], s76 offen nt lds\n"
      : "=&{s[4:19]}"(ka), "=&{v128}"(pre), "=&{v[160:191]}"(qq), "=&{s92}"(tch), [vk] "=&v"(voffK), [tid] "+v"(tid)
      : [kp] "s"(__builtin_amdgcn_kernarg_segment_ptr()), [blk] "s"(blk)
      : "v112", "v113", "v116", "v117", "v118", "v119", "v120", "s64", "s65", "s66", "s67", "s69", "s70", "s71", "s74", "s75", "s76", "s77", "s78", "s84", "s85", "s86", "s87", "s96", "s97", "scc", "memory");
  asm volatile("" : "+s"(blk));
  const int lane = tid & 63;
  const int wid = __builtin_amdgcn_readfirstlane(tid >> 6);
  const unsigned wbase = wid * 2048;
  const int rh = wid & 1, kh = wid >> 1;
  const int xg = blk & 7, jj = blk >> 3, bk = xg * 16 + (jj >> 1), split = jj & 1;
  const int c16 = lane & 15, g = lane >> 4;
  const unsigned lds0 = (unsigned)(uintptr_t)smem;

  const unsigned voffV = (unsigned)((8 * wid + (lane >> 3)) * 256 + (((lane & 7) ^ (g << 1)) << 4));
  const unsigned so0 = (unsigned)(bk * C_ + split * 2 * TILE) * (D_ * 2) + (bk & 1) * KIMG, inc = KIMG - 2 * (bk & 1) * KIMG;
  u32x8 ad;
#pragma unroll
  for (int j = 0; j < 2; ++j)
    ad[j] = lds0 + (unsigned)(2048 * (c16 >> 3) + 128 * (c16 & 7) + (((4 * j + g) ^ (c16 >> 1)) << 4) + kh * 8192);
  {
    const int q4 = c16 >> 2, p = lane & 3;
#pragma unroll
    for (int j = 0; j < 4; ++j)
      ad[2 + j] = lds0 + (unsigned)(2048 * (g >> 1) + 128 * (4 * (g & 1) + q4) + kh * 8192 + KIMG +
                                    (((2 * (j ^ (2 * kh)) + (p >> 1)) ^ ((2 * (g & 1) + (q4 >> 1)) << 1)) << 4) + 8 * (p & 1));
  }
  ad[6] = voffK; ad[7] = voffV + 128 * split;
  u32x2 ra = {(unsigned)(lane < 4 ? WS_REF + lane * 512 : 1 << 26), (unsigned)(WS_REF + (jj & (4 - 1)) * 512 + (lane & 7) * 4 + (wid << 26))};
  auto xarea = [&](int w) { return lds0 + ((0x8d802000u >> (8 * w)) & 255u) * 1024; };
  const unsigned pg = jj >> 2, px = pg - (pg > 160 / 32) + xg * 5;
  const unsigned n1 = 4 + max(px - 6 * (px * 43 >> 8), ((0x70000000u | 6u << 160 / 8) >> 4 * pg) & 7);
  asm volatile(
      "s_memrealtime s[82:83]\n"
      "s_mov_b32 s60, %[so0]\n"
      "s_mov_b32 s62, %[wb]\n"
      "v_add_u32 v120, 0x2000, v248\n"
      "s_and_b32 s80, %[fo], 0xffffff80\n"
      "v_mov_b32 v240, s80\n"
      "s_mov_b32 s22, 0x8000000\n"
      "s_mov_b32 s23, 0x20000\n"
      "s_mov_b32 s20, s6\n"
      "s_and_b32 s21, s7, 0xffff\n"
      "s_nop 0\n"
      "v_mov_b64 v[216:217], 0\n"
      "v_mov_b64 v[218:219], 0\n"
      "s_mov_b32 s86, 0x100000\n"
      "s_mov_b32 s87, 0x20000\n"
      "v_mfma_f32_32x32x16_bf16 v[32:47], v[216:219], v[216:219], 0\n"
      "s_lshr_b32 s75, %[qo], 2\n"
      "s_mov_b32 s84, s12\n"
      "s_and_b32 s85, s13, 0xffff\n"
      "v_mfma_f32_32x32x16_bf16 v[48:63], v[216:219], v[216:219], 0\n"
      "s_nop 0\n"
      "v_xor_b32 v121, 0x80, v249\n"
      "s_sub_u32 s78, 0x10000, %[inc]\n"
      "s_mov_b32 s94, 0x3e0293ee\n"
      "v_mfma_f32_32x32x16_bf16 v[64:79], v[216:219], v[216:219], 0\n"
      "s_mov_b32 s95, 0x3e0293ee\n"
      "s_mov_b32 s74, 0xffff0000\n"
      "s_mov_b32 s76, 0xceabfb8f\n"
      "v_mfma_f32_32x32x16_bf16 v[80:95], v[216:219], v[216:219], 0\n"
      "v_mbcnt_lo_u32_b32 v112, -1, 0\n"
      "s_nop 0\n"
      "v_mbcnt_hi_u32_b32 v112, -1, v112\n"
      "s_add_u32 s69, %[wb], 0x18000\n"
      "v_mfma_f32_32x32x16_bf16 v[192:207], v[216:219], v[216:219], 0\n"
      "v_lshl_add_u32 v226, v112, 4, s69\n"
      "v_lshrrev_b32 v115, 4, v112\n"
      "v_and_b32 v112, 15, v112\n"
      "v_mfma_f32_32x32x16_bf16 a[128:143], v[216:219], v[216:219], 0\n"
      "v_lshlrev_b32 v115, 3, v115\n"
      "v_lshl_add_u32 v115, v112, 6, v115\n"
      "s_add_u32 m0, s62, 9088\n"
      "s_nop 0\n"
      "buffer_load_dwordx4 v120, s[20:23], s60 offen offset:128 nt lds\n"
      "v_add_u32 v115, 140288, v115\n"
      "v_mfma_f32_32x32x16_bf16 a[144:159], v[216:219], v[216:219], 0\n"
      "v_add_u32 v118, %[qoff], v242\n"
      "v_add_u32 v224, %[toff], v242\n"
      "v_add_u32 v119, %[qoff], v243\n"
      "v_mfma_f32_32x32x16_bf16 a[160:175], v[216:219], v[216:219], 0\n"
      "v_add_u32 v225, %[toff], v243\n"
      "v_mfma_f32_32x32x16_bf16 a[176:191], v[216:219], v[216:219], 0\n"
      "s_add_u32 s62, s62, 0x8000\n"
      "s_and_b32 s62, s62, 0x1ffff\n"
      "s_add_u32 s60, s60, %[inc]\n"
      "s_waitcnt vmcnt(13)\n"
      "v_lshlrev_b32 v232, 16, v160\n"
      "v_and_b32 v233, s74, v160\n"
      "v_lshlrev_b32 v234, 16, v161\n"
      "v_and_b32 v235, s74, v161\n"
      "v_lshlrev_b32 v236, 16, v162\n"
      "v_and_b32 v237, s74, v162\n"
      "v_lshlrev_b32 v238, 16, v163\n"
      "v_and_b32 v239, s74, v163\n"
      "v_pk_mul_f32 v[232:233], v[232:233], s[94:95]\n"
      "v_pk_mul_f32 v[234:235], v[234:235], s[94:95]\n"
      "v_pk_mul_f32 v[236:237], v[236:237], s[94:95]\n"
      "v_pk_mul_f32 v[238:239], v[238:239], s[94:95]\n"
      "v_cvt_pk_bf16_f32 v160, v232, v233\n"
      "v_cvt_pk_bf16_f32 v161, v234, v235\n"
      "v_cvt_pk_bf16_f32 v162, v236, v237\n"
      "v_cvt_pk_bf16_f32 v163, v238, v239\n"
      "ds_write_b128 v226, v[160:163] offset:0\n"
      "s_waitcnt vmcnt(12)\n"
      "v_lshlrev_b32 v232, 16, v164\n"
      "v_and_b32 v233, s74, v164\n"
      "v_lshlrev_b32 v234, 16, v165\n"
      "v_and_b32 v235, s74, v165\n"
      "v_lshlrev_b32 v236, 16, v166\n"
      "v_and_b32 v237, s74, v166\n"
      "v_lshlrev_b32 v238, 16, v167\n"
      "v_and_b32 v239, s74, v167\n"
      "v_pk_mul_f32 v[232:233], v[232:233], s[94:95]\n"
      "v_pk_mul_f32 v[234:235], v[234:235], s[94:95]\n"
      "v_pk_mul_f32 v[236:237], v[236:237], s[94:95]\n"
      "v_pk_mul_f32 v[238:239], v[238:239], s[94:95]\n"
      "v_cvt_pk_bf16_f32 v164, v232, v233\n"
      "v_cvt_pk_bf16_f32 v165, v234, v235\n"
      "v_cvt_pk_bf16_f32 v166, v236, v237\n"
      "v_cvt_pk_bf16_f32 v167, v238, v239\n"
      "ds_write_b128 v226, v[164:167] offset:1024\n"
      "s_waitcnt vmcnt(11)\n"
      "v_lshlrev_b32 v232, 16, v168\n"
      "v_and_b32 v233, s74, v168\n"
      "v_lshlrev_b32 v234, 16, v169\n"
      "v_and_b32 v235, s74, v169\n"
      "v_lshlrev_b32 v236, 16, v170\n"
      "v_and_b32 v237, s74, v170\n"
      "v_lshlrev_b32 v238, 16, v171\n"
      "v_and_b32 v239, s74, v171\n"
      "v_pk_mul_f32 v[232:233], v[232:233], s[94:95]\n"
      "v_pk_mul_f32 v[234:235], v[234:235], s[94:95]\n"
      "v_pk_mul_f32 v[236:237], v[236:237], s[94:95]\n"
      "v_pk_mul_f32 v[238:239], v[238:239], s[94:95]\n"
      "v_cvt_pk_bf16_f32 v168, v232, v233\n"
      "v_cvt_pk_bf16_f32 v169, v234, v235\n"
      "v_cvt_pk_bf16_f32 v170, v236, v237\n"
      "v_cvt_pk_bf16_f32 v171, v238, v239\n"
      "ds_write_b128 v226, v[168:171] offset:8192\n"
      "s_add_u32 m0, s62, 0\n"
      "s_nop 0\n"
      "buffer_load_dwordx4 v248, s[20:23], s60 offen nt lds\n"
      "s_waitcnt vmcnt(11)\n"
      "v_lshlrev_b32 v232, 16, v172\n"
      "v_and_b32 v233, s74, v172\n"
      "v_lshlrev_b32 v234, 16, v173\n"
      "v_and_b32 v235, s74, v173\n"
      "v_lshlrev_b32 v236, 16, v174\n"
      "v_and_b32 v237, s74, v174\n"
      "v_lshlrev_b32 v238, 16, v175\n"
      "v_and_b32 v239, s74, v175\n"
      "v_pk_mul_f32 v[232:233], v[232:233], s[94:95]\n"
      "v_pk_mul_f32 v[234:235], v[234:235], s[94:95]\n"
      "v_pk_mul_f32 v[236:237], v[236:237], s[94:95]\n"
      "v_pk_mul_f32 v[238:239], v[238:239], s[94:95]\n"
      "v_cvt_pk_bf16_f32 v172, v232, v233\n"
      "v_cvt_pk_bf16_f32 v173, v234, v235\n"
      "v_cvt_pk_bf16_f32 v174, v236, v237\n"
      "v_cvt_pk_bf16_f32 v175, v238, v239\n"
      "ds_write_b128 v226, v[172:175] offset:9216\n"
      "s_add_u32 m0, s62, 896\n"
      "s_nop 0\n"
      "buffer_load_dwordx4 v248, s[20:23], s60 offen offset:128 nt lds\n"
      "s_waitcnt vmcnt(11)\n"
      "v_lshlrev_b32 v232, 16, v176\n"
      "v_and_b32 v233, s74, v176\n"
      "v_lshlrev_b32 v234, 16, v177\n"
      "v_and_b32 v235, s74, v177\n"
      "v_lshlrev_b32 v236, 16, v178\n"
      "v_and_b32 v237, s74, v178\n"
      "v_lshlrev_b32 v238, 16, v179\n"
      "v_and_b32 v239, s74, v179\n"
      "v_pk_mul_f32 v[232:233], v[232:233], s[94:95]\n"
      "v_pk_mul_f32 v[234:235], v[234:235], s[94:95]\n"
      "v_pk_mul_f32 v[236:237], v[236:237], s[94:95]\n"
      "v_pk_mul_f32 v[238:239], v[238:239], s[94:95]\n"
      "v_cvt_pk_bf16_f32 v176, v232, v233\n"
      "v_cvt_pk_bf16_f32 v177, v234, v235\n"
      "v_cvt_pk_bf16_f32 v178, v236, v237\n"
      "v_cvt_pk_bf16_f32 v179, v238, v239\n"
      "ds_write_b128 v226, v[176:179] offset:16384\n"
      "s_add_u32 m0, s62, 8192\n"
      "s_nop 0\n"
      "buffer_load_dwordx4 v120, s[20:23], s60 offen nt lds\n"
      "s_waitcnt vmcnt(11)\n"
      "v_lshlrev_b32 v232, 16, v180\n"
      "v_and_b32 v233, s74, v180\n"
      "v_lshlrev_b32 v234, 16, v181\n"
      "v_and_b32 v235, s74, v181\n"
      "v_lshlrev_b32 v236, 16, v182\n"
      "v_and_b32 v237, s74, v182\n"
      "v_lshlrev_b32 v238, 16, v183\n"
      "v_and_b32 v239, s74, v183\n"
      "v_pk_mul_f32 v[232:233], v[232:233], s[94:95]\n"
      "v_pk_mul_f32 v[234:235], v[234:235], s[94:95]\n"
      "v_pk_mul_f32 v[236:237], v[236:237], s[94:95]\n"
      "v_pk_mul_f32 v[238:239], v[238:239], s[94:95]\n"
      "v_cvt_pk_bf16_f32 v180, v232, v233\n"
      "v_cvt_pk_bf16_f32 v181, v234, v235\n"
      "v_cvt_pk_bf16_f32 v182, v236, v237\n"
      "v_cvt_pk_bf16_f32 v183, v238, v239\n"
      "ds_write_b128 v226, v[180:183] offset:17408\n"
      "s_add_u32 m0, s62, 9088\n"
      "s_nop 0\n"
      "buffer_load_dwordx4 v120, s[20:23], s60 offen offset:128 nt lds\n"
      "s_waitcnt vmcnt(11)\n"
      "v_lshlrev_b32 v232, 16, v184\n"
      "v_and_b32 v233, s74, v184\n"
      "v_lshlrev_b32 v234, 16, v185\n"
      "v_and_b32 v235, s74, v185\n"
      "v_lshlrev_b32 v236, 16, v186\n"
      "v_and_b32 v237, s74, v186\n"
      "v_lshlrev_b32 v238, 16, v187\n"
      "v_and_b32 v239, s74, v187\n"
      "v_pk_mul_f32 v[232:233], v[232:233], s[94:95]\n"
      "v_pk_mul_f32 v[234:235], v[234:235], s[94:95]\n"
      "v_pk_mul_f32 v[236:237], v[236:237], s[94:95]\n"
      "v_pk_mul_f32 v[238:239], v[238:239], s[94:95]\n"
      "v_cvt_pk_bf16_f32 v184, v232, v233\n"
      "v_cvt_pk_bf16_f32 v185, v234, v235\n"
      "v_cvt_pk_bf16_f32 v186, v236, v237\n"
      "v_cvt_pk_bf16_f32 v187, v238, v239\n"
      "ds_write_b128 v226, v[184:187] offset:24576\n"
      "s_add_u32 m0, %[tv], 0\n"
      "s_nop 0\n"
      "buffer_load_dwordx4 v249, s[84:87], s75 offen nt lds\n"
      "s_add_u32 m0, %[tv], 1024\n"
      "s_nop 0\n"
      "buffer_load_dwordx4 v121, s[84:87], s75 offen nt lds\n"
      "s_waitcnt vmcnt(12)\n"
      "v_lshlrev_b32 v232, 16, v188\n"
      "v_and_b32 v233, s74, v188\n"
      "v_lshlrev_b32 v234, 16, v189\n"
      "v_and_b32 v235, s74, v189\n"
      "v_lshlrev_b32 v236, 16, v190\n"
      "v_and_b32 v237, s74, v190\n"
      "v_lshlrev_b32 v238, 16, v191\n"
      "v_and_b32 v239, s74, v191\n"
      "v_pk_mul_f32 v[232:233], v[232:233], s[94:95]\n"
      "v_pk_mul_f32 v[234:235], v[234:235], s[94:95]\n"
      "v_pk_mul_f32 v[236:237], v[236:237], s[94:95]\n"
      "v_pk_mul_f32 v[238:239], v[238:239], s[94:95]\n"
      "v_cvt_pk_bf16_f32 v188, v232, v233\n"
      "v_cvt_pk_bf16_f32 v189, v234, v235\n"
      "v_cvt_pk_bf16_f32 v190, v236, v237\n"
      "v_cvt_pk_bf16_f32 v191, v238, v239\n"
      "ds_write_b128 v226, v[188:191] offset:25600\n"
      "s_add_u32 s62, s62, 0x8000\n"
      "s_and_b32 s62, s62, 0x1ffff\n"
      "s_add_u32 s60, s60, s78\n"
      "s_waitcnt vmcnt(10) lgkmcnt(0)\n"
      "s_barrier\n"
      "s_memrealtime s[68:69]\n"
      "ds_read_b64 v[208:209], v115 offset:0\n"
      "ds_read_b64 v[210:211], v115 offset:32\n"
      "ds_read_b64 v[212:213], v115 offset:1024\n"
      "ds_read_b64 v[214:215], v115 offset:1056\n"
      "ds_read_b128 a[96:99], v224 offset:0\n"
      "ds_read_b128 a[100:103], v224 offset:4096\n"
      "ds_read_b128 a[0:3], v118 offset:0\n"
      "ds_read_b128 a[4:7], v118 offset:4096\n"
      "ds_read_b128 a[8:11], v118 offset:8192\n"
      "ds_read_b128 a[12:15], v118 offset:12288\n"
      "ds_read_b128 a[104:107], v225 offset:0\n"
      "ds_read_b128 a[108:111], v225 offset:4096\n"
      "global_load_dwordx2 v[240:241], v240, s[18:19] sc1\n"
      "s_mov_b32 s28, s18\n"
      "s_and_b32 s29, s19, 0xffff\n"
      "s_mov_b32 s30, 0x688000\n"
      "s_mov_b32 s31, 0x20000\n"
      "s_cmp_lt_u32 %[rep], 128\n"
      "s_cselect_b32 s89, 0x2ffe, 0\n"
      "s_or_b32 s89, s89, 0x2aae\n"
      "s_waitcnt lgkmcnt(8)\n"
      "ds_read_b128 a[16:19], v119 offset:0\n"
      "ds_read_b128 a[20:23], v119 offset:4096\n"
      "ds_read_b128 a[24:27], v119 offset:8192\n"
      "ds_read_b128 a[28:31], v119 offset:12288\n"
      "v_lshlrev_b32 v96, 16, v208\n"
      "v_and_b32 v97, s74, v208\n"
      "v_lshlrev_b32 v98, 16, v209\n"
      "v_and_b32 v99, s74, v209\n"
      "v_lshlrev_b32 v100, 16, v210\n"
      "v_and_b32 v101, s74, v210\n"
      "v_lshlrev_b32 v102, 16, v211\n"
      "v_and_b32 v103, s74, v211\n"
      "v_lshlrev_b32 v104, 16, v212\n"
      "v_and_b32 v105, s74, v212\n"
      "v_lshlrev_b32 v106, 16, v213\n"
      "v_and_b32 v107, s74, v213\n"
      "v_lshlrev_b32 v108, 16, v214\n"
      "v_and_b32 v109, s74, v214\n"
      "v_lshlrev_b32 v110, 16, v215\n"
      "v_and_b32 v111, s74, v215\n"
      "v_fma_f32 v96, -v96, s76, s76\n"
      "v_fma_f32 v97, -v97, s76, s76\n"
      "v_fma_f32 v98, -v98, s76, s76\n"
      "v_fma_f32 v99, -v99, s76, s76\n"
      "v_fma_f32 v100, -v100, s76, s76\n"
      "v_fma_f32 v101, -v101, s76, s76\n"
      "v_fma_f32 v102, -v102, s76, s76\n"
      "v_fma_f32 v103, -v103, s76, s76\n"
      "v_fma_f32 v104, -v104, s76, s76\n"
      "v_fma_f32 v105, -v105, s76, s76\n"
      "v_fma_f32 v106, -v106, s76, s76\n"
      "v_fma_f32 v107, -v107, s76, s76\n"
      "v_fma_f32 v108, -v108, s76, s76\n"
      "v_fma_f32 v109, -v109, s76, s76\n"
      "v_fma_f32 v110, -v110, s76, s76\n"
      "v_fma_f32 v111, -v111, s76, s76\n"
      "s_waitcnt lgkmcnt(0)\n"
      "ds_read_b128 a[112:115], v224 offset:1024\n"
      "ds_read_b128 a[116:119], v224 offset:5120\n"
      "ds_read_b128 a[32:35], v118 offset:1024\n"
      "ds_read_b128 a[36:39], v118 offset:5120\n"
      "ds_read_b128 a[40:43], v118 offset:9216\n"
      "ds_read_b128 a[44:47], v118 offset:13312\n"
      "ds_read_b128 a[120:123], v225 offset:1024\n"
      "ds_read_b128 a[124:127], v225 offset:5120\n"
      "ds_read_b128 a[48:51], v119 offset:1024\n"
      "ds_read_b128 a[52:55], v119 offset:5120\n"
      "ds_read_b128 a[56:59], v119 offset:9216\n"
      "ds_read_b128 a[60:63], v119 offset:13312\n"
      "s_cmp_lt_u32 %[lead], 32\n"
      "s_cbranch_scc0 NOLEAD_%=\n"
      "v_mov_b32 v115, s68\n"
      "buffer_store_dword v115, v250, s[28:31], %[lead] offen sc1\n"
      "NOLEAD_%=:\n"
      "v_mfma_f32_16x16x32_bf16 v[160:163], a[96:99], a[0:3], v[96:99]\n"
      "s_mov_b32 s83, 0\n"
      "v_add_u32 v122, 0x2000, v249\n"
      "v_add_u32 v123, 0x2000, v121\n"
      "v_mfma_f32_16x16x32_bf16 v[168:171], a[96:99], a[4:7], v[104:107]\n"
      "s_add_u32 m0, s62, 0\n"
      "s_sub_u32 s67, s68, s82\n"
      "v_mov_b32 v113, 0x3f803f80\n"
      "v_accvgpr_write_b32 a192, 0\n"
      "v_mfma_f32_16x16x32_bf16 v[176:179], a[96:99], a[8:11], v[96:99]\n"
      "s_min_u32 s67, s67, 140\n"
      "buffer_load_dwordx4 v248, s[20:23], s60 offen nt lds\n"
      "v_cmp_eq_u32 vcc, 0, v112\n"
      "v_mfma_f32_16x16x32_bf16 v[184:187], a[96:99], a[12:15], v[104:107]\n"
      "s_add_u32 m0, s62, 896\n"
      "s_add_u32 s66, s82, s67\n"
      "v_mfma_f32_16x16x32_bf16 v[164:167], a[100:103], a[0:3], v[100:103]\n"
      "s_add_u32 s66, s66, 78\n"
      "buffer_load_dwordx4 v248, s[20:23], s60 offen offset:128 nt lds\n"
      "v_cndmask_b32 v114, 0, v113, vcc\n"
      "v_mfma_f32_16x16x32_bf16 v[172:175], a[100:103], a[4:7], v[108:111]\n"
      "s_add_u32 m0, s62, 8192\n"
      "s_sub_u32 s71, s68, 400\n"
      "v_accvgpr_write_b32 a196, v114\n"
      "v_accvgpr_write_b32 a197, v114\n"
      "v_mfma_f32_16x16x32_bf16 v[180:183], a[100:103], a[8:11], v[100:103]\n"
      "s_mov_b32 s68, s66\n"
      "buffer_load_dwordx4 v120, s[20:23], s60 offen nt lds\n"
      "v_accvgpr_write_b32 a198, v114\n"
      "v_accvgpr_write_b32 a199, v114\n"
      "v_mfma_f32_16x16x32_bf16 v[188:191], a[100:103], a[12:15], v[108:111]\n"
      "s_add_u32 m0, s62, 9088\n"
      "s_mov_b32 s82, s66\n"
      "v_cmp_eq_u32 vcc, 4, v112\n"
      "v_mfma_f32_16x16x32_bf16 v[160:163], a[104:107], a[16:19], v[160:163]\n"
      "s_add_u32 s62, s62, 0x8000\n"
      "buffer_load_dwordx4 v120, s[20:23], s60 offen offset:128 nt lds\n"
      "s_mov_b32 s61, %[so0]\n"
      "v_mfma_f32_16x16x32_bf16 v[168:171], a[104:107], a[20:23], v[168:171]\n"
      "s_and_b32 s62, s62, 0x1ffff\n"
      "s_add_u32 s77, %[wb], 0x10000\n"
      "v_cndmask_b32 v114, 0, v113, vcc\n"
      "v_mfma_f32_16x16x32_bf16 v[176:179], a[104:107], a[24:27], v[176:179]\n"
      "s_add_u32 s60, s60, %[inc]\n"
      "s_mov_b32 s70, 300\n"
      "v_accvgpr_write_b32 a200, v114\n"
      "v_accvgpr_write_b32 a201, v114\n"
      "v_mfma_f32_16x16x32_bf16 v[184:187], a[104:107], a[28:31], v[184:187]\n"
      "s_mov_b32 s80, 0x12a\n"
      "v_accvgpr_write_b32 a202, v114\n"
      "v_accvgpr_write_b32 a203, v114\n"
      "v_mfma_f32_16x16x32_bf16 v[164:167], a[108:111], a[16:19], v[164:167]\n"
      "s_mov_b32 s81, 0x0\n"
      "v_cmp_eq_u32 vcc, 8, v112\n"
      "v_mfma_f32_16x16x32_bf16 v[172:175], a[108:111], a[20:23], v[172:175]\n"
      "s_mov_b32 s101, 1000\n"
      "v_mfma_f32_16x16x32_bf16 v[180:183], a[108:111], a[24:27], v[180:183]\n"
      "s_mov_b32 s98, 110\n"
      "v_cndmask_b32 v114, 0, v113, vcc\n"
      "v_mfma_f32_16x16x32_bf16 v[188:191], a[108:111], a[28:31], v[188:191]\n"
      "s_waitcnt lgkmcnt(0)\n"
      "s_mov_b32 s93, 28\n"
      "v_accvgpr_write_b32 a204, v114\n"
      "v_accvgpr_write_b32 a205, v114\n"
      "v_mfma_f32_16x16x32_bf16 v[160:163], a[112:115], a[32:35], v[160:163]\n"
      "s_mov_b32 s99, 27\n"
      "v_accvgpr_write_b32 a206, v114\n"
      "v_accvgpr_write_b32 a207, v114\n"
      "v_mfma_f32_16x16x32_bf16 v[168:171], a[112:115], a[36:39], v[168:171]\n"
      "s_mov_b32 s100, 285\n"
      "v_cmp_eq_u32 vcc, 12, v112\n"
      "v_mfma_f32_16x16x32_bf16 v[176:179], a[112:115], a[40:43], v[176:179]\n"
      "s_lshl_b32 s91, %[wb], 18\n"
      "v_mfma_f32_16x16x32_bf16 v[184:187], a[112:115], a[44:47], v[184:187]\n"
      "s_sub_u32 s91, 0x10000000, s91\n"
      "v_cndmask_b32 v114, 0, v113, vcc\n"
      "v_mfma_f32_16x16x32_bf16 v[164:167], a[116:119], a[32:35], v[164:167]\n"
      "s_mov_b32 s86, 110\n"
      "v_accvgpr_write_b32 a208, v114\n"
      "v_accvgpr_write_b32 a209, v114\n"
      "v_mfma_f32_16x16x32_bf16 v[172:175], a[116:119], a[36:39], v[172:175]\n"
      "s_mov_b32 s87, 0\n"
      "v_accvgpr_write_b32 a210, v114\n"
      "v_accvgpr_write_b32 a211, v114\n"
      "s_waitcnt vmcnt(11)\n"
      "s_barrier\n"
      "v_mfma_f32_16x16x32_bf16 v[180:183], a[116:119], a[40:43], v[180:183]\n"
      "s_mov_b32 s26, 0x8000000\n"
      "ds_read_b128 a[64:67], v242 offset:0\n"
      "v_mfma_f32_16x16x32_bf16 v[188:191], a[116:119], a[44:47], v[188:191]\n"
      "s_mov_b32 s27, 0x20000\n"
      "ds_read_b128 a[68:71], v242 offset:4096\n"
      "v_mfma_f32_16x16x32_bf16 v[160:163], a[120:123], a[48:51], v[160:163]\n"
      "s_mov_b32 s24, s8\n"
      "ds_read_b128 a[72:75], v243 offset:0\n"
      "v_mfma_f32_16x16x32_bf16 v[168:171], a[120:123], a[52:55], v[168:171]\n"
      "s_and_b32 s25, s9, 0xffff\n"
      "ds_read_b128 a[76:79], v243 offset:4096\n"
      "v_mfma_f32_16x16x32_bf16 v[176:179], a[120:123], a[56:59], v[176:179]\n"
      "ds_read_b128 a[80:83], v242 offset:1024\n"
      "v_mfma_f32_16x16x32_bf16 v[184:187], a[120:123], a[60:63], v[184:187]\n"
      "ds_read_b128 a[84:87], v242 offset:5120\n"
      "v_mfma_f32_16x16x32_bf16 v[164:167], a[124:127], a[48:51], v[164:167]\n"
      "ds_read_b128 a[88:91], v243 offset:1024\n"
      "v_mfma_f32_16x16x32_bf16 v[172:175], a[124:127], a[52:55], v[172:175]\n"
      "ds_read_b128 a[92:95], v243 offset:5120\n"
      "v_mfma_f32_16x16x32_bf16 v[180:183], a[124:127], a[56:59], v[180:183]\n"
      "v_mfma_f32_16x16x32_bf16 v[188:191], a[124:127], a[60:63], v[188:191]\n"
      "s_nop 3\n"
      "v_max3_f32 v112, v160, v161, v162\n"
      "v_max3_f32 v113, v168, v169, v170\n"
      "v_max3_f32 v114, v176, v177, v178\n"
      "v_max3_f32 v115, v184, v185, v186\n"
      "v_max3_f32 v112, v112, v163, v164\n"
      "v_max3_f32 v113, v113, v171, v172\n"
      "v_max3_f32 v114, v114, v179, v180\n"
      "v_max3_f32 v115, v115, v187, v188\n"
      "v_max3_f32 v112, v112, v165, v166\n"
      "v_max3_f32 v113, v113, v173, v174\n"
      "v_max3_f32 v114, v114, v181, v182\n"
      "v_max3_f32 v115, v115, v189, v190\n"
      "v_max_f32 v112, v112, v167\n"
      "v_max_f32 v113, v113, v175\n"
      "v_max_f32 v114, v114, v183\n"
      "v_max_f32 v115, v115, v191\n"
      "v_mov_b32 v208, v112\n"
      "v_mov_b32 v209, v113\n"
      "v_mov_b32 v210, v114\n"
      "v_mov_b32 v211, v115\n"
      "v_permlane16_swap_b32 v112, v208\n"
      "v_permlane16_swap_b32 v113, v209\n"
      "v_permlane16_swap_b32 v114, v210\n"
      "v_permlane16_swap_b32 v115, v211\n"
      "v_max_f32 v112, v112, v208\n"
      "v_max_f32 v113, v113, v209\n"
      "v_max_f32 v114, v114, v210\n"
      "v_max_f32 v115, v115, v211\n"
      "v_mov_b32 v208, v112\n"
      "v_mov_b32 v209, v113\n"
      "v_mov_b32 v210, v114\n"
      "v_mov_b32 v211, v115\n"
      "v_permlane32_swap_b32 v112, v208\n"
      "v_permlane32_swap_b32 v113, v209\n"
      "v_permlane32_swap_b32 v114, v210\n"
      "v_permlane32_swap_b32 v115, v211\n"
      "v_max_f32 v112, v112, v208\n"
      "v_max_f32 v113, v113, v209\n"
      "v_max_f32 v114, v114, v210\n"
      "v_max_f32 v115, v115, v211\n"
      "v_sub_f32 v112, 0xc1800000, v112\n"
      "v_sub_f32 v113, 0xc1800000, v113\n"
      "v_sub_f32 v114, 0xc1800000, v114\n"
      "v_sub_f32 v115, 0xc1800000, v115\n"
      "v_floor_f32 v112, v112\n"
      "v_floor_f32 v113, v113\n"
      "v_floor_f32 v114, v114\n"
      "v_floor_f32 v115, v115\n"
      "v_min_f32 v224, 0x7f000000, v112\n"
      "v_min_f32 v228, 0x7f000000, v113\n"
      "v_min_f32 v232, 0x7f000000, v114\n"
      "v_min_f32 v236, 0x7f000000, v115\n"
      "v_add_f32 v112, %[neg], v224\n"
      "v_add_f32 v113, %[neg], v228\n"
      "v_add_f32 v114, %[neg], v232\n"
      "v_add_f32 v115, %[neg], v236\n"
      "v_mov_b32 v225, v224\n"
      "v_mov_b32 v229, v228\n"
      "v_mov_b32 v233, v232\n"
      "v_mov_b32 v237, v236\n"
      "v_mov_b64 v[226:227], v[224:225]\n"
      "v_mov_b64 v[230:231], v[228:229]\n"
      "v_mov_b64 v[234:235], v[232:233]\n"
      "v_mov_b64 v[238:239], v[236:237]\n"
      "s_add_u32 m0, s77, 16384\n"
      "v_pk_add_f32 v[160:161], v[160:161], v[112:113] op_sel:[0,0] op_sel_hi:[1,0]\n"
      "buffer_load_dwordx4 v249, s[24:27], s61 offen nt lds\n"
      "v_pk_add_f32 v[162:163], v[162:163], v[112:113] op_sel:[0,0] op_sel_hi:[1,0]\n"
      "v_pk_add_f32 v[164:165], v[164:165], v[112:113] op_sel:[0,0] op_sel_hi:[1,0]\n"
      "v_pk_add_f32 v[166:167], v[166:167], v[112:113] op_sel:[0,0] op_sel_hi:[1,0]\n"
      "s_add_u32 m0, s77, 17408\n"
      "v_pk_add_f32 v[168:169], v[168:169], v[112:113] op_sel:[0,1] op_sel_hi:[1,1]\n"
      "buffer_load_dwordx4 v121, s[24:27], s61 offen nt lds\n"
      "v_pk_add_f32 v[170:171], v[170:171], v[112:113] op_sel:[0,1] op_sel_hi:[1,1]\n"
      "v_pk_add_f32 v[172:173], v[172:173], v[112:113] op_sel:[0,1] op_sel_hi:[1,1]\n"
      "v_pk_add_f32 v[174:175], v[174:175], v[112:113] op_sel:[0,1] op_sel_hi:[1,1]\n"
      "s_add_u32 m0, s77, 24576\n"
      "v_pk_add_f32 v[176:177], v[176:177], v[114:115] op_sel:[0,0] op_sel_hi:[1,0]\n"
      "buffer_load_dwordx4 v122, s[24:27], s61 offen nt lds\n"
      "v_pk_add_f32 v[178:179], v[178:179], v[114:115] op_sel:[0,0] op_sel_hi:[1,0]\n"
      "v_pk_add_f32 v[180:181], v[180:181], v[114:115] op_sel:[0,0] op_sel_hi:[1,0]\n"
      "v_pk_add_f32 v[182:183], v[182:183], v[114:115] op_sel:[0,0] op_sel_hi:[1,0]\n"
      "s_add_u32 m0, s77, 25600\n"
      "v_pk_add_f32 v[184:185], v[184:185], v[114:115] op_sel:[0,1] op_sel_hi:[1,1]\n"
      "buffer_load_dwordx4 v123, s[24:27], s61 offen nt lds\n"
      "v_pk_add_f32 v[186:187], v[186:187], v[114:115] op_sel:[0,1] op_sel_hi:[1,1]\n"
      "v_pk_add_f32 v[188:189], v[188:189], v[114:115] op_sel:[0,1] op_sel_hi:[1,1]\n"
      "v_pk_add_f32 v[190:191], v[190:191], v[114:115] op_sel:[0,1] op_sel_hi:[1,1]\n"
      "s_add_u32 s77, s77, 0x8000\n"
      "s_and_b32 s77, s77, 0x1ffff\n"
      "s_add_u32 s61, s61, %[inc]\n"
      "v_exp_f32 v112, v160\n"
      "v_exp_f32 v113, v161\n"
      "v_exp_f32 v114, v162\n"
      "v_cvt_pk_bf16_f32 v208, v112, v113\n"
      "v_exp_f32 v115, v163\n"
      "v_exp_f32 v112, v164\n"
      "v_cvt_pk_bf16_f32 v209, v114, v115\n"
      "v_exp_f32 v113, v165\n"
      "v_or_b32 v116, v208, v209\n"
      "v_exp_f32 v114, v166\n"
      "v_cvt_pk_bf16_f32 v210, v112, v113\n"
      "v_exp_f32 v115, v167\n"
      "v_exp_f32 v112, v168\n"
      "v_cvt_pk_bf16_f32 v211, v114, v115\n"
      "v_exp_f32 v113, v169\n"
      "v_or3_b32 v116, v116, v210, v211\n"
      "v_exp_f32 v114, v170\n"
      "v_cvt_pk_bf16_f32 v212, v112, v113\n"
      "v_exp_f32 v115, v171\n"
      "v_exp_f32 v112, v172\n"
      "v_cvt_pk_bf16_f32 v213, v114, v115\n"
      "v_exp_f32 v113, v173\n"
      "v_or3_b32 v116, v116, v212, v213\n"
      "s_waitcnt lgkmcnt(0)\n"
      "v_cvt_pk_bf16_f32 v214, v112, v113\n"
      "s_sub_u32 s63, %[n1], 1\n"
      "s_branch ENTRY_%=\n"
      "LOOP1_%=:\n"
      "s_waitcnt vmcnt(12) lgkmcnt(0)\n"
      "s_sub_u32 s67, s68, s90\n"
      "s_cmp_lt_i32 s67, 0\n"
      "s_cbranch_scc1 GW0_%=\n"
      "GX0_%=:\n"
      "s_barrier\n"
      "v_mfma_f32_16x16x32_bf16 v[40:43], a[96:99], v[216:219], v[40:43]\n"
      "s_memrealtime s[68:69]\n"
      "buffer_load_dword v250, v251, s[28:31], 0 offen sc1\n"
      "ds_read_b128 a[64:67], v242 offset:0\n"
      "v_exp_f32 v114, v166\n"
      "v_mfma_f32_16x16x32_bf16 v[44:47], a[96:99], v[220:223], v[44:47]\n"
      "s_add_u32 m0, s77, 16384\n"
      "ds_read_b128 a[68:71], v242 offset:4096\n"
      "ds_read_b128 a[72:75], v243 offset:0\n"
      "v_mfma_f32_16x16x32_bf16 v[60:63], a[100:103], v[220:223], v[60:63]\n"
      "buffer_load_dwordx4 v249, s[24:27], s61 offen nt lds\n"
      "v_exp_f32 v115, v167\n"
      "v_mfma_f32_16x16x32_bf16 v[56:59], a[100:103], v[216:219], v[56:59]\n"
      "s_add_u32 m0, s77, 17408\n"
      "ds_read_b128 a[76:79], v243 offset:4096\n"
      "v_cvt_pk_bf16_f32 v211, v114, v115\n"
      "v_mfma_f32_16x16x32_bf16 a[136:139], a[104:107], v[216:219], a[136:139]\n"
      "buffer_load_dwordx4 v121, s[24:27], s61 offen nt lds\n"
      "v_exp_f32 v112, v168\n"
      "v_mfma_f32_16x16x32_bf16 a[140:143], a[104:107], v[220:223], a[140:143]\n"
      "s_add_u32 m0, s77, 24576\n"
      "ds_read_b128 a[80:83], v242 offset:1024\n"
      "v_or3_b32 v116, v116, v210, v211\n"
      "v_mfma_f32_16x16x32_bf16 a[156:159], a[108:111], v[220:223], a[156:159]\n"
      "buffer_load_dwordx4 v122, s[24:27], s61 offen nt lds\n"
      "v_exp_f32 v113, v169\n"
      "v_mfma_f32_16x16x32_bf16 a[152:155], a[108:111], v[216:219], a[152:155]\n"
      "s_add_u32 m0, s77, 25600\n"
      "ds_read_b128 a[84:87], v242 offset:5120\n"
      "v_cvt_pk_bf16_f32 v212, v112, v113\n"
      "v_mfma_f32_16x16x32_bf16 v[72:75], a[112:115], v[216:219], v[72:75]\n"
      "s_add_u32 s77, s77, 0x8000\n"
      "buffer_load_dwordx4 v123, s[24:27], s61 offen nt lds\n"
      "v_exp_f32 v114, v170\n"
      "v_mfma_f32_16x16x32_bf16 v[76:79], a[112:115], v[220:223], v[76:79]\n"
      "s_and_b32 s77, s77, 0x1ffff\n"
      "ds_read_b128 a[88:91], v243 offset:1024\n"
      "ds_read_b128 a[92:95], v243 offset:5120\n"
      "v_mfma_f32_16x16x32_bf16 v[92:95], a[116:119], v[220:223], v[92:95]\n"
      "s_add_u32 s61, s61, %[inc]\n"
      "v_exp_f32 v115, v171\n"
      "v_mfma_f32_16x16x32_bf16 v[88:91], a[116:119], v[216:219], v[88:91]\n"
      "v_cvt_pk_bf16_f32 v213, v114, v115\n"
      "v_mfma_f32_16x16x32_bf16 a[168:171], a[120:123], v[216:219], a[168:171]\n"
      "v_exp_f32 v112, v172\n"
      "v_mfma_f32_16x16x32_bf16 a[172:175], a[120:123], v[220:223], a[172:175]\n"
      "v_or3_b32 v116, v116, v212, v213\n"
      "v_mfma_f32_16x16x32_bf16 a[188:191], a[124:127], v[220:223], a[188:191]\n"
      "v_exp_f32 v113, v173\n"
      "v_mfma_f32_16x16x32_bf16 a[184:187], a[124:127], v[216:219], a[184:187]\n"
      "s_waitcnt lgkmcnt(0)\n"
      "v_cvt_pk_bf16_f32 v214, v112, v113\n"
      "ENTRY_%=:\n"
      "v_mfma_f32_16x16x32_bf16 v[128:131], a[64:67], a[0:3], v[224:227]\n"
      "s_sub_u32 s67, s68, s66\n"
      "v_exp_f32 v114, v174\n"
      "v_mfma_f32_16x16x32_bf16 v[128:131], a[72:75], a[16:19], v[128:131]\n"
      "s_cmp_gt_i32 s67, s70\n"
      "ds_read_b64_tr_b16 a[96:97], v244 offset:0\n"
      "ds_read_b64_tr_b16 a[98:99], v244 offset:4096\n"
      "v_mfma_f32_16x16x32_bf16 v[128:131], a[80:83], a[32:35], v[128:131]\n"
      "s_cselect_b32 s69, s86, 0\n"
      "v_exp_f32 v115, v175\n"
      "v_mfma_f32_16x16x32_bf16 v[128:131], a[88:91], a[48:51], v[128:131]\n"
      "s_add_u32 s66, s66, s69\n"
      "ds_read_b64_tr_b16 a[100:101], v245 offset:0\n"
      "v_cvt_pk_bf16_f32 v215, v114, v115\n"
      "v_mfma_f32_16x16x32_bf16 v[132:135], a[92:95], a[48:51], v[224:227]\n"
      "s_add_u32 s67, s67, s101\n"
      "v_exp_f32 v112, v176\n"
      "v_mfma_f32_16x16x32_bf16 v[132:135], a[84:87], a[32:35], v[132:135]\n"
      "s_min_i32 s70, s70, s67\n"
      "ds_read_b64_tr_b16 a[102:103], v245 offset:4096\n"
      "v_or3_b32 v116, v116, v214, v215\n"
      "v_mfma_f32_16x16x32_bf16 v[132:135], a[76:79], a[16:19], v[132:135]\n"
      "s_max_i32 s70, s70, 85\n"
      "v_exp_f32 v113, v177\n"
      "v_mfma_f32_16x16x32_bf16 v[132:135], a[68:71], a[0:3], v[132:135]\n"
      "s_add_u32 s66, s66, s86\n"
      "ds_read_b64_tr_b16 a[104:105], v246 offset:0\n"
      "v_cvt_pk_bf16_f32 v216, v112, v113\n"
      "v_mfma_f32_16x16x32_bf16 v[140:143], a[68:71], a[4:7], v[228:231]\n"
      "s_and_b32 s76, s80, 31\n"
      "v_exp_f32 v114, v178\n"
      "v_mfma_f32_16x16x32_bf16 v[140:143], a[76:79], a[20:23], v[140:143]\n"
      "s_lshr_b64 s[80:81], s[80:81], 5\n"
      "ds_read_b64_tr_b16 a[106:107], v246 offset:4096\n"
      "ds_read_b64_tr_b16 a[108:109], v247 offset:0\n"
      "v_mfma_f32_16x16x32_bf16 v[140:143], a[84:87], a[36:39], v[140:143]\n"
      "s_add_u32 s66, s66, s76\n"
      "v_exp_f32 v115, v179\n"
      "v_mfma_f32_16x16x32_bf16 v[140:143], a[92:95], a[52:55], v[140:143]\n"
      "s_sub_u32 s67, s68, s82\n"
      "ds_read_b64_tr_b16 a[110:111], v247 offset:4096\n"
      "v_cvt_pk_bf16_f32 v217, v114, v115\n"
      "v_mfma_f32_16x16x32_bf16 v[136:139], a[88:91], a[52:55], v[228:231]\n"
      "s_min_i32 s76, s67, s83\n"
      "v_exp_f32 v112, v180\n"
      "v_mfma_f32_16x16x32_bf16 v[136:139], a[80:83], a[36:39], v[136:139]\n"
      "s_mov_b32 s83, s67\n"
      "ds_read_b64_tr_b16 a[112:113], v244 offset:1024\n"
      "v_or3_b32 v116, v116, v216, v217\n"
      "v_mfma_f32_16x16x32_bf16 v[136:139], a[72:75], a[20:23], v[136:139]\n"
      "s_sub_u32 s76, s76, 36\n"
      "v_exp_f32 v113, v181\n"
      "v_mfma_f32_16x16x32_bf16 v[136:139], a[64:67], a[4:7], v[136:139]\n"
      "s_min_i32 s76, s76, 148\n"
      "ds_read_b64_tr_b16 a[114:115], v244 offset:5120\n"
      "v_cvt_pk_bf16_f32 v218, v112, v113\n"
      "v_mfma_f32_16x16x32_bf16 v[144:147], a[64:67], a[8:11], v[232:235]\n"
      "s_max_i32 s76, s76, 70\n"
      "v_exp_f32 v114, v182\n"
      "v_mfma_f32_16x16x32_bf16 v[144:147], a[72:75], a[24:27], v[144:147]\n"
      "s_mov_b32 s82, s68\n"
      "ds_read_b64_tr_b16 a[116:117], v245 offset:1024\n"
      "ds_read_b64_tr_b16 a[118:119], v245 offset:5120\n"
      "v_mfma_f32_16x16x32_bf16 v[144:147], a[80:83], a[40:43], v[144:147]\n"
      "s_add_u32 s65, s82, s76\n"
      "v_exp_f32 v115, v183\n"
      "v_mfma_f32_16x16x32_bf16 v[144:147], a[88:91], a[56:59], v[144:147]\n"
      "s_sub_u32 s93, s93, 1\n"
      "ds_read_b64_tr_b16 a[120:121], v246 offset:1024\n"
      "v_cvt_pk_bf16_f32 v219, v114, v115\n"
      "v_mfma_f32_16x16x32_bf16 v[148:151], a[92:95], a[56:59], v[232:235]\n"
      "s_cmp_le_i32 s93, 14\n"
      "v_exp_f32 v112, v184\n"
      "v_mfma_f32_16x16x32_bf16 v[148:151], a[84:87], a[40:43], v[148:151]\n"
      "s_cselect_b32 s86, 107, s86\n"
      "ds_read_b64_tr_b16 a[122:123], v246 offset:5120\n"
      "v_or3_b32 v116, v116, v218, v219\n"
      "v_mfma_f32_16x16x32_bf16 v[148:151], a[76:79], a[24:27], v[148:151]\n"
      "s_sub_u32 s67, s65, s66\n"
      "v_exp_f32 v113, v185\n"
      "v_mfma_f32_16x16x32_bf16 v[148:151], a[68:71], a[8:11], v[148:151]\n"
      "s_cmp_lt_i32 s67, 0\n"
      "ds_read_b64_tr_b16 a[124:125], v247 offset:1024\n"
      "v_cvt_pk_bf16_f32 v220, v112, v113\n"
      "v_mfma_f32_16x16x32_bf16 v[156:159], a[68:71], a[12:15], v[236:239]\n"
      "s_cselect_b32 s88, s66, s65\n"
      "v_exp_f32 v114, v186\n"
      "v_mfma_f32_16x16x32_bf16 v[156:159], a[76:79], a[28:31], v[156:159]\n"
      "s_add_u32 s90, s88, s91\n"
      "ds_read_b64_tr_b16 a[126:127], v247 offset:5120\n"
      "v_mfma_f32_16x16x32_bf16 v[156:159], a[84:87], a[44:47], v[156:159]\n"
      "v_exp_f32 v115, v187\n"
      "v_mfma_f32_16x16x32_bf16 v[156:159], a[92:95], a[60:63], v[156:159]\n"
      "v_cvt_pk_bf16_f32 v221, v114, v115\n"
      "v_mfma_f32_16x16x32_bf16 v[152:155], a[88:91], a[60:63], v[236:239]\n"
      "v_exp_f32 v112, v188\n"
      "v_mfma_f32_16x16x32_bf16 v[152:155], a[80:83], a[44:47], v[152:155]\n"
      "v_or3_b32 v116, v116, v220, v221\n"
      "v_mfma_f32_16x16x32_bf16 v[152:155], a[72:75], a[28:31], v[152:155]\n"
      "v_exp_f32 v113, v189\n"
      "v_mfma_f32_16x16x32_bf16 v[152:155], a[64:67], a[12:15], v[152:155]\n"
      "s_waitcnt lgkmcnt(10)\n"
      "v_cvt_pk_bf16_f32 v222, v112, v113\n"
      "v_mfma_f32_16x16x32_bf16 v[32:35], a[96:99], v[192:195], v[32:35]\n"
      "v_exp_f32 v114, v190\n"
      "v_mfma_f32_16x16x32_bf16 v[48:51], a[100:103], v[192:195], v[48:51]\n"
      "v_mfma_f32_16x16x32_bf16 a[128:131], a[104:107], v[192:195], a[128:131]\n"
      "s_waitcnt lgkmcnt(6)\n"
      "v_exp_f32 v115, v191\n"
      "v_mfma_f32_16x16x32_bf16 a[144:147], a[108:111], v[192:195], a[144:147]\n"
      "v_cvt_pk_bf16_f32 v223, v114, v115\n"
      "v_mfma_f32_16x16x32_bf16 v[64:67], a[112:115], v[192:195], v[64:67]\n"
      "s_waitcnt lgkmcnt(2)\n"
      "v_mfma_f32_16x16x32_bf16 v[80:83], a[116:119], v[192:195], v[80:83]\n"
      "v_or3_b32 v116, v116, v222, v223\n"
      "v_mfma_f32_16x16x32_bf16 a[160:163], a[120:123], v[192:195], a[160:163]\n"
      "s_mov_b32 s64, 0\n"
      "s_waitcnt lgkmcnt(0)\n"
      "v_and_b32 v116, 0x40004000, v116\n"
      "v_mfma_f32_16x16x32_bf16 a[176:179], a[124:127], v[192:195], a[176:179]\n"
      "v_cmp_ne_u32 vcc, 0, v116\n"
      "s_cbranch_vccnz FB1_%=\n"
      "RET1_0_%=:\n"
      "v_mfma_f32_16x16x32_bf16 a[180:183], a[124:127], v[196:199], a[180:183]\n"
      "v_exp_f32 v112, v128\n"
      "s_barrier\n"
      "v_mfma_f32_16x16x32_bf16 a[164:167], a[120:123], v[196:199], a[164:167]\n"
      "v_mfma_f32_16x16x32_bf16 a[192:195], a[196:199], v[208:211], a[192:195]\n"
      "s_add_u32 m0, s62, 0\n"
      "v_exp_f32 v113, v129\n"
      "v_mfma_f32_16x16x32_bf16 v[84:87], a[116:119], v[196:199], v[84:87]\n"
      "buffer_load_dwordx4 v248, s[20:23], s60 offen nt lds\n"
      "v_cvt_pk_bf16_f32 v192, v112, v113\n"
      "v_mfma_f32_16x16x32_bf16 v[68:71], a[112:115], v[196:199], v[68:71]\n"
      "s_add_u32 m0, s62, 896\n"
      "v_exp_f32 v114, v130\n"
      "v_mfma_f32_16x16x32_bf16 a[192:195], a[200:203], v[212:215], a[192:195]\n"
      "buffer_load_dwordx4 v248, s[20:23], s60 offen offset:128 nt lds\n"
      "v_mfma_f32_16x16x32_bf16 a[148:151], a[108:111], v[196:199], a[148:151]\n"
      "s_add_u32 m0, s62, 8192\n"
      "v_exp_f32 v115, v131\n"
      "v_mfma_f32_16x16x32_bf16 a[132:135], a[104:107], v[196:199], a[132:135]\n"
      "s_memrealtime s[68:69]\n"
      "buffer_load_dwordx4 v120, s[20:23], s60 offen nt lds\n"
      "v_cvt_pk_bf16_f32 v193, v114, v115\n"
      "v_mfma_f32_16x16x32_bf16 a[192:195], a[204:207], v[216:219], a[192:195]\n"
      "s_add_u32 m0, s62, 9088\n"
      "v_exp_f32 v112, v132\n"
      "v_mfma_f32_16x16x32_bf16 v[52:55], a[100:103], v[196:199], v[52:55]\n"
      "s_add_u32 s62, s62, 0x8000\n"
      "buffer_load_dwordx4 v120, s[20:23], s60 offen offset:128 nt lds\n"
      "v_or_b32 v116, v192, v193\n"
      "v_mfma_f32_16x16x32_bf16 v[36:39], a[96:99], v[196:199], v[36:39]\n"
      "s_and_b32 s62, s62, 0x1ffff\n"
      "v_exp_f32 v113, v133\n"
      "v_mfma_f32_16x16x32_bf16 a[192:195], a[208:211], v[220:223], a[192:195]\n"
      "s_add_u32 s60, s60, s78\n"
      "v_cvt_pk_bf16_f32 v194, v112, v113\n"
      "s_waitcnt vmcnt(12) lgkmcnt(0)\n"
      "s_sub_u32 s67, s68, s90\n"
      "s_cmp_lt_i32 s67, 0\n"
      "s_cbranch_scc1 GW1_%=\n"
      "GX1_%=:\n"
      "s_barrier\n"
      "v_mfma_f32_16x16x32_bf16 v[40:43], a[96:99], v[200:203], v[40:43]\n"
      "s_memrealtime s[68:69]\n"
      "ds_read_b128 a[64:67], v242 offset:32768\n"
      "v_exp_f32 v114, v134\n"
      "v_mfma_f32_16x16x32_bf16 v[44:47], a[96:99], v[204:207], v[44:47]\n"
      "s_add_u32 m0, s77, 16384\n"
      "ds_read_b128 a[68:71], v242 offset:36864\n"
      "ds_read_b128 a[72:75], v243 offset:32768\n"
      "v_mfma_f32_16x16x32_bf16 v[60:63], a[100:103], v[204:207], v[60:63]\n"
      "buffer_load_dwordx4 v249, s[24:27], s61 offen nt lds\n"
      "v_exp_f32 v115, v135\n"
      "v_mfma_f32_16x16x32_bf16 v[56:59], a[100:103], v[200:203], v[56:59]\n"
      "s_add_u32 m0, s77, 17408\n"
      "ds_read_b128 a[76:79], v243 offset:36864\n"
      "v_cvt_pk_bf16_f32 v195, v114, v115\n"
      "v_mfma_f32_16x16x32_bf16 a[136:139], a[104:107], v[200:203], a[136:139]\n"
      "buffer_load_dwordx4 v121, s[24:27], s61 offen nt lds\n"
      "v_exp_f32 v112, v136\n"
      "v_mfma_f32_16x16x32_bf16 a[140:143], a[104:107], v[204:207], a[140:143]\n"
      "s_add_u32 m0, s77, 24576\n"
      "ds_read_b128 a[80:83], v242 offset:33792\n"
      "v_or3_b32 v116, v116, v194, v195\n"
      "v_mfma_f32_16x16x32_bf16 a[156:159], a[108:111], v[204:207], a[156:159]\n"
      "buffer_load_dwordx4 v122, s[24:27], s61 offen nt lds\n"
      "v_exp_f32 v113, v137\n"
      "v_mfma_f32_16x16x32_bf16 a[152:155], a[108:111], v[200:203], a[152:155]\n"
      "s_add_u32 m0, s77, 25600\n"
      "ds_read_b128 a[84:87], v242 offset:37888\n"
      "v_cvt_pk_bf16_f32 v196, v112, v113\n"
      "v_mfma_f32_16x16x32_bf16 v[72:75], a[112:115], v[200:203], v[72:75]\n"
      "s_add_u32 s77, s77, 0x8000\n"
      "buffer_load_dwordx4 v123, s[24:27], s61 offen nt lds\n"
      "v_exp_f32 v114, v138\n"
      "v_mfma_f32_16x16x32_bf16 v[76:79], a[112:115], v[204:207], v[76:79]\n"
      "s_and_b32 s77, s77, 0x1ffff\n"
      "ds_read_b128 a[88:91], v243 offset:33792\n"
      "ds_read_b128 a[92:95], v243 offset:37888\n"
      "v_mfma_f32_16x16x32_bf16 v[92:95], a[116:119], v[204:207], v[92:95]\n"
      "s_add_u32 s61, s61, s78\n"
      "v_exp_f32 v115, v139\n"
      "v_mfma_f32_16x16x32_bf16 v[88:91], a[116:119], v[200:203], v[88:91]\n"
      "v_cvt_pk_bf16_f32 v197, v114, v115\n"
      "v_mfma_f32_16x16x32_bf16 a[168:171], a[120:123], v[200:203], a[168:171]\n"
      "v_exp_f32 v112, v140\n"
      "v_mfma_f32_16x16x32_bf16 a[172:175], a[120:123], v[204:207], a[172:175]\n"
      "v_or3_b32 v116, v116, v196, v197\n"
      "v_mfma_f32_16x16x32_bf16 a[188:191], a[124:127], v[204:207], a[188:191]\n"
      "v_exp_f32 v113, v141\n"
      "v_mfma_f32_16x16x32_bf16 a[184:187], a[124:127], v[200:203], a[184:187]\n"
      "s_waitcnt lgkmcnt(0)\n"
      "v_cvt_pk_bf16_f32 v198, v112, v113\n"
      "v_mfma_f32_16x16x32_bf16 v[160:163], a[64:67], a[0:3], v[224:227]\n"
      "s_sub_u32 s67, s68, s66\n"
      "v_exp_f32 v114, v142\n"
      "v_mfma_f32_16x16x32_bf16 v[160:163], a[72:75], a[16:19], v[160:163]\n"
      "s_cmp_gt_i32 s67, s70\n"
      "ds_read_b64_tr_b16 a[96:97], v244 offset:32768\n"
      "ds_read_b64_tr_b16 a[98:99], v244 offset:36864\n"
      "v_mfma_f32_16x16x32_bf16 v[160:163], a[80:83], a[32:35], v[160:163]\n"
      "s_cselect_b32 s69, s86, 0\n"
      "v_exp_f32 v115, v143\n"
      "v_mfma_f32_16x16x32_bf16 v[160:163], a[88:91], a[48:51], v[160:163]\n"
      "s_add_u32 s66, s66, s69\n"
      "ds_read_b64_tr_b16 a[100:101], v245 offset:32768\n"
      "v_cvt_pk_bf16_f32 v199, v114, v115\n"
      "v_mfma_f32_16x16x32_bf16 v[164:167], a[92:95], a[48:51], v[224:227]\n"
      "s_add_u32 s67, s67, s101\n"
      "v_exp_f32 v112, v144\n"
      "v_mfma_f32_16x16x32_bf16 v[164:167], a[84:87], a[32:35], v[164:167]\n"
      "s_min_i32 s70, s70, s67\n"
      "ds_read_b64_tr_b16 a[102:103], v245 offset:36864\n"
      "v_or3_b32 v116, v116, v198, v199\n"
      "v_mfma_f32_16x16x32_bf16 v[164:167], a[76:79], a[16:19], v[164:167]\n"
      "s_max_i32 s70, s70, 85\n"
      "v_exp_f32 v113, v145\n"
      "v_mfma_f32_16x16x32_bf16 v[164:167], a[68:71], a[0:3], v[164:167]\n"
      "s_add_u32 s66, s66, s86\n"
      "ds_read_b64_tr_b16 a[104:105], v246 offset:32768\n"
      "v_cvt_pk_bf16_f32 v200, v112, v113\n"
      "v_mfma_f32_16x16x32_bf16 v[172:175], a[68:71], a[4:7], v[228:231]\n"
      "s_and_b32 s76, s80, 31\n"
      "v_exp_f32 v114, v146\n"
      "v_mfma_f32_16x16x32_bf16 v[172:175], a[76:79], a[20:23], v[172:175]\n"
      "s_lshr_b64 s[80:81], s[80:81], 5\n"
      "ds_read_b64_tr_b16 a[106:107], v246 offset:36864\n"
      "ds_read_b64_tr_b16 a[108:109], v247 offset:32768\n"
      "v_mfma_f32_16x16x32_bf16 v[172:175], a[84:87], a[36:39], v[172:175]\n"
      "s_add_u32 s66, s66, s76\n"
      "v_exp_f32 v115, v147\n"
      "v_mfma_f32_16x16x32_bf16 v[172:175], a[92:95], a[52:55], v[172:175]\n"
      "s_sub_u32 s67, s68, s82\n"
      "ds_read_b64_tr_b16 a[110:111], v247 offset:36864\n"
      "v_cvt_pk_bf16_f32 v201, v114, v115\n"
      "v_mfma_f32_16x16x32_bf16 v[168:171], a[88:91], a[52:55], v[228:231]\n"
      "s_min_i32 s76, s67, s83\n"
      "v_exp_f32 v112, v148\n"
      "v_mfma_f32_16x16x32_bf16 v[168:171], a[80:83], a[36:39], v[168:171]\n"
      "s_mov_b32 s83, s67\n"
      "ds_read_b64_tr_b16 a[112:113], v244 offset:33792\n"
      "v_or3_b32 v116, v116, v200, v201\n"
      "v_mfma_f32_16x16x32_bf16 v[168:171], a[72:75], a[20:23], v[168:171]\n"
      "s_sub_u32 s76, s76, 36\n"
      "v_exp_f32 v113, v149\n"
      "v_mfma_f32_16x16x32_bf16 v[168:171], a[64:67], a[4:7], v[168:171]\n"
      "s_min_i32 s76, s76, 148\n"
      "ds_read_b64_tr_b16 a[114:115], v244 offset:37888\n"
      "v_cvt_pk_bf16_f32 v202, v112, v113\n"
      "v_mfma_f32_16x16x32_bf16 v[176:179], a[64:67], a[8:11], v[232:235]\n"
      "s_max_i32 s76, s76, 70\n"
      "v_exp_f32 v114, v150\n"
      "v_mfma_f32_16x16x32_bf16 v[176:179], a[72:75], a[24:27], v[176:179]\n"
      "s_mov_b32 s82, s68\n"
      "ds_read_b64_tr_b16 a[116:117], v245 offset:33792\n"
      "ds_read_b64_tr_b16 a[118:119], v245 offset:37888\n"
      "v_mfma_f32_16x16x32_bf16 v[176:179], a[80:83], a[40:43], v[176:179]\n"
      "s_add_u32 s65, s82, s76\n"
      "v_exp_f32 v115, v151\n"
      "v_mfma_f32_16x16x32_bf16 v[176:179], a[88:91], a[56:59], v[176:179]\n"
      "s_sub_u32 s93, s93, 1\n"
      "ds_read_b64_tr_b16 a[120:121], v246 offset:33792\n"
      "v_cvt_pk_bf16_f32 v203, v114, v115\n"
      "v_mfma_f32_16x16x32_bf16 v[180:183], a[92:95], a[56:59], v[232:235]\n"
      "s_cmp_le_i32 s93, 14\n"
      "v_exp_f32 v112, v152\n"
      "v_mfma_f32_16x16x32_bf16 v[180:183], a[84:87], a[40:43], v[180:183]\n"
      "s_cselect_b32 s86, 107, s86\n"
      "ds_read_b64_tr_b16 a[122:123], v246 offset:37888\n"
      "v_or3_b32 v116, v116, v202, v203\n"
      "v_mfma_f32_16x16x32_bf16 v[180:183], a[76:79], a[24:27], v[180:183]\n"
      "s_sub_u32 s67, s65, s66\n"
      "v_exp_f32 v113, v153\n"
      "v_mfma_f32_16x16x32_bf16 v[180:183], a[68:71], a[8:11], v[180:183]\n"
      "s_cmp_lt_i32 s67, 0\n"
      "ds_read_b64_tr_b16 a[124:125], v247 offset:33792\n"
      "v_cvt_pk_bf16_f32 v204, v112, v113\n"
      "v_mfma_f32_16x16x32_bf16 v[188:191], a[68:71], a[12:15], v[236:239]\n"
      "s_cselect_b32 s88, s66, s65\n"
      "v_exp_f32 v114, v154\n"
      "v_mfma_f32_16x16x32_bf16 v[188:191], a[76:79], a[28:31], v[188:191]\n"
      "s_add_u32 s90, s88, s91\n"
      "ds_read_b64_tr_b16 a[126:127], v247 offset:37888\n"
      "v_mfma_f32_16x16x32_bf16 v[188:191], a[84:87], a[44:47], v[188:191]\n"
      "v_exp_f32 v115, v155\n"
      "v_mfma_f32_16x16x32_bf16 v[188:191], a[92:95], a[60:63], v[188:191]\n"
      "v_cvt_pk_bf16_f32 v205, v114, v115\n"
      "v_mfma_f32_16x16x32_bf16 v[184:187], a[88:91], a[60:63], v[236:239]\n"
      "v_exp_f32 v112, v156\n"
      "v_mfma_f32_16x16x32_bf16 v[184:187], a[80:83], a[44:47], v[184:187]\n"
      "v_or3_b32 v116, v116, v204, v205\n"
      "v_mfma_f32_16x16x32_bf16 v[184:187], a[72:75], a[28:31], v[184:187]\n"
      "v_exp_f32 v113, v157\n"
      "v_mfma_f32_16x16x32_bf16 v[184:187], a[64:67], a[12:15], v[184:187]\n"
      "s_waitcnt lgkmcnt(10)\n"
      "v_cvt_pk_bf16_f32 v206, v112, v113\n"
      "v_mfma_f32_16x16x32_bf16 v[32:35], a[96:99], v[208:211], v[32:35]\n"
      "v_exp_f32 v114, v158\n"
      "v_mfma_f32_16x16x32_bf16 v[48:51], a[100:103], v[208:211], v[48:51]\n"
      "v_mfma_f32_16x16x32_bf16 a[128:131], a[104:107], v[208:211], a[128:131]\n"
      "s_waitcnt lgkmcnt(6)\n"
      "v_exp_f32 v115, v159\n"
      "v_mfma_f32_16x16x32_bf16 a[144:147], a[108:111], v[208:211], a[144:147]\n"
      "v_cvt_pk_bf16_f32 v207, v114, v115\n"
      "v_mfma_f32_16x16x32_bf16 v[64:67], a[112:115], v[208:211], v[64:67]\n"
      "s_waitcnt lgkmcnt(2)\n"
      "v_mfma_f32_16x16x32_bf16 v[80:83], a[116:119], v[208:211], v[80:83]\n"
      "v_or3_b32 v116, v116, v206, v207\n"
      "v_mfma_f32_16x16x32_bf16 a[160:163], a[120:123], v[208:211], a[160:163]\n"
      "s_mov_b32 s64, 0\n"
      "s_waitcnt lgkmcnt(0)\n"
      "v_and_b32 v116, 0x40004000, v116\n"
      "v_mfma_f32_16x16x32_bf16 a[176:179], a[124:127], v[208:211], a[176:179]\n"
      "v_cmp_ne_u32 vcc, 0, v116\n"
      "s_cbranch_vccnz FB0_%=\n"
      "RET0_0_%=:\n"
      "v_mfma_f32_16x16x32_bf16 a[180:183], a[124:127], v[212:215], a[180:183]\n"
      "v_exp_f32 v112, v160\n"
      "s_barrier\n"
      "v_mfma_f32_16x16x32_bf16 a[164:167], a[120:123], v[212:215], a[164:167]\n"
      "v_xor_b32 v242, 0x10000, v242\n"
      "v_mfma_f32_16x16x32_bf16 a[192:195], a[196:199], v[192:195], a[192:195]\n"
      "s_add_u32 m0, s62, 0\n"
      "v_exp_f32 v113, v161\n"
      "v_mfma_f32_16x16x32_bf16 v[84:87], a[116:119], v[212:215], v[84:87]\n"
      "buffer_load_dwordx4 v248, s[20:23], s60 offen nt lds\n"
      "v_cvt_pk_bf16_f32 v208, v112, v113\n"
      "v_xor_b32 v243, 0x10000, v243\n"
      "v_mfma_f32_16x16x32_bf16 v[68:71], a[112:115], v[212:215], v[68:71]\n"
      "s_add_u32 m0, s62, 896\n"
      "v_exp_f32 v114, v162\n"
      "v_mfma_f32_16x16x32_bf16 a[192:195], a[200:203], v[196:199], a[192:195]\n"
      "buffer_load_dwordx4 v248, s[20:23], s60 offen offset:128 nt lds\n"
      "v_xor_b32 v244, 0x10000, v244\n"
      "v_mfma_f32_16x16x32_bf16 a[148:151], a[108:111], v[212:215], a[148:151]\n"
      "s_add_u32 m0, s62, 8192\n"
      "v_exp_f32 v115, v163\n"
      "v_mfma_f32_16x16x32_bf16 a[132:135], a[104:107], v[212:215], a[132:135]\n"
      "s_memrealtime s[68:69]\n"
      "buffer_load_dwordx4 v120, s[20:23], s60 offen nt lds\n"
      "v_cvt_pk_bf16_f32 v209, v114, v115\n"
      "v_xor_b32 v245, 0x10000, v245\n"
      "v_mfma_f32_16x16x32_bf16 a[192:195], a[204:207], v[200:203], a[192:195]\n"
      "s_add_u32 m0, s62, 9088\n"
      "v_exp_f32 v112, v164\n"
      "v_mfma_f32_16x16x32_bf16 v[52:55], a[100:103], v[212:215], v[52:55]\n"
      "s_add_u32 s62, s62, 0x8000\n"
      "buffer_load_dwordx4 v120, s[20:23], s60 offen offset:128 nt lds\n"
      "v_or_b32 v116, v208, v209\n"
      "v_xor_b32 v246, 0x10000, v246\n"
      "v_mfma_f32_16x16x32_bf16 v[36:39], a[96:99], v[212:215], v[36:39]\n"
      "s_and_b32 s62, s62, 0x1ffff\n"
      "s_add_u32 s60, s60, %[inc]\n"
      "v_exp_f32 v113, v165\n"
      "v_mfma_f32_16x16x32_bf16 a[192:195], a[208:211], v[204:207], a[192:195]\n"
      "s_sub_u32 s63, s63, 1\n"
      "v_cvt_pk_bf16_f32 v210, v112, v113\n"
      "v_xor_b32 v247, 0x10000, v247\n"
      "s_cbranch_scc0 LOOP1_%=\n"
      "v_readfirstlane_b32 s72, v240\n"
      "v_readfirstlane_b32 s79, v241\n"
      "s_waitcnt vmcnt(12) lgkmcnt(0)\n"
      "s_sub_u32 s67, s68, s90\n"
      "s_cmp_lt_i32 s67, 0\n"
      "s_cbranch_scc1 GW2_%=\n"
      "GX2_%=:\n"
      "s_barrier\n"
      "v_mfma_f32_16x16x32_bf16 v[40:43], a[96:99], v[216:219], v[40:43]\n"
      "s_memrealtime s[68:69]\n"
      "buffer_load_dword v250, v251, s[28:31], 0 offen sc1\n"
      "ds_read_b128 a[64:67], v242 offset:0\n"
      "v_exp_f32 v114, v166\n"
      "v_mfma_f32_16x16x32_bf16 v[44:47], a[96:99], v[220:223], v[44:47]\n"
      "s_add_u32 m0, s77, 16384\n"
      "ds_read_b128 a[68:71], v242 offset:4096\n"
      "ds_read_b128 a[72:75], v243 offset:0\n"
      "v_mfma_f32_16x16x32_bf16 v[60:63], a[100:103], v[220:223], v[60:63]\n"
      "buffer_load_dwordx4 v249, s[24:27], s61 offen nt lds\n"
      "v_exp_f32 v115, v167\n"
      "v_mfma_f32_16x16x32_bf16 v[56:59], a[100:103], v[216:219], v[56:59]\n"
      "s_add_u32 m0, s77, 17408\n"
      "ds_read_b128 a[76:79], v243 offset:4096\n"
      "v_cvt_pk_bf16_f32 v211, v114, v115\n"
      "v_mfma_f32_16x16x32_bf16 a[136:139], a[104:107], v[216:219], a[136:139]\n"
      "buffer_load_dwordx4 v121, s[24:27], s61 offen nt lds\n"
      "v_exp_f32 v112, v168\n"
      "v_mfma_f32_16x16x32_bf16 a[140:143], a[104:107], v[220:223], a[140:143]\n"
      "s_add_u32 m0, s77, 24576\n"
      "ds_read_b128 a[80:83], v242 offset:1024\n"
      "v_or3_b32 v116, v116, v210, v211\n"
      "v_mfma_f32_16x16x32_bf16 a[156:159], a[108:111], v[220:223], a[156:159]\n"
      "buffer_load_dwordx4 v122, s[24:27], s61 offen nt lds\n"
      "v_exp_f32 v113, v169\n"
      "v_mfma_f32_16x16x32_bf16 a[152:155], a[108:111], v[216:219], a[152:155]\n"
      "s_add_u32 m0, s77, 25600\n"
      "ds_read_b128 a[84:87], v242 offset:5120\n"
      "v_cvt_pk_bf16_f32 v212, v112, v113\n"
      "v_mfma_f32_16x16x32_bf16 v[72:75], a[112:115], v[216:219], v[72:75]\n"
      "s_add_u32 s77, s77, 0x8000\n"
      "buffer_load_dwordx4 v123, s[24:27], s61 offen nt lds\n"
      "v_exp_f32 v114, v170\n"
      "v_mfma_f32_16x16x32_bf16 v[76:79], a[112:115], v[220:223], v[76:79]\n"
      "s_and_b32 s77, s77, 0x1ffff\n"
      "ds_read_b128 a[88:91], v243 offset:1024\n"
      "ds_read_b128 a[92:95], v243 offset:5120\n"
      "v_mfma_f32_16x16x32_bf16 v[92:95], a[116:119], v[220:223], v[92:95]\n"
      "s_add_u32 s61, s61, %[inc]\n"
      "v_exp_f32 v115, v171\n"
      "v_mfma_f32_16x16x32_bf16 v[88:91], a[116:119], v[216:219], v[88:91]\n"
      "v_cvt_pk_bf16_f32 v213, v114, v115\n"
      "v_mfma_f32_16x16x32_bf16 a[168:171], a[120:123], v[216:219], a[168:171]\n"
      "v_exp_f32 v112, v172\n"
      "v_mfma_f32_16x16x32_bf16 a[172:175], a[120:123], v[220:223], a[172:175]\n"
      "v_or3_b32 v116, v116, v212, v213\n"
      "v_mfma_f32_16x16x32_bf16 a[188:191], a[124:127], v[220:223], a[188:191]\n"
      "v_exp_f32 v113, v173\n"
      "v_mfma_f32_16x16x32_bf16 a[184:187], a[124:127], v[216:219], a[184:187]\n"
      "s_waitcnt lgkmcnt(0)\n"
      "v_cvt_pk_bf16_f32 v214, v112, v113\n"
      "v_mfma_f32_16x16x32_bf16 v[128:131], a[64:67], a[0:3], v[224:227]\n"
      "s_sub_u32 s67, s68, s66\n"
      "v_exp_f32 v114, v174\n"
      "v_mfma_f32_16x16x32_bf16 v[128:131], a[72:75], a[16:19], v[128:131]\n"
      "s_cmp_gt_i32 s67, s70\n"
      "ds_read_b64_tr_b16 a[96:97], v244 offset:0\n"
      "ds_read_b64_tr_b16 a[98:99], v244 offset:4096\n"
      "v_mfma_f32_16x16x32_bf16 v[128:131], a[80:83], a[32:35], v[128:131]\n"
      "s_cselect_b32 s69, s86, 0\n"
      "v_exp_f32 v115, v175\n"
      "v_mfma_f32_16x16x32_bf16 v[128:131], a[88:91], a[48:51], v[128:131]\n"
      "s_add_u32 s66, s66, s69\n"
      "ds_read_b64_tr_b16 a[100:101], v245 offset:0\n"
      "v_cvt_pk_bf16_f32 v215, v114, v115\n"
      "v_mfma_f32_16x16x32_bf16 v[132:135], a[92:95], a[48:51], v[224:227]\n"
      "s_add_u32 s67, s67, s101\n"
      "v_exp_f32 v112, v176\n"
      "v_mfma_f32_16x16x32_bf16 v[132:135], a[84:87], a[32:35], v[132:135]\n"
      "s_min_i32 s70, s70, s67\n"
      "ds_read_b64_tr_b16 a[102:103], v245 offset:4096\n"
      "v_or3_b32 v116, v116, v214, v215\n"
      "v_mfma_f32_16x16x32_bf16 v[132:135], a[76:79], a[16:19], v[132:135]\n"
      "s_max_i32 s70, s70, 85\n"
      "v_exp_f32 v113, v177\n"
      "v_mfma_f32_16x16x32_bf16 v[132:135], a[68:71], a[0:3], v[132:135]\n"
      "s_add_u32 s66, s66, s86\n"
      "ds_read_b64_tr_b16 a[104:105], v246 offset:0\n"
      "v_cvt_pk_bf16_f32 v216, v112, v113\n"
      "v_mfma_f32_16x16x32_bf16 v[140:143], a[68:71], a[4:7], v[228:231]\n"
      "s_and_b32 s76, s80, 31\n"
      "v_exp_f32 v114, v178\n"
      "v_mfma_f32_16x16x32_bf16 v[140:143], a[76:79], a[20:23], v[140:143]\n"
      "s_lshr_b64 s[80:81], s[80:81], 5\n"
      "ds_read_b64_tr_b16 a[106:107], v246 offset:4096\n"
      "ds_read_b64_tr_b16 a[108:109], v247 offset:0\n"
      "v_mfma_f32_16x16x32_bf16 v[140:143], a[84:87], a[36:39], v[140:143]\n"
      "s_add_u32 s66, s66, s76\n"
      "v_exp_f32 v115, v179\n"
      "v_mfma_f32_16x16x32_bf16 v[140:143], a[92:95], a[52:55], v[140:143]\n"
      "s_sub_u32 s67, s68, s82\n"
      "ds_read_b64_tr_b16 a[110:111], v247 offset:4096\n"
      "v_cvt_pk_bf16_f32 v217, v114, v115\n"
      "v_mfma_f32_16x16x32_bf16 v[136:139], a[88:91], a[52:55], v[228:231]\n"
      "s_min_i32 s76, s67, s83\n"
      "v_exp_f32 v112, v180\n"
      "v_mfma_f32_16x16x32_bf16 v[136:139], a[80:83], a[36:39], v[136:139]\n"
      "s_mov_b32 s83, s67\n"
      "ds_read_b64_tr_b16 a[112:113], v244 offset:1024\n"
      "v_or3_b32 v116, v116, v216, v217\n"
      "v_mfma_f32_16x16x32_bf16 v[136:139], a[72:75], a[20:23], v[136:139]\n"
      "s_sub_u32 s76, s76, 36\n"
      "v_exp_f32 v113, v181\n"
      "v_mfma_f32_16x16x32_bf16 v[136:139], a[64:67], a[4:7], v[136:139]\n"
      "s_min_i32 s76, s76, 148\n"
      "ds_read_b64_tr_b16 a[114:115], v244 offset:5120\n"
      "v_cvt_pk_bf16_f32 v218, v112, v113\n"
      "v_mfma_f32_16x16x32_bf16 v[144:147], a[64:67], a[8:11], v[232:235]\n"
      "s_max_i32 s76, s76, 70\n"
      "v_exp_f32 v114, v182\n"
      "v_mfma_f32_16x16x32_bf16 v[144:147], a[72:75], a[24:27], v[144:147]\n"
      "s_mov_b32 s82, s68\n"
      "ds_read_b64_tr_b16 a[116:117], v245 offset:1024\n"
      "ds_read_b64_tr_b16 a[118:119], v245 offset:5120\n"
      "v_mfma_f32_16x16x32_bf16 v[144:147], a[80:83], a[40:43], v[144:147]\n"
      "s_lshr_b32 s69, s86, 1\n"
      "v_exp_f32 v115, v183\n"
      "v_mfma_f32_16x16x32_bf16 v[144:147], a[88:91], a[56:59], v[144:147]\n"
      "s_add_u32 s66, s66, s69\n"
      "ds_read_b64_tr_b16 a[120:121], v246 offset:1024\n"
      "v_cvt_pk_bf16_f32 v219, v114, v115\n"
      "v_mfma_f32_16x16x32_bf16 v[148:151], a[92:95], a[56:59], v[232:235]\n"
      "s_add_u32 s82, s82, s69\n"
      "v_exp_f32 v112, v184\n"
      "v_mfma_f32_16x16x32_bf16 v[148:151], a[84:87], a[40:43], v[148:151]\n"
      "s_add_u32 s65, s82, s76\n"
      "ds_read_b64_tr_b16 a[122:123], v246 offset:5120\n"
      "v_or3_b32 v116, v116, v218, v219\n"
      "v_mfma_f32_16x16x32_bf16 v[148:151], a[76:79], a[24:27], v[148:151]\n"
      "s_sub_u32 s93, s93, 1\n"
      "v_exp_f32 v113, v185\n"
      "v_mfma_f32_16x16x32_bf16 v[148:151], a[68:71], a[8:11], v[148:151]\n"
      "s_cmp_le_i32 s93, 14\n"
      "ds_read_b64_tr_b16 a[124:125], v247 offset:1024\n"
      "v_cvt_pk_bf16_f32 v220, v112, v113\n"
      "v_mfma_f32_16x16x32_bf16 v[156:159], a[68:71], a[12:15], v[236:239]\n"
      "s_cselect_b32 s86, 107, s86\n"
      "v_exp_f32 v114, v186\n"
      "v_mfma_f32_16x16x32_bf16 v[156:159], a[76:79], a[28:31], v[156:159]\n"
      "s_sub_u32 s67, s65, s66\n"
      "ds_read_b64_tr_b16 a[126:127], v247 offset:5120\n"
      "v_mfma_f32_16x16x32_bf16 v[156:159], a[84:87], a[44:47], v[156:159]\n"
      "s_cmp_lt_i32 s67, 0\n"
      "v_exp_f32 v115, v187\n"
      "v_mfma_f32_16x16x32_bf16 v[156:159], a[92:95], a[60:63], v[156:159]\n"
      "s_cselect_b32 s88, s66, s65\n"
      "v_cvt_pk_bf16_f32 v221, v114, v115\n"
      "v_mfma_f32_16x16x32_bf16 v[152:155], a[88:91], a[60:63], v[236:239]\n"
      "s_add_u32 s90, s88, s91\n"
      "v_exp_f32 v112, v188\n"
      "v_mfma_f32_16x16x32_bf16 v[152:155], a[80:83], a[44:47], v[152:155]\n"
      "v_or3_b32 v116, v116, v220, v221\n"
      "v_mfma_f32_16x16x32_bf16 v[152:155], a[72:75], a[28:31], v[152:155]\n"
      "v_exp_f32 v113, v189\n"
      "v_mfma_f32_16x16x32_bf16 v[152:155], a[64:67], a[12:15], v[152:155]\n"
      "s_waitcnt lgkmcnt(10)\n"
      "v_cvt_pk_bf16_f32 v222, v112, v113\n"
      "v_mfma_f32_16x16x32_bf16 v[32:35], a[96:99], v[192:195], v[32:35]\n"
      "v_exp_f32 v114, v190\n"
      "v_mfma_f32_16x16x32_bf16 v[48:51], a[100:103], v[192:195], v[48:51]\n"
      "v_mfma_f32_16x16x32_bf16 a[128:131], a[104:107], v[192:195], a[128:131]\n"
      "s_waitcnt lgkmcnt(6)\n"
      "v_exp_f32 v115, v191\n"
      "v_mfma_f32_16x16x32_bf16 a[144:147], a[108:111], v[192:195], a[144:147]\n"
      "v_cvt_pk_bf16_f32 v223, v114, v115\n"
      "v_mfma_f32_16x16x32_bf16 v[64:67], a[112:115], v[192:195], v[64:67]\n"
      "s_waitcnt lgkmcnt(2)\n"
      "v_mfma_f32_16x16x32_bf16 v[80:83], a[116:119], v[192:195], v[80:83]\n"
      "v_or3_b32 v116, v116, v222, v223\n"
      "v_mfma_f32_16x16x32_bf16 a[160:163], a[120:123], v[192:195], a[160:163]\n"
      "s_mov_b32 s64, 1\n"
      "s_waitcnt lgkmcnt(0)\n"
      "v_and_b32 v116, 0x40004000, v116\n"
      "v_mfma_f32_16x16x32_bf16 a[176:179], a[124:127], v[192:195], a[176:179]\n"
      "v_cmp_ne_u32 vcc, 0, v116\n"
      "s_cbranch_vccnz FB1_%=\n"
      "RET1_1_%=:\n"
      "v_mfma_f32_16x16x32_bf16 a[180:183], a[124:127], v[196:199], a[180:183]\n"
      "v_exp_f32 v112, v128\n"
      "s_barrier\n"
      "v_mfma_f32_16x16x32_bf16 a[164:167], a[120:123], v[196:199], a[164:167]\n"
      "v_mfma_f32_16x16x32_bf16 a[192:195], a[196:199], v[208:211], a[192:195]\n"
      "s_add_u32 m0, s62, 0\n"
      "v_exp_f32 v113, v129\n"
      "v_mfma_f32_16x16x32_bf16 v[84:87], a[116:119], v[196:199], v[84:87]\n"
      "buffer_load_dwordx4 v248, s[20:23], s60 offen nt lds\n"
      "v_cvt_pk_bf16_f32 v192, v112, v113\n"
      "v_mfma_f32_16x16x32_bf16 v[68:71], a[112:115], v[196:199], v[68:71]\n"
      "s_add_u32 m0, s62, 896\n"
      "v_exp_f32 v114, v130\n"
      "v_mfma_f32_16x16x32_bf16 a[192:195], a[200:203], v[212:215], a[192:195]\n"
      "buffer_load_dwordx4 v248, s[20:23], s60 offen offset:128 nt lds\n"
      "v_mfma_f32_16x16x32_bf16 a[148:151], a[108:111], v[196:199], a[148:151]\n"
      "s_add_u32 m0, s62, 8192\n"
      "v_exp_f32 v115, v131\n"
      "v_mfma_f32_16x16x32_bf16 a[132:135], a[104:107], v[196:199], a[132:135]\n"
      "s_memrealtime s[68:69]\n"
      "buffer_load_dwordx4 v120, s[20:23], s60 offen nt lds\n"
      "v_cvt_pk_bf16_f32 v193, v114, v115\n"
      "v_mfma_f32_16x16x32_bf16 a[192:195], a[204:207], v[216:219], a[192:195]\n"
      "s_add_u32 m0, s62, 9088\n"
      "v_exp_f32 v112, v132\n"
      "v_mfma_f32_16x16x32_bf16 v[52:55], a[100:103], v[196:199], v[52:55]\n"
      "s_add_u32 s62, s62, 0x8000\n"
      "buffer_load_dwordx4 v120, s[20:23], s60 offen offset:128 nt lds\n"
      "v_or_b32 v116, v192, v193\n"
      "v_mfma_f32_16x16x32_bf16 v[36:39], a[96:99], v[196:199], v[36:39]\n"
      "s_and_b32 s62, s62, 0x1ffff\n"
      "v_exp_f32 v113, v133\n"
      "v_mfma_f32_16x16x32_bf16 a[192:195], a[208:211], v[220:223], a[192:195]\n"
      "s_add_u32 s60, s60, s78\n"
      "v_cvt_pk_bf16_f32 v194, v112, v113\n"
      "s_waitcnt vmcnt(12) lgkmcnt(0)\n"
      "s_barrier\n"
      "v_mfma_f32_16x16x32_bf16 v[40:43], a[96:99], v[200:203], v[40:43]\n"
      "ds_read_b128 a[64:67], v242 offset:32768\n"
      "v_exp_f32 v114, v134\n"
      "v_mfma_f32_16x16x32_bf16 v[44:47], a[96:99], v[204:207], v[44:47]\n"
      "ds_read_b128 a[68:71], v242 offset:36864\n"
      "ds_read_b128 a[72:75], v243 offset:32768\n"
      "v_mfma_f32_16x16x32_bf16 v[60:63], a[100:103], v[204:207], v[60:63]\n"
      "v_exp_f32 v115, v135\n"
      "v_mfma_f32_16x16x32_bf16 v[56:59], a[100:103], v[200:203], v[56:59]\n"
      "ds_read_b128 a[76:79], v243 offset:36864\n"
      "v_cvt_pk_bf16_f32 v195, v114, v115\n"
      "v_mfma_f32_16x16x32_bf16 a[136:139], a[104:107], v[200:203], a[136:139]\n"
      "v_exp_f32 v112, v136\n"
      "v_mfma_f32_16x16x32_bf16 a[140:143], a[104:107], v[204:207], a[140:143]\n"
      "ds_read_b128 a[80:83], v242 offset:33792\n"
      "v_or3_b32 v116, v116, v194, v195\n"
      "v_mfma_f32_16x16x32_bf16 a[156:159], a[108:111], v[204:207], a[156:159]\n"
      "v_exp_f32 v113, v137\n"
      "v_mfma_f32_16x16x32_bf16 a[152:155], a[108:111], v[200:203], a[152:155]\n"
      "ds_read_b128 a[84:87], v242 offset:37888\n"
      "v_cvt_pk_bf16_f32 v196, v112, v113\n"
      "v_mfma_f32_16x16x32_bf16 v[72:75], a[112:115], v[200:203], v[72:75]\n"
      "v_exp_f32 v114, v138\n"
      "v_mfma_f32_16x16x32_bf16 v[76:79], a[112:115], v[204:207], v[76:79]\n"
      "ds_read_b128 a[88:91], v243 offset:33792\n"
      "ds_read_b128 a[92:95], v243 offset:37888\n"
      "v_mfma_f32_16x16x32_bf16 v[92:95], a[116:119], v[204:207], v[92:95]\n"
      "v_exp_f32 v115, v139\n"
      "v_mfma_f32_16x16x32_bf16 v[88:91], a[116:119], v[200:203], v[88:91]\n"
      "v_cvt_pk_bf16_f32 v197, v114, v115\n"
      "v_mfma_f32_16x16x32_bf16 a[168:171], a[120:123], v[200:203], a[168:171]\n"
      "v_exp_f32 v112, v140\n"
      "v_mfma_f32_16x16x32_bf16 a[172:175], a[120:123], v[204:207], a[172:175]\n"
      "v_or3_b32 v116, v116, v196, v197\n"
      "v_mfma_f32_16x16x32_bf16 a[188:191], a[124:127], v[204:207], a[188:191]\n"
      "v_exp_f32 v113, v141\n"
      "v_mfma_f32_16x16x32_bf16 a[184:187], a[124:127], v[200:203], a[184:187]\n"
      "s_waitcnt lgkmcnt(0)\n"
      "v_cvt_pk_bf16_f32 v198, v112, v113\n"
      "v_mfma_f32_16x16x32_bf16 v[160:163], a[64:67], a[0:3], v[224:227]\n"
      "v_exp_f32 v114, v142\n"
      "v_mfma_f32_16x16x32_bf16 v[160:163], a[72:75], a[16:19], v[160:163]\n"
      "ds_read_b64_tr_b16 a[96:97], v244 offset:32768\n"
      "ds_read_b64_tr_b16 a[98:99], v244 offset:36864\n"
      "v_mfma_f32_16x16x32_bf16 v[160:163], a[80:83], a[32:35], v[160:163]\n"
      "v_exp_f32 v115, v143\n"
      "v_mfma_f32_16x16x32_bf16 v[160:163], a[88:91], a[48:51], v[160:163]\n"
      "ds_read_b64_tr_b16 a[100:101], v245 offset:32768\n"
      "v_cvt_pk_bf16_f32 v199, v114, v115\n"
      "v_mfma_f32_16x16x32_bf16 v[164:167], a[92:95], a[48:51], v[224:227]\n"
      "v_exp_f32 v112, v144\n"
      "v_mfma_f32_16x16x32_bf16 v[164:167], a[84:87], a[32:35], v[164:167]\n"
      "ds_read_b64_tr_b16 a[102:103], v245 offset:36864\n"
      "v_or3_b32 v116, v116, v198, v199\n"
      "v_mfma_f32_16x16x32_bf16 v[164:167], a[76:79], a[16:19], v[164:167]\n"
      "v_exp_f32 v113, v145\n"
      "v_mfma_f32_16x16x32_bf16 v[164:167], a[68:71], a[0:3], v[164:167]\n"
      "ds_read_b64_tr_b16 a[104:105], v246 offset:32768\n"
      "v_cvt_pk_bf16_f32 v200, v112, v113\n"
      "v_mfma_f32_16x16x32_bf16 v[172:175], a[68:71], a[4:7], v[228:231]\n"
      "v_exp_f32 v114, v146\n"
      "v_mfma_f32_16x16x32_bf16 v[172:175], a[76:79], a[20:23], v[172:175]\n"
      "ds_read_b64_tr_b16 a[106:107], v246 offset:36864\n"
      "ds_read_b64_tr_b16 a[108:109], v247 offset:32768\n"
      "v_mfma_f32_16x16x32_bf16 v[172:175], a[84:87], a[36:39], v[172:175]\n"
      "v_exp_f32 v115, v147\n"
      "v_mfma_f32_16x16x32_bf16 v[172:175], a[92:95], a[52:55], v[172:175]\n"
      "ds_read_b64_tr_b16 a[110:111], v247 offset:36864\n"
      "v_cvt_pk_bf16_f32 v201, v114, v115\n"
      "v_mfma_f32_16x16x32_bf16 v[168:171], a[88:91], a[52:55], v[228:231]\n"
      "v_exp_f32 v112, v148\n"
      "v_mfma_f32_16x16x32_bf16 v[168:171], a[80:83], a[36:39], v[168:171]\n"
      "ds_read_b64_tr_b16 a[112:113], v244 offset:33792\n"
      "v_or3_b32 v116, v116, v200, v201\n"
      "v_mfma_f32_16x16x32_bf16 v[168:171], a[72:75], a[20:23], v[168:171]\n"
      "v_exp_f32 v113, v149\n"
      "v_mfma_f32_16x16x32_bf16 v[168:171], a[64:67], a[4:7], v[168:171]\n"
      "ds_read_b64_tr_b16 a[114:115], v244 offset:37888\n"
      "v_cvt_pk_bf16_f32 v202, v112, v113\n"
      "v_mfma_f32_16x16x32_bf16 v[176:179], a[64:67], a[8:11], v[232:235]\n"
      "v_exp_f32 v114, v150\n"
      "v_mfma_f32_16x16x32_bf16 v[176:179], a[72:75], a[24:27], v[176:179]\n"
      "ds_read_b64_tr_b16 a[116:117], v245 offset:33792\n"
      "ds_read_b64_tr_b16 a[118:119], v245 offset:37888\n"
      "v_mfma_f32_16x16x32_bf16 v[176:179], a[80:83], a[40:43], v[176:179]\n"
      "v_exp_f32 v115, v151\n"
      "v_mfma_f32_16x16x32_bf16 v[176:179], a[88:91], a[56:59], v[176:179]\n"
      "ds_read_b64_tr_b16 a[120:121], v246 offset:33792\n"
      "v_cvt_pk_bf16_f32 v203, v114, v115\n"
      "v_mfma_f32_16x16x32_bf16 v[180:183], a[92:95], a[56:59], v[232:235]\n"
      "v_exp_f32 v112, v152\n"
      "v_mfma_f32_16x16x32_bf16 v[180:183], a[84:87], a[40:43], v[180:183]\n"
      "ds_read_b64_tr_b16 a[122:123], v246 offset:37888\n"
      "v_or3_b32 v116, v116, v202, v203\n"
      "v_mfma_f32_16x16x32_bf16 v[180:183], a[76:79], a[24:27], v[180:183]\n"
      "v_exp_f32 v113, v153\n"
      "v_mfma_f32_16x16x32_bf16 v[180:183], a[68:71], a[8:11], v[180:183]\n"
      "ds_read_b64_tr_b16 a[124:125], v247 offset:33792\n"
      "v_cvt_pk_bf16_f32 v204, v112, v113\n"
      "v_mfma_f32_16x16x32_bf16 v[188:191], a[68:71], a[12:15], v[236:239]\n"
      "v_exp_f32 v114, v154\n"
      "v_mfma_f32_16x16x32_bf16 v[188:191], a[76:79], a[28:31], v[188:191]\n"
      "ds_read_b64_tr_b16 a[126:127], v247 offset:37888\n"
      "v_mfma_f32_16x16x32_bf16 v[188:191], a[84:87], a[44:47], v[188:191]\n"
      "v_exp_f32 v115, v155\n"
      "v_mfma_f32_16x16x32_bf16 v[188:191], a[92:95], a[60:63], v[188:191]\n"
      "v_cvt_pk_bf16_f32 v205, v114, v115\n"
      "v_mfma_f32_16x16x32_bf16 v[184:187], a[88:91], a[60:63], v[236:239]\n"
      "v_exp_f32 v112, v156\n"
      "v_mfma_f32_16x16x32_bf16 v[184:187], a[80:83], a[44:47], v[184:187]\n"
      "v_or3_b32 v116, v116, v204, v205\n"
      "v_mfma_f32_16x16x32_bf16 v[184:187], a[72:75], a[28:31], v[184:187]\n"
      "v_exp_f32 v113, v157\n"
      "v_mfma_f32_16x16x32_bf16 v[184:187], a[64:67], a[12:15], v[184:187]\n"
      "s_waitcnt lgkmcnt(10)\n"
      "v_cvt_pk_bf16_f32 v206, v112, v113\n"
      "v_mfma_f32_16x16x32_bf16 v[32:35], a[96:99], v[208:211], v[32:35]\n"
      "v_exp_f32 v114, v158\n"
      "v_mfma_f32_16x16x32_bf16 v[48:51], a[100:103], v[208:211], v[48:51]\n"
      "v_mfma_f32_16x16x32_bf16 a[128:131], a[104:107], v[208:211], a[128:131]\n"
      "s_waitcnt lgkmcnt(6)\n"
      "v_exp_f32 v115, v159\n"
      "v_mfma_f32_16x16x32_bf16 a[144:147], a[108:111], v[208:211], a[144:147]\n"
      "v_cvt_pk_bf16_f32 v207, v114, v115\n"
      "v_mfma_f32_16x16x32_bf16 v[64:67], a[112:115], v[208:211], v[64:67]\n"
      "s_waitcnt lgkmcnt(2)\n"
      "v_mfma_f32_16x16x32_bf16 v[80:83], a[116:119], v[208:211], v[80:83]\n"
      "v_or3_b32 v116, v116, v206, v207\n"
      "v_mfma_f32_16x16x32_bf16 a[160:163], a[120:123], v[208:211], a[160:163]\n"
      "s_mov_b32 s64, 1\n"
      "s_waitcnt lgkmcnt(0)\n"
      "v_and_b32 v116, 0x40004000, v116\n"
      "v_mfma_f32_16x16x32_bf16 a[176:179], a[124:127], v[208:211], a[176:179]\n"
      "v_cmp_ne_u32 vcc, 0, v116\n"
      "s_cbranch_vccnz FB0_%=\n"
      "RET0_1_%=:\n"
      "v_mfma_f32_16x16x32_bf16 a[180:183], a[124:127], v[212:215], a[180:183]\n"
      "v_exp_f32 v112, v160\n"
      "v_mfma_f32_16x16x32_bf16 a[164:167], a[120:123], v[212:215], a[164:167]\n"
      "v_xor_b32 v242, 0x10000, v242\n"
      "v_mfma_f32_16x16x32_bf16 a[192:195], a[196:199], v[192:195], a[192:195]\n"
      "s_add_u32 m0, s77, 16384\n"
      "v_exp_f32 v113, v161\n"
      "v_mfma_f32_16x16x32_bf16 v[84:87], a[116:119], v[212:215], v[84:87]\n"
      "buffer_load_dwordx4 v249, s[24:27], s61 offen nt lds\n"
      "v_cvt_pk_bf16_f32 v208, v112, v113\n"
      "v_xor_b32 v243, 0x10000, v243\n"
      "v_mfma_f32_16x16x32_bf16 v[68:71], a[112:115], v[212:215], v[68:71]\n"
      "s_add_u32 m0, s77, 17408\n"
      "v_exp_f32 v114, v162\n"
      "v_mfma_f32_16x16x32_bf16 a[192:195], a[200:203], v[196:199], a[192:195]\n"
      "buffer_load_dwordx4 v121, s[24:27], s61 offen nt lds\n"
      "v_xor_b32 v244, 0x10000, v244\n"
      "v_mfma_f32_16x16x32_bf16 a[148:151], a[108:111], v[212:215], a[148:151]\n"
      "s_add_u32 m0, s77, 24576\n"
      "v_exp_f32 v115, v163\n"
      "v_mfma_f32_16x16x32_bf16 a[132:135], a[104:107], v[212:215], a[132:135]\n"
      "s_memrealtime s[68:69]\n"
      "buffer_load_dwordx4 v122, s[24:27], s61 offen nt lds\n"
      "v_cvt_pk_bf16_f32 v209, v114, v115\n"
      "v_xor_b32 v245, 0x10000, v245\n"
      "v_mfma_f32_16x16x32_bf16 a[192:195], a[204:207], v[200:203], a[192:195]\n"
      "s_add_u32 m0, s77, 25600\n"
      "v_exp_f32 v112, v164\n"
      "v_mfma_f32_16x16x32_bf16 v[52:55], a[100:103], v[212:215], v[52:55]\n"
      "s_add_u32 s77, s77, 0x8000\n"
      "buffer_load_dwordx4 v123, s[24:27], s61 offen nt lds\n"
      "v_or_b32 v116, v208, v209\n"
      "v_xor_b32 v246, 0x10000, v246\n"
      "v_mfma_f32_16x16x32_bf16 v[36:39], a[96:99], v[212:215], v[36:39]\n"
      "s_and_b32 s77, s77, 0x1ffff\n"
      "v_exp_f32 v113, v165\n"
      "v_mfma_f32_16x16x32_bf16 a[192:195], a[208:211], v[204:207], a[192:195]\n"
      "s_add_u32 s61, s61, s78\n"
      "v_cvt_pk_bf16_f32 v210, v112, v113\n"
      "v_xor_b32 v247, 0x10000, v247\n"
      "s_waitcnt vmcnt(8) lgkmcnt(0)\n"
      "s_sub_u32 s67, s68, s90\n"
      "s_cmp_lt_i32 s67, 0\n"
      "s_cbranch_scc1 GW3_%=\n"
      "GX3_%=:\n"
      "s_barrier\n"
      "v_mfma_f32_16x16x32_bf16 v[40:43], a[96:99], v[216:219], v[40:43]\n"
      "s_memrealtime s[68:69]\n"
      "buffer_load_dword v250, v251, s[28:31], 0 offen sc1\n"
      "ds_read_b128 a[64:67], v242 offset:0\n"
      "v_exp_f32 v114, v166\n"
      "v_mfma_f32_16x16x32_bf16 v[44:47], a[96:99], v[220:223], v[44:47]\n"
      "s_add_u32 m0, s62, 0\n"
      "ds_read_b128 a[68:71], v242 offset:4096\n"
      "ds_read_b128 a[72:75], v243 offset:0\n"
      "v_mfma_f32_16x16x32_bf16 v[60:63], a[100:103], v[220:223], v[60:63]\n"
      "buffer_load_dwordx4 v248, s[20:23], s60 offen nt lds\n"
      "v_exp_f32 v115, v167\n"
      "v_mfma_f32_16x16x32_bf16 v[56:59], a[100:103], v[216:219], v[56:59]\n"
      "s_add_u32 m0, s62, 896\n"
      "ds_read_b128 a[76:79], v243 offset:4096\n"
      "v_cvt_pk_bf16_f32 v211, v114, v115\n"
      "v_mfma_f32_16x16x32_bf16 a[136:139], a[104:107], v[216:219], a[136:139]\n"
      "buffer_load_dwordx4 v248, s[20:23], s60 offen offset:128 nt lds\n"
      "v_exp_f32 v112, v168\n"
      "v_mfma_f32_16x16x32_bf16 a[140:143], a[104:107], v[220:223], a[140:143]\n"
      "s_add_u32 m0, s62, 8192\n"
      "ds_read_b128 a[80:83], v242 offset:1024\n"
      "v_or3_b32 v116, v116, v210, v211\n"
      "v_mfma_f32_16x16x32_bf16 a[156:159], a[108:111], v[220:223], a[156:159]\n"
      "buffer_load_dwordx4 v120, s[20:23], s60 offen nt lds\n"
      "v_exp_f32 v113, v169\n"
      "v_mfma_f32_16x16x32_bf16 a[152:155], a[108:111], v[216:219], a[152:155]\n"
      "s_add_u32 m0, s62, 9088\n"
      "ds_read_b128 a[84:87], v242 offset:5120\n"
      "v_cvt_pk_bf16_f32 v212, v112, v113\n"
      "v_mfma_f32_16x16x32_bf16 v[72:75], a[112:115], v[216:219], v[72:75]\n"
      "s_add_u32 s62, s62, 0x8000\n"
      "buffer_load_dwordx4 v120, s[20:23], s60 offen offset:128 nt lds\n"
      "v_exp_f32 v114, v170\n"
      "v_mfma_f32_16x16x32_bf16 v[76:79], a[112:115], v[220:223], v[76:79]\n"
      "s_and_b32 s62, s62, 0x1ffff\n"
      "ds_read_b128 a[88:91], v243 offset:1024\n"
      "ds_read_b128 a[92:95], v243 offset:5120\n"
      "v_mfma_f32_16x16x32_bf16 v[92:95], a[116:119], v[220:223], v[92:95]\n"
      "s_add_u32 s60, s60, %[inc]\n"
      "v_exp_f32 v115, v171\n"
      "v_mfma_f32_16x16x32_bf16 v[88:91], a[116:119], v[216:219], v[88:91]\n"
      "v_cvt_pk_bf16_f32 v213, v114, v115\n"
      "v_mfma_f32_16x16x32_bf16 a[168:171], a[120:123], v[216:219], a[168:171]\n"
      "v_exp_f32 v112, v172\n"
      "v_mfma_f32_16x16x32_bf16 a[172:175], a[120:123], v[220:223], a[172:175]\n"
      "v_or3_b32 v116, v116, v212, v213\n"
      "v_mfma_f32_16x16x32_bf16 a[188:191], a[124:127], v[220:223], a[188:191]\n"
      "v_exp_f32 v113, v173\n"
      "v_mfma_f32_16x16x32_bf16 a[184:187], a[124:127], v[216:219], a[184:187]\n"
      "s_waitcnt lgkmcnt(0)\n"
      "v_cvt_pk_bf16_f32 v214, v112, v113\n"
      "v_mfma_f32_16x16x32_bf16 v[128:131], a[64:67], a[0:3], v[224:227]\n"
      "s_sub_u32 s67, s68, s66\n"
      "v_exp_f32 v114, v174\n"
      "v_mfma_f32_16x16x32_bf16 v[128:131], a[72:75], a[16:19], v[128:131]\n"
      "s_cmp_gt_i32 s67, s70\n"
      "ds_read_b64_tr_b16 a[96:97], v244 offset:0\n"
      "ds_read_b64_tr_b16 a[98:99], v244 offset:4096\n"
      "v_mfma_f32_16x16x32_bf16 v[128:131], a[80:83], a[32:35], v[128:131]\n"
      "s_cselect_b32 s69, s86, 0\n"
      "v_exp_f32 v115, v175\n"
      "v_mfma_f32_16x16x32_bf16 v[128:131], a[88:91], a[48:51], v[128:131]\n"
      "s_add_u32 s66, s66, s69\n"
      "ds_read_b64_tr_b16 a[100:101], v245 offset:0\n"
      "v_cvt_pk_bf16_f32 v215, v114, v115\n"
      "v_mfma_f32_16x16x32_bf16 v[132:135], a[92:95], a[48:51], v[224:227]\n"
      "s_add_u32 s67, s67, s101\n"
      "v_exp_f32 v112, v176\n"
      "v_mfma_f32_16x16x32_bf16 v[132:135], a[84:87], a[32:35], v[132:135]\n"
      "s_min_i32 s70, s70, s67\n"
      "ds_read_b64_tr_b16 a[102:103], v245 offset:4096\n"
      "v_or3_b32 v116, v116, v214, v215\n"
      "v_mfma_f32_16x16x32_bf16 v[132:135], a[76:79], a[16:19], v[132:135]\n"
      "s_max_i32 s70, s70, 85\n"
      "v_exp_f32 v113, v177\n"
      "v_mfma_f32_16x16x32_bf16 v[132:135], a[68:71], a[0:3], v[132:135]\n"
      "s_add_u32 s66, s66, s86\n"
      "ds_read_b64_tr_b16 a[104:105], v246 offset:0\n"
      "v_cvt_pk_bf16_f32 v216, v112, v113\n"
      "v_mfma_f32_16x16x32_bf16 v[140:143], a[68:71], a[4:7], v[228:231]\n"
      "s_and_b32 s76, s80, 31\n"
      "v_exp_f32 v114, v178\n"
      "v_mfma_f32_16x16x32_bf16 v[140:143], a[76:79], a[20:23], v[140:143]\n"
      "s_lshr_b64 s[80:81], s[80:81], 5\n"
      "ds_read_b64_tr_b16 a[106:107], v246 offset:4096\n"
      "ds_read_b64_tr_b16 a[108:109], v247 offset:0\n"
      "v_mfma_f32_16x16x32_bf16 v[140:143], a[84:87], a[36:39], v[140:143]\n"
      "s_add_u32 s66, s66, s76\n"
      "v_exp_f32 v115, v179\n"
      "v_mfma_f32_16x16x32_bf16 v[140:143], a[92:95], a[52:55], v[140:143]\n"
      "s_sub_u32 s67, s68, s82\n"
      "ds_read_b64_tr_b16 a[110:111], v247 offset:4096\n"
      "v_cvt_pk_bf16_f32 v217, v114, v115\n"
      "v_mfma_f32_16x16x32_bf16 v[136:139], a[88:91], a[52:55], v[228:231]\n"
      "s_min_i32 s76, s67, s83\n"
      "v_exp_f32 v112, v180\n"
      "v_mfma_f32_16x16x32_bf16 v[136:139], a[80:83], a[36:39], v[136:139]\n"
      "s_mov_b32 s83, s67\n"
      "ds_read_b64_tr_b16 a[112:113], v244 offset:1024\n"
      "v_or3_b32 v116, v116, v216, v217\n"
      "v_mfma_f32_16x16x32_bf16 v[136:139], a[72:75], a[20:23], v[136:139]\n"
      "s_sub_u32 s76, s76, 36\n"
      "v_exp_f32 v113, v181\n"
      "v_mfma_f32_16x16x32_bf16 v[136:139], a[64:67], a[4:7], v[136:139]\n"
      "s_min_i32 s76, s76, 148\n"
      "ds_read_b64_tr_b16 a[114:115], v244 offset:5120\n"
      "v_cvt_pk_bf16_f32 v218, v112, v113\n"
      "v_mfma_f32_16x16x32_bf16 v[144:147], a[64:67], a[8:11], v[232:235]\n"
      "s_max_i32 s76, s76, 70\n"
      "v_exp_f32 v114, v182\n"
      "v_mfma_f32_16x16x32_bf16 v[144:147], a[72:75], a[24:27], v[144:147]\n"
      "s_mov_b32 s82, s68\n"
      "ds_read_b64_tr_b16 a[116:117], v245 offset:1024\n"
      "ds_read_b64_tr_b16 a[118:119], v245 offset:5120\n"
      "v_mfma_f32_16x16x32_bf16 v[144:147], a[80:83], a[40:43], v[144:147]\n"
      "s_lshr_b32 s69, s86, 1\n"
      "v_exp_f32 v115, v183\n"
      "v_mfma_f32_16x16x32_bf16 v[144:147], a[88:91], a[56:59], v[144:147]\n"
      "s_add_u32 s66, s66, s69\n"
      "ds_read_b64_tr_b16 a[120:121], v246 offset:1024\n"
      "v_cvt_pk_bf16_f32 v219, v114, v115\n"
      "v_mfma_f32_16x16x32_bf16 v[148:151], a[92:95], a[56:59], v[232:235]\n"
      "s_add_u32 s82, s82, s69\n"
      "v_exp_f32 v112, v184\n"
      "v_mfma_f32_16x16x32_bf16 v[148:151], a[84:87], a[40:43], v[148:151]\n"
      "s_add_u32 s65, s82, s76\n"
      "ds_read_b64_tr_b16 a[122:123], v246 offset:5120\n"
      "v_or3_b32 v116, v116, v218, v219\n"
      "v_mfma_f32_16x16x32_bf16 v[148:151], a[76:79], a[24:27], v[148:151]\n"
      "s_sub_u32 s93, s93, 1\n"
      "v_exp_f32 v113, v185\n"
      "v_mfma_f32_16x16x32_bf16 v[148:151], a[68:71], a[8:11], v[148:151]\n"
      "s_cmp_le_i32 s93, 14\n"
      "ds_read_b64_tr_b16 a[124:125], v247 offset:1024\n"
      "v_cvt_pk_bf16_f32 v220, v112, v113\n"
      "v_mfma_f32_16x16x32_bf16 v[156:159], a[68:71], a[12:15], v[236:239]\n"
      "s_cselect_b32 s86, 107, s86\n"
      "v_exp_f32 v114, v186\n"
      "v_mfma_f32_16x16x32_bf16 v[156:159], a[76:79], a[28:31], v[156:159]\n"
      "s_sub_u32 s67, s65, s66\n"
      "ds_read_b64_tr_b16 a[126:127], v247 offset:5120\n"
      "v_mfma_f32_16x16x32_bf16 v[156:159], a[84:87], a[44:47], v[156:159]\n"
      "s_cmp_lt_i32 s67, 0\n"
      "v_exp_f32 v115, v187\n"
      "v_mfma_f32_16x16x32_bf16 v[156:159], a[92:95], a[60:63], v[156:159]\n"
      "s_cselect_b32 s88, s66, s65\n"
      "v_cvt_pk_bf16_f32 v221, v114, v115\n"
      "v_mfma_f32_16x16x32_bf16 v[152:155], a[88:91], a[60:63], v[236:239]\n"
      "s_add_u32 s90, s88, s91\n"
      "v_exp_f32 v112, v188\n"
      "v_mfma_f32_16x16x32_bf16 v[152:155], a[80:83], a[44:47], v[152:155]\n"
      "v_or3_b32 v116, v116, v220, v221\n"
      "v_mfma_f32_16x16x32_bf16 v[152:155], a[72:75], a[28:31], v[152:155]\n"
      "v_exp_f32 v113, v189\n"
      "v_mfma_f32_16x16x32_bf16 v[152:155], a[64:67], a[12:15], v[152:155]\n"
      "s_waitcnt lgkmcnt(10)\n"
      "v_cvt_pk_bf16_f32 v222, v112, v113\n"
      "v_mfma_f32_16x16x32_bf16 v[32:35], a[96:99], v[192:195], v[32:35]\n"
      "v_exp_f32 v114, v190\n"
      "v_mfma_f32_16x16x32_bf16 v[48:51], a[100:103], v[192:195], v[48:51]\n"
      "v_mfma_f32_16x16x32_bf16 a[128:131], a[104:107], v[192:195], a[128:131]\n"
      "s_waitcnt lgkmcnt(6)\n"
      "v_exp_f32 v115, v191\n"
      "v_mfma_f32_16x16x32_bf16 a[144:147], a[108:111], v[192:195], a[144:147]\n"
      "v_cvt_pk_bf16_f32 v223, v114, v115\n"
      "v_mfma_f32_16x16x32_bf16 v[64:67], a[112:115], v[192:195], v[64:67]\n"
      "s_waitcnt lgkmcnt(2)\n"
      "v_mfma_f32_16x16x32_bf16 v[80:83], a[116:119], v[192:195], v[80:83]\n"
      "v_or3_b32 v116, v116, v222, v223\n"
      "v_mfma_f32_16x16x32_bf16 a[160:163], a[120:123], v[192:195], a[160:163]\n"
      "s_mov_b32 s64, 2\n"
      "s_waitcnt lgkmcnt(0)\n"
      "v_and_b32 v116, 0x40004000, v116\n"
      "v_mfma_f32_16x16x32_bf16 a[176:179], a[124:127], v[192:195], a[176:179]\n"
      "v_cmp_ne_u32 vcc, 0, v116\n"
      "s_cbranch_vccnz FB1_%=\n"
      "RET1_2_%=:\n"
      "v_mfma_f32_16x16x32_bf16 a[180:183], a[124:127], v[196:199], a[180:183]\n"
      "v_exp_f32 v112, v128\n"
      "v_mfma_f32_16x16x32_bf16 a[164:167], a[120:123], v[196:199], a[164:167]\n"
      "v_mfma_f32_16x16x32_bf16 a[192:195], a[196:199], v[208:211], a[192:195]\n"
      "s_add_u32 m0, s77, 16384\n"
      "v_exp_f32 v113, v129\n"
      "v_mfma_f32_16x16x32_bf16 v[84:87], a[116:119], v[196:199], v[84:87]\n"
      "buffer_load_dwordx4 v249, s[24:27], s61 offen nt lds\n"
      "v_cvt_pk_bf16_f32 v192, v112, v113\n"
      "v_mfma_f32_16x16x32_bf16 v[68:71], a[112:115], v[196:199], v[68:71]\n"
      "s_add_u32 m0, s77, 17408\n"
      "v_exp_f32 v114, v130\n"
      "v_mfma_f32_16x16x32_bf16 a[192:195], a[200:203], v[212:215], a[192:195]\n"
      "buffer_load_dwordx4 v121, s[24:27], s61 offen nt lds\n"
      "v_mfma_f32_16x16x32_bf16 a[148:151], a[108:111], v[196:199], a[148:151]\n"
      "s_add_u32 m0, s77, 24576\n"
      "v_exp_f32 v115, v131\n"
      "v_mfma_f32_16x16x32_bf16 a[132:135], a[104:107], v[196:199], a[132:135]\n"
      "s_memrealtime s[68:69]\n"
      "buffer_load_dwordx4 v122, s[24:27], s61 offen nt lds\n"
      "v_cvt_pk_bf16_f32 v193, v114, v115\n"
      "v_mfma_f32_16x16x32_bf16 a[192:195], a[204:207], v[216:219], a[192:195]\n"
      "s_add_u32 m0, s77, 25600\n"
      "v_exp_f32 v112, v132\n"
      "v_mfma_f32_16x16x32_bf16 v[52:55], a[100:103], v[196:199], v[52:55]\n"
      "s_add_u32 s77, s77, 0x8000\n"
      "buffer_load_dwordx4 v123, s[24:27], s61 offen nt lds\n"
      "v_or_b32 v116, v192, v193\n"
      "v_mfma_f32_16x16x32_bf16 v[36:39], a[96:99], v[196:199], v[36:39]\n"
      "s_and_b32 s77, s77, 0x1ffff\n"
      "v_exp_f32 v113, v133\n"
      "v_mfma_f32_16x16x32_bf16 a[192:195], a[208:211], v[220:223], a[192:195]\n"
      "s_add_u32 s61, s61, %[inc]\n"
      "v_cvt_pk_bf16_f32 v194, v112, v113\n"
      "s_waitcnt vmcnt(9) lgkmcnt(0)\n"
      "s_barrier\n"
      "v_mfma_f32_16x16x32_bf16 v[40:43], a[96:99], v[200:203], v[40:43]\n"
      "ds_read_b128 a[64:67], v242 offset:32768\n"
      "v_exp_f32 v114, v134\n"
      "v_mfma_f32_16x16x32_bf16 v[44:47], a[96:99], v[204:207], v[44:47]\n"
      "ds_read_b128 a[68:71], v242 offset:36864\n"
      "ds_read_b128 a[72:75], v243 offset:32768\n"
      "v_mfma_f32_16x16x32_bf16 v[60:63], a[100:103], v[204:207], v[60:63]\n"
      "v_exp_f32 v115, v135\n"
      "v_mfma_f32_16x16x32_bf16 v[56:59], a[100:103], v[200:203], v[56:59]\n"
      "ds_read_b128 a[76:79], v243 offset:36864\n"
      "v_cvt_pk_bf16_f32 v195, v114, v115\n"
      "v_mfma_f32_16x16x32_bf16 a[136:139], a[104:107], v[200:203], a[136:139]\n"
      "v_exp_f32 v112, v136\n"
      "v_mfma_f32_16x16x32_bf16 a[140:143], a[104:107], v[204:207], a[140:143]\n"
      "ds_read_b128 a[80:83], v242 offset:33792\n"
      "v_or3_b32 v116, v116, v194, v195\n"
      "v_mfma_f32_16x16x32_bf16 a[156:159], a[108:111], v[204:207], a[156:159]\n"
      "v_exp_f32 v113, v137\n"
      "v_mfma_f32_16x16x32_bf16 a[152:155], a[108:111], v[200:203], a[152:155]\n"
      "ds_read_b128 a[84:87], v242 offset:37888\n"
      "v_cvt_pk_bf16_f32 v196, v112, v113\n"
      "v_mfma_f32_16x16x32_bf16 v[72:75], a[112:115], v[200:203], v[72:75]\n"
      "v_exp_f32 v114, v138\n"
      "v_mfma_f32_16x16x32_bf16 v[76:79], a[112:115], v[204:207], v[76:79]\n"
      "ds_read_b128 a[88:91], v243 offset:33792\n"
      "ds_read_b128 a[92:95], v243 offset:37888\n"
      "v_mfma_f32_16x16x32_bf16 v[92:95], a[116:119], v[204:207], v[92:95]\n"
      "v_exp_f32 v115, v139\n"
      "v_mfma_f32_16x16x32_bf16 v[88:91], a[116:119], v[200:203], v[88:91]\n"
      "v_cvt_pk_bf16_f32 v197, v114, v115\n"
      "v_mfma_f32_16x16x32_bf16 a[168:171], a[120:123], v[200:203], a[168:171]\n"
      "v_exp_f32 v112, v140\n"
      "v_mfma_f32_16x16x32_bf16 a[172:175], a[120:123], v[204:207], a[172:175]\n"
      "v_or3_b32 v116, v116, v196, v197\n"
      "v_mfma_f32_16x16x32_bf16 a[188:191], a[124:127], v[204:207], a[188:191]\n"
      "v_exp_f32 v113, v141\n"
      "v_mfma_f32_16x16x32_bf16 a[184:187], a[124:127], v[200:203], a[184:187]\n"
      "s_waitcnt lgkmcnt(0)\n"
      "v_cvt_pk_bf16_f32 v198, v112, v113\n"
      "v_mfma_f32_16x16x32_bf16 v[160:163], a[64:67], a[0:3], v[224:227]\n"
      "v_exp_f32 v114, v142\n"
      "v_mfma_f32_16x16x32_bf16 v[160:163], a[72:75], a[16:19], v[160:163]\n"
      "ds_read_b64_tr_b16 a[96:97], v244 offset:32768\n"
      "ds_read_b64_tr_b16 a[98:99], v244 offset:36864\n"
      "v_mfma_f32_16x16x32_bf16 v[160:163], a[80:83], a[32:35], v[160:163]\n"
      "v_exp_f32 v115, v143\n"
      "v_mfma_f32_16x16x32_bf16 v[160:163], a[88:91], a[48:51], v[160:163]\n"
      "ds_read_b64_tr_b16 a[100:101], v245 offset:32768\n"
      "v_cvt_pk_bf16_f32 v199, v114, v115\n"
      "v_mfma_f32_16x16x32_bf16 v[164:167], a[92:95], a[48:51], v[224:227]\n"
      "v_exp_f32 v112, v144\n"
      "v_mfma_f32_16x16x32_bf16 v[164:167], a[84:87], a[32:35], v[164:167]\n"
      "ds_read_b64_tr_b16 a[102:103], v245 offset:36864\n"
      "v_or3_b32 v116, v116, v198, v199\n"
      "v_mfma_f32_16x16x32_bf16 v[164:167], a[76:79], a[16:19], v[164:167]\n"
      "v_exp_f32 v113, v145\n"
      "v_mfma_f32_16x16x32_bf16 v[164:167], a[68:71], a[0:3], v[164:167]\n"
      "ds_read_b64_tr_b16 a[104:105], v246 offset:32768\n"
      "v_cvt_pk_bf16_f32 v200, v112, v113\n"
      "v_mfma_f32_16x16x32_bf16 v[172:175], a[68:71], a[4:7], v[228:231]\n"
      "v_exp_f32 v114, v146\n"
      "v_mfma_f32_16x16x32_bf16 v[172:175], a[76:79], a[20:23], v[172:175]\n"
      "ds_read_b64_tr_b16 a[106:107], v246 offset:36864\n"
      "ds_read_b64_tr_b16 a[108:109], v247 offset:32768\n"
      "v_mfma_f32_16x16x32_bf16 v[172:175], a[84:87], a[36:39], v[172:175]\n"
      "v_exp_f32 v115, v147\n"
      "v_mfma_f32_16x16x32_bf16 v[172:175], a[92:95], a[52:55], v[172:175]\n"
      "ds_read_b64_tr_b16 a[110:111], v247 offset:36864\n"
      "v_cvt_pk_bf16_f32 v201, v114, v115\n"
      "v_mfma_f32_16x16x32_bf16 v[168:171], a[88:91], a[52:55], v[228:231]\n"
      "v_exp_f32 v112, v148\n"
      "v_mfma_f32_16x16x32_bf16 v[168:171], a[80:83], a[36:39], v[168:171]\n"
      "ds_read_b64_tr_b16 a[112:113], v244 offset:33792\n"
      "v_or3_b32 v116, v116, v200, v201\n"
      "v_mfma_f32_16x16x32_bf16 v[168:171], a[72:75], a[20:23], v[168:171]\n"
      "v_exp_f32 v113, v149\n"
      "v_mfma_f32_16x16x32_bf16 v[168:171], a[64:67], a[4:7], v[168:171]\n"
      "ds_read_b64_tr_b16 a[114:115], v244 offset:37888\n"
      "v_cvt_pk_bf16_f32 v202, v112, v113\n"
      "v_mfma_f32_16x16x32_bf16 v[176:179], a[64:67], a[8:11], v[232:235]\n"
      "v_exp_f32 v114, v150\n"
      "v_mfma_f32_16x16x32_bf16 v[176:179], a[72:75], a[24:27], v[176:179]\n"
      "ds_read_b64_tr_b16 a[116:117], v245 offset:33792\n"
      "ds_read_b64_tr_b16 a[118:119], v245 offset:37888\n"
      "v_mfma_f32_16x16x32_bf16 v[176:179], a[80:83], a[40:43], v[176:179]\n"
      "v_exp_f32 v115, v151\n"
      "v_mfma_f32_16x16x32_bf16 v[176:179], a[88:91], a[56:59], v[176:179]\n"
      "ds_read_b64_tr_b16 a[120:121], v246 offset:33792\n"
      "v_cvt_pk_bf16_f32 v203, v114, v115\n"
      "v_mfma_f32_16x16x32_bf16 v[180:183], a[92:95], a[56:59], v[232:235]\n"
      "v_exp_f32 v112, v152\n"
      "v_mfma_f32_16x16x32_bf16 v[180:183], a[84:87], a[40:43], v[180:183]\n"
      "ds_read_b64_tr_b16 a[122:123], v246 offset:37888\n"
      "v_or3_b32 v116, v116, v202, v203\n"
      "v_mfma_f32_16x16x32_bf16 v[180:183], a[76:79], a[24:27], v[180:183]\n"
      "v_exp_f32 v113, v153\n"
      "v_mfma_f32_16x16x32_bf16 v[180:183], a[68:71], a[8:11], v[180:183]\n"
      "ds_read_b64_tr_b16 a[124:125], v247 offset:33792\n"
      "v_cvt_pk_bf16_f32 v204, v112, v113\n"
      "v_mfma_f32_16x16x32_bf16 v[188:191], a[68:71], a[12:15], v[236:239]\n"
      "v_exp_f32 v114, v154\n"
      "v_mfma_f32_16x16x32_bf16 v[188:191], a[76:79], a[28:31], v[188:191]\n"
      "ds_read_b64_tr_b16 a[126:127], v247 offset:37888\n"
      "v_mfma_f32_16x16x32_bf16 v[188:191], a[84:87], a[44:47], v[188:191]\n"
      "v_exp_f32 v115, v155\n"
      "v_mfma_f32_16x16x32_bf16 v[188:191], a[92:95], a[60:63], v[188:191]\n"
      "v_cvt_pk_bf16_f32 v205, v114, v115\n"
      "v_mfma_f32_16x16x32_bf16 v[184:187], a[88:91], a[60:63], v[236:239]\n"
      "v_exp_f32 v112, v156\n"
      "v_mfma_f32_16x16x32_bf16 v[184:187], a[80:83], a[44:47], v[184:187]\n"
      "v_or3_b32 v116, v116, v204, v205\n"
      "v_mfma_f32_16x16x32_bf16 v[184:187], a[72:75], a[28:31], v[184:187]\n"
      "v_exp_f32 v113, v157\n"
      "v_mfma_f32_16x16x32_bf16 v[184:187], a[64:67], a[12:15], v[184:187]\n"
      "s_waitcnt lgkmcnt(10)\n"
      "v_cvt_pk_bf16_f32 v206, v112, v113\n"
      "v_mfma_f32_16x16x32_bf16 v[32:35], a[96:99], v[208:211], v[32:35]\n"
      "v_exp_f32 v114, v158\n"
      "v_mfma_f32_16x16x32_bf16 v[48:51], a[100:103], v[208:211], v[48:51]\n"
      "v_mfma_f32_16x16x32_bf16 a[128:131], a[104:107], v[208:211], a[128:131]\n"
      "s_waitcnt lgkmcnt(6)\n"
      "v_exp_f32 v115, v159\n"
      "v_mfma_f32_16x16x32_bf16 a[144:147], a[108:111], v[208:211], a[144:147]\n"
      "v_cvt_pk_bf16_f32 v207, v114, v115\n"
      "v_mfma_f32_16x16x32_bf16 v[64:67], a[112:115], v[208:211], v[64:67]\n"
      "s_waitcnt lgkmcnt(2)\n"
      "v_mfma_f32_16x16x32_bf16 v[80:83], a[116:119], v[208:211], v[80:83]\n"
      "v_or3_b32 v116, v116, v206, v207\n"
      "v_mfma_f32_16x16x32_bf16 a[160:163], a[120:123], v[208:211], a[160:163]\n"
      "s_mov_b32 s64, 2\n"
      "s_waitcnt lgkmcnt(0)\n"
      "v_and_b32 v116, 0x40004000, v116\n"
      "v_mfma_f32_16x16x32_bf16 a[176:179], a[124:127], v[208:211], a[176:179]\n"
      "v_cmp_ne_u32 vcc, 0, v116\n"
      "s_cbranch_vccnz FB0_%=\n"
      "RET0_2_%=:\n"
      "v_mfma_f32_16x16x32_bf16 a[180:183], a[124:127], v[212:215], a[180:183]\n"
      "v_exp_f32 v112, v160\n"
      "v_mfma_f32_16x16x32_bf16 a[164:167], a[120:123], v[212:215], a[164:167]\n"
      "v_xor_b32 v242, 0x10000, v242\n"
      "v_mfma_f32_16x16x32_bf16 a[192:195], a[196:199], v[192:195], a[192:195]\n"
      "s_add_u32 m0, s62, 0\n"
      "v_exp_f32 v113, v161\n"
      "v_mfma_f32_16x16x32_bf16 v[84:87], a[116:119], v[212:215], v[84:87]\n"
      "buffer_load_dwordx4 v248, s[20:23], s60 offen nt lds\n"
      "v_cvt_pk_bf16_f32 v208, v112, v113\n"
      "v_xor_b32 v243, 0x10000, v243\n"
      "v_mfma_f32_16x16x32_bf16 v[68:71], a[112:115], v[212:215], v[68:71]\n"
      "s_add_u32 m0, s62, 896\n"
      "v_exp_f32 v114, v162\n"
      "v_mfma_f32_16x16x32_bf16 a[192:195], a[200:203], v[196:199], a[192:195]\n"
      "buffer_load_dwordx4 v248, s[20:23], s60 offen offset:128 nt lds\n"
      "v_xor_b32 v244, 0x10000, v244\n"
      "v_mfma_f32_16x16x32_bf16 a[148:151], a[108:111], v[212:215], a[148:151]\n"
      "s_add_u32 m0, s62, 8192\n"
      "v_exp_f32 v115, v163\n"
      "v_mfma_f32_16x16x32_bf16 a[132:135], a[104:107], v[212:215], a[132:135]\n"
      "s_memrealtime s[68:69]\n"
      "buffer_load_dwordx4 v120, s[20:23], s60 offen nt lds\n"
      "v_cvt_pk_bf16_f32 v209, v114, v115\n"
      "v_xor_b32 v245, 0x10000, v245\n"
      "v_mfma_f32_16x16x32_bf16 a[192:195], a[204:207], v[200:203], a[192:195]\n"
      "s_add_u32 m0, s62, 9088\n"
      "v_exp_f32 v112, v164\n"
      "v_mfma_f32_16x16x32_bf16 v[52:55], a[100:103], v[212:215], v[52:55]\n"
      "s_add_u32 s62, s62, 0x8000\n"
      "buffer_load_dwordx4 v120, s[20:23], s60 offen offset:128 nt lds\n"
      "v_or_b32 v116, v208, v209\n"
      "v_xor_b32 v246, 0x10000, v246\n"
      "v_mfma_f32_16x16x32_bf16 v[36:39], a[96:99], v[212:215], v[36:39]\n"
      "s_and_b32 s62, s62, 0x1ffff\n"
      "v_exp_f32 v113, v165\n"
      "v_mfma_f32_16x16x32_bf16 a[192:195], a[208:211], v[204:207], a[192:195]\n"
      "s_add_u32 s60, s60, s78\n"
      "v_cvt_pk_bf16_f32 v210, v112, v113\n"
      "v_xor_b32 v247, 0x10000, v247\n"
      "s_waitcnt vmcnt(8)\n"
      "s_sub_u32 s63, 11, %[n1]\n"
      "LOOP2_%=:\n"
      "s_waitcnt vmcnt(9) lgkmcnt(0)\n"
      "s_sub_u32 s67, s68, s90\n"
      "s_cmp_lt_i32 s67, 0\n"
      "s_cbranch_scc1 GW4_%=\n"
      "GX4_%=:\n"
      "s_barrier\n"
      "v_mfma_f32_16x16x32_bf16 v[40:43], a[96:99], v[216:219], v[40:43]\n"
      "s_memrealtime s[68:69]\n"
      "ds_read_b128 a[64:67], v242 offset:0\n"
      "v_exp_f32 v114, v166\n"
      "v_mfma_f32_16x16x32_bf16 v[44:47], a[96:99], v[220:223], v[44:47]\n"
      "s_add_u32 m0, s77, 16384\n"
      "ds_read_b128 a[68:71], v242 offset:4096\n"
      "ds_read_b128 a[72:75], v243 offset:0\n"
      "v_mfma_f32_16x16x32_bf16 v[60:63], a[100:103], v[220:223], v[60:63]\n"
      "buffer_load_dwordx4 v249, s[24:27], s61 offen nt lds\n"
      "v_exp_f32 v115, v167\n"
      "v_mfma_f32_16x16x32_bf16 v[56:59], a[100:103], v[216:219], v[56:59]\n"
      "s_add_u32 m0, s77, 17408\n"
      "ds_read_b128 a[76:79], v243 offset:4096\n"
      "v_cvt_pk_bf16_f32 v211, v114, v115\n"
      "v_mfma_f32_16x16x32_bf16 a[136:139], a[104:107], v[216:219], a[136:139]\n"
      "buffer_load_dwordx4 v121, s[24:27], s61 offen nt lds\n"
      "v_exp_f32 v112, v168\n"
      "v_mfma_f32_16x16x32_bf16 a[140:143], a[104:107], v[220:223], a[140:143]\n"
      "s_add_u32 m0, s77, 24576\n"
      "ds_read_b128 a[80:83], v242 offset:1024\n"
      "v_or3_b32 v116, v116, v210, v211\n"
      "v_mfma_f32_16x16x32_bf16 a[156:159], a[108:111], v[220:223], a[156:159]\n"
      "buffer_load_dwordx4 v122, s[24:27], s61 offen nt lds\n"
      "v_exp_f32 v113, v169\n"
      "v_mfma_f32_16x16x32_bf16 a[152:155], a[108:111], v[216:219], a[152:155]\n"
      "s_add_u32 m0, s77, 25600\n"
      "ds_read_b128 a[84:87], v242 offset:5120\n"
      "v_cvt_pk_bf16_f32 v212, v112, v113\n"
      "v_mfma_f32_16x16x32_bf16 v[72:75], a[112:115], v[216:219], v[72:75]\n"
      "s_add_u32 s77, s77, 0x8000\n"
      "buffer_load_dwordx4 v123, s[24:27], s61 offen nt lds\n"
      "v_exp_f32 v114, v170\n"
      "v_mfma_f32_16x16x32_bf16 v[76:79], a[112:115], v[220:223], v[76:79]\n"
      "s_and_b32 s77, s77, 0x1ffff\n"
      "ds_read_b128 a[88:91], v243 offset:1024\n"
      "ds_read_b128 a[92:95], v243 offset:5120\n"
      "v_mfma_f32_16x16x32_bf16 v[92:95], a[116:119], v[220:223], v[92:95]\n"
      "s_add_u32 s61, s61, s78\n"
      "v_exp_f32 v115, v171\n"
      "v_mfma_f32_16x16x32_bf16 v[88:91], a[116:119], v[216:219], v[88:91]\n"
      "v_cvt_pk_bf16_f32 v213, v114, v115\n"
      "v_mfma_f32_16x16x32_bf16 a[168:171], a[120:123], v[216:219], a[168:171]\n"
      "v_exp_f32 v112, v172\n"
      "v_mfma_f32_16x16x32_bf16 a[172:175], a[120:123], v[220:223], a[172:175]\n"
      "v_or3_b32 v116, v116, v212, v213\n"
      "v_mfma_f32_16x16x32_bf16 a[188:191], a[124:127], v[220:223], a[188:191]\n"
      "v_exp_f32 v113, v173\n"
      "v_mfma_f32_16x16x32_bf16 a[184:187], a[124:127], v[216:219], a[184:187]\n"
      "s_waitcnt lgkmcnt(0)\n"
      "v_cvt_pk_bf16_f32 v214, v112, v113\n"
      "v_mfma_f32_16x16x32_bf16 v[128:131], a[64:67], a[0:3], v[224:227]\n"
      "s_sub_u32 s67, s68, s66\n"
      "v_exp_f32 v114, v174\n"
      "v_mfma_f32_16x16x32_bf16 v[128:131], a[72:75], a[16:19], v[128:131]\n"
      "s_cmp_gt_i32 s67, s70\n"
      "v_mfma_f32_16x16x32_bf16 v[128:131], a[80:83], a[32:35], v[128:131]\n"
      "s_cselect_b32 s69, s86, 0\n"
      "v_exp_f32 v115, v175\n"
      "v_mfma_f32_16x16x32_bf16 v[128:131], a[88:91], a[48:51], v[128:131]\n"
      "s_add_u32 s66, s66, s69\n"
      "v_cvt_pk_bf16_f32 v215, v114, v115\n"
      "v_mfma_f32_16x16x32_bf16 v[132:135], a[92:95], a[48:51], v[224:227]\n"
      "s_add_u32 s67, s67, s101\n"
      "v_exp_f32 v112, v176\n"
      "v_mfma_f32_16x16x32_bf16 v[132:135], a[84:87], a[32:35], v[132:135]\n"
      "s_min_i32 s70, s70, s67\n"
      "v_or3_b32 v116, v116, v214, v215\n"
      "v_mfma_f32_16x16x32_bf16 v[132:135], a[76:79], a[16:19], v[132:135]\n"
      "s_max_i32 s70, s70, 85\n"
      "v_exp_f32 v113, v177\n"
      "v_mfma_f32_16x16x32_bf16 v[132:135], a[68:71], a[0:3], v[132:135]\n"
      "s_add_u32 s66, s66, s86\n"
      "v_cvt_pk_bf16_f32 v216, v112, v113\n"
      "v_mfma_f32_16x16x32_bf16 v[140:143], a[68:71], a[4:7], v[228:231]\n"
      "s_and_b32 s76, s80, 31\n"
      "v_exp_f32 v114, v178\n"
      "v_mfma_f32_16x16x32_bf16 v[140:143], a[76:79], a[20:23], v[140:143]\n"
      "s_lshr_b64 s[80:81], s[80:81], 5\n"
      "v_mfma_f32_16x16x32_bf16 v[140:143], a[84:87], a[36:39], v[140:143]\n"
      "s_add_u32 s66, s66, s76\n"
      "v_exp_f32 v115, v179\n"
      "v_mfma_f32_16x16x32_bf16 v[140:143], a[92:95], a[52:55], v[140:143]\n"
      "s_sub_u32 s67, s68, s82\n"
      "v_cvt_pk_bf16_f32 v217, v114, v115\n"
      "v_mfma_f32_16x16x32_bf16 v[136:139], a[88:91], a[52:55], v[228:231]\n"
      "s_min_i32 s76, s67, s83\n"
      "v_exp_f32 v112, v180\n"
      "s_waitcnt vmcnt(8)\n"
      "s_barrier\n"
      "v_mfma_f32_16x16x32_bf16 v[136:139], a[80:83], a[36:39], v[136:139]\n"
      "s_mov_b32 s83, s67\n"
      "ds_read_b64_tr_b16 a[96:97], v244 offset:0\n"
      "v_or3_b32 v116, v116, v216, v217\n"
      "v_mfma_f32_16x16x32_bf16 v[136:139], a[72:75], a[20:23], v[136:139]\n"
      "s_sub_u32 s76, s76, 36\n"
      "v_exp_f32 v113, v181\n"
      "v_mfma_f32_16x16x32_bf16 v[136:139], a[64:67], a[4:7], v[136:139]\n"
      "s_min_i32 s76, s76, 148\n"
      "ds_read_b64_tr_b16 a[98:99], v244 offset:4096\n"
      "v_cvt_pk_bf16_f32 v218, v112, v113\n"
      "v_mfma_f32_16x16x32_bf16 v[144:147], a[64:67], a[8:11], v[232:235]\n"
      "s_max_i32 s76, s76, 70\n"
      "v_exp_f32 v114, v182\n"
      "v_mfma_f32_16x16x32_bf16 v[144:147], a[72:75], a[24:27], v[144:147]\n"
      "s_mov_b32 s82, s68\n"
      "ds_read_b64_tr_b16 a[100:101], v245 offset:0\n"
      "ds_read_b64_tr_b16 a[102:103], v245 offset:4096\n"
      "v_mfma_f32_16x16x32_bf16 v[144:147], a[80:83], a[40:43], v[144:147]\n"
      "s_add_u32 s65, s82, s76\n"
      "v_exp_f32 v115, v183\n"
      "v_mfma_f32_16x16x32_bf16 v[144:147], a[88:91], a[56:59], v[144:147]\n"
      "s_sub_u32 s93, s93, 1\n"
      "ds_read_b64_tr_b16 a[104:105], v246 offset:0\n"
      "v_cvt_pk_bf16_f32 v219, v114, v115\n"
      "v_mfma_f32_16x16x32_bf16 v[148:151], a[92:95], a[56:59], v[232:235]\n"
      "s_cmp_le_i32 s93, 14\n"
      "v_exp_f32 v112, v184\n"
      "v_mfma_f32_16x16x32_bf16 v[148:151], a[84:87], a[40:43], v[148:151]\n"
      "s_cselect_b32 s86, 107, s86\n"
      "ds_read_b64_tr_b16 a[106:107], v246 offset:4096\n"
      "v_or3_b32 v116, v116, v218, v219\n"
      "v_mfma_f32_16x16x32_bf16 v[148:151], a[76:79], a[24:27], v[148:151]\n"
      "s_sub_u32 s67, s65, s66\n"
      "v_exp_f32 v113, v185\n"
      "v_mfma_f32_16x16x32_bf16 v[148:151], a[68:71], a[8:11], v[148:151]\n"
      "s_cmp_lt_i32 s67, 0\n"
      "ds_read_b64_tr_b16 a[108:109], v247 offset:0\n"
      "v_cvt_pk_bf16_f32 v220, v112, v113\n"
      "v_mfma_f32_16x16x32_bf16 v[156:159], a[68:71], a[12:15], v[236:239]\n"
      "s_cselect_b32 s88, s66, s65\n"
      "v_exp_f32 v114, v186\n"
      "v_mfma_f32_16x16x32_bf16 v[156:159], a[76:79], a[28:31], v[156:159]\n"
      "s_add_u32 s90, s88, s91\n"
      "ds_read_b64_tr_b16 a[110:111], v247 offset:4096\n"
      "ds_read_b64_tr_b16 a[112:113], v244 offset:1024\n"
      "v_mfma_f32_16x16x32_bf16 v[156:159], a[84:87], a[44:47], v[156:159]\n"
      "v_exp_f32 v115, v187\n"
      "v_mfma_f32_16x16x32_bf16 v[156:159], a[92:95], a[60:63], v[156:159]\n"
      "ds_read_b64_tr_b16 a[114:115], v244 offset:5120\n"
      "v_cvt_pk_bf16_f32 v221, v114, v115\n"
      "v_mfma_f32_16x16x32_bf16 v[152:155], a[88:91], a[60:63], v[236:239]\n"
      "v_exp_f32 v112, v188\n"
      "v_mfma_f32_16x16x32_bf16 v[152:155], a[80:83], a[44:47], v[152:155]\n"
      "ds_read_b64_tr_b16 a[116:117], v245 offset:1024\n"
      "v_or3_b32 v116, v116, v220, v221\n"
      "v_mfma_f32_16x16x32_bf16 v[152:155], a[72:75], a[28:31], v[152:155]\n"
      "v_exp_f32 v113, v189\n"
      "v_mfma_f32_16x16x32_bf16 v[152:155], a[64:67], a[12:15], v[152:155]\n"
      "ds_read_b64_tr_b16 a[118:119], v245 offset:5120\n"
      "v_cvt_pk_bf16_f32 v222, v112, v113\n"
      "s_waitcnt lgkmcnt(6)\n"
      "v_mfma_f32_16x16x32_bf16 v[32:35], a[96:99], v[192:195], v[32:35]\n"
      "v_exp_f32 v114, v190\n"
      "v_mfma_f32_16x16x32_bf16 v[48:51], a[100:103], v[192:195], v[48:51]\n"
      "ds_read_b64_tr_b16 a[120:121], v246 offset:1024\n"
      "ds_read_b64_tr_b16 a[122:123], v246 offset:5120\n"
      "v_mfma_f32_16x16x32_bf16 a[128:131], a[104:107], v[192:195], a[128:131]\n"
      "s_waitcnt lgkmcnt(4)\n"
      "v_exp_f32 v115, v191\n"
      "v_mfma_f32_16x16x32_bf16 a[144:147], a[108:111], v[192:195], a[144:147]\n"
      "ds_read_b64_tr_b16 a[124:125], v247 offset:1024\n"
      "v_cvt_pk_bf16_f32 v223, v114, v115\n"
      "v_mfma_f32_16x16x32_bf16 v[64:67], a[112:115], v[192:195], v[64:67]\n"
      "ds_read_b64_tr_b16 a[126:127], v247 offset:5120\n"
      "s_waitcnt lgkmcnt(2)\n"
      "v_mfma_f32_16x16x32_bf16 v[80:83], a[116:119], v[192:195], v[80:83]\n"
      "v_or3_b32 v116, v116, v222, v223\n"
      "v_mfma_f32_16x16x32_bf16 a[160:163], a[120:123], v[192:195], a[160:163]\n"
      "s_mov_b32 s64, 3\n"
      "s_waitcnt lgkmcnt(0)\n"
      "v_and_b32 v116, 0x40004000, v116\n"
      "v_mfma_f32_16x16x32_bf16 a[176:179], a[124:127], v[192:195], a[176:179]\n"
      "v_cmp_ne_u32 vcc, 0, v116\n"
      "s_cbranch_vccnz FB1_%=\n"
      "RET1_3_%=:\n"
      "v_mfma_f32_16x16x32_bf16 a[180:183], a[124:127], v[196:199], a[180:183]\n"
      "v_exp_f32 v112, v128\n"
      "v_mfma_f32_16x16x32_bf16 a[164:167], a[120:123], v[196:199], a[164:167]\n"
      "v_mfma_f32_16x16x32_bf16 a[192:195], a[196:199], v[208:211], a[192:195]\n"
      "s_add_u32 m0, s62, 0\n"
      "v_exp_f32 v113, v129\n"
      "v_mfma_f32_16x16x32_bf16 v[84:87], a[116:119], v[196:199], v[84:87]\n"
      "buffer_load_dwordx4 v248, s[20:23], s60 offen nt lds\n"
      "v_cvt_pk_bf16_f32 v192, v112, v113\n"
      "v_mfma_f32_16x16x32_bf16 v[68:71], a[112:115], v[196:199], v[68:71]\n"
      "s_add_u32 m0, s62, 896\n"
      "v_exp_f32 v114, v130\n"
      "v_mfma_f32_16x16x32_bf16 a[192:195], a[200:203], v[212:215], a[192:195]\n"
      "buffer_load_dwordx4 v248, s[20:23], s60 offen offset:128 nt lds\n"
      "v_mfma_f32_16x16x32_bf16 a[148:151], a[108:111], v[196:199], a[148:151]\n"
      "s_add_u32 m0, s62, 8192\n"
      "v_exp_f32 v115, v131\n"
      "v_mfma_f32_16x16x32_bf16 a[132:135], a[104:107], v[196:199], a[132:135]\n"
      "s_memrealtime s[68:69]\n"
      "buffer_load_dwordx4 v120, s[20:23], s60 offen nt lds\n"
      "v_cvt_pk_bf16_f32 v193, v114, v115\n"
      "v_mfma_f32_16x16x32_bf16 a[192:195], a[204:207], v[216:219], a[192:195]\n"
      "s_add_u32 m0, s62, 9088\n"
      "v_exp_f32 v112, v132\n"
      "v_mfma_f32_16x16x32_bf16 v[52:55], a[100:103], v[196:199], v[52:55]\n"
      "s_add_u32 s62, s62, 0x8000\n"
      "buffer_load_dwordx4 v120, s[20:23], s60 offen offset:128 nt lds\n"
      "v_or_b32 v116, v192, v193\n"
      "v_mfma_f32_16x16x32_bf16 v[36:39], a[96:99], v[196:199], v[36:39]\n"
      "s_and_b32 s62, s62, 0x1ffff\n"
      "v_exp_f32 v113, v133\n"
      "v_mfma_f32_16x16x32_bf16 a[192:195], a[208:211], v[220:223], a[192:195]\n"
      "s_add_u32 s60, s60, %[inc]\n"
      "v_cvt_pk_bf16_f32 v194, v112, v113\n"
      "s_waitcnt vmcnt(8) lgkmcnt(0)\n"
      "s_sub_u32 s67, s68, s90\n"
      "s_cmp_lt_i32 s67, 0\n"
      "s_cbranch_scc1 GW5_%=\n"
      "GX5_%=:\n"
      "s_barrier\n"
      "v_mfma_f32_16x16x32_bf16 v[40:43], a[96:99], v[200:203], v[40:43]\n"
      "s_memrealtime s[68:69]\n"
      "buffer_load_dword v250, v251, s[28:31], 0 offen sc1\n"
      "ds_read_b128 a[64:67], v242 offset:32768\n"
      "v_exp_f32 v114, v134\n"
      "v_mfma_f32_16x16x32_bf16 v[44:47], a[96:99], v[204:207], v[44:47]\n"
      "s_add_u32 m0, s77, 16384\n"
      "ds_read_b128 a[68:71], v242 offset:36864\n"
      "ds_read_b128 a[72:75], v243 offset:32768\n"
      "v_mfma_f32_16x16x32_bf16 v[60:63], a[100:103], v[204:207], v[60:63]\n"
      "buffer_load_dwordx4 v249, s[24:27], s61 offen nt lds\n"
      "v_exp_f32 v115, v135\n"
      "v_mfma_f32_16x16x32_bf16 v[56:59], a[100:103], v[200:203], v[56:59]\n"
      "s_add_u32 m0, s77, 17408\n"
      "ds_read_b128 a[76:79], v243 offset:36864\n"
      "v_cvt_pk_bf16_f32 v195, v114, v115\n"
      "v_mfma_f32_16x16x32_bf16 a[136:139], a[104:107], v[200:203], a[136:139]\n"
      "buffer_load_dwordx4 v121, s[24:27], s61 offen nt lds\n"
      "v_exp_f32 v112, v136\n"
      "v_mfma_f32_16x16x32_bf16 a[140:143], a[104:107], v[204:207], a[140:143]\n"
      "s_add_u32 m0, s77, 24576\n"
      "ds_read_b128 a[80:83], v242 offset:33792\n"
      "v_or3_b32 v116, v116, v194, v195\n"
      "v_mfma_f32_16x16x32_bf16 a[156:159], a[108:111], v[204:207], a[156:159]\n"
      "buffer_load_dwordx4 v122, s[24:27], s61 offen nt lds\n"
      "v_exp_f32 v113, v137\n"
      "v_mfma_f32_16x16x32_bf16 a[152:155], a[108:111], v[200:203], a[152:155]\n"
      "s_add_u32 m0, s77, 25600\n"
      "ds_read_b128 a[84:87], v242 offset:37888\n"
      "v_cvt_pk_bf16_f32 v196, v112, v113\n"
      "v_mfma_f32_16x16x32_bf16 v[72:75], a[112:115], v[200:203], v[72:75]\n"
      "s_add_u32 s77, s77, 0x8000\n"
      "buffer_load_dwordx4 v123, s[24:27], s61 offen nt lds\n"
      "v_exp_f32 v114, v138\n"
      "v_mfma_f32_16x16x32_bf16 v[76:79], a[112:115], v[204:207], v[76:79]\n"
      "s_and_b32 s77, s77, 0x1ffff\n"
      "ds_read_b128 a[88:91], v243 offset:33792\n"
      "ds_read_b128 a[92:95], v243 offset:37888\n"
      "v_mfma_f32_16x16x32_bf16 v[92:95], a[116:119], v[204:207], v[92:95]\n"
      "s_add_u32 s61, s61, %[inc]\n"
      "v_exp_f32 v115, v139\n"
      "v_mfma_f32_16x16x32_bf16 v[88:91], a[116:119], v[200:203], v[88:91]\n"
      "v_cvt_pk_bf16_f32 v197, v114, v115\n"
      "v_mfma_f32_16x16x32_bf16 a[168:171], a[120:123], v[200:203], a[168:171]\n"
      "v_exp_f32 v112, v140\n"
      "v_mfma_f32_16x16x32_bf16 a[172:175], a[120:123], v[204:207], a[172:175]\n"
      "v_or3_b32 v116, v116, v196, v197\n"
      "v_mfma_f32_16x16x32_bf16 a[188:191], a[124:127], v[204:207], a[188:191]\n"
      "v_exp_f32 v113, v141\n"
      "v_mfma_f32_16x16x32_bf16 a[184:187], a[124:127], v[200:203], a[184:187]\n"
      "s_waitcnt lgkmcnt(0)\n"
      "v_cvt_pk_bf16_f32 v198, v112, v113\n"
      "v_mfma_f32_16x16x32_bf16 v[160:163], a[64:67], a[0:3], v[224:227]\n"
      "s_sub_u32 s67, s68, s66\n"
      "v_exp_f32 v114, v142\n"
      "v_mfma_f32_16x16x32_bf16 v[160:163], a[72:75], a[16:19], v[160:163]\n"
      "s_cmp_gt_i32 s67, s70\n"
      "v_mfma_f32_16x16x32_bf16 v[160:163], a[80:83], a[32:35], v[160:163]\n"
      "s_cselect_b32 s69, s86, 0\n"
      "v_exp_f32 v115, v143\n"
      "v_mfma_f32_16x16x32_bf16 v[160:163], a[88:91], a[48:51], v[160:163]\n"
      "s_add_u32 s66, s66, s69\n"
      "v_cvt_pk_bf16_f32 v199, v114, v115\n"
      "v_mfma_f32_16x16x32_bf16 v[164:167], a[92:95], a[48:51], v[224:227]\n"
      "s_add_u32 s67, s67, s101\n"
      "v_exp_f32 v112, v144\n"
      "v_mfma_f32_16x16x32_bf16 v[164:167], a[84:87], a[32:35], v[164:167]\n"
      "s_min_i32 s70, s70, s67\n"
      "v_or3_b32 v116, v116, v198, v199\n"
      "v_mfma_f32_16x16x32_bf16 v[164:167], a[76:79], a[16:19], v[164:167]\n"
      "s_max_i32 s70, s70, 85\n"
      "v_exp_f32 v113, v145\n"
      "v_mfma_f32_16x16x32_bf16 v[164:167], a[68:71], a[0:3], v[164:167]\n"
      "s_add_u32 s66, s66, s86\n"
      "v_cvt_pk_bf16_f32 v200, v112, v113\n"
      "v_mfma_f32_16x16x32_bf16 v[172:175], a[68:71], a[4:7], v[228:231]\n"
      "s_and_b32 s76, s80, 31\n"
      "v_exp_f32 v114, v146\n"
      "v_mfma_f32_16x16x32_bf16 v[172:175], a[76:79], a[20:23], v[172:175]\n"
      "s_lshr_b64 s[80:81], s[80:81], 5\n"
      "v_mfma_f32_16x16x32_bf16 v[172:175], a[84:87], a[36:39], v[172:175]\n"
      "s_add_u32 s66, s66, s76\n"
      "v_exp_f32 v115, v147\n"
      "v_mfma_f32_16x16x32_bf16 v[172:175], a[92:95], a[52:55], v[172:175]\n"
      "s_sub_u32 s67, s68, s82\n"
      "v_cvt_pk_bf16_f32 v201, v114, v115\n"
      "v_mfma_f32_16x16x32_bf16 v[168:171], a[88:91], a[52:55], v[228:231]\n"
      "s_min_i32 s76, s67, s83\n"
      "v_exp_f32 v112, v148\n"
      "s_waitcnt vmcnt(9)\n"
      "s_barrier\n"
      "v_mfma_f32_16x16x32_bf16 v[168:171], a[80:83], a[36:39], v[168:171]\n"
      "s_mov_b32 s83, s67\n"
      "ds_read_b64_tr_b16 a[96:97], v244 offset:32768\n"
      "v_or3_b32 v116, v116, v200, v201\n"
      "v_mfma_f32_16x16x32_bf16 v[168:171], a[72:75], a[20:23], v[168:171]\n"
      "s_sub_u32 s76, s76, 36\n"
      "v_exp_f32 v113, v149\n"
      "v_mfma_f32_16x16x32_bf16 v[168:171], a[64:67], a[4:7], v[168:171]\n"
      "s_min_i32 s76, s76, 148\n"
      "ds_read_b64_tr_b16 a[98:99], v244 offset:36864\n"
      "v_cvt_pk_bf16_f32 v202, v112, v113\n"
      "v_mfma_f32_16x16x32_bf16 v[176:179], a[64:67], a[8:11], v[232:235]\n"
      "s_max_i32 s76, s76, 70\n"
      "v_exp_f32 v114, v150\n"
      "v_mfma_f32_16x16x32_bf16 v[176:179], a[72:75], a[24:27], v[176:179]\n"
      "s_mov_b32 s82, s68\n"
      "ds_read_b64_tr_b16 a[100:101], v245 offset:32768\n"
      "ds_read_b64_tr_b16 a[102:103], v245 offset:36864\n"
      "v_mfma_f32_16x16x32_bf16 v[176:179], a[80:83], a[40:43], v[176:179]\n"
      "s_add_u32 s65, s82, s76\n"
      "v_exp_f32 v115, v151\n"
      "v_mfma_f32_16x16x32_bf16 v[176:179], a[88:91], a[56:59], v[176:179]\n"
      "s_sub_u32 s93, s93, 1\n"
      "ds_read_b64_tr_b16 a[104:105], v246 offset:32768\n"
      "v_cvt_pk_bf16_f32 v203, v114, v115\n"
      "v_mfma_f32_16x16x32_bf16 v[180:183], a[92:95], a[56:59], v[232:235]\n"
      "s_cmp_le_i32 s93, 14\n"
      "v_exp_f32 v112, v152\n"
      "v_mfma_f32_16x16x32_bf16 v[180:183], a[84:87], a[40:43], v[180:183]\n"
      "s_cselect_b32 s86, 107, s86\n"
      "ds_read_b64_tr_b16 a[106:107], v246 offset:36864\n"
      "v_or3_b32 v116, v116, v202, v203\n"
      "v_mfma_f32_16x16x32_bf16 v[180:183], a[76:79], a[24:27], v[180:183]\n"
      "s_sub_u32 s67, s65, s66\n"
      "v_exp_f32 v113, v153\n"
      "v_mfma_f32_16x16x32_bf16 v[180:183], a[68:71], a[8:11], v[180:183]\n"
      "s_cmp_lt_i32 s67, 0\n"
      "ds_read_b64_tr_b16 a[108:109], v247 offset:32768\n"
      "v_cvt_pk_bf16_f32 v204, v112, v113\n"
      "v_mfma_f32_16x16x32_bf16 v[188:191], a[68:71], a[12:15], v[236:239]\n"
      "s_cselect_b32 s88, s66, s65\n"
      "v_exp_f32 v114, v154\n"
      "v_mfma_f32_16x16x32_bf16 v[188:191], a[76:79], a[28:31], v[188:191]\n"
      "s_add_u32 s90, s88, s91\n"
      "ds_read_b64_tr_b16 a[110:111], v247 offset:36864\n"
      "ds_read_b64_tr_b16 a[112:113], v244 offset:33792\n"
      "v_mfma_f32_16x16x32_bf16 v[188:191], a[84:87], a[44:47], v[188:191]\n"
      "v_exp_f32 v115, v155\n"
      "v_mfma_f32_16x16x32_bf16 v[188:191], a[92:95], a[60:63], v[188:191]\n"
      "ds_read_b64_tr_b16 a[114:115], v244 offset:37888\n"
      "v_cvt_pk_bf16_f32 v205, v114, v115\n"
      "v_mfma_f32_16x16x32_bf16 v[184:187], a[88:91], a[60:63], v[236:239]\n"
      "v_exp_f32 v112, v156\n"
      "v_mfma_f32_16x16x32_bf16 v[184:187], a[80:83], a[44:47], v[184:187]\n"
      "ds_read_b64_tr_b16 a[116:117], v245 offset:33792\n"
      "v_or3_b32 v116, v116, v204, v205\n"
      "v_mfma_f32_16x16x32_bf16 v[184:187], a[72:75], a[28:31], v[184:187]\n"
      "v_exp_f32 v113, v157\n"
      "v_mfma_f32_16x16x32_bf16 v[184:187], a[64:67], a[12:15], v[184:187]\n"
      "ds_read_b64_tr_b16 a[118:119], v245 offset:37888\n"
      "v_cvt_pk_bf16_f32 v206, v112, v113\n"
      "s_waitcnt lgkmcnt(6)\n"
      "v_mfma_f32_16x16x32_bf16 v[32:35], a[96:99], v[208:211], v[32:35]\n"
      "v_exp_f32 v114, v158\n"
      "v_mfma_f32_16x16x32_bf16 v[48:51], a[100:103], v[208:211], v[48:51]\n"
      "ds_read_b64_tr_b16 a[120:121], v246 offset:33792\n"
      "ds_read_b64_tr_b16 a[122:123], v246 offset:37888\n"
      "v_mfma_f32_16x16x32_bf16 a[128:131], a[104:107], v[208:211], a[128:131]\n"
      "s_waitcnt lgkmcnt(4)\n"
      "v_exp_f32 v115, v159\n"
      "v_mfma_f32_16x16x32_bf16 a[144:147], a[108:111], v[208:211], a[144:147]\n"
      "ds_read_b64_tr_b16 a[124:125], v247 offset:33792\n"
      "v_cvt_pk_bf16_f32 v207, v114, v115\n"
      "v_mfma_f32_16x16x32_bf16 v[64:67], a[112:115], v[208:211], v[64:67]\n"
      "ds_read_b64_tr_b16 a[126:127], v247 offset:37888\n"
      "s_waitcnt lgkmcnt(2)\n"
      "v_mfma_f32_16x16x32_bf16 v[80:83], a[116:119], v[208:211], v[80:83]\n"
      "v_or3_b32 v116, v116, v206, v207\n"
      "v_mfma_f32_16x16x32_bf16 a[160:163], a[120:123], v[208:211], a[160:163]\n"
      "s_mov_b32 s64, 3\n"
      "s_waitcnt lgkmcnt(0)\n"
      "v_and_b32 v116, 0x40004000, v116\n"
      "v_mfma_f32_16x16x32_bf16 a[176:179], a[124:127], v[208:211], a[176:179]\n"
      "v_cmp_ne_u32 vcc, 0, v116\n"
      "s_cbranch_vccnz FB0_%=\n"
      "RET0_3_%=:\n"
      "v_mfma_f32_16x16x32_bf16 a[180:183], a[124:127], v[212:215], a[180:183]\n"
      "v_exp_f32 v112, v160\n"
      "v_mfma_f32_16x16x32_bf16 a[164:167], a[120:123], v[212:215], a[164:167]\n"
      "v_xor_b32 v242, 0x10000, v242\n"
      "v_mfma_f32_16x16x32_bf16 a[192:195], a[196:199], v[192:195], a[192:195]\n"
      "s_add_u32 m0, s62, 0\n"
      "v_exp_f32 v113, v161\n"
      "v_mfma_f32_16x16x32_bf16 v[84:87], a[116:119], v[212:215], v[84:87]\n"
      "buffer_load_dwordx4 v248, s[20:23], s60 offen nt lds\n"
      "v_cvt_pk_bf16_f32 v208, v112, v113\n"
      "v_xor_b32 v243, 0x10000, v243\n"
      "v_mfma_f32_16x16x32_bf16 v[68:71], a[112:115], v[212:215], v[68:71]\n"
      "s_add_u32 m0, s62, 896\n"
      "v_exp_f32 v114, v162\n"
      "v_mfma_f32_16x16x32_bf16 a[192:195], a[200:203], v[196:199], a[192:195]\n"
      "buffer_load_dwordx4 v248, s[20:23], s60 offen offset:128 nt lds\n"
      "v_xor_b32 v244, 0x10000, v244\n"
      "v_mfma_f32_16x16x32_bf16 a[148:151], a[108:111], v[212:215], a[148:151]\n"
      "s_add_u32 m0, s62, 8192\n"
      "v_exp_f32 v115, v163\n"
      "v_mfma_f32_16x16x32_bf16 a[132:135], a[104:107], v[212:215], a[132:135]\n"
      "s_memrealtime s[68:69]\n"
      "buffer_load_dwordx4 v120, s[20:23], s60 offen nt lds\n"
      "v_cvt_pk_bf16_f32 v209, v114, v115\n"
      "v_xor_b32 v245, 0x10000, v245\n"
      "v_mfma_f32_16x16x32_bf16 a[192:195], a[204:207], v[200:203], a[192:195]\n"
      "s_add_u32 m0, s62, 9088\n"
      "v_exp_f32 v112, v164\n"
      "v_mfma_f32_16x16x32_bf16 v[52:55], a[100:103], v[212:215], v[52:55]\n"
      "s_add_u32 s62, s62, 0x8000\n"
      "buffer_load_dwordx4 v120, s[20:23], s60 offen offset:128 nt lds\n"
      "v_or_b32 v116, v208, v209\n"
      "v_xor_b32 v246, 0x10000, v246\n"
      "v_mfma_f32_16x16x32_bf16 v[36:39], a[96:99], v[212:215], v[36:39]\n"
      "s_and_b32 s62, s62, 0x1ffff\n"
      "s_add_u32 s60, s60, s78\n"
      "v_exp_f32 v113, v165\n"
      "v_mfma_f32_16x16x32_bf16 a[192:195], a[208:211], v[204:207], a[192:195]\n"
      "s_sub_u32 s63, s63, 1\n"
      "v_cvt_pk_bf16_f32 v210, v112, v113\n"
      "v_xor_b32 v247, 0x10000, v247\n"
      "s_cbranch_scc0 LOOP2_%=\n"
      "s_waitcnt vmcnt(9) lgkmcnt(0)\n"
      "s_sub_u32 s67, s68, s90\n"
      "s_cmp_lt_i32 s67, 0\n"
      "s_cbranch_scc1 GW6_%=\n"
      "GX6_%=:\n"
      "s_barrier\n"
      "v_mfma_f32_16x16x32_bf16 v[40:43], a[96:99], v[216:219], v[40:43]\n"
      "s_memrealtime s[68:69]\n"
      "ds_read_b128 a[64:67], v242 offset:0\n"
      "v_exp_f32 v114, v166\n"
      "v_mfma_f32_16x16x32_bf16 v[44:47], a[96:99], v[220:223], v[44:47]\n"
      "s_add_u32 m0, %[wb], 50176\n"
      "ds_read_b128 a[68:71], v242 offset:4096\n"
      "ds_read_b128 a[72:75], v243 offset:0\n"
      "v_mfma_f32_16x16x32_bf16 v[60:63], a[100:103], v[220:223], v[60:63]\n"
      "s_add_u32 s94, s61, s78\n"
      "buffer_load_dwordx4 v121, s[24:27], s61 offen nt lds\n"
      "v_exp_f32 v115, v167\n"
      "v_mfma_f32_16x16x32_bf16 v[56:59], a[100:103], v[216:219], v[56:59]\n"
      "s_add_u32 m0, %[wb], 58368\n"
      "ds_read_b128 a[76:79], v243 offset:4096\n"
      "v_cvt_pk_bf16_f32 v211, v114, v115\n"
      "v_mfma_f32_16x16x32_bf16 a[136:139], a[104:107], v[216:219], a[136:139]\n"
      "s_add_u32 s95, s61, 0x10000\n"
      "buffer_load_dwordx4 v123, s[24:27], s61 offen nt lds\n"
      "v_exp_f32 v112, v168\n"
      "v_mfma_f32_16x16x32_bf16 a[140:143], a[104:107], v[220:223], a[140:143]\n"
      "s_add_u32 s77, s94, 0x10000\n"
      "ds_read_b128 a[80:83], v242 offset:1024\n"
      "v_or3_b32 v116, v116, v210, v211\n"
      "v_mfma_f32_16x16x32_bf16 a[156:159], a[108:111], v[220:223], a[156:159]\n"
      "s_add_u32 s78, s61, 0x20000\n"
      "v_exp_f32 v113, v169\n"
      "v_mfma_f32_16x16x32_bf16 a[152:155], a[108:111], v[216:219], a[152:155]\n"
      "ds_read_b128 a[84:87], v242 offset:5120\n"
      "v_cvt_pk_bf16_f32 v212, v112, v113\n"
      "v_mfma_f32_16x16x32_bf16 v[72:75], a[112:115], v[216:219], v[72:75]\n"
      "v_exp_f32 v114, v170\n"
      "v_mfma_f32_16x16x32_bf16 v[76:79], a[112:115], v[220:223], v[76:79]\n"
      "ds_read_b128 a[88:91], v243 offset:1024\n"
      "ds_read_b128 a[92:95], v243 offset:5120\n"
      "v_mfma_f32_16x16x32_bf16 v[92:95], a[116:119], v[220:223], v[92:95]\n"
      "v_exp_f32 v115, v171\n"
      "v_mfma_f32_16x16x32_bf16 v[88:91], a[116:119], v[216:219], v[88:91]\n"
      "v_cvt_pk_bf16_f32 v213, v114, v115\n"
      "v_mfma_f32_16x16x32_bf16 a[168:171], a[120:123], v[216:219], a[168:171]\n"
      "v_exp_f32 v112, v172\n"
      "v_mfma_f32_16x16x32_bf16 a[172:175], a[120:123], v[220:223], a[172:175]\n"
      "v_or3_b32 v116, v116, v212, v213\n"
      "v_mfma_f32_16x16x32_bf16 a[188:191], a[124:127], v[220:223], a[188:191]\n"
      "v_exp_f32 v113, v173\n"
      "v_mfma_f32_16x16x32_bf16 a[184:187], a[124:127], v[216:219], a[184:187]\n"
      "s_waitcnt lgkmcnt(0)\n"
      "v_cvt_pk_bf16_f32 v214, v112, v113\n"
      "v_mfma_f32_16x16x32_bf16 v[128:131], a[64:67], a[0:3], v[224:227]\n"
      "s_sub_u32 s67, s68, s66\n"
      "v_exp_f32 v114, v174\n"
      "v_mfma_f32_16x16x32_bf16 v[128:131], a[72:75], a[16:19], v[128:131]\n"
      "s_cmp_gt_i32 s67, s70\n"
      "v_mfma_f32_16x16x32_bf16 v[128:131], a[80:83], a[32:35], v[128:131]\n"
      "s_cselect_b32 s69, s86, 0\n"
      "v_exp_f32 v115, v175\n"
      "v_mfma_f32_16x16x32_bf16 v[128:131], a[88:91], a[48:51], v[128:131]\n"
      "s_add_u32 s66, s66, s69\n"
      "v_cvt_pk_bf16_f32 v215, v114, v115\n"
      "v_mfma_f32_16x16x32_bf16 v[132:135], a[92:95], a[48:51], v[224:227]\n"
      "s_add_u32 s67, s67, s101\n"
      "v_exp_f32 v112, v176\n"
      "v_mfma_f32_16x16x32_bf16 v[132:135], a[84:87], a[32:35], v[132:135]\n"
      "s_min_i32 s70, s70, s67\n"
      "v_or3_b32 v116, v116, v214, v215\n"
      "v_mfma_f32_16x16x32_bf16 v[132:135], a[76:79], a[16:19], v[132:135]\n"
      "s_max_i32 s70, s70, 85\n"
      "v_exp_f32 v113, v177\n"
      "v_mfma_f32_16x16x32_bf16 v[132:135], a[68:71], a[0:3], v[132:135]\n"
      "s_add_u32 s66, s66, s86\n"
      "v_cvt_pk_bf16_f32 v216, v112, v113\n"
      "v_mfma_f32_16x16x32_bf16 v[140:143], a[68:71], a[4:7], v[228:231]\n"
      "s_and_b32 s76, s80, 31\n"
      "v_exp_f32 v114, v178\n"
      "v_mfma_f32_16x16x32_bf16 v[140:143], a[76:79], a[20:23], v[140:143]\n"
      "s_lshr_b64 s[80:81], s[80:81], 5\n"
      "v_mfma_f32_16x16x32_bf16 v[140:143], a[84:87], a[36:39], v[140:143]\n"
      "s_add_u32 s66, s66, s76\n"
      "v_exp_f32 v115, v179\n"
      "v_mfma_f32_16x16x32_bf16 v[140:143], a[92:95], a[52:55], v[140:143]\n"
      "s_sub_u32 s67, s68, s82\n"
      "v_cvt_pk_bf16_f32 v217, v114, v115\n"
      "v_mfma_f32_16x16x32_bf16 v[136:139], a[88:91], a[52:55], v[228:231]\n"
      "s_min_i32 s76, s67, s83\n"
      "v_exp_f32 v112, v180\n"
      "s_waitcnt vmcnt(6)\n"
      "s_barrier\n"
      "v_mfma_f32_16x16x32_bf16 v[136:139], a[80:83], a[36:39], v[136:139]\n"
      "s_mov_b32 s83, s67\n"
      "ds_read_b64_tr_b16 a[96:97], v244 offset:0\n"
      "v_or3_b32 v116, v116, v216, v217\n"
      "v_mfma_f32_16x16x32_bf16 v[136:139], a[72:75], a[20:23], v[136:139]\n"
      "s_add_u32 m0, s62, 0\n"
      "v_exp_f32 v113, v181\n"
      "v_mfma_f32_16x16x32_bf16 v[136:139], a[64:67], a[4:7], v[136:139]\n"
      "s_sub_u32 s76, s76, 36\n"
      "ds_read_b64_tr_b16 a[98:99], v244 offset:4096\n"
      "buffer_load_dwordx4 v248, s[20:23], s60 offen nt lds\n"
      "v_cvt_pk_bf16_f32 v218, v112, v113\n"
      "v_mfma_f32_16x16x32_bf16 v[144:147], a[64:67], a[8:11], v[232:235]\n"
      "s_add_u32 m0, s62, 896\n"
      "v_exp_f32 v114, v182\n"
      "v_mfma_f32_16x16x32_bf16 v[144:147], a[72:75], a[24:27], v[144:147]\n"
      "s_min_i32 s76, s76, 148\n"
      "ds_read_b64_tr_b16 a[100:101], v245 offset:0\n"
      "ds_read_b64_tr_b16 a[102:103], v245 offset:4096\n"
      "buffer_load_dwordx4 v248, s[20:23], s60 offen offset:128 nt lds\n"
      "v_mfma_f32_16x16x32_bf16 v[144:147], a[80:83], a[40:43], v[144:147]\n"
      "s_add_u32 m0, s62, 8192\n"
      "v_exp_f32 v115, v183\n"
      "v_mfma_f32_16x16x32_bf16 v[144:147], a[88:91], a[56:59], v[144:147]\n"
      "s_max_i32 s76, s76, 70\n"
      "ds_read_b64_tr_b16 a[104:105], v246 offset:0\n"
      "buffer_load_dwordx4 v120, s[20:23], s60 offen nt lds\n"
      "v_cvt_pk_bf16_f32 v219, v114, v115\n"
      "v_mfma_f32_16x16x32_bf16 v[148:151], a[92:95], a[56:59], v[232:235]\n"
      "s_add_u32 m0, s62, 9088\n"
      "v_exp_f32 v112, v184\n"
      "v_mfma_f32_16x16x32_bf16 v[148:151], a[84:87], a[40:43], v[148:151]\n"
      "s_add_u32 s62, s62, 0x8000\n"
      "ds_read_b64_tr_b16 a[106:107], v246 offset:4096\n"
      "buffer_load_dwordx4 v120, s[20:23], s60 offen offset:128 nt lds\n"
      "v_or3_b32 v116, v116, v218, v219\n"
      "v_mfma_f32_16x16x32_bf16 v[148:151], a[76:79], a[24:27], v[148:151]\n"
      "s_and_b32 s62, s62, 0x1ffff\n"
      "v_exp_f32 v113, v185\n"
      "v_mfma_f32_16x16x32_bf16 v[148:151], a[68:71], a[8:11], v[148:151]\n"
      "s_add_u32 s60, s60, %[inc]\n"
      "ds_read_b64_tr_b16 a[108:109], v247 offset:0\n"
      "v_cvt_pk_bf16_f32 v220, v112, v113\n"
      "v_mfma_f32_16x16x32_bf16 v[156:159], a[68:71], a[12:15], v[236:239]\n"
      "s_mov_b32 s82, s68\n"
      "v_exp_f32 v114, v186\n"
      "v_mfma_f32_16x16x32_bf16 v[156:159], a[76:79], a[28:31], v[156:159]\n"
      "s_sub_u32 s66, s66, s99\n"
      "ds_read_b64_tr_b16 a[110:111], v247 offset:4096\n"
      "ds_read_b64_tr_b16 a[112:113], v244 offset:1024\n"
      "v_mfma_f32_16x16x32_bf16 v[156:159], a[84:87], a[44:47], v[156:159]\n"
      "s_sub_u32 s82, s82, s99\n"
      "v_exp_f32 v115, v187\n"
      "v_mfma_f32_16x16x32_bf16 v[156:159], a[92:95], a[60:63], v[156:159]\n"
      "s_add_u32 s65, s82, s76\n"
      "ds_read_b64_tr_b16 a[114:115], v244 offset:5120\n"
      "v_cvt_pk_bf16_f32 v221, v114, v115\n"
      "v_mfma_f32_16x16x32_bf16 v[152:155], a[88:91], a[60:63], v[236:239]\n"
      "s_sub_u32 s93, s93, 1\n"
      "v_exp_f32 v112, v188\n"
      "v_mfma_f32_16x16x32_bf16 v[152:155], a[80:83], a[44:47], v[152:155]\n"
      "s_cmp_le_i32 s93, 14\n"
      "ds_read_b64_tr_b16 a[116:117], v245 offset:1024\n"
      "v_or3_b32 v116, v116, v220, v221\n"
      "v_mfma_f32_16x16x32_bf16 v[152:155], a[72:75], a[28:31], v[152:155]\n"
      "s_cselect_b32 s86, 107, s86\n"
      "v_exp_f32 v113, v189\n"
      "v_mfma_f32_16x16x32_bf16 v[152:155], a[64:67], a[12:15], v[152:155]\n"
      "ds_read_b64_tr_b16 a[118:119], v245 offset:5120\n"
      "v_cvt_pk_bf16_f32 v222, v112, v113\n"
      "s_waitcnt lgkmcnt(6)\n"
      "v_mfma_f32_16x16x32_bf16 v[32:35], a[96:99], v[192:195], v[32:35]\n"
      "s_sub_u32 s67, s65, s66\n"
      "v_exp_f32 v114, v190\n"
      "v_mfma_f32_16x16x32_bf16 v[48:51], a[100:103], v[192:195], v[48:51]\n"
      "s_cmp_lt_i32 s67, 0\n"
      "s_cselect_b32 s88, s66, s65\n"
      "ds_read_b64_tr_b16 a[120:121], v246 offset:1024\n"
      "ds_read_b64_tr_b16 a[122:123], v246 offset:5120\n"
      "v_mfma_f32_16x16x32_bf16 a[128:131], a[104:107], v[192:195], a[128:131]\n"
      "s_waitcnt lgkmcnt(4)\n"
      "v_exp_f32 v115, v191\n"
      "v_mfma_f32_16x16x32_bf16 a[144:147], a[108:111], v[192:195], a[144:147]\n"
      "s_add_u32 s90, s88, s91\n"
      "ds_read_b64_tr_b16 a[124:125], v247 offset:1024\n"
      "v_cvt_pk_bf16_f32 v223, v114, v115\n"
      "v_mfma_f32_16x16x32_bf16 v[64:67], a[112:115], v[192:195], v[64:67]\n"
      "ds_read_b64_tr_b16 a[126:127], v247 offset:5120\n"
      "s_waitcnt lgkmcnt(2)\n"
      "v_mfma_f32_16x16x32_bf16 v[80:83], a[116:119], v[192:195], v[80:83]\n"
      "v_or3_b32 v116, v116, v222, v223\n"
      "v_mfma_f32_16x16x32_bf16 a[160:163], a[120:123], v[192:195], a[160:163]\n"
      "s_mov_b32 s64, 4\n"
      "s_waitcnt lgkmcnt(0)\n"
      "v_and_b32 v116, 0x40004000, v116\n"
      "v_mfma_f32_16x16x32_bf16 a[176:179], a[124:127], v[192:195], a[176:179]\n"
      "v_cmp_ne_u32 vcc, 0, v116\n"
      "s_cbranch_vccnz FB1_%=\n"
      "RET1_4_%=:\n"
      "v_mfma_f32_16x16x32_bf16 a[180:183], a[124:127], v[196:199], a[180:183]\n"
      "v_exp_f32 v112, v128\n"
      "v_mfma_f32_16x16x32_bf16 a[164:167], a[120:123], v[196:199], a[164:167]\n"
      "v_xor_b32 v240, 0x10000, v244\n"
      "v_mfma_f32_16x16x32_bf16 a[192:195], a[196:199], v[208:211], a[192:195]\n"
      "v_exp_f32 v113, v129\n"
      "v_mfma_f32_16x16x32_bf16 v[84:87], a[116:119], v[196:199], v[84:87]\n"
      "v_cvt_pk_bf16_f32 v192, v112, v113\n"
      "v_xor_b32 v241, 0x10000, v245\n"
      "v_mfma_f32_16x16x32_bf16 v[68:71], a[112:115], v[196:199], v[68:71]\n"
      "v_exp_f32 v114, v130\n"
      "v_mfma_f32_16x16x32_bf16 a[192:195], a[200:203], v[212:215], a[192:195]\n"
      "v_xor_b32 v250, 0x10000, v246\n"
      "v_mfma_f32_16x16x32_bf16 a[148:151], a[108:111], v[196:199], a[148:151]\n"
      "v_exp_f32 v115, v131\n"
      "v_mfma_f32_16x16x32_bf16 a[132:135], a[104:107], v[196:199], a[132:135]\n"
      "s_memrealtime s[68:69]\n"
      "v_cvt_pk_bf16_f32 v193, v114, v115\n"
      "v_xor_b32 v251, 0x10000, v247\n"
      "v_mfma_f32_16x16x32_bf16 a[192:195], a[204:207], v[216:219], a[192:195]\n"
      "v_exp_f32 v112, v132\n"
      "v_mfma_f32_16x16x32_bf16 v[52:55], a[100:103], v[196:199], v[52:55]\n"
      "v_or_b32 v116, v192, v193\n"
      "v_mfma_f32_16x16x32_bf16 v[36:39], a[96:99], v[196:199], v[36:39]\n"
      "v_exp_f32 v113, v133\n"
      "v_mfma_f32_16x16x32_bf16 a[192:195], a[208:211], v[220:223], a[192:195]\n"
      "v_cvt_pk_bf16_f32 v194, v112, v113\n"
      "s_waitcnt vmcnt(6) lgkmcnt(0)\n"
      "s_sub_u32 s67, s68, s90\n"
      "s_cmp_lt_i32 s67, 0\n"
      "s_cbranch_scc1 GW7_%=\n"
      "GX7_%=:\n"
      "s_barrier\n"
      "v_mfma_f32_16x16x32_bf16 v[40:43], a[96:99], v[200:203], v[40:43]\n"
      "s_memrealtime s[68:69]\n"
      "ds_read_b128 a[64:67], v242 offset:32768\n"
      "v_exp_f32 v114, v134\n"
      "v_mfma_f32_16x16x32_bf16 v[44:47], a[96:99], v[204:207], v[44:47]\n"
      "s_add_u32 m0, %[wb], 82944\n"
      "ds_read_b128 a[68:71], v242 offset:36864\n"
      "v_mfma_f32_16x16x32_bf16 v[60:63], a[100:103], v[204:207], v[60:63]\n"
      "ds_read_b128 a[72:75], v243 offset:32768\n"
      "buffer_load_dwordx4 v121, s[24:27], s94 offen nt lds\n"
      "v_mfma_f32_16x16x32_bf16 v[56:59], a[100:103], v[200:203], v[56:59]\n"
      "s_add_u32 m0, %[wb], 91136\n"
      "ds_read_b128 a[76:79], v243 offset:36864\n"
      "v_mfma_f32_16x16x32_bf16 a[136:139], a[104:107], v[200:203], a[136:139]\n"
      "ds_read_b128 a[80:83], v242 offset:33792\n"
      "buffer_load_dwordx4 v123, s[24:27], s94 offen nt lds\n"
      "v_mfma_f32_16x16x32_bf16 a[140:143], a[104:107], v[204:207], a[140:143]\n"
      "ds_read_b128 a[84:87], v242 offset:37888\n"
      "v_mfma_f32_16x16x32_bf16 a[156:159], a[108:111], v[204:207], a[156:159]\n"
      "ds_read_b128 a[88:91], v243 offset:33792\n"
      "v_mfma_f32_16x16x32_bf16 a[152:155], a[108:111], v[200:203], a[152:155]\n"
      "ds_read_b128 a[92:95], v243 offset:37888\n"
      "v_mfma_f32_16x16x32_bf16 v[72:75], a[112:115], v[200:203], v[72:75]\n"
      "v_exp_f32 v115, v135\n"
      "v_mfma_f32_16x16x32_bf16 v[76:79], a[112:115], v[204:207], v[76:79]\n"
      "v_exp_f32 v112, v136\n"
      "v_mfma_f32_16x16x32_bf16 v[92:95], a[116:119], v[204:207], v[92:95]\n"
      "v_cvt_pk_bf16_f32 v195, v114, v115\n"
      "v_exp_f32 v113, v137\n"
      "v_mfma_f32_16x16x32_bf16 v[88:91], a[116:119], v[200:203], v[88:91]\n"
      "v_or3_b32 v116, v116, v194, v195\n"
      "v_mfma_f32_16x16x32_bf16 a[168:171], a[120:123], v[200:203], a[168:171]\n"
      "v_exp_f32 v114, v138\n"
      "v_mfma_f32_16x16x32_bf16 a[172:175], a[120:123], v[204:207], a[172:175]\n"
      "v_cvt_pk_bf16_f32 v196, v112, v113\n"
      "v_exp_f32 v115, v139\n"
      "v_mfma_f32_16x16x32_bf16 a[188:191], a[124:127], v[204:207], a[188:191]\n"
      "v_exp_f32 v112, v140\n"
      "v_mfma_f32_16x16x32_bf16 a[184:187], a[124:127], v[200:203], a[184:187]\n"
      "s_waitcnt lgkmcnt(0)\n"
      "v_cvt_pk_bf16_f32 v197, v114, v115\n"
      "v_mfma_f32_16x16x32_bf16 v[160:163], a[64:67], a[0:3], v[224:227]\n"
      "s_sub_u32 s67, s68, s66\n"
      "v_exp_f32 v113, v141\n"
      "v_mfma_f32_16x16x32_bf16 v[168:171], a[64:67], a[4:7], v[228:231]\n"
      "s_cmp_gt_i32 s67, s70\n"
      "v_or3_b32 v116, v116, v196, v197\n"
      "v_exp_f32 v114, v142\n"
      "v_mfma_f32_16x16x32_bf16 v[176:179], a[64:67], a[8:11], v[232:235]\n"
      "s_cselect_b32 s69, s86, 0\n"
      "v_cvt_pk_bf16_f32 v198, v112, v113\n"
      "v_mfma_f32_16x16x32_bf16 v[184:187], a[64:67], a[12:15], v[236:239]\n"
      "s_add_u32 s66, s66, s69\n"
      "v_exp_f32 v115, v143\n"
      "v_mfma_f32_16x16x32_bf16 v[164:167], a[68:71], a[0:3], v[224:227]\n"
      "s_add_u32 s67, s67, s101\n"
      "v_exp_f32 v112, v144\n"
      "v_mfma_f32_16x16x32_bf16 v[172:175], a[68:71], a[4:7], v[228:231]\n"
      "s_min_i32 s70, s70, s67\n"
      "v_cvt_pk_bf16_f32 v199, v114, v115\n"
      "v_exp_f32 v113, v145\n"
      "v_mfma_f32_16x16x32_bf16 v[180:183], a[68:71], a[8:11], v[232:235]\n"
      "s_max_i32 s70, s70, 85\n"
      "v_or3_b32 v116, v116, v198, v199\n"
      "v_mfma_f32_16x16x32_bf16 v[188:191], a[68:71], a[12:15], v[236:239]\n"
      "s_waitcnt lgkmcnt(4)\n"
      "v_exp_f32 v114, v146\n"
      "v_mfma_f32_16x16x32_bf16 v[160:163], a[72:75], a[16:19], v[160:163]\n"
      "s_add_u32 s66, s66, s86\n"
      "v_cvt_pk_bf16_f32 v200, v112, v113\n"
      "v_exp_f32 v115, v147\n"
      "v_mfma_f32_16x16x32_bf16 v[168:171], a[72:75], a[20:23], v[168:171]\n"
      "s_and_b32 s76, s80, 31\n"
      "v_exp_f32 v112, v148\n"
      "v_mfma_f32_16x16x32_bf16 v[176:179], a[72:75], a[24:27], v[176:179]\n"
      "s_lshr_b64 s[80:81], s[80:81], 5\n"
      "v_cvt_pk_bf16_f32 v201, v114, v115\n"
      "v_mfma_f32_16x16x32_bf16 v[184:187], a[72:75], a[28:31], v[184:187]\n"
      "s_add_u32 s66, s66, s76\n"
      "v_exp_f32 v113, v149\n"
      "v_mfma_f32_16x16x32_bf16 v[164:167], a[76:79], a[16:19], v[164:167]\n"
      "s_sub_u32 s67, s68, s82\n"
      "v_or3_b32 v116, v116, v200, v201\n"
      "v_exp_f32 v114, v150\n"
      "v_mfma_f32_16x16x32_bf16 v[172:175], a[76:79], a[20:23], v[172:175]\n"
      "s_min_i32 s76, s67, s83\n"
      "v_cvt_pk_bf16_f32 v202, v112, v113\n"
      "v_mfma_f32_16x16x32_bf16 v[180:183], a[76:79], a[24:27], v[180:183]\n"
      "s_add_u32 m0, s62, 0\n"
      "v_exp_f32 v115, v151\n"
      "v_mfma_f32_16x16x32_bf16 v[188:191], a[76:79], a[28:31], v[188:191]\n"
      "buffer_load_dwordx4 v248, s[20:23], s60 offen nt lds\n"
      "s_waitcnt lgkmcnt(2)\n"
      "v_mfma_f32_16x16x32_bf16 v[160:163], a[80:83], a[32:35], v[160:163]\n"
      "s_add_u32 m0, s62, 896\n"
      "v_exp_f32 v112, v152\n"
      "v_mfma_f32_16x16x32_bf16 v[168:171], a[80:83], a[36:39], v[168:171]\n"
      "s_mov_b32 s83, s67\n"
      "buffer_load_dwordx4 v248, s[20:23], s60 offen offset:128 nt lds\n"
      "v_mfma_f32_16x16x32_bf16 v[176:179], a[80:83], a[40:43], v[176:179]\n"
      "s_add_u32 m0, s62, 8192\n"
      "v_mfma_f32_16x16x32_bf16 v[184:187], a[80:83], a[44:47], v[184:187]\n"
      "s_sub_u32 s76, s76, 36\n"
      "buffer_load_dwordx4 v120, s[20:23], s60 offen nt lds\n"
      "s_waitcnt vmcnt(9)\n"
      "s_barrier\n"
      "v_mfma_f32_16x16x32_bf16 v[164:167], a[84:87], a[32:35], v[164:167]\n"
      "s_add_u32 m0, s62, 9088\n"
      "ds_read_b64_tr_b16 a[96:97], v246 offset:33792\n"
      "v_mfma_f32_16x16x32_bf16 v[172:175], a[84:87], a[36:39], v[172:175]\n"
      "s_min_i32 s76, s76, 148\n"
      "ds_read_b64_tr_b16 a[98:99], v246 offset:37888\n"
      "buffer_load_dwordx4 v120, s[20:23], s60 offen offset:128 nt lds\n"
      "v_mfma_f32_16x16x32_bf16 v[180:183], a[84:87], a[40:43], v[180:183]\n"
      "s_max_i32 s76, s76, 70\n"
      "ds_read_b64_tr_b16 a[100:101], v247 offset:33792\n"
      "v_mfma_f32_16x16x32_bf16 v[188:191], a[84:87], a[44:47], v[188:191]\n"
      "ds_read_b64_tr_b16 a[102:103], v247 offset:37888\n"
      "s_waitcnt lgkmcnt(4)\n"
      "v_mfma_f32_16x16x32_bf16 v[160:163], a[88:91], a[48:51], v[160:163]\n"
      "s_mov_b32 s82, s68\n"
      "ds_read_b64_tr_b16 a[104:105], v244 offset:33792\n"
      "v_cvt_pk_bf16_f32 v203, v114, v115\n"
      "v_mfma_f32_16x16x32_bf16 v[168:171], a[88:91], a[52:55], v[168:171]\n"
      "s_sub_u32 s66, s66, s99\n"
      "ds_read_b64_tr_b16 a[106:107], v244 offset:37888\n"
      "v_exp_f32 v113, v153\n"
      "v_mfma_f32_16x16x32_bf16 v[176:179], a[88:91], a[56:59], v[176:179]\n"
      "s_sub_u32 s82, s82, s99\n"
      "ds_read_b64_tr_b16 a[108:109], v245 offset:33792\n"
      "v_mfma_f32_16x16x32_bf16 v[184:187], a[88:91], a[60:63], v[184:187]\n"
      "s_add_u32 s65, s82, s76\n"
      "ds_read_b64_tr_b16 a[110:111], v245 offset:37888\n"
      "v_or3_b32 v116, v116, v202, v203\n"
      "v_mfma_f32_16x16x32_bf16 v[164:167], a[92:95], a[48:51], v[164:167]\n"
      "s_sub_u32 s93, s93, 1\n"
      "v_exp_f32 v114, v154\n"
      "v_mfma_f32_16x16x32_bf16 v[172:175], a[92:95], a[52:55], v[172:175]\n"
      "s_cmp_le_i32 s93, 14\n"
      "v_cvt_pk_bf16_f32 v204, v112, v113\n"
      "v_exp_f32 v115, v155\n"
      "v_mfma_f32_16x16x32_bf16 v[180:183], a[92:95], a[56:59], v[180:183]\n"
      "s_cselect_b32 s86, 107, s86\n"
      "v_exp_f32 v112, v156\n"
      "v_mfma_f32_16x16x32_bf16 v[188:191], a[92:95], a[60:63], v[188:191]\n"
      "s_waitcnt lgkmcnt(4)\n"
      "v_cvt_pk_bf16_f32 v205, v114, v115\n"
      "v_mfma_f32_16x16x32_bf16 a[160:163], a[96:99], v[208:211], a[160:163]\n"
      "s_sub_u32 s67, s65, s66\n"
      "v_exp_f32 v113, v157\n"
      "v_mfma_f32_16x16x32_bf16 a[164:167], a[96:99], v[212:215], a[164:167]\n"
      "s_cmp_lt_i32 s67, 0\n"
      "v_or3_b32 v116, v116, v204, v205\n"
      "v_exp_f32 v114, v158\n"
      "v_mfma_f32_16x16x32_bf16 a[168:171], a[96:99], v[216:219], a[168:171]\n"
      "s_cselect_b32 s88, s66, s65\n"
      "v_cvt_pk_bf16_f32 v206, v112, v113\n"
      "v_mfma_f32_16x16x32_bf16 a[172:175], a[96:99], v[220:223], a[172:175]\n"
      "s_add_u32 s90, s88, s91\n"
      "v_exp_f32 v115, v159\n"
      "s_nop 0\n"
      "v_mfma_f32_16x16x32_bf16 a[176:179], a[100:103], v[208:211], a[176:179]\n"
      "s_mov_b32 s64, 4\n"
      "v_cvt_pk_bf16_f32 v207, v114, v115\n"
      "v_or3_b32 v116, v116, v206, v207\n"
      "v_mfma_f32_16x16x32_bf16 a[180:183], a[100:103], v[212:215], a[180:183]\n"
      "v_and_b32 v116, 0x40004000, v116\n"
      "v_cmp_ne_u32 vcc, 0, v116\n"
      "s_cbranch_vccnz FB0_%=\n"
      "RET0_4_%=:\n"
      "v_mfma_f32_16x16x32_bf16 a[184:187], a[100:103], v[216:219], a[184:187]\n"
      "v_exp_f32 v112, v160\n"
      "v_mfma_f32_16x16x32_bf16 a[188:191], a[100:103], v[220:223], a[188:191]\n"
      "s_memrealtime s[68:69]\n"
      "v_xor_b32 v242, 0x10000, v242\n"
      "v_mfma_f32_16x16x32_bf16 a[192:195], a[196:199], v[192:195], a[192:195]\n"
      "v_exp_f32 v113, v161\n"
      "v_mfma_f32_16x16x32_bf16 a[192:195], a[200:203], v[196:199], a[192:195]\n"
      "v_xor_b32 v243, 0x10000, v243\n"
      "v_exp_f32 v114, v162\n"
      "v_mfma_f32_16x16x32_bf16 a[192:195], a[204:207], v[200:203], a[192:195]\n"
      "v_cvt_pk_bf16_f32 v0, v112, v113\n"
      "v_mfma_f32_16x16x32_bf16 a[192:195], a[208:211], v[204:207], a[192:195]\n"
      "v_exp_f32 v115, v163\n"
      "s_waitcnt vmcnt(6) lgkmcnt(0)\n"
      "s_sub_u32 s67, s68, s90\n"
      "s_cmp_lt_i32 s67, 0\n"
      "s_cbranch_scc1 GW8_%=\n"
      "GX8_%=:\n"
      "s_barrier\n"
      "v_mfma_f32_16x16x32_bf16 v[64:67], a[104:107], v[208:211], v[64:67]\n"
      "ds_read_b128 a[64:67], v242 offset:0\n"
      "ds_read_b128 a[68:71], v242 offset:4096\n"
      "v_mfma_f32_16x16x32_bf16 v[68:71], a[104:107], v[212:215], v[68:71]\n"
      "ds_read_b128 a[72:75], v243 offset:0\n"
      "ds_read_b128 a[76:79], v243 offset:4096\n"
      "v_mfma_f32_16x16x32_bf16 v[72:75], a[104:107], v[216:219], v[72:75]\n"
      "ds_read_b128 a[80:83], v242 offset:1024\n"
      "ds_read_b128 a[84:87], v242 offset:5120\n"
      "v_exp_f32 v112, v164\n"
      "v_mfma_f32_16x16x32_bf16 v[76:79], a[104:107], v[220:223], v[76:79]\n"
      "ds_read_b128 a[88:91], v243 offset:1024\n"
      "ds_read_b128 a[92:95], v243 offset:5120\n"
      "v_mfma_f32_16x16x32_bf16 v[80:83], a[108:111], v[208:211], v[80:83]\n"
      "v_cvt_pk_bf16_f32 v1, v114, v115\n"
      "v_mfma_f32_16x16x32_bf16 v[84:87], a[108:111], v[212:215], v[84:87]\n"
      "v_exp_f32 v113, v165\n"
      "v_or_b32 v116, v0, v1\n"
      "v_mfma_f32_16x16x32_bf16 v[88:91], a[108:111], v[216:219], v[88:91]\n"
      "v_exp_f32 v114, v166\n"
      "v_cvt_pk_bf16_f32 v2, v112, v113\n"
      "v_mfma_f32_16x16x32_bf16 v[92:95], a[108:111], v[220:223], v[92:95]\n"
      "s_waitcnt lgkmcnt(6)\n"
      "v_exp_f32 v115, v167\n"
      "v_mfma_f32_16x16x32_bf16 v[128:131], a[64:67], a[0:3], v[224:227]\n"
      "v_exp_f32 v112, v168\n"
      "v_mfma_f32_16x16x32_bf16 v[136:139], a[64:67], a[4:7], v[228:231]\n"
      "s_add_u32 m0, %[wb], 115712\n"
      "v_cvt_pk_bf16_f32 v3, v114, v115\n"
      "v_exp_f32 v113, v169\n"
      "v_mfma_f32_16x16x32_bf16 v[144:147], a[64:67], a[8:11], v[232:235]\n"
      "buffer_load_dwordx4 v121, s[24:27], s95 offen nt lds\n"
      "v_mfma_f32_16x16x32_bf16 v[152:155], a[64:67], a[12:15], v[236:239]\n"
      "s_add_u32 m0, %[wb], 123904\n"
      "v_or3_b32 v116, v116, v2, v3\n"
      "v_mfma_f32_16x16x32_bf16 v[132:135], a[68:71], a[0:3], v[224:227]\n"
      "buffer_load_dwordx4 v123, s[24:27], s95 offen nt lds\n"
      "v_mfma_f32_16x16x32_bf16 v[140:143], a[68:71], a[4:7], v[228:231]\n"
      "s_add_u32 m0, %[wb], 17408\n"
      "v_exp_f32 v114, v170\n"
      "v_mfma_f32_16x16x32_bf16 v[148:151], a[68:71], a[8:11], v[232:235]\n"
      "buffer_load_dwordx4 v121, s[24:27], s77 offen nt lds\n"
      "v_mfma_f32_16x16x32_bf16 v[156:159], a[68:71], a[12:15], v[236:239]\n"
      "s_waitcnt lgkmcnt(4)\n"
      "s_add_u32 m0, %[wb], 25600\n"
      "v_cvt_pk_bf16_f32 v4, v112, v113\n"
      "v_mfma_f32_16x16x32_bf16 v[128:131], a[72:75], a[16:19], v[128:131]\n"
      "buffer_load_dwordx4 v123, s[24:27], s77 offen nt lds\n"
      "v_mfma_f32_16x16x32_bf16 v[136:139], a[72:75], a[20:23], v[136:139]\n"
      "v_exp_f32 v115, v171\n"
      "v_mfma_f32_16x16x32_bf16 v[144:147], a[72:75], a[24:27], v[144:147]\n"
      "v_exp_f32 v112, v172\n"
      "v_cvt_pk_bf16_f32 v5, v114, v115\n"
      "v_mfma_f32_16x16x32_bf16 v[152:155], a[72:75], a[28:31], v[152:155]\n"
      "v_exp_f32 v113, v173\n"
      "v_mfma_f32_16x16x32_bf16 v[132:135], a[76:79], a[16:19], v[132:135]\n"
      "v_or3_b32 v116, v116, v4, v5\n"
      "v_exp_f32 v114, v174\n"
      "v_mfma_f32_16x16x32_bf16 v[140:143], a[76:79], a[20:23], v[140:143]\n"
      "v_cvt_pk_bf16_f32 v6, v112, v113\n"
      "v_exp_f32 v115, v175\n"
      "v_mfma_f32_16x16x32_bf16 v[148:151], a[76:79], a[24:27], v[148:151]\n"
      "v_exp_f32 v112, v176\n"
      "v_mfma_f32_16x16x32_bf16 v[156:159], a[76:79], a[28:31], v[156:159]\n"
      "s_waitcnt lgkmcnt(2)\n"
      "v_cvt_pk_bf16_f32 v7, v114, v115\n"
      "v_mfma_f32_16x16x32_bf16 v[128:131], a[80:83], a[32:35], v[128:131]\n"
      "v_exp_f32 v113, v177\n"
      "v_or3_b32 v116, v116, v6, v7\n"
      "v_mfma_f32_16x16x32_bf16 v[136:139], a[80:83], a[36:39], v[136:139]\n"
      "v_exp_f32 v114, v178\n"
      "v_cvt_pk_bf16_f32 v8, v112, v113\n"
      "v_mfma_f32_16x16x32_bf16 v[144:147], a[80:83], a[40:43], v[144:147]\n"
      "v_exp_f32 v115, v179\n"
      "v_mfma_f32_16x16x32_bf16 v[152:155], a[80:83], a[44:47], v[152:155]\n"
      "v_exp_f32 v112, v180\n"
      "s_waitcnt vmcnt(8)\n"
      "s_barrier\n"
      "v_mfma_f32_16x16x32_bf16 v[132:135], a[84:87], a[32:35], v[132:135]\n"
      "ds_read_b64_tr_b16 a[112:113], v250 offset:1024\n"
      "v_cvt_pk_bf16_f32 v9, v114, v115\n"
      "v_mfma_f32_16x16x32_bf16 v[140:143], a[84:87], a[36:39], v[140:143]\n"
      "ds_read_b64_tr_b16 a[114:115], v250 offset:5120\n"
      "v_exp_f32 v113, v181\n"
      "v_mfma_f32_16x16x32_bf16 v[148:151], a[84:87], a[40:43], v[148:151]\n"
      "ds_read_b64_tr_b16 a[116:117], v251 offset:1024\n"
      "v_or3_b32 v116, v116, v8, v9\n"
      "v_mfma_f32_16x16x32_bf16 v[156:159], a[84:87], a[44:47], v[156:159]\n"
      "ds_read_b64_tr_b16 a[118:119], v251 offset:5120\n"
      "v_exp_f32 v114, v182\n"
      "s_waitcnt lgkmcnt(4)\n"
      "v_mfma_f32_16x16x32_bf16 v[128:131], a[88:91], a[48:51], v[128:131]\n"
      "ds_read_b64_tr_b16 a[120:121], v240 offset:1024\n"
      "v_cvt_pk_bf16_f32 v10, v112, v113\n"
      "v_mfma_f32_16x16x32_bf16 v[136:139], a[88:91], a[52:55], v[136:139]\n"
      "ds_read_b64_tr_b16 a[122:123], v240 offset:5120\n"
      "v_exp_f32 v115, v183\n"
      "v_mfma_f32_16x16x32_bf16 v[144:147], a[88:91], a[56:59], v[144:147]\n"
      "ds_read_b64_tr_b16 a[124:125], v241 offset:1024\n"
      "v_mfma_f32_16x16x32_bf16 v[152:155], a[88:91], a[60:63], v[152:155]\n"
      "ds_read_b64_tr_b16 a[126:127], v241 offset:5120\n"
      "v_exp_f32 v112, v184\n"
      "v_mfma_f32_16x16x32_bf16 v[132:135], a[92:95], a[48:51], v[132:135]\n"
      "v_cvt_pk_bf16_f32 v11, v114, v115\n"
      "v_exp_f32 v113, v185\n"
      "v_mfma_f32_16x16x32_bf16 v[140:143], a[92:95], a[52:55], v[140:143]\n"
      "v_or3_b32 v116, v116, v10, v11\n"
      "v_exp_f32 v114, v186\n"
      "v_mfma_f32_16x16x32_bf16 v[148:151], a[92:95], a[56:59], v[148:151]\n"
      "v_cvt_pk_bf16_f32 v12, v112, v113\n"
      "v_mfma_f32_16x16x32_bf16 v[156:159], a[92:95], a[60:63], v[156:159]\n"
      "s_waitcnt lgkmcnt(4)\n"
      "v_exp_f32 v115, v187\n"
      "v_mfma_f32_16x16x32_bf16 a[160:163], a[112:115], v[192:195], a[160:163]\n"
      "v_exp_f32 v112, v188\n"
      "v_cvt_pk_bf16_f32 v13, v114, v115\n"
      "v_mfma_f32_16x16x32_bf16 a[164:167], a[112:115], v[196:199], a[164:167]\n"
      "v_exp_f32 v113, v189\n"
      "v_or3_b32 v116, v116, v12, v13\n"
      "v_mfma_f32_16x16x32_bf16 a[168:171], a[112:115], v[200:203], a[168:171]\n"
      "v_exp_f32 v114, v190\n"
      "v_mfma_f32_16x16x32_bf16 a[172:175], a[112:115], v[204:207], a[172:175]\n"
      "v_cvt_pk_bf16_f32 v14, v112, v113\n"
      "v_exp_f32 v115, v191\n"
      "v_mfma_f32_16x16x32_bf16 a[176:179], a[116:119], v[192:195], a[176:179]\n"
      "s_mov_b32 s64, 5\n"
      "s_nop 0\n"
      "v_cvt_pk_bf16_f32 v15, v114, v115\n"
      "v_or3_b32 v116, v116, v14, v15\n"
      "v_mfma_f32_16x16x32_bf16 a[180:183], a[116:119], v[196:199], a[180:183]\n"
      "v_and_b32 v116, 0x40004000, v116\n"
      "v_cmp_ne_u32 vcc, 0, v116\n"
      "s_cbranch_vccnz FB1_%=\n"
      "RET1_5_%=:\n"
      "v_mfma_f32_16x16x32_bf16 a[184:187], a[116:119], v[200:203], a[184:187]\n"
      "v_exp_f32 v112, v128\n"
      "v_exp_f32 v113, v129\n"
      "v_mfma_f32_16x16x32_bf16 a[188:191], a[116:119], v[204:207], a[188:191]\n"
      "s_memrealtime s[68:69]\n"
      "s_add_u32 m0, %[wb], 50176\n"
      "v_exp_f32 v114, v130\n"
      "v_cvt_pk_bf16_f32 v16, v112, v113\n"
      "v_mfma_f32_16x16x32_bf16 a[192:195], a[196:199], v[0:3], a[192:195]\n"
      "buffer_load_dwordx4 v121, s[24:27], s78 offen nt lds\n"
      "v_mfma_f32_16x16x32_bf16 a[192:195], a[200:203], v[4:7], a[192:195]\n"
      "s_add_u32 m0, %[wb], 58368\n"
      "v_exp_f32 v115, v131\n"
      "v_exp_f32 v112, v132\n"
      "v_mfma_f32_16x16x32_bf16 a[192:195], a[204:207], v[8:11], a[192:195]\n"
      "buffer_load_dwordx4 v123, s[24:27], s78 offen nt lds\n"
      "v_cvt_pk_bf16_f32 v17, v114, v115\n"
      "v_mfma_f32_16x16x32_bf16 a[192:195], a[208:211], v[12:15], a[192:195]\n"
      "v_exp_f32 v113, v133\n"
      "v_or_b32 v116, v16, v17\n"
      "s_add_u32 s72, s72, 0x7f4a7c15\n"
      "s_addc_u32 s76, s79, 0x9e3779b9\n"
      "s_waitcnt vmcnt(6) lgkmcnt(0)\n"
      "s_barrier\n"
      "v_mfma_f32_16x16x32_bf16 v[64:67], a[120:123], v[192:195], v[64:67]\n"
      "s_xor_b32 s80, %[xo], 0x400000\n"
      "ds_read_b128 a[64:67], v242 offset:32768\n"
      "ds_read_b128 a[68:71], v242 offset:36864\n"
      "v_mfma_f32_16x16x32_bf16 v[68:71], a[120:123], v[196:199], v[68:71]\n"
      "s_add_u32 s79, s80, 0x1000\n"
      "ds_read_b128 a[72:75], v243 offset:32768\n"
      "ds_read_b128 a[76:79], v243 offset:36864\n"
      "v_exp_f32 v114, v134\n"
      "v_mfma_f32_16x16x32_bf16 v[72:75], a[120:123], v[200:203], v[72:75]\n"
      "s_add_u32 s81, %[xo], 0x1000\n"
      "ds_read_b128 a[80:83], v242 offset:33792\n"
      "ds_read_b128 a[84:87], v242 offset:37888\n"
      "v_cvt_pk_bf16_f32 v18, v112, v113\n"
      "v_mfma_f32_16x16x32_bf16 v[76:79], a[120:123], v[204:207], v[76:79]\n"
      "s_mov_b32 s63, 0x100000\n"
      "ds_read_b128 a[88:91], v243 offset:33792\n"
      "ds_read_b128 a[92:95], v243 offset:37888\n"
      "v_mfma_f32_16x16x32_bf16 v[80:83], a[124:127], v[192:195], v[80:83]\n"
      "s_mov_b32 s60, 0x928b2f\n"
      "v_exp_f32 v115, v135\n"
      "v_exp_f32 v112, v136\n"
      "v_mfma_f32_16x16x32_bf16 v[84:87], a[124:127], v[196:199], v[84:87]\n"
      "s_mov_b32 s62, 0x9818e9\n"
      "v_cvt_pk_bf16_f32 v19, v114, v115\n"
      "v_exp_f32 v113, v137\n"
      "v_mfma_f32_16x16x32_bf16 v[88:91], a[124:127], v[200:203], v[88:91]\n"
      "s_mov_b32 s70, 0xdd9dc9\n"
      "v_xor_b32 v248, 0x2000, v244\n"
      "v_or3_b32 v116, v116, v18, v19\n"
      "v_mfma_f32_16x16x32_bf16 v[92:95], a[124:127], v[204:207], v[92:95]\n"
      "s_waitcnt lgkmcnt(6)\n"
      "v_exp_f32 v114, v138\n"
      "v_cvt_pk_bf16_f32 v20, v112, v113\n"
      "v_mfma_f32_16x16x32_bf16 v[160:163], a[64:67], a[0:3], v[224:227]\n"
      "s_mov_b32 s71, 0x8ed905\n"
      "v_exp_f32 v115, v139\n"
      "v_mfma_f32_16x16x32_bf16 v[168:171], a[64:67], a[4:7], v[228:231]\n"
      "s_sub_u32 s88, 311, %[cph]\n"
      "v_xor_b32 v120, 0x2000, v245\n"
      "v_exp_f32 v112, v140\n"
      "v_cvt_pk_bf16_f32 v21, v114, v115\n"
      "v_mfma_f32_16x16x32_bf16 v[176:179], a[64:67], a[8:11], v[232:235]\n"
      "s_add_u32 s88, s88, s66\n"
      "v_exp_f32 v113, v141\n"
      "v_mfma_f32_16x16x32_bf16 v[184:187], a[64:67], a[12:15], v[236:239]\n"
      "s_add_u32 s88, s88, s100\n"
      "v_or3_b32 v116, v116, v20, v21\n"
      "v_exp_f32 v114, v142\n"
      "v_mfma_f32_16x16x32_bf16 v[164:167], a[68:71], a[0:3], v[224:227]\n"
      "v_mbcnt_lo_u32_b32 v127, -1, 0\n"
      "v_cvt_pk_bf16_f32 v22, v112, v113\n"
      "v_exp_f32 v115, v143\n"
      "v_mfma_f32_16x16x32_bf16 v[172:175], a[68:71], a[4:7], v[228:231]\n"
      "v_exp_f32 v112, v144\n"
      "v_mfma_f32_16x16x32_bf16 v[180:183], a[68:71], a[8:11], v[232:235]\n"
      "v_cvt_pk_bf16_f32 v23, v114, v115\n"
      "v_exp_f32 v113, v145\n"
      "v_mfma_f32_16x16x32_bf16 v[188:191], a[68:71], a[12:15], v[236:239]\n"
      "s_waitcnt lgkmcnt(4)\n"
      "v_mbcnt_hi_u32_b32 v127, -1, v127\n"
      "v_or3_b32 v116, v116, v22, v23\n"
      "v_mfma_f32_16x16x32_bf16 v[160:163], a[72:75], a[16:19], v[160:163]\n"
      "v_exp_f32 v114, v146\n"
      "v_cvt_pk_bf16_f32 v24, v112, v113\n"
      "v_mfma_f32_16x16x32_bf16 v[168:171], a[72:75], a[20:23], v[168:171]\n"
      "s_add_u32 m0, %[wb], 49152\n"
      "v_exp_f32 v115, v147\n"
      "v_exp_f32 v112, v148\n"
      "v_mfma_f32_16x16x32_bf16 v[176:179], a[72:75], a[24:27], v[176:179]\n"
      "buffer_load_dwordx4 v249, s[24:27], s61 offen nt lds\n"
      "v_lshlrev_b32 v127, 4, v127\n"
      "v_mfma_f32_16x16x32_bf16 v[184:187], a[72:75], a[28:31], v[184:187]\n"
      "s_add_u32 m0, %[wb], 57344\n"
      "v_cvt_pk_bf16_f32 v25, v114, v115\n"
      "v_mfma_f32_16x16x32_bf16 v[164:167], a[76:79], a[16:19], v[164:167]\n"
      "buffer_load_dwordx4 v122, s[24:27], s61 offen nt lds\n"
      "v_mfma_f32_16x16x32_bf16 v[172:175], a[76:79], a[20:23], v[172:175]\n"
      "v_add_u32 v125, %[xw], v127\n"
      "v_exp_f32 v113, v149\n"
      "v_mfma_f32_16x16x32_bf16 v[180:183], a[76:79], a[24:27], v[180:183]\n"
      "v_or3_b32 v116, v116, v24, v25\n"
      "v_exp_f32 v114, v150\n"
      "v_mfma_f32_16x16x32_bf16 v[188:191], a[76:79], a[28:31], v[188:191]\n"
      "s_waitcnt lgkmcnt(2)\n"
      "v_cvt_pk_bf16_f32 v26, v112, v113\n"
      "v_exp_f32 v115, v151\n"
      "v_mfma_f32_16x16x32_bf16 v[160:163], a[80:83], a[32:35], v[160:163]\n"
      "v_add_u32 v126, %[xr], v127\n"
      "v_exp_f32 v112, v152\n"
      "v_mfma_f32_16x16x32_bf16 v[168:171], a[80:83], a[36:39], v[168:171]\n"
      "v_cvt_pk_bf16_f32 v27, v114, v115\n"
      "v_exp_f32 v113, v153\n"
      "v_mfma_f32_16x16x32_bf16 v[176:179], a[80:83], a[40:43], v[176:179]\n"
      "v_or3_b32 v116, v116, v26, v27\n"
      "v_mfma_f32_16x16x32_bf16 v[184:187], a[80:83], a[44:47], v[184:187]\n"
      "v_exp_f32 v114, v154\n"
      "v_cvt_pk_bf16_f32 v28, v112, v113\n"
      "v_mfma_f32_16x16x32_bf16 v[164:167], a[84:87], a[32:35], v[164:167]\n"
      "v_exp_f32 v115, v155\n"
      "v_exp_f32 v112, v156\n"
      "v_mfma_f32_16x16x32_bf16 v[172:175], a[84:87], a[36:39], v[172:175]\n"
      "v_cvt_pk_bf16_f32 v29, v114, v115\n"
      "v_exp_f32 v113, v157\n"
      "v_mfma_f32_16x16x32_bf16 v[180:183], a[84:87], a[40:43], v[180:183]\n"
      "v_or3_b32 v116, v116, v28, v29\n"
      "v_exp_f32 v114, v158\n"
      "v_mov_b32 v123, s88\n"
      "v_mfma_f32_16x16x32_bf16 v[188:191], a[84:87], a[44:47], v[188:191]\n"
      "s_waitcnt lgkmcnt(0)\n"
      "v_cvt_pk_bf16_f32 v30, v112, v113\n"
      "v_exp_f32 v115, v159\n"
      "v_add_u32 v124, 12288, v125\n"
      "s_waitcnt vmcnt(4)\n"
      "s_barrier\n"
      "v_mfma_f32_16x16x32_bf16 v[160:163], a[88:91], a[48:51], v[160:163]\n"
      "ds_read_b64_tr_b16 a[96:97], v250 offset:33792\n"
      "ds_read_b64_tr_b16 a[98:99], v250 offset:37888\n"
      "s_nop 0\n"
      "v_mfma_f32_16x16x32_bf16 v[168:171], a[88:91], a[52:55], v[168:171]\n"
      "s_add_u32 m0, %[wb], 81920\n"
      "ds_read_b64_tr_b16 a[100:101], v251 offset:33792\n"
      "ds_read_b64_tr_b16 a[102:103], v251 offset:37888\n"
      "v_cvt_pk_bf16_f32 v31, v114, v115\n"
      "v_mfma_f32_16x16x32_bf16 v[176:179], a[88:91], a[56:59], v[176:179]\n"
      "buffer_load_dwordx4 v249, s[24:27], s94 offen nt lds\n"
      "ds_read_b64_tr_b16 a[112:113], v246 offset:1024\n"
      "ds_read_b64_tr_b16 a[114:115], v246 offset:5120\n"
      "v_mfma_f32_16x16x32_bf16 v[184:187], a[88:91], a[60:63], v[184:187]\n"
      "s_add_u32 m0, %[wb], 90112\n"
      "ds_read_b64_tr_b16 a[116:117], v247 offset:1024\n"
      "ds_read_b64_tr_b16 a[118:119], v247 offset:5120\n"
      "v_mfma_f32_16x16x32_bf16 v[164:167], a[92:95], a[48:51], v[164:167]\n"
      "buffer_load_dwordx4 v122, s[24:27], s94 offen nt lds\n"
      "ds_read_b64_tr_b16 a[104:105], v240 offset:33792\n"
      "v_mfma_f32_16x16x32_bf16 v[172:175], a[92:95], a[52:55], v[172:175]\n"
      "s_mov_b32 s64, 5\n"
      "ds_read_b64_tr_b16 a[106:107], v240 offset:37888\n"
      "v_mfma_f32_16x16x32_bf16 v[180:183], a[92:95], a[56:59], v[180:183]\n"
      "ds_read_b64_tr_b16 a[108:109], v241 offset:33792\n"
      "v_or3_b32 v116, v116, v30, v31\n"
      "v_mfma_f32_16x16x32_bf16 v[188:191], a[92:95], a[60:63], v[188:191]\n"
      "ds_read_b64_tr_b16 a[110:111], v241 offset:37888\n"
      "v_and_b32 v116, 0x40004000, v116\n"
      "v_cmp_ne_u32 vcc, 0, v116\n"
      "s_waitcnt lgkmcnt(4)\n"
      "s_cbranch_vccnz FB0_%=\n"
      "RET0_5_%=:\n"
      "v_mfma_f32_16x16x32_bf16 a[160:163], a[96:99], v[0:3], a[160:163]\n"
      "ds_read_b64_tr_b16 a[120:121], v244 offset:1024\n"
      "v_mov_b32 v117, v224\n"
      "v_xor_b32 v242, 0x10000, v242\n"
      "v_xor_b32 v243, 0x10000, v243\n"
      "v_mfma_f32_16x16x32_bf16 a[176:179], a[100:103], v[0:3], a[176:179]\n"
      "ds_read_b64_tr_b16 a[122:123], v244 offset:5120\n"
      "v_exp_f32 v112, v160\n"
      "v_mfma_f32_16x16x32_bf16 a[160:163], a[112:115], v[16:19], a[160:163]\n"
      "s_mov_b32 s64, 6\n"
      "ds_read_b64_tr_b16 a[124:125], v245 offset:1024\n"
      "v_exp_f32 v113, v161\n"
      "v_exp_f32 v114, v162\n"
      "v_mfma_f32_16x16x32_bf16 a[176:179], a[116:119], v[16:19], a[176:179]\n"
      "ds_read_b64_tr_b16 a[126:127], v245 offset:5120\n"
      "v_cvt_pk_bf16_f32 v96, v112, v113\n"
      "v_exp_f32 v115, v163\n"
      "v_mfma_f32_16x16x32_bf16 a[164:167], a[96:99], v[4:7], a[164:167]\n"
      "v_mov_b32_dpp v117, v228 quad_perm:[0,1,2,3] row_mask:0x2 bank_mask:0xf\n"
      "v_exp_f32 v112, v164\n"
      "v_mfma_f32_16x16x32_bf16 a[180:183], a[100:103], v[4:7], a[180:183]\n"
      "v_cvt_pk_bf16_f32 v97, v114, v115\n"
      "v_exp_f32 v113, v165\n"
      "v_or_b32 v116, v96, v97\n"
      "v_mfma_f32_16x16x32_bf16 a[164:167], a[112:115], v[20:23], a[164:167]\n"
      "v_exp_f32 v114, v166\n"
      "v_cvt_pk_bf16_f32 v98, v112, v113\n"
      "v_mfma_f32_16x16x32_bf16 a[180:183], a[116:119], v[20:23], a[180:183]\n"
      "ds_write_b128 v125, a[160:163] offset:0\n"
      "v_exp_f32 v115, v167\n"
      "v_mfma_f32_16x16x32_bf16 a[168:171], a[96:99], v[8:11], a[168:171]\n"
      "v_mov_b32_dpp v117, v232 quad_perm:[0,1,2,3] row_mask:0x4 bank_mask:0xf\n"
      "v_exp_f32 v112, v168\n"
      "v_cvt_pk_bf16_f32 v99, v114, v115\n"
      "v_mfma_f32_16x16x32_bf16 a[184:187], a[100:103], v[8:11], a[184:187]\n"
      "s_add_u32 m0, %[wb], 114688\n"
      "ds_write_b128 v125, v[96:99] offset:8192\n"
      "v_exp_f32 v113, v169\n"
      "v_mfma_f32_16x16x32_bf16 a[168:171], a[112:115], v[24:27], a[168:171]\n"
      "buffer_load_dwordx4 v249, s[24:27], s95 offen nt lds\n"
      "v_or3_b32 v116, v116, v98, v99\n"
      "v_mfma_f32_16x16x32_bf16 a[184:187], a[116:119], v[24:27], a[184:187]\n"
      "s_add_u32 m0, %[wb], 122880\n"
      "v_exp_f32 v114, v170\n"
      "v_cvt_pk_bf16_f32 v100, v112, v113\n"
      "v_mfma_f32_16x16x32_bf16 a[172:175], a[96:99], v[12:15], a[172:175]\n"
      "buffer_load_dwordx4 v122, s[24:27], s95 offen nt lds\n"
      "ds_write_b128 v125, a[176:179] offset:4096\n"
      "v_mov_b32_dpp v117, v236 quad_perm:[0,1,2,3] row_mask:0x8 bank_mask:0xf\n"
      "v_mfma_f32_16x16x32_bf16 a[188:191], a[100:103], v[12:15], a[188:191]\n"
      "v_exp_f32 v115, v171\n"
      "v_mfma_f32_16x16x32_bf16 a[172:175], a[112:115], v[28:31], a[172:175]\n"
      "v_exp_f32 v112, v172\n"
      "v_cvt_pk_bf16_f32 v101, v114, v115\n"
      "v_exp_f32 v113, v173\n"
      "v_mfma_f32_16x16x32_bf16 a[188:191], a[116:119], v[28:31], a[188:191]\n"
      "ds_write_b128 v125, a[164:167] offset:1024\n"
      "v_or3_b32 v116, v116, v100, v101\n"
      "v_mfma_f32_16x16x32_bf16 a[192:195], a[196:199], v[16:19], a[192:195]\n"
      "ds_write2_b32 v124, v117, v123 offset0:1 offset1:2\n"
      "v_exp_f32 v114, v174\n"
      "v_cvt_pk_bf16_f32 v102, v112, v113\n"
      "v_mfma_f32_16x16x32_bf16 a[192:195], a[200:203], v[20:23], a[192:195]\n"
      "v_exp_f32 v115, v175\n"
      "v_exp_f32 v112, v176\n"
      "v_mfma_f32_16x16x32_bf16 a[192:195], a[204:207], v[24:27], a[192:195]\n"
      "ds_write_b128 v125, a[180:183] offset:5120\n"
      "v_cvt_pk_bf16_f32 v103, v114, v115\n"
      "v_mfma_f32_16x16x32_bf16 a[192:195], a[208:211], v[28:31], a[192:195]\n"
      "s_waitcnt lgkmcnt(6)\n"
      "v_exp_f32 v113, v177\n"
      "v_or3_b32 v116, v116, v102, v103\n"
      "v_exp_f32 v114, v178\n"
      "v_mfma_f32_16x16x32_bf16 v[64:67], a[104:107], v[0:3], v[64:67]\n"
      "v_cvt_pk_bf16_f32 v104, v112, v113\n"
      "v_exp_f32 v115, v179\n"
      "v_mfma_f32_16x16x32_bf16 v[68:71], a[104:107], v[4:7], v[68:71]\n"
      "ds_write_b128 v125, v[100:103] offset:9216\n"
      "v_exp_f32 v112, v180\n"
      "s_waitcnt vmcnt(6)\n"
      "s_barrier\n"
      "v_mfma_f32_16x16x32_bf16 v[72:75], a[104:107], v[8:11], v[72:75]\n"
      "ds_read_b64_tr_b16 a[212:213], v244 offset:33792\n"
      "v_cvt_pk_bf16_f32 v105, v114, v115\n"
      "v_exp_f32 v113, v181\n"
      "v_mfma_f32_16x16x32_bf16 v[76:79], a[104:107], v[12:15], v[76:79]\n"
      "s_add_u32 m0, %[wb], 16384\n"
      "ds_write_b128 v125, a[168:171] offset:2048\n"
      "ds_read_b64_tr_b16 a[214:215], v244 offset:37888\n"
      "v_or3_b32 v116, v116, v104, v105\n"
      "v_mfma_f32_16x16x32_bf16 a[228:231], a[196:199], v[96:99], a[192:195]\n"
      "buffer_load_dwordx4 v249, s[24:27], s77 offen nt lds\n"
      "ds_read_b64_tr_b16 a[216:217], v245 offset:33792\n"
      "v_mfma_f32_16x16x32_bf16 v[80:83], a[108:111], v[0:3], v[80:83]\n"
      "s_add_u32 m0, %[wb], 24576\n"
      "ds_read_b64_tr_b16 a[218:219], v245 offset:37888\n"
      "v_exp_f32 v114, v182\n"
      "v_mfma_f32_16x16x32_bf16 v[84:87], a[108:111], v[4:7], v[84:87]\n"
      "buffer_load_dwordx4 v122, s[24:27], s77 offen nt lds\n"
      "ds_write_b128 v125, a[184:187] offset:6144\n"
      "ds_read_b64_tr_b16 a[220:221], v248 offset:33792\n"
      "v_mfma_f32_16x16x32_bf16 v[88:91], a[108:111], v[8:11], v[88:91]\n"
      "ds_read_b64_tr_b16 a[222:223], v248 offset:37888\n"
      "v_cvt_pk_bf16_f32 v106, v112, v113\n"
      "v_mfma_f32_16x16x32_bf16 v[92:95], a[108:111], v[12:15], v[92:95]\n"
      "ds_read_b64_tr_b16 a[224:225], v120 offset:33792\n"
      "v_exp_f32 v115, v183\n"
      "v_mfma_f32_16x16x32_bf16 v[64:67], a[120:123], v[16:19], v[64:67]\n"
      "ds_write_b128 v125, a[172:175] offset:3072\n"
      "ds_read_b64_tr_b16 a[226:227], v120 offset:37888\n"
      "v_exp_f32 v112, v184\n"
      "v_mfma_f32_16x16x32_bf16 v[68:71], a[120:123], v[20:23], v[68:71]\n"
      "v_cvt_pk_bf16_f32 v107, v114, v115\n"
      "v_exp_f32 v113, v185\n"
      "v_mfma_f32_16x16x32_bf16 v[72:75], a[120:123], v[24:27], v[72:75]\n"
      "v_or3_b32 v116, v116, v106, v107\n"
      "v_exp_f32 v114, v186\n"
      "v_cvt_pk_bf16_f32 v108, v112, v113\n"
      "v_mfma_f32_16x16x32_bf16 v[76:79], a[120:123], v[28:31], v[76:79]\n"
      "ds_write_b128 v125, v[104:107] offset:10240\n"
      "v_exp_f32 v115, v187\n"
      "v_mfma_f32_16x16x32_bf16 a[228:231], a[200:203], v[100:103], a[228:231]\n"
      "v_exp_f32 v112, v188\n"
      "v_cvt_pk_bf16_f32 v109, v114, v115\n"
      "v_mfma_f32_16x16x32_bf16 v[80:83], a[124:127], v[16:19], v[80:83]\n"
      "v_exp_f32 v113, v189\n"
      "v_or3_b32 v116, v116, v108, v109\n"
      "v_exp_f32 v114, v190\n"
      "v_mfma_f32_16x16x32_bf16 v[84:87], a[124:127], v[20:23], v[84:87]\n"
      "s_add_u32 m0, %[wb], 82944\n"
      "ds_write_b128 v125, a[188:191] offset:7168\n"
      "v_cvt_pk_bf16_f32 v110, v112, v113\n"
      "v_mfma_f32_16x16x32_bf16 v[88:91], a[124:127], v[24:27], v[88:91]\n"
      "buffer_load_dwordx4 v249, s[24:27], s78 offen nt lds\n"
      "v_exp_f32 v115, v191\n"
      "v_mfma_f32_16x16x32_bf16 v[92:95], a[124:127], v[28:31], v[92:95]\n"
      "s_add_u32 m0, %[wb], 91136\n"
      "s_nop 0\n"
      "v_cvt_pk_bf16_f32 v111, v114, v115\n"
      "v_or3_b32 v116, v116, v110, v111\n"
      "v_and_b32 v116, 0x40004000, v116\n"
      "v_mfma_f32_16x16x32_bf16 a[228:231], a[204:207], v[104:107], a[228:231]\n"
      "buffer_load_dwordx4 v122, s[24:27], s78 offen nt lds\n"
      "v_cmp_ne_u32 vcc, 0, v116\n"
      "s_cbranch_vccnz FB1_%=\n"
      "RET1_6_%=:\n"
      "ds_write_b128 v125, v[108:111] offset:11264\n"
      "v_mfma_f32_16x16x32_bf16 a[228:231], a[208:211], v[108:111], a[228:231]\n"
      "s_waitcnt lgkmcnt(9)\n"
      "v_mfma_f32_16x16x32_bf16 v[64:67], a[212:215], v[96:99], v[64:67]\n"
      "v_mov_b32 v148, 1.0\n"
      "v_mfma_f32_16x16x32_bf16 v[80:83], a[216:219], v[96:99], v[80:83]\n"
      "v_mov_b32 v149, 1.0\n"
      "v_mfma_f32_16x16x32_bf16 v[68:71], a[212:215], v[100:103], v[68:71]\n"
      "v_mov_b32 v157, %[fo]\n"
      "v_mfma_f32_16x16x32_bf16 v[84:87], a[216:219], v[100:103], v[84:87]\n"
      "v_mov_b32 v150, s72\n"
      "v_mfma_f32_16x16x32_bf16 v[72:75], a[212:215], v[104:107], v[72:75]\n"
      "v_mov_b32 v151, s76\n"
      "ds_write_b32 v125, a228 offset:12288\n"
      "v_mfma_f32_16x16x32_bf16 v[88:91], a[216:219], v[104:107], v[88:91]\n"
      "v_xor_b32 v158, 8, v157\n"
      "v_mfma_f32_16x16x32_bf16 v[76:79], a[212:215], v[108:111], v[76:79]\n"
      "v_mfma_f32_16x16x32_bf16 v[92:95], a[216:219], v[108:111], v[92:95]\n"
      "s_waitcnt lgkmcnt(0)\n"
      "s_barrier\n"
      "s_memrealtime s[68:69]\n"
      "ds_read_b32 v237, v127 offset:12296\n"
      "ds_read_b64 v[152:153], v126 offset:12288\n"
      "ds_read_b128 v[160:163], v126 offset:0\n"
      "ds_read_b128 v[176:179], v126 offset:4096\n"
      "ds_read_b128 v[164:167], v126 offset:1024\n"
      "ds_read_b128 v[180:183], v126 offset:5120\n"
      "ds_read_b128 v[168:171], v126 offset:2048\n"
      "ds_read_b128 v[184:187], v126 offset:6144\n"
      "ds_read_b128 v[172:175], v126 offset:3072\n"
      "ds_read_b128 v[188:191], v126 offset:7168\n"
      "ds_read_b128 a[64:67], v126 offset:8192\n"
      "ds_read_b128 a[68:71], v126 offset:9216\n"
      "ds_read_b128 a[72:75], v126 offset:10240\n"
      "ds_read_b128 a[76:79], v126 offset:11264\n"
      "v_accvgpr_read_b32 v144, a228\n"
      "v_mov_b32 v236, v150\n"
      "s_waitcnt lgkmcnt(12)\n"
      "v_cmp_neq_f32 vcc, v153, v117\n"
      "v_readfirstlane_b32 s88, v237\n"
      "s_cbranch_vccnz MIS_%=\n"
      "MR_%=:\n"
      "v_mul_f32 v144, v144, v148\n"
      "v_mov_b32 v145, v117\n"
      "v_fmac_f32 v144, v152, v149\n"
      "s_waitcnt lgkmcnt(10)\n"
      "v_pk_add_f32 v[64:65], v[64:65], v[160:161]\n"
      "v_pk_add_f32 v[66:67], v[66:67], v[162:163]\n"
      "v_pk_add_f32 v[80:81], v[80:81], v[176:177]\n"
      "v_pk_add_f32 v[82:83], v[82:83], v[178:179]\n"
      "s_waitcnt lgkmcnt(8)\n"
      "v_pk_add_f32 v[68:69], v[68:69], v[164:165]\n"
      "v_pk_add_f32 v[70:71], v[70:71], v[166:167]\n"
      "v_pk_add_f32 v[84:85], v[84:85], v[180:181]\n"
      "v_pk_add_f32 v[86:87], v[86:87], v[182:183]\n"
      "s_waitcnt lgkmcnt(6)\n"
      "v_pk_add_f32 v[72:73], v[72:73], v[168:169]\n"
      "v_pk_add_f32 v[74:75], v[74:75], v[170:171]\n"
      "v_pk_add_f32 v[88:89], v[88:89], v[184:185]\n"
      "v_pk_add_f32 v[90:91], v[90:91], v[186:187]\n"
      "s_waitcnt lgkmcnt(4)\n"
      "v_pk_add_f32 v[76:77], v[76:77], v[172:173]\n"
      "v_pk_add_f32 v[78:79], v[78:79], v[174:175]\n"
      "v_pk_add_f32 v[92:93], v[92:93], v[188:189]\n"
      "v_pk_add_f32 v[94:95], v[94:95], v[190:191]\n"
      "s_waitcnt lgkmcnt(0)\n"
      "s_add_u32 s65, s68, 55\n"
      "s_mov_b32 s66, s88\n"
      "s_sub_u32 s67, s65, s66\n"
      "s_cmp_lt_i32 s67, 0\n"
      "s_cselect_b32 s88, s66, s65\n"
      "v_mov_b32 v237, s88\n"
      "s_mov_b32 s88, s66\n"
      "s_cmp_lg_u32 %[xw], 0\n"
      "s_cbranch_scc1 NH_%=\n"
      "s_mov_b64 exec, 1\n"
      "global_store_dwordx2 v157, v[236:237], s[18:19] sc1\n"
      "s_mov_b64 exec, -1\n"
      "NH_%=:\n"
      "v_mfma_f32_16x16x32_bf16 v[64:67], a[220:223], a[64:67], v[64:67]\n"
      "v_and_b32 v118, 0xf0, v127\n"
      "v_lshrrev_b32 v156, 3, v127\n"
      "v_mfma_f32_16x16x32_bf16 v[80:83], a[224:227], a[64:67], v[80:83]\n"
      "v_and_b32 v156, 32, v156\n"
      "v_lshl_or_b32 v156, v118, 4, v156\n"
      "v_mfma_f32_16x16x32_bf16 v[68:71], a[220:223], a[68:71], v[68:71]\n"
      "v_lshrrev_b32 v118, 5, v127\n"
      "v_and_or_b32 v156, v118, 16, v156\n"
      "v_mfma_f32_16x16x32_bf16 v[84:87], a[224:227], a[68:71], v[84:87]\n"
      "v_add_u32 v156, %[oo], v156\n"
      "v_add_u32 v155, 0x2000, v156\n"
      "v_mfma_f32_16x16x32_bf16 v[72:75], a[220:223], a[72:75], v[72:75]\n"
      "v_cvt_pk_bf16_f32 v128, v64, v65\n"
      "v_cvt_pk_bf16_f32 v129, v66, v67\n"
      "v_mfma_f32_16x16x32_bf16 v[88:91], a[224:227], a[72:75], v[88:91]\n"
      "v_cvt_pk_bf16_f32 v130, v80, v81\n"
      "v_cvt_pk_bf16_f32 v131, v82, v83\n"
      "v_mfma_f32_16x16x32_bf16 v[76:79], a[220:223], a[76:79], v[76:79]\n"
      "v_xor_b32 v242, 0x2000, v240\n"
      "v_xor_b32 v243, 0x2000, v241\n"
      "v_mfma_f32_16x16x32_bf16 v[92:95], a[224:227], a[76:79], v[92:95]\n"
      "v_cvt_pk_bf16_f32 v132, v68, v69\n"
      "v_cvt_pk_bf16_f32 v133, v70, v71\n"
      "s_memrealtime s[68:69]\n"
      "v_cvt_pk_bf16_f32 v134, v84, v85\n"
      "v_cvt_pk_bf16_f32 v135, v86, v87\n"
      "v_mad_u32_u24 v147, v144, s60, v151\n"
      "v_mad_u32_u24 v147, v145, s62, v147\n"
      "v_add3_u32 v146, v150, v144, v145\n"
      "v_mad_u32_u24 v147, v128, s60, v147\n"
      "v_mad_u32_u24 v147, v129, s62, v147\n"
      "v_add3_u32 v146, v146, v128, v129\n"
      "v_mad_u32_u24 v147, v130, s70, v147\n"
      "v_mad_u32_u24 v147, v131, s71, v147\n"
      "v_add3_u32 v146, v146, v130, v131\n"
      "v_cvt_pk_bf16_f32 v136, v72, v73\n"
      "v_cvt_pk_bf16_f32 v137, v74, v75\n"
      "v_cvt_pk_bf16_f32 v138, v88, v89\n"
      "v_cvt_pk_bf16_f32 v139, v90, v91\n"
      "v_cvt_pk_bf16_f32 v140, v76, v77\n"
      "v_cvt_pk_bf16_f32 v141, v78, v79\n"
      "v_cvt_pk_bf16_f32 v142, v92, v93\n"
      "v_cvt_pk_bf16_f32 v143, v94, v95\n"
      "v_mad_u32_u24 v147, v132, s60, v147\n"
      "v_mad_u32_u24 v147, v133, s62, v147\n"
      "v_add3_u32 v146, v146, v132, v133\n"
      "v_mad_u32_u24 v147, v134, s70, v147\n"
      "v_mad_u32_u24 v147, v135, s71, v147\n"
      "v_add3_u32 v146, v146, v134, v135\n"
      "v_mad_u32_u24 v147, v136, s60, v147\n"
      "v_mad_u32_u24 v147, v137, s62, v147\n"
      "v_add3_u32 v146, v146, v136, v137\n"
      "v_mad_u32_u24 v147, v138, s70, v147\n"
      "v_mad_u32_u24 v147, v139, s71, v147\n"
      "v_add3_u32 v146, v146, v138, v139\n"
      "v_mad_u32_u24 v147, v140, s60, v147\n"
      "v_mad_u32_u24 v147, v141, s62, v147\n"
      "v_add3_u32 v146, v146, v140, v141\n"
      "v_mad_u32_u24 v147, v142, s70, v147\n"
      "v_mad_u32_u24 v147, v143, s71, v147\n"
      "v_add3_u32 v146, v146, v142, v143\n"
      "s_waitcnt lgkmcnt(0)\n"
      "s_call_b64 s[84:85], SPIN_%=\n"
      "global_load_dwordx2 v[232:233], v158, s[18:19] sc1\n"
      "buffer_store_dwordx4 v[128:131], v127, s[28:31], %[xo] offen offset:0 sc1\n"
      "buffer_store_dwordx4 v[132:135], v127, s[28:31], %[xo] offen offset:1024 sc1\n"
      "buffer_store_dwordx4 v[136:139], v127, s[28:31], %[xo] offen offset:2048 sc1\n"
      "buffer_store_dwordx4 v[140:143], v127, s[28:31], %[xo] offen offset:3072 sc1\n"
      "buffer_store_dwordx4 v[144:147], v127, s[28:31], s81 offen sc1\n"
      "global_load_dwordx2 v[234:235], v158, s[18:19] sc1\n"
      "s_waitcnt vmcnt(7)\n"
      "s_barrier\n"
      "ds_read_b64_tr_b16 a[96:97], v246 offset:32768\n"
      "ds_read_b64_tr_b16 a[98:99], v246 offset:36864\n"
      "ds_read_b64_tr_b16 a[100:101], v247 offset:32768\n"
      "ds_read_b64_tr_b16 a[102:103], v247 offset:36864\n"
      "ds_read_b64_tr_b16 a[112:113], v250 offset:0\n"
      "ds_read_b64_tr_b16 a[114:115], v250 offset:4096\n"
      "ds_read_b64_tr_b16 a[116:117], v251 offset:0\n"
      "ds_read_b64_tr_b16 a[118:119], v251 offset:4096\n"
      "ds_read_b64_tr_b16 a[0:1], v250 offset:32768\n"
      "ds_read_b64_tr_b16 a[2:3], v250 offset:36864\n"
      "ds_read_b64_tr_b16 a[4:5], v251 offset:32768\n"
      "ds_read_b64_tr_b16 a[6:7], v251 offset:36864\n"
      "s_waitcnt lgkmcnt(8)\n"
      "v_mfma_f32_16x16x32_bf16 a[128:131], a[96:99], v[208:211], a[128:131]\n"
      "ds_read_b64_tr_b16 a[104:105], v244 offset:32768\n"
      "ds_read_b64_tr_b16 a[106:107], v244 offset:36864\n"
      "v_mfma_f32_16x16x32_bf16 a[144:147], a[100:103], v[208:211], a[144:147]\n"
      "ds_read_b64_tr_b16 a[108:109], v245 offset:32768\n"
      "ds_read_b64_tr_b16 a[110:111], v245 offset:36864\n"
      "s_waitcnt lgkmcnt(8)\n"
      "v_mfma_f32_16x16x32_bf16 a[128:131], a[112:115], v[192:195], a[128:131]\n"
      "ds_read_b64_tr_b16 a[120:121], v240 offset:0\n"
      "ds_read_b64_tr_b16 a[122:123], v240 offset:4096\n"
      "v_mfma_f32_16x16x32_bf16 a[144:147], a[116:119], v[192:195], a[144:147]\n"
      "ds_read_b64_tr_b16 a[124:125], v241 offset:0\n"
      "ds_read_b64_tr_b16 a[126:127], v241 offset:4096\n"
      "s_waitcnt lgkmcnt(8)\n"
      "v_mfma_f32_16x16x32_bf16 a[128:131], a[0:3], v[0:3], a[128:131]\n"
      "ds_read_b64_tr_b16 a[8:9], v240 offset:32768\n"
      "ds_read_b64_tr_b16 a[10:11], v240 offset:36864\n"
      "v_mfma_f32_16x16x32_bf16 a[144:147], a[4:7], v[0:3], a[144:147]\n"
      "ds_read_b64_tr_b16 a[12:13], v241 offset:32768\n"
      "ds_read_b64_tr_b16 a[14:15], v241 offset:36864\n"
      "v_mfma_f32_16x16x32_bf16 a[132:135], a[96:99], v[212:215], a[132:135]\n"
      "ds_write_b128 v125, v[16:19] offset:8192\n"
      "v_lshrrev_b32 v118, 2, v127\n"
      "v_mfma_f32_16x16x32_bf16 a[148:151], a[100:103], v[212:215], a[148:151]\n"
      "v_xor_b32 v118, v118, v127\n"
      "v_mfma_f32_16x16x32_bf16 a[132:135], a[112:115], v[196:199], a[132:135]\n"
      "v_and_b32 v118, 0xc0, v118\n"
      "v_mfma_f32_16x16x32_bf16 a[148:151], a[116:119], v[196:199], a[148:151]\n"
      "v_and_or_b32 v118, v127, 32, v118\n"
      "v_mfma_f32_16x16x32_bf16 a[132:135], a[0:3], v[4:7], a[132:135]\n"
      "ds_write_b128 v125, v[20:23] offset:9216\n"
      "v_and_b32 v119, 16, v127\n"
      "v_mfma_f32_16x16x32_bf16 a[148:151], a[4:7], v[4:7], a[148:151]\n"
      "v_mov_b32 v84, 0x3f80\n"
      "v_mfma_f32_16x16x32_bf16 a[136:139], a[96:99], v[216:219], a[136:139]\n"
      "v_lshlrev_b32 v119, v119, v84\n"
      "v_mfma_f32_16x16x32_bf16 a[152:155], a[100:103], v[216:219], a[152:155]\n"
      "v_cmp_eq_u32 vcc, 0, v118\n"
      "v_mfma_f32_16x16x32_bf16 a[136:139], a[112:115], v[200:203], a[136:139]\n"
      "ds_write_b128 v125, v[24:27] offset:10240\n"
      "v_mov_b32 v84, 0\n"
      "v_mfma_f32_16x16x32_bf16 a[152:155], a[116:119], v[200:203], a[152:155]\n"
      "v_cndmask_b32 v86, 0, v119, vcc\n"
      "v_mfma_f32_16x16x32_bf16 a[136:139], a[0:3], v[8:11], a[136:139]\n"
      "v_cmp_eq_u32 vcc, 32, v118\n"
      "v_mfma_f32_16x16x32_bf16 a[152:155], a[4:7], v[8:11], a[152:155]\n"
      "v_mov_b32 v85, 0\n"
      "v_mfma_f32_16x16x32_bf16 a[140:143], a[96:99], v[220:223], a[140:143]\n"
      "ds_write_b128 v125, v[28:31] offset:11264\n"
      "v_cndmask_b32 v87, 0, v119, vcc\n"
      "v_mfma_f32_16x16x32_bf16 a[156:159], a[100:103], v[220:223], a[156:159]\n"
      "v_mov_b32 v88, 0\n"
      "v_mfma_f32_16x16x32_bf16 a[140:143], a[112:115], v[204:207], a[140:143]\n"
      "v_mov_b32 v89, 0\n"
      "v_mfma_f32_16x16x32_bf16 a[156:159], a[116:119], v[204:207], a[156:159]\n"
      "v_mfma_f32_16x16x32_bf16 a[140:143], a[0:3], v[12:15], a[140:143]\n"
      "ds_write_b128 v125, a[128:131] offset:0\n"
      "v_mfma_f32_16x16x32_bf16 a[156:159], a[4:7], v[12:15], a[156:159]\n"
      "s_waitcnt lgkmcnt(13)\n"
      "v_mfma_f32_16x16x32_bf16 v[32:35], a[104:107], v[208:211], v[32:35]\n"
      "ds_read_b64_tr_b16 a[32:33], v240 offset:1024\n"
      "v_mfma_f32_16x16x32_bf16 v[36:39], a[104:107], v[212:215], v[36:39]\n"
      "ds_read_b64_tr_b16 a[34:35], v240 offset:5120\n"
      "v_mfma_f32_16x16x32_bf16 v[40:43], a[104:107], v[216:219], v[40:43]\n"
      "ds_write_b128 v125, a[144:147] offset:4096\n"
      "ds_read_b64_tr_b16 a[36:37], v241 offset:1024\n"
      "v_mfma_f32_16x16x32_bf16 v[44:47], a[104:107], v[220:223], v[44:47]\n"
      "ds_read_b64_tr_b16 a[38:39], v241 offset:5120\n"
      "v_mfma_f32_16x16x32_bf16 v[48:51], a[108:111], v[208:211], v[48:51]\n"
      "ds_read_b64_tr_b16 a[40:41], v242 offset:1024\n"
      "v_mfma_f32_16x16x32_bf16 v[52:55], a[108:111], v[212:215], v[52:55]\n"
      "ds_read_b64_tr_b16 a[42:43], v242 offset:5120\n"
      "v_mfma_f32_16x16x32_bf16 v[56:59], a[108:111], v[216:219], v[56:59]\n"
      "ds_write_b128 v125, a[132:135] offset:1024\n"
      "ds_read_b64_tr_b16 a[44:45], v243 offset:1024\n"
      "v_mfma_f32_16x16x32_bf16 v[60:63], a[108:111], v[220:223], v[60:63]\n"
      "ds_read_b64_tr_b16 a[46:47], v243 offset:5120\n"
      "s_waitcnt lgkmcnt(15)\n"
      "v_mfma_f32_16x16x32_bf16 v[32:35], a[120:123], v[192:195], v[32:35]\n"
      "ds_read_b64_tr_b16 a[16:17], v244 offset:0\n"
      "v_mfma_f32_16x16x32_bf16 v[36:39], a[120:123], v[196:199], v[36:39]\n"
      "ds_read_b64_tr_b16 a[18:19], v244 offset:4096\n"
      "v_mfma_f32_16x16x32_bf16 v[40:43], a[120:123], v[200:203], v[40:43]\n"
      "ds_write_b128 v125, a[148:151] offset:5120\n"
      "ds_read_b64_tr_b16 a[20:21], v245 offset:0\n"
      "v_mfma_f32_16x16x32_bf16 v[44:47], a[120:123], v[204:207], v[44:47]\n"
      "ds_read_b64_tr_b16 a[22:23], v245 offset:4096\n"
      "v_mfma_f32_16x16x32_bf16 v[48:51], a[124:127], v[192:195], v[48:51]\n"
      "ds_read_b64_tr_b16 a[24:25], v248 offset:0\n"
      "v_mfma_f32_16x16x32_bf16 v[52:55], a[124:127], v[196:199], v[52:55]\n"
      "ds_read_b64_tr_b16 a[26:27], v248 offset:4096\n"
      "v_mfma_f32_16x16x32_bf16 v[56:59], a[124:127], v[200:203], v[56:59]\n"
      "ds_write_b128 v125, a[136:139] offset:2048\n"
      "ds_read_b64_tr_b16 a[28:29], v120 offset:0\n"
      "v_mfma_f32_16x16x32_bf16 v[60:63], a[124:127], v[204:207], v[60:63]\n"
      "ds_read_b64_tr_b16 a[30:31], v120 offset:4096\n"
      "s_waitcnt lgkmcnt(15)\n"
      "v_mfma_f32_16x16x32_bf16 v[32:35], a[8:11], v[0:3], v[32:35]\n"
      "v_mfma_f32_16x16x32_bf16 v[36:39], a[8:11], v[4:7], v[36:39]\n"
      "v_mfma_f32_16x16x32_bf16 v[40:43], a[8:11], v[8:11], v[40:43]\n"
      "ds_write_b128 v125, a[152:155] offset:6144\n"
      "v_mfma_f32_16x16x32_bf16 v[44:47], a[8:11], v[12:15], v[44:47]\n"
      "s_waitcnt vmcnt(6)\n"
      "v_cmp_ne_u32 vcc, v232, v150\n"
      "s_memrealtime s[68:69]\n"
      "s_cbranch_vccnz POLL_%=\n"
      "GOT_%=:\n"
      "v_readfirstlane_b32 s88, v233\n"
      "s_waitcnt lgkmcnt(0)\n"
      "s_add_u32 s88, s88, 60\n"
      "s_sub_u32 s67, s88, s68\n"
      "s_cmp_gt_u32 s67, 1000\n"
      "s_cbranch_scc1 LD_%=\n"
      "s_call_b64 s[84:85], SPIN_%=\n"
      "LD_%=:\n"
      "buffer_load_dwordx4 v[80:83], v127, s[28:31], s79 offen sc1\n"
      "buffer_load_dwordx4 v[64:67], v127, s[28:31], s80 offen offset:0 sc1\n"
      "buffer_load_dwordx4 v[68:71], v127, s[28:31], s80 offen offset:1024 sc1\n"
      "buffer_load_dwordx4 v[72:75], v127, s[28:31], s80 offen offset:2048 sc1\n"
      "buffer_load_dwordx4 v[76:79], v127, s[28:31], s80 offen offset:3072 sc1\n"
      "v_mfma_f32_16x16x32_bf16 v[48:51], a[12:15], v[0:3], v[48:51]\n"
      "v_mfma_f32_16x16x32_bf16 v[52:55], a[12:15], v[4:7], v[52:55]\n"
      "v_mfma_f32_16x16x32_bf16 v[56:59], a[12:15], v[8:11], v[56:59]\n"
      "ds_write_b128 v125, a[140:143] offset:3072\n"
      "v_mfma_f32_16x16x32_bf16 v[60:63], a[12:15], v[12:15], v[60:63]\n"
      "s_waitcnt lgkmcnt(15)\n"
      "v_mfma_f32_16x16x32_bf16 v[32:35], a[32:35], v[96:99], v[32:35]\n"
      "v_mfma_f32_16x16x32_bf16 v[48:51], a[36:39], v[96:99], v[48:51]\n"
      "v_mfma_f32_16x16x32_bf16 v[36:39], a[32:35], v[100:103], v[36:39]\n"
      "ds_write_b128 v125, a[156:159] offset:7168\n"
      "v_mfma_f32_16x16x32_bf16 v[52:55], a[36:39], v[100:103], v[52:55]\n"
      "v_mfma_f32_16x16x32_bf16 v[40:43], a[32:35], v[104:107], v[40:43]\n"
      "v_mfma_f32_16x16x32_bf16 v[56:59], a[36:39], v[104:107], v[56:59]\n"
      "v_mfma_f32_16x16x32_bf16 v[44:47], a[32:35], v[108:111], v[44:47]\n"
      "v_mfma_f32_16x16x32_bf16 v[60:63], a[36:39], v[108:111], v[60:63]\n"
      "s_waitcnt lgkmcnt(13)\n"
      "v_mfma_f32_16x16x32_bf16 v[32:35], a[40:43], a[64:67], v[32:35]\n"
      "v_mfma_f32_16x16x32_bf16 v[48:51], a[44:47], a[64:67], v[48:51]\n"
      "v_mfma_f32_16x16x32_bf16 v[36:39], a[40:43], a[68:71], v[36:39]\n"
      "v_mfma_f32_16x16x32_bf16 v[52:55], a[44:47], a[68:71], v[52:55]\n"
      "v_mfma_f32_16x16x32_bf16 v[40:43], a[40:43], a[72:75], v[40:43]\n"
      "v_mfma_f32_16x16x32_bf16 v[56:59], a[44:47], a[72:75], v[56:59]\n"
      "v_mfma_f32_16x16x32_bf16 v[44:47], a[40:43], a[76:79], v[44:47]\n"
      "v_mfma_f32_16x16x32_bf16 v[60:63], a[44:47], a[76:79], v[60:63]\n"
      "s_waitcnt lgkmcnt(8)\n"
      "v_mfma_f32_16x16x32_bf16 v[32:35], a[16:19], v[16:19], v[32:35]\n"
      "v_mfma_f32_16x16x32_bf16 v[48:51], a[20:23], v[16:19], v[48:51]\n"
      "v_mfma_f32_16x16x32_bf16 v[36:39], a[16:19], v[20:23], v[36:39]\n"
      "v_mfma_f32_16x16x32_bf16 v[52:55], a[20:23], v[20:23], v[52:55]\n"
      "v_mfma_f32_16x16x32_bf16 v[40:43], a[16:19], v[24:27], v[40:43]\n"
      "v_mfma_f32_16x16x32_bf16 v[56:59], a[20:23], v[24:27], v[56:59]\n"
      "v_mfma_f32_16x16x32_bf16 v[44:47], a[16:19], v[28:31], v[44:47]\n"
      "v_mfma_f32_16x16x32_bf16 v[60:63], a[20:23], v[28:31], v[60:63]\n"
      "s_waitcnt lgkmcnt(0)\n"
      "s_barrier\n"
      "ds_read_b128 v[160:163], v126 offset:0\n"
      "ds_read_b128 v[176:179], v126 offset:4096\n"
      "ds_read_b128 v[164:167], v126 offset:1024\n"
      "ds_read_b128 v[180:183], v126 offset:5120\n"
      "ds_read_b128 v[168:171], v126 offset:2048\n"
      "ds_read_b128 v[184:187], v126 offset:6144\n"
      "ds_read_b128 v[172:175], v126 offset:3072\n"
      "ds_read_b128 v[188:191], v126 offset:7168\n"
      "ds_read_b128 a[80:83], v126 offset:8192\n"
      "ds_read_b128 a[84:87], v126 offset:9216\n"
      "ds_read_b128 a[88:91], v126 offset:10240\n"
      "ds_read_b128 a[92:95], v126 offset:11264\n"
      "s_waitcnt lgkmcnt(10)\n"
      "v_pk_add_f32 v[32:33], v[32:33], v[160:161]\n"
      "v_pk_add_f32 v[34:35], v[34:35], v[162:163]\n"
      "v_pk_add_f32 v[48:49], v[48:49], v[176:177]\n"
      "v_pk_add_f32 v[50:51], v[50:51], v[178:179]\n"
      "s_waitcnt lgkmcnt(8)\n"
      "v_pk_add_f32 v[36:37], v[36:37], v[164:165]\n"
      "v_pk_add_f32 v[38:39], v[38:39], v[166:167]\n"
      "v_pk_add_f32 v[52:53], v[52:53], v[180:181]\n"
      "v_pk_add_f32 v[54:55], v[54:55], v[182:183]\n"
      "s_waitcnt lgkmcnt(6)\n"
      "v_pk_add_f32 v[40:41], v[40:41], v[168:169]\n"
      "v_pk_add_f32 v[42:43], v[42:43], v[170:171]\n"
      "v_pk_add_f32 v[56:57], v[56:57], v[184:185]\n"
      "v_pk_add_f32 v[58:59], v[58:59], v[186:187]\n"
      "s_waitcnt lgkmcnt(4)\n"
      "v_pk_add_f32 v[44:45], v[44:45], v[172:173]\n"
      "v_pk_add_f32 v[46:47], v[46:47], v[174:175]\n"
      "v_pk_add_f32 v[60:61], v[60:61], v[188:189]\n"
      "v_pk_add_f32 v[62:63], v[62:63], v[190:191]\n"
      "s_waitcnt lgkmcnt(0)\n"
      "v_mfma_f32_16x16x32_bf16 v[32:35], a[24:27], a[80:83], v[32:35]\n"
      "v_mfma_f32_16x16x32_bf16 v[48:51], a[28:31], a[80:83], v[48:51]\n"
      "v_mfma_f32_16x16x32_bf16 v[36:39], a[24:27], a[84:87], v[36:39]\n"
      "v_mfma_f32_16x16x32_bf16 v[52:55], a[28:31], a[84:87], v[52:55]\n"
      "v_mfma_f32_16x16x32_bf16 v[40:43], a[24:27], a[88:91], v[40:43]\n"
      "v_mfma_f32_16x16x32_bf16 v[56:59], a[28:31], a[88:91], v[56:59]\n"
      "v_mfma_f32_16x16x32_bf16 v[44:47], a[24:27], a[92:95], v[44:47]\n"
      "v_mfma_f32_16x16x32_bf16 v[60:63], a[28:31], a[92:95], v[60:63]\n"
      "CHK_%=:\n"
      "s_waitcnt vmcnt(4)\n"
      "v_add_f32 v224, v144, v80\n"
      "v_rcp_f32 v224, v224\n"
      "v_mad_u32_u24 v119, v80, s60, v151\n"
      "v_mad_u32_u24 v119, v81, s62, v119\n"
      "v_add3_u32 v118, v150, v80, v81\n"
      "s_waitcnt vmcnt(3)\n"
      "v_mfma_f32_16x16x32_bf16 v[160:163], v[86:89], v[64:67], v[32:35]\n"
      "v_mad_u32_u24 v119, v64, s60, v119\n"
      "v_mad_u32_u24 v119, v65, s62, v119\n"
      "v_add3_u32 v118, v118, v64, v65\n"
      "v_mov_b32 v226, v224\n"
      "v_mfma_f32_16x16x32_bf16 v[176:179], v[84:87], v[64:67], v[48:51]\n"
      "v_mad_u32_u24 v119, v66, s70, v119\n"
      "v_mad_u32_u24 v119, v67, s71, v119\n"
      "v_add3_u32 v118, v118, v66, v67\n"
      "v_permlane32_swap_b32 v224, v226\n"
      "s_waitcnt vmcnt(2)\n"
      "v_mfma_f32_16x16x32_bf16 v[164:167], v[86:89], v[68:71], v[36:39]\n"
      "v_mad_u32_u24 v119, v68, s60, v119\n"
      "v_mad_u32_u24 v119, v69, s62, v119\n"
      "v_add3_u32 v118, v118, v68, v69\n"
      "v_mov_b32 v225, v224\n"
      "v_mov_b32 v227, v226\n"
      "v_mfma_f32_16x16x32_bf16 v[180:183], v[84:87], v[68:71], v[52:55]\n"
      "v_mad_u32_u24 v119, v70, s70, v119\n"
      "v_mad_u32_u24 v119, v71, s71, v119\n"
      "v_add3_u32 v118, v118, v70, v71\n"
      "v_permlane16_swap_b32 v224, v225\n"
      "s_waitcnt vmcnt(1)\n"
      "v_mfma_f32_16x16x32_bf16 v[168:171], v[86:89], v[72:75], v[40:43]\n"
      "v_mad_u32_u24 v119, v72, s60, v119\n"
      "v_mad_u32_u24 v119, v73, s62, v119\n"
      "v_add3_u32 v118, v118, v72, v73\n"
      "v_permlane16_swap_b32 v226, v227\n"
      "v_mfma_f32_16x16x32_bf16 v[184:187], v[84:87], v[72:75], v[56:59]\n"
      "v_mad_u32_u24 v119, v74, s70, v119\n"
      "v_mad_u32_u24 v119, v75, s71, v119\n"
      "v_add3_u32 v118, v118, v74, v75\n"
      "s_waitcnt vmcnt(0)\n"
      "v_mfma_f32_16x16x32_bf16 v[172:175], v[86:89], v[76:79], v[44:47]\n"
      "v_mad_u32_u24 v119, v76, s60, v119\n"
      "v_mad_u32_u24 v119, v77, s62, v119\n"
      "v_add3_u32 v118, v118, v76, v77\n"
      "v_mfma_f32_16x16x32_bf16 v[188:191], v[84:87], v[76:79], v[60:63]\n"
      "v_mad_u32_u24 v119, v78, s70, v119\n"
      "v_mad_u32_u24 v119, v79, s71, v119\n"
      "v_add3_u32 v118, v118, v78, v79\n"
      "v_cmp_ne_u64 vcc, v[118:119], v[82:83]\n"
      "s_cbranch_vccnz RETRY_%=\n"
      "OK_%=:\n"
      "v_cmp_neq_f32 vcc, v81, v117\n"
      "s_cbranch_vccnz FIX_%=\n"
      "FR_%=:\n"
      "v_pk_mul_f32 v[160:161], v[160:161], v[224:225] op_sel:[0,0] op_sel_hi:[1,0]\n"
      "v_pk_mul_f32 v[162:163], v[162:163], v[224:225] op_sel:[0,0] op_sel_hi:[1,0]\n"
      "v_pk_mul_f32 v[176:177], v[176:177], v[224:225] op_sel:[0,0] op_sel_hi:[1,0]\n"
      "v_pk_mul_f32 v[178:179], v[178:179], v[224:225] op_sel:[0,0] op_sel_hi:[1,0]\n"
      "v_cvt_pk_bf16_f32 v128, v160, v161\n"
      "v_cvt_pk_bf16_f32 v130, v176, v177\n"
      "v_cvt_pk_bf16_f32 v129, v162, v163\n"
      "v_cvt_pk_bf16_f32 v131, v178, v179\n"
      "v_pk_mul_f32 v[164:165], v[164:165], v[224:225] op_sel:[0,1] op_sel_hi:[1,1]\n"
      "v_permlane16_swap_b32 v128, v130\n"
      "v_permlane16_swap_b32 v129, v131\n"
      "global_store_dwordx4 v156, v[128:131], s[16:17] offset:-4096 sc1\n"
      "v_pk_mul_f32 v[166:167], v[166:167], v[224:225] op_sel:[0,1] op_sel_hi:[1,1]\n"
      "v_pk_mul_f32 v[180:181], v[180:181], v[224:225] op_sel:[0,1] op_sel_hi:[1,1]\n"
      "v_pk_mul_f32 v[182:183], v[182:183], v[224:225] op_sel:[0,1] op_sel_hi:[1,1]\n"
      "v_cvt_pk_bf16_f32 v132, v164, v165\n"
      "v_cvt_pk_bf16_f32 v134, v180, v181\n"
      "v_cvt_pk_bf16_f32 v133, v166, v167\n"
      "v_cvt_pk_bf16_f32 v135, v182, v183\n"
      "v_pk_mul_f32 v[168:169], v[168:169], v[226:227] op_sel:[0,0] op_sel_hi:[1,0]\n"
      "v_permlane16_swap_b32 v132, v134\n"
      "v_permlane16_swap_b32 v133, v135\n"
      "global_store_dwordx4 v156, v[132:135], s[16:17] offset:0 sc1\n"
      "v_pk_mul_f32 v[170:171], v[170:171], v[226:227] op_sel:[0,0] op_sel_hi:[1,0]\n"
      "v_pk_mul_f32 v[184:185], v[184:185], v[226:227] op_sel:[0,0] op_sel_hi:[1,0]\n"
      "v_pk_mul_f32 v[186:187], v[186:187], v[226:227] op_sel:[0,0] op_sel_hi:[1,0]\n"
      "v_cvt_pk_bf16_f32 v136, v168, v169\n"
      "v_cvt_pk_bf16_f32 v138, v184, v185\n"
      "v_cvt_pk_bf16_f32 v137, v170, v171\n"
      "v_cvt_pk_bf16_f32 v139, v186, v187\n"
      "v_pk_mul_f32 v[172:173], v[172:173], v[226:227] op_sel:[0,1] op_sel_hi:[1,1]\n"
      "v_permlane16_swap_b32 v136, v138\n"
      "v_permlane16_swap_b32 v137, v139\n"
      "global_store_dwordx4 v155, v[136:139], s[16:17] offset:-4096 sc1\n"
      "v_pk_mul_f32 v[174:175], v[174:175], v[226:227] op_sel:[0,1] op_sel_hi:[1,1]\n"
      "v_pk_mul_f32 v[188:189], v[188:189], v[226:227] op_sel:[0,1] op_sel_hi:[1,1]\n"
      "v_pk_mul_f32 v[190:191], v[190:191], v[226:227] op_sel:[0,1] op_sel_hi:[1,1]\n"
      "v_cvt_pk_bf16_f32 v140, v172, v173\n"
      "v_cvt_pk_bf16_f32 v142, v188, v189\n"
      "v_cvt_pk_bf16_f32 v141, v174, v175\n"
      "v_cvt_pk_bf16_f32 v143, v190, v191\n"
      "v_and_b32 v157, 0xffffff80, v157\n"
      "v_permlane16_swap_b32 v140, v142\n"
      "v_permlane16_swap_b32 v141, v143\n"
      "global_store_dwordx4 v155, v[140:143], s[16:17] offset:0 sc1\n"
      "s_and_b32 s67, %[fo], 8\n"
      "s_or_b32 s67, s67, %[xw]\n"
      "s_cbranch_scc1 END_%=\n"
      "s_mov_b64 exec, 1\n"
      "global_store_dwordx2 v157, v[150:151], s[18:19] sc1\n"
      "s_mov_b64 exec, -1\n"
      "END_%=:\n"
      ".subsection 1\n"
      "POLL_%=:\n"
      "s_waitcnt vmcnt(0) lgkmcnt(0)\n"
      "v_cmp_ne_u32 vcc, v234, v150\n"
      "v_mov_b32 v233, v235\n"
      "s_memrealtime s[68:69]\n"
      "s_sub_u32 s63, s63, 1\n"
      "s_cbranch_vccz GOT_%=\n"
      "global_load_dwordx2 v[234:235], v158, s[18:19] sc1\n"
      "s_cbranch_scc0 POLL_%=\n"
      "s_branch GOT_%=\n"
      "RETRY_%=:\n"
      "s_sub_u32 s63, s63, 1\n"
      "s_cbranch_scc1 OK_%=\n"
      "s_sleep 4\n"
      "buffer_load_dwordx4 v[80:83], v127, s[28:31], s79 offen sc1\n"
      "buffer_load_dwordx4 v[64:67], v127, s[28:31], s80 offen offset:0 sc1\n"
      "buffer_load_dwordx4 v[68:71], v127, s[28:31], s80 offen offset:1024 sc1\n"
      "buffer_load_dwordx4 v[72:75], v127, s[28:31], s80 offen offset:2048 sc1\n"
      "buffer_load_dwordx4 v[76:79], v127, s[28:31], s80 offen offset:3072 sc1\n"
      "s_branch CHK_%=\n"
      "GW0_%=:\n"
      "s_call_b64 s[84:85], WAIT_%=\n"
      "s_branch GX0_%=\n"
      "GW1_%=:\n"
      "s_call_b64 s[84:85], WAIT_%=\n"
      "s_branch GX1_%=\n"
      "GW2_%=:\n"
      "s_call_b64 s[84:85], WAIT_%=\n"
      "s_branch GX2_%=\n"
      "GW3_%=:\n"
      "s_call_b64 s[84:85], WAIT_%=\n"
      "s_add_u32 s87, s87, 1\n"
      "s_branch GX3_%=\n"
      "GW4_%=:\n"
      "s_call_b64 s[84:85], WAIT_%=\n"
      "s_branch GX4_%=\n"
      "GW5_%=:\n"
      "s_call_b64 s[84:85], WAIT_%=\n"
      "s_branch GX5_%=\n"
      "GW6_%=:\n"
      "s_call_b64 s[84:85], WAIT_%=\n"
      "s_branch GX6_%=\n"
      "GW7_%=:\n"
      "s_call_b64 s[84:85], WAIT_%=\n"
      "s_branch GX7_%=\n"
      "GW8_%=:\n"
      "s_call_b64 s[84:85], WAIT_%=\n"
      "s_branch GX8_%=\n"
      "WAIT_%=:\n"
      "s_add_u32 s87, s87, 1\n"
      "s_bitcmp1_b32 s89, s87\n"
      "s_cbranch_scc1 H1_%=\n"
      "SPIN_%=:\n"
      "s_sub_u32 s67, s68, s88\n"
      "s_cmp_ge_i32 s67, 0\n"
      "s_cbranch_scc1 GO_%=\n"
      "s_sleep 1\n"
      "s_memrealtime s[68:69]\n"
      "s_waitcnt lgkmcnt(0)\n"
      "s_branch SPIN_%=\n"
      "GO_%=:\n"
      "s_setpc_b64 s[84:85]\n"
      "H1_%=:\n"
      "s_cmp_gt_i32 s93, 50\n"
      "s_cbranch_scc1 HS_%=\n"
      "s_bitcmp1_b32 0x2a80, s87\n"
      "s_cbranch_scc0 H2_%=\n"
      "s_add_u32 s67, s66, 400\n"
      "s_waitcnt vmcnt(8)\n"
      "v_sub_u32 v250, s67, v250\n"
      "v_cmp_gt_u32 vcc, 900, v250\n"
      "v_add_u32 v250, 0xa00, v250\n"
      "v_xor_b32 v251, 0x100, v251\n"
      "s_sub_u32 s69, s66, s71\n"
      "s_cmp_ge_u32 s87, 12\n"
      "s_cselect_b32 s101, 50, s101\n"
      "s_bcnt1_i32_b32 s74, vcc_lo\n"
      "s_bcnt1_i32_b32 s75, vcc_hi\n"
      "v_cmp_gt_u32 vcc, 0x800, v250\n"
      "s_add_u32 s67, s74, s75\n"
      "s_cmp_eq_u32 s87, 7\n"
      "s_cselect_b32 s76, 1, 0\n"
      "s_lshr_b32 s76, s67, s76\n"
      "s_cmp_le_u32 s76, 2\n"
      "s_cselect_b32 s86, 107, s98\n"
      "s_cmp_ge_u32 s76, 9\n"
      "s_cselect_b32 s86, 112, s86\n"
      "s_cbranch_vccnz H6_%=\n"
      "s_cmp_ge_u32 s74, 12\n"
      "s_cbranch_scc1 H3_%=\n"
      "s_cmp_eq_u32 s87, 13\n"
      "s_cbranch_scc1 H4_%=\n"
      "H8_%=:\n"
      "s_lshr_b32 s67, %[rep], 7\n"
      "s_cmp_eq_u32 s67, 1\n"
      "s_cbranch_scc0 SPIN_%=\n"
      "H5_%=:\n"
      "s_cmp_gt_i32 s93, 50\n"
      "s_cselect_b32 s75, -10, 0\n"
      "v_mov_b32 v127, s66\n"
      "s_add_u32 s74, s87, 2\n"
      "s_bfe_u32 s74, s74, 0x10001\n"
      "s_lshl_b32 s74, s74, 8\n"
      "s_add_u32 s74, s74, %[rep]\n"
      "RS_%=:\n"
      "s_sub_u32 s67, s68, s88\n"
      "s_cmp_ge_i32 s67, s75\n"
      "s_cbranch_scc1 RG_%=\n"
      "s_sleep 1\n"
      "s_memrealtime s[68:69]\n"
      "s_waitcnt lgkmcnt(0)\n"
      "s_branch RS_%=\n"
      "RG_%=:\n"
      "s_getreg_b32 s67, hwreg(HW_REG_IB_STS, 0, 4)\n"
      "s_cmp_le_u32 s67, 4\n"
      "s_cbranch_scc1 SPIN_%=\n"
      "buffer_store_dword v127, v124, s[28:31], s74 offen sc1\n"
      "s_branch SPIN_%=\n"
      "H6_%=:\n"
      "s_ff1_i32_b64 s67, vcc\n"
      "v_sub_u32 v250, s66, v250\n"
      "s_nop 3\n"
      "v_readlane_b32 s76, v250, s67\n"
      "s_add_u32 s76, s76, 2960\n"
      "s_and_b32 s76, s76, 31\n"
      "s_lshl_b32 s76, s76, 3\n"
      "s_lshl_b32 s89, 4, s87\n"
      "s_branch H7_%=\n"
      "H3_%=:\n"
      "s_mul_i32 s76, s69, 110\n"
      "s_ashr_i32 s76, s76, 10\n"
      "s_min_i32 s76, s76, 40\n"
      "s_add_u32 s76, s76, 76\n"
      "s_cmp_lt_u32 s75, 24\n"
      "s_cselect_b32 s76, 56, s76\n"
      "s_cmp_lt_u32 s75, 13\n"
      "s_cselect_b32 s76, 40, s76\n"
      "s_cmp_lt_u32 s75, 5\n"
      "s_cselect_b32 s76, 24, s76\n"
      "s_add_u32 s67, s66, 2000\n"
      "s_and_b32 s67, s67, -32\n"
      "s_lshr_b32 s75, s76, 3\n"
      "s_or_b32 s67, s67, s75\n"
      "v_mov_b32 v127, s67\n"
      "s_add_u32 s74, s87, 2\n"
      "s_bfe_u32 s74, s74, 0x10001\n"
      "s_lshl_b32 s74, s74, 8\n"
      "s_add_u32 s74, s74, %[rep]\n"
      "s_cmp_lt_u32 %[rep], 128\n"
      "s_cselect_b32 s89, 22, 20\n"
      "s_lshl_b32 s89, s89, s87\n"
      "s_sub_u32 s75, %[rep], 128\n"
      "s_cmp_lt_u32 s75, 32\n"
      "s_cbranch_scc0 H7_%=\n"
      "buffer_store_dword v127, v124, s[28:31], s74 offen sc1\n"
      "H7_%=:\n"
      "s_add_u32 s66, s66, s76\n"
      "s_mov_b32 s99, 17\n"
      "s_mov_b32 s100, 288\n"
      "s_add_u32 s93, s93, 99\n"
      "s_mov_b32 s86, 110\n"
      "s_mov_b32 s80, 0xc853212b\n"
      "s_mov_b32 s81, 0x842210\n"
      "s_cmp_le_u32 s76, 40\n"
      "s_cselect_b32 s75, 15, 0\n"
      "s_cmp_le_u32 s76, 24\n"
      "s_cselect_b32 s75, 25, s75\n"
      "s_lshr_b64 s[80:81], s[80:81], s75\n"
      "s_sub_u32 s67, s65, s66\n"
      "s_cmp_lt_i32 s67, 0\n"
      "s_cselect_b32 s88, s66, s65\n"
      "s_mov_b32 s101, 200\n"
      "s_branch SPIN_%=\n"
      "H4_%=:\n"
      "s_mov_b32 s91, 0\n"
      "s_mov_b32 s89, 0\n"
      "v_mov_b32 v251, 0x4000000\n"
      "s_branch SPIN_%=\n"
      "HS_%=:\n"
      "s_bitcmp1_b32 s87, 0\n"
      "s_cbranch_scc0 H5_%=\n"
      "v_xor_b32 v251, 0x100, v251\n"
      "s_lshr_b32 s67, s89, s87\n"
      "s_cmp_gt_u32 s67, 1\n"
      "s_cbranch_scc1 H8_%=\n"
      "s_add_u32 s67, s66, 400\n"
      "s_waitcnt vmcnt(8)\n"
      "v_sub_u32 v250, s67, v250\n"
      "v_cmp_gt_u32 vcc, 900, v250\n"
      "s_nop 3\n"
      "s_bcnt1_i32_b32 s74, vcc_lo\n"
      "s_bcnt1_i32_b32 s75, vcc_hi\n"
      "s_cmp_ge_u32 s75, 5\n"
      "s_cbranch_scc1 HT_%=\n"
      "s_cmp_gt_u32 s74, 2\n"
      "s_cbranch_scc1 H4_%=\n"
      "s_mov_b32 s99, 27\n"
      "s_mov_b32 s100, 285\n"
      "s_mov_b32 s86, 107\n"
      "s_mov_b64 s[80:81], 0\n"
      "s_branch H4_%=\n"
      "HT_%=:\n"
      "s_add_u32 s66, s66, 64\n"
      "s_mov_b32 s80, 0x86429909\n"
      "s_mov_b32 s81, 0x42110\n"
      "s_sub_u32 s67, s65, s66\n"
      "s_cmp_lt_i32 s67, 0\n"
      "s_cselect_b32 s88, s66, s65\n"
      "s_branch H4_%=\n"
      "H0_%=:\n"
      "buffer_load_dword v250, v251, s[28:31], 0 offen sc1\n"
      "s_branch SPIN_%=\n"
      "H9_%=:\n"
      "s_bitcmp1_b32 s87, 0\n"
      "s_cbranch_scc0 H5_%=\n"
      "v_xor_b32 v251, 0x100, v251\n"
      "s_cmp_eq_u32 s87, 5\n"
      "s_cbranch_scc1 H8_%=\n"
      "s_branch SPIN_%=\n"
      "H2_%=:\n"
      "s_cmp_gt_u32 s87, 2\n"
      "s_cbranch_scc1 H9_%=\n"
      "s_cmp_eq_u32 s87, 1\n"
      "s_cbranch_scc1 H0_%=\n"
      "s_waitcnt vmcnt(8)\n"
      "v_subrev_u32 v250, s71, v250\n"
      "v_min_u32 v250, 0x258, v250\n"
      "v_mbcnt_lo_u32_b32 v251, -1, 0\n"
      "v_mbcnt_hi_u32_b32 v251, -1, v251\n"
      "v_add_u32_dpp v250, v250, v250 row_shr:1 row_mask:0xf bank_mask:0xf bound_ctrl:0\n"
      "v_lshlrev_b32 v124, 23, v251\n"
      "s_nop 0\n"
      "v_add_u32_dpp v250, v250, v250 row_shr:2 row_mask:0xf bank_mask:0xf bound_ctrl:0\n"
      "v_lshlrev_b32 v251, 2, v251\n"
      "v_add_u32 v124, 0x6000, v124\n"
      "v_add_u32_dpp v250, v250, v250 row_shr:4 row_mask:0xf bank_mask:0xf bound_ctrl:0\n"
      "v_add_u32 v251, 0x6100, v251\n"
      "v_readlane_b32 s66, v250, 7\n"
      "s_nop 0\n"
      "s_lshr_b32 s66, s66, 3\n"
      "s_add_u32 s66, s66, s71\n"
      "s_add_u32 s66, s66, %[cph]\n"
      "s_add_u32 s71, s66, 550\n"
      "s_sub_u32 s67, s65, s66\n"
      "s_cmp_lt_i32 s67, 0\n"
      "s_cselect_b32 s88, s66, s65\n"
      "s_branch SPIN_%=\n"
      "FB0_%=:\n"
      "s_nop 7\n"
      "s_nop 7\n"
      "v_xor_b32 v118, 0x0, v242\n"
      "v_xor_b32 v119, 0x0, v243\n"
      "ds_read_b128 a[64:67], v118 offset:0\n"
      "ds_read_b128 a[68:71], v118 offset:4096\n"
      "ds_read_b128 a[72:75], v119 offset:0\n"
      "ds_read_b128 a[76:79], v119 offset:4096\n"
      "ds_read_b128 a[80:83], v118 offset:1024\n"
      "ds_read_b128 a[84:87], v118 offset:5120\n"
      "ds_read_b128 a[88:91], v119 offset:1024\n"
      "ds_read_b128 a[92:95], v119 offset:5120\n"
      "s_waitcnt lgkmcnt(0)\n"
      "v_mfma_f32_16x16x32_bf16 v[128:131], a[64:67], a[0:3], 0\n"
      "v_mfma_f32_16x16x32_bf16 v[136:139], a[64:67], a[4:7], 0\n"
      "v_mfma_f32_16x16x32_bf16 v[144:147], a[64:67], a[8:11], 0\n"
      "v_mfma_f32_16x16x32_bf16 v[152:155], a[64:67], a[12:15], 0\n"
      "v_mfma_f32_16x16x32_bf16 v[132:135], a[68:71], a[0:3], 0\n"
      "v_mfma_f32_16x16x32_bf16 v[140:143], a[68:71], a[4:7], 0\n"
      "v_mfma_f32_16x16x32_bf16 v[148:151], a[68:71], a[8:11], 0\n"
      "v_mfma_f32_16x16x32_bf16 v[156:159], a[68:71], a[12:15], 0\n"
      "v_mfma_f32_16x16x32_bf16 v[128:131], a[72:75], a[16:19], v[128:131]\n"
      "v_mfma_f32_16x16x32_bf16 v[136:139], a[72:75], a[20:23], v[136:139]\n"
      "v_mfma_f32_16x16x32_bf16 v[144:147], a[72:75], a[24:27], v[144:147]\n"
      "v_mfma_f32_16x16x32_bf16 v[152:155], a[72:75], a[28:31], v[152:155]\n"
      "v_mfma_f32_16x16x32_bf16 v[132:135], a[76:79], a[16:19], v[132:135]\n"
      "v_mfma_f32_16x16x32_bf16 v[140:143], a[76:79], a[20:23], v[140:143]\n"
      "v_mfma_f32_16x16x32_bf16 v[148:151], a[76:79], a[24:27], v[148:151]\n"
      "v_mfma_f32_16x16x32_bf16 v[156:159], a[76:79], a[28:31], v[156:159]\n"
      "v_mfma_f32_16x16x32_bf16 v[128:131], a[80:83], a[32:35], v[128:131]\n"
      "v_mfma_f32_16x16x32_bf16 v[136:139], a[80:83], a[36:39], v[136:139]\n"
      "v_mfma_f32_16x16x32_bf16 v[144:147], a[80:83], a[40:43], v[144:147]\n"
      "v_mfma_f32_16x16x32_bf16 v[152:155], a[80:83], a[44:47], v[152:155]\n"
      "v_mfma_f32_16x16x32_bf16 v[132:135], a[84:87], a[32:35], v[132:135]\n"
      "v_mfma_f32_16x16x32_bf16 v[140:143], a[84:87], a[36:39], v[140:143]\n"
      "v_mfma_f32_16x16x32_bf16 v[148:151], a[84:87], a[40:43], v[148:151]\n"
      "v_mfma_f32_16x16x32_bf16 v[156:159], a[84:87], a[44:47], v[156:159]\n"
      "v_mfma_f32_16x16x32_bf16 v[128:131], a[88:91], a[48:51], v[128:131]\n"
      "v_mfma_f32_16x16x32_bf16 v[136:139], a[88:91], a[52:55], v[136:139]\n"
      "v_mfma_f32_16x16x32_bf16 v[144:147], a[88:91], a[56:59], v[144:147]\n"
      "v_mfma_f32_16x16x32_bf16 v[152:155], a[88:91], a[60:63], v[152:155]\n"
      "v_mfma_f32_16x16x32_bf16 v[132:135], a[92:95], a[48:51], v[132:135]\n"
      "v_mfma_f32_16x16x32_bf16 v[140:143], a[92:95], a[52:55], v[140:143]\n"
      "v_mfma_f32_16x16x32_bf16 v[148:151], a[92:95], a[56:59], v[148:151]\n"
      "v_mfma_f32_16x16x32_bf16 v[156:159], a[92:95], a[60:63], v[156:159]\n"
      "s_nop 7\n"
      "v_max3_f32 v112, v128, v129, v130\n"
      "v_max3_f32 v113, v136, v137, v138\n"
      "v_max3_f32 v114, v144, v145, v146\n"
      "v_max3_f32 v115, v152, v153, v154\n"
      "v_max3_f32 v112, v112, v131, v132\n"
      "v_max3_f32 v113, v113, v139, v140\n"
      "v_max3_f32 v114, v114, v147, v148\n"
      "v_max3_f32 v115, v115, v155, v156\n"
      "v_max3_f32 v112, v112, v133, v134\n"
      "v_max3_f32 v113, v113, v141, v142\n"
      "v_max3_f32 v114, v114, v149, v150\n"
      "v_max3_f32 v115, v115, v157, v158\n"
      "v_max_f32 v112, v112, v135\n"
      "v_max_f32 v113, v113, v143\n"
      "v_max_f32 v114, v114, v151\n"
      "v_max_f32 v115, v115, v159\n"
      "v_mov_b32 v160, v112\n"
      "v_mov_b32 v161, v113\n"
      "v_mov_b32 v162, v114\n"
      "v_mov_b32 v163, v115\n"
      "v_permlane16_swap_b32 v112, v160\n"
      "v_permlane16_swap_b32 v113, v161\n"
      "v_permlane16_swap_b32 v114, v162\n"
      "v_permlane16_swap_b32 v115, v163\n"
      "v_max_f32 v112, v112, v160\n"
      "v_max_f32 v113, v113, v161\n"
      "v_max_f32 v114, v114, v162\n"
      "v_max_f32 v115, v115, v163\n"
      "v_mov_b32 v160, v112\n"
      "v_mov_b32 v161, v113\n"
      "v_mov_b32 v162, v114\n"
      "v_mov_b32 v163, v115\n"
      "v_permlane32_swap_b32 v112, v160\n"
      "v_permlane32_swap_b32 v113, v161\n"
      "v_permlane32_swap_b32 v114, v162\n"
      "v_permlane32_swap_b32 v115, v163\n"
      "v_max_f32 v112, v112, v160\n"
      "v_max_f32 v113, v113, v161\n"
      "v_max_f32 v114, v114, v162\n"
      "v_max_f32 v115, v115, v163\n"
      "v_sub_f32 v112, 0xc1800000, v112\n"
      "v_sub_f32 v113, 0xc1800000, v113\n"
      "v_sub_f32 v114, 0xc1800000, v114\n"
      "v_sub_f32 v115, 0xc1800000, v115\n"
      "v_floor_f32 v112, v112\n"
      "v_floor_f32 v113, v113\n"
      "v_floor_f32 v114, v114\n"
      "v_floor_f32 v115, v115\n"
      "v_min_f32 v112, v112, v224\n"
      "v_min_f32 v113, v113, v228\n"
      "v_min_f32 v114, v114, v232\n"
      "v_min_f32 v115, v115, v236\n"
      "v_sub_f32 v164, v112, v224\n"
      "v_sub_f32 v165, v113, v228\n"
      "v_sub_f32 v166, v114, v232\n"
      "v_sub_f32 v167, v115, v236\n"
      "v_mov_b32 v224, v112\n"
      "v_mov_b32 v228, v113\n"
      "v_mov_b32 v232, v114\n"
      "v_mov_b32 v236, v115\n"
      "v_exp_f32 v112, v164\n"
      "v_exp_f32 v113, v165\n"
      "v_exp_f32 v114, v166\n"
      "v_exp_f32 v115, v167\n"
      "v_mov_b32 v225, v224\n"
      "v_mov_b32 v229, v228\n"
      "v_mov_b32 v233, v232\n"
      "v_mov_b32 v237, v236\n"
      "v_mov_b64 v[226:227], v[224:225]\n"
      "v_mov_b64 v[230:231], v[228:229]\n"
      "v_mov_b64 v[234:235], v[232:233]\n"
      "v_mov_b64 v[238:239], v[236:237]\n"
      "s_call_b64 s[84:85], SCALE_%=\n"
      "v_pk_add_f32 v[128:129], v[128:129], v[224:225]\n"
      "v_pk_add_f32 v[130:131], v[130:131], v[224:225]\n"
      "v_pk_add_f32 v[132:133], v[132:133], v[224:225]\n"
      "v_pk_add_f32 v[134:135], v[134:135], v[224:225]\n"
      "v_pk_add_f32 v[136:137], v[136:137], v[228:229]\n"
      "v_pk_add_f32 v[138:139], v[138:139], v[228:229]\n"
      "v_pk_add_f32 v[140:141], v[140:141], v[228:229]\n"
      "v_pk_add_f32 v[142:143], v[142:143], v[228:229]\n"
      "v_pk_add_f32 v[144:145], v[144:145], v[232:233]\n"
      "v_pk_add_f32 v[146:147], v[146:147], v[232:233]\n"
      "v_pk_add_f32 v[148:149], v[148:149], v[232:233]\n"
      "v_pk_add_f32 v[150:151], v[150:151], v[232:233]\n"
      "v_pk_add_f32 v[152:153], v[152:153], v[236:237]\n"
      "v_pk_add_f32 v[154:155], v[154:155], v[236:237]\n"
      "v_pk_add_f32 v[156:157], v[156:157], v[236:237]\n"
      "v_pk_add_f32 v[158:159], v[158:159], v[236:237]\n"
      "v_exp_f32 v112, v128\n"
      "v_exp_f32 v113, v129\n"
      "v_exp_f32 v114, v130\n"
      "v_cvt_pk_bf16_f32 v128, v112, v113\n"
      "v_exp_f32 v115, v131\n"
      "v_exp_f32 v112, v132\n"
      "v_cvt_pk_bf16_f32 v129, v114, v115\n"
      "v_exp_f32 v113, v133\n"
      "v_exp_f32 v114, v134\n"
      "v_cvt_pk_bf16_f32 v130, v112, v113\n"
      "v_exp_f32 v115, v135\n"
      "v_exp_f32 v112, v136\n"
      "v_cvt_pk_bf16_f32 v131, v114, v115\n"
      "v_exp_f32 v113, v137\n"
      "v_exp_f32 v114, v138\n"
      "v_cvt_pk_bf16_f32 v132, v112, v113\n"
      "v_exp_f32 v115, v139\n"
      "v_exp_f32 v112, v140\n"
      "v_cvt_pk_bf16_f32 v133, v114, v115\n"
      "v_exp_f32 v113, v141\n"
      "v_exp_f32 v114, v142\n"
      "v_cvt_pk_bf16_f32 v134, v112, v113\n"
      "v_exp_f32 v115, v143\n"
      "v_exp_f32 v112, v144\n"
      "v_cvt_pk_bf16_f32 v135, v114, v115\n"
      "v_exp_f32 v113, v145\n"
      "v_exp_f32 v114, v146\n"
      "v_cvt_pk_bf16_f32 v136, v112, v113\n"
      "v_exp_f32 v115, v147\n"
      "v_exp_f32 v112, v148\n"
      "v_cvt_pk_bf16_f32 v137, v114, v115\n"
      "v_exp_f32 v113, v149\n"
      "v_exp_f32 v114, v150\n"
      "v_cvt_pk_bf16_f32 v138, v112, v113\n"
      "v_exp_f32 v115, v151\n"
      "v_exp_f32 v112, v152\n"
      "v_cvt_pk_bf16_f32 v139, v114, v115\n"
      "v_exp_f32 v113, v153\n"
      "v_exp_f32 v114, v154\n"
      "v_cvt_pk_bf16_f32 v140, v112, v113\n"
      "v_exp_f32 v115, v155\n"
      "v_exp_f32 v112, v156\n"
      "v_cvt_pk_bf16_f32 v141, v114, v115\n"
      "v_exp_f32 v113, v157\n"
      "v_exp_f32 v114, v158\n"
      "v_cvt_pk_bf16_f32 v142, v112, v113\n"
      "v_exp_f32 v115, v159\n"
      "s_nop 0\n"
      "v_cvt_pk_bf16_f32 v143, v114, v115\n"
      "ds_read_b128 a[64:67], v242 offset:32768\n"
      "ds_read_b128 a[68:71], v242 offset:36864\n"
      "ds_read_b128 a[72:75], v243 offset:32768\n"
      "ds_read_b128 a[76:79], v243 offset:36864\n"
      "ds_read_b128 a[80:83], v242 offset:33792\n"
      "ds_read_b128 a[84:87], v242 offset:37888\n"
      "ds_read_b128 a[88:91], v243 offset:33792\n"
      "ds_read_b128 a[92:95], v243 offset:37888\n"
      "s_waitcnt lgkmcnt(0)\n"
      "v_mfma_f32_16x16x32_bf16 v[160:163], a[64:67], a[0:3], v[224:227]\n"
      "v_mfma_f32_16x16x32_bf16 v[168:171], a[64:67], a[4:7], v[228:231]\n"
      "v_mfma_f32_16x16x32_bf16 v[176:179], a[64:67], a[8:11], v[232:235]\n"
      "v_mfma_f32_16x16x32_bf16 v[184:187], a[64:67], a[12:15], v[236:239]\n"
      "v_mfma_f32_16x16x32_bf16 v[164:167], a[68:71], a[0:3], v[224:227]\n"
      "v_mfma_f32_16x16x32_bf16 v[172:175], a[68:71], a[4:7], v[228:231]\n"
      "v_mfma_f32_16x16x32_bf16 v[180:183], a[68:71], a[8:11], v[232:235]\n"
      "v_mfma_f32_16x16x32_bf16 v[188:191], a[68:71], a[12:15], v[236:239]\n"
      "v_mfma_f32_16x16x32_bf16 v[160:163], a[72:75], a[16:19], v[160:163]\n"
      "v_mfma_f32_16x16x32_bf16 v[168:171], a[72:75], a[20:23], v[168:171]\n"
      "v_mfma_f32_16x16x32_bf16 v[176:179], a[72:75], a[24:27], v[176:179]\n"
      "v_mfma_f32_16x16x32_bf16 v[184:187], a[72:75], a[28:31], v[184:187]\n"
      "v_mfma_f32_16x16x32_bf16 v[164:167], a[76:79], a[16:19], v[164:167]\n"
      "v_mfma_f32_16x16x32_bf16 v[172:175], a[76:79], a[20:23], v[172:175]\n"
      "v_mfma_f32_16x16x32_bf16 v[180:183], a[76:79], a[24:27], v[180:183]\n"
      "v_mfma_f32_16x16x32_bf16 v[188:191], a[76:79], a[28:31], v[188:191]\n"
      "v_mfma_f32_16x16x32_bf16 v[160:163], a[80:83], a[32:35], v[160:163]\n"
      "v_mfma_f32_16x16x32_bf16 v[168:171], a[80:83], a[36:39], v[168:171]\n"
      "v_mfma_f32_16x16x32_bf16 v[176:179], a[80:83], a[40:43], v[176:179]\n"
      "v_mfma_f32_16x16x32_bf16 v[184:187], a[80:83], a[44:47], v[184:187]\n"
      "v_mfma_f32_16x16x32_bf16 v[164:167], a[84:87], a[32:35], v[164:167]\n"
      "v_mfma_f32_16x16x32_bf16 v[172:175], a[84:87], a[36:39], v[172:175]\n"
      "v_mfma_f32_16x16x32_bf16 v[180:183], a[84:87], a[40:43], v[180:183]\n"
      "v_mfma_f32_16x16x32_bf16 v[188:191], a[84:87], a[44:47], v[188:191]\n"
      "v_mfma_f32_16x16x32_bf16 v[160:163], a[88:91], a[48:51], v[160:163]\n"
      "v_mfma_f32_16x16x32_bf16 v[168:171], a[88:91], a[52:55], v[168:171]\n"
      "v_mfma_f32_16x16x32_bf16 v[176:179], a[88:91], a[56:59], v[176:179]\n"
      "v_mfma_f32_16x16x32_bf16 v[184:187], a[88:91], a[60:63], v[184:187]\n"
      "v_mfma_f32_16x16x32_bf16 v[164:167], a[92:95], a[48:51], v[164:167]\n"
      "v_mfma_f32_16x16x32_bf16 v[172:175], a[92:95], a[52:55], v[172:175]\n"
      "v_mfma_f32_16x16x32_bf16 v[180:183], a[92:95], a[56:59], v[180:183]\n"
      "v_mfma_f32_16x16x32_bf16 v[188:191], a[92:95], a[60:63], v[188:191]\n"
      "s_cmp_eq_u32 s64, 0\n"
      "s_cbranch_scc0 NRET0_0_%=\n"
      "v_mov_b64 v[192:193], v[128:129]\n"
      "v_mov_b64 v[194:195], v[130:131]\n"
      "v_mov_b64 v[196:197], v[132:133]\n"
      "v_mov_b64 v[198:199], v[134:135]\n"
      "v_mov_b64 v[200:201], v[136:137]\n"
      "v_mov_b64 v[202:203], v[138:139]\n"
      "v_mov_b64 v[204:205], v[140:141]\n"
      "v_mov_b64 v[206:207], v[142:143]\n"
      "s_nop 1\n"
      "s_branch RET0_0_%=\n"
      "NRET0_0_%=:\n"
      "s_cmp_eq_u32 s64, 1\n"
      "s_cbranch_scc0 NRET0_1_%=\n"
      "v_mov_b64 v[192:193], v[128:129]\n"
      "v_mov_b64 v[194:195], v[130:131]\n"
      "v_mov_b64 v[196:197], v[132:133]\n"
      "v_mov_b64 v[198:199], v[134:135]\n"
      "v_mov_b64 v[200:201], v[136:137]\n"
      "v_mov_b64 v[202:203], v[138:139]\n"
      "v_mov_b64 v[204:205], v[140:141]\n"
      "v_mov_b64 v[206:207], v[142:143]\n"
      "s_nop 1\n"
      "s_branch RET0_1_%=\n"
      "NRET0_1_%=:\n"
      "s_cmp_eq_u32 s64, 2\n"
      "s_cbranch_scc0 NRET0_2_%=\n"
      "v_mov_b64 v[192:193], v[128:129]\n"
      "v_mov_b64 v[194:195], v[130:131]\n"
      "v_mov_b64 v[196:197], v[132:133]\n"
      "v_mov_b64 v[198:199], v[134:135]\n"
      "v_mov_b64 v[200:201], v[136:137]\n"
      "v_mov_b64 v[202:203], v[138:139]\n"
      "v_mov_b64 v[204:205], v[140:141]\n"
      "v_mov_b64 v[206:207], v[142:143]\n"
      "s_nop 1\n"
      "s_branch RET0_2_%=\n"
      "NRET0_2_%=:\n"
      "s_cmp_eq_u32 s64, 3\n"
      "s_cbranch_scc0 NRET0_3_%=\n"
      "v_mov_b64 v[192:193], v[128:129]\n"
      "v_mov_b64 v[194:195], v[130:131]\n"
      "v_mov_b64 v[196:197], v[132:133]\n"
      "v_mov_b64 v[198:199], v[134:135]\n"
      "v_mov_b64 v[200:201], v[136:137]\n"
      "v_mov_b64 v[202:203], v[138:139]\n"
      "v_mov_b64 v[204:205], v[140:141]\n"
      "v_mov_b64 v[206:207], v[142:143]\n"
      "s_nop 1\n"
      "s_branch RET0_3_%=\n"
      "NRET0_3_%=:\n"
      "s_cmp_eq_u32 s64, 4\n"
      "s_cbranch_scc0 NRET0_4_%=\n"
      "v_mov_b64 v[192:193], v[128:129]\n"
      "v_mov_b64 v[194:195], v[130:131]\n"
      "v_mov_b64 v[196:197], v[132:133]\n"
      "v_mov_b64 v[198:199], v[134:135]\n"
      "v_mov_b64 v[200:201], v[136:137]\n"
      "v_mov_b64 v[202:203], v[138:139]\n"
      "v_mov_b64 v[204:205], v[140:141]\n"
      "v_mov_b64 v[206:207], v[142:143]\n"
      "s_nop 1\n"
      "s_branch RET0_4_%=\n"
      "NRET0_4_%=:\n"
      "s_cmp_eq_u32 s64, 5\n"
      "s_cbranch_scc0 NRET0_5_%=\n"
      "v_mov_b64 v[16:17], v[128:129]\n"
      "v_mov_b64 v[18:19], v[130:131]\n"
      "v_mov_b64 v[20:21], v[132:133]\n"
      "v_mov_b64 v[22:23], v[134:135]\n"
      "v_mov_b64 v[24:25], v[136:137]\n"
      "v_mov_b64 v[26:27], v[138:139]\n"
      "v_mov_b64 v[28:29], v[140:141]\n"
      "v_mov_b64 v[30:31], v[142:143]\n"
      "s_nop 1\n"
      "s_branch RET0_5_%=\n"
      "NRET0_5_%=:\n"
      "FB1_%=:\n"
      "s_add_u32 s84, s63, 1\n"
      "s_sub_u32 s84, s84, %[n1]\n"
      "s_or_b32 s84, s84, s64\n"
      "s_cbranch_scc0 RET1_0_%=\n"
      "s_nop 7\n"
      "s_nop 7\n"
      "v_xor_b32 v118, 0x10000, v242\n"
      "v_xor_b32 v119, 0x10000, v243\n"
      "ds_read_b128 a[64:67], v118 offset:32768\n"
      "ds_read_b128 a[68:71], v118 offset:36864\n"
      "ds_read_b128 a[72:75], v119 offset:32768\n"
      "ds_read_b128 a[76:79], v119 offset:36864\n"
      "ds_read_b128 a[80:83], v118 offset:33792\n"
      "ds_read_b128 a[84:87], v118 offset:37888\n"
      "ds_read_b128 a[88:91], v119 offset:33792\n"
      "ds_read_b128 a[92:95], v119 offset:37888\n"
      "s_waitcnt lgkmcnt(0)\n"
      "v_mfma_f32_16x16x32_bf16 v[160:163], a[64:67], a[0:3], 0\n"
      "v_mfma_f32_16x16x32_bf16 v[168:171], a[64:67], a[4:7], 0\n"
      "v_mfma_f32_16x16x32_bf16 v[176:179], a[64:67], a[8:11], 0\n"
      "v_mfma_f32_16x16x32_bf16 v[184:187], a[64:67], a[12:15], 0\n"
      "v_mfma_f32_16x16x32_bf16 v[164:167], a[68:71], a[0:3], 0\n"
      "v_mfma_f32_16x16x32_bf16 v[172:175], a[68:71], a[4:7], 0\n"
      "v_mfma_f32_16x16x32_bf16 v[180:183], a[68:71], a[8:11], 0\n"
      "v_mfma_f32_16x16x32_bf16 v[188:191], a[68:71], a[12:15], 0\n"
      "v_mfma_f32_16x16x32_bf16 v[160:163], a[72:75], a[16:19], v[160:163]\n"
      "v_mfma_f32_16x16x32_bf16 v[168:171], a[72:75], a[20:23], v[168:171]\n"
      "v_mfma_f32_16x16x32_bf16 v[176:179], a[72:75], a[24:27], v[176:179]\n"
      "v_mfma_f32_16x16x32_bf16 v[184:187], a[72:75], a[28:31], v[184:187]\n"
      "v_mfma_f32_16x16x32_bf16 v[164:167], a[76:79], a[16:19], v[164:167]\n"
      "v_mfma_f32_16x16x32_bf16 v[172:175], a[76:79], a[20:23], v[172:175]\n"
      "v_mfma_f32_16x16x32_bf16 v[180:183], a[76:79], a[24:27], v[180:183]\n"
      "v_mfma_f32_16x16x32_bf16 v[188:191], a[76:79], a[28:31], v[188:191]\n"
      "v_mfma_f32_16x16x32_bf16 v[160:163], a[80:83], a[32:35], v[160:163]\n"
      "v_mfma_f32_16x16x32_bf16 v[168:171], a[80:83], a[36:39], v[168:171]\n"
      "v_mfma_f32_16x16x32_bf16 v[176:179], a[80:83], a[40:43], v[176:179]\n"
      "v_mfma_f32_16x16x32_bf16 v[184:187], a[80:83], a[44:47], v[184:187]\n"
      "v_mfma_f32_16x16x32_bf16 v[164:167], a[84:87], a[32:35], v[164:167]\n"
      "v_mfma_f32_16x16x32_bf16 v[172:175], a[84:87], a[36:39], v[172:175]\n"
      "v_mfma_f32_16x16x32_bf16 v[180:183], a[84:87], a[40:43], v[180:183]\n"
      "v_mfma_f32_16x16x32_bf16 v[188:191], a[84:87], a[44:47], v[188:191]\n"
      "v_mfma_f32_16x16x32_bf16 v[160:163], a[88:91], a[48:51], v[160:163]\n"
      "v_mfma_f32_16x16x32_bf16 v[168:171], a[88:91], a[52:55], v[168:171]\n"
      "v_mfma_f32_16x16x32_bf16 v[176:179], a[88:91], a[56:59], v[176:179]\n"
      "v_mfma_f32_16x16x32_bf16 v[184:187], a[88:91], a[60:63], v[184:187]\n"
      "v_mfma_f32_16x16x32_bf16 v[164:167], a[92:95], a[48:51], v[164:167]\n"
      "v_mfma_f32_16x16x32_bf16 v[172:175], a[92:95], a[52:55], v[172:175]\n"
      "v_mfma_f32_16x16x32_bf16 v[180:183], a[92:95], a[56:59], v[180:183]\n"
      "v_mfma_f32_16x16x32_bf16 v[188:191], a[92:95], a[60:63], v[188:191]\n"
      "s_nop 7\n"
      "v_max3_f32 v112, v160, v161, v162\n"
      "v_max3_f32 v113, v168, v169, v170\n"
      "v_max3_f32 v114, v176, v177, v178\n"
      "v_max3_f32 v115, v184, v185, v186\n"
      "v_max3_f32 v112, v112, v163, v164\n"
      "v_max3_f32 v113, v113, v171, v172\n"
      "v_max3_f32 v114, v114, v179, v180\n"
      "v_max3_f32 v115, v115, v187, v188\n"
      "v_max3_f32 v112, v112, v165, v166\n"
      "v_max3_f32 v113, v113, v173, v174\n"
      "v_max3_f32 v114, v114, v181, v182\n"
      "v_max3_f32 v115, v115, v189, v190\n"
      "v_max_f32 v112, v112, v167\n"
      "v_max_f32 v113, v113, v175\n"
      "v_max_f32 v114, v114, v183\n"
      "v_max_f32 v115, v115, v191\n"
      "v_mov_b32 v128, v112\n"
      "v_mov_b32 v129, v113\n"
      "v_mov_b32 v130, v114\n"
      "v_mov_b32 v131, v115\n"
      "v_permlane16_swap_b32 v112, v128\n"
      "v_permlane16_swap_b32 v113, v129\n"
      "v_permlane16_swap_b32 v114, v130\n"
      "v_permlane16_swap_b32 v115, v131\n"
      "v_max_f32 v112, v112, v128\n"
      "v_max_f32 v113, v113, v129\n"
      "v_max_f32 v114, v114, v130\n"
      "v_max_f32 v115, v115, v131\n"
      "v_mov_b32 v128, v112\n"
      "v_mov_b32 v129, v113\n"
      "v_mov_b32 v130, v114\n"
      "v_mov_b32 v131, v115\n"
      "v_permlane32_swap_b32 v112, v128\n"
      "v_permlane32_swap_b32 v113, v129\n"
      "v_permlane32_swap_b32 v114, v130\n"
      "v_permlane32_swap_b32 v115, v131\n"
      "v_max_f32 v112, v112, v128\n"
      "v_max_f32 v113, v113, v129\n"
      "v_max_f32 v114, v114, v130\n"
      "v_max_f32 v115, v115, v131\n"
      "v_sub_f32 v112, 0xc1800000, v112\n"
      "v_sub_f32 v113, 0xc1800000, v113\n"
      "v_sub_f32 v114, 0xc1800000, v114\n"
      "v_sub_f32 v115, 0xc1800000, v115\n"
      "v_floor_f32 v112, v112\n"
      "v_floor_f32 v113, v113\n"
      "v_floor_f32 v114, v114\n"
      "v_floor_f32 v115, v115\n"
      "v_min_f32 v112, v112, v224\n"
      "v_min_f32 v113, v113, v228\n"
      "v_min_f32 v114, v114, v232\n"
      "v_min_f32 v115, v115, v236\n"
      "v_sub_f32 v132, v112, v224\n"
      "v_sub_f32 v133, v113, v228\n"
      "v_sub_f32 v134, v114, v232\n"
      "v_sub_f32 v135, v115, v236\n"
      "v_mov_b32 v224, v112\n"
      "v_mov_b32 v228, v113\n"
      "v_mov_b32 v232, v114\n"
      "v_mov_b32 v236, v115\n"
      "v_exp_f32 v112, v132\n"
      "v_exp_f32 v113, v133\n"
      "v_exp_f32 v114, v134\n"
      "v_exp_f32 v115, v135\n"
      "v_mov_b32 v225, v224\n"
      "v_mov_b32 v229, v228\n"
      "v_mov_b32 v233, v232\n"
      "v_mov_b32 v237, v236\n"
      "v_mov_b64 v[226:227], v[224:225]\n"
      "v_mov_b64 v[230:231], v[228:229]\n"
      "v_mov_b64 v[234:235], v[232:233]\n"
      "v_mov_b64 v[238:239], v[236:237]\n"
      "s_call_b64 s[84:85], SCALE_%=\n"
      "v_pk_add_f32 v[160:161], v[160:161], v[224:225]\n"
      "v_pk_add_f32 v[162:163], v[162:163], v[224:225]\n"
      "v_pk_add_f32 v[164:165], v[164:165], v[224:225]\n"
      "v_pk_add_f32 v[166:167], v[166:167], v[224:225]\n"
      "v_pk_add_f32 v[168:169], v[168:169], v[228:229]\n"
      "v_pk_add_f32 v[170:171], v[170:171], v[228:229]\n"
      "v_pk_add_f32 v[172:173], v[172:173], v[228:229]\n"
      "v_pk_add_f32 v[174:175], v[174:175], v[228:229]\n"
      "v_pk_add_f32 v[176:177], v[176:177], v[232:233]\n"
      "v_pk_add_f32 v[178:179], v[178:179], v[232:233]\n"
      "v_pk_add_f32 v[180:181], v[180:181], v[232:233]\n"
      "v_pk_add_f32 v[182:183], v[182:183], v[232:233]\n"
      "v_pk_add_f32 v[184:185], v[184:185], v[236:237]\n"
      "v_pk_add_f32 v[186:187], v[186:187], v[236:237]\n"
      "v_pk_add_f32 v[188:189], v[188:189], v[236:237]\n"
      "v_pk_add_f32 v[190:191], v[190:191], v[236:237]\n"
      "v_exp_f32 v112, v160\n"
      "v_exp_f32 v113, v161\n"
      "v_exp_f32 v114, v162\n"
      "v_cvt_pk_bf16_f32 v160, v112, v113\n"
      "v_exp_f32 v115, v163\n"
      "v_exp_f32 v112, v164\n"
      "v_cvt_pk_bf16_f32 v161, v114, v115\n"
      "v_exp_f32 v113, v165\n"
      "v_exp_f32 v114, v166\n"
      "v_cvt_pk_bf16_f32 v162, v112, v113\n"
      "v_exp_f32 v115, v167\n"
      "v_exp_f32 v112, v168\n"
      "v_cvt_pk_bf16_f32 v163, v114, v115\n"
      "v_exp_f32 v113, v169\n"
      "v_exp_f32 v114, v170\n"
      "v_cvt_pk_bf16_f32 v164, v112, v113\n"
      "v_exp_f32 v115, v171\n"
      "v_exp_f32 v112, v172\n"
      "v_cvt_pk_bf16_f32 v165, v114, v115\n"
      "v_exp_f32 v113, v173\n"
      "v_exp_f32 v114, v174\n"
      "v_cvt_pk_bf16_f32 v166, v112, v113\n"
      "v_exp_f32 v115, v175\n"
      "v_exp_f32 v112, v176\n"
      "v_cvt_pk_bf16_f32 v167, v114, v115\n"
      "v_exp_f32 v113, v177\n"
      "v_exp_f32 v114, v178\n"
      "v_cvt_pk_bf16_f32 v168, v112, v113\n"
      "v_exp_f32 v115, v179\n"
      "v_exp_f32 v112, v180\n"
      "v_cvt_pk_bf16_f32 v169, v114, v115\n"
      "v_exp_f32 v113, v181\n"
      "v_exp_f32 v114, v182\n"
      "v_cvt_pk_bf16_f32 v170, v112, v113\n"
      "v_exp_f32 v115, v183\n"
      "v_exp_f32 v112, v184\n"
      "v_cvt_pk_bf16_f32 v171, v114, v115\n"
      "v_exp_f32 v113, v185\n"
      "v_exp_f32 v114, v186\n"
      "v_cvt_pk_bf16_f32 v172, v112, v113\n"
      "v_exp_f32 v115, v187\n"
      "v_exp_f32 v112, v188\n"
      "v_cvt_pk_bf16_f32 v173, v114, v115\n"
      "v_exp_f32 v113, v189\n"
      "v_exp_f32 v114, v190\n"
      "v_cvt_pk_bf16_f32 v174, v112, v113\n"
      "v_exp_f32 v115, v191\n"
      "s_nop 0\n"
      "v_cvt_pk_bf16_f32 v175, v114, v115\n"
      "ds_read_b128 a[64:67], v242 offset:0\n"
      "ds_read_b128 a[68:71], v242 offset:4096\n"
      "ds_read_b128 a[72:75], v243 offset:0\n"
      "ds_read_b128 a[76:79], v243 offset:4096\n"
      "ds_read_b128 a[80:83], v242 offset:1024\n"
      "ds_read_b128 a[84:87], v242 offset:5120\n"
      "ds_read_b128 a[88:91], v243 offset:1024\n"
      "ds_read_b128 a[92:95], v243 offset:5120\n"
      "s_waitcnt lgkmcnt(0)\n"
      "v_mfma_f32_16x16x32_bf16 v[128:131], a[64:67], a[0:3], v[224:227]\n"
      "v_mfma_f32_16x16x32_bf16 v[136:139], a[64:67], a[4:7], v[228:231]\n"
      "v_mfma_f32_16x16x32_bf16 v[144:147], a[64:67], a[8:11], v[232:235]\n"
      "v_mfma_f32_16x16x32_bf16 v[152:155], a[64:67], a[12:15], v[236:239]\n"
      "v_mfma_f32_16x16x32_bf16 v[132:135], a[68:71], a[0:3], v[224:227]\n"
      "v_mfma_f32_16x16x32_bf16 v[140:143], a[68:71], a[4:7], v[228:231]\n"
      "v_mfma_f32_16x16x32_bf16 v[148:151], a[68:71], a[8:11], v[232:235]\n"
      "v_mfma_f32_16x16x32_bf16 v[156:159], a[68:71], a[12:15], v[236:239]\n"
      "v_mfma_f32_16x16x32_bf16 v[128:131], a[72:75], a[16:19], v[128:131]\n"
      "v_mfma_f32_16x16x32_bf16 v[136:139], a[72:75], a[20:23], v[136:139]\n"
      "v_mfma_f32_16x16x32_bf16 v[144:147], a[72:75], a[24:27], v[144:147]\n"
      "v_mfma_f32_16x16x32_bf16 v[152:155], a[72:75], a[28:31], v[152:155]\n"
      "v_mfma_f32_16x16x32_bf16 v[132:135], a[76:79], a[16:19], v[132:135]\n"
      "v_mfma_f32_16x16x32_bf16 v[140:143], a[76:79], a[20:23], v[140:143]\n"
      "v_mfma_f32_16x16x32_bf16 v[148:151], a[76:79], a[24:27], v[148:151]\n"
      "v_mfma_f32_16x16x32_bf16 v[156:159], a[76:79], a[28:31], v[156:159]\n"
      "v_mfma_f32_16x16x32_bf16 v[128:131], a[80:83], a[32:35], v[128:131]\n"
      "v_mfma_f32_16x16x32_bf16 v[136:139], a[80:83], a[36:39], v[136:139]\n"
      "v_mfma_f32_16x16x32_bf16 v[144:147], a[80:83], a[40:43], v[144:147]\n"
      "v_mfma_f32_16x16x32_bf16 v[152:155], a[80:83], a[44:47], v[152:155]\n"
      "v_mfma_f32_16x16x32_bf16 v[132:135], a[84:87], a[32:35], v[132:135]\n"
      "v_mfma_f32_16x16x32_bf16 v[140:143], a[84:87], a[36:39], v[140:143]\n"
      "v_mfma_f32_16x16x32_bf16 v[148:151], a[84:87], a[40:43], v[148:151]\n"
      "v_mfma_f32_16x16x32_bf16 v[156:159], a[84:87], a[44:47], v[156:159]\n"
      "v_mfma_f32_16x16x32_bf16 v[128:131], a[88:91], a[48:51], v[128:131]\n"
      "v_mfma_f32_16x16x32_bf16 v[136:139], a[88:91], a[52:55], v[136:139]\n"
      "v_mfma_f32_16x16x32_bf16 v[144:147], a[88:91], a[56:59], v[144:147]\n"
      "v_mfma_f32_16x16x32_bf16 v[152:155], a[88:91], a[60:63], v[152:155]\n"
      "v_mfma_f32_16x16x32_bf16 v[132:135], a[92:95], a[48:51], v[132:135]\n"
      "v_mfma_f32_16x16x32_bf16 v[140:143], a[92:95], a[52:55], v[140:143]\n"
      "v_mfma_f32_16x16x32_bf16 v[148:151], a[92:95], a[56:59], v[148:151]\n"
      "v_mfma_f32_16x16x32_bf16 v[156:159], a[92:95], a[60:63], v[156:159]\n"
      "s_cmp_eq_u32 s64, 0\n"
      "s_cbranch_scc0 NRET1_0_%=\n"
      "v_mov_b64 v[208:209], v[160:161]\n"
      "v_mov_b64 v[210:211], v[162:163]\n"
      "v_mov_b64 v[212:213], v[164:165]\n"
      "v_mov_b64 v[214:215], v[166:167]\n"
      "v_mov_b64 v[216:217], v[168:169]\n"
      "v_mov_b64 v[218:219], v[170:171]\n"
      "v_mov_b64 v[220:221], v[172:173]\n"
      "v_mov_b64 v[222:223], v[174:175]\n"
      "s_nop 1\n"
      "s_branch RET1_0_%=\n"
      "NRET1_0_%=:\n"
      "s_cmp_eq_u32 s64, 1\n"
      "s_cbranch_scc0 NRET1_1_%=\n"
      "v_mov_b64 v[208:209], v[160:161]\n"
      "v_mov_b64 v[210:211], v[162:163]\n"
      "v_mov_b64 v[212:213], v[164:165]\n"
      "v_mov_b64 v[214:215], v[166:167]\n"
      "v_mov_b64 v[216:217], v[168:169]\n"
      "v_mov_b64 v[218:219], v[170:171]\n"
      "v_mov_b64 v[220:221], v[172:173]\n"
      "v_mov_b64 v[222:223], v[174:175]\n"
      "s_nop 1\n"
      "s_branch RET1_1_%=\n"
      "NRET1_1_%=:\n"
      "s_cmp_eq_u32 s64, 2\n"
      "s_cbranch_scc0 NRET1_2_%=\n"
      "v_mov_b64 v[208:209], v[160:161]\n"
      "v_mov_b64 v[210:211], v[162:163]\n"
      "v_mov_b64 v[212:213], v[164:165]\n"
      "v_mov_b64 v[214:215], v[166:167]\n"
      "v_mov_b64 v[216:217], v[168:169]\n"
      "v_mov_b64 v[218:219], v[170:171]\n"
      "v_mov_b64 v[220:221], v[172:173]\n"
      "v_mov_b64 v[222:223], v[174:175]\n"
      "s_nop 1\n"
      "s_branch RET1_2_%=\n"
      "NRET1_2_%=:\n"
      "s_cmp_eq_u32 s64, 3\n"
      "s_cbranch_scc0 NRET1_3_%=\n"
      "v_mov_b64 v[208:209], v[160:161]\n"
      "v_mov_b64 v[210:211], v[162:163]\n"
      "v_mov_b64 v[212:213], v[164:165]\n"
      "v_mov_b64 v[214:215], v[166:167]\n"
      "v_mov_b64 v[216:217], v[168:169]\n"
      "v_mov_b64 v[218:219], v[170:171]\n"
      "v_mov_b64 v[220:221], v[172:173]\n"
      "v_mov_b64 v[222:223], v[174:175]\n"
      "s_nop 1\n"
      "s_branch RET1_3_%=\n"
      "NRET1_3_%=:\n"
      "s_cmp_eq_u32 s64, 4\n"
      "s_cbranch_scc0 NRET1_4_%=\n"
      "v_mov_b64 v[208:209], v[160:161]\n"
      "v_mov_b64 v[210:211], v[162:163]\n"
      "v_mov_b64 v[212:213], v[164:165]\n"
      "v_mov_b64 v[214:215], v[166:167]\n"
      "v_mov_b64 v[216:217], v[168:169]\n"
      "v_mov_b64 v[218:219], v[170:171]\n"
      "v_mov_b64 v[220:221], v[172:173]\n"
      "v_mov_b64 v[222:223], v[174:175]\n"
      "s_nop 1\n"
      "s_branch RET1_4_%=\n"
      "NRET1_4_%=:\n"
      "s_cmp_eq_u32 s64, 5\n"
      "s_cbranch_scc0 NRET1_5_%=\n"
      "v_mov_b64 v[0:1], v[160:161]\n"
      "v_mov_b64 v[2:3], v[162:163]\n"
      "v_mov_b64 v[4:5], v[164:165]\n"
      "v_mov_b64 v[6:7], v[166:167]\n"
      "v_mov_b64 v[8:9], v[168:169]\n"
      "v_mov_b64 v[10:11], v[170:171]\n"
      "v_mov_b64 v[12:13], v[172:173]\n"
      "v_mov_b64 v[14:15], v[174:175]\n"
      "s_nop 1\n"
      "s_branch RET1_5_%=\n"
      "NRET1_5_%=:\n"
      "s_cmp_eq_u32 s64, 6\n"
      "s_cbranch_scc0 NRET1_6_%=\n"
      "v_mov_b64 v[96:97], v[160:161]\n"
      "v_mov_b64 v[98:99], v[162:163]\n"
      "v_mov_b64 v[100:101], v[164:165]\n"
      "v_mov_b64 v[102:103], v[166:167]\n"
      "v_mov_b64 v[104:105], v[168:169]\n"
      "v_mov_b64 v[106:107], v[170:171]\n"
      "v_mov_b64 v[108:109], v[172:173]\n"
      "v_mov_b64 v[110:111], v[174:175]\n"
      "v_mov_b32 v117, v224\n"
      "s_nop 1\n"
      "v_mov_b32_dpp v117, v228 quad_perm:[0,1,2,3] row_mask:0x2 bank_mask:0xf\n"
      "s_nop 1\n"
      "v_mov_b32_dpp v117, v232 quad_perm:[0,1,2,3] row_mask:0x4 bank_mask:0xf\n"
      "s_nop 1\n"
      "v_mov_b32_dpp v117, v236 quad_perm:[0,1,2,3] row_mask:0x8 bank_mask:0xf\n"
      "v_mfma_f32_16x16x32_bf16 a[228:231], a[196:199], v[96:99], a[192:195]\n"
      "v_mfma_f32_16x16x32_bf16 a[228:231], a[200:203], v[100:103], a[228:231]\n"
      "v_mfma_f32_16x16x32_bf16 a[228:231], a[204:207], v[104:107], a[228:231]\n"
      "ds_write_b128 v125, a[160:163] offset:0\n"
      "ds_write_b128 v125, a[164:167] offset:1024\n"
      "ds_write_b128 v125, a[168:171] offset:2048\n"
      "ds_write_b128 v125, a[172:175] offset:3072\n"
      "ds_write_b128 v125, a[176:179] offset:4096\n"
      "ds_write_b128 v125, a[180:183] offset:5120\n"
      "ds_write_b128 v125, a[184:187] offset:6144\n"
      "ds_write_b128 v125, a[188:191] offset:7168\n"
      "ds_write_b128 v125, v[96:99] offset:8192\n"
      "ds_write_b128 v125, v[100:103] offset:9216\n"
      "ds_write_b128 v125, v[104:107] offset:10240\n"
      "ds_write2_b32 v124, v117, v123 offset0:1 offset1:2\n"
      "s_nop 1\n"
      "s_branch RET1_6_%=\n"
      "NRET1_6_%=:\n"
      "SCALE_%=:\n"
      "v_pk_mul_f32 v[32:33], v[32:33], v[112:113] op_sel:[0,0] op_sel_hi:[1,0]\n"
      "v_pk_mul_f32 v[34:35], v[34:35], v[112:113] op_sel:[0,0] op_sel_hi:[1,0]\n"
      "v_pk_mul_f32 v[36:37], v[36:37], v[112:113] op_sel:[0,1] op_sel_hi:[1,1]\n"
      "v_pk_mul_f32 v[38:39], v[38:39], v[112:113] op_sel:[0,1] op_sel_hi:[1,1]\n"
      "v_pk_mul_f32 v[40:41], v[40:41], v[114:115] op_sel:[0,0] op_sel_hi:[1,0]\n"
      "v_pk_mul_f32 v[42:43], v[42:43], v[114:115] op_sel:[0,0] op_sel_hi:[1,0]\n"
      "v_pk_mul_f32 v[44:45], v[44:45], v[114:115] op_sel:[0,1] op_sel_hi:[1,1]\n"
      "v_pk_mul_f32 v[46:47], v[46:47], v[114:115] op_sel:[0,1] op_sel_hi:[1,1]\n"
      "v_pk_mul_f32 v[48:49], v[48:49], v[112:113] op_sel:[0,0] op_sel_hi:[1,0]\n"
      "v_pk_mul_f32 v[50:51], v[50:51], v[112:113] op_sel:[0,0] op_sel_hi:[1,0]\n"
      "v_pk_mul_f32 v[52:53], v[52:53], v[112:113] op_sel:[0,1] op_sel_hi:[1,1]\n"
      "v_pk_mul_f32 v[54:55], v[54:55], v[112:113] op_sel:[0,1] op_sel_hi:[1,1]\n"
      "v_pk_mul_f32 v[56:57], v[56:57], v[114:115] op_sel:[0,0] op_sel_hi:[1,0]\n"
      "v_pk_mul_f32 v[58:59], v[58:59], v[114:115] op_sel:[0,0] op_sel_hi:[1,0]\n"
      "v_pk_mul_f32 v[60:61], v[60:61], v[114:115] op_sel:[0,1] op_sel_hi:[1,1]\n"
      "v_pk_mul_f32 v[62:63], v[62:63], v[114:115] op_sel:[0,1] op_sel_hi:[1,1]\n"
      "v_pk_mul_f32 v[64:65], v[64:65], v[112:113] op_sel:[0,0] op_sel_hi:[1,0]\n"
      "v_pk_mul_f32 v[66:67], v[66:67], v[112:113] op_sel:[0,0] op_sel_hi:[1,0]\n"
      "v_pk_mul_f32 v[68:69], v[68:69], v[112:113] op_sel:[0,1] op_sel_hi:[1,1]\n"
      "v_pk_mul_f32 v[70:71], v[70:71], v[112:113] op_sel:[0,1] op_sel_hi:[1,1]\n"
      "v_pk_mul_f32 v[72:73], v[72:73], v[114:115] op_sel:[0,0] op_sel_hi:[1,0]\n"
      "v_pk_mul_f32 v[74:75], v[74:75], v[114:115] op_sel:[0,0] op_sel_hi:[1,0]\n"
      "v_pk_mul_f32 v[76:77], v[76:77], v[114:115] op_sel:[0,1] op_sel_hi:[1,1]\n"
      "v_pk_mul_f32 v[78:79], v[78:79], v[114:115] op_sel:[0,1] op_sel_hi:[1,1]\n"
      "v_pk_mul_f32 v[80:81], v[80:81], v[112:113] op_sel:[0,0] op_sel_hi:[1,0]\n"
      "v_pk_mul_f32 v[82:83], v[82:83], v[112:113] op_sel:[0,0] op_sel_hi:[1,0]\n"
      "v_pk_mul_f32 v[84:85], v[84:85], v[112:113] op_sel:[0,1] op_sel_hi:[1,1]\n"
      "v_pk_mul_f32 v[86:87], v[86:87], v[112:113] op_sel:[0,1] op_sel_hi:[1,1]\n"
      "v_pk_mul_f32 v[88:89], v[88:89], v[114:115] op_sel:[0,0] op_sel_hi:[1,0]\n"
      "v_pk_mul_f32 v[90:91], v[90:91], v[114:115] op_sel:[0,0] op_sel_hi:[1,0]\n"
      "v_pk_mul_f32 v[92:93], v[92:93], v[114:115] op_sel:[0,1] op_sel_hi:[1,1]\n"
      "v_pk_mul_f32 v[94:95], v[94:95], v[114:115] op_sel:[0,1] op_sel_hi:[1,1]\n"
      "v_accvgpr_read_b32 v118, a128\n"
      "v_accvgpr_read_b32 v119, a129\n"
      "v_pk_mul_f32 v[118:119], v[118:119], v[112:113] op_sel:[0,0] op_sel_hi:[1,0]\n"
      "v_accvgpr_write_b32 a128, v118\n"
      "v_accvgpr_write_b32 a129, v119\n"
      "v_accvgpr_read_b32 v118, a130\n"
      "v_accvgpr_read_b32 v119, a131\n"
      "v_pk_mul_f32 v[118:119], v[118:119], v[112:113] op_sel:[0,0] op_sel_hi:[1,0]\n"
      "v_accvgpr_write_b32 a130, v118\n"
      "v_accvgpr_write_b32 a131, v119\n"
      "v_accvgpr_read_b32 v118, a132\n"
      "v_accvgpr_read_b32 v119, a133\n"
      "v_pk_mul_f32 v[118:119], v[118:119], v[112:113] op_sel:[0,1] op_sel_hi:[1,1]\n"
      "v_accvgpr_write_b32 a132, v118\n"
      "v_accvgpr_write_b32 a133, v119\n"
      "v_accvgpr_read_b32 v118, a134\n"
      "v_accvgpr_read_b32 v119, a135\n"
      "v_pk_mul_f32 v[118:119], v[118:119], v[112:113] op_sel:[0,1] op_sel_hi:[1,1]\n"
      "v_accvgpr_write_b32 a134, v118\n"
      "v_accvgpr_write_b32 a135, v119\n"
      "v_accvgpr_read_b32 v118, a136\n"
      "v_accvgpr_read_b32 v119, a137\n"
      "v_pk_mul_f32 v[118:119], v[118:119], v[114:115] op_sel:[0,0] op_sel_hi:[1,0]\n"
      "v_accvgpr_write_b32 a136, v118\n"
      "v_accvgpr_write_b32 a137, v119\n"
      "v_accvgpr_read_b32 v118, a138\n"
      "v_accvgpr_read_b32 v119, a139\n"
      "v_pk_mul_f32 v[118:119], v[118:119], v[114:115] op_sel:[0,0] op_sel_hi:[1,0]\n"
      "v_accvgpr_write_b32 a138, v118\n"
      "v_accvgpr_write_b32 a139, v119\n"
      "v_accvgpr_read_b32 v118, a140\n"
      "v_accvgpr_read_b32 v119, a141\n"
      "v_pk_mul_f32 v[118:119], v[118:119], v[114:115] op_sel:[0,1] op_sel_hi:[1,1]\n"
      "v_accvgpr_write_b32 a140, v118\n"
      "v_accvgpr_write_b32 a141, v119\n"
      "v_accvgpr_read_b32 v118, a142\n"
      "v_accvgpr_read_b32 v119, a143\n"
      "v_pk_mul_f32 v[118:119], v[118:119], v[114:115] op_sel:[0,1] op_sel_hi:[1,1]\n"
      "v_accvgpr_write_b32 a142, v118\n"
      "v_accvgpr_write_b32 a143, v119\n"
      "v_accvgpr_read_b32 v118, a144\n"
      "v_accvgpr_read_b32 v119, a145\n"
      "v_pk_mul_f32 v[118:119], v[118:119], v[112:113] op_sel:[0,0] op_sel_hi:[1,0]\n"
      "v_accvgpr_write_b32 a144, v118\n"
      "v_accvgpr_write_b32 a145, v119\n"
      "v_accvgpr_read_b32 v118, a146\n"
      "v_accvgpr_read_b32 v119, a147\n"
      "v_pk_mul_f32 v[118:119], v[118:119], v[112:113] op_sel:[0,0] op_sel_hi:[1,0]\n"
      "v_accvgpr_write_b32 a146, v118\n"
      "v_accvgpr_write_b32 a147, v119\n"
      "v_accvgpr_read_b32 v118, a148\n"
      "v_accvgpr_read_b32 v119, a149\n"
      "v_pk_mul_f32 v[118:119], v[118:119], v[112:113] op_sel:[0,1] op_sel_hi:[1,1]\n"
      "v_accvgpr_write_b32 a148, v118\n"
      "v_accvgpr_write_b32 a149, v119\n"
      "v_accvgpr_read_b32 v118, a150\n"
      "v_accvgpr_read_b32 v119, a151\n"
      "v_pk_mul_f32 v[118:119], v[118:119], v[112:113] op_sel:[0,1] op_sel_hi:[1,1]\n"
      "v_accvgpr_write_b32 a150, v118\n"
      "v_accvgpr_write_b32 a151, v119\n"
      "v_accvgpr_read_b32 v118, a152\n"
      "v_accvgpr_read_b32 v119, a153\n"
      "v_pk_mul_f32 v[118:119], v[118:119], v[114:115] op_sel:[0,0] op_sel_hi:[1,0]\n"
      "v_accvgpr_write_b32 a152, v118\n"
      "v_accvgpr_write_b32 a153, v119\n"
      "v_accvgpr_read_b32 v118, a154\n"
      "v_accvgpr_read_b32 v119, a155\n"
      "v_pk_mul_f32 v[118:119], v[118:119], v[114:115] op_sel:[0,0] op_sel_hi:[1,0]\n"
      "v_accvgpr_write_b32 a154, v118\n"
      "v_accvgpr_write_b32 a155, v119\n"
      "v_accvgpr_read_b32 v118, a156\n"
      "v_accvgpr_read_b32 v119, a157\n"
      "v_pk_mul_f32 v[118:119], v[118:119], v[114:115] op_sel:[0,1] op_sel_hi:[1,1]\n"
      "v_accvgpr_write_b32 a156, v118\n"
      "v_accvgpr_write_b32 a157, v119\n"
      "v_accvgpr_read_b32 v118, a158\n"
      "v_accvgpr_read_b32 v119, a159\n"
      "v_pk_mul_f32 v[118:119], v[118:119], v[114:115] op_sel:[0,1] op_sel_hi:[1,1]\n"
      "v_accvgpr_write_b32 a158, v118\n"
      "v_accvgpr_write_b32 a159, v119\n"
      "v_accvgpr_read_b32 v118, a160\n"
      "v_accvgpr_read_b32 v119, a161\n"
      "v_pk_mul_f32 v[118:119], v[118:119], v[112:113] op_sel:[0,0] op_sel_hi:[1,0]\n"
      "v_accvgpr_write_b32 a160, v118\n"
      "v_accvgpr_write_b32 a161, v119\n"
      "v_accvgpr_read_b32 v118, a162\n"
      "v_accvgpr_read_b32 v119, a163\n"
      "v_pk_mul_f32 v[118:119], v[118:119], v[112:113] op_sel:[0,0] op_sel_hi:[1,0]\n"
      "v_accvgpr_write_b32 a162, v118\n"
      "v_accvgpr_write_b32 a163, v119\n"
      "v_accvgpr_read_b32 v118, a164\n"
      "v_accvgpr_read_b32 v119, a165\n"
      "v_pk_mul_f32 v[118:119], v[118:119], v[112:113] op_sel:[0,1] op_sel_hi:[1,1]\n"
      "v_accvgpr_write_b32 a164, v118\n"
      "v_accvgpr_write_b32 a165, v119\n"
      "v_accvgpr_read_b32 v118, a166\n"
      "v_accvgpr_read_b32 v119, a167\n"
      "v_pk_mul_f32 v[118:119], v[118:119], v[112:113] op_sel:[0,1] op_sel_hi:[1,1]\n"
      "v_accvgpr_write_b32 a166, v118\n"
      "v_accvgpr_write_b32 a167, v119\n"
      "v_accvgpr_read_b32 v118, a168\n"
      "v_accvgpr_read_b32 v119, a169\n"
      "v_pk_mul_f32 v[118:119], v[118:119], v[114:115] op_sel:[0,0] op_sel_hi:[1,0]\n"
      "v_accvgpr_write_b32 a168, v118\n"
      "v_accvgpr_write_b32 a169, v119\n"
      "v_accvgpr_read_b32 v118, a170\n"
      "v_accvgpr_read_b32 v119, a171\n"
      "v_pk_mul_f32 v[118:119], v[118:119], v[114:115] op_sel:[0,0] op_sel_hi:[1,0]\n"
      "v_accvgpr_write_b32 a170, v118\n"
      "v_accvgpr_write_b32 a171, v119\n"
      "v_accvgpr_read_b32 v118, a172\n"
      "v_accvgpr_read_b32 v119, a173\n"
      "v_pk_mul_f32 v[118:119], v[118:119], v[114:115] op_sel:[0,1] op_sel_hi:[1,1]\n"
      "v_accvgpr_write_b32 a172, v118\n"
      "v_accvgpr_write_b32 a173, v119\n"
      "v_accvgpr_read_b32 v118, a174\n"
      "v_accvgpr_read_b32 v119, a175\n"
      "v_pk_mul_f32 v[118:119], v[118:119], v[114:115] op_sel:[0,1] op_sel_hi:[1,1]\n"
      "v_accvgpr_write_b32 a174, v118\n"
      "v_accvgpr_write_b32 a175, v119\n"
      "v_accvgpr_read_b32 v118, a176\n"
      "v_accvgpr_read_b32 v119, a177\n"
      "v_pk_mul_f32 v[118:119], v[118:119], v[112:113] op_sel:[0,0] op_sel_hi:[1,0]\n"
      "v_accvgpr_write_b32 a176, v118\n"
      "v_accvgpr_write_b32 a177, v119\n"
      "v_accvgpr_read_b32 v118, a178\n"
      "v_accvgpr_read_b32 v119, a179\n"
      "v_pk_mul_f32 v[118:119], v[118:119], v[112:113] op_sel:[0,0] op_sel_hi:[1,0]\n"
      "v_accvgpr_write_b32 a178, v118\n"
      "v_accvgpr_write_b32 a179, v119\n"
      "v_accvgpr_read_b32 v118, a180\n"
      "v_accvgpr_read_b32 v119, a181\n"
      "v_pk_mul_f32 v[118:119], v[118:119], v[112:113] op_sel:[0,1] op_sel_hi:[1,1]\n"
      "v_accvgpr_write_b32 a180, v118\n"
      "v_accvgpr_write_b32 a181, v119\n"
      "v_accvgpr_read_b32 v118, a182\n"
      "v_accvgpr_read_b32 v119, a183\n"
      "v_pk_mul_f32 v[118:119], v[118:119], v[112:113] op_sel:[0,1] op_sel_hi:[1,1]\n"
      "v_accvgpr_write_b32 a182, v118\n"
      "v_accvgpr_write_b32 a183, v119\n"
      "v_accvgpr_read_b32 v118, a184\n"
      "v_accvgpr_read_b32 v119, a185\n"
      "v_pk_mul_f32 v[118:119], v[118:119], v[114:115] op_sel:[0,0] op_sel_hi:[1,0]\n"
      "v_accvgpr_write_b32 a184, v118\n"
      "v_accvgpr_write_b32 a185, v119\n"
      "v_accvgpr_read_b32 v118, a186\n"
      "v_accvgpr_read_b32 v119, a187\n"
      "v_pk_mul_f32 v[118:119], v[118:119], v[114:115] op_sel:[0,0] op_sel_hi:[1,0]\n"
      "v_accvgpr_write_b32 a186, v118\n"
      "v_accvgpr_write_b32 a187, v119\n"
      "v_accvgpr_read_b32 v118, a188\n"
      "v_accvgpr_read_b32 v119, a189\n"
      "v_pk_mul_f32 v[118:119], v[118:119], v[114:115] op_sel:[0,1] op_sel_hi:[1,1]\n"
      "v_accvgpr_write_b32 a188, v118\n"
      "v_accvgpr_write_b32 a189, v119\n"
      "v_accvgpr_read_b32 v118, a190\n"
      "v_accvgpr_read_b32 v119, a191\n"
      "v_pk_mul_f32 v[118:119], v[118:119], v[114:115] op_sel:[0,1] op_sel_hi:[1,1]\n"
      "v_accvgpr_write_b32 a190, v118\n"
      "v_accvgpr_write_b32 a191, v119\n"
      "v_mov_b32 v118, v112\n"
      "s_nop 1\n"
      "v_mov_b32_dpp v118, v113 quad_perm:[0,1,2,3] row_mask:0x2 bank_mask:0xf\n"
      "s_nop 1\n"
      "v_mov_b32_dpp v118, v114 quad_perm:[0,1,2,3] row_mask:0x4 bank_mask:0xf\n"
      "s_nop 1\n"
      "v_mov_b32_dpp v118, v115 quad_perm:[0,1,2,3] row_mask:0x8 bank_mask:0xf\n"
      "v_accvgpr_read_b32 v119, a192\n"
      "v_mul_f32 v118, v118, v119\n"
      "v_accvgpr_write_b32 a192, v118\n"
      "v_lshlrev_b32 v118, 16, v208\n"
      "v_and_b32 v119, 0xffff0000, v208\n"
      "v_pk_mul_f32 v[118:119], v[118:119], v[112:113] op_sel:[0,0] op_sel_hi:[1,0]\n"
      "v_cvt_pk_bf16_f32 v208, v118, v119\n"
      "v_lshlrev_b32 v118, 16, v209\n"
      "v_and_b32 v119, 0xffff0000, v209\n"
      "v_pk_mul_f32 v[118:119], v[118:119], v[112:113] op_sel:[0,0] op_sel_hi:[1,0]\n"
      "v_cvt_pk_bf16_f32 v209, v118, v119\n"
      "v_lshlrev_b32 v118, 16, v210\n"
      "v_and_b32 v119, 0xffff0000, v210\n"
      "v_pk_mul_f32 v[118:119], v[118:119], v[112:113] op_sel:[0,0] op_sel_hi:[1,0]\n"
      "v_cvt_pk_bf16_f32 v210, v118, v119\n"
      "v_lshlrev_b32 v118, 16, v211\n"
      "v_and_b32 v119, 0xffff0000, v211\n"
      "v_pk_mul_f32 v[118:119], v[118:119], v[112:113] op_sel:[0,0] op_sel_hi:[1,0]\n"
      "v_cvt_pk_bf16_f32 v211, v118, v119\n"
      "v_lshlrev_b32 v118, 16, v212\n"
      "v_and_b32 v119, 0xffff0000, v212\n"
      "v_pk_mul_f32 v[118:119], v[118:119], v[112:113] op_sel:[0,1] op_sel_hi:[1,1]\n"
      "v_cvt_pk_bf16_f32 v212, v118, v119\n"
      "v_lshlrev_b32 v118, 16, v213\n"
      "v_and_b32 v119, 0xffff0000, v213\n"
      "v_pk_mul_f32 v[118:119], v[118:119], v[112:113] op_sel:[0,1] op_sel_hi:[1,1]\n"
      "v_cvt_pk_bf16_f32 v213, v118, v119\n"
      "v_lshlrev_b32 v118, 16, v214\n"
      "v_and_b32 v119, 0xffff0000, v214\n"
      "v_pk_mul_f32 v[118:119], v[118:119], v[112:113] op_sel:[0,1] op_sel_hi:[1,1]\n"
      "v_cvt_pk_bf16_f32 v214, v118, v119\n"
      "v_lshlrev_b32 v118, 16, v215\n"
      "v_and_b32 v119, 0xffff0000, v215\n"
      "v_pk_mul_f32 v[118:119], v[118:119], v[112:113] op_sel:[0,1] op_sel_hi:[1,1]\n"
      "v_cvt_pk_bf16_f32 v215, v118, v119\n"
      "v_lshlrev_b32 v118, 16, v216\n"
      "v_and_b32 v119, 0xffff0000, v216\n"
      "v_pk_mul_f32 v[118:119], v[118:119], v[114:115] op_sel:[0,0] op_sel_hi:[1,0]\n"
      "v_cvt_pk_bf16_f32 v216, v118, v119\n"
      "v_lshlrev_b32 v118, 16, v217\n"
      "v_and_b32 v119, 0xffff0000, v217\n"
      "v_pk_mul_f32 v[118:119], v[118:119], v[114:115] op_sel:[0,0] op_sel_hi:[1,0]\n"
      "v_cvt_pk_bf16_f32 v217, v118, v119\n"
      "v_lshlrev_b32 v118, 16, v218\n"
      "v_and_b32 v119, 0xffff0000, v218\n"
      "v_pk_mul_f32 v[118:119], v[118:119], v[114:115] op_sel:[0,0] op_sel_hi:[1,0]\n"
      "v_cvt_pk_bf16_f32 v218, v118, v119\n"
      "v_lshlrev_b32 v118, 16, v219\n"
      "v_and_b32 v119, 0xffff0000, v219\n"
      "v_pk_mul_f32 v[118:119], v[118:119], v[114:115] op_sel:[0,0] op_sel_hi:[1,0]\n"
      "v_cvt_pk_bf16_f32 v219, v118, v119\n"
      "v_lshlrev_b32 v118, 16, v220\n"
      "v_and_b32 v119, 0xffff0000, v220\n"
      "v_pk_mul_f32 v[118:119], v[118:119], v[114:115] op_sel:[0,1] op_sel_hi:[1,1]\n"
      "v_cvt_pk_bf16_f32 v220, v118, v119\n"
      "v_lshlrev_b32 v118, 16, v221\n"
      "v_and_b32 v119, 0xffff0000, v221\n"
      "v_pk_mul_f32 v[118:119], v[118:119], v[114:115] op_sel:[0,1] op_sel_hi:[1,1]\n"
      "v_cvt_pk_bf16_f32 v221, v118, v119\n"
      "v_lshlrev_b32 v118, 16, v222\n"
      "v_and_b32 v119, 0xffff0000, v222\n"
      "v_pk_mul_f32 v[118:119], v[118:119], v[114:115] op_sel:[0,1] op_sel_hi:[1,1]\n"
      "v_cvt_pk_bf16_f32 v222, v118, v119\n"
      "v_lshlrev_b32 v118, 16, v223\n"
      "v_and_b32 v119, 0xffff0000, v223\n"
      "v_pk_mul_f32 v[118:119], v[118:119], v[114:115] op_sel:[0,1] op_sel_hi:[1,1]\n"
      "v_cvt_pk_bf16_f32 v223, v118, v119\n"
      "v_lshlrev_b32 v118, 16, v192\n"
      "v_and_b32 v119, 0xffff0000, v192\n"
      "v_pk_mul_f32 v[118:119], v[118:119], v[112:113] op_sel:[0,0] op_sel_hi:[1,0]\n"
      "v_cvt_pk_bf16_f32 v192, v118, v119\n"
      "v_lshlrev_b32 v118, 16, v193\n"
      "v_and_b32 v119, 0xffff0000, v193\n"
      "v_pk_mul_f32 v[118:119], v[118:119], v[112:113] op_sel:[0,0] op_sel_hi:[1,0]\n"
      "v_cvt_pk_bf16_f32 v193, v118, v119\n"
      "v_lshlrev_b32 v118, 16, v194\n"
      "v_and_b32 v119, 0xffff0000, v194\n"
      "v_pk_mul_f32 v[118:119], v[118:119], v[112:113] op_sel:[0,0] op_sel_hi:[1,0]\n"
      "v_cvt_pk_bf16_f32 v194, v118, v119\n"
      "v_lshlrev_b32 v118, 16, v195\n"
      "v_and_b32 v119, 0xffff0000, v195\n"
      "v_pk_mul_f32 v[118:119], v[118:119], v[112:113] op_sel:[0,0] op_sel_hi:[1,0]\n"
      "v_cvt_pk_bf16_f32 v195, v118, v119\n"
      "v_lshlrev_b32 v118, 16, v196\n"
      "v_and_b32 v119, 0xffff0000, v196\n"
      "v_pk_mul_f32 v[118:119], v[118:119], v[112:113] op_sel:[0,1] op_sel_hi:[1,1]\n"
      "v_cvt_pk_bf16_f32 v196, v118, v119\n"
      "v_lshlrev_b32 v118, 16, v197\n"
      "v_and_b32 v119, 0xffff0000, v197\n"
      "v_pk_mul_f32 v[118:119], v[118:119], v[112:113] op_sel:[0,1] op_sel_hi:[1,1]\n"
      "v_cvt_pk_bf16_f32 v197, v118, v119\n"
      "v_lshlrev_b32 v118, 16, v198\n"
      "v_and_b32 v119, 0xffff0000, v198\n"
      "v_pk_mul_f32 v[118:119], v[118:119], v[112:113] op_sel:[0,1] op_sel_hi:[1,1]\n"
      "v_cvt_pk_bf16_f32 v198, v118, v119\n"
      "v_lshlrev_b32 v118, 16, v199\n"
      "v_and_b32 v119, 0xffff0000, v199\n"
      "v_pk_mul_f32 v[118:119], v[118:119], v[112:113] op_sel:[0,1] op_sel_hi:[1,1]\n"
      "v_cvt_pk_bf16_f32 v199, v118, v119\n"
      "v_lshlrev_b32 v118, 16, v200\n"
      "v_and_b32 v119, 0xffff0000, v200\n"
      "v_pk_mul_f32 v[118:119], v[118:119], v[114:115] op_sel:[0,0] op_sel_hi:[1,0]\n"
      "v_cvt_pk_bf16_f32 v200, v118, v119\n"
      "v_lshlrev_b32 v118, 16, v201\n"
      "v_and_b32 v119, 0xffff0000, v201\n"
      "v_pk_mul_f32 v[118:119], v[118:119], v[114:115] op_sel:[0,0] op_sel_hi:[1,0]\n"
      "v_cvt_pk_bf16_f32 v201, v118, v119\n"
      "v_lshlrev_b32 v118, 16, v202\n"
      "v_and_b32 v119, 0xffff0000, v202\n"
      "v_pk_mul_f32 v[118:119], v[118:119], v[114:115] op_sel:[0,0] op_sel_hi:[1,0]\n"
      "v_cvt_pk_bf16_f32 v202, v118, v119\n"
      "v_lshlrev_b32 v118, 16, v203\n"
      "v_and_b32 v119, 0xffff0000, v203\n"
      "v_pk_mul_f32 v[118:119], v[118:119], v[114:115] op_sel:[0,0] op_sel_hi:[1,0]\n"
      "v_cvt_pk_bf16_f32 v203, v118, v119\n"
      "v_lshlrev_b32 v118, 16, v204\n"
      "v_and_b32 v119, 0xffff0000, v204\n"
      "v_pk_mul_f32 v[118:119], v[118:119], v[114:115] op_sel:[0,1] op_sel_hi:[1,1]\n"
      "v_cvt_pk_bf16_f32 v204, v118, v119\n"
      "v_lshlrev_b32 v118, 16, v205\n"
      "v_and_b32 v119, 0xffff0000, v205\n"
      "v_pk_mul_f32 v[118:119], v[118:119], v[114:115] op_sel:[0,1] op_sel_hi:[1,1]\n"
      "v_cvt_pk_bf16_f32 v205, v118, v119\n"
      "v_lshlrev_b32 v118, 16, v206\n"
      "v_and_b32 v119, 0xffff0000, v206\n"
      "v_pk_mul_f32 v[118:119], v[118:119], v[114:115] op_sel:[0,1] op_sel_hi:[1,1]\n"
      "v_cvt_pk_bf16_f32 v206, v118, v119\n"
      "v_lshlrev_b32 v118, 16, v207\n"
      "v_and_b32 v119, 0xffff0000, v207\n"
      "v_pk_mul_f32 v[118:119], v[118:119], v[114:115] op_sel:[0,1] op_sel_hi:[1,1]\n"
      "v_cvt_pk_bf16_f32 v207, v118, v119\n"
      "v_lshlrev_b32 v118, 16, v0\n"
      "v_and_b32 v119, 0xffff0000, v0\n"
      "v_pk_mul_f32 v[118:119], v[118:119], v[112:113] op_sel:[0,0] op_sel_hi:[1,0]\n"
      "v_cvt_pk_bf16_f32 v0, v118, v119\n"
      "v_lshlrev_b32 v118, 16, v1\n"
      "v_and_b32 v119, 0xffff0000, v1\n"
      "v_pk_mul_f32 v[118:119], v[118:119], v[112:113] op_sel:[0,0] op_sel_hi:[1,0]\n"
      "v_cvt_pk_bf16_f32 v1, v118, v119\n"
      "v_lshlrev_b32 v118, 16, v2\n"
      "v_and_b32 v119, 0xffff0000, v2\n"
      "v_pk_mul_f32 v[118:119], v[118:119], v[112:113] op_sel:[0,0] op_sel_hi:[1,0]\n"
      "v_cvt_pk_bf16_f32 v2, v118, v119\n"
      "v_lshlrev_b32 v118, 16, v3\n"
      "v_and_b32 v119, 0xffff0000, v3\n"
      "v_pk_mul_f32 v[118:119], v[118:119], v[112:113] op_sel:[0,0] op_sel_hi:[1,0]\n"
      "v_cvt_pk_bf16_f32 v3, v118, v119\n"
      "v_lshlrev_b32 v118, 16, v4\n"
      "v_and_b32 v119, 0xffff0000, v4\n"
      "v_pk_mul_f32 v[118:119], v[118:119], v[112:113] op_sel:[0,1] op_sel_hi:[1,1]\n"
      "v_cvt_pk_bf16_f32 v4, v118, v119\n"
      "v_lshlrev_b32 v118, 16, v5\n"
      "v_and_b32 v119, 0xffff0000, v5\n"
      "v_pk_mul_f32 v[118:119], v[118:119], v[112:113] op_sel:[0,1] op_sel_hi:[1,1]\n"
      "v_cvt_pk_bf16_f32 v5, v118, v119\n"
      "v_lshlrev_b32 v118, 16, v6\n"
      "v_and_b32 v119, 0xffff0000, v6\n"
      "v_pk_mul_f32 v[118:119], v[118:119], v[112:113] op_sel:[0,1] op_sel_hi:[1,1]\n"
      "v_cvt_pk_bf16_f32 v6, v118, v119\n"
      "v_lshlrev_b32 v118, 16, v7\n"
      "v_and_b32 v119, 0xffff0000, v7\n"
      "v_pk_mul_f32 v[118:119], v[118:119], v[112:113] op_sel:[0,1] op_sel_hi:[1,1]\n"
      "v_cvt_pk_bf16_f32 v7, v118, v119\n"
      "v_lshlrev_b32 v118, 16, v8\n"
      "v_and_b32 v119, 0xffff0000, v8\n"
      "v_pk_mul_f32 v[118:119], v[118:119], v[114:115] op_sel:[0,0] op_sel_hi:[1,0]\n"
      "v_cvt_pk_bf16_f32 v8, v118, v119\n"
      "v_lshlrev_b32 v118, 16, v9\n"
      "v_and_b32 v119, 0xffff0000, v9\n"
      "v_pk_mul_f32 v[118:119], v[118:119], v[114:115] op_sel:[0,0] op_sel_hi:[1,0]\n"
      "v_cvt_pk_bf16_f32 v9, v118, v119\n"
      "v_lshlrev_b32 v118, 16, v10\n"
      "v_and_b32 v119, 0xffff0000, v10\n"
      "v_pk_mul_f32 v[118:119], v[118:119], v[114:115] op_sel:[0,0] op_sel_hi:[1,0]\n"
      "v_cvt_pk_bf16_f32 v10, v118, v119\n"
      "v_lshlrev_b32 v118, 16, v11\n"
      "v_and_b32 v119, 0xffff0000, v11\n"
      "v_pk_mul_f32 v[118:119], v[118:119], v[114:115] op_sel:[0,0] op_sel_hi:[1,0]\n"
      "v_cvt_pk_bf16_f32 v11, v118, v119\n"
      "v_lshlrev_b32 v118, 16, v12\n"
      "v_and_b32 v119, 0xffff0000, v12\n"
      "v_pk_mul_f32 v[118:119], v[118:119], v[114:115] op_sel:[0,1] op_sel_hi:[1,1]\n"
      "v_cvt_pk_bf16_f32 v12, v118, v119\n"
      "v_lshlrev_b32 v118, 16, v13\n"
      "v_and_b32 v119, 0xffff0000, v13\n"
      "v_pk_mul_f32 v[118:119], v[118:119], v[114:115] op_sel:[0,1] op_sel_hi:[1,1]\n"
      "v_cvt_pk_bf16_f32 v13, v118, v119\n"
      "v_lshlrev_b32 v118, 16, v14\n"
      "v_and_b32 v119, 0xffff0000, v14\n"
      "v_pk_mul_f32 v[118:119], v[118:119], v[114:115] op_sel:[0,1] op_sel_hi:[1,1]\n"
      "v_cvt_pk_bf16_f32 v14, v118, v119\n"
      "v_lshlrev_b32 v118, 16, v15\n"
      "v_and_b32 v119, 0xffff0000, v15\n"
      "v_pk_mul_f32 v[118:119], v[118:119], v[114:115] op_sel:[0,1] op_sel_hi:[1,1]\n"
      "v_cvt_pk_bf16_f32 v15, v118, v119\n"
      "v_lshlrev_b32 v118, 16, v16\n"
      "v_and_b32 v119, 0xffff0000, v16\n"
      "v_pk_mul_f32 v[118:119], v[118:119], v[112:113] op_sel:[0,0] op_sel_hi:[1,0]\n"
      "v_cvt_pk_bf16_f32 v16, v118, v119\n"
      "v_lshlrev_b32 v118, 16, v17\n"
      "v_and_b32 v119, 0xffff0000, v17\n"
      "v_pk_mul_f32 v[118:119], v[118:119], v[112:113] op_sel:[0,0] op_sel_hi:[1,0]\n"
      "v_cvt_pk_bf16_f32 v17, v118, v119\n"
      "v_lshlrev_b32 v118, 16, v18\n"
      "v_and_b32 v119, 0xffff0000, v18\n"
      "v_pk_mul_f32 v[118:119], v[118:119], v[112:113] op_sel:[0,0] op_sel_hi:[1,0]\n"
      "v_cvt_pk_bf16_f32 v18, v118, v119\n"
      "v_lshlrev_b32 v118, 16, v19\n"
      "v_and_b32 v119, 0xffff0000, v19\n"
      "v_pk_mul_f32 v[118:119], v[118:119], v[112:113] op_sel:[0,0] op_sel_hi:[1,0]\n"
      "v_cvt_pk_bf16_f32 v19, v118, v119\n"
      "v_lshlrev_b32 v118, 16, v20\n"
      "v_and_b32 v119, 0xffff0000, v20\n"
      "v_pk_mul_f32 v[118:119], v[118:119], v[112:113] op_sel:[0,1] op_sel_hi:[1,1]\n"
      "v_cvt_pk_bf16_f32 v20, v118, v119\n"
      "v_lshlrev_b32 v118, 16, v21\n"
      "v_and_b32 v119, 0xffff0000, v21\n"
      "v_pk_mul_f32 v[118:119], v[118:119], v[112:113] op_sel:[0,1] op_sel_hi:[1,1]\n"
      "v_cvt_pk_bf16_f32 v21, v118, v119\n"
      "v_lshlrev_b32 v118, 16, v22\n"
      "v_and_b32 v119, 0xffff0000, v22\n"
      "v_pk_mul_f32 v[118:119], v[118:119], v[112:113] op_sel:[0,1] op_sel_hi:[1,1]\n"
      "v_cvt_pk_bf16_f32 v22, v118, v119\n"
      "v_lshlrev_b32 v118, 16, v23\n"
      "v_and_b32 v119, 0xffff0000, v23\n"
      "v_pk_mul_f32 v[118:119], v[118:119], v[112:113] op_sel:[0,1] op_sel_hi:[1,1]\n"
      "v_cvt_pk_bf16_f32 v23, v118, v119\n"
      "v_lshlrev_b32 v118, 16, v24\n"
      "v_and_b32 v119, 0xffff0000, v24\n"
      "v_pk_mul_f32 v[118:119], v[118:119], v[114:115] op_sel:[0,0] op_sel_hi:[1,0]\n"
      "v_cvt_pk_bf16_f32 v24, v118, v119\n"
      "v_lshlrev_b32 v118, 16, v25\n"
      "v_and_b32 v119, 0xffff0000, v25\n"
      "v_pk_mul_f32 v[118:119], v[118:119], v[114:115] op_sel:[0,0] op_sel_hi:[1,0]\n"
      "v_cvt_pk_bf16_f32 v25, v118, v119\n"
      "v_lshlrev_b32 v118, 16, v26\n"
      "v_and_b32 v119, 0xffff0000, v26\n"
      "v_pk_mul_f32 v[118:119], v[118:119], v[114:115] op_sel:[0,0] op_sel_hi:[1,0]\n"
      "v_cvt_pk_bf16_f32 v26, v118, v119\n"
      "v_lshlrev_b32 v118, 16, v27\n"
      "v_and_b32 v119, 0xffff0000, v27\n"
      "v_pk_mul_f32 v[118:119], v[118:119], v[114:115] op_sel:[0,0] op_sel_hi:[1,0]\n"
      "v_cvt_pk_bf16_f32 v27, v118, v119\n"
      "v_lshlrev_b32 v118, 16, v28\n"
      "v_and_b32 v119, 0xffff0000, v28\n"
      "v_pk_mul_f32 v[118:119], v[118:119], v[114:115] op_sel:[0,1] op_sel_hi:[1,1]\n"
      "v_cvt_pk_bf16_f32 v28, v118, v119\n"
      "v_lshlrev_b32 v118, 16, v29\n"
      "v_and_b32 v119, 0xffff0000, v29\n"
      "v_pk_mul_f32 v[118:119], v[118:119], v[114:115] op_sel:[0,1] op_sel_hi:[1,1]\n"
      "v_cvt_pk_bf16_f32 v29, v118, v119\n"
      "v_lshlrev_b32 v118, 16, v30\n"
      "v_and_b32 v119, 0xffff0000, v30\n"
      "v_pk_mul_f32 v[118:119], v[118:119], v[114:115] op_sel:[0,1] op_sel_hi:[1,1]\n"
      "v_cvt_pk_bf16_f32 v30, v118, v119\n"
      "v_lshlrev_b32 v118, 16, v31\n"
      "v_and_b32 v119, 0xffff0000, v31\n"
      "v_pk_mul_f32 v[118:119], v[118:119], v[114:115] op_sel:[0,1] op_sel_hi:[1,1]\n"
      "v_cvt_pk_bf16_f32 v31, v118, v119\n"
      "v_lshlrev_b32 v118, 16, v96\n"
      "v_and_b32 v119, 0xffff0000, v96\n"
      "v_pk_mul_f32 v[118:119], v[118:119], v[112:113] op_sel:[0,0] op_sel_hi:[1,0]\n"
      "v_cvt_pk_bf16_f32 v96, v118, v119\n"
      "v_lshlrev_b32 v118, 16, v97\n"
      "v_and_b32 v119, 0xffff0000, v97\n"
      "v_pk_mul_f32 v[118:119], v[118:119], v[112:113] op_sel:[0,0] op_sel_hi:[1,0]\n"
      "v_cvt_pk_bf16_f32 v97, v118, v119\n"
      "v_lshlrev_b32 v118, 16, v98\n"
      "v_and_b32 v119, 0xffff0000, v98\n"
      "v_pk_mul_f32 v[118:119], v[118:119], v[112:113] op_sel:[0,0] op_sel_hi:[1,0]\n"
      "v_cvt_pk_bf16_f32 v98, v118, v119\n"
      "v_lshlrev_b32 v118, 16, v99\n"
      "v_and_b32 v119, 0xffff0000, v99\n"
      "v_pk_mul_f32 v[118:119], v[118:119], v[112:113] op_sel:[0,0] op_sel_hi:[1,0]\n"
      "v_cvt_pk_bf16_f32 v99, v118, v119\n"
      "v_lshlrev_b32 v118, 16, v100\n"
      "v_and_b32 v119, 0xffff0000, v100\n"
      "v_pk_mul_f32 v[118:119], v[118:119], v[112:113] op_sel:[0,1] op_sel_hi:[1,1]\n"
      "v_cvt_pk_bf16_f32 v100, v118, v119\n"
      "v_lshlrev_b32 v118, 16, v101\n"
      "v_and_b32 v119, 0xffff0000, v101\n"
      "v_pk_mul_f32 v[118:119], v[118:119], v[112:113] op_sel:[0,1] op_sel_hi:[1,1]\n"
      "v_cvt_pk_bf16_f32 v101, v118, v119\n"
      "v_lshlrev_b32 v118, 16, v102\n"
      "v_and_b32 v119, 0xffff0000, v102\n"
      "v_pk_mul_f32 v[118:119], v[118:119], v[112:113] op_sel:[0,1] op_sel_hi:[1,1]\n"
      "v_cvt_pk_bf16_f32 v102, v118, v119\n"
      "v_lshlrev_b32 v118, 16, v103\n"
      "v_and_b32 v119, 0xffff0000, v103\n"
      "v_pk_mul_f32 v[118:119], v[118:119], v[112:113] op_sel:[0,1] op_sel_hi:[1,1]\n"
      "v_cvt_pk_bf16_f32 v103, v118, v119\n"
      "v_lshlrev_b32 v118, 16, v104\n"
      "v_and_b32 v119, 0xffff0000, v104\n"
      "v_pk_mul_f32 v[118:119], v[118:119], v[114:115] op_sel:[0,0] op_sel_hi:[1,0]\n"
      "v_cvt_pk_bf16_f32 v104, v118, v119\n"
      "v_lshlrev_b32 v118, 16, v105\n"
      "v_and_b32 v119, 0xffff0000, v105\n"
      "v_pk_mul_f32 v[118:119], v[118:119], v[114:115] op_sel:[0,0] op_sel_hi:[1,0]\n"
      "v_cvt_pk_bf16_f32 v105, v118, v119\n"
      "v_lshlrev_b32 v118, 16, v106\n"
      "v_and_b32 v119, 0xffff0000, v106\n"
      "v_pk_mul_f32 v[118:119], v[118:119], v[114:115] op_sel:[0,0] op_sel_hi:[1,0]\n"
      "v_cvt_pk_bf16_f32 v106, v118, v119\n"
      "v_lshlrev_b32 v118, 16, v107\n"
      "v_and_b32 v119, 0xffff0000, v107\n"
      "v_pk_mul_f32 v[118:119], v[118:119], v[114:115] op_sel:[0,0] op_sel_hi:[1,0]\n"
      "v_cvt_pk_bf16_f32 v107, v118, v119\n"
      "v_lshlrev_b32 v118, 16, v108\n"
      "v_and_b32 v119, 0xffff0000, v108\n"
      "v_pk_mul_f32 v[118:119], v[118:119], v[114:115] op_sel:[0,1] op_sel_hi:[1,1]\n"
      "v_cvt_pk_bf16_f32 v108, v118, v119\n"
      "v_lshlrev_b32 v118, 16, v109\n"
      "v_and_b32 v119, 0xffff0000, v109\n"
      "v_pk_mul_f32 v[118:119], v[118:119], v[114:115] op_sel:[0,1] op_sel_hi:[1,1]\n"
      "v_cvt_pk_bf16_f32 v109, v118, v119\n"
      "v_lshlrev_b32 v118, 16, v110\n"
      "v_and_b32 v119, 0xffff0000, v110\n"
      "v_pk_mul_f32 v[118:119], v[118:119], v[114:115] op_sel:[0,1] op_sel_hi:[1,1]\n"
      "v_cvt_pk_bf16_f32 v110, v118, v119\n"
      "v_lshlrev_b32 v118, 16, v111\n"
      "v_and_b32 v119, 0xffff0000, v111\n"
      "v_pk_mul_f32 v[118:119], v[118:119], v[114:115] op_sel:[0,1] op_sel_hi:[1,1]\n"
      "v_cvt_pk_bf16_f32 v111, v118, v119\n"
      "s_setpc_b64 s[84:85]\n"
      "MIS_%=:\n"
      "s_waitcnt lgkmcnt(0)\n"
      "v_sub_f32 v148, v153, v117\n"
      "v_min_f32 v149, 0, -v148\n"
      "v_min_f32 v148, 0, v148\n"
      "v_exp_f32 v149, v149\n"
      "v_exp_f32 v148, v148\n"
      "v_min_f32 v117, v117, v153\n"
      "v_mov_b32 v112, v148\n"
      "v_mov_b32 v228, v149\n"
      "v_mov_b32 v114, v112\n"
      "v_mov_b32 v230, v228\n"
      "s_nop 0\n"
      "v_permlane32_swap_b32 v112, v114\n"
      "v_permlane32_swap_b32 v228, v230\n"
      "v_mov_b32 v113, v112\n"
      "v_mov_b32 v229, v228\n"
      "v_mov_b32 v115, v114\n"
      "v_mov_b32 v231, v230\n"
      "v_permlane16_swap_b32 v112, v113\n"
      "v_permlane16_swap_b32 v228, v229\n"
      "v_permlane16_swap_b32 v114, v115\n"
      "v_permlane16_swap_b32 v230, v231\n"
      "s_call_b64 s[84:85], SCALE_%=\n"
      "v_pk_mul_f32 v[160:161], v[160:161], v[228:229] op_sel:[0,0] op_sel_hi:[1,0]\n"
      "v_pk_mul_f32 v[162:163], v[162:163], v[228:229] op_sel:[0,0] op_sel_hi:[1,0]\n"
      "v_pk_mul_f32 v[164:165], v[164:165], v[228:229] op_sel:[0,1] op_sel_hi:[1,1]\n"
      "v_pk_mul_f32 v[166:167], v[166:167], v[228:229] op_sel:[0,1] op_sel_hi:[1,1]\n"
      "v_pk_mul_f32 v[168:169], v[168:169], v[230:231] op_sel:[0,0] op_sel_hi:[1,0]\n"
      "v_pk_mul_f32 v[170:171], v[170:171], v[230:231] op_sel:[0,0] op_sel_hi:[1,0]\n"
      "v_pk_mul_f32 v[172:173], v[172:173], v[230:231] op_sel:[0,1] op_sel_hi:[1,1]\n"
      "v_pk_mul_f32 v[174:175], v[174:175], v[230:231] op_sel:[0,1] op_sel_hi:[1,1]\n"
      "v_pk_mul_f32 v[176:177], v[176:177], v[228:229] op_sel:[0,0] op_sel_hi:[1,0]\n"
      "v_pk_mul_f32 v[178:179], v[178:179], v[228:229] op_sel:[0,0] op_sel_hi:[1,0]\n"
      "v_pk_mul_f32 v[180:181], v[180:181], v[228:229] op_sel:[0,1] op_sel_hi:[1,1]\n"
      "v_pk_mul_f32 v[182:183], v[182:183], v[228:229] op_sel:[0,1] op_sel_hi:[1,1]\n"
      "v_pk_mul_f32 v[184:185], v[184:185], v[230:231] op_sel:[0,0] op_sel_hi:[1,0]\n"
      "v_pk_mul_f32 v[186:187], v[186:187], v[230:231] op_sel:[0,0] op_sel_hi:[1,0]\n"
      "v_pk_mul_f32 v[188:189], v[188:189], v[230:231] op_sel:[0,1] op_sel_hi:[1,1]\n"
      "v_pk_mul_f32 v[190:191], v[190:191], v[230:231] op_sel:[0,1] op_sel_hi:[1,1]\n"
      "v_mov_b32 v112, v228\n"
      "v_mov_b32 v113, v229\n"
      "v_mov_b32 v114, v230\n"
      "v_mov_b32 v115, v231\n"
      "v_accvgpr_read_b32 v128, a64\n"
      "v_accvgpr_read_b32 v129, a65\n"
      "v_accvgpr_read_b32 v130, a66\n"
      "v_accvgpr_read_b32 v131, a67\n"
      "v_accvgpr_read_b32 v132, a68\n"
      "v_accvgpr_read_b32 v133, a69\n"
      "v_accvgpr_read_b32 v134, a70\n"
      "v_accvgpr_read_b32 v135, a71\n"
      "v_accvgpr_read_b32 v136, a72\n"
      "v_accvgpr_read_b32 v137, a73\n"
      "v_accvgpr_read_b32 v138, a74\n"
      "v_accvgpr_read_b32 v139, a75\n"
      "v_accvgpr_read_b32 v140, a76\n"
      "v_accvgpr_read_b32 v141, a77\n"
      "v_accvgpr_read_b32 v142, a78\n"
      "v_accvgpr_read_b32 v143, a79\n"
      "v_lshlrev_b32 v118, 16, v128\n"
      "v_and_b32 v119, 0xffff0000, v128\n"
      "v_pk_mul_f32 v[118:119], v[118:119], v[112:113] op_sel:[0,0] op_sel_hi:[1,0]\n"
      "v_cvt_pk_bf16_f32 v128, v118, v119\n"
      "v_lshlrev_b32 v118, 16, v129\n"
      "v_and_b32 v119, 0xffff0000, v129\n"
      "v_pk_mul_f32 v[118:119], v[118:119], v[112:113] op_sel:[0,0] op_sel_hi:[1,0]\n"
      "v_cvt_pk_bf16_f32 v129, v118, v119\n"
      "v_lshlrev_b32 v118, 16, v130\n"
      "v_and_b32 v119, 0xffff0000, v130\n"
      "v_pk_mul_f32 v[118:119], v[118:119], v[112:113] op_sel:[0,0] op_sel_hi:[1,0]\n"
      "v_cvt_pk_bf16_f32 v130, v118, v119\n"
      "v_lshlrev_b32 v118, 16, v131\n"
      "v_and_b32 v119, 0xffff0000, v131\n"
      "v_pk_mul_f32 v[118:119], v[118:119], v[112:113] op_sel:[0,0] op_sel_hi:[1,0]\n"
      "v_cvt_pk_bf16_f32 v131, v118, v119\n"
      "v_lshlrev_b32 v118, 16, v132\n"
      "v_and_b32 v119, 0xffff0000, v132\n"
      "v_pk_mul_f32 v[118:119], v[118:119], v[112:113] op_sel:[0,1] op_sel_hi:[1,1]\n"
      "v_cvt_pk_bf16_f32 v132, v118, v119\n"
      "v_lshlrev_b32 v118, 16, v133\n"
      "v_and_b32 v119, 0xffff0000, v133\n"
      "v_pk_mul_f32 v[118:119], v[118:119], v[112:113] op_sel:[0,1] op_sel_hi:[1,1]\n"
      "v_cvt_pk_bf16_f32 v133, v118, v119\n"
      "v_lshlrev_b32 v118, 16, v134\n"
      "v_and_b32 v119, 0xffff0000, v134\n"
      "v_pk_mul_f32 v[118:119], v[118:119], v[112:113] op_sel:[0,1] op_sel_hi:[1,1]\n"
      "v_cvt_pk_bf16_f32 v134, v118, v119\n"
      "v_lshlrev_b32 v118, 16, v135\n"
      "v_and_b32 v119, 0xffff0000, v135\n"
      "v_pk_mul_f32 v[118:119], v[118:119], v[112:113] op_sel:[0,1] op_sel_hi:[1,1]\n"
      "v_cvt_pk_bf16_f32 v135, v118, v119\n"
      "v_lshlrev_b32 v118, 16, v136\n"
      "v_and_b32 v119, 0xffff0000, v136\n"
      "v_pk_mul_f32 v[118:119], v[118:119], v[114:115] op_sel:[0,0] op_sel_hi:[1,0]\n"
      "v_cvt_pk_bf16_f32 v136, v118, v119\n"
      "v_lshlrev_b32 v118, 16, v137\n"
      "v_and_b32 v119, 0xffff0000, v137\n"
      "v_pk_mul_f32 v[118:119], v[118:119], v[114:115] op_sel:[0,0] op_sel_hi:[1,0]\n"
      "v_cvt_pk_bf16_f32 v137, v118, v119\n"
      "v_lshlrev_b32 v118, 16, v138\n"
      "v_and_b32 v119, 0xffff0000, v138\n"
      "v_pk_mul_f32 v[118:119], v[118:119], v[114:115] op_sel:[0,0] op_sel_hi:[1,0]\n"
      "v_cvt_pk_bf16_f32 v138, v118, v119\n"
      "v_lshlrev_b32 v118, 16, v139\n"
      "v_and_b32 v119, 0xffff0000, v139\n"
      "v_pk_mul_f32 v[118:119], v[118:119], v[114:115] op_sel:[0,0] op_sel_hi:[1,0]\n"
      "v_cvt_pk_bf16_f32 v139, v118, v119\n"
      "v_lshlrev_b32 v118, 16, v140\n"
      "v_and_b32 v119, 0xffff0000, v140\n"
      "v_pk_mul_f32 v[118:119], v[118:119], v[114:115] op_sel:[0,1] op_sel_hi:[1,1]\n"
      "v_cvt_pk_bf16_f32 v140, v118, v119\n"
      "v_lshlrev_b32 v118, 16, v141\n"
      "v_and_b32 v119, 0xffff0000, v141\n"
      "v_pk_mul_f32 v[118:119], v[118:119], v[114:115] op_sel:[0,1] op_sel_hi:[1,1]\n"
      "v_cvt_pk_bf16_f32 v141, v118, v119\n"
      "v_lshlrev_b32 v118, 16, v142\n"
      "v_and_b32 v119, 0xffff0000, v142\n"
      "v_pk_mul_f32 v[118:119], v[118:119], v[114:115] op_sel:[0,1] op_sel_hi:[1,1]\n"
      "v_cvt_pk_bf16_f32 v142, v118, v119\n"
      "v_lshlrev_b32 v118, 16, v143\n"
      "v_and_b32 v119, 0xffff0000, v143\n"
      "v_pk_mul_f32 v[118:119], v[118:119], v[114:115] op_sel:[0,1] op_sel_hi:[1,1]\n"
      "v_cvt_pk_bf16_f32 v143, v118, v119\n"
      "v_accvgpr_write_b32 a64, v128\n"
      "v_accvgpr_write_b32 a65, v129\n"
      "v_accvgpr_write_b32 a66, v130\n"
      "v_accvgpr_write_b32 a67, v131\n"
      "v_accvgpr_write_b32 a68, v132\n"
      "v_accvgpr_write_b32 a69, v133\n"
      "v_accvgpr_write_b32 a70, v134\n"
      "v_accvgpr_write_b32 a71, v135\n"
      "v_accvgpr_write_b32 a72, v136\n"
      "v_accvgpr_write_b32 a73, v137\n"
      "v_accvgpr_write_b32 a74, v138\n"
      "v_accvgpr_write_b32 a75, v139\n"
      "v_accvgpr_write_b32 a76, v140\n"
      "v_accvgpr_write_b32 a77, v141\n"
      "v_accvgpr_write_b32 a78, v142\n"
      "v_accvgpr_write_b32 a79, v143\n"
      "s_branch MR_%=\n"
      "FIX_%=:\n"
      "s_nop 7\n"
      "v_sub_f32 v224, v81, v117\n"
      "v_min_f32 v112, 0, -v224\n"
      "v_min_f32 v224, 0, v224\n"
      "v_exp_f32 v112, v112\n"
      "v_exp_f32 v224, v224\n"
      "s_nop 0\n"
      "v_mul_f32 v144, v144, v224\n"
      "v_mul_f32 v80, v80, v112\n"
      "v_mov_b32 v226, v224\n"
      "v_mov_b32 v114, v112\n"
      "s_nop 0\n"
      "v_permlane32_swap_b32 v224, v226\n"
      "v_permlane32_swap_b32 v112, v114\n"
      "v_mov_b32 v225, v224\n"
      "v_mov_b32 v113, v112\n"
      "v_mov_b32 v227, v226\n"
      "v_mov_b32 v115, v114\n"
      "v_permlane16_swap_b32 v224, v225\n"
      "v_permlane16_swap_b32 v112, v113\n"
      "v_permlane16_swap_b32 v226, v227\n"
      "v_permlane16_swap_b32 v114, v115\n"
      "v_pk_mul_f32 v[32:33], v[32:33], v[224:225] op_sel:[0,0] op_sel_hi:[1,0]\n"
      "v_pk_mul_f32 v[34:35], v[34:35], v[224:225] op_sel:[0,0] op_sel_hi:[1,0]\n"
      "v_pk_mul_f32 v[36:37], v[36:37], v[224:225] op_sel:[0,1] op_sel_hi:[1,1]\n"
      "v_pk_mul_f32 v[38:39], v[38:39], v[224:225] op_sel:[0,1] op_sel_hi:[1,1]\n"
      "v_pk_mul_f32 v[40:41], v[40:41], v[226:227] op_sel:[0,0] op_sel_hi:[1,0]\n"
      "v_pk_mul_f32 v[42:43], v[42:43], v[226:227] op_sel:[0,0] op_sel_hi:[1,0]\n"
      "v_pk_mul_f32 v[44:45], v[44:45], v[226:227] op_sel:[0,1] op_sel_hi:[1,1]\n"
      "v_pk_mul_f32 v[46:47], v[46:47], v[226:227] op_sel:[0,1] op_sel_hi:[1,1]\n"
      "v_pk_mul_f32 v[48:49], v[48:49], v[224:225] op_sel:[0,0] op_sel_hi:[1,0]\n"
      "v_pk_mul_f32 v[50:51], v[50:51], v[224:225] op_sel:[0,0] op_sel_hi:[1,0]\n"
      "v_pk_mul_f32 v[52:53], v[52:53], v[224:225] op_sel:[0,1] op_sel_hi:[1,1]\n"
      "v_pk_mul_f32 v[54:55], v[54:55], v[224:225] op_sel:[0,1] op_sel_hi:[1,1]\n"
      "v_pk_mul_f32 v[56:57], v[56:57], v[226:227] op_sel:[0,0] op_sel_hi:[1,0]\n"
      "v_pk_mul_f32 v[58:59], v[58:59], v[226:227] op_sel:[0,0] op_sel_hi:[1,0]\n"
      "v_pk_mul_f32 v[60:61], v[60:61], v[226:227] op_sel:[0,1] op_sel_hi:[1,1]\n"
      "v_pk_mul_f32 v[62:63], v[62:63], v[226:227] op_sel:[0,1] op_sel_hi:[1,1]\n"
      "v_lshlrev_b32 v118, 16, v64\n"
      "v_and_b32 v119, 0xffff0000, v64\n"
      "v_pk_mul_f32 v[118:119], v[118:119], v[112:113] op_sel:[0,0] op_sel_hi:[1,0]\n"
      "v_cvt_pk_bf16_f32 v64, v118, v119\n"
      "v_lshlrev_b32 v118, 16, v65\n"
      "v_and_b32 v119, 0xffff0000, v65\n"
      "v_pk_mul_f32 v[118:119], v[118:119], v[112:113] op_sel:[0,0] op_sel_hi:[1,0]\n"
      "v_cvt_pk_bf16_f32 v65, v118, v119\n"
      "v_lshlrev_b32 v118, 16, v66\n"
      "v_and_b32 v119, 0xffff0000, v66\n"
      "v_pk_mul_f32 v[118:119], v[118:119], v[112:113] op_sel:[0,0] op_sel_hi:[1,0]\n"
      "v_cvt_pk_bf16_f32 v66, v118, v119\n"
      "v_lshlrev_b32 v118, 16, v67\n"
      "v_and_b32 v119, 0xffff0000, v67\n"
      "v_pk_mul_f32 v[118:119], v[118:119], v[112:113] op_sel:[0,0] op_sel_hi:[1,0]\n"
      "v_cvt_pk_bf16_f32 v67, v118, v119\n"
      "v_lshlrev_b32 v118, 16, v68\n"
      "v_and_b32 v119, 0xffff0000, v68\n"
      "v_pk_mul_f32 v[118:119], v[118:119], v[112:113] op_sel:[0,1] op_sel_hi:[1,1]\n"
      "v_cvt_pk_bf16_f32 v68, v118, v119\n"
      "v_lshlrev_b32 v118, 16, v69\n"
      "v_and_b32 v119, 0xffff0000, v69\n"
      "v_pk_mul_f32 v[118:119], v[118:119], v[112:113] op_sel:[0,1] op_sel_hi:[1,1]\n"
      "v_cvt_pk_bf16_f32 v69, v118, v119\n"
      "v_lshlrev_b32 v118, 16, v70\n"
      "v_and_b32 v119, 0xffff0000, v70\n"
      "v_pk_mul_f32 v[118:119], v[118:119], v[112:113] op_sel:[0,1] op_sel_hi:[1,1]\n"
      "v_cvt_pk_bf16_f32 v70, v118, v119\n"
      "v_lshlrev_b32 v118, 16, v71\n"
      "v_and_b32 v119, 0xffff0000, v71\n"
      "v_pk_mul_f32 v[118:119], v[118:119], v[112:113] op_sel:[0,1] op_sel_hi:[1,1]\n"
      "v_cvt_pk_bf16_f32 v71, v118, v119\n"
      "v_lshlrev_b32 v118, 16, v72\n"
      "v_and_b32 v119, 0xffff0000, v72\n"
      "v_pk_mul_f32 v[118:119], v[118:119], v[114:115] op_sel:[0,0] op_sel_hi:[1,0]\n"
      "v_cvt_pk_bf16_f32 v72, v118, v119\n"
      "v_lshlrev_b32 v118, 16, v73\n"
      "v_and_b32 v119, 0xffff0000, v73\n"
      "v_pk_mul_f32 v[118:119], v[118:119], v[114:115] op_sel:[0,0] op_sel_hi:[1,0]\n"
      "v_cvt_pk_bf16_f32 v73, v118, v119\n"
      "v_lshlrev_b32 v118, 16, v74\n"
      "v_and_b32 v119, 0xffff0000, v74\n"
      "v_pk_mul_f32 v[118:119], v[118:119], v[114:115] op_sel:[0,0] op_sel_hi:[1,0]\n"
      "v_cvt_pk_bf16_f32 v74, v118, v119\n"
      "v_lshlrev_b32 v118, 16, v75\n"
      "v_and_b32 v119, 0xffff0000, v75\n"
      "v_pk_mul_f32 v[118:119], v[118:119], v[114:115] op_sel:[0,0] op_sel_hi:[1,0]\n"
      "v_cvt_pk_bf16_f32 v75, v118, v119\n"
      "v_lshlrev_b32 v118, 16, v76\n"
      "v_and_b32 v119, 0xffff0000, v76\n"
      "v_pk_mul_f32 v[118:119], v[118:119], v[114:115] op_sel:[0,1] op_sel_hi:[1,1]\n"
      "v_cvt_pk_bf16_f32 v76, v118, v119\n"
      "v_lshlrev_b32 v118, 16, v77\n"
      "v_and_b32 v119, 0xffff0000, v77\n"
      "v_pk_mul_f32 v[118:119], v[118:119], v[114:115] op_sel:[0,1] op_sel_hi:[1,1]\n"
      "v_cvt_pk_bf16_f32 v77, v118, v119\n"
      "v_lshlrev_b32 v118, 16, v78\n"
      "v_and_b32 v119, 0xffff0000, v78\n"
      "v_pk_mul_f32 v[118:119], v[118:119], v[114:115] op_sel:[0,1] op_sel_hi:[1,1]\n"
      "v_cvt_pk_bf16_f32 v78, v118, v119\n"
      "v_lshlrev_b32 v118, 16, v79\n"
      "v_and_b32 v119, 0xffff0000, v79\n"
      "v_pk_mul_f32 v[118:119], v[118:119], v[114:115] op_sel:[0,1] op_sel_hi:[1,1]\n"
      "v_cvt_pk_bf16_f32 v79, v118, v119\n"
      "v_add_f32 v224, v144, v80\n"
      "v_rcp_f32 v224, v224\n"
      "v_mfma_f32_16x16x32_bf16 v[160:163], v[86:89], v[64:67], v[32:35]\n"
      "v_mfma_f32_16x16x32_bf16 v[176:179], v[84:87], v[64:67], v[48:51]\n"
      "v_mfma_f32_16x16x32_bf16 v[164:167], v[86:89], v[68:71], v[36:39]\n"
      "v_mfma_f32_16x16x32_bf16 v[180:183], v[84:87], v[68:71], v[52:55]\n"
      "v_mfma_f32_16x16x32_bf16 v[168:171], v[86:89], v[72:75], v[40:43]\n"
      "v_mfma_f32_16x16x32_bf16 v[184:187], v[84:87], v[72:75], v[56:59]\n"
      "v_mfma_f32_16x16x32_bf16 v[172:175], v[86:89], v[76:79], v[44:47]\n"
      "v_mfma_f32_16x16x32_bf16 v[188:191], v[84:87], v[76:79], v[60:63]\n"
      "v_mov_b32 v226, v224\n"
      "s_nop 1\n"
      "s_nop 0\n"
      "s_nop 1\n"
      "v_permlane32_swap_b32 v224, v226\n"
      "s_nop 1\n"
      "v_mov_b32 v225, v224\n"
      "s_nop 1\n"
      "v_mov_b32 v227, v226\n"
      "s_nop 1\n"
      "v_permlane16_swap_b32 v224, v225\n"
      "s_nop 1\n"
      "v_permlane16_swap_b32 v226, v227\n"
      "s_nop 1\n"
      "s_branch FR_%=\n"
      ".subsection 0\n"
      : "+{s[4:19]}"(ka), "+{v128}"(pre), "+{v[160:191]}"(qq), "+{s92}"(tch), "+{v[242:249]}"(ad), "+{v[250:251]}"(ra)
      : [qo] "s"(bk * 128 * D_ * 2), [oo] "s"((bk * 128 + rh * 64) * (D_ * 2) + (2 * split + kh) * 64 + 4096),
        [fo] "s"((unsigned)WS_EPOCH + bk * 128 + 64 + split * 8), [xo] "s"((unsigned)(WS_X + split * WS_X1 + (bk * 4 + wid) * XW_BYTES)),
        [xw] "s"(xarea(wid)), [xr] "s"(xarea(wid ^ 2)),
        [so0] "s"(so0), [inc] "s"(inc), [wb] "s"(wbase), [tv] "s"(wbase + STAGE_BYTES + KIMG + split * 8192), [n1] "s"(n1),
        [qoff] "s"(Q_OFF + rh * 16384 - kh * 8192), [toff] "s"(TREE_OFF - kh * 8192),
        [neg] "s"((wid ^ (3 * split)) ? 0xff800000u : 0u),
        [lead] "s"(blk * 4 + wid * 32), [cph] "s"(C2 + (unsigned)((jj & ~3) * 8 + xg * 4) * WIN / 256u),
        [rep] "s"((jj >> 2) == 160 / 32 ? 128 + (blk & 31) * 4 : (blk - 224) * 4)
      : "v0", "v1", "v2", "v3", "v4", "v5", "v6", "v7", "v8", "v9", "v10", "v11", "v12", "v13", "v14", "v15", "v16", "v17", "v18", "v19", "v20", "v21", "v22", "v23", "v24", "v25", "v26", "v27", "v28", "v29", "v30", "v31", "v32", "v33", "v34", "v35", "v36", "v37", "v38", "v39", "v40", "v41", "v42", "v43", "v44", "v45", "v46", "v47", "v48", "v49", "v50", "v51", "v52", "v53", "v54", "v55", "v56", "v57", "v58", "v59", "v60", "v61", "v62", "v63", "v64", "v65", "v66", "v67", "v68", "v69", "v70", "v71", "v72", "v73", "v74", "v75", "v76", "v77", "v78", "v79", "v80", "v81", "v82", "v83", "v84", "v85", "v86", "v87", "v88", "v89", "v90", "v91", "v92", "v93", "v94", "v95", "v96", "v97", "v98", "v99", "v100", "v101", "v102", "v103", "v104", "v105", "v106", "v107", "v108", "v109", "v110", "v111", "v112", "v113", "v114", "v115", "v116", "v117", "v118", "v119", "v120", "v121", "v122", "v123", "v124", "v125", "v126", "v127", "v129", "v130", "v131", "v132", "v133", "v134", "v135", "v136", "v137", "v138", "v139", "v140", "v141", "v142", "v143", "v144", "v145", "v146", "v147", "v148", "v149", "v150", "v151", "v152", "v153", "v154", "v155", "v156", "v157", "v158", "v159", "v192", "v193", "v194", "v195", "v196", "v197", "v198", "v199", "v200", "v201", "v202", "v203", "v204", "v205", "v206", "v207", "v208", "v209", "v210", "v211", "v212", "v213", "v214", "v215", "v216", "v217", "v218", "v219", "v220", "v221", "v222", "v223", "v224", "v225", "v226", "v227", "v228", "v229", "v230", "v231", "v232", "v233", "v234", "v235", "v236", "v237", "v238", "v239", "v240", "v241", "a0", "a1", "a2", "a3", "a4", "a5", "a6", "a7", "a8", "a9", "a10", "a11", "a12", "a13", "a14", "a15", "a16", "a17", "a18", "a19", "a20", "a21", "a22", "a23", "a24", "a25", "a26", "a27", "a28", "a29", "a30", "a31", "a32", "a33", "a34", "a35", "a36", "a37", "a38", "a39", "a40", "a41", "a42", "a43", "a44", "a45", "a46", "a47", "a48", "a49", "a50", "a51", "a52", "a53", "a54", "a55", "a56", "a57", "a58", "a59", "a60", "a61", "a62", "a63", "a64", "a65", "a66", "a67", "a68", "a69", "a70", "a71", "a72", "a73", "a74", "a75", "a76", "a77", "a78", "a79", "a80", "a81", "a82", "a83", "a84", "a85", "a86", "a87", "a88", "a89", "a90", "a91", "a92", "a93", "a94", "a95", "a96", "a97", "a98", "a99", "a100", "a101", "a102", "a103", "a104", "a105", "a106", "a107", "a108", "a109", "a110", "a111", "a112", "a113", "a114", "a115", "a116", "a117", "a118", "a119", "a120", "a121", "a122", "a123", "a124", "a125", "a126", "a127", "a128", "a129", "a130", "a131", "a132", "a133", "a134", "a135", "a136", "a137", "a138", "a139", "a140", "a141", "a142", "a143", "a144", "a145", "a146", "a147", "a148", "a149", "a150", "a151", "a152", "a153", "a154", "a155", "a156", "a157", "a158", "a159", "a160", "a161", "a162", "a163", "a164", "a165", "a166", "a167", "a168", "a169", "a170", "a171", "a172", "a173", "a174", "a175", "a176", "a177", "a178", "a179", "a180", "a181", "a182", "a183", "a184", "a185", "a186", "a187", "a188", "a189", "a190", "a191", "a192", "a193", "a194", "a195", "a196", "a197", "a198", "a199", "a200", "a201", "a202", "a203", "a204", "a205", "a206", "a207", "a208", "a209", "a210", "a211", "a212", "a213", "a214", "a215", "a216", "a217", "a218", "a219", "a220", "a221", "a222", "a223", "a224", "a225", "a226", "a227", "a228", "a229", "a230", "a231", "s20", "s21", "s22", "s23", "s24", "s25", "s26", "s27", "s28", "s29", "s30", "s31", "s60", "s61", "s62", "s63", "s64", "s65", "s66", "s67", "s68", "s69", "s70", "s71", "s72", "s73", "s74", "s75", "s76", "s77", "s78", "s79", "s80", "s81", "s82", "s83", "s84", "s85", "s86", "s87", "s88", "s89", "s90", "s91", "s93", "s94", "s95", "s98", "s99", "s100", "s101", "vcc", "scc", "memory");
}

}

extern "C" void kernel_launch(void* const* d_in, const int* in_sizes, int n_in, void* d_out, int out_size,
                              void* d_ws, size_t ws_size, hipStream_t stream) {
  if (n_in != 6 || ws_size < WS_END) return;
  const __hip_bfloat16* q = (const __hip_bfloat16*)d_in[0];
  const __hip_bfloat16* kc = (const __hip_bfloat16*)d_in[1];
  const __hip_bfloat16* vc = (const __hip_bfloat16*)d_in[2];
  const __hip_bfloat16* kt = (const __hip_bfloat16*)d_in[3];
  const __hip_bfloat16* vt = (const __hip_bfloat16*)d_in[4];
  const __hip_bfloat16* tm = (const __hip_bfloat16*)d_in[5];
  hipLaunchKernelGGL(tree_attn_main, dim3(B_ * HKV_ * 2), dim3(256), 0, stream, q, kc, vc, kt, vt, tm,
                     (__hip_bfloat16*)d_out, (char*)d_ws);
}
```

```cpp
#include <hip/hip_runtime.h>
#include <hip/hip_bf16.h>
#include <stdint.h>

#pragma clang diagnostic ignored "-Wint-to-pointer-cast"
#pragma clang diagnostic ignored "-Winline-asm"

namespace {

constexpr int B_ = 16, HQ_ = 32, HKV_ = 8, T_ = 32, C_ = 4096, D_ = 128;
constexpr int TILE = 64;
constexpr int KIMG = TILE * D_ * 2;
constexpr int STAGE_BYTES = 2 * KIMG;
constexpr int TREE_OFF = 4 * STAGE_BYTES;
constexpr int Q_OFF = 3 * STAGE_BYTES;
constexpr int LDS_BYTES = TREE_OFF + 2 * 13312;

constexpr size_t WS_EPOCH = 0;
constexpr size_t WS_REF = WS_EPOCH + 128 * 128;
constexpr size_t WS_X = WS_REF + 16384, WS_X1 = 4 << 20;
constexpr size_t XW_BYTES = 4096 + 1024;
constexpr size_t WS_END = 0x680000 + WS_X;
static_assert(WS_X1 + WS_X + 128 * 4 * XW_BYTES <= WS_END, "");

typedef unsigned long long u64x8 __attribute__((ext_vector_type(8)));
typedef unsigned u32x32 __attribute__((ext_vector_type(32)));
typedef unsigned u32x8  __attribute__((ext_vector_type(8)));
typedef unsigned u32x4  __attribute__((ext_vector_type(4)));
typedef unsigned u32x2  __attribute__((ext_vector_type(2)));
typedef __attribute__((address_space(1))) unsigned long long gu64;

__global__ __launch_bounds__(256, 1)
void tree_attn_main(const __hip_bfloat16*, const __hip_bfloat16*, const __hip_bfloat16*, const __hip_bfloat16*,
                    const __hip_bfloat16*, const __hip_bfloat16*, __hip_bfloat16*, char*) {
  __shared__ __attribute__((aligned(1024))) char smem[LDS_BYTES];
  unsigned blk = blockIdx.x;
  u64x8 ka; u32x32 qq; unsigned pre, tch, voffK; int tid = threadIdx.x;
  asm volatile(
      "s_load_dwordx16 s[4:19], %[kp], 0x0\n"
      "s_getpc_b64 s[84:85]\n"
      "v_lshlrev_b32 v112, 4, %[tid]\n"
      "s_lshl_b32 s69, %[blk], 7\n"
      "s_and_b32 s69, s69, 0x7c00\n"
      "v_lshl_add_u32 v120, %[tid], 2, s69\n"
      "global_load_dword v128, v120, s[84:85]\n"
      "v_readfirstlane_b32 s77, %[tid]\n"
      "v_lshrrev_b32 %[vk], 4, %[tid]\n"
      "s_and_b32 s78, %[blk], 7\n"
      "v_xor_b32 %[vk], %[vk], %[tid]\n"
      "s_lshr_b32 s74, %[blk], 4\n"
      "v_and_b32 %[vk], 7, %[vk]\n"
      "s_lshl4_add_u32 s78, s78, s74\n"
      "v_lshrrev_b32 v120, 3, %[tid]\n"
      "s_lshl_b32 s78, s78, 15\n"
      "v_lshlrev_b32 %[vk], 4, %[vk]\n"
      "s_lshl_b32 s77, s77, 5\n"
      "v_lshl_or_b32 %[vk], v120, 8, %[vk]\n"
      "s_bfm_b32 s66, 1, 22\n"
      "s_bfm_b32 s67, 1, 17\n"
      "v_add_u32 v120, 0x2000, %[vk]\n"
      "s_add_u32 s74, s78, 0x4000\n"
      "s_lshl_b32 s76, s78, 5\n"
      "s_add_u32 m0, s77, 0x20000\n"
      "s_mov_b32 s86, 0x100000\n"
      "s_mov_b32 s87, 0x20000\n"
      "s_lshr_b32 s75, s78, 2\n"
      "v_mov_b64 v[116:117], 0\n"
      "v_mov_b64 v[118:119], 0\n"
      "s_lshl_b32 s69, %[blk], 10\n"
      "s_andn2_b32 s69, 0x2000, s69\n"
      "v_add_u32 v113, s69, v112\n"
      "ds_write_b128 v112, v[116:119] offset:16384\n"
      "ds_write_b128 v112, v[116:119] offset:20480\n"
      "ds_write_b128 v112, v[116:119] offset:24576\n"
      "ds_write_b128 v112, v[116:119] offset:28672\n"
      "s_waitcnt lgkmcnt(0)\n"
      "s_mov_b32 s64, s4\n"
      "s_and_b32 s65, s5, 0xffff\n"
      "buffer_load_dwordx4 v[160:163], %[vk], s[64:67], s78 offen nt\n"
      "buffer_load_dwordx4 v[164:167], %[vk], s[64:67], s78 offen offset:128 nt\n"
      "buffer_load_dwordx4 v[168:171], v120, s[64:67], s78 offen nt\n"
      "buffer_load_dwordx4 v[172:175], v120, s[64:67], s78 offen offset:128 nt\n"
      "s_load_dword s92, s[8:9], s76\n"
      "buffer_load_dwordx4 v[176:179], %[vk], s[64:67], s74 offen nt\n"
      "buffer_load_dwordx4 v[180:183], %[vk], s[64:67], s74 offen offset:128 nt\n"
      "buffer_load_dwordx4 v[184:187], v120, s[64:67], s74 offen nt\n"
      "buffer_load_dwordx4 v[188:191], v120, s[64:67], s74 offen offset:128 nt\n"
      "s_mov_b32 s84, s10\n"
      "s_and_b32 s85, s11, 0xffff\n"
      "s_lshr_b32 s76, s78, 18\n"
      "s_lshl_b32 s76, s76, 11\n"
      "s_lshr_b32 s69, s77, 1\n"
      "buffer_load_dwordx4 %[vk], s[84:87], s75 offen nt lds\n"
      "s_add_u32 m0, s77, 131968\n"
      "s_cmp_gt_u32 s77, 2048\n"
      "buffer_load_dwordx4 %[vk], s[84:87], s75 offen offset:128 nt lds\n"
      "s_cbranch_scc1 NOMASK_%=\n"
      "s_add_u32 m0, s69, 140288\n"
      "s_mov_b32 s64, s14\n"
      "s_and_b32 s65, s15, 0xffff\n"
      "s_mov_b32 s66, 0x8000\n"
      "buffer_load_dwordx4 v112, s[64:67], s76 offen lds\n"
      "NOMASK_%=:\n"
      "ds_write_b128 v113, v[116:119] offset:49152\n"
      "ds_write_b128 v113, v[116:119] offset:53248\n"
      "s_mov_b32 s64, s6\n"
      "s_and_b32 s65, s7, 0xffff\n"
      "s_bfm_b32 s66, 1, 27\n"
      "s_lshl_b32 s76, s78, 5\n"
      "s_bfe_u32 s74, %[blk], 0x10003\n"
      "s_lshl_b32 s74, s74, 15\n"
      "s_add_u32 s76, s76, s74\n"
      "s_bfe_u32 s74, %[blk], 0x10004\n"
      "s_lshl_b32 s74, s74, 14\n"
      "s_add_u32 s76, s76, s74\n"
      "s_add_u32 m0, s77, 0\n"
      "s_nop 0\n"
      "buffer_load_dwordx4 %[vk], s[64:67], s76 offen nt lds\n"
      "s_add_u32 m0, s77, 896\n"
      "s_nop 0\n"
      "buffer_load_dwordx4 %[vk], s[64:67], s76 offen offset:128 nt lds\n"
      "s_add_u32 m0, s77, 8192\n"
      "s_nop 0\n"
      "buffer_load_dwordx4 v120, s[64:67], s76 offen nt lds\n"
      : "=&{s[4:19]}"(ka), "=&{v128}"(pre), "=&{v[160:191]}"(qq), "=&{s92}"(tch), [vk] "=&v"(voffK), [tid] "+v"(tid)
      : [kp] "s"(__builtin_amdgcn_kernarg_segment_ptr()), [blk] "s"(blk)
      : "v112", "v113", "v116", "v117", "v118", "v119", "v120", "s64", "s65", "s66", "s67", "s69", "s70", "s71", "s74", "s75", "s76", "s77", "s78", "s84", "s85", "s86", "s87", "s96", "s97", "scc", "memory");
  asm volatile("" : "+s"(blk));
  const int lane = tid & 63;
  const int wid = __builtin_amdgcn_readfirstlane(tid >> 6);
  const unsigned wbase = wid * 2048;
  const int rh = wid & 1, kh = wid >> 1;
  const int xg = blk & 7, jj = blk >> 3, bk = xg * 16 + (jj >> 1), split = jj & 1;
  const int c16 = lane & 15, g = lane >> 4;
  const unsigned lds0 = (unsigned)(uintptr_t)smem;

  const unsigned voffV = (unsigned)((8 * wid + (lane >> 3)) * 256 + (((lane & 7) ^ (g << 1)) << 4));
  const unsigned so0 = (unsigned)(bk * C_ + split * 2 * TILE) * (D_ * 2) + (bk & 1) * KIMG, inc = KIMG - 2 * (bk & 1) * KIMG;
  u32x8 ad;
#pragma unroll
  for (int j = 0; j < 2; ++j)
    ad[j] = lds0 + (unsigned)(2048 * (c16 >> 3) + 128 * (c16 & 7) + (((4 * j + g) ^ (c16 >> 1)) << 4) + kh * 8192);
  {
    const int q4 = c16 >> 2, p = lane & 3;
#pragma unroll
    for (int j = 0; j < 4; ++j)
      ad[2 + j] = lds0 + (unsigned)(2048 * (g >> 1) + 128 * (4 * (g & 1) + q4) + kh * 8192 + KIMG +
                                    (((2 * (j ^ (2 * kh)) + (p >> 1)) ^ ((2 * (g & 1) + (q4 >> 1)) << 1)) << 4) + 8 * (p & 1));
  }
  ad[6] = voffK; ad[7] = voffV + 128 * split;
  u32x2 ra = {(unsigned)(lane < 4 ? WS_REF + lane * 512 : 1 << 26), (unsigned)(WS_REF + (jj & (4 - 1)) * 512 + (lane & 7) * 4 + (wid << 26))};
  asm volatile(
      "s_memrealtime s[82:83]\n"
      "s_mov_b32 s60, %[so0]\n"
      "s_mov_b32 s62, %[wb]\n"
      "v_add_u32 v120, 0x2000, v248\n"
      "s_lshr_b32 s80, %[qo], 8\n"
      "v_mov_b32 v240, s80\n"
      "s_mov_b32 s22, 0x8000000\n"
      "s_mov_b32 s23, 0x20000\n"
      "s_mov_b32 s20, s6\n"
      "s_and_b32 s21, s7, 0xffff\n"
      "s_nop 0\n"
      "v_mov_b64 v[216:217], 0\n"
      "v_mov_b64 v[218:219], 0\n"
      "s_mov_b32 s86, 0x100000\n"
      "s_mov_b32 s87, 0x20000\n"
      "v_mfma_f32_32x32x16_bf16 v[32:47], v[216:219], v[216:219], 0\n"
      "s_lshr_b32 s75, %[qo], 2\n"
      "s_mov_b32 s84, s12\n"
      "s_and_b32 s85, s13, 0xffff\n"
      "v_mfma_f32_32x32x16_bf16 v[48:63], v[216:219], v[216:219], 0\n"
      "s_nop 0\n"
      "v_xor_b32 v121, 0x80, v249\n"
      "s_sub_u32 s78, 0x10000, %[inc]\n"
      "s_mov_b32 s94, 0x3e0293ee\n"
      "v_mfma_f32_32x32x16_bf16 v[64:79], v[216:219], v[216:219], 0\n"
      "s_mov_b32 s95, 0x3e0293ee\n"
      "s_mov_b32 s74, 0xffff0000\n"
      "s_mov_b32 s76, 0xceabfb8f\n"
      "v_mfma_f32_32x32x16_bf16 v[80:95], v[216:219], v[216:219], 0\n"
      "v_mbcnt_lo_u32_b32 v112, -1, 0\n"
      "s_nop 0\n"
      "v_mbcnt_hi_u32_b32 v112, -1, v112\n"
      "s_add_u32 s69, %[wb], 0x18000\n"
      "v_mfma_f32_32x32x16_bf16 v[192:207], v[216:219], v[216:219], 0\n"
      "v_lshl_add_u32 v226, v112, 4, s69\n"
      "v_lshrrev_b32 v115, 4, v112\n"
      "v_and_b32 v112, 15, v112\n"
      "v_mfma_f32_32x32x16_bf16 a[128:143], v[216:219], v[216:219], 0\n"
      "v_lshlrev_b32 v115, 3, v115\n"
      "v_lshl_add_u32 v115, v112, 6, v115\n"
      "s_add_u32 m0, s62, 9088\n"
      "s_nop 0\n"
      "buffer_load_dwordx4 v120, s[20:23], s60 offen offset:128 nt lds\n"
      "v_add_u32 v115, 140288, v115\n"
      "v_mfma_f32_32x32x16_bf16 a[144:159], v[216:219], v[216:219], 0\n"
      "v_add_u32 v118, %[qoff], v242\n"
      "v_add_u32 v224, %[toff], v242\n"
      "v_add_u32 v119, %[qoff], v243\n"
      "v_mfma_f32_32x32x16_bf16 a[160:175], v[216:219], v[216:219], 0\n"
      "v_add_u32 v225, %[toff], v243\n"
      "v_mfma_f32_32x32x16_bf16 a[176:191], v[216:219], v[216:219], 0\n"
      "s_add_u32 s62, s62, 0x8000\n"
      "s_and_b32 s62, s62, 0x1ffff\n"
      "s_add_u32 s60, s60, %[inc]\n"
      "s_waitcnt vmcnt(13)\n"
      "v_lshlrev_b32 v232, 16, v160\n"
      "v_and_b32 v233, s74, v160\n"
      "v_lshlrev_b32 v234, 16, v161\n"
      "v_and_b32 v235, s74, v161\n"
      "v_lshlrev_b32 v236, 16, v162\n"
      "v_and_b32 v237, s74, v162\n"
      "v_lshlrev_b32 v238, 16, v163\n"
      "v_and_b32 v239, s74, v163\n"
      "v_pk_mul_f32 v[232:233], v[232:233], s[94:95]\n"
      "v_pk_mul_f32 v[234:235], v[234:235], s[94:95]\n"
      "v_pk_mul_f32 v[236:237], v[236:237], s[94:95]\n"
      "v_pk_mul_f32 v[238:239], v[238:239], s[94:95]\n"
      "v_cvt_pk_bf16_f32 v160, v232, v233\n"
      "v_cvt_pk_bf16_f32 v161, v234, v235\n"
      "v_cvt_pk_bf16_f32 v162, v236, v237\n"
      "v_cvt_pk_bf16_f32 v163, v238, v239\n"
      "ds_write_b128 v226, v[160:163] offset:0\n"
      "s_waitcnt vmcnt(12)\n"
      "v_lshlrev_b32 v232, 16, v164\n"
      "v_and_b32 v233, s74, v164\n"
      "v_lshlrev_b32 v234, 16, v165\n"
      "v_and_b32 v235, s74, v165\n"
      "v_lshlrev_b32 v236, 16, v166\n"
      "v_and_b32 v237, s74, v166\n"
      "v_lshlrev_b32 v238, 16, v167\n"
      "v_and_b32 v239, s74, v167\n"
      "v_pk_mul_f32 v[232:233], v[232:233], s[94:95]\n"
      "v_pk_mul_f32 v[234:235], v[234:235], s[94:95]\n"
      "v_pk_mul_f32 v[236:237], v[236:237], s[94:95]\n"
      "v_pk_mul_f32 v[238:239], v[238:239], s[94:95]\n"
      "v_cvt_pk_bf16_f32 v164, v232, v233\n"
      "v_cvt_pk_bf16_f32 v165, v234, v235\n"
      "v_cvt_pk_bf16_f32 v166, v236, v237\n"
      "v_cvt_pk_bf16_f32 v167, v238, v239\n"
      "ds_write_b128 v226, v[164:167] offset:1024\n"
      "s_waitcnt vmcnt(11)\n"
      "v_lshlrev_b32 v232, 16, v168\n"
      "v_and_b32 v233, s74, v168\n"
      "v_lshlrev_b32 v234, 16, v169\n"
      "v_and_b32 v235, s74, v169\n"
      "v_lshlrev_b32 v236, 16, v170\n"
      "v_and_b32 v237, s74, v170\n"
      "v_lshlrev_b32 v238, 16, v171\n"
      "v_and_b32 v239, s74, v171\n"
      "v_pk_mul_f32 v[232:233], v[232:233], s[94:95]\n"
      "v_pk_mul_f32 v[234:235], v[234:235], s[94:95]\n"
      "v_pk_mul_f32 v[236:237], v[236:237], s[94:95]\n"
      "v_pk_mul_f32 v[238:239], v[238:239], s[94:95]\n"
      "v_cvt_pk_bf16_f32 v168, v232, v233\n"
      "v_cvt_pk_bf16_f32 v169, v234, v235\n"
      "v_cvt_pk_bf16_f32 v170, v236, v237\n"
      "v_cvt_pk_bf16_f32 v171, v238, v239\n"
      "ds_write_b128 v226, v[168:171] offset:8192\n"
      "s_add_u32 m0, s62, 0\n"
      "s_nop 0\n"
      "buffer_load_dwordx4 v248, s[20:23], s60 offen nt lds\n"
      "s_waitcnt vmcnt(11)\n"
      "v_lshlrev_b32 v232, 16, v172\n"
      "v_and_b32 v233, s74, v172\n"
      "v_lshlrev_b32 v234, 16, v173\n"
      "v_and_b32 v235, s74, v173\n"
      "v_lshlrev_b32 v236, 16, v174\n"
      "v_and_b32 v237, s74, v174\n"
      "v_lshlrev_b32 v238, 16, v175\n"
      "v_and_b32 v239, s74, v175\n"
      "v_pk_mul_f32 v[232:233], v[232:233], s[94:95]\n"
      "v_pk_mul_f32 v[234:235], v[234:235], s[94:95]\n"
      "v_pk_mul_f32 v[236:237], v[236:237], s[94:95]\n"
      "v_pk_mul_f32 v[238:239], v[238:239], s[94:95]\n"
      "v_cvt_pk_bf16_f32 v172, v232, v233\n"
      "v_cvt_pk_bf16_f32 v173, v234, v235\n"
      "v_cvt_pk_bf16_f32 v174, v236, v237\n"
      "v_cvt_pk_bf16_f32 v175, v238, v239\n"
      "ds_write_b128 v226, v[172:175] offset:9216\n"
      "s_add_u32 m0, s62, 896\n"
      "s_nop 0\n"
      "buffer_load_dwordx4 v248, s[20:23], s60 offen offset:128 nt lds\n"
      "s_waitcnt vmcnt(11)\n"
      "v_lshlrev_b32 v232, 16, v176\n"
      "v_and_b32 v233, s74, v176\n"
      "v_lshlrev_b32 v234, 16, v177\n"
      "v_and_b32 v235, s74, v177\n"
      "v_lshlrev_b32 v236, 16, v178\n"
      "v_and_b32 v237, s74, v178\n"
      "v_lshlrev_b32 v238, 16, v179\n"
      "v_and_b32 v239, s74, v179\n"
      "v_pk_mul_f32 v[232:233], v[232:233], s[94:95]\n"
      "v_pk_mul_f32 v[234:235], v[234:235], s[94:95]\n"
      "v_pk_mul_f32 v[236:237], v[236:237], s[94:95]\n"
      "v_pk_mul_f32 v[238:239], v[238:239], s[94:95]\n"
      "v_cvt_pk_bf16_f32 v176, v232, v233\n"
      "v_cvt_pk_bf16_f32 v177, v234, v235\n"
      "v_cvt_pk_bf16_f32 v178, v236, v237\n"
      "v_cvt_pk_bf16_f32 v179, v238, v239\n"
      "ds_write_b128 v226, v[176:179] offset:16384\n"
      "s_add_u32 m0, s62, 8192\n"
      "s_nop 0\n"
      "buffer_load_dwordx4 v120, s[20:23], s60 offen nt lds\n"
      "s_waitcnt vmcnt(11)\n"
      "v_lshlrev_b32 v232, 16, v180\n"
      "v_and_b32 v233, s74, v180\n"
      "v_lshlrev_b32 v234, 16, v181\n"
      "v_and_b32 v235, s74, v181\n"
      "v_lshlrev_b32 v236, 16, v182\n"
      "v_and_b32 v237, s74, v182\n"
      "v_lshlrev_b32 v238, 16, v183\n"
      "v_and_b32 v239, s74, v183\n"
      "v_pk_mul_f32 v[232:233], v[232:233], s[94:95]\n"
      "v_pk_mul_f32 v[234:235], v[234:235], s[94:95]\n"
      "v_pk_mul_f32 v[236:237], v[236:237], s[94:95]\n"
      "v_pk_mul_f32 v[238:239], v[238:239], s[94:95]\n"
      "v_cvt_pk_bf16_f32 v180, v232, v233\n"
      "v_cvt_pk_bf16_f32 v181, v234, v235\n"
      "v_cvt_pk_bf16_f32 v182, v236, v237\n"
      "v_cvt_pk_bf16_f32 v183, v238, v239\n"
      "ds_write_b128 v226, v[180:183] offset:17408\n"
      "s_add_u32 m0, s62, 9088\n"
      "s_nop 0\n"
      "buffer_load_dwordx4 v120, s[20:23], s60 offen offset:128 nt lds\n"
      "s_waitcnt vmcnt(11)\n"
      "v_lshlrev_b32 v232, 16, v184\n"
      "v_and_b32 v233, s74, v184\n"
      "v_lshlrev_b32 v234, 16, v185\n"
      "v_and_b32 v235, s74, v185\n"
      "v_lshlrev_b32 v236, 16, v186\n"
      "v_and_b32 v237, s74, v186\n"
      "v_lshlrev_b32 v238, 16, v187\n"
      "v_and_b32 v239, s74, v187\n"
      "v_pk_mul_f32 v[232:233], v[232:233], s[94:95]\n"
      "v_pk_mul_f32 v[234:235], v[234:235], s[94:95]\n"
      "v_pk_mul_f32 v[236:237], v[236:237], s[94:95]\n"
      "v_pk_mul_f32 v[238:239], v[238:239], s[94:95]\n"
      "v_cvt_pk_bf16_f32 v184, v232, v233\n"
      "v_cvt_pk_bf16_f32 v185, v234, v235\n"
      "v_cvt_pk_bf16_f32 v186, v236, v237\n"
      "v_cvt_pk_bf16_f32 v187, v238, v239\n"
      "ds_write_b128 v226, v[184:187] offset:24576\n"
      "s_add_u32 m0, %[tv], 0\n"
      "s_nop 0\n"
      "buffer_load_dwordx4 v249, s[84:87], s75 offen nt lds\n"
      "s_add_u32 m0, %[tv], 1024\n"
      "s_nop 0\n"
      "buffer_load_dwordx4 v121, s[84:87], s75 offen nt lds\n"
      "s_waitcnt vmcnt(12)\n"
      "v_lshlrev_b32 v232, 16, v188\n"
      "v_and_b32 v233, s74, v188\n"
      "v_lshlrev_b32 v234, 16, v189\n"
      "v_and_b32 v235, s74, v189\n"
      "v_lshlrev_b32 v236, 16, v190\n"
      "v_and_b32 v237, s74, v190\n"
      "v_lshlrev_b32 v238, 16, v191\n"
      "v_and_b32 v239, s74, v191\n"
      "v_pk_mul_f32 v[232:233], v[232:233], s[94:95]\n"
      "v_pk_mul_f32 v[234:235], v[234:235], s[94:95]\n"
      "v_pk_mul_f32 v[236:237], v[236:237], s[94:95]\n"
      "v_pk_mul_f32 v[238:239], v[238:239], s[94:95]\n"
      "v_cvt_pk_bf16_f32 v188, v232, v233\n"
      "v_cvt_pk_bf16_f32 v189, v234, v235\n"
      "v_cvt_pk_bf16_f32 v190, v236, v237\n"
      "v_cvt_pk_bf16_f32 v191, v238, v239\n"
      "ds_write_b128 v226, v[188:191] offset:25600\n"
      "s_add_u32 s62, s62, 0x8000\n"
      "s_and_b32 s62, s62, 0x1ffff\n"
      "s_add_u32 s60, s60, s78\n"
      "s_waitcnt vmcnt(10) lgkmcnt(0)\n"
      "s_barrier\n"
      "s_memrealtime s[68:69]\n"
      "ds_read_b64 v[208:209], v115 offset:0\n"
      "ds_read_b64 v[210:211], v115 offset:32\n"
      "ds_read_b64 v[212:213], v115 offset:1024\n"
      "ds_read_b64 v[214:215], v115 offset:1056\n"
      "ds_read_b128 a[96:99], v224 offset:0\n"
      "ds_read_b128 a[100:103], v224 offset:4096\n"
      "ds_read_b128 a[0:3], v118 offset:0\n"
      "ds_read_b128 a[4:7], v118 offset:4096\n"
      "ds_read_b128 a[8:11], v118 offset:8192\n"
      "ds_read_b128 a[12:15], v118 offset:12288\n"
      "ds_read_b128 a[104:107], v225 offset:0\n"
      "ds_read_b128 a[108:111], v225 offset:4096\n"
      "global_load_dwordx2 v[240:241], v240, s[18:19] sc1\n"
      "s_mov_b32 s28, s18\n"
      "s_and_b32 s29, s19, 0xffff\n"
      "s_mov_b32 s30, 0x688000\n"
      "s_mov_b32 s31, 0x20000\n"
      "s_lshl_b32 s41, %[blk], 2\n"
      "s_and_b32 s43, s41, 0x7c\n"
      "s_add_u32 s43, s43, 128\n"
      "s_sub_u32 s39, s41, 896\n"
      "s_lshr_b32 s44, %[blk], 5\n"
      "s_cmp_eq_u32 s44, 5\n"
      "s_cselect_b32 s39, s43, s39\n"
      "s_lshr_b32 s43, %[wb], 6\n"
      "s_add_u32 s41, s41, s43\n"
      "s_cmp_lt_u32 s39, 128\n"
      "s_cselect_b32 s89, 0x2ffe, 0\n"
      "s_or_b32 s89, s89, 0x2aae\n"
      "s_waitcnt lgkmcnt(8)\n"
      "ds_read_b128 a[16:19], v119 offset:0\n"
      "ds_read_b128 a[20:23], v119 offset:4096\n"
      "ds_read_b128 a[24:27], v119 offset:8192\n"
      "ds_read_b128 a[28:31], v119 offset:12288\n"
      "v_lshlrev_b32 v96, 16, v208\n"
      "v_and_b32 v97, s74, v208\n"
      "v_lshlrev_b32 v98, 16, v209\n"
      "v_and_b32 v99, s74, v209\n"
      "v_lshlrev_b32 v100, 16, v210\n"
      "v_and_b32 v101, s74, v210\n"
      "v_lshlrev_b32 v102, 16, v211\n"
      "v_and_b32 v103, s74, v211\n"
      "v_lshlrev_b32 v104, 16, v212\n"
      "v_and_b32 v105, s74, v212\n"
      "v_lshlrev_b32 v106, 16, v213\n"
      "v_and_b32 v107, s74, v213\n"
      "v_lshlrev_b32 v108, 16, v214\n"
      "v_and_b32 v109, s74, v214\n"
      "v_lshlrev_b32 v110, 16, v215\n"
      "v_and_b32 v111, s74, v215\n"
      "v_fma_f32 v96, -v96, s76, s76\n"
      "v_fma_f32 v97, -v97, s76, s76\n"
      "v_fma_f32 v98, -v98, s76, s76\n"
      "v_fma_f32 v99, -v99, s76, s76\n"
      "v_fma_f32 v100, -v100, s76, s76\n"
      "v_fma_f32 v101, -v101, s76, s76\n"
      "v_fma_f32 v102, -v102, s76, s76\n"
      "v_fma_f32 v103, -v103, s76, s76\n"
      "v_fma_f32 v104, -v104, s76, s76\n"
      "v_fma_f32 v105, -v105, s76, s76\n"
      "v_fma_f32 v106, -v106, s76, s76\n"
      "v_fma_f32 v107, -v107, s76, s76\n"
      "v_fma_f32 v108, -v108, s76, s76\n"
      "v_fma_f32 v109, -v109, s76, s76\n"
      "v_fma_f32 v110, -v110, s76, s76\n"
      "v_fma_f32 v111, -v111, s76, s76\n"
      "s_waitcnt lgkmcnt(0)\n"
      "ds_read_b128 a[112:115], v224 offset:1024\n"
      "ds_read_b128 a[116:119], v224 offset:5120\n"
      "ds_read_b128 a[32:35], v118 offset:1024\n"
      "ds_read_b128 a[36:39], v118 offset:5120\n"
      "ds_read_b128 a[40:43], v118 offset:9216\n"
      "ds_read_b128 a[44:47], v118 offset:13312\n"
      "ds_read_b128 a[120:123], v225 offset:1024\n"
      "ds_read_b128 a[124:127], v225 offset:5120\n"
      "ds_read_b128 a[48:51], v119 offset:1024\n"
      "ds_read_b128 a[52:55], v119 offset:5120\n"
      "ds_read_b128 a[56:59], v119 offset:9216\n"
      "ds_read_b128 a[60:63], v119 offset:13312\n"
      "s_cmp_lt_u32 s41, 32\n"
      "s_cbranch_scc0 NOLEAD_%=\n"
      "v_mov_b32 v115, s68\n"
      "buffer_store_dword v115, v250, s[28:31], s41 offen sc1\n"
      "NOLEAD_%=:\n"
      "v_mfma_f32_16x16x32_bf16 v[160:163], a[96:99], a[0:3], v[96:99]\n"
      "s_mov_b32 s83, 0\n"
      "v_add_u32 v122, 0x2000, v249\n"
      "v_add_u32 v123, 0x2000, v121\n"
      "v_mfma_f32_16x16x32_bf16 v[168:171], a[96:99], a[4:7], v[104:107]\n"
      "s_add_u32 m0, s62, 0\n"
      "s_sub_u32 s67, s68, s82\n"
      "v_mov_b32 v113, 0x3f803f80\n"
      "v_accvgpr_write_b32 a192, 0\n"
      "v_mfma_f32_16x16x32_bf16 v[176:179], a[96:99], a[8:11], v[96:99]\n"
      "s_min_u32 s67, s67, 140\n"
      "buffer_load_dwordx4 v248, s[20:23], s60 offen nt lds\n"
      "v_cmp_eq_u32 vcc, 0, v112\n"
      "v_mfma_f32_16x16x32_bf16 v[184:187], a[96:99], a[12:15], v[104:107]\n"
      "s_add_u32 m0, s62, 896\n"
      "s_add_u32 s66, s82, s67\n"
      "v_mfma_f32_16x16x32_bf16 v[164:167], a[100:103], a[0:3], v[100:103]\n"
      "s_add_u32 s66, s66, 91\n"
      "buffer_load_dwordx4 v248, s[20:23], s60 offen offset:128 nt lds\n"
      "v_cndmask_b32 v114, 0, v113, vcc\n"
      "v_mfma_f32_16x16x32_bf16 v[172:175], a[100:103], a[4:7], v[108:111]\n"
      "s_add_u32 m0, s62, 8192\n"
      "s_sub_u32 s71, s68, 400\n"
      "v_accvgpr_write_b32 a196, v114\n"
      "v_accvgpr_write_b32 a197, v114\n"
      "v_mfma_f32_16x16x32_bf16 v[180:183], a[100:103], a[8:11], v[100:103]\n"
      "s_mov_b32 s68, s66\n"
      "buffer_load_dwordx4 v120, s[20:23], s60 offen nt lds\n"
      "v_accvgpr_write_b32 a198, v114\n"
      "v_accvgpr_write_b32 a199, v114\n"
      "v_mfma_f32_16x16x32_bf16 v[188:191], a[100:103], a[12:15], v[108:111]\n"
      "s_add_u32 m0, s62, 9088\n"
      "s_mov_b32 s82, s66\n"
      "v_cmp_eq_u32 vcc, 4, v112\n"
      "v_mfma_f32_16x16x32_bf16 v[160:163], a[104:107], a[16:19], v[160:163]\n"
      "s_add_u32 s62, s62, 0x8000\n"
      "buffer_load_dwordx4 v120, s[20:23], s60 offen offset:128 nt lds\n"
      "s_mov_b32 s61, %[so0]\n"
      "v_mfma_f32_16x16x32_bf16 v[168:171], a[104:107], a[20:23], v[168:171]\n"
      "s_and_b32 s62, s62, 0x1ffff\n"
      "s_add_u32 s77, %[wb], 0x10000\n"
      "v_cndmask_b32 v114, 0, v113, vcc\n"
      "v_mfma_f32_16x16x32_bf16 v[176:179], a[104:107], a[24:27], v[176:179]\n"
      "s_add_u32 s60, s60, %[inc]\n"
      "s_mov_b32 s70, 300\n"
      "v_accvgpr_write_b32 a200, v114\n"
      "v_accvgpr_write_b32 a201, v114\n"
      "v_mfma_f32_16x16x32_bf16 v[184:187], a[104:107], a[28:31], v[184:187]\n"
      "s_mov_b32 s80, 0x12a\n"
      "v_accvgpr_write_b32 a202, v114\n"
      "v_accvgpr_write_b32 a203, v114\n"
      "s_and_b32 s43, %[blk], 7\n"
      "v_mfma_f32_16x16x32_bf16 v[164:167], a[108:111], a[16:19], v[164:167]\n"
      "s_mov_b32 s81, 0x0\n"
      "v_cmp_eq_u32 vcc, 8, v112\n"
      "s_and_b32 s38, %[blk], -32\n"
      "v_mfma_f32_16x16x32_bf16 v[172:175], a[108:111], a[20:23], v[172:175]\n"
      "s_mov_b32 s101, 1000\n"
      "s_lshl2_add_u32 s38, s43, s38\n"
      "v_mfma_f32_16x16x32_bf16 v[180:183], a[108:111], a[24:27], v[180:183]\n"
      "s_mov_b32 s98, 110\n"
      "v_cndmask_b32 v114, 0, v113, vcc\n"
      "s_mul_i32 s38, s38, 52\n"
      "v_mfma_f32_16x16x32_bf16 v[188:191], a[108:111], a[28:31], v[188:191]\n"
      "s_waitcnt lgkmcnt(0)\n"
      "s_mov_b32 s93, 28\n"
      "v_accvgpr_write_b32 a204, v114\n"
      "v_accvgpr_write_b32 a205, v114\n"
      "s_lshr_b32 s38, s38, 8\n"
      "v_mfma_f32_16x16x32_bf16 v[160:163], a[112:115], a[32:35], v[160:163]\n"
      "s_mov_b32 s99, 27\n"
      "v_accvgpr_write_b32 a206, v114\n"
      "v_accvgpr_write_b32 a207, v114\n"
      "s_add_u32 s38, s38, 331\n"
      "v_mfma_f32_16x16x32_bf16 v[168:171], a[112:115], a[36:39], v[168:171]\n"
      "s_mov_b32 s100, 285\n"
      "v_cmp_eq_u32 vcc, 12, v112\n"
      "s_mul_i32 s43, s43, 5\n"
      "v_mfma_f32_16x16x32_bf16 v[176:179], a[112:115], a[40:43], v[176:179]\n"
      "s_lshl_b32 s91, %[wb], 18\n"
      "s_add_u32 s43, s43, s44\n"
      "v_mfma_f32_16x16x32_bf16 v[184:187], a[112:115], a[44:47], v[184:187]\n"
      "s_sub_u32 s91, 0x10000000, s91\n"
      "v_cndmask_b32 v114, 0, v113, vcc\n"
      "s_cmp_gt_u32 s44, 5\n"
      "s_subb_u32 s43, s43, 0\n"
      "v_mfma_f32_16x16x32_bf16 v[164:167], a[116:119], a[32:35], v[164:167]\n"
      "s_mov_b32 s86, 110\n"
      "v_accvgpr_write_b32 a208, v114\n"
      "v_accvgpr_write_b32 a209, v114\n"
      "s_mul_i32 s40, s43, 43\n"
      "v_mfma_f32_16x16x32_bf16 v[172:175], a[116:119], a[36:39], v[172:175]\n"
      "s_mov_b32 s87, 0\n"
      "v_accvgpr_write_b32 a210, v114\n"
      "v_accvgpr_write_b32 a211, v114\n"
      "s_lshr_b32 s40, s40, 8\n"
      "s_waitcnt vmcnt(11)\n"
      "s_barrier\n"
      "v_mfma_f32_16x16x32_bf16 v[180:183], a[116:119], a[40:43], v[180:183]\n"
      "s_mov_b32 s26, 0x8000000\n"
      "ds_read_b128 a[64:67], v242 offset:0\n"
      "s_mul_i32 s40, s40, -6\n"
      "v_mfma_f32_16x16x32_bf16 v[188:191], a[116:119], a[44:47], v[188:191]\n"
      "s_mov_b32 s27, 0x20000\n"
      "ds_read_b128 a[68:71], v242 offset:4096\n"
      "s_add_u32 s40, s40, s43\n"
      "v_mfma_f32_16x16x32_bf16 v[160:163], a[120:123], a[48:51], v[160:163]\n"
      "s_mov_b32 s24, s8\n"
      "ds_read_b128 a[72:75], v243 offset:0\n"
      "s_lshl_b32 s44, s44, 2\n"
      "v_mfma_f32_16x16x32_bf16 v[168:171], a[120:123], a[52:55], v[168:171]\n"
      "s_and_b32 s25, s9, 0xffff\n"
      "ds_read_b128 a[76:79], v243 offset:4096\n"
      "s_lshr_b32 s43, 0x70600000, s44\n"
      "v_mfma_f32_16x16x32_bf16 v[176:179], a[120:123], a[56:59], v[176:179]\n"
      "ds_read_b128 a[80:83], v242 offset:1024\n"
      "s_and_b32 s43, s43, 7\n"
      "v_mfma_f32_16x16x32_bf16 v[184:187], a[120:123], a[60:63], v[184:187]\n"
      "ds_read_b128 a[84:87], v242 offset:5120\n"
      "s_max_u32 s40, s40, s43\n"
      "v_mfma_f32_16x16x32_bf16 v[164:167], a[124:127], a[48:51], v[164:167]\n"
      "ds_read_b128 a[88:91], v243 offset:1024\n"
      "s_add_u32 s40, s40, 4\n"
      "v_mfma_f32_16x16x32_bf16 v[172:175], a[124:127], a[52:55], v[172:175]\n"
      "ds_read_b128 a[92:95], v243 offset:5120\n"
      "s_and_b32 s43, %[blk], 8\n"
      "v_mfma_f32_16x16x32_bf16 v[180:183], a[124:127], a[56:59], v[180:183]\n"
      "s_mul_i32 s43, s43, 768\n"
      "v_mfma_f32_16x16x32_bf16 v[188:191], a[124:127], a[60:63], v[188:191]\n"
      "s_cmp_eq_u32 s43, %[wb]\n"
      "s_cselect_b32 s42, 0, 0xff800000\n"
      "s_nop 3\n"
      "v_max3_f32 v112, v160, v161, v162\n"
      "v_max3_f32 v113, v168, v169, v170\n"
      "v_max3_f32 v114, v176, v177, v178\n"
      "v_max3_f32 v115, v184, v185, v186\n"
      "v_max3_f32 v112, v112, v163, v164\n"
      "v_max3_f32 v113, v113, v171, v172\n"
      "v_max3_f32 v114, v114, v179, v180\n"
      "v_max3_f32 v115, v115, v187, v188\n"
      "v_max3_f32 v112, v112, v165, v166\n"
      "v_max3_f32 v113, v113, v173, v174\n"
      "v_max3_f32 v114, v114, v181, v182\n"
      "v_max3_f32 v115, v115, v189, v190\n"
      "v_max_f32 v112, v112, v167\n"
      "v_max_f32 v113, v113, v175\n"
      "v_max_f32 v114, v114, v183\n"
      "v_max_f32 v115, v115, v191\n"
      "v_mov_b32 v208, v112\n"
      "v_mov_b32 v209, v113\n"
      "v_mov_b32 v210, v114\n"
      "v_mov_b32 v211, v115\n"
      "v_permlane16_swap_b32 v112, v208\n"
      "v_permlane16_swap_b32 v113, v209\n"
      "v_permlane16_swap_b32 v114, v210\n"
      "v_permlane16_swap_b32 v115, v211\n"
      "v_max_f32 v112, v112, v208\n"
      "v_max_f32 v113, v113, v209\n"
      "v_max_f32 v114, v114, v210\n"
      "v_max_f32 v115, v115, v211\n"
      "v_mov_b32 v208, v112\n"
      "v_mov_b32 v209, v113\n"
      "v_mov_b32 v210, v114\n"
      "v_mov_b32 v211, v115\n"
      "v_permlane32_swap_b32 v112, v208\n"
      "v_permlane32_swap_b32 v113, v209\n"
      "v_permlane32_swap_b32 v114, v210\n"
      "v_permlane32_swap_b32 v115, v211\n"
      "v_max_f32 v112, v112, v208\n"
      "v_max_f32 v113, v113, v209\n"
      "v_max_f32 v114, v114, v210\n"
      "v_max_f32 v115, v115, v211\n"
      "v_sub_f32 v112, 0xc1800000, v112\n"
      "v_sub_f32 v113, 0xc1800000, v113\n"
      "v_sub_f32 v114, 0xc1800000, v114\n"
      "v_sub_f32 v115, 0xc1800000, v115\n"
      "v_floor_f32 v112, v112\n"
      "v_floor_f32 v113, v113\n"
      "v_floor_f32 v114, v114\n"
      "v_floor_f32 v115, v115\n"
      "v_min_f32 v224, 0x7f000000, v112\n"
      "v_min_f32 v228, 0x7f000000, v113\n"
      "v_min_f32 v232, 0x7f000000, v114\n"
      "v_min_f32 v236, 0x7f000000, v115\n"
      "v_add_f32 v112, s42, v224\n"
      "v_add_f32 v113, s42, v228\n"
      "v_add_f32 v114, s42, v232\n"
      "v_add_f32 v115, s42, v236\n"
      "v_mov_b32 v225, v224\n"
      "v_mov_b32 v229, v228\n"
      "v_mov_b32 v233, v232\n"
      "v_mov_b32 v237, v236\n"
      "v_mov_b64 v[226:227], v[224:225]\n"
      "v_mov_b64 v[230:231], v[228:229]\n"
      "v_mov_b64 v[234:235], v[232:233]\n"
      "v_mov_b64 v[238:239], v[236:237]\n"
      "s_add_u32 m0, s77, 16384\n"
      "v_pk_add_f32 v[160:161], v[160:161], v[112:113] op_sel:[0,0] op_sel_hi:[1,0]\n"
      "buffer_load_dwordx4 v249, s[24:27], s61 offen nt lds\n"
      "v_pk_add_f32 v[162:163], v[162:163], v[112:113] op_sel:[0,0] op_sel_hi:[1,0]\n"
      "v_pk_add_f32 v[164:165], v[164:165], v[112:113] op_sel:[0,0] op_sel_hi:[1,0]\n"
      "v_pk_add_f32 v[166:167], v[166:167], v[112:113] op_sel:[0,0] op_sel_hi:[1,0]\n"
      "s_add_u32 m0, s77, 17408\n"
      "v_pk_add_f32 v[168:169], v[168:169], v[112:113] op_sel:[0,1] op_sel_hi:[1,1]\n"
      "buffer_load_dwordx4 v121, s[24:27], s61 offen nt lds\n"
      "v_pk_add_f32 v[170:171], v[170:171], v[112:113] op_sel:[0,1] op_sel_hi:[1,1]\n"
      "v_pk_add_f32 v[172:173], v[172:173], v[112:113] op_sel:[0,1] op_sel_hi:[1,1]\n"
      "v_pk_add_f32 v[174:175], v[174:175], v[112:113] op_sel:[0,1] op_sel_hi:[1,1]\n"
      "s_add_u32 m0, s77, 24576\n"
      "v_pk_add_f32 v[176:177], v[176:177], v[114:115] op_sel:[0,0] op_sel_hi:[1,0]\n"
      "buffer_load_dwordx4 v122, s[24:27], s61 offen nt lds\n"
      "v_pk_add_f32 v[178:179], v[178:179], v[114:115] op_sel:[0,0] op_sel_hi:[1,0]\n"
      "v_pk_add_f32 v[180:181], v[180:181], v[114:115] op_sel:[0,0] op_sel_hi:[1,0]\n"
      "v_pk_add_f32 v[182:183], v[182:183], v[114:115] op_sel:[0,0] op_sel_hi:[1,0]\n"
      "s_add_u32 m0, s77, 25600\n"
      "v_pk_add_f32 v[184:185], v[184:185], v[114:115] op_sel:[0,1] op_sel_hi:[1,1]\n"
      "buffer_load_dwordx4 v123, s[24:27], s61 offen nt lds\n"
      "v_pk_add_f32 v[186:187], v[186:187], v[114:115] op_sel:[0,1] op_sel_hi:[1,1]\n"
      "v_pk_add_f32 v[188:189], v[188:189], v[114:115] op_sel:[0,1] op_sel_hi:[1,1]\n"
      "v_pk_add_f32 v[190:191], v[190:191], v[114:115] op_sel:[0,1] op_sel_hi:[1,1]\n"
      "s_add_u32 s77, s77, 0x8000\n"
      "s_and_b32 s77, s77, 0x1ffff\n"
      "s_add_u32 s61, s61, %[inc]\n"
      "v_exp_f32 v112, v160\n"
      "v_exp_f32 v113, v161\n"
      "v_exp_f32 v114, v162\n"
      "v_cvt_pk_bf16_f32 v208, v112, v113\n"
      "v_exp_f32 v115, v163\n"
      "v_exp_f32 v112, v164\n"
      "v_cvt_pk_bf16_f32 v209, v114, v115\n"
      "v_exp_f32 v113, v165\n"
      "v_or_b32 v116, v208, v209\n"
      "v_exp_f32 v114, v166\n"
      "v_cvt_pk_bf16_f32 v210, v112, v113\n"
      "v_exp_f32 v115, v167\n"
      "v_exp_f32 v112, v168\n"
      "v_cvt_pk_bf16_f32 v211, v114, v115\n"
      "v_exp_f32 v113, v169\n"
      "v_or3_b32 v116, v116, v210, v211\n"
      "v_exp_f32 v114, v170\n"
      "v_cvt_pk_bf16_f32 v212, v112, v113\n"
      "v_exp_f32 v115, v171\n"
      "v_exp_f32 v112, v172\n"
      "v_cvt_pk_bf16_f32 v213, v114, v115\n"
      "v_exp_f32 v113, v173\n"
      "v_or3_b32 v116, v116, v212, v213\n"
      "s_waitcnt lgkmcnt(0)\n"
      "v_cvt_pk_bf16_f32 v214, v112, v113\n"
      "s_sub_u32 s63, s40, 1\n"
      "s_branch ENTRY_%=\n"
      "LOOP1_%=:\n"
      "s_waitcnt vmcnt(12) lgkmcnt(0)\n"
      "s_sub_u32 s67, s68, s90\n"
      "s_cmp_lt_i32 s67, 0\n"
      "s_cbranch_scc1 GW0_%=\n"
      "GX0_%=:\n"
      "s_barrier\n"
      "v_mfma_f32_16x16x32_bf16 v[40:43], a[96:99], v[216:219], v[40:43]\n"
      "s_memrealtime s[68:69]\n"
      "buffer_load_dword v250, v251, s[28:31], 0 offen sc1\n"
      "ds_read_b128 a[64:67], v242 offset:0\n"
      "v_exp_f32 v114, v166\n"
      "v_mfma_f32_16x16x32_bf16 v[44:47], a[96:99], v[220:223], v[44:47]\n"
      "s_add_u32 m0, s77, 16384\n"
      "ds_read_b128 a[68:71], v242 offset:4096\n"
      "ds_read_b128 a[72:75], v243 offset:0\n"
      "v_mfma_f32_16x16x32_bf16 v[60:63], a[100:103], v[220:223], v[60:63]\n"
      "buffer_load_dwordx4 v249, s[24:27], s61 offen nt lds\n"
      "v_exp_f32 v115, v167\n"
      "v_mfma_f32_16x16x32_bf16 v[56:59], a[100:103], v[216:219], v[56:59]\n"
      "s_add_u32 m0, s77, 17408\n"
      "ds_read_b128 a[76:79], v243 offset:4096\n"
      "v_cvt_pk_bf16_f32 v211, v114, v115\n"
      "v_mfma_f32_16x16x32_bf16 a[136:139], a[104:107], v[216:219], a[136:139]\n"
      "buffer_load_dwordx4 v121, s[24:27], s61 offen nt lds\n"
      "v_exp_f32 v112, v168\n"
      "v_mfma_f32_16x16x32_bf16 a[140:143], a[104:107], v[220:223], a[140:143]\n"
      "s_add_u32 m0, s77, 24576\n"
      "ds_read_b128 a[80:83], v242 offset:1024\n"
      "v_or3_b32 v116, v116, v210, v211\n"
      "v_mfma_f32_16x16x32_bf16 a[156:159], a[108:111], v[220:223], a[156:159]\n"
      "buffer_load_dwordx4 v122, s[24:27], s61 offen nt lds\n"
      "v_exp_f32 v113, v169\n"
      "v_mfma_f32_16x16x32_bf16 a[152:155], a[108:111], v[216:219], a[152:155]\n"
      "s_add_u32 m0, s77, 25600\n"
      "ds_read_b128 a[84:87], v242 offset:5120\n"
      "v_cvt_pk_bf16_f32 v212, v112, v113\n"
      "v_mfma_f32_16x16x32_bf16 v[72:75], a[112:115], v[216:219], v[72:75]\n"
      "s_add_u32 s77, s77, 0x8000\n"
      "buffer_load_dwordx4 v123, s[24:27], s61 offen nt lds\n"
      "v_exp_f32 v114, v170\n"
      "v_mfma_f32_16x16x32_bf16 v[76:79], a[112:115], v[220:223], v[76:79]\n"
      "s_and_b32 s77, s77, 0x1ffff\n"
      "ds_read_b128 a[88:91], v243 offset:1024\n"
      "ds_read_b128 a[92:95], v243 offset:5120\n"
      "v_mfma_f32_16x16x32_bf16 v[92:95], a[116:119], v[220:223], v[92:95]\n"
      "s_add_u32 s61, s61, %[inc]\n"
      "v_exp_f32 v115, v171\n"
      "v_mfma_f32_16x16x32_bf16 v[88:91], a[116:119], v[216:219], v[88:91]\n"
      "v_cvt_pk_bf16_f32 v213, v114, v115\n"
      "v_mfma_f32_16x16x32_bf16 a[168:171], a[120:123], v[216:219], a[168:171]\n"
      "v_exp_f32 v112, v172\n"
      "v_mfma_f32_16x16x32_bf16 a[172:175], a[120:123], v[220:223], a[172:175]\n"
      "v_or3_b32 v116, v116, v212, v213\n"
      "v_mfma_f32_16x16x32_bf16 a[188:191], a[124:127], v[220:223], a[188:191]\n"
      "v_exp_f32 v113, v173\n"
      "v_mfma_f32_16x16x32_bf16 a[184:187], a[124:127], v[216:219], a[184:187]\n"
      "s_waitcnt lgkmcnt(0)\n"
      "v_cvt_pk_bf16_f32 v214, v112, v113\n"
      "ENTRY_%=:\n"
      "v_mfma_f32_16x16x32_bf16 v[128:131], a[64:67], a[0:3], v[224:227]\n"
      "s_sub_u32 s67, s68, s66\n"
      "v_exp_f32 v114, v174\n"
      "v_mfma_f32_16x16x32_bf16 v[128:131], a[72:75], a[16:19], v[128:131]\n"
      "s_cmp_gt_i32 s67, s70\n"
      "ds_read_b64_tr_b16 a[96:97], v244 offset:0\n"
      "ds_read_b64_tr_b16 a[98:99], v244 offset:4096\n"
      "v_mfma_f32_16x16x32_bf16 v[128:131], a[80:83], a[32:35], v[128:131]\n"
      "s_cselect_b32 s69, s86, 0\n"
      "v_exp_f32 v115, v175\n"
      "v_mfma_f32_16x16x32_bf16 v[128:131], a[88:91], a[48:51], v[128:131]\n"
      "s_add_u32 s66, s66, s69\n"
      "ds_read_b64_tr_b16 a[100:101], v245 offset:0\n"
      "v_cvt_pk_bf16_f32 v215, v114, v115\n"
      "v_mfma_f32_16x16x32_bf16 v[132:135], a[92:95], a[48:51], v[224:227]\n"
      "s_add_u32 s67, s67, s101\n"
      "v_exp_f32 v112, v176\n"
      "v_mfma_f32_16x16x32_bf16 v[132:135], a[84:87], a[32:35], v[132:135]\n"
      "s_min_i32 s70, s70, s67\n"
      "ds_read_b64_tr_b16 a[102:103], v245 offset:4096\n"
      "v_or3_b32 v116, v116, v214, v215\n"
      "v_mfma_f32_16x16x32_bf16 v[132:135], a[76:79], a[16:19], v[132:135]\n"
      "s_max_i32 s70, s70, 85\n"
      "v_exp_f32 v113, v177\n"
      "v_mfma_f32_16x16x32_bf16 v[132:135], a[68:71], a[0:3], v[132:135]\n"
      "s_add_u32 s66, s66, s86\n"
      "ds_read_b64_tr_b16 a[104:105], v246 offset:0\n"
      "v_cvt_pk_bf16_f32 v216, v112, v113\n"
      "v_mfma_f32_16x16x32_bf16 v[140:143], a[68:71], a[4:7], v[228:231]\n"
      "s_and_b32 s76, s80, 31\n"
      "v_exp_f32 v114, v178\n"
      "v_mfma_f32_16x16x32_bf16 v[140:143], a[76:79], a[20:23], v[140:143]\n"
      "s_lshr_b64 s[80:81], s[80:81], 5\n"
      "ds_read_b64_tr_b16 a[106:107], v246 offset:4096\n"
      "ds_read_b64_tr_b16 a[108:109], v247 offset:0\n"
      "v_mfma_f32_16x16x32_bf16 v[140:143], a[84:87], a[36:39], v[140:143]\n"
      "s_add_u32 s66, s66, s76\n"
      "v_exp_f32 v115, v179\n"
      "v_mfma_f32_16x16x32_bf16 v[140:143], a[92:95], a[52:55], v[140:143]\n"
      "s_sub_u32 s67, s68, s82\n"
      "ds_read_b64_tr_b16 a[110:111], v247 offset:4096\n"
      "v_cvt_pk_bf16_f32 v217, v114, v115\n"
      "v_mfma_f32_16x16x32_bf16 v[136:139], a[88:91], a[52:55], v[228:231]\n"
      "s_min_i32 s76, s67, s83\n"
      "v_exp_f32 v112, v180\n"
      "v_mfma_f32_16x16x32_bf16 v[136:139], a[80:83], a[36:39], v[136:139]\n"
      "s_mov_b32 s83, s67\n"
      "ds_read_b64_tr_b16 a[112:113], v244 offset:1024\n"
      "v_or3_b32 v116, v116, v216, v217\n"
      "v_mfma_f32_16x16x32_bf16 v[136:139], a[72:75], a[20:23], v[136:139]\n"
      "s_sub_u32 s76, s76, 36\n"
      "v_exp_f32 v113, v181\n"
      "v_mfma_f32_16x16x32_bf16 v[136:139], a[64:67], a[4:7], v[136:139]\n"
      "s_min_i32 s76, s76, 148\n"
      "ds_read_b64_tr_b16 a[114:115], v244 offset:5120\n"
      "v_cvt_pk_bf16_f32 v218, v112, v113\n"
      "v_mfma_f32_16x16x32_bf16 v[144:147], a[64:67], a[8:11], v[232:235]\n"
      "s_max_i32 s76, s76, 70\n"
      "v_exp_f32 v114, v182\n"
      "v_mfma_f32_16x16x32_bf16 v[144:147], a[72:75], a[24:27], v[144:147]\n"
      "s_mov_b32 s82, s68\n"
      "ds_read_b64_tr_b16 a[116:117], v245 offset:1024\n"
      "ds_read_b64_tr_b16 a[118:119], v245 offset:5120\n"
      "v_mfma_f32_16x16x32_bf16 v[144:147], a[80:83], a[40:43], v[144:147]\n"
      "s_add_u32 s65, s82, s76\n"
      "v_exp_f32 v115, v183\n"
      "v_mfma_f32_16x16x32_bf16 v[144:147], a[88:91], a[56:59], v[144:147]\n"
      "s_sub_u32 s93, s93, 1\n"
      "ds_read_b64_tr_b16 a[120:121], v246 offset:1024\n"
      "v_cvt_pk_bf16_f32 v219, v114, v115\n"
      "v_mfma_f32_16x16x32_bf16 v[148:151], a[92:95], a[56:59], v[232:235]\n"
      "s_cmp_le_i32 s93, 14\n"
      "v_exp_f32 v112, v184\n"
      "v_mfma_f32_16x16x32_bf16 v[148:151], a[84:87], a[40:43], v[148:151]\n"
      "s_cselect_b32 s86, 107, s86\n"
      "ds_read_b64_tr_b16 a[122:123], v246 offset:5120\n"
      "v_or3_b32 v116, v116, v218, v219\n"
      "v_mfma_f32_16x16x32_bf16 v[148:151], a[76:79], a[24:27], v[148:151]\n"
      "s_sub_u32 s67, s65, s66\n"
      "v_exp_f32 v113, v185\n"
      "v_mfma_f32_16x16x32_bf16 v[148:151], a[68:71], a[8:11], v[148:151]\n"
      "s_cmp_lt_i32 s67, 0\n"
      "ds_read_b64_tr_b16 a[124:125], v247 offset:1024\n"
      "v_cvt_pk_bf16_f32 v220, v112, v113\n"
      "v_mfma_f32_16x16x32_bf16 v[156:159], a[68:71], a[12:15], v[236:239]\n"
      "s_cselect_b32 s88, s66, s65\n"
      "v_exp_f32 v114, v186\n"
      "v_mfma_f32_16x16x32_bf16 v[156:159], a[76:79], a[28:31], v[156:159]\n"
      "s_add_u32 s90, s88, s91\n"
      "ds_read_b64_tr_b16 a[126:127], v247 offset:5120\n"
      "v_mfma_f32_16x16x32_bf16 v[156:159], a[84:87], a[44:47], v[156:159]\n"
      "v_exp_f32 v115, v187\n"
      "v_mfma_f32_16x16x32_bf16 v[156:159], a[92:95], a[60:63], v[156:159]\n"
      "v_cvt_pk_bf16_f32 v221, v114, v115\n"
      "v_mfma_f32_16x16x32_bf16 v[152:155], a[88:91], a[60:63], v[236:239]\n"
      "v_exp_f32 v112, v188\n"
      "v_mfma_f32_16x16x32_bf16 v[152:155], a[80:83], a[44:47], v[152:155]\n"
      "v_or3_b32 v116, v116, v220, v221\n"
      "v_mfma_f32_16x16x32_bf16 v[152:155], a[72:75], a[28:31], v[152:155]\n"
      "v_exp_f32 v113, v189\n"
      "v_mfma_f32_16x16x32_bf16 v[152:155], a[64:67], a[12:15], v[152:155]\n"
      "s_waitcnt lgkmcnt(10)\n"
      "v_cvt_pk_bf16_f32 v222, v112, v113\n"
      "v_mfma_f32_16x16x32_bf16 v[32:35], a[96:99], v[192:195], v[32:35]\n"
      "v_exp_f32 v114, v190\n"
      "v_mfma_f32_16x16x32_bf16 v[48:51], a[100:103], v[192:195], v[48:51]\n"
      "v_mfma_f32_16x16x32_bf16 a[128:131], a[104:107], v[192:195], a[128:131]\n"
      "s_waitcnt lgkmcnt(6)\n"
      "v_exp_f32 v115, v191\n"
      "v_mfma_f32_16x16x32_bf16 a[144:147], a[108:111], v[192:195], a[144:147]\n"
      "v_cvt_pk_bf16_f32 v223, v114, v115\n"
      "v_mfma_f32_16x16x32_bf16 v[64:67], a[112:115], v[192:195], v[64:67]\n"
      "s_waitcnt lgkmcnt(2)\n"
      "v_mfma_f32_16x16x32_bf16 v[80:83], a[116:119], v[192:195], v[80:83]\n"
      "v_or3_b32 v116, v116, v222, v223\n"
      "v_mfma_f32_16x16x32_bf16 a[160:163], a[120:123], v[192:195], a[160:163]\n"
      "s_mov_b32 s64, 0\n"
      "s_waitcnt lgkmcnt(0)\n"
      "v_and_b32 v116, 0x40004000, v116\n"
      "v_mfma_f32_16x16x32_bf16 a[176:179], a[124:127], v[192:195], a[176:179]\n"
      "v_cmp_ne_u32 vcc, 0, v116\n"
      "s_cbranch_vccnz FB1_%=\n"
      "RET1_0_%=:\n"
      "v_mfma_f32_16x16x32_bf16 a[180:183], a[124:127], v[196:199], a[180:183]\n"
      "v_exp_f32 v112, v128\n"
      "s_barrier\n"
      "v_mfma_f32_16x16x32_bf16 a[164:167], a[120:123], v[196:199], a[164:167]\n"
      "v_mfma_f32_16x16x32_bf16 a[192:195], a[196:199], v[208:211], a[192:195]\n"
      "s_add_u32 m0, s62, 0\n"
      "v_exp_f32 v113, v129\n"
      "v_mfma_f32_16x16x32_bf16 v[84:87], a[116:119], v[196:199], v[84:87]\n"
      "buffer_load_dwordx4 v248, s[20:23], s60 offen nt lds\n"
      "v_cvt_pk_bf16_f32 v192, v112, v113\n"
      "v_mfma_f32_16x16x32_bf16 v[68:71], a[112:115], v[196:199], v[68:71]\n"
      "s_add_u32 m0, s62, 896\n"
      "v_exp_f32 v114, v130\n"
      "v_mfma_f32_16x16x32_bf16 a[192:195], a[200:203], v[212:215], a[192:195]\n"
      "buffer_load_dwordx4 v248, s[20:23], s60 offen offset:128 nt lds\n"
      "v_mfma_f32_16x16x32_bf16 a[148:151], a[108:111], v[196:199], a[148:151]\n"
      "s_add_u32 m0, s62, 8192\n"
      "v_exp_f32 v115, v131\n"
      "v_mfma_f32_16x16x32_bf16 a[132:135], a[104:107], v[196:199], a[132:135]\n"
      "s_memrealtime s[68:69]\n"
      "buffer_load_dwordx4 v120, s[20:23], s60 offen nt lds\n"
      "v_cvt_pk_bf16_f32 v193, v114, v115\n"
      "v_mfma_f32_16x16x32_bf16 a[192:195], a[204:207], v[216:219], a[192:195]\n"
      "s_add_u32 m0, s62, 9088\n"
      "v_exp_f32 v112, v132\n"
      "v_mfma_f32_16x16x32_bf16 v[52:55], a[100:103], v[196:199], v[52:55]\n"
      "s_add_u32 s62, s62, 0x8000\n"
      "buffer_load_dwordx4 v120, s[20:23], s60 offen offset:128 nt lds\n"
      "v_or_b32 v116, v192, v193\n"
      "v_mfma_f32_16x16x32_bf16 v[36:39], a[96:99], v[196:199], v[36:39]\n"
      "s_and_b32 s62, s62, 0x1ffff\n"
      "v_exp_f32 v113, v133\n"
      "v_mfma_f32_16x16x32_bf16 a[192:195], a[208:211], v[220:223], a[192:195]\n"
      "s_add_u32 s60, s60, s78\n"
      "v_cvt_pk_bf16_f32 v194, v112, v113\n"
      "s_waitcnt vmcnt(12) lgkmcnt(0)\n"
      "s_sub_u32 s67, s68, s90\n"
      "s_cmp_lt_i32 s67, 0\n"
      "s_cbranch_scc1 GW1_%=\n"
      "GX1_%=:\n"
      "s_barrier\n"
      "v_mfma_f32_16x16x32_bf16 v[40:43], a[96:99], v[200:203], v[40:43]\n"
      "s_memrealtime s[68:69]\n"
      "ds_read_b128 a[64:67], v242 offset:32768\n"
      "v_exp_f32 v114, v134\n"
      "v_mfma_f32_16x16x32_bf16 v[44:47], a[96:99], v[204:207], v[44:47]\n"
      "s_add_u32 m0, s77, 16384\n"
      "ds_read_b128 a[68:71], v242 offset:36864\n"
      "ds_read_b128 a[72:75], v243 offset:32768\n"
      "v_mfma_f32_16x16x32_bf16 v[60:63], a[100:103], v[204:207], v[60:63]\n"
      "buffer_load_dwordx4 v249, s[24:27], s61 offen nt lds\n"
      "v_exp_f32 v115, v135\n"
      "v_mfma_f32_16x16x32_bf16 v[56:59], a[100:103], v[200:203], v[56:59]\n"
      "s_add_u32 m0, s77, 17408\n"
      "ds_read_b128 a[76:79], v243 offset:36864\n"
      "v_cvt_pk_bf16_f32 v195, v114, v115\n"
      "v_mfma_f32_16x16x32_bf16 a[136:139], a[104:107], v[200:203], a[136:139]\n"
      "buffer_load_dwordx4 v121, s[24:27], s61 offen nt lds\n"
      "v_exp_f32 v112, v136\n"
      "v_mfma_f32_16x16x32_bf16 a[140:143], a[104:107], v[204:207], a[140:143]\n"
      "s_add_u32 m0, s77, 24576\n"
      "ds_read_b128 a[80:83], v242 offset:33792\n"
      "v_or3_b32 v116, v116, v194, v195\n"
      "v_mfma_f32_16x16x32_bf16 a[156:159], a[108:111], v[204:207], a[156:159]\n"
      "buffer_load_dwordx4 v122, s[24:27], s61 offen nt lds\n"
      "v_exp_f32 v113, v137\n"
      "v_mfma_f32_16x16x32_bf16 a[152:155], a[108:111], v[200:203], a[152:155]\n"
      "s_add_u32 m0, s77, 25600\n"
      "ds_read_b128 a[84:87], v242 offset:37888\n"
      "v_cvt_pk_bf16_f32 v196, v112, v113\n"
      "v_mfma_f32_16x16x32_bf16 v[72:75], a[112:115], v[200:203], v[72:75]\n"
      "s_add_u32 s77, s77, 0x8000\n"
      "buffer_load_dwordx4 v123, s[24:27], s61 offen nt lds\n"
      "v_exp_f32 v114, v138\n"
      "v_mfma_f32_16x16x32_bf16 v[76:79], a[112:115], v[204:207], v[76:79]\n"
      "s_and_b32 s77, s77, 0x1ffff\n"
      "ds_read_b128 a[88:91], v243 offset:33792\n"
      "ds_read_b128 a[92:95], v243 offset:37888\n"
      "v_mfma_f32_16x16x32_bf16 v[92:95], a[116:119], v[204:207], v[92:95]\n"
      "s_add_u32 s61, s61, s78\n"
      "v_exp_f32 v115, v139\n"
      "v_mfma_f32_16x16x32_bf16 v[88:91], a[116:119], v[200:203], v[88:91]\n"
      "v_cvt_pk_bf16_f32 v197, v114, v115\n"
      "v_mfma_f32_16x16x32_bf16 a[168:171], a[120:123], v[200:203], a[168:171]\n"
      "v_exp_f32 v112, v140\n"
      "v_mfma_f32_16x16x32_bf16 a[172:175], a[120:123], v[204:207], a[172:175]\n"
      "v_or3_b32 v116, v116, v196, v197\n"
      "v_mfma_f32_16x16x32_bf16 a[188:191], a[124:127], v[204:207], a[188:191]\n"
      "v_exp_f32 v113, v141\n"
      "v_mfma_f32_16x16x32_bf16 a[184:187], a[124:127], v[200:203], a[184:187]\n"
      "s_waitcnt lgkmcnt(0)\n"
      "v_cvt_pk_bf16_f32 v198, v112, v113\n"
      "v_mfma_f32_16x16x32_bf16 v[160:163], a[64:67], a[0:3], v[224:227]\n"
      "s_sub_u32 s67, s68, s66\n"
      "v_exp_f32 v114, v142\n"
      "v_mfma_f32_16x16x32_bf16 v[160:163], a[72:75], a[16:19], v[160:163]\n"
      "s_cmp_gt_i32 s67, s70\n"
      "ds_read_b64_tr_b16 a[96:97], v244 offset:32768\n"
      "ds_read_b64_tr_b16 a[98:99], v244 offset:36864\n"
      "v_mfma_f32_16x16x32_bf16 v[160:163], a[80:83], a[32:35], v[160:163]\n"
      "s_cselect_b32 s69, s86, 0\n"
      "v_exp_f32 v115, v143\n"
      "v_mfma_f32_16x16x32_bf16 v[160:163], a[88:91], a[48:51], v[160:163]\n"
      "s_add_u32 s66, s66, s69\n"
      "ds_read_b64_tr_b16 a[100:101], v245 offset:32768\n"
      "v_cvt_pk_bf16_f32 v199, v114, v115\n"
      "v_mfma_f32_16x16x32_bf16 v[164:167], a[92:95], a[48:51], v[224:227]\n"
      "s_add_u32 s67, s67, s101\n"
      "v_exp_f32 v112, v144\n"
      "v_mfma_f32_16x16x32_bf16 v[164:167], a[84:87], a[32:35], v[164:167]\n"
      "s_min_i32 s70, s70, s67\n"
      "ds_read_b64_tr_b16 a[102:103], v245 offset:36864\n"
      "v_or3_b32 v116, v116, v198, v199\n"
      "v_mfma_f32_16x16x32_bf16 v[164:167], a[76:79], a[16:19], v[164:167]\n"
      "s_max_i32 s70, s70, 85\n"
      "v_exp_f32 v113, v145\n"
      "v_mfma_f32_16x16x32_bf16 v[164:167], a[68:71], a[0:3], v[164:167]\n"
      "s_add_u32 s66, s66, s86\n"
      "ds_read_b64_tr_b16 a[104:105], v246 offset:32768\n"
      "v_cvt_pk_bf16_f32 v200, v112, v113\n"
      "v_mfma_f32_16x16x32_bf16 v[172:175], a[68:71], a[4:7], v[228:231]\n"
      "s_and_b32 s76, s80, 31\n"
      "v_exp_f32 v114, v146\n"
      "v_mfma_f32_16x16x32_bf16 v[172:175], a[76:79], a[20:23], v[172:175]\n"
      "s_lshr_b64 s[80:81], s[80:81], 5\n"
      "ds_read_b64_tr_b16 a[106:107], v246 offset:36864\n"
      "ds_read_b64_tr_b16 a[108:109], v247 offset:32768\n"
      "v_mfma_f32_16x16x32_bf16 v[172:175], a[84:87], a[36:39], v[172:175]\n"
      "s_add_u32 s66, s66, s76\n"
      "v_exp_f32 v115, v147\n"
      "v_mfma_f32_16x16x32_bf16 v[172:175], a[92:95], a[52:55], v[172:175]\n"
      "s_sub_u32 s67, s68, s82\n"
      "ds_read_b64_tr_b16 a[110:111], v247 offset:36864\n"
      "v_cvt_pk_bf16_f32 v201, v114, v115\n"
      "v_mfma_f32_16x16x32_bf16 v[168:171], a[88:91], a[52:55], v[228:231]\n"
      "s_min_i32 s76, s67, s83\n"
      "v_exp_f32 v112, v148\n"
      "v_mfma_f32_16x16x32_bf16 v[168:171], a[80:83], a[36:39], v[168:171]\n"
      "s_mov_b32 s83, s67\n"
      "ds_read_b64_tr_b16 a[112:113], v244 offset:33792\n"
      "v_or3_b32 v116, v116, v200, v201\n"
      "v_mfma_f32_16x16x32_bf16 v[168:171], a[72:75], a[20:23], v[168:171]\n"
      "s_sub_u32 s76, s76, 36\n"
      "v_exp_f32 v113, v149\n"
      "v_mfma_f32_16x16x32_bf16 v[168:171], a[64:67], a[4:7], v[168:171]\n"
      "s_min_i32 s76, s76, 148\n"
      "ds_read_b64_tr_b16 a[114:115], v244 offset:37888\n"
      "v_cvt_pk_bf16_f32 v202, v112, v113\n"
      "v_mfma_f32_16x16x32_bf16 v[176:179], a[64:67], a[8:11], v[232:235]\n"
      "s_max_i32 s76, s76, 70\n"
      "v_exp_f32 v114, v150\n"
      "v_mfma_f32_16x16x32_bf16 v[176:179], a[72:75], a[24:27], v[176:179]\n"
      "s_mov_b32 s82, s68\n"
      "ds_read_b64_tr_b16 a[116:117], v245 offset:33792\n"
      "ds_read_b64_tr_b16 a[118:119], v245 offset:37888\n"
      "v_mfma_f32_16x16x32_bf16 v[176:179], a[80:83], a[40:43], v[176:179]\n"
      "s_add_u32 s65, s82, s76\n"
      "v_exp_f32 v115, v151\n"
      "v_mfma_f32_16x16x32_bf16 v[176:179], a[88:91], a[56:59], v[176:179]\n"
      "s_sub_u32 s93, s93, 1\n"
      "ds_read_b64_tr_b16 a[120:121], v246 offset:33792\n"
      "v_cvt_pk_bf16_f32 v203, v114, v115\n"
      "v_mfma_f32_16x16x32_bf16 v[180:183], a[92:95], a[56:59], v[232:235]\n"
      "s_cmp_le_i32 s93, 14\n"
      "v_exp_f32 v112, v152\n"
      "v_mfma_f32_16x16x32_bf16 v[180:183], a[84:87], a[40:43], v[180:183]\n"
      "s_cselect_b32 s86, 107, s86\n"
      "ds_read_b64_tr_b16 a[122:123], v246 offset:37888\n"
      "v_or3_b32 v116, v116, v202, v203\n"
      "v_mfma_f32_16x16x32_bf16 v[180:183], a[76:79], a[24:27], v[180:183]\n"
      "s_sub_u32 s67, s65, s66\n"
      "v_exp_f32 v113, v153\n"
      "v_mfma_f32_16x16x32_bf16 v[180:183], a[68:71], a[8:11], v[180:183]\n"
      "s_cmp_lt_i32 s67, 0\n"
      "ds_read_b64_tr_b16 a[124:125], v247 offset:33792\n"
      "v_cvt_pk_bf16_f32 v204, v112, v113\n"
      "v_mfma_f32_16x16x32_bf16 v[188:191], a[68:71], a[12:15], v[236:239]\n"
      "s_cselect_b32 s88, s66, s65\n"
      "v_exp_f32 v114, v154\n"
      "v_mfma_f32_16x16x32_bf16 v[188:191], a[76:79], a[28:31], v[188:191]\n"
      "s_add_u32 s90, s88, s91\n"
      "ds_read_b64_tr_b16 a[126:127], v247 offset:37888\n"
      "v_mfma_f32_16x16x32_bf16 v[188:191], a[84:87], a[44:47], v[188:191]\n"
      "v_exp_f32 v115, v155\n"
      "v_mfma_f32_16x16x32_bf16 v[188:191], a[92:95], a[60:63], v[188:191]\n"
      "v_cvt_pk_bf16_f32 v205, v114, v115\n"
      "v_mfma_f32_16x16x32_bf16 v[184:187], a[88:91], a[60:63], v[236:239]\n"
      "v_exp_f32 v112, v156\n"
      "v_mfma_f32_16x16x32_bf16 v[184:187], a[80:83], a[44:47], v[184:187]\n"
      "v_or3_b32 v116, v116, v204, v205\n"
      "v_mfma_f32_16x16x32_bf16 v[184:187], a[72:75], a[28:31], v[184:187]\n"
      "v_exp_f32 v113, v157\n"
      "v_mfma_f32_16x16x32_bf16 v[184:187], a[64:67], a[12:15], v[184:187]\n"
      "s_waitcnt lgkmcnt(10)\n"
      "v_cvt_pk_bf16_f32 v206, v112, v113\n"
      "v_mfma_f32_16x16x32_bf16 v[32:35], a[96:99], v[208:211], v[32:35]\n"
      "v_exp_f32 v114, v158\n"
      "v_mfma_f32_16x16x32_bf16 v[48:51], a[100:103], v[208:211], v[48:51]\n"
      "v_mfma_f32_16x16x32_bf16 a[128:131], a[104:107], v[208:211], a[128:131]\n"
      "s_waitcnt lgkmcnt(6)\n"
      "v_exp_f32 v115, v159\n"
      "v_mfma_f32_16x16x32_bf16 a[144:147], a[108:111], v[208:211], a[144:147]\n"
      "v_cvt_pk_bf16_f32 v207, v114, v115\n"
      "v_mfma_f32_16x16x32_bf16 v[64:67], a[112:115], v[208:211], v[64:67]\n"
      "s_waitcnt lgkmcnt(2)\n"
      "v_mfma_f32_16x16x32_bf16 v[80:83], a[116:119], v[208:211], v[80:83]\n"
      "v_or3_b32 v116, v116, v206, v207\n"
      "v_mfma_f32_16x16x32_bf16 a[160:163], a[120:123], v[208:211], a[160:163]\n"
      "s_mov_b32 s64, 0\n"
      "s_waitcnt lgkmcnt(0)\n"
      "v_and_b32 v116, 0x40004000, v116\n"
      "v_mfma_f32_16x16x32_bf16 a[176:179], a[124:127], v[208:211], a[176:179]\n"
      "v_cmp_ne_u32 vcc, 0, v116\n"
      "s_cbranch_vccnz FB0_%=\n"
      "RET0_0_%=:\n"
      "v_mfma_f32_16x16x32_bf16 a[180:183], a[124:127], v[212:215], a[180:183]\n"
      "v_exp_f32 v112, v160\n"
      "s_barrier\n"
      "v_mfma_f32_16x16x32_bf16 a[164:167], a[120:123], v[212:215], a[164:167]\n"
      "v_xor_b32 v242, 0x10000, v242\n"
      "v_mfma_f32_16x16x32_bf16 a[192:195], a[196:199], v[192:195], a[192:195]\n"
      "s_add_u32 m0, s62, 0\n"
      "v_exp_f32 v113, v161\n"
      "v_mfma_f32_16x16x32_bf16 v[84:87], a[116:119], v[212:215], v[84:87]\n"
      "buffer_load_dwordx4 v248, s[20:23], s60 offen nt lds\n"
      "v_cvt_pk_bf16_f32 v208, v112, v113\n"
      "v_xor_b32 v243, 0x10000, v243\n"
      "v_mfma_f32_16x16x32_bf16 v[68:71], a[112:115], v[212:215], v[68:71]\n"
      "s_add_u32 m0, s62, 896\n"
      "v_exp_f32 v114, v162\n"
      "v_mfma_f32_16x16x32_bf16 a[192:195], a[200:203], v[196:199], a[192:195]\n"
      "buffer_load_dwordx4 v248, s[20:23], s60 offen offset:128 nt lds\n"
      "v_xor_b32 v244, 0x10000, v244\n"
      "v_mfma_f32_16x16x32_bf16 a[148:151], a[108:111], v[212:215], a[148:151]\n"
      "s_add_u32 m0, s62, 8192\n"
      "v_exp_f32 v115, v163\n"
      "v_mfma_f32_16x16x32_bf16 a[132:135], a[104:107], v[212:215], a[132:135]\n"
      "s_memrealtime s[68:69]\n"
      "buffer_load_dwordx4 v120, s[20:23], s60 offen nt lds\n"
      "v_cvt_pk_bf16_f32 v209, v114, v115\n"
      "v_xor_b32 v245, 0x10000, v245\n"
      "v_mfma_f32_16x16x32_bf16 a[192:195], a[204:207], v[200:203], a[192:195]\n"
      "s_add_u32 m0, s62, 9088\n"
      "v_exp_f32 v112, v164\n"
      "v_mfma_f32_16x16x32_bf16 v[52:55], a[100:103], v[212:215], v[52:55]\n"
      "s_add_u32 s62, s62, 0x8000\n"
      "buffer_load_dwordx4 v120, s[20:23], s60 offen offset:128 nt lds\n"
      "v_or_b32 v116, v208, v209\n"
      "v_xor_b32 v246, 0x10000, v246\n"
      "v_mfma_f32_16x16x32_bf16 v[36:39], a[96:99], v[212:215], v[36:39]\n"
      "s_and_b32 s62, s62, 0x1ffff\n"
      "s_add_u32 s60, s60, %[inc]\n"
      "v_exp_f32 v113, v165\n"
      "v_mfma_f32_16x16x32_bf16 a[192:195], a[208:211], v[204:207], a[192:195]\n"
      "s_sub_u32 s63, s63, 1\n"
      "v_cvt_pk_bf16_f32 v210, v112, v113\n"
      "v_xor_b32 v247, 0x10000, v247\n"
      "s_cbranch_scc0 LOOP1_%=\n"
      "v_readfirstlane_b32 s72, v240\n"
      "v_readfirstlane_b32 s79, v241\n"
      "s_waitcnt vmcnt(12) lgkmcnt(0)\n"
      "s_sub_u32 s67, s68, s90\n"
      "s_cmp_lt_i32 s67, 0\n"
      "s_cbranch_scc1 GW2_%=\n"
      "GX2_%=:\n"
      "s_barrier\n"
      "v_mfma_f32_16x16x32_bf16 v[40:43], a[96:99], v[216:219], v[40:43]\n"
      "s_memrealtime s[68:69]\n"
      "buffer_load_dword v250, v251, s[28:31], 0 offen sc1\n"
      "ds_read_b128 a[64:67], v242 offset:0\n"
      "v_exp_f32 v114, v166\n"
      "v_mfma_f32_16x16x32_bf16 v[44:47], a[96:99], v[220:223], v[44:47]\n"
      "s_add_u32 m0, s77, 16384\n"
      "ds_read_b128 a[68:71], v242 offset:4096\n"
      "ds_read_b128 a[72:75], v243 offset:0\n"
      "v_mfma_f32_16x16x32_bf16 v[60:63], a[100:103], v[220:223], v[60:63]\n"
      "buffer_load_dwordx4 v249, s[24:27], s61 offen nt lds\n"
      "v_exp_f32 v115, v167\n"
      "v_mfma_f32_16x16x32_bf16 v[56:59], a[100:103], v[216:219], v[56:59]\n"
      "s_add_u32 m0, s77, 17408\n"
      "ds_read_b128 a[76:79], v243 offset:4096\n"
      "v_cvt_pk_bf16_f32 v211, v114, v115\n"
      "v_mfma_f32_16x16x32_bf16 a[136:139], a[104:107], v[216:219], a[136:139]\n"
      "buffer_load_dwordx4 v121, s[24:27], s61 offen nt lds\n"
      "v_exp_f32 v112, v168\n"
      "v_mfma_f32_16x16x32_bf16 a[140:143], a[104:107], v[220:223], a[140:143]\n"
      "s_add_u32 m0, s77, 24576\n"
      "ds_read_b128 a[80:83], v242 offset:1024\n"
      "v_or3_b32 v116, v116, v210, v211\n"
      "v_mfma_f32_16x16x32_bf16 a[156:159], a[108:111], v[220:223], a[156:159]\n"
      "buffer_load_dwordx4 v122, s[24:27], s61 offen nt lds\n"
      "v_exp_f32 v113, v169\n"
      "v_mfma_f32_16x16x32_bf16 a[152:155], a[108:111], v[216:219], a[152:155]\n"
      "s_add_u32 m0, s77, 25600\n"
      "ds_read_b128 a[84:87], v242 offset:5120\n"
      "v_cvt_pk_bf16_f32 v212, v112, v113\n"
      "v_mfma_f32_16x16x32_bf16 v[72:75], a[112:115], v[216:219], v[72:75]\n"
      "s_add_u32 s77, s77, 0x8000\n"
      "buffer_load_dwordx4 v123, s[24:27], s61 offen nt lds\n"
      "v_exp_f32 v114, v170\n"
      "v_mfma_f32_16x16x32_bf16 v[76:79], a[112:115], v[220:223], v[76:79]\n"
      "s_and_b32 s77, s77, 0x1ffff\n"
      "ds_read_b128 a[88:91], v243 offset:1024\n"
      "ds_read_b128 a[92:95], v243 offset:5120\n"
      "v_mfma_f32_16x16x32_bf16 v[92:95], a[116:119], v[220:223], v[92:95]\n"
      "s_add_u32 s61, s61, %[inc]\n"
      "v_exp_f32 v115, v171\n"
      "v_mfma_f32_16x16x32_bf16 v[88:91], a[116:119], v[216:219], v[88:91]\n"
      "v_cvt_pk_bf16_f32 v213, v114, v115\n"
      "v_mfma_f32_16x16x32_bf16 a[168:171], a[120:123], v[216:219], a[168:171]\n"
      "v_exp_f32 v112, v172\n"
      "v_mfma_f32_16x16x32_bf16 a[172:175], a[120:123], v[220:223], a[172:175]\n"
      "v_or3_b32 v116, v116, v212, v213\n"
      "v_mfma_f32_16x16x32_bf16 a[188:191], a[124:127], v[220:223], a[188:191]\n"
      "v_exp_f32 v113, v173\n"
      "v_mfma_f32_16x16x32_bf16 a[184:187], a[124:127], v[216:219], a[184:187]\n"
      "s_waitcnt lgkmcnt(0)\n"
      "v_cvt_pk_bf16_f32 v214, v112, v113\n"
      "v_mfma_f32_16x16x32_bf16 v[128:131], a[64:67], a[0:3], v[224:227]\n"
      "s_sub_u32 s67, s68, s66\n"
      "v_exp_f32 v114, v174\n"
      "v_mfma_f32_16x16x32_bf16 v[128:131], a[72:75], a[16:19], v[128:131]\n"
      "s_cmp_gt_i32 s67, s70\n"
      "ds_read_b64_tr_b16 a[96:97], v244 offset:0\n"
      "ds_read_b64_tr_b16 a[98:99], v244 offset:4096\n"
      "v_mfma_f32_16x16x32_bf16 v[128:131], a[80:83], a[32:35], v[128:131]\n"
      "s_cselect_b32 s69, s86, 0\n"
      "v_exp_f32 v115, v175\n"
      "v_mfma_f32_16x16x32_bf16 v[128:131], a[88:91], a[48:51], v[128:131]\n"
      "s_add_u32 s66, s66, s69\n"
      "ds_read_b64_tr_b16 a[100:101], v245 offset:0\n"
      "v_cvt_pk_bf16_f32 v215, v114, v115\n"
      "v_mfma_f32_16x16x32_bf16 v[132:135], a[92:95], a[48:51], v[224:227]\n"
      "s_add_u32 s67, s67, s101\n"
      "v_exp_f32 v112, v176\n"
      "v_mfma_f32_16x16x32_bf16 v[132:135], a[84:87], a[32:35], v[132:135]\n"
      "s_min_i32 s70, s70, s67\n"
      "ds_read_b64_tr_b16 a[102:103], v245 offset:4096\n"
      "v_or3_b32 v116, v116, v214, v215\n"
      "v_mfma_f32_16x16x32_bf16 v[132:135], a[76:79], a[16:19], v[132:135]\n"
      "s_max_i32 s70, s70, 85\n"
      "v_exp_f32 v113, v177\n"
      "v_mfma_f32_16x16x32_bf16 v[132:135], a[68:71], a[0:3], v[132:135]\n"
      "s_add_u32 s66, s66, s86\n"
      "ds_read_b64_tr_b16 a[104:105], v246 offset:0\n"
      "v_cvt_pk_bf16_f32 v216, v112, v113\n"
      "v_mfma_f32_16x16x32_bf16 v[140:143], a[68:71], a[4:7], v[228:231]\n"
      "s_and_b32 s76, s80, 31\n"
      "v_exp_f32 v114, v178\n"
      "v_mfma_f32_16x16x32_bf16 v[140:143], a[76:79], a[20:23], v[140:143]\n"
      "s_lshr_b64 s[80:81], s[80:81], 5\n"
      "ds_read_b64_tr_b16 a[106:107], v246 offset:4096\n"
      "ds_read_b64_tr_b16 a[108:109], v247 offset:0\n"
      "v_mfma_f32_16x16x32_bf16 v[140:143], a[84:87], a[36:39], v[140:143]\n"
      "s_add_u32 s66, s66, s76\n"
      "v_exp_f32 v115, v179\n"
      "v_mfma_f32_16x16x32_bf16 v[140:143], a[92:95], a[52:55], v[140:143]\n"
      "s_sub_u32 s67, s68, s82\n"
      "ds_read_b64_tr_b16 a[110:111], v247 offset:4096\n"
      "v_cvt_pk_bf16_f32 v217, v114, v115\n"
      "v_mfma_f32_16x16x32_bf16 v[136:139], a[88:91], a[52:55], v[228:231]\n"
      "s_min_i32 s76, s67, s83\n"
      "v_exp_f32 v112, v180\n"
      "v_mfma_f32_16x16x32_bf16 v[136:139], a[80:83], a[36:39], v[136:139]\n"
      "s_mov_b32 s83, s67\n"
      "ds_read_b64_tr_b16 a[112:113], v244 offset:1024\n"
      "v_or3_b32 v116, v116, v216, v217\n"
      "v_mfma_f32_16x16x32_bf16 v[136:139], a[72:75], a[20:23], v[136:139]\n"
      "s_sub_u32 s76, s76, 36\n"
      "v_exp_f32 v113, v181\n"
      "v_mfma_f32_16x16x32_bf16 v[136:139], a[64:67], a[4:7], v[136:139]\n"
      "s_min_i32 s76, s76, 148\n"
      "ds_read_b64_tr_b16 a[114:115], v244 offset:5120\n"
      "v_cvt_pk_bf16_f32 v218, v112, v113\n"
      "v_mfma_f32_16x16x32_bf16 v[144:147], a[64:67], a[8:11], v[232:235]\n"
      "s_max_i32 s76, s76, 70\n"
      "v_exp_f32 v114, v182\n"
      "v_mfma_f32_16x16x32_bf16 v[144:147], a[72:75], a[24:27], v[144:147]\n"
      "s_mov_b32 s82, s68\n"
      "ds_read_b64_tr_b16 a[116:117], v245 offset:1024\n"
      "ds_read_b64_tr_b16 a[118:119], v245 offset:5120\n"
      "v_mfma_f32_16x16x32_bf16 v[144:147], a[80:83], a[40:43], v[144:147]\n"
      "s_lshr_b32 s69, s86, 1\n"
      "v_exp_f32 v115, v183\n"
      "v_mfma_f32_16x16x32_bf16 v[144:147], a[88:91], a[56:59], v[144:147]\n"
      "s_add_u32 s66, s66, s69\n"
      "ds_read_b64_tr_b16 a[120:121], v246 offset:1024\n"
      "v_cvt_pk_bf16_f32 v219, v114, v115\n"
      "v_mfma_f32_16x16x32_bf16 v[148:151], a[92:95], a[56:59], v[232:235]\n"
      "s_add_u32 s82, s82, s69\n"
      "v_exp_f32 v112, v184\n"
      "v_mfma_f32_16x16x32_bf16 v[148:151], a[84:87], a[40:43], v[148:151]\n"
      "s_add_u32 s65, s82, s76\n"
      "ds_read_b64_tr_b16 a[122:123], v246 offset:5120\n"
      "v_or3_b32 v116, v116, v218, v219\n"
      "v_mfma_f32_16x16x32_bf16 v[148:151], a[76:79], a[24:27], v[148:151]\n"
      "s_sub_u32 s93, s93, 1\n"
      "v_exp_f32 v113, v185\n"
      "v_mfma_f32_16x16x32_bf16 v[148:151], a[68:71], a[8:11], v[148:151]\n"
      "s_cmp_le_i32 s93, 14\n"
      "ds_read_b64_tr_b16 a[124:125], v247 offset:1024\n"
      "v_cvt_pk_bf16_f32 v220, v112, v113\n"
      "v_mfma_f32_16x16x32_bf16 v[156:159], a[68:71], a[12:15], v[236:239]\n"
      "s_cselect_b32 s86, 107, s86\n"
      "v_exp_f32 v114, v186\n"
      "v_mfma_f32_16x16x32_bf16 v[156:159], a[76:79], a[28:31], v[156:159]\n"
      "s_sub_u32 s67, s65, s66\n"
      "ds_read_b64_tr_b16 a[126:127], v247 offset:5120\n"
      "v_mfma_f32_16x16x32_bf16 v[156:159], a[84:87], a[44:47], v[156:159]\n"
      "s_cmp_lt_i32 s67, 0\n"
      "v_exp_f32 v115, v187\n"
      "v_mfma_f32_16x16x32_bf16 v[156:159], a[92:95], a[60:63], v[156:159]\n"
      "s_cselect_b32 s88, s66, s65\n"
      "v_cvt_pk_bf16_f32 v221, v114, v115\n"
      "v_mfma_f32_16x16x32_bf16 v[152:155], a[88:91], a[60:63], v[236:239]\n"
      "s_add_u32 s90, s88, s91\n"
      "v_exp_f32 v112, v188\n"
      "v_mfma_f32_16x16x32_bf16 v[152:155], a[80:83], a[44:47], v[152:155]\n"
      "v_or3_b32 v116, v116, v220, v221\n"
      "v_mfma_f32_16x16x32_bf16 v[152:155], a[72:75], a[28:31], v[152:155]\n"
      "v_exp_f32 v113, v189\n"
      "v_mfma_f32_16x16x32_bf16 v[152:155], a[64:67], a[12:15], v[152:155]\n"
      "s_waitcnt lgkmcnt(10)\n"
      "v_cvt_pk_bf16_f32 v222, v112, v113\n"
      "v_mfma_f32_16x16x32_bf16 v[32:35], a[96:99], v[192:195], v[32:35]\n"
      "v_exp_f32 v114, v190\n"
      "v_mfma_f32_16x16x32_bf16 v[48:51], a[100:103], v[192:195], v[48:51]\n"
      "v_mfma_f32_16x16x32_bf16 a[128:131], a[104:107], v[192:195], a[128:131]\n"
      "s_waitcnt lgkmcnt(6)\n"
      "v_exp_f32 v115, v191\n"
      "v_mfma_f32_16x16x32_bf16 a[144:147], a[108:111], v[192:195], a[144:147]\n"
      "v_cvt_pk_bf16_f32 v223, v114, v115\n"
      "v_mfma_f32_16x16x32_bf16 v[64:67], a[112:115], v[192:195], v[64:67]\n"
      "s_waitcnt lgkmcnt(2)\n"
      "v_mfma_f32_16x16x32_bf16 v[80:83], a[116:119], v[192:195], v[80:83]\n"
      "v_or3_b32 v116, v116, v222, v223\n"
      "v_mfma_f32_16x16x32_bf16 a[160:163], a[120:123], v[192:195], a[160:163]\n"
      "s_mov_b32 s64, 1\n"
      "s_waitcnt lgkmcnt(0)\n"
      "v_and_b32 v116, 0x40004000, v116\n"
      "v_mfma_f32_16x16x32_bf16 a[176:179], a[124:127], v[192:195], a[176:179]\n"
      "v_cmp_ne_u32 vcc, 0, v116\n"
      "s_cbranch_vccnz FB1_%=\n"
      "RET1_1_%=:\n"
      "v_mfma_f32_16x16x32_bf16 a[180:183], a[124:127], v[196:199], a[180:183]\n"
      "v_exp_f32 v112, v128\n"
      "s_barrier\n"
      "v_mfma_f32_16x16x32_bf16 a[164:167], a[120:123], v[196:199], a[164:167]\n"
      "v_mfma_f32_16x16x32_bf16 a[192:195], a[196:199], v[208:211], a[192:195]\n"
      "s_add_u32 m0, s62, 0\n"
      "v_exp_f32 v113, v129\n"
      "v_mfma_f32_16x16x32_bf16 v[84:87], a[116:119], v[196:199], v[84:87]\n"
      "buffer_load_dwordx4 v248, s[20:23], s60 offen nt lds\n"
      "v_cvt_pk_bf16_f32 v192, v112, v113\n"
      "v_mfma_f32_16x16x32_bf16 v[68:71], a[112:115], v[196:199], v[68:71]\n"
      "s_add_u32 m0, s62, 896\n"
      "v_exp_f32 v114, v130\n"
      "v_mfma_f32_16x16x32_bf16 a[192:195], a[200:203], v[212:215], a[192:195]\n"
      "buffer_load_dwordx4 v248, s[20:23], s60 offen offset:128 nt lds\n"
      "v_mfma_f32_16x16x32_bf16 a[148:151], a[108:111], v[196:199], a[148:151]\n"
      "s_add_u32 m0, s62, 8192\n"
      "v_exp_f32 v115, v131\n"
      "v_mfma_f32_16x16x32_bf16 a[132:135], a[104:107], v[196:199], a[132:135]\n"
      "s_memrealtime s[68:69]\n"
      "buffer_load_dwordx4 v120, s[20:23], s60 offen nt lds\n"
      "v_cvt_pk_bf16_f32 v193, v114, v115\n"
      "v_mfma_f32_16x16x32_bf16 a[192:195], a[204:207], v[216:219], a[192:195]\n"
      "s_add_u32 m0, s62, 9088\n"
      "v_exp_f32 v112, v132\n"
      "v_mfma_f32_16x16x32_bf16 v[52:55], a[100:103], v[196:199], v[52:55]\n"
      "s_add_u32 s62, s62, 0x8000\n"
      "buffer_load_dwordx4 v120, s[20:23], s60 offen offset:128 nt lds\n"
      "v_or_b32 v116, v192, v193\n"
      "v_mfma_f32_16x16x32_bf16 v[36:39], a[96:99], v[196:199], v[36:39]\n"
      "s_and_b32 s62, s62, 0x1ffff\n"
      "v_exp_f32 v113, v133\n"
      "v_mfma_f32_16x16x32_bf16 a[192:195], a[208:211], v[220:223], a[192:195]\n"
      "s_add_u32 s60, s60, s78\n"
      "v_cvt_pk_bf16_f32 v194, v112, v113\n"
      "s_waitcnt vmcnt(12) lgkmcnt(0)\n"
      "s_barrier\n"
      "v_mfma_f32_16x16x32_bf16 v[40:43], a[96:99], v[200:203], v[40:43]\n"
      "s_lshr_b32 s43, %[wb], 8\n"
      "ds_read_b128 a[64:67], v242 offset:32768\n"
      "v_exp_f32 v114, v134\n"
      "v_mfma_f32_16x16x32_bf16 v[44:47], a[96:99], v[204:207], v[44:47]\n"
      "s_lshr_b32 s33, 0x8d802000, s43\n"
      "ds_read_b128 a[68:71], v242 offset:36864\n"
      "ds_read_b128 a[72:75], v243 offset:32768\n"
      "v_mfma_f32_16x16x32_bf16 v[60:63], a[100:103], v[204:207], v[60:63]\n"
      "s_and_b32 s33, s33, 255\n"
      "v_exp_f32 v115, v135\n"
      "v_mfma_f32_16x16x32_bf16 v[56:59], a[100:103], v[200:203], v[56:59]\n"
      "s_lshl_b32 s33, s33, 10\n"
      "ds_read_b128 a[76:79], v243 offset:36864\n"
      "v_cvt_pk_bf16_f32 v195, v114, v115\n"
      "v_mfma_f32_16x16x32_bf16 a[136:139], a[104:107], v[200:203], a[136:139]\n"
      "s_xor_b32 s43, s43, 16\n"
      "v_exp_f32 v112, v136\n"
      "v_mfma_f32_16x16x32_bf16 a[140:143], a[104:107], v[204:207], a[140:143]\n"
      "s_lshr_b32 s34, 0x8d802000, s43\n"
      "ds_read_b128 a[80:83], v242 offset:33792\n"
      "v_or3_b32 v116, v116, v194, v195\n"
      "v_mfma_f32_16x16x32_bf16 a[156:159], a[108:111], v[204:207], a[156:159]\n"
      "s_and_b32 s34, s34, 255\n"
      "v_exp_f32 v113, v137\n"
      "v_mfma_f32_16x16x32_bf16 a[152:155], a[108:111], v[200:203], a[152:155]\n"
      "s_lshl_b32 s34, s34, 10\n"
      "ds_read_b128 a[84:87], v242 offset:37888\n"
      "v_cvt_pk_bf16_f32 v196, v112, v113\n"
      "v_mfma_f32_16x16x32_bf16 v[72:75], a[112:115], v[200:203], v[72:75]\n"
      "s_lshr_b32 s35, %[qo], 13\n"
      "v_exp_f32 v114, v138\n"
      "v_mfma_f32_16x16x32_bf16 v[76:79], a[112:115], v[204:207], v[76:79]\n"
      "s_lshr_b32 s43, %[wb], 11\n"
      "ds_read_b128 a[88:91], v243 offset:33792\n"
      "ds_read_b128 a[92:95], v243 offset:37888\n"
      "v_mfma_f32_16x16x32_bf16 v[92:95], a[116:119], v[204:207], v[92:95]\n"
      "s_add_u32 s35, s35, s43\n"
      "v_exp_f32 v115, v139\n"
      "v_mfma_f32_16x16x32_bf16 v[88:91], a[116:119], v[200:203], v[88:91]\n"
      "s_mul_i32 s35, s35, 5120\n"
      "v_cvt_pk_bf16_f32 v197, v114, v115\n"
      "v_mfma_f32_16x16x32_bf16 a[168:171], a[120:123], v[200:203], a[168:171]\n"
      "s_and_b32 s43, %[blk], 8\n"
      "v_exp_f32 v112, v140\n"
      "v_mfma_f32_16x16x32_bf16 a[172:175], a[120:123], v[204:207], a[172:175]\n"
      "s_lshr_b32 s37, %[qo], 8\n"
      "v_or3_b32 v116, v116, v196, v197\n"
      "v_mfma_f32_16x16x32_bf16 a[188:191], a[124:127], v[204:207], a[188:191]\n"
      "s_add_u32 s37, s37, s43\n"
      "v_exp_f32 v113, v141\n"
      "v_mfma_f32_16x16x32_bf16 a[184:187], a[124:127], v[200:203], a[184:187]\n"
      "s_waitcnt lgkmcnt(0)\n"
      "v_cvt_pk_bf16_f32 v198, v112, v113\n"
      "v_mfma_f32_16x16x32_bf16 v[160:163], a[64:67], a[0:3], v[224:227]\n"
      "s_add_u32 s37, s37, 64\n"
      "v_exp_f32 v114, v142\n"
      "v_mfma_f32_16x16x32_bf16 v[160:163], a[72:75], a[16:19], v[160:163]\n"
      "s_lshl_b32 s36, s43, 4\n"
      "ds_read_b64_tr_b16 a[96:97], v244 offset:32768\n"
      "ds_read_b64_tr_b16 a[98:99], v244 offset:36864\n"
      "v_mfma_f32_16x16x32_bf16 v[160:163], a[80:83], a[32:35], v[160:163]\n"
      "s_lshl_b32 s43, s43, 19\n"
      "v_exp_f32 v115, v143\n"
      "v_mfma_f32_16x16x32_bf16 v[160:163], a[88:91], a[48:51], v[160:163]\n"
      "s_add_u32 s35, s35, s43\n"
      "ds_read_b64_tr_b16 a[100:101], v245 offset:32768\n"
      "v_cvt_pk_bf16_f32 v199, v114, v115\n"
      "v_mfma_f32_16x16x32_bf16 v[164:167], a[92:95], a[48:51], v[224:227]\n"
      "s_add_u32 s35, s35, 0x8000\n"
      "v_exp_f32 v112, v144\n"
      "v_mfma_f32_16x16x32_bf16 v[164:167], a[84:87], a[32:35], v[164:167]\n"
      "s_add_u32 s36, s36, %[qo]\n"
      "ds_read_b64_tr_b16 a[102:103], v245 offset:36864\n"
      "v_or3_b32 v116, v116, v198, v199\n"
      "v_mfma_f32_16x16x32_bf16 v[164:167], a[76:79], a[16:19], v[164:167]\n"
      "s_and_b32 s43, %[wb], 2048\n"
      "v_exp_f32 v113, v145\n"
      "v_mfma_f32_16x16x32_bf16 v[164:167], a[68:71], a[0:3], v[164:167]\n"
      "s_lshl3_add_u32 s36, s43, s36\n"
      "ds_read_b64_tr_b16 a[104:105], v246 offset:32768\n"
      "v_cvt_pk_bf16_f32 v200, v112, v113\n"
      "v_mfma_f32_16x16x32_bf16 v[172:175], a[68:71], a[4:7], v[228:231]\n"
      "s_lshr_b32 s43, %[wb], 6\n"
      "v_exp_f32 v114, v146\n"
      "v_mfma_f32_16x16x32_bf16 v[172:175], a[76:79], a[20:23], v[172:175]\n"
      "s_and_b32 s43, s43, 64\n"
      "ds_read_b64_tr_b16 a[106:107], v246 offset:36864\n"
      "ds_read_b64_tr_b16 a[108:109], v247 offset:32768\n"
      "v_mfma_f32_16x16x32_bf16 v[172:175], a[84:87], a[36:39], v[172:175]\n"
      "s_add_u32 s36, s36, s43\n"
      "v_exp_f32 v115, v147\n"
      "v_mfma_f32_16x16x32_bf16 v[172:175], a[92:95], a[52:55], v[172:175]\n"
      "s_add_u32 s36, s36, 4096\n"
      "ds_read_b64_tr_b16 a[110:111], v247 offset:36864\n"
      "v_cvt_pk_bf16_f32 v201, v114, v115\n"
      "v_mfma_f32_16x16x32_bf16 v[168:171], a[88:91], a[52:55], v[228:231]\n"
      "v_exp_f32 v112, v148\n"
      "v_mfma_f32_16x16x32_bf16 v[168:171], a[80:83], a[36:39], v[168:171]\n"
      "ds_read_b64_tr_b16 a[112:113], v244 offset:33792\n"
      "v_or3_b32 v116, v116, v200, v201\n"
      "v_mfma_f32_16x16x32_bf16 v[168:171], a[72:75], a[20:23], v[168:171]\n"
      "v_exp_f32 v113, v149\n"
      "v_mfma_f32_16x16x32_bf16 v[168:171], a[64:67], a[4:7], v[168:171]\n"
      "ds_read_b64_tr_b16 a[114:115], v244 offset:37888\n"
      "v_cvt_pk_bf16_f32 v202, v112, v113\n"
      "v_mfma_f32_16x16x32_bf16 v[176:179], a[64:67], a[8:11], v[232:235]\n"
      "v_exp_f32 v114, v150\n"
      "v_mfma_f32_16x16x32_bf16 v[176:179], a[72:75], a[24:27], v[176:179]\n"
      "ds_read_b64_tr_b16 a[116:117], v245 offset:33792\n"
      "ds_read_b64_tr_b16 a[118:119], v245 offset:37888\n"
      "v_mfma_f32_16x16x32_bf16 v[176:179], a[80:83], a[40:43], v[176:179]\n"
      "v_exp_f32 v115, v151\n"
      "v_mfma_f32_16x16x32_bf16 v[176:179], a[88:91], a[56:59], v[176:179]\n"
      "ds_read_b64_tr_b16 a[120:121], v246 offset:33792\n"
      "v_cvt_pk_bf16_f32 v203, v114, v115\n"
      "v_mfma_f32_16x16x32_bf16 v[180:183], a[92:95], a[56:59], v[232:235]\n"
      "v_exp_f32 v112, v152\n"
      "v_mfma_f32_16x16x32_bf16 v[180:183], a[84:87], a[40:43], v[180:183]\n"
      "ds_read_b64_tr_b16 a[122:123], v246 offset:37888\n"
      "v_or3_b32 v116, v116, v202, v203\n"
      "v_mfma_f32_16x16x32_bf16 v[180:183], a[76:79], a[24:27], v[180:183]\n"
      "v_exp_f32 v113, v153\n"
      "v_mfma_f32_16x16x32_bf16 v[180:183], a[68:71], a[8:11], v[180:183]\n"
      "ds_read_b64_tr_b16 a[124:125], v247 offset:33792\n"
      "v_cvt_pk_bf16_f32 v204, v112, v113\n"
      "v_mfma_f32_16x16x32_bf16 v[188:191], a[68:71], a[12:15], v[236:239]\n"
      "v_exp_f32 v114, v154\n"
      "v_mfma_f32_16x16x32_bf16 v[188:191], a[76:79], a[28:31], v[188:191]\n"
      "ds_read_b64_tr_b16 a[126:127], v247 offset:37888\n"
      "v_mfma_f32_16x16x32_bf16 v[188:191], a[84:87], a[44:47], v[188:191]\n"
      "v_exp_f32 v115, v155\n"
      "v_mfma_f32_16x16x32_bf16 v[188:191], a[92:95], a[60:63], v[188:191]\n"
      "v_cvt_pk_bf16_f32 v205, v114, v115\n"
      "v_mfma_f32_16x16x32_bf16 v[184:187], a[88:91], a[60:63], v[236:239]\n"
      "v_exp_f32 v112, v156\n"
      "v_mfma_f32_16x16x32_bf16 v[184:187], a[80:83], a[44:47], v[184:187]\n"
      "v_or3_b32 v116, v116, v204, v205\n"
      "v_mfma_f32_16x16x32_bf16 v[184:187], a[72:75], a[28:31], v[184:187]\n"
      "v_exp_f32 v113, v157\n"
      "v_mfma_f32_16x16x32_bf16 v[184:187], a[64:67], a[12:15], v[184:187]\n"
      "s_waitcnt lgkmcnt(10)\n"
      "v_cvt_pk_bf16_f32 v206, v112, v113\n"
      "v_mfma_f32_16x16x32_bf16 v[32:35], a[96:99], v[208:211], v[32:35]\n"
      "v_exp_f32 v114, v158\n"
      "v_mfma_f32_16x16x32_bf16 v[48:51], a[100:103], v[208:211], v[48:51]\n"
      "v_mfma_f32_16x16x32_bf16 a[128:131], a[104:107], v[208:211], a[128:131]\n"
      "s_waitcnt lgkmcnt(6)\n"
      "v_exp_f32 v115, v159\n"
      "v_mfma_f32_16x16x32_bf16 a[144:147], a[108:111], v[208:211], a[144:147]\n"
      "v_cvt_pk_bf16_f32 v207, v114, v115\n"
      "v_mfma_f32_16x16x32_bf16 v[64:67], a[112:115], v[208:211], v[64:67]\n"
      "s_waitcnt lgkmcnt(2)\n"
      "v_mfma_f32_16x16x32_bf16 v[80:83], a[116:119], v[208:211], v[80:83]\n"
      "v_or3_b32 v116, v116, v206, v207\n"
      "v_mfma_f32_16x16x32_bf16 a[160:163], a[120:123], v[208:211], a[160:163]\n"
      "s_mov_b32 s64, 1\n"
      "s_waitcnt lgkmcnt(0)\n"
      "v_and_b32 v116, 0x40004000, v116\n"
      "v_mfma_f32_16x16x32_bf16 a[176:179], a[124:127], v[208:211], a[176:179]\n"
      "v_cmp_ne_u32 vcc, 0, v116\n"
      "s_cbranch_vccnz FB0_%=\n"
      "RET0_1_%=:\n"
      "v_mfma_f32_16x16x32_bf16 a[180:183], a[124:127], v[212:215], a[180:183]\n"
      "v_exp_f32 v112, v160\n"
      "v_mfma_f32_16x16x32_bf16 a[164:167], a[120:123], v[212:215], a[164:167]\n"
      "v_xor_b32 v242, 0x10000, v242\n"
      "v_mfma_f32_16x16x32_bf16 a[192:195], a[196:199], v[192:195], a[192:195]\n"
      "s_add_u32 m0, s77, 16384\n"
      "v_exp_f32 v113, v161\n"
      "v_mfma_f32_16x16x32_bf16 v[84:87], a[116:119], v[212:215], v[84:87]\n"
      "buffer_load_dwordx4 v249, s[24:27], s61 offen nt lds\n"
      "v_cvt_pk_bf16_f32 v208, v112, v113\n"
      "v_xor_b32 v243, 0x10000, v243\n"
      "v_mfma_f32_16x16x32_bf16 v[68:71], a[112:115], v[212:215], v[68:71]\n"
      "s_add_u32 m0, s77, 17408\n"
      "v_exp_f32 v114, v162\n"
      "v_mfma_f32_16x16x32_bf16 a[192:195], a[200:203], v[196:199], a[192:195]\n"
      "buffer_load_dwordx4 v121, s[24:27], s61 offen nt lds\n"
      "v_xor_b32 v244, 0x10000, v244\n"
      "v_mfma_f32_16x16x32_bf16 a[148:151], a[108:111], v[212:215], a[148:151]\n"
      "s_add_u32 m0, s77, 24576\n"
      "v_exp_f32 v115, v163\n"
      "v_mfma_f32_16x16x32_bf16 a[132:135], a[104:107], v[212:215], a[132:135]\n"
      "s_memrealtime s[68:69]\n"
      "buffer_load_dwordx4 v122, s[24:27], s61 offen nt lds\n"
      "v_cvt_pk_bf16_f32 v209, v114, v115\n"
      "v_xor_b32 v245, 0x10000, v245\n"
      "v_mfma_f32_16x16x32_bf16 a[192:195], a[204:207], v[200:203], a[192:195]\n"
      "s_add_u32 m0, s77, 25600\n"
      "v_exp_f32 v112, v164\n"
      "v_mfma_f32_16x16x32_bf16 v[52:55], a[100:103], v[212:215], v[52:55]\n"
      "s_add_u32 s77, s77, 0x8000\n"
      "buffer_load_dwordx4 v123, s[24:27], s61 offen nt lds\n"
      "v_or_b32 v116, v208, v209\n"
      "v_xor_b32 v246, 0x10000, v246\n"
      "v_mfma_f32_16x16x32_bf16 v[36:39], a[96:99], v[212:215], v[36:39]\n"
      "s_and_b32 s77, s77, 0x1ffff\n"
      "v_exp_f32 v113, v165\n"
      "v_mfma_f32_16x16x32_bf16 a[192:195], a[208:211], v[204:207], a[192:195]\n"
      "s_add_u32 s61, s61, s78\n"
      "v_cvt_pk_bf16_f32 v210, v112, v113\n"
      "v_xor_b32 v247, 0x10000, v247\n"
      "s_waitcnt vmcnt(8) lgkmcnt(0)\n"
      "s_sub_u32 s67, s68, s90\n"
      "s_cmp_lt_i32 s67, 0\n"
      "s_cbranch_scc1 GW3_%=\n"
      "GX3_%=:\n"
      "s_barrier\n"
      "v_mfma_f32_16x16x32_bf16 v[40:43], a[96:99], v[216:219], v[40:43]\n"
      "s_memrealtime s[68:69]\n"
      "buffer_load_dword v250, v251, s[28:31], 0 offen sc1\n"
      "ds_read_b128 a[64:67], v242 offset:0\n"
      "v_exp_f32 v114, v166\n"
      "v_mfma_f32_16x16x32_bf16 v[44:47], a[96:99], v[220:223], v[44:47]\n"
      "s_add_u32 m0, s62, 0\n"
      "ds_read_b128 a[68:71], v242 offset:4096\n"
      "ds_read_b128 a[72:75], v243 offset:0\n"
      "v_mfma_f32_16x16x32_bf16 v[60:63], a[100:103], v[220:223], v[60:63]\n"
      "buffer_load_dwordx4 v248, s[20:23], s60 offen nt lds\n"
      "v_exp_f32 v115, v167\n"
      "v_mfma_f32_16x16x32_bf16 v[56:59], a[100:103], v[216:219], v[56:59]\n"
      "s_add_u32 m0, s62, 896\n"
      "ds_read_b128 a[76:79], v243 offset:4096\n"
      "v_cvt_pk_bf16_f32 v211, v114, v115\n"
      "v_mfma_f32_16x16x32_bf16 a[136:139], a[104:107], v[216:219], a[136:139]\n"
      "buffer_load_dwordx4 v248, s[20:23], s60 offen offset:128 nt lds\n"
      "v_exp_f32 v112, v168\n"
      "v_mfma_f32_16x16x32_bf16 a[140:143], a[104:107], v[220:223], a[140:143]\n"
      "s_add_u32 m0, s62, 8192\n"
      "ds_read_b128 a[80:83], v242 offset:1024\n"
      "v_or3_b32 v116, v116, v210, v211\n"
      "v_mfma_f32_16x16x32_bf16 a[156:159], a[108:111], v[220:223], a[156:159]\n"
      "buffer_load_dwordx4 v120, s[20:23], s60 offen nt lds\n"
      "v_exp_f32 v113, v169\n"
      "v_mfma_f32_16x16x32_bf16 a[152:155], a[108:111], v[216:219], a[152:155]\n"
      "s_add_u32 m0, s62, 9088\n"
      "ds_read_b128 a[84:87], v242 offset:5120\n"
      "v_cvt_pk_bf16_f32 v212, v112, v113\n"
      "v_mfma_f32_16x16x32_bf16 v[72:75], a[112:115], v[216:219], v[72:75]\n"
      "s_add_u32 s62, s62, 0x8000\n"
      "buffer_load_dwordx4 v120, s[20:23], s60 offen offset:128 nt lds\n"
      "v_exp_f32 v114, v170\n"
      "v_mfma_f32_16x16x32_bf16 v[76:79], a[112:115], v[220:223], v[76:79]\n"
      "s_and_b32 s62, s62, 0x1ffff\n"
      "ds_read_b128 a[88:91], v243 offset:1024\n"
      "ds_read_b128 a[92:95], v243 offset:5120\n"
      "v_mfma_f32_16x16x32_bf16 v[92:95], a[116:119], v[220:223], v[92:95]\n"
      "s_add_u32 s60, s60, %[inc]\n"
      "v_exp_f32 v115, v171\n"
      "v_mfma_f32_16x16x32_bf16 v[88:91], a[116:119], v[216:219], v[88:91]\n"
      "v_cvt_pk_bf16_f32 v213, v114, v115\n"
      "v_mfma_f32_16x16x32_bf16 a[168:171], a[120:123], v[216:219], a[168:171]\n"
      "v_exp_f32 v112, v172\n"
      "v_mfma_f32_16x16x32_bf16 a[172:175], a[120:123], v[220:223], a[172:175]\n"
      "v_or3_b32 v116, v116, v212, v213\n"
      "v_mfma_f32_16x16x32_bf16 a[188:191], a[124:127], v[220:223], a[188:191]\n"
      "v_exp_f32 v113, v173\n"
      "v_mfma_f32_16x16x32_bf16 a[184:187], a[124:127], v[216:219], a[184:187]\n"
      "s_waitcnt lgkmcnt(0)\n"
      "v_cvt_pk_bf16_f32 v214, v112, v113\n"
      "v_mfma_f32_16x16x32_bf16 v[128:131], a[64:67], a[0:3], v[224:227]\n"
      "s_sub_u32 s67, s68, s66\n"
      "v_exp_f32 v114, v174\n"
      "v_mfma_f32_16x16x32_bf16 v[128:131], a[72:75], a[16:19], v[128:131]\n"
      "s_cmp_gt_i32 s67, s70\n"
      "ds_read_b64_tr_b16 a[96:97], v244 offset:0\n"
      "ds_read_b64_tr_b16 a[98:99], v244 offset:4096\n"
      "v_mfma_f32_16x16x32_bf16 v[128:131], a[80:83], a[32:35], v[128:131]\n"
      "s_cselect_b32 s69, s86, 0\n"
      "v_exp_f32 v115, v175\n"
      "v_mfma_f32_16x16x32_bf16 v[128:131], a[88:91], a[48:51], v[128:131]\n"
      "s_add_u32 s66, s66, s69\n"
      "ds_read_b64_tr_b16 a[100:101], v245 offset:0\n"
      "v_cvt_pk_bf16_f32 v215, v114, v115\n"
      "v_mfma_f32_16x16x32_bf16 v[132:135], a[92:95], a[48:51], v[224:227]\n"
      "s_add_u32 s67, s67, s101\n"
      "v_exp_f32 v112, v176\n"
      "v_mfma_f32_16x16x32_bf16 v[132:135], a[84:87], a[32:35], v[132:135]\n"
      "s_min_i32 s70, s70, s67\n"
      "ds_read_b64_tr_b16 a[102:103], v245 offset:4096\n"
      "v_or3_b32 v116, v116, v214, v215\n"
      "v_mfma_f32_16x16x32_bf16 v[132:135], a[76:79], a[16:19], v[132:135]\n"
      "s_max_i32 s70, s70, 85\n"
      "v_exp_f32 v113, v177\n"
      "v_mfma_f32_16x16x32_bf16 v[132:135], a[68:71], a[0:3], v[132:135]\n"
      "s_add_u32 s66, s66, s86\n"
      "ds_read_b64_tr_b16 a[104:105], v246 offset:0\n"
      "v_cvt_pk_bf16_f32 v216, v112, v113\n"
      "v_mfma_f32_16x16x32_bf16 v[140:143], a[68:71], a[4:7], v[228:231]\n"
      "s_and_b32 s76, s80, 31\n"
      "v_exp_f32 v114, v178\n"
      "v_mfma_f32_16x16x32_bf16 v[140:143], a[76:79], a[20:23], v[140:143]\n"
      "s_lshr_b64 s[80:81], s[80:81], 5\n"
      "ds_read_b64_tr_b16 a[106:107], v246 offset:4096\n"
      "ds_read_b64_tr_b16 a[108:109], v247 offset:0\n"
      "v_mfma_f32_16x16x32_bf16 v[140:143], a[84:87], a[36:39], v[140:143]\n"
      "s_add_u32 s66, s66, s76\n"
      "v_exp_f32 v115, v179\n"
      "v_mfma_f32_16x16x32_bf16 v[140:143], a[92:95], a[52:55], v[140:143]\n"
      "s_sub_u32 s67, s68, s82\n"
      "ds_read_b64_tr_b16 a[110:111], v247 offset:4096\n"
      "v_cvt_pk_bf16_f32 v217, v114, v115\n"
      "v_mfma_f32_16x16x32_bf16 v[136:139], a[88:91], a[52:55], v[228:231]\n"
      "s_min_i32 s76, s67, s83\n"
      "v_exp_f32 v112, v180\n"
      "v_mfma_f32_16x16x32_bf16 v[136:139], a[80:83], a[36:39], v[136:139]\n"
      "s_mov_b32 s83, s67\n"
      "ds_read_b64_tr_b16 a[112:113], v244 offset:1024\n"
      "v_or3_b32 v116, v116, v216, v217\n"
      "v_mfma_f32_16x16x32_bf16 v[136:139], a[72:75], a[20:23], v[136:139]\n"
      "s_sub_u32 s76, s76, 36\n"
      "v_exp_f32 v113, v181\n"
      "v_mfma_f32_16x16x32_bf16 v[136:139], a[64:67], a[4:7], v[136:139]\n"
      "s_min_i32 s76, s76, 148\n"
      "ds_read_b64_tr_b16 a[114:115], v244 offset:5120\n"
      "v_cvt_pk_bf16_f32 v218, v112, v113\n"
      "v_mfma_f32_16x16x32_bf16 v[144:147], a[64:67], a[8:11], v[232:235]\n"
      "s_max_i32 s76, s76, 70\n"
      "v_exp_f32 v114, v182\n"
      "v_mfma_f32_16x16x32_bf16 v[144:147], a[72:75], a[24:27], v[144:147]\n"
      "s_mov_b32 s82, s68\n"
      "ds_read_b64_tr_b16 a[116:117], v245 offset:1024\n"
      "ds_read_b64_tr_b16 a[118:119], v245 offset:5120\n"
      "v_mfma_f32_16x16x32_bf16 v[144:147], a[80:83], a[40:43], v[144:147]\n"
      "s_lshr_b32 s69, s86, 1\n"
      "v_exp_f32 v115, v183\n"
      "v_mfma_f32_16x16x32_bf16 v[144:147], a[88:91], a[56:59], v[144:147]\n"
      "s_add_u32 s66, s66, s69\n"
      "ds_read_b64_tr_b16 a[120:121], v246 offset:1024\n"
      "v_cvt_pk_bf16_f32 v219, v114, v115\n"
      "v_mfma_f32_16x16x32_bf16 v[148:151], a[92:95], a[56:59], v[232:235]\n"
      "s_add_u32 s82, s82, s69\n"
      "v_exp_f32 v112, v184\n"
      "v_mfma_f32_16x16x32_bf16 v[148:151], a[84:87], a[40:43], v[148:151]\n"
      "s_add_u32 s65, s82, s76\n"
      "ds_read_b64_tr_b16 a[122:123], v246 offset:5120\n"
      "v_or3_b32 v116, v116, v218, v219\n"
      "v_mfma_f32_16x16x32_bf16 v[148:151], a[76:79], a[24:27], v[148:151]\n"
      "s_sub_u32 s93, s93, 1\n"
      "v_exp_f32 v113, v185\n"
      "v_mfma_f32_16x16x32_bf16 v[148:151], a[68:71], a[8:11], v[148:151]\n"
      "s_cmp_le_i32 s93, 14\n"
      "ds_read_b64_tr_b16 a[124:125], v247 offset:1024\n"
      "v_cvt_pk_bf16_f32 v220, v112, v113\n"
      "v_mfma_f32_16x16x32_bf16 v[156:159], a[68:71], a[12:15], v[236:239]\n"
      "s_cselect_b32 s86, 107, s86\n"
      "v_exp_f32 v114, v186\n"
      "v_mfma_f32_16x16x32_bf16 v[156:159], a[76:79], a[28:31], v[156:159]\n"
      "s_sub_u32 s67, s65, s66\n"
      "ds_read_b64_tr_b16 a[126:127], v247 offset:5120\n"
      "v_mfma_f32_16x16x32_bf16 v[156:159], a[84:87], a[44:47], v[156:159]\n"
      "s_cmp_lt_i32 s67, 0\n"
      "v_exp_f32 v115, v187\n"
      "v_mfma_f32_16x16x32_bf16 v[156:159], a[92:95], a[60:63], v[156:159]\n"
      "s_cselect_b32 s88, s66, s65\n"
      "v_cvt_pk_bf16_f32 v221, v114, v115\n"
      "v_mfma_f32_16x16x32_bf16 v[152:155], a[88:91], a[60:63], v[236:239]\n"
      "s_add_u32 s90, s88, s91\n"
      "v_exp_f32 v112, v188\n"
      "v_mfma_f32_16x16x32_bf16 v[152:155], a[80:83], a[44:47], v[152:155]\n"
      "v_or3_b32 v116, v116, v220, v221\n"
      "v_mfma_f32_16x16x32_bf16 v[152:155], a[72:75], a[28:31], v[152:155]\n"
      "v_exp_f32 v113, v189\n"
      "v_mfma_f32_16x16x32_bf16 v[152:155], a[64:67], a[12:15], v[152:155]\n"
      "s_waitcnt lgkmcnt(10)\n"
      "v_cvt_pk_bf16_f32 v222, v112, v113\n"
      "v_mfma_f32_16x16x32_bf16 v[32:35], a[96:99], v[192:195], v[32:35]\n"
      "v_exp_f32 v114, v190\n"
      "v_mfma_f32_16x16x32_bf16 v[48:51], a[100:103], v[192:195], v[48:51]\n"
      "v_mfma_f32_16x16x32_bf16 a[128:131], a[104:107], v[192:195], a[128:131]\n"
      "s_waitcnt lgkmcnt(6)\n"
      "v_exp_f32 v115, v191\n"
      "v_mfma_f32_16x16x32_bf16 a[144:147], a[108:111], v[192:195], a[144:147]\n"
      "v_cvt_pk_bf16_f32 v223, v114, v115\n"
      "v_mfma_f32_16x16x32_bf16 v[64:67], a[112:115], v[192:195], v[64:67]\n"
      "s_waitcnt lgkmcnt(2)\n"
      "v_mfma_f32_16x16x32_bf16 v[80:83], a[116:119], v[192:195], v[80:83]\n"
      "v_or3_b32 v116, v116, v222, v223\n"
      "v_mfma_f32_16x16x32_bf16 a[160:163], a[120:123], v[192:195], a[160:163]\n"
      "s_mov_b32 s64, 2\n"
      "s_waitcnt lgkmcnt(0)\n"
      "v_and_b32 v116, 0x40004000, v116\n"
      "v_mfma_f32_16x16x32_bf16 a[176:179], a[124:127], v[192:195], a[176:179]\n"
      "v_cmp_ne_u32 vcc, 0, v116\n"
      "s_cbranch_vccnz FB1_%=\n"
      "RET1_2_%=:\n"
      "v_mfma_f32_16x16x32_bf16 a[180:183], a[124:127], v[196:199], a[180:183]\n"
      "v_exp_f32 v112, v128\n"
      "v_mfma_f32_16x16x32_bf16 a[164:167], a[120:123], v[196:199], a[164:167]\n"
      "v_mfma_f32_16x16x32_bf16 a[192:195], a[196:199], v[208:211], a[192:195]\n"
      "s_add_u32 m0, s77, 16384\n"
      "v_exp_f32 v113, v129\n"
      "v_mfma_f32_16x16x32_bf16 v[84:87], a[116:119], v[196:199], v[84:87]\n"
      "buffer_load_dwordx4 v249, s[24:27], s61 offen nt lds\n"
      "v_cvt_pk_bf16_f32 v192, v112, v113\n"
      "v_mfma_f32_16x16x32_bf16 v[68:71], a[112:115], v[196:199], v[68:71]\n"
      "s_add_u32 m0, s77, 17408\n"
      "v_exp_f32 v114, v130\n"
      "v_mfma_f32_16x16x32_bf16 a[192:195], a[200:203], v[212:215], a[192:195]\n"
      "buffer_load_dwordx4 v121, s[24:27], s61 offen nt lds\n"
      "v_mfma_f32_16x16x32_bf16 a[148:151], a[108:111], v[196:199], a[148:151]\n"
      "s_add_u32 m0, s77, 24576\n"
      "v_exp_f32 v115, v131\n"
      "v_mfma_f32_16x16x32_bf16 a[132:135], a[104:107], v[196:199], a[132:135]\n"
      "s_memrealtime s[68:69]\n"
      "buffer_load_dwordx4 v122, s[24:27], s61 offen nt lds\n"
      "v_cvt_pk_bf16_f32 v193, v114, v115\n"
      "v_mfma_f32_16x16x32_bf16 a[192:195], a[204:207], v[216:219], a[192:195]\n"
      "s_add_u32 m0, s77, 25600\n"
      "v_exp_f32 v112, v132\n"
      "v_mfma_f32_16x16x32_bf16 v[52:55], a[100:103], v[196:199], v[52:55]\n"
      "s_add_u32 s77, s77, 0x8000\n"
      "buffer_load_dwordx4 v123, s[24:27], s61 offen nt lds\n"
      "v_or_b32 v116, v192, v193\n"
      "v_mfma_f32_16x16x32_bf16 v[36:39], a[96:99], v[196:199], v[36:39]\n"
      "s_and_b32 s77, s77, 0x1ffff\n"
      "v_exp_f32 v113, v133\n"
      "v_mfma_f32_16x16x32_bf16 a[192:195], a[208:211], v[220:223], a[192:195]\n"
      "s_add_u32 s61, s61, %[inc]\n"
      "v_cvt_pk_bf16_f32 v194, v112, v113\n"
      "s_waitcnt vmcnt(9) lgkmcnt(0)\n"
      "s_barrier\n"
      "v_mfma_f32_16x16x32_bf16 v[40:43], a[96:99], v[200:203], v[40:43]\n"
      "ds_read_b128 a[64:67], v242 offset:32768\n"
      "v_exp_f32 v114, v134\n"
      "v_mfma_f32_16x16x32_bf16 v[44:47], a[96:99], v[204:207], v[44:47]\n"
      "ds_read_b128 a[68:71], v242 offset:36864\n"
      "ds_read_b128 a[72:75], v243 offset:32768\n"
      "v_mfma_f32_16x16x32_bf16 v[60:63], a[100:103], v[204:207], v[60:63]\n"
      "v_exp_f32 v115, v135\n"
      "v_mfma_f32_16x16x32_bf16 v[56:59], a[100:103], v[200:203], v[56:59]\n"
      "ds_read_b128 a[76:79], v243 offset:36864\n"
      "v_cvt_pk_bf16_f32 v195, v114, v115\n"
      "v_mfma_f32_16x16x32_bf16 a[136:139], a[104:107], v[200:203], a[136:139]\n"
      "v_exp_f32 v112, v136\n"
      "v_mfma_f32_16x16x32_bf16 a[140:143], a[104:107], v[204:207], a[140:143]\n"
      "ds_read_b128 a[80:83], v242 offset:33792\n"
      "v_or3_b32 v116, v116, v194, v195\n"
      "v_mfma_f32_16x16x32_bf16 a[156:159], a[108:111], v[204:207], a[156:159]\n"
      "v_exp_f32 v113, v137\n"
      "v_mfma_f32_16x16x32_bf16 a[152:155], a[108:111], v[200:203], a[152:155]\n"
      "ds_read_b128 a[84:87], v242 offset:37888\n"
      "v_cvt_pk_bf16_f32 v196, v112, v113\n"
      "v_mfma_f32_16x16x32_bf16 v[72:75], a[112:115], v[200:203], v[72:75]\n"
      "v_exp_f32 v114, v138\n"
      "v_mfma_f32_16x16x32_bf16 v[76:79], a[112:115], v[204:207], v[76:79]\n"
      "ds_read_b128 a[88:91], v243 offset:33792\n"
      "ds_read_b128 a[92:95], v243 offset:37888\n"
      "v_mfma_f32_16x16x32_bf16 v[92:95], a[116:119], v[204:207], v[92:95]\n"
      "v_exp_f32 v115, v139\n"
      "v_mfma_f32_16x16x32_bf16 v[88:91], a[116:119], v[200:203], v[88:91]\n"
      "v_cvt_pk_bf16_f32 v197, v114, v115\n"
      "v_mfma_f32_16x16x32_bf16 a[168:171], a[120:123], v[200:203], a[168:171]\n"
      "v_exp_f32 v112, v140\n"
      "v_mfma_f32_16x16x32_bf16 a[172:175], a[120:123], v[204:207], a[172:175]\n"
      "v_or3_b32 v116, v116, v196, v197\n"
      "v_mfma_f32_16x16x32_bf16 a[188:191], a[124:127], v[204:207], a[188:191]\n"
      "v_exp_f32 v113, v141\n"
      "v_mfma_f32_16x16x32_bf16 a[184:187], a[124:127], v[200:203], a[184:187]\n"
      "s_waitcnt lgkmcnt(0)\n"
      "v_cvt_pk_bf16_f32 v198, v112, v113\n"
      "v_mfma_f32_16x16x32_bf16 v[160:163], a[64:67], a[0:3], v[224:227]\n"
      "v_exp_f32 v114, v142\n"
      "v_mfma_f32_16x16x32_bf16 v[160:163], a[72:75], a[16:19], v[160:163]\n"
      "ds_read_b64_tr_b16 a[96:97], v244 offset:32768\n"
      "ds_read_b64_tr_b16 a[98:99], v244 offset:36864\n"
      "v_mfma_f32_16x16x32_bf16 v[160:163], a[80:83], a[32:35], v[160:163]\n"
      "v_exp_f32 v115, v143\n"
      "v_mfma_f32_16x16x32_bf16 v[160:163], a[88:91], a[48:51], v[160:163]\n"
      "ds_read_b64_tr_b16 a[100:101], v245 offset:32768\n"
      "v_cvt_pk_bf16_f32 v199, v114, v115\n"
      "v_mfma_f32_16x16x32_bf16 v[164:167], a[92:95], a[48:51], v[224:227]\n"
      "v_exp_f32 v112, v144\n"
      "v_mfma_f32_16x16x32_bf16 v[164:167], a[84:87], a[32:35], v[164:167]\n"
      "ds_read_b64_tr_b16 a[102:103], v245 offset:36864\n"
      "v_or3_b32 v116, v116, v198, v199\n"
      "v_mfma_f32_16x16x32_bf16 v[164:167], a[76:79], a[16:19], v[164:167]\n"
      "v_exp_f32 v113, v145\n"
      "v_mfma_f32_16x16x32_bf16 v[164:167], a[68:71], a[0:3], v[164:167]\n"
      "ds_read_b64_tr_b16 a[104:105], v246 offset:32768\n"
      "v_cvt_pk_bf16_f32 v200, v112, v113\n"
      "v_mfma_f32_16x16x32_bf16 v[172:175], a[68:71], a[4:7], v[228:231]\n"
      "v_exp_f32 v114, v146\n"
      "v_mfma_f32_16x16x32_bf16 v[172:175], a[76:79], a[20:23], v[172:175]\n"
      "ds_read_b64_tr_b16 a[106:107], v246 offset:36864\n"
      "ds_read_b64_tr_b16 a[108:109], v247 offset:32768\n"
      "v_mfma_f32_16x16x32_bf16 v[172:175], a[84:87], a[36:39], v[172:175]\n"
      "v_exp_f32 v115, v147\n"
      "v_mfma_f32_16x16x32_bf16 v[172:175], a[92:95], a[52:55], v[172:175]\n"
      "ds_read_b64_tr_b16 a[110:111], v247 offset:36864\n"
      "v_cvt_pk_bf16_f32 v201, v114, v115\n"
      "v_mfma_f32_16x16x32_bf16 v[168:171], a[88:91], a[52:55], v[228:231]\n"
      "v_exp_f32 v112, v148\n"
      "v_mfma_f32_16x16x32_bf16 v[168:171], a[80:83], a[36:39], v[168:171]\n"
      "ds_read_b64_tr_b16 a[112:113], v244 offset:33792\n"
      "v_or3_b32 v116, v116, v200, v201\n"
      "v_mfma_f32_16x16x32_bf16 v[168:171], a[72:75], a[20:23], v[168:171]\n"
      "v_exp_f32 v113, v149\n"
      "v_mfma_f32_16x16x32_bf16 v[168:171], a[64:67], a[4:7], v[168:171]\n"
      "ds_read_b64_tr_b16 a[114:115], v244 offset:37888\n"
      "v_cvt_pk_bf16_f32 v202, v112, v113\n"
      "v_mfma_f32_16x16x32_bf16 v[176:179], a[64:67], a[8:11], v[232:235]\n"
      "v_exp_f32 v114, v150\n"
      "v_mfma_f32_16x16x32_bf16 v[176:179], a[72:75], a[24:27], v[176:179]\n"
      "ds_read_b64_tr_b16 a[116:117], v245 offset:33792\n"
      "ds_read_b64_tr_b16 a[118:119], v245 offset:37888\n"
      "v_mfma_f32_16x16x32_bf16 v[176:179], a[80:83], a[40:43], v[176:179]\n"
      "v_exp_f32 v115, v151\n"
      "v_mfma_f32_16x16x32_bf16 v[176:179], a[88:91], a[56:59], v[176:179]\n"
      "ds_read_b64_tr_b16 a[120:121], v246 offset:33792\n"
      "v_cvt_pk_bf16_f32 v203, v114, v115\n"
      "v_mfma_f32_16x16x32_bf16 v[180:183], a[92:95], a[56:59], v[232:235]\n"
      "v_exp_f32 v112, v152\n"
      "v_mfma_f32_16x16x32_bf16 v[180:183], a[84:87], a[40:43], v[180:183]\n"
      "ds_read_b64_tr_b16 a[122:123], v246 offset:37888\n"
      "v_or3_b32 v116, v116, v202, v203\n"
      "v_mfma_f32_16x16x32_bf16 v[180:183], a[76:79], a[24:27], v[180:183]\n"
      "v_exp_f32 v113, v153\n"
      "v_mfma_f32_16x16x32_bf16 v[180:183], a[68:71], a[8:11], v[180:183]\n"
      "ds_read_b64_tr_b16 a[124:125], v247 offset:33792\n"
      "v_cvt_pk_bf16_f32 v204, v112, v113\n"
      "v_mfma_f32_16x16x32_bf16 v[188:191], a[68:71], a[12:15], v[236:239]\n"
      "v_exp_f32 v114, v154\n"
      "v_mfma_f32_16x16x32_bf16 v[188:191], a[76:79], a[28:31], v[188:191]\n"
      "ds_read_b64_tr_b16 a[126:127], v247 offset:37888\n"
      "v_mfma_f32_16x16x32_bf16 v[188:191], a[84:87], a[44:47], v[188:191]\n"
      "v_exp_f32 v115, v155\n"
      "v_mfma_f32_16x16x32_bf16 v[188:191], a[92:95], a[60:63], v[188:191]\n"
      "v_cvt_pk_bf16_f32 v205, v114, v115\n"
      "v_mfma_f32_16x16x32_bf16 v[184:187], a[88:91], a[60:63], v[236:239]\n"
      "v_exp_f32 v112, v156\n"
      "v_mfma_f32_16x16x32_bf16 v[184:187], a[80:83], a[44:47], v[184:187]\n"
      "v_or3_b32 v116, v116, v204, v205\n"
      "v_mfma_f32_16x16x32_bf16 v[184:187], a[72:75], a[28:31], v[184:187]\n"
      "v_exp_f32 v113, v157\n"
      "v_mfma_f32_16x16x32_bf16 v[184:187], a[64:67], a[12:15], v[184:187]\n"
      "s_waitcnt lgkmcnt(10)\n"
      "v_cvt_pk_bf16_f32 v206, v112, v113\n"
      "v_mfma_f32_16x16x32_bf16 v[32:35], a[96:99], v[208:211], v[32:35]\n"
      "v_exp_f32 v114, v158\n"
      "v_mfma_f32_16x16x32_bf16 v[48:51], a[100:103], v[208:211], v[48:51]\n"
      "v_mfma_f32_16x16x32_bf16 a[128:131], a[104:107], v[208:211], a[128:131]\n"
      "s_waitcnt lgkmcnt(6)\n"
      "v_exp_f32 v115, v159\n"
      "v_mfma_f32_16x16x32_bf16 a[144:147], a[108:111], v[208:211], a[144:147]\n"
      "v_cvt_pk_bf16_f32 v207, v114, v115\n"
      "v_mfma_f32_16x16x32_bf16 v[64:67], a[112:115], v[208:211], v[64:67]\n"
      "s_waitcnt lgkmcnt(2)\n"
      "v_mfma_f32_16x16x32_bf16 v[80:83], a[116:119], v[208:211], v[80:83]\n"
      "v_or3_b32 v116, v116, v206, v207\n"
      "v_mfma_f32_16x16x32_bf16 a[160:163], a[120:123], v[208:211], a[160:163]\n"
      "s_mov_b32 s64, 2\n"
      "s_waitcnt lgkmcnt(0)\n"
      "v_and_b32 v116, 0x40004000, v116\n"
      "v_mfma_f32_16x16x32_bf16 a[176:179], a[124:127], v[208:211], a[176:179]\n"
      "v_cmp_ne_u32 vcc, 0, v116\n"
      "s_cbranch_vccnz FB0_%=\n"
      "RET0_2_%=:\n"
      "v_mfma_f32_16x16x32_bf16 a[180:183], a[124:127], v[212:215], a[180:183]\n"
      "v_exp_f32 v112, v160\n"
      "v_mfma_f32_16x16x32_bf16 a[164:167], a[120:123], v[212:215], a[164:167]\n"
      "v_xor_b32 v242, 0x10000, v242\n"
      "v_mfma_f32_16x16x32_bf16 a[192:195], a[196:199], v[192:195], a[192:195]\n"
      "s_add_u32 m0, s62, 0\n"
      "v_exp_f32 v113, v161\n"
      "v_mfma_f32_16x16x32_bf16 v[84:87], a[116:119], v[212:215], v[84:87]\n"
      "buffer_load_dwordx4 v248, s[20:23], s60 offen nt lds\n"
      "v_cvt_pk_bf16_f32 v208, v112, v113\n"
      "v_xor_b32 v243, 0x10000, v243\n"
      "v_mfma_f32_16x16x32_bf16 v[68:71], a[112:115], v[212:215], v[68:71]\n"
      "s_add_u32 m0, s62, 896\n"
      "v_exp_f32 v114, v162\n"
      "v_mfma_f32_16x16x32_bf16 a[192:195], a[200:203], v[196:199], a[192:195]\n"
      "buffer_load_dwordx4 v248, s[20:23], s60 offen offset:128 nt lds\n"
      "v_xor_b32 v244, 0x10000, v244\n"
      "v_mfma_f32_16x16x32_bf16 a[148:151], a[108:111], v[212:215], a[148:151]\n"
      "s_add_u32 m0, s62, 8192\n"
      "v_exp_f32 v115, v163\n"
      "v_mfma_f32_16x16x32_bf16 a[132:135], a[104:107], v[212:215], a[132:135]\n"
      "s_memrealtime s[68:69]\n"
      "buffer_load_dwordx4 v120, s[20:23], s60 offen nt lds\n"
      "v_cvt_pk_bf16_f32 v209, v114, v115\n"
      "v_xor_b32 v245, 0x10000, v245\n"
      "v_mfma_f32_16x16x32_bf16 a[192:195], a[204:207], v[200:203], a[192:195]\n"
      "s_add_u32 m0, s62, 9088\n"
      "v_exp_f32 v112, v164\n"
      "v_mfma_f32_16x16x32_bf16 v[52:55], a[100:103], v[212:215], v[52:55]\n"
      "s_add_u32 s62, s62, 0x8000\n"
      "buffer_load_dwordx4 v120, s[20:23], s60 offen offset:128 nt lds\n"
      "v_or_b32 v116, v208, v209\n"
      "v_xor_b32 v246, 0x10000, v246\n"
      "v_mfma_f32_16x16x32_bf16 v[36:39], a[96:99], v[212:215], v[36:39]\n"
      "s_and_b32 s62, s62, 0x1ffff\n"
      "v_exp_f32 v113, v165\n"
      "v_mfma_f32_16x16x32_bf16 a[192:195], a[208:211], v[204:207], a[192:195]\n"
      "s_add_u32 s60, s60, s78\n"
      "v_cvt_pk_bf16_f32 v210, v112, v113\n"
      "v_xor_b32 v247, 0x10000, v247\n"
      "s_waitcnt vmcnt(8)\n"
      "s_sub_u32 s63, 11, s40\n"
      "LOOP2_%=:\n"
      "s_waitcnt vmcnt(9) lgkmcnt(0)\n"
      "s_sub_u32 s67, s68, s90\n"
      "s_cmp_lt_i32 s67, 0\n"
      "s_cbranch_scc1 GW4_%=\n"
      "GX4_%=:\n"
      "s_barrier\n"
      "v_mfma_f32_16x16x32_bf16 v[40:43], a[96:99], v[216:219], v[40:43]\n"
      "s_memrealtime s[68:69]\n"
      "ds_read_b128 a[64:67], v242 offset:0\n"
      "v_exp_f32 v114, v166\n"
      "v_mfma_f32_16x16x32_bf16 v[44:47], a[96:99], v[220:223], v[44:47]\n"
      "s_add_u32 m0, s77, 16384\n"
      "ds_read_b128 a[68:71], v242 offset:4096\n"
      "ds_read_b128 a[72:75], v243 offset:0\n"
      "v_mfma_f32_16x16x32_bf16 v[60:63], a[100:103], v[220:223], v[60:63]\n"
      "buffer_load_dwordx4 v249, s[24:27], s61 offen nt lds\n"
      "v_exp_f32 v115, v167\n"
      "v_mfma_f32_16x16x32_bf16 v[56:59], a[100:103], v[216:219], v[56:59]\n"
      "s_add_u32 m0, s77, 17408\n"
      "ds_read_b128 a[76:79], v243 offset:4096\n"
      "v_cvt_pk_bf16_f32 v211, v114, v115\n"
      "v_mfma_f32_16x16x32_bf16 a[136:139], a[104:107], v[216:219], a[136:139]\n"
      "buffer_load_dwordx4 v121, s[24:27], s61 offen nt lds\n"
      "v_exp_f32 v112, v168\n"
      "v_mfma_f32_16x16x32_bf16 a[140:143], a[104:107], v[220:223], a[140:143]\n"
      "s_add_u32 m0, s77, 24576\n"
      "ds_read_b128 a[80:83], v242 offset:1024\n"
      "v_or3_b32 v116, v116, v210, v211\n"
      "v_mfma_f32_16x16x32_bf16 a[156:159], a[108:111], v[220:223], a[156:159]\n"
      "buffer_load_dwordx4 v122, s[24:27], s61 offen nt lds\n"
      "v_exp_f32 v113, v169\n"
      "v_mfma_f32_16x16x32_bf16 a[152:155], a[108:111], v[216:219], a[152:155]\n"
      "s_add_u32 m0, s77, 25600\n"
      "ds_read_b128 a[84:87], v242 offset:5120\n"
      "v_cvt_pk_bf16_f32 v212, v112, v113\n"
      "v_mfma_f32_16x16x32_bf16 v[72:75], a[112:115], v[216:219], v[72:75]\n"
      "s_add_u32 s77, s77, 0x8000\n"
      "buffer_load_dwordx4 v123, s[24:27], s61 offen nt lds\n"
      "v_exp_f32 v114, v170\n"
      "v_mfma_f32_16x16x32_bf16 v[76:79], a[112:115], v[220:223], v[76:79]\n"
      "s_and_b32 s77, s77, 0x1ffff\n"
      "ds_read_b128 a[88:91], v243 offset:1024\n"
      "ds_read_b128 a[92:95], v243 offset:5120\n"
      "v_mfma_f32_16x16x32_bf16 v[92:95], a[116:119], v[220:223], v[92:95]\n"
      "s_add_u32 s61, s61, s78\n"
      "v_exp_f32 v115, v171\n"
      "v_mfma_f32_16x16x32_bf16 v[88:91], a[116:119], v[216:219], v[88:91]\n"
      "v_cvt_pk_bf16_f32 v213, v114, v115\n"
      "v_mfma_f32_16x16x32_bf16 a[168:171], a[120:123], v[216:219], a[168:171]\n"
      "v_exp_f32 v112, v172\n"
      "v_mfma_f32_16x16x32_bf16 a[172:175], a[120:123], v[220:223], a[172:175]\n"
      "v_or3_b32 v116, v116, v212, v213\n"
      "v_mfma_f32_16x16x32_bf16 a[188:191], a[124:127], v[220:223], a[188:191]\n"
      "v_exp_f32 v113, v173\n"
      "v_mfma_f32_16x16x32_bf16 a[184:187], a[124:127], v[216:219], a[184:187]\n"
      "s_waitcnt lgkmcnt(0)\n"
      "v_cvt_pk_bf16_f32 v214, v112, v113\n"
      "v_mfma_f32_16x16x32_bf16 v[128:131], a[64:67], a[0:3], v[224:227]\n"
      "s_sub_u32 s67, s68, s66\n"
      "v_exp_f32 v114, v174\n"
      "v_mfma_f32_16x16x32_bf16 v[128:131], a[72:75], a[16:19], v[128:131]\n"
      "s_cmp_gt_i32 s67, s70\n"
      "v_mfma_f32_16x16x32_bf16 v[128:131], a[80:83], a[32:35], v[128:131]\n"
      "s_cselect_b32 s69, s86, 0\n"
      "v_exp_f32 v115, v175\n"
      "v_mfma_f32_16x16x32_bf16 v[128:131], a[88:91], a[48:51], v[128:131]\n"
      "s_add_u32 s66, s66, s69\n"
      "v_cvt_pk_bf16_f32 v215, v114, v115\n"
      "v_mfma_f32_16x16x32_bf16 v[132:135], a[92:95], a[48:51], v[224:227]\n"
      "s_add_u32 s67, s67, s101\n"
      "v_exp_f32 v112, v176\n"
      "v_mfma_f32_16x16x32_bf16 v[132:135], a[84:87], a[32:35], v[132:135]\n"
      "s_min_i32 s70, s70, s67\n"
      "v_or3_b32 v116, v116, v214, v215\n"
      "v_mfma_f32_16x16x32_bf16 v[132:135], a[76:79], a[16:19], v[132:135]\n"
      "s_max_i32 s70, s70, 85\n"
      "v_exp_f32 v113, v177\n"
      "v_mfma_f32_16x16x32_bf16 v[132:135], a[68:71], a[0:3], v[132:135]\n"
      "s_add_u32 s66, s66, s86\n"
      "v_cvt_pk_bf16_f32 v216, v112, v113\n"
      "v_mfma_f32_16x16x32_bf16 v[140:143], a[68:71], a[4:7], v[228:231]\n"
      "s_and_b32 s76, s80, 31\n"
      "v_exp_f32 v114, v178\n"
      "v_mfma_f32_16x16x32_bf16 v[140:143], a[76:79], a[20:23], v[140:143]\n"
      "s_lshr_b64 s[80:81], s[80:81], 5\n"
      "v_mfma_f32_16x16x32_bf16 v[140:143], a[84:87], a[36:39], v[140:143]\n"
      "s_add_u32 s66, s66, s76\n"
      "v_exp_f32 v115, v179\n"
      "v_mfma_f32_16x16x32_bf16 v[140:143], a[92:95], a[52:55], v[140:143]\n"
      "s_sub_u32 s67, s68, s82\n"
      "v_cvt_pk_bf16_f32 v217, v114, v115\n"
      "v_mfma_f32_16x16x32_bf16 v[136:139], a[88:91], a[52:55], v[228:231]\n"
      "s_min_i32 s76, s67, s83\n"
      "v_exp_f32 v112, v180\n"
      "s_waitcnt vmcnt(8)\n"
      "s_barrier\n"
      "v_mfma_f32_16x16x32_bf16 v[136:139], a[80:83], a[36:39], v[136:139]\n"
      "s_mov_b32 s83, s67\n"
      "ds_read_b64_tr_b16 a[96:97], v244 offset:0\n"
      "v_or3_b32 v116, v116, v216, v217\n"
      "v_mfma_f32_16x16x32_bf16 v[136:139], a[72:75], a[20:23], v[136:139]\n"
      "s_sub_u32 s76, s76, 36\n"
      "v_exp_f32 v113, v181\n"
      "v_mfma_f32_16x16x32_bf16 v[136:139], a[64:67], a[4:7], v[136:139]\n"
      "s_min_i32 s76, s76, 148\n"
      "ds_read_b64_tr_b16 a[98:99], v244 offset:4096\n"
      "v_cvt_pk_bf16_f32 v218, v112, v113\n"
      "v_mfma_f32_16x16x32_bf16 v[144:147], a[64:67], a[8:11], v[232:235]\n"
      "s_max_i32 s76, s76, 70\n"
      "v_exp_f32 v114, v182\n"
      "v_mfma_f32_16x16x32_bf16 v[144:147], a[72:75], a[24:27], v[144:147]\n"
      "s_mov_b32 s82, s68\n"
      "ds_read_b64_tr_b16 a[100:101], v245 offset:0\n"
      "ds_read_b64_tr_b16 a[102:103], v245 offset:4096\n"
      "v_mfma_f32_16x16x32_bf16 v[144:147], a[80:83], a[40:43], v[144:147]\n"
      "s_add_u32 s65, s82, s76\n"
      "v_exp_f32 v115, v183\n"
      "v_mfma_f32_16x16x32_bf16 v[144:147], a[88:91], a[56:59], v[144:147]\n"
      "s_sub_u32 s93, s93, 1\n"
      "ds_read_b64_tr_b16 a[104:105], v246 offset:0\n"
      "v_cvt_pk_bf16_f32 v219, v114, v115\n"
      "v_mfma_f32_16x16x32_bf16 v[148:151], a[92:95], a[56:59], v[232:235]\n"
      "s_cmp_le_i32 s93, 14\n"
      "v_exp_f32 v112, v184\n"
      "v_mfma_f32_16x16x32_bf16 v[148:151], a[84:87], a[40:43], v[148:151]\n"
      "s_cselect_b32 s86, 107, s86\n"
      "ds_read_b64_tr_b16 a[106:107], v246 offset:4096\n"
      "v_or3_b32 v116, v116, v218, v219\n"
      "v_mfma_f32_16x16x32_bf16 v[148:151], a[76:79], a[24:27], v[148:151]\n"
      "s_sub_u32 s67, s65, s66\n"
      "v_exp_f32 v113, v185\n"
      "v_mfma_f32_16x16x32_bf16 v[148:151], a[68:71], a[8:11], v[148:151]\n"
      "s_cmp_lt_i32 s67, 0\n"
      "ds_read_b64_tr_b16 a[108:109], v247 offset:0\n"
      "v_cvt_pk_bf16_f32 v220, v112, v113\n"
      "v_mfma_f32_16x16x32_bf16 v[156:159], a[68:71], a[12:15], v[236:239]\n"
      "s_cselect_b32 s88, s66, s65\n"
      "v_exp_f32 v114, v186\n"
      "v_mfma_f32_16x16x32_bf16 v[156:159], a[76:79], a[28:31], v[156:159]\n"
      "s_add_u32 s90, s88, s91\n"
      "ds_read_b64_tr_b16 a[110:111], v247 offset:4096\n"
      "ds_read_b64_tr_b16 a[112:113], v244 offset:1024\n"
      "v_mfma_f32_16x16x32_bf16 v[156:159], a[84:87], a[44:47], v[156:159]\n"
      "v_exp_f32 v115, v187\n"
      "v_mfma_f32_16x16x32_bf16 v[156:159], a[92:95], a[60:63], v[156:159]\n"
      "ds_read_b64_tr_b16 a[114:115], v244 offset:5120\n"
      "v_cvt_pk_bf16_f32 v221, v114, v115\n"
      "v_mfma_f32_16x16x32_bf16 v[152:155], a[88:91], a[60:63], v[236:239]\n"
      "v_exp_f32 v112, v188\n"
      "v_mfma_f32_16x16x32_bf16 v[152:155], a[80:83], a[44:47], v[152:155]\n"
      "ds_read_b64_tr_b16 a[116:117], v245 offset:1024\n"
      "v_or3_b32 v116, v116, v220, v221\n"
      "v_mfma_f32_16x16x32_bf16 v[152:155], a[72:75], a[28:31], v[152:155]\n"
      "v_exp_f32 v113, v189\n"
      "v_mfma_f32_16x16x32_bf16 v[152:155], a[64:67], a[12:15], v[152:155]\n"
      "ds_read_b64_tr_b16 a[118:119], v245 offset:5120\n"
      "v_cvt_pk_bf16_f32 v222, v112, v113\n"
      "s_waitcnt lgkmcnt(6)\n"
      "v_mfma_f32_16x16x32_bf16 v[32:35], a[96:99], v[192:195], v[32:35]\n"
      "v_exp_f32 v114, v190\n"
      "v_mfma_f32_16x16x32_bf16 v[48:51], a[100:103], v[192:195], v[48:51]\n"
      "ds_read_b64_tr_b16 a[120:121], v246 offset:1024\n"
      "ds_read_b64_tr_b16 a[122:123], v246 offset:5120\n"
      "v_mfma_f32_16x16x32_bf16 a[128:131], a[104:107], v[192:195], a[128:131]\n"
      "s_waitcnt lgkmcnt(4)\n"
      "v_exp_f32 v115, v191\n"
      "v_mfma_f32_16x16x32_bf16 a[144:147], a[108:111], v[192:195], a[144:147]\n"
      "ds_read_b64_tr_b16 a[124:125], v247 offset:1024\n"
      "v_cvt_pk_bf16_f32 v223, v114, v115\n"
      "v_mfma_f32_16x16x32_bf16 v[64:67], a[112:115], v[192:195], v[64:67]\n"
      "ds_read_b64_tr_b16 a[126:127], v247 offset:5120\n"
      "s_waitcnt lgkmcnt(2)\n"
      "v_mfma_f32_16x16x32_bf16 v[80:83], a[116:119], v[192:195], v[80:83]\n"
      "v_or3_b32 v116, v116, v222, v223\n"
      "v_mfma_f32_16x16x32_bf16 a[160:163], a[120:123], v[192:195], a[160:163]\n"
      "s_mov_b32 s64, 3\n"
      "s_waitcnt lgkmcnt(0)\n"
      "v_and_b32 v116, 0x40004000, v116\n"
      "v_mfma_f32_16x16x32_bf16 a[176:179], a[124:127], v[192:195], a[176:179]\n"
      "v_cmp_ne_u32 vcc, 0, v116\n"
      "s_cbranch_vccnz FB1_%=\n"
      "RET1_3_%=:\n"
      "v_mfma_f32_16x16x32_bf16 a[180:183], a[124:127], v[196:199], a[180:183]\n"
      "v_exp_f32 v112, v128\n"
      "v_mfma_f32_16x16x32_bf16 a[164:167], a[120:123], v[196:199], a[164:167]\n"
      "v_mfma_f32_16x16x32_bf16 a[192:195], a[196:199], v[208:211], a[192:195]\n"
      "s_add_u32 m0, s62, 0\n"
      "v_exp_f32 v113, v129\n"
      "v_mfma_f32_16x16x32_bf16 v[84:87], a[116:119], v[196:199], v[84:87]\n"
      "buffer_load_dwordx4 v248, s[20:23], s60 offen nt lds\n"
      "v_cvt_pk_bf16_f32 v192, v112, v113\n"
      "v_mfma_f32_16x16x32_bf16 v[68:71], a[112:115], v[196:199], v[68:71]\n"
      "s_add_u32 m0, s62, 896\n"
      "v_exp_f32 v114, v130\n"
      "v_mfma_f32_16x16x32_bf16 a[192:195], a[200:203], v[212:215], a[192:195]\n"
      "buffer_load_dwordx4 v248, s[20:23], s60 offen offset:128 nt lds\n"
      "v_mfma_f32_16x16x32_bf16 a[148:151], a[108:111], v[196:199], a[148:151]\n"
      "s_add_u32 m0, s62, 8192\n"
      "v_exp_f32 v115, v131\n"
      "v_mfma_f32_16x16x32_bf16 a[132:135], a[104:107], v[196:199], a[132:135]\n"
      "s_memrealtime s[68:69]\n"
      "buffer_load_dwordx4 v120, s[20:23], s60 offen nt lds\n"
      "v_cvt_pk_bf16_f32 v193, v114, v115\n"
      "v_mfma_f32_16x16x32_bf16 a[192:195], a[204:207], v[216:219], a[192:195]\n"
      "s_add_u32 m0, s62, 9088\n"
      "v_exp_f32 v112, v132\n"
      "v_mfma_f32_16x16x32_bf16 v[52:55], a[100:103], v[196:199], v[52:55]\n"
      "s_add_u32 s62, s62, 0x8000\n"
      "buffer_load_dwordx4 v120, s[20:23], s60 offen offset:128 nt lds\n"
      "v_or_b32 v116, v192, v193\n"
      "v_mfma_f32_16x16x32_bf16 v[36:39], a[96:99], v[196:199], v[36:39]\n"
      "s_and_b32 s62, s62, 0x1ffff\n"
      "v_exp_f32 v113, v133\n"
      "v_mfma_f32_16x16x32_bf16 a[192:195], a[208:211], v[220:223], a[192:195]\n"
      "s_add_u32 s60, s60, %[inc]\n"
      "v_cvt_pk_bf16_f32 v194, v112, v113\n"
      "s_waitcnt vmcnt(8) lgkmcnt(0)\n"
      "s_sub_u32 s67, s68, s90\n"
      "s_cmp_lt_i32 s67, 0\n"
      "s_cbranch_scc1 GW5_%=\n"
      "GX5_%=:\n"
      "s_barrier\n"
      "v_mfma_f32_16x16x32_bf16 v[40:43], a[96:99], v[200:203], v[40:43]\n"
      "s_memrealtime s[68:69]\n"
      "buffer_load_dword v250, v251, s[28:31], 0 offen sc1\n"
      "ds_read_b128 a[64:67], v242 offset:32768\n"
      "v_exp_f32 v114, v134\n"
      "v_mfma_f32_16x16x32_bf16 v[44:47], a[96:99], v[204:207], v[44:47]\n"
      "s_add_u32 m0, s77, 16384\n"
      "ds_read_b128 a[68:71], v242 offset:36864\n"
      "ds_read_b128 a[72:75], v243 offset:32768\n"
      "v_mfma_f32_16x16x32_bf16 v[60:63], a[100:103], v[204:207], v[60:63]\n"
      "buffer_load_dwordx4 v249, s[24:27], s61 offen nt lds\n"
      "v_exp_f32 v115, v135\n"
      "v_mfma_f32_16x16x32_bf16 v[56:59], a[100:103], v[200:203], v[56:59]\n"
      "s_add_u32 m0, s77, 17408\n"
      "ds_read_b128 a[76:79], v243 offset:36864\n"
      "v_cvt_pk_bf16_f32 v195, v114, v115\n"
      "v_mfma_f32_16x16x32_bf16 a[136:139], a[104:107], v[200:203], a[136:139]\n"
      "buffer_load_dwordx4 v121, s[24:27], s61 offen nt lds\n"
      "v_exp_f32 v112, v136\n"
      "v_mfma_f32_16x16x32_bf16 a[140:143], a[104:107], v[204:207], a[140:143]\n"
      "s_add_u32 m0, s77, 24576\n"
      "ds_read_b128 a[80:83], v242 offset:33792\n"
      "v_or3_b32 v116, v116, v194, v195\n"
      "v_mfma_f32_16x16x32_bf16 a[156:159], a[108:111], v[204:207], a[156:159]\n"
      "buffer_load_dwordx4 v122, s[24:27], s61 offen nt lds\n"
      "v_exp_f32 v113, v137\n"
      "v_mfma_f32_16x16x32_bf16 a[152:155], a[108:111], v[200:203], a[152:155]\n"
      "s_add_u32 m0, s77, 25600\n"
      "ds_read_b128 a[84:87], v242 offset:37888\n"
      "v_cvt_pk_bf16_f32 v196, v112, v113\n"
      "v_mfma_f32_16x16x32_bf16 v[72:75], a[112:115], v[200:203], v[72:75]\n"
      "s_add_u32 s77, s77, 0x8000\n"
      "buffer_load_dwordx4 v123, s[24:27], s61 offen nt lds\n"
      "v_exp_f32 v114, v138\n"
      "v_mfma_f32_16x16x32_bf16 v[76:79], a[112:115], v[204:207], v[76:79]\n"
      "s_and_b32 s77, s77, 0x1ffff\n"
      "ds_read_b128 a[88:91], v243 offset:33792\n"
      "ds_read_b128 a[92:95], v243 offset:37888\n"
      "v_mfma_f32_16x16x32_bf16 v[92:95], a[116:119], v[204:207], v[92:95]\n"
      "s_add_u32 s61, s61, %[inc]\n"
      "v_exp_f32 v115, v139\n"
      "v_mfma_f32_16x16x32_bf16 v[88:91], a[116:119], v[200:203], v[88:91]\n"
      "v_cvt_pk_bf16_f32 v197, v114, v115\n"
      "v_mfma_f32_16x16x32_bf16 a[168:171], a[120:123], v[200:203], a[168:171]\n"
      "v_exp_f32 v112, v140\n"
      "v_mfma_f32_16x16x32_bf16 a[172:175], a[120:123], v[204:207], a[172:175]\n"
      "v_or3_b32 v116, v116, v196, v197\n"
      "v_mfma_f32_16x16x32_bf16 a[188:191], a[124:127], v[204:207], a[188:191]\n"
      "v_exp_f32 v113, v141\n"
      "v_mfma_f32_16x16x32_bf16 a[184:187], a[124:127], v[200:203], a[184:187]\n"
      "s_waitcnt lgkmcnt(0)\n"
      "v_cvt_pk_bf16_f32 v198, v112, v113\n"
      "v_mfma_f32_16x16x32_bf16 v[160:163], a[64:67], a[0:3], v[224:227]\n"
      "s_sub_u32 s67, s68, s66\n"
      "v_exp_f32 v114, v142\n"
      "v_mfma_f32_16x16x32_bf16 v[160:163], a[72:75], a[16:19], v[160:163]\n"
      "s_cmp_gt_i32 s67, s70\n"
      "v_mfma_f32_16x16x32_bf16 v[160:163], a[80:83], a[32:35], v[160:163]\n"
      "s_cselect_b32 s69, s86, 0\n"
      "v_exp_f32 v115, v143\n"
      "v_mfma_f32_16x16x32_bf16 v[160:163], a[88:91], a[48:51], v[160:163]\n"
      "s_add_u32 s66, s66, s69\n"
      "v_cvt_pk_bf16_f32 v199, v114, v115\n"
      "v_mfma_f32_16x16x32_bf16 v[164:167], a[92:95], a[48:51], v[224:227]\n"
      "s_add_u32 s67, s67, s101\n"
      "v_exp_f32 v112, v144\n"
      "v_mfma_f32_16x16x32_bf16 v[164:167], a[84:87], a[32:35], v[164:167]\n"
      "s_min_i32 s70, s70, s67\n"
      "v_or3_b32 v116, v116, v198, v199\n"
      "v_mfma_f32_16x16x32_bf16 v[164:167], a[76:79], a[16:19], v[164:167]\n"
      "s_max_i32 s70, s70, 85\n"
      "v_exp_f32 v113, v145\n"
      "v_mfma_f32_16x16x32_bf16 v[164:167], a[68:71], a[0:3], v[164:167]\n"
      "s_add_u32 s66, s66, s86\n"
      "v_cvt_pk_bf16_f32 v200, v112, v113\n"
      "v_mfma_f32_16x16x32_bf16 v[172:175], a[68:71], a[4:7], v[228:231]\n"
      "s_and_b32 s76, s80, 31\n"
      "v_exp_f32 v114, v146\n"
      "v_mfma_f32_16x16x32_bf16 v[172:175], a[76:79], a[20:23], v[172:175]\n"
      "s_lshr_b64 s[80:81], s[80:81], 5\n"
      "v_mfma_f32_16x16x32_bf16 v[172:175], a[84:87], a[36:39], v[172:175]\n"
      "s_add_u32 s66, s66, s76\n"
      "v_exp_f32 v115, v147\n"
      "v_mfma_f32_16x16x32_bf16 v[172:175], a[92:95], a[52:55], v[172:175]\n"
      "s_sub_u32 s67, s68, s82\n"
      "v_cvt_pk_bf16_f32 v201, v114, v115\n"
      "v_mfma_f32_16x16x32_bf16 v[168:171], a[88:91], a[52:55], v[228:231]\n"
      "s_min_i32 s76, s67, s83\n"
      "v_exp_f32 v112, v148\n"
      "s_waitcnt vmcnt(9)\n"
      "s_barrier\n"
      "v_mfma_f32_16x16x32_bf16 v[168:171], a[80:83], a[36:39], v[168:171]\n"
      "s_mov_b32 s83, s67\n"
      "ds_read_b64_tr_b16 a[96:97], v244 offset:32768\n"
      "v_or3_b32 v116, v116, v200, v201\n"
      "v_mfma_f32_16x16x32_bf16 v[168:171], a[72:75], a[20:23], v[168:171]\n"
      "s_sub_u32 s76, s76, 36\n"
      "v_exp_f32 v113, v149\n"
      "v_mfma_f32_16x16x32_bf16 v[168:171], a[64:67], a[4:7], v[168:171]\n"
      "s_min_i32 s76, s76, 148\n"
      "ds_read_b64_tr_b16 a[98:99], v244 offset:36864\n"
      "v_cvt_pk_bf16_f32 v202, v112, v113\n"
      "v_mfma_f32_16x16x32_bf16 v[176:179], a[64:67], a[8:11], v[232:235]\n"
      "s_max_i32 s76, s76, 70\n"
      "v_exp_f32 v114, v150\n"
      "v_mfma_f32_16x16x32_bf16 v[176:179], a[72:75], a[24:27], v[176:179]\n"
      "s_mov_b32 s82, s68\n"
      "ds_read_b64_tr_b16 a[100:101], v245 offset:32768\n"
      "ds_read_b64_tr_b16 a[102:103], v245 offset:36864\n"
      "v_mfma_f32_16x16x32_bf16 v[176:179], a[80:83], a[40:43], v[176:179]\n"
      "s_add_u32 s65, s82, s76\n"
      "v_exp_f32 v115, v151\n"
      "v_mfma_f32_16x16x32_bf16 v[176:179], a[88:91], a[56:59], v[176:179]\n"
      "s_sub_u32 s93, s93, 1\n"
      "ds_read_b64_tr_b16 a[104:105], v246 offset:32768\n"
      "v_cvt_pk_bf16_f32 v203, v114, v115\n"
      "v_mfma_f32_16x16x32_bf16 v[180:183], a[92:95], a[56:59], v[232:235]\n"
      "s_cmp_le_i32 s93, 14\n"
      "v_exp_f32 v112, v152\n"
      "v_mfma_f32_16x16x32_bf16 v[180:183], a[84:87], a[40:43], v[180:183]\n"
      "s_cselect_b32 s86, 107, s86\n"
      "ds_read_b64_tr_b16 a[106:107], v246 offset:36864\n"
      "v_or3_b32 v116, v116, v202, v203\n"
      "v_mfma_f32_16x16x32_bf16 v[180:183], a[76:79], a[24:27], v[180:183]\n"
      "s_sub_u32 s67, s65, s66\n"
      "v_exp_f32 v113, v153\n"
      "v_mfma_f32_16x16x32_bf16 v[180:183], a[68:71], a[8:11], v[180:183]\n"
      "s_cmp_lt_i32 s67, 0\n"
      "ds_read_b64_tr_b16 a[108:109], v247 offset:32768\n"
      "v_cvt_pk_bf16_f32 v204, v112, v113\n"
      "v_mfma_f32_16x16x32_bf16 v[188:191], a[68:71], a[12:15], v[236:239]\n"
      "s_cselect_b32 s88, s66, s65\n"
      "v_exp_f32 v114, v154\n"
      "v_mfma_f32_16x16x32_bf16 v[188:191], a[76:79], a[28:31], v[188:191]\n"
      "s_add_u32 s90, s88, s91\n"
      "ds_read_b64_tr_b16 a[110:111], v247 offset:36864\n"
      "ds_read_b64_tr_b16 a[112:113], v244 offset:33792\n"
      "v_mfma_f32_16x16x32_bf16 v[188:191], a[84:87], a[44:47], v[188:191]\n"
      "v_exp_f32 v115, v155\n"
      "v_mfma_f32_16x16x32_bf16 v[188:191], a[92:95], a[60:63], v[188:191]\n"
      "ds_read_b64_tr_b16 a[114:115], v244 offset:37888\n"
      "v_cvt_pk_bf16_f32 v205, v114, v115\n"
      "v_mfma_f32_16x16x32_bf16 v[184:187], a[88:91], a[60:63], v[236:239]\n"
      "v_exp_f32 v112, v156\n"
      "v_mfma_f32_16x16x32_bf16 v[184:187], a[80:83], a[44:47], v[184:187]\n"
      "ds_read_b64_tr_b16 a[116:117], v245 offset:33792\n"
      "v_or3_b32 v116, v116, v204, v205\n"
      "v_mfma_f32_16x16x32_bf16 v[184:187], a[72:75], a[28:31], v[184:187]\n"
      "v_exp_f32 v113, v157\n"
      "v_mfma_f32_16x16x32_bf16 v[184:187], a[64:67], a[12:15], v[184:187]\n"
      "ds_read_b64_tr_b16 a[118:119], v245 offset:37888\n"
      "v_cvt_pk_bf16_f32 v206, v112, v113\n"
      "s_waitcnt lgkmcnt(6)\n"
      "v_mfma_f32_16x16x32_bf16 v[32:35], a[96:99], v[208:211], v[32:35]\n"
      "v_exp_f32 v114, v158\n"
      "v_mfma_f32_16x16x32_bf16 v[48:51], a[100:103], v[208:211], v[48:51]\n"
      "ds_read_b64_tr_b16 a[120:121], v246 offset:33792\n"
      "ds_read_b64_tr_b16 a[122:123], v246 offset:37888\n"
      "v_mfma_f32_16x16x32_bf16 a[128:131], a[104:107], v[208:211], a[128:131]\n"
      "s_waitcnt lgkmcnt(4)\n"
      "v_exp_f32 v115, v159\n"
      "v_mfma_f32_16x16x32_bf16 a[144:147], a[108:111], v[208:211], a[144:147]\n"
      "ds_read_b64_tr_b16 a[124:125], v247 offset:33792\n"
      "v_cvt_pk_bf16_f32 v207, v114, v115\n"
      "v_mfma_f32_16x16x32_bf16 v[64:67], a[112:115], v[208:211], v[64:67]\n"
      "ds_read_b64_tr_b16 a[126:127], v247 offset:37888\n"
      "s_waitcnt lgkmcnt(2)\n"
      "v_mfma_f32_16x16x32_bf16 v[80:83], a[116:119], v[208:211], v[80:83]\n"
      "v_or3_b32 v116, v116, v206, v207\n"
      "v_mfma_f32_16x16x32_bf16 a[160:163], a[120:123], v[208:211], a[160:163]\n"
      "s_mov_b32 s64, 3\n"
      "s_waitcnt lgkmcnt(0)\n"
      "v_and_b32 v116, 0x40004000, v116\n"
      "v_mfma_f32_16x16x32_bf16 a[176:179], a[124:127], v[208:211], a[176:179]\n"
      "v_cmp_ne_u32 vcc, 0, v116\n"
      "s_cbranch_vccnz FB0_%=\n"
      "RET0_3_%=:\n"
      "v_mfma_f32_16x16x32_bf16 a[180:183], a[124:127], v[212:215], a[180:183]\n"
      "v_exp_f32 v112, v160\n"
      "v_mfma_f32_16x16x32_bf16 a[164:167], a[120:123], v[212:215], a[164:167]\n"
      "v_xor_b32 v242, 0x10000, v242\n"
      "v_mfma_f32_16x16x32_bf16 a[192:195], a[196:199], v[192:195], a[192:195]\n"
      "s_add_u32 m0, s62, 0\n"
      "v_exp_f32 v113, v161\n"
      "v_mfma_f32_16x16x32_bf16 v[84:87], a[116:119], v[212:215], v[84:87]\n"
      "buffer_load_dwordx4 v248, s[20:23], s60 offen nt lds\n"
      "v_cvt_pk_bf16_f32 v208, v112, v113\n"
      "v_xor_b32 v243, 0x10000, v243\n"
      "v_mfma_f32_16x16x32_bf16 v[68:71], a[112:115], v[212:215], v[68:71]\n"
      "s_add_u32 m0, s62, 896\n"
      "v_exp_f32 v114, v162\n"
      "v_mfma_f32_16x16x32_bf16 a[192:195], a[200:203], v[196:199], a[192:195]\n"
      "buffer_load_dwordx4 v248, s[20:23], s60 offen offset:128 nt lds\n"
      "v_xor_b32 v244, 0x10000, v244\n"
      "v_mfma_f32_16x16x32_bf16 a[148:151], a[108:111], v[212:215], a[148:151]\n"
      "s_add_u32 m0, s62, 8192\n"
      "v_exp_f32 v115, v163\n"
      "v_mfma_f32_16x16x32_bf16 a[132:135], a[104:107], v[212:215], a[132:135]\n"
      "s_memrealtime s[68:69]\n"
      "buffer_load_dwordx4 v120, s[20:23], s60 offen nt lds\n"
      "v_cvt_pk_bf16_f32 v209, v114, v115\n"
      "v_xor_b32 v245, 0x10000, v245\n"
      "v_mfma_f32_16x16x32_bf16 a[192:195], a[204:207], v[200:203], a[192:195]\n"
      "s_add_u32 m0, s62, 9088\n"
      "v_exp_f32 v112, v164\n"
      "v_mfma_f32_16x16x32_bf16 v[52:55], a[100:103], v[212:215], v[52:55]\n"
      "s_add_u32 s62, s62, 0x8000\n"
      "buffer_load_dwordx4 v120, s[20:23], s60 offen offset:128 nt lds\n"
      "v_or_b32 v116, v208, v209\n"
      "v_xor_b32 v246, 0x10000, v246\n"
      "v_mfma_f32_16x16x32_bf16 v[36:39], a[96:99], v[212:215], v[36:39]\n"
      "s_and_b32 s62, s62, 0x1ffff\n"
      "s_add_u32 s60, s60, s78\n"
      "v_exp_f32 v113, v165\n"
      "v_mfma_f32_16x16x32_bf16 a[192:195], a[208:211], v[204:207], a[192:195]\n"
      "s_sub_u32 s63, s63, 1\n"
      "v_cvt_pk_bf16_f32 v210, v112, v113\n"
      "v_xor_b32 v247, 0x10000, v247\n"
      "s_cbranch_scc0 LOOP2_%=\n"
      "s_waitcnt vmcnt(9) lgkmcnt(0)\n"
      "s_sub_u32 s67, s68, s90\n"
      "s_cmp_lt_i32 s67, 0\n"
      "s_cbranch_scc1 GW6_%=\n"
      "GX6_%=:\n"
      "s_barrier\n"
      "v_mfma_f32_16x16x32_bf16 v[40:43], a[96:99], v[216:219], v[40:43]\n"
      "s_memrealtime s[68:69]\n"
      "ds_read_b128 a[64:67], v242 offset:0\n"
      "v_exp_f32 v114, v166\n"
      "v_mfma_f32_16x16x32_bf16 v[44:47], a[96:99], v[220:223], v[44:47]\n"
      "s_add_u32 m0, %[wb], 50176\n"
      "ds_read_b128 a[68:71], v242 offset:4096\n"
      "ds_read_b128 a[72:75], v243 offset:0\n"
      "v_mfma_f32_16x16x32_bf16 v[60:63], a[100:103], v[220:223], v[60:63]\n"
      "s_add_u32 s94, s61, s78\n"
      "buffer_load_dwordx4 v121, s[24:27], s61 offen nt lds\n"
      "v_exp_f32 v115, v167\n"
      "v_mfma_f32_16x16x32_bf16 v[56:59], a[100:103], v[216:219], v[56:59]\n"
      "s_add_u32 m0, %[wb], 58368\n"
      "ds_read_b128 a[76:79], v243 offset:4096\n"
      "v_cvt_pk_bf16_f32 v211, v114, v115\n"
      "v_mfma_f32_16x16x32_bf16 a[136:139], a[104:107], v[216:219], a[136:139]\n"
      "s_add_u32 s95, s61, 0x10000\n"
      "buffer_load_dwordx4 v123, s[24:27], s61 offen nt lds\n"
      "v_exp_f32 v112, v168\n"
      "v_mfma_f32_16x16x32_bf16 a[140:143], a[104:107], v[220:223], a[140:143]\n"
      "s_add_u32 s77, s94, 0x10000\n"
      "ds_read_b128 a[80:83], v242 offset:1024\n"
      "v_or3_b32 v116, v116, v210, v211\n"
      "v_mfma_f32_16x16x32_bf16 a[156:159], a[108:111], v[220:223], a[156:159]\n"
      "s_add_u32 s78, s61, 0x20000\n"
      "v_exp_f32 v113, v169\n"
      "v_mfma_f32_16x16x32_bf16 a[152:155], a[108:111], v[216:219], a[152:155]\n"
      "ds_read_b128 a[84:87], v242 offset:5120\n"
      "v_cvt_pk_bf16_f32 v212, v112, v113\n"
      "v_mfma_f32_16x16x32_bf16 v[72:75], a[112:115], v[216:219], v[72:75]\n"
      "v_exp_f32 v114, v170\n"
      "v_mfma_f32_16x16x32_bf16 v[76:79], a[112:115], v[220:223], v[76:79]\n"
      "ds_read_b128 a[88:91], v243 offset:1024\n"
      "ds_read_b128 a[92:95], v243 offset:5120\n"
      "v_mfma_f32_16x16x32_bf16 v[92:95], a[116:119], v[220:223], v[92:95]\n"
      "v_exp_f32 v115, v171\n"
      "v_mfma_f32_16x16x32_bf16 v[88:91], a[116:119], v[216:219], v[88:91]\n"
      "v_cvt_pk_bf16_f32 v213, v114, v115\n"
      "v_mfma_f32_16x16x32_bf16 a[168:171], a[120:123], v[216:219], a[168:171]\n"
      "v_exp_f32 v112, v172\n"
      "v_mfma_f32_16x16x32_bf16 a[172:175], a[120:123], v[220:223], a[172:175]\n"
      "v_or3_b32 v116, v116, v212, v213\n"
      "v_mfma_f32_16x16x32_bf16 a[188:191], a[124:127], v[220:223], a[188:191]\n"
      "v_exp_f32 v113, v173\n"
      "v_mfma_f32_16x16x32_bf16 a[184:187], a[124:127], v[216:219], a[184:187]\n"
      "s_waitcnt lgkmcnt(0)\n"
      "v_cvt_pk_bf16_f32 v214, v112, v113\n"
      "v_mfma_f32_16x16x32_bf16 v[128:131], a[64:67], a[0:3], v[224:227]\n"
      "s_sub_u32 s67, s68, s66\n"
      "v_exp_f32 v114, v174\n"
      "v_mfma_f32_16x16x32_bf16 v[128:131], a[72:75], a[16:19], v[128:131]\n"
      "s_cmp_gt_i32 s67, s70\n"
      "v_mfma_f32_16x16x32_bf16 v[128:131], a[80:83], a[32:35], v[128:131]\n"
      "s_cselect_b32 s69, s86, 0\n"
      "v_exp_f32 v115, v175\n"
      "v_mfma_f32_16x16x32_bf16 v[128:131], a[88:91], a[48:51], v[128:131]\n"
      "s_add_u32 s66, s66, s69\n"
      "v_cvt_pk_bf16_f32 v215, v114, v115\n"
      "v_mfma_f32_16x16x32_bf16 v[132:135], a[92:95], a[48:51], v[224:227]\n"
      "s_add_u32 s67, s67, s101\n"
      "v_exp_f32 v112, v176\n"
      "v_mfma_f32_16x16x32_bf16 v[132:135], a[84:87], a[32:35], v[132:135]\n"
      "s_min_i32 s70, s70, s67\n"
      "v_or3_b32 v116, v116, v214, v215\n"
      "v_mfma_f32_16x16x32_bf16 v[132:135], a[76:79], a[16:19], v[132:135]\n"
      "s_max_i32 s70, s70, 85\n"
      "v_exp_f32 v113, v177\n"
      "v_mfma_f32_16x16x32_bf16 v[132:135], a[68:71], a[0:3], v[132:135]\n"
      "s_add_u32 s66, s66, s86\n"
      "v_cvt_pk_bf16_f32 v216, v112, v113\n"
      "v_mfma_f32_16x16x32_bf16 v[140:143], a[68:71], a[4:7], v[228:231]\n"
      "s_and_b32 s76, s80, 31\n"
      "v_exp_f32 v114, v178\n"
      "v_mfma_f32_16x16x32_bf16 v[140:143], a[76:79], a[20:23], v[140:143]\n"
      "s_lshr_b64 s[80:81], s[80:81], 5\n"
      "v_mfma_f32_16x16x32_bf16 v[140:143], a[84:87], a[36:39], v[140:143]\n"
      "s_add_u32 s66, s66, s76\n"
      "v_exp_f32 v115, v179\n"
      "v_mfma_f32_16x16x32_bf16 v[140:143], a[92:95], a[52:55], v[140:143]\n"
      "s_sub_u32 s67, s68, s82\n"
      "v_cvt_pk_bf16_f32 v217, v114, v115\n"
      "v_mfma_f32_16x16x32_bf16 v[136:139], a[88:91], a[52:55], v[228:231]\n"
      "s_min_i32 s76, s67, s83\n"
      "v_exp_f32 v112, v180\n"
      "s_waitcnt vmcnt(6)\n"
      "s_barrier\n"
      "v_mfma_f32_16x16x32_bf16 v[136:139], a[80:83], a[36:39], v[136:139]\n"
      "s_mov_b32 s83, s67\n"
      "ds_read_b64_tr_b16 a[96:97], v244 offset:0\n"
      "v_or3_b32 v116, v116, v216, v217\n"
      "v_mfma_f32_16x16x32_bf16 v[136:139], a[72:75], a[20:23], v[136:139]\n"
      "s_add_u32 m0, s62, 0\n"
      "v_exp_f32 v113, v181\n"
      "v_mfma_f32_16x16x32_bf16 v[136:139], a[64:67], a[4:7], v[136:139]\n"
      "s_sub_u32 s76, s76, 36\n"
      "ds_read_b64_tr_b16 a[98:99], v244 offset:4096\n"
      "buffer_load_dwordx4 v248, s[20:23], s60 offen nt lds\n"
      "v_cvt_pk_bf16_f32 v218, v112, v113\n"
      "v_mfma_f32_16x16x32_bf16 v[144:147], a[64:67], a[8:11], v[232:235]\n"
      "s_add_u32 m0, s62, 896\n"
      "v_exp_f32 v114, v182\n"
      "v_mfma_f32_16x16x32_bf16 v[144:147], a[72:75], a[24:27], v[144:147]\n"
      "s_min_i32 s76, s76, 148\n"
      "ds_read_b64_tr_b16 a[100:101], v245 offset:0\n"
      "ds_read_b64_tr_b16 a[102:103], v245 offset:4096\n"
      "buffer_load_dwordx4 v248, s[20:23], s60 offen offset:128 nt lds\n"
      "v_mfma_f32_16x16x32_bf16 v[144:147], a[80:83], a[40:43], v[144:147]\n"
      "s_add_u32 m0, s62, 8192\n"
      "v_exp_f32 v115, v183\n"
      "v_mfma_f32_16x16x32_bf16 v[144:147], a[88:91], a[56:59], v[144:147]\n"
      "s_max_i32 s76, s76, 70\n"
      "ds_read_b64_tr_b16 a[104:105], v246 offset:0\n"
      "buffer_load_dwordx4 v120, s[20:23], s60 offen nt lds\n"
      "v_cvt_pk_bf16_f32 v219, v114, v115\n"
      "v_mfma_f32_16x16x32_bf16 v[148:151], a[92:95], a[56:59], v[232:235]\n"
      "s_add_u32 m0, s62, 9088\n"
      "v_exp_f32 v112, v184\n"
      "v_mfma_f32_16x16x32_bf16 v[148:151], a[84:87], a[40:43], v[148:151]\n"
      "s_add_u32 s62, s62, 0x8000\n"
      "ds_read_b64_tr_b16 a[106:107], v246 offset:4096\n"
      "buffer_load_dwordx4 v120, s[20:23], s60 offen offset:128 nt lds\n"
      "v_or3_b32 v116, v116, v218, v219\n"
      "v_mfma_f32_16x16x32_bf16 v[148:151], a[76:79], a[24:27], v[148:151]\n"
      "s_and_b32 s62, s62, 0x1ffff\n"
      "v_exp_f32 v113, v185\n"
      "v_mfma_f32_16x16x32_bf16 v[148:151], a[68:71], a[8:11], v[148:151]\n"
      "s_add_u32 s60, s60, %[inc]\n"
      "ds_read_b64_tr_b16 a[108:109], v247 offset:0\n"
      "v_cvt_pk_bf16_f32 v220, v112, v113\n"
      "v_mfma_f32_16x16x32_bf16 v[156:159], a[68:71], a[12:15], v[236:239]\n"
      "s_mov_b32 s82, s68\n"
      "v_exp_f32 v114, v186\n"
      "v_mfma_f32_16x16x32_bf16 v[156:159], a[76:79], a[28:31], v[156:159]\n"
      "s_sub_u32 s66, s66, s99\n"
      "ds_read_b64_tr_b16 a[110:111], v247 offset:4096\n"
      "ds_read_b64_tr_b16 a[112:113], v244 offset:1024\n"
      "v_mfma_f32_16x16x32_bf16 v[156:159], a[84:87], a[44:47], v[156:159]\n"
      "s_sub_u32 s82, s82, s99\n"
      "v_exp_f32 v115, v187\n"
      "v_mfma_f32_16x16x32_bf16 v[156:159], a[92:95], a[60:63], v[156:159]\n"
      "s_add_u32 s65, s82, s76\n"
      "ds_read_b64_tr_b16 a[114:115], v244 offset:5120\n"
      "v_cvt_pk_bf16_f32 v221, v114, v115\n"
      "v_mfma_f32_16x16x32_bf16 v[152:155], a[88:91], a[60:63], v[236:239]\n"
      "s_sub_u32 s93, s93, 1\n"
      "v_exp_f32 v112, v188\n"
      "v_mfma_f32_16x16x32_bf16 v[152:155], a[80:83], a[44:47], v[152:155]\n"
      "s_cmp_le_i32 s93, 14\n"
      "ds_read_b64_tr_b16 a[116:117], v245 offset:1024\n"
      "v_or3_b32 v116, v116, v220, v221\n"
      "v_mfma_f32_16x16x32_bf16 v[152:155], a[72:75], a[28:31], v[152:155]\n"
      "s_cselect_b32 s86, 107, s86\n"
      "v_exp_f32 v113, v189\n"
      "v_mfma_f32_16x16x32_bf16 v[152:155], a[64:67], a[12:15], v[152:155]\n"
      "ds_read_b64_tr_b16 a[118:119], v245 offset:5120\n"
      "v_cvt_pk_bf16_f32 v222, v112, v113\n"
      "s_waitcnt lgkmcnt(6)\n"
      "v_mfma_f32_16x16x32_bf16 v[32:35], a[96:99], v[192:195], v[32:35]\n"
      "s_sub_u32 s67, s65, s66\n"
      "v_exp_f32 v114, v190\n"
      "v_mfma_f32_16x16x32_bf16 v[48:51], a[100:103], v[192:195], v[48:51]\n"
      "s_cmp_lt_i32 s67, 0\n"
      "s_cselect_b32 s88, s66, s65\n"
      "ds_read_b64_tr_b16 a[120:121], v246 offset:1024\n"
      "ds_read_b64_tr_b16 a[122:123], v246 offset:5120\n"
      "v_mfma_f32_16x16x32_bf16 a[128:131], a[104:107], v[192:195], a[128:131]\n"
      "s_waitcnt lgkmcnt(4)\n"
      "v_exp_f32 v115, v191\n"
      "v_mfma_f32_16x16x32_bf16 a[144:147], a[108:111], v[192:195], a[144:147]\n"
      "s_add_u32 s90, s88, s91\n"
      "ds_read_b64_tr_b16 a[124:125], v247 offset:1024\n"
      "v_cvt_pk_bf16_f32 v223, v114, v115\n"
      "v_mfma_f32_16x16x32_bf16 v[64:67], a[112:115], v[192:195], v[64:67]\n"
      "ds_read_b64_tr_b16 a[126:127], v247 offset:5120\n"
      "s_waitcnt lgkmcnt(2)\n"
      "v_mfma_f32_16x16x32_bf16 v[80:83], a[116:119], v[192:195], v[80:83]\n"
      "v_or3_b32 v116, v116, v222, v223\n"
      "v_mfma_f32_16x16x32_bf16 a[160:163], a[120:123], v[192:195], a[160:163]\n"
      "s_mov_b32 s64, 4\n"
      "s_waitcnt lgkmcnt(0)\n"
      "v_and_b32 v116, 0x40004000, v116\n"
      "v_mfma_f32_16x16x32_bf16 a[176:179], a[124:127], v[192:195], a[176:179]\n"
      "v_cmp_ne_u32 vcc, 0, v116\n"
      "s_cbranch_vccnz FB1_%=\n"
      "RET1_4_%=:\n"
      "v_mfma_f32_16x16x32_bf16 a[180:183], a[124:127], v[196:199], a[180:183]\n"
      "v_exp_f32 v112, v128\n"
      "v_mfma_f32_16x16x32_bf16 a[164:167], a[120:123], v[196:199], a[164:167]\n"
      "v_xor_b32 v240, 0x10000, v244\n"
      "v_mfma_f32_16x16x32_bf16 a[192:195], a[196:199], v[208:211], a[192:195]\n"
      "v_exp_f32 v113, v129\n"
      "v_mfma_f32_16x16x32_bf16 v[84:87], a[116:119], v[196:199], v[84:87]\n"
      "v_cvt_pk_bf16_f32 v192, v112, v113\n"
      "v_xor_b32 v241, 0x10000, v245\n"
      "v_mfma_f32_16x16x32_bf16 v[68:71], a[112:115], v[196:199], v[68:71]\n"
      "v_exp_f32 v114, v130\n"
      "v_mfma_f32_16x16x32_bf16 a[192:195], a[200:203], v[212:215], a[192:195]\n"
      "v_xor_b32 v250, 0x10000, v246\n"
      "v_mfma_f32_16x16x32_bf16 a[148:151], a[108:111], v[196:199], a[148:151]\n"
      "v_exp_f32 v115, v131\n"
      "v_mfma_f32_16x16x32_bf16 a[132:135], a[104:107], v[196:199], a[132:135]\n"
      "s_memrealtime s[68:69]\n"
      "v_cvt_pk_bf16_f32 v193, v114, v115\n"
      "v_xor_b32 v251, 0x10000, v247\n"
      "v_mfma_f32_16x16x32_bf16 a[192:195], a[204:207], v[216:219], a[192:195]\n"
      "v_exp_f32 v112, v132\n"
      "v_mfma_f32_16x16x32_bf16 v[52:55], a[100:103], v[196:199], v[52:55]\n"
      "v_or_b32 v116, v192, v193\n"
      "v_mfma_f32_16x16x32_bf16 v[36:39], a[96:99], v[196:199], v[36:39]\n"
      "v_exp_f32 v113, v133\n"
      "v_mfma_f32_16x16x32_bf16 a[192:195], a[208:211], v[220:223], a[192:195]\n"
      "v_cvt_pk_bf16_f32 v194, v112, v113\n"
      "s_waitcnt vmcnt(6) lgkmcnt(0)\n"
      "s_sub_u32 s67, s68, s90\n"
      "s_cmp_lt_i32 s67, 0\n"
      "s_cbranch_scc1 GW7_%=\n"
      "GX7_%=:\n"
      "s_barrier\n"
      "v_mfma_f32_16x16x32_bf16 v[40:43], a[96:99], v[200:203], v[40:43]\n"
      "s_memrealtime s[68:69]\n"
      "ds_read_b128 a[64:67], v242 offset:32768\n"
      "v_exp_f32 v114, v134\n"
      "v_mfma_f32_16x16x32_bf16 v[44:47], a[96:99], v[204:207], v[44:47]\n"
      "s_add_u32 m0, %[wb], 82944\n"
      "ds_read_b128 a[68:71], v242 offset:36864\n"
      "v_mfma_f32_16x16x32_bf16 v[60:63], a[100:103], v[204:207], v[60:63]\n"
      "ds_read_b128 a[72:75], v243 offset:32768\n"
      "buffer_load_dwordx4 v121, s[24:27], s94 offen nt lds\n"
      "v_mfma_f32_16x16x32_bf16 v[56:59], a[100:103], v[200:203], v[56:59]\n"
      "s_add_u32 m0, %[wb], 91136\n"
      "ds_read_b128 a[76:79], v243 offset:36864\n"
      "v_mfma_f32_16x16x32_bf16 a[136:139], a[104:107], v[200:203], a[136:139]\n"
      "ds_read_b128 a[80:83], v242 offset:33792\n"
      "buffer_load_dwordx4 v123, s[24:27], s94 offen nt lds\n"
      "v_mfma_f32_16x16x32_bf16 a[140:143], a[104:107], v[204:207], a[140:143]\n"
      "ds_read_b128 a[84:87], v242 offset:37888\n"
      "v_mfma_f32_16x16x32_bf16 a[156:159], a[108:111], v[204:207], a[156:159]\n"
      "ds_read_b128 a[88:91], v243 offset:33792\n"
      "v_mfma_f32_16x16x32_bf16 a[152:155], a[108:111], v[200:203], a[152:155]\n"
      "ds_read_b128 a[92:95], v243 offset:37888\n"
      "v_mfma_f32_16x16x32_bf16 v[72:75], a[112:115], v[200:203], v[72:75]\n"
      "v_exp_f32 v115, v135\n"
      "v_mfma_f32_16x16x32_bf16 v[76:79], a[112:115], v[204:207], v[76:79]\n"
      "v_exp_f32 v112, v136\n"
      "v_mfma_f32_16x16x32_bf16 v[92:95], a[116:119], v[204:207], v[92:95]\n"
      "v_cvt_pk_bf16_f32 v195, v114, v115\n"
      "v_exp_f32 v113, v137\n"
      "v_mfma_f32_16x16x32_bf16 v[88:91], a[116:119], v[200:203], v[88:91]\n"
      "v_or3_b32 v116, v116, v194, v195\n"
      "v_mfma_f32_16x16x32_bf16 a[168:171], a[120:123], v[200:203], a[168:171]\n"
      "v_exp_f32 v114, v138\n"
      "v_mfma_f32_16x16x32_bf16 a[172:175], a[120:123], v[204:207], a[172:175]\n"
      "v_cvt_pk_bf16_f32 v196, v112, v113\n"
      "v_exp_f32 v115, v139\n"
      "v_mfma_f32_16x16x32_bf16 a[188:191], a[124:127], v[204:207], a[188:191]\n"
      "v_exp_f32 v112, v140\n"
      "v_mfma_f32_16x16x32_bf16 a[184:187], a[124:127], v[200:203], a[184:187]\n"
      "s_waitcnt lgkmcnt(0)\n"
      "v_cvt_pk_bf16_f32 v197, v114, v115\n"
      "v_mfma_f32_16x16x32_bf16 v[160:163], a[64:67], a[0:3], v[224:227]\n"
      "s_sub_u32 s67, s68, s66\n"
      "v_exp_f32 v113, v141\n"
      "v_mfma_f32_16x16x32_bf16 v[168:171], a[64:67], a[4:7], v[228:231]\n"
      "s_cmp_gt_i32 s67, s70\n"
      "v_or3_b32 v116, v116, v196, v197\n"
      "v_exp_f32 v114, v142\n"
      "v_mfma_f32_16x16x32_bf16 v[176:179], a[64:67], a[8:11], v[232:235]\n"
      "s_cselect_b32 s69, s86, 0\n"
      "v_cvt_pk_bf16_f32 v198, v112, v113\n"
      "v_mfma_f32_16x16x32_bf16 v[184:187], a[64:67], a[12:15], v[236:239]\n"
      "s_add_u32 s66, s66, s69\n"
      "v_exp_f32 v115, v143\n"
      "v_mfma_f32_16x16x32_bf16 v[164:167], a[68:71], a[0:3], v[224:227]\n"
      "s_add_u32 s67, s67, s101\n"
      "v_exp_f32 v112, v144\n"
      "v_mfma_f32_16x16x32_bf16 v[172:175], a[68:71], a[4:7], v[228:231]\n"
      "s_min_i32 s70, s70, s67\n"
      "v_cvt_pk_bf16_f32 v199, v114, v115\n"
      "v_exp_f32 v113, v145\n"
      "v_mfma_f32_16x16x32_bf16 v[180:183], a[68:71], a[8:11], v[232:235]\n"
      "s_max_i32 s70, s70, 85\n"
      "v_or3_b32 v116, v116, v198, v199\n"
      "v_mfma_f32_16x16x32_bf16 v[188:191], a[68:71], a[12:15], v[236:239]\n"
      "s_waitcnt lgkmcnt(4)\n"
      "v_exp_f32 v114, v146\n"
      "v_mfma_f32_16x16x32_bf16 v[160:163], a[72:75], a[16:19], v[160:163]\n"
      "s_add_u32 s66, s66, s86\n"
      "v_cvt_pk_bf16_f32 v200, v112, v113\n"
      "v_exp_f32 v115, v147\n"
      "v_mfma_f32_16x16x32_bf16 v[168:171], a[72:75], a[20:23], v[168:171]\n"
      "s_and_b32 s76, s80, 31\n"
      "v_exp_f32 v112, v148\n"
      "v_mfma_f32_16x16x32_bf16 v[176:179], a[72:75], a[24:27], v[176:179]\n"
      "s_lshr_b64 s[80:81], s[80:81], 5\n"
      "v_cvt_pk_bf16_f32 v201, v114, v115\n"
      "v_mfma_f32_16x16x32_bf16 v[184:187], a[72:75], a[28:31], v[184:187]\n"
      "s_add_u32 s66, s66, s76\n"
      "v_exp_f32 v113, v149\n"
      "v_mfma_f32_16x16x32_bf16 v[164:167], a[76:79], a[16:19], v[164:167]\n"
      "s_sub_u32 s67, s68, s82\n"
      "v_or3_b32 v116, v116, v200, v201\n"
      "v_exp_f32 v114, v150\n"
      "v_mfma_f32_16x16x32_bf16 v[172:175], a[76:79], a[20:23], v[172:175]\n"
      "s_min_i32 s76, s67, s83\n"
      "v_cvt_pk_bf16_f32 v202, v112, v113\n"
      "v_mfma_f32_16x16x32_bf16 v[180:183], a[76:79], a[24:27], v[180:183]\n"
      "s_add_u32 m0, s62, 0\n"
      "v_exp_f32 v115, v151\n"
      "v_mfma_f32_16x16x32_bf16 v[188:191], a[76:79], a[28:31], v[188:191]\n"
      "buffer_load_dwordx4 v248, s[20:23], s60 offen nt lds\n"
      "s_waitcnt lgkmcnt(2)\n"
      "v_mfma_f32_16x16x32_bf16 v[160:163], a[80:83], a[32:35], v[160:163]\n"
      "s_add_u32 m0, s62, 896\n"
      "v_exp_f32 v112, v152\n"
      "v_mfma_f32_16x16x32_bf16 v[168:171], a[80:83], a[36:39], v[168:171]\n"
      "s_mov_b32 s83, s67\n"
      "buffer_load_dwordx4 v248, s[20:23], s60 offen offset:128 nt lds\n"
      "v_mfma_f32_16x16x32_bf16 v[176:179], a[80:83], a[40:43], v[176:179]\n"
      "s_add_u32 m0, s62, 8192\n"
      "v_mfma_f32_16x16x32_bf16 v[184:187], a[80:83], a[44:47], v[184:187]\n"
      "s_sub_u32 s76, s76, 36\n"
      "buffer_load_dwordx4 v120, s[20:23], s60 offen nt lds\n"
      "s_waitcnt vmcnt(9)\n"
      "s_barrier\n"
      "v_mfma_f32_16x16x32_bf16 v[164:167], a[84:87], a[32:35], v[164:167]\n"
      "s_add_u32 m0, s62, 9088\n"
      "ds_read_b64_tr_b16 a[96:97], v246 offset:33792\n"
      "v_mfma_f32_16x16x32_bf16 v[172:175], a[84:87], a[36:39], v[172:175]\n"
      "s_min_i32 s76, s76, 148\n"
      "ds_read_b64_tr_b16 a[98:99], v246 offset:37888\n"
      "buffer_load_dwordx4 v120, s[20:23], s60 offen offset:128 nt lds\n"
      "v_mfma_f32_16x16x32_bf16 v[180:183], a[84:87], a[40:43], v[180:183]\n"
      "s_max_i32 s76, s76, 70\n"
      "ds_read_b64_tr_b16 a[100:101], v247 offset:33792\n"
      "v_mfma_f32_16x16x32_bf16 v[188:191], a[84:87], a[44:47], v[188:191]\n"
      "ds_read_b64_tr_b16 a[102:103], v247 offset:37888\n"
      "s_waitcnt lgkmcnt(4)\n"
      "v_mfma_f32_16x16x32_bf16 v[160:163], a[88:91], a[48:51], v[160:163]\n"
      "s_mov_b32 s82, s68\n"
      "ds_read_b64_tr_b16 a[104:105], v244 offset:33792\n"
      "v_cvt_pk_bf16_f32 v203, v114, v115\n"
      "v_mfma_f32_16x16x32_bf16 v[168:171], a[88:91], a[52:55], v[168:171]\n"
      "s_sub_u32 s66, s66, s99\n"
      "ds_read_b64_tr_b16 a[106:107], v244 offset:37888\n"
      "v_exp_f32 v113, v153\n"
      "v_mfma_f32_16x16x32_bf16 v[176:179], a[88:91], a[56:59], v[176:179]\n"
      "s_sub_u32 s82, s82, s99\n"
      "ds_read_b64_tr_b16 a[108:109], v245 offset:33792\n"
      "v_mfma_f32_16x16x32_bf16 v[184:187], a[88:91], a[60:63], v[184:187]\n"
      "s_add_u32 s65, s82, s76\n"
      "ds_read_b64_tr_b16 a[110:111], v245 offset:37888\n"
      "v_or3_b32 v116, v116, v202, v203\n"
      "v_mfma_f32_16x16x32_bf16 v[164:167], a[92:95], a[48:51], v[164:167]\n"
      "s_sub_u32 s93, s93, 1\n"
      "v_exp_f32 v114, v154\n"
      "v_mfma_f32_16x16x32_bf16 v[172:175], a[92:95], a[52:55], v[172:175]\n"
      "s_cmp_le_i32 s93, 14\n"
      "v_cvt_pk_bf16_f32 v204, v112, v113\n"
      "v_exp_f32 v115, v155\n"
      "v_mfma_f32_16x16x32_bf16 v[180:183], a[92:95], a[56:59], v[180:183]\n"
      "s_cselect_b32 s86, 107, s86\n"
      "v_exp_f32 v112, v156\n"
      "v_mfma_f32_16x16x32_bf16 v[188:191], a[92:95], a[60:63], v[188:191]\n"
      "s_waitcnt lgkmcnt(4)\n"
      "v_cvt_pk_bf16_f32 v205, v114, v115\n"
      "v_mfma_f32_16x16x32_bf16 a[160:163], a[96:99], v[208:211], a[160:163]\n"
      "s_sub_u32 s67, s65, s66\n"
      "v_exp_f32 v113, v157\n"
      "v_mfma_f32_16x16x32_bf16 a[164:167], a[96:99], v[212:215], a[164:167]\n"
      "s_cmp_lt_i32 s67, 0\n"
      "v_or3_b32 v116, v116, v204, v205\n"
      "v_exp_f32 v114, v158\n"
      "v_mfma_f32_16x16x32_bf16 a[168:171], a[96:99], v[216:219], a[168:171]\n"
      "s_cselect_b32 s88, s66, s65\n"
      "v_cvt_pk_bf16_f32 v206, v112, v113\n"
      "v_mfma_f32_16x16x32_bf16 a[172:175], a[96:99], v[220:223], a[172:175]\n"
      "s_add_u32 s90, s88, s91\n"
      "v_exp_f32 v115, v159\n"
      "s_nop 0\n"
      "v_mfma_f32_16x16x32_bf16 a[176:179], a[100:103], v[208:211], a[176:179]\n"
      "s_mov_b32 s64, 4\n"
      "v_cvt_pk_bf16_f32 v207, v114, v115\n"
      "v_or3_b32 v116, v116, v206, v207\n"
      "v_mfma_f32_16x16x32_bf16 a[180:183], a[100:103], v[212:215], a[180:183]\n"
      "v_and_b32 v116, 0x40004000, v116\n"
      "v_cmp_ne_u32 vcc, 0, v116\n"
      "s_cbranch_vccnz FB0_%=\n"
      "RET0_4_%=:\n"
      "v_mfma_f32_16x16x32_bf16 a[184:187], a[100:103], v[216:219], a[184:187]\n"
      "v_exp_f32 v112, v160\n"
      "v_mfma_f32_16x16x32_bf16 a[188:191], a[100:103], v[220:223], a[188:191]\n"
      "s_memrealtime s[68:69]\n"
      "v_xor_b32 v242, 0x10000, v242\n"
      "v_mfma_f32_16x16x32_bf16 a[192:195], a[196:199], v[192:195], a[192:195]\n"
      "v_exp_f32 v113, v161\n"
      "v_mfma_f32_16x16x32_bf16 a[192:195], a[200:203], v[196:199], a[192:195]\n"
      "v_xor_b32 v243, 0x10000, v243\n"
      "v_exp_f32 v114, v162\n"
      "v_mfma_f32_16x16x32_bf16 a[192:195], a[204:207], v[200:203], a[192:195]\n"
      "v_cvt_pk_bf16_f32 v0, v112, v113\n"
      "v_mfma_f32_16x16x32_bf16 a[192:195], a[208:211], v[204:207], a[192:195]\n"
      "v_exp_f32 v115, v163\n"
      "s_waitcnt vmcnt(6) lgkmcnt(0)\n"
      "s_sub_u32 s67, s68, s90\n"
      "s_cmp_lt_i32 s67, 0\n"
      "s_cbranch_scc1 GW8_%=\n"
      "GX8_%=:\n"
      "s_barrier\n"
      "v_mfma_f32_16x16x32_bf16 v[64:67], a[104:107], v[208:211], v[64:67]\n"
      "ds_read_b128 a[64:67], v242 offset:0\n"
      "ds_read_b128 a[68:71], v242 offset:4096\n"
      "v_mfma_f32_16x16x32_bf16 v[68:71], a[104:107], v[212:215], v[68:71]\n"
      "ds_read_b128 a[72:75], v243 offset:0\n"
      "ds_read_b128 a[76:79], v243 offset:4096\n"
      "v_mfma_f32_16x16x32_bf16 v[72:75], a[104:107], v[216:219], v[72:75]\n"
      "ds_read_b128 a[80:83], v242 offset:1024\n"
      "ds_read_b128 a[84:87], v242 offset:5120\n"
      "v_exp_f32 v112, v164\n"
      "v_mfma_f32_16x16x32_bf16 v[76:79], a[104:107], v[220:223], v[76:79]\n"
      "ds_read_b128 a[88:91], v243 offset:1024\n"
      "ds_read_b128 a[92:95], v243 offset:5120\n"
      "v_mfma_f32_16x16x32_bf16 v[80:83], a[108:111], v[208:211], v[80:83]\n"
      "v_cvt_pk_bf16_f32 v1, v114, v115\n"
      "v_mfma_f32_16x16x32_bf16 v[84:87], a[108:111], v[212:215], v[84:87]\n"
      "v_exp_f32 v113, v165\n"
      "v_or_b32 v116, v0, v1\n"
      "v_mfma_f32_16x16x32_bf16 v[88:91], a[108:111], v[216:219], v[88:91]\n"
      "v_exp_f32 v114, v166\n"
      "v_cvt_pk_bf16_f32 v2, v112, v113\n"
      "v_mfma_f32_16x16x32_bf16 v[92:95], a[108:111], v[220:223], v[92:95]\n"
      "s_waitcnt lgkmcnt(6)\n"
      "v_exp_f32 v115, v167\n"
      "v_mfma_f32_16x16x32_bf16 v[128:131], a[64:67], a[0:3], v[224:227]\n"
      "v_exp_f32 v112, v168\n"
      "v_mfma_f32_16x16x32_bf16 v[136:139], a[64:67], a[4:7], v[228:231]\n"
      "s_add_u32 m0, %[wb], 115712\n"
      "v_cvt_pk_bf16_f32 v3, v114, v115\n"
      "v_exp_f32 v113, v169\n"
      "v_mfma_f32_16x16x32_bf16 v[144:147], a[64:67], a[8:11], v[232:235]\n"
      "buffer_load_dwordx4 v121, s[24:27], s95 offen nt lds\n"
      "v_mfma_f32_16x16x32_bf16 v[152:155], a[64:67], a[12:15], v[236:239]\n"
      "s_add_u32 m0, %[wb], 123904\n"
      "v_or3_b32 v116, v116, v2, v3\n"
      "v_mfma_f32_16x16x32_bf16 v[132:135], a[68:71], a[0:3], v[224:227]\n"
      "buffer_load_dwordx4 v123, s[24:27], s95 offen nt lds\n"
      "v_mfma_f32_16x16x32_bf16 v[140:143], a[68:71], a[4:7], v[228:231]\n"
      "s_add_u32 m0, %[wb], 17408\n"
      "v_exp_f32 v114, v170\n"
      "v_mfma_f32_16x16x32_bf16 v[148:151], a[68:71], a[8:11], v[232:235]\n"
      "buffer_load_dwordx4 v121, s[24:27], s77 offen nt lds\n"
      "v_mfma_f32_16x16x32_bf16 v[156:159], a[68:71], a[12:15], v[236:239]\n"
      "s_waitcnt lgkmcnt(4)\n"
      "s_add_u32 m0, %[wb], 25600\n"
      "v_cvt_pk_bf16_f32 v4, v112, v113\n"
      "v_mfma_f32_16x16x32_bf16 v[128:131], a[72:75], a[16:19], v[128:131]\n"
      "buffer_load_dwordx4 v123, s[24:27], s77 offen nt lds\n"
      "v_mfma_f32_16x16x32_bf16 v[136:139], a[72:75], a[20:23], v[136:139]\n"
      "v_exp_f32 v115, v171\n"
      "v_mfma_f32_16x16x32_bf16 v[144:147], a[72:75], a[24:27], v[144:147]\n"
      "v_exp_f32 v112, v172\n"
      "v_cvt_pk_bf16_f32 v5, v114, v115\n"
      "v_mfma_f32_16x16x32_bf16 v[152:155], a[72:75], a[28:31], v[152:155]\n"
      "v_exp_f32 v113, v173\n"
      "v_mfma_f32_16x16x32_bf16 v[132:135], a[76:79], a[16:19], v[132:135]\n"
      "v_or3_b32 v116, v116, v4, v5\n"
      "v_exp_f32 v114, v174\n"
      "v_mfma_f32_16x16x32_bf16 v[140:143], a[76:79], a[20:23], v[140:143]\n"
      "v_cvt_pk_bf16_f32 v6, v112, v113\n"
      "v_exp_f32 v115, v175\n"
      "v_mfma_f32_16x16x32_bf16 v[148:151], a[76:79], a[24:27], v[148:151]\n"
      "v_exp_f32 v112, v176\n"
      "v_mfma_f32_16x16x32_bf16 v[156:159], a[76:79], a[28:31], v[156:159]\n"
      "s_waitcnt lgkmcnt(2)\n"
      "v_cvt_pk_bf16_f32 v7, v114, v115\n"
      "v_mfma_f32_16x16x32_bf16 v[128:131], a[80:83], a[32:35], v[128:131]\n"
      "v_exp_f32 v113, v177\n"
      "v_or3_b32 v116, v116, v6, v7\n"
      "v_mfma_f32_16x16x32_bf16 v[136:139], a[80:83], a[36:39], v[136:139]\n"
      "v_exp_f32 v114, v178\n"
      "v_cvt_pk_bf16_f32 v8, v112, v113\n"
      "v_mfma_f32_16x16x32_bf16 v[144:147], a[80:83], a[40:43], v[144:147]\n"
      "v_exp_f32 v115, v179\n"
      "v_mfma_f32_16x16x32_bf16 v[152:155], a[80:83], a[44:47], v[152:155]\n"
      "v_exp_f32 v112, v180\n"
      "s_waitcnt vmcnt(8)\n"
      "s_barrier\n"
      "v_mfma_f32_16x16x32_bf16 v[132:135], a[84:87], a[32:35], v[132:135]\n"
      "ds_read_b64_tr_b16 a[112:113], v250 offset:1024\n"
      "v_cvt_pk_bf16_f32 v9, v114, v115\n"
      "v_mfma_f32_16x16x32_bf16 v[140:143], a[84:87], a[36:39], v[140:143]\n"
      "ds_read_b64_tr_b16 a[114:115], v250 offset:5120\n"
      "v_exp_f32 v113, v181\n"
      "v_mfma_f32_16x16x32_bf16 v[148:151], a[84:87], a[40:43], v[148:151]\n"
      "ds_read_b64_tr_b16 a[116:117], v251 offset:1024\n"
      "v_or3_b32 v116, v116, v8, v9\n"
      "v_mfma_f32_16x16x32_bf16 v[156:159], a[84:87], a[44:47], v[156:159]\n"
      "ds_read_b64_tr_b16 a[118:119], v251 offset:5120\n"
      "v_exp_f32 v114, v182\n"
      "s_waitcnt lgkmcnt(4)\n"
      "v_mfma_f32_16x16x32_bf16 v[128:131], a[88:91], a[48:51], v[128:131]\n"
      "ds_read_b64_tr_b16 a[120:121], v240 offset:1024\n"
      "v_cvt_pk_bf16_f32 v10, v112, v113\n"
      "v_mfma_f32_16x16x32_bf16 v[136:139], a[88:91], a[52:55], v[136:139]\n"
      "ds_read_b64_tr_b16 a[122:123], v240 offset:5120\n"
      "v_exp_f32 v115, v183\n"
      "v_mfma_f32_16x16x32_bf16 v[144:147], a[88:91], a[56:59], v[144:147]\n"
      "ds_read_b64_tr_b16 a[124:125], v241 offset:1024\n"
      "v_mfma_f32_16x16x32_bf16 v[152:155], a[88:91], a[60:63], v[152:155]\n"
      "ds_read_b64_tr_b16 a[126:127], v241 offset:5120\n"
      "v_exp_f32 v112, v184\n"
      "v_mfma_f32_16x16x32_bf16 v[132:135], a[92:95], a[48:51], v[132:135]\n"
      "v_cvt_pk_bf16_f32 v11, v114, v115\n"
      "v_exp_f32 v113, v185\n"
      "v_mfma_f32_16x16x32_bf16 v[140:143], a[92:95], a[52:55], v[140:143]\n"
      "v_or3_b32 v116, v116, v10, v11\n"
      "v_exp_f32 v114, v186\n"
      "v_mfma_f32_16x16x32_bf16 v[148:151], a[92:95], a[56:59], v[148:151]\n"
      "v_cvt_pk_bf16_f32 v12, v112, v113\n"
      "v_mfma_f32_16x16x32_bf16 v[156:159], a[92:95], a[60:63], v[156:159]\n"
      "s_waitcnt lgkmcnt(4)\n"
      "v_exp_f32 v115, v187\n"
      "v_mfma_f32_16x16x32_bf16 a[160:163], a[112:115], v[192:195], a[160:163]\n"
      "v_exp_f32 v112, v188\n"
      "v_cvt_pk_bf16_f32 v13, v114, v115\n"
      "v_mfma_f32_16x16x32_bf16 a[164:167], a[112:115], v[196:199], a[164:167]\n"
      "v_exp_f32 v113, v189\n"
      "v_or3_b32 v116, v116, v12, v13\n"
      "v_mfma_f32_16x16x32_bf16 a[168:171], a[112:115], v[200:203], a[168:171]\n"
      "v_exp_f32 v114, v190\n"
      "v_mfma_f32_16x16x32_bf16 a[172:175], a[112:115], v[204:207], a[172:175]\n"
      "v_cvt_pk_bf16_f32 v14, v112, v113\n"
      "v_exp_f32 v115, v191\n"
      "v_mfma_f32_16x16x32_bf16 a[176:179], a[116:119], v[192:195], a[176:179]\n"
      "s_mov_b32 s64, 5\n"
      "s_nop 0\n"
      "v_cvt_pk_bf16_f32 v15, v114, v115\n"
      "v_or3_b32 v116, v116, v14, v15\n"
      "v_mfma_f32_16x16x32_bf16 a[180:183], a[116:119], v[196:199], a[180:183]\n"
      "v_and_b32 v116, 0x40004000, v116\n"
      "v_cmp_ne_u32 vcc, 0, v116\n"
      "s_cbranch_vccnz FB1_%=\n"
      "RET1_5_%=:\n"
      "v_mfma_f32_16x16x32_bf16 a[184:187], a[116:119], v[200:203], a[184:187]\n"
      "v_exp_f32 v112, v128\n"
      "v_exp_f32 v113, v129\n"
      "v_mfma_f32_16x16x32_bf16 a[188:191], a[116:119], v[204:207], a[188:191]\n"
      "s_memrealtime s[68:69]\n"
      "s_add_u32 m0, %[wb], 50176\n"
      "v_exp_f32 v114, v130\n"
      "v_cvt_pk_bf16_f32 v16, v112, v113\n"
      "v_mfma_f32_16x16x32_bf16 a[192:195], a[196:199], v[0:3], a[192:195]\n"
      "buffer_load_dwordx4 v121, s[24:27], s78 offen nt lds\n"
      "v_mfma_f32_16x16x32_bf16 a[192:195], a[200:203], v[4:7], a[192:195]\n"
      "s_add_u32 m0, %[wb], 58368\n"
      "v_exp_f32 v115, v131\n"
      "v_exp_f32 v112, v132\n"
      "v_mfma_f32_16x16x32_bf16 a[192:195], a[204:207], v[8:11], a[192:195]\n"
      "buffer_load_dwordx4 v123, s[24:27], s78 offen nt lds\n"
      "v_cvt_pk_bf16_f32 v17, v114, v115\n"
      "v_mfma_f32_16x16x32_bf16 a[192:195], a[208:211], v[12:15], a[192:195]\n"
      "v_exp_f32 v113, v133\n"
      "v_or_b32 v116, v16, v17\n"
      "s_add_u32 s72, s72, 0x7f4a7c15\n"
      "s_addc_u32 s76, s79, 0x9e3779b9\n"
      "s_waitcnt vmcnt(6) lgkmcnt(0)\n"
      "s_barrier\n"
      "v_mfma_f32_16x16x32_bf16 v[64:67], a[120:123], v[192:195], v[64:67]\n"
      "s_xor_b32 s80, s35, 0x400000\n"
      "ds_read_b128 a[64:67], v242 offset:32768\n"
      "ds_read_b128 a[68:71], v242 offset:36864\n"
      "v_mfma_f32_16x16x32_bf16 v[68:71], a[120:123], v[196:199], v[68:71]\n"
      "s_add_u32 s79, s80, 0x1000\n"
      "ds_read_b128 a[72:75], v243 offset:32768\n"
      "ds_read_b128 a[76:79], v243 offset:36864\n"
      "v_exp_f32 v114, v134\n"
      "v_mfma_f32_16x16x32_bf16 v[72:75], a[120:123], v[200:203], v[72:75]\n"
      "s_add_u32 s81, s35, 0x1000\n"
      "ds_read_b128 a[80:83], v242 offset:33792\n"
      "ds_read_b128 a[84:87], v242 offset:37888\n"
      "v_cvt_pk_bf16_f32 v18, v112, v113\n"
      "v_mfma_f32_16x16x32_bf16 v[76:79], a[120:123], v[204:207], v[76:79]\n"
      "s_mov_b32 s63, 0x100000\n"
      "ds_read_b128 a[88:91], v243 offset:33792\n"
      "ds_read_b128 a[92:95], v243 offset:37888\n"
      "v_mfma_f32_16x16x32_bf16 v[80:83], a[124:127], v[192:195], v[80:83]\n"
      "s_mov_b32 s60, 0x928b2f\n"
      "v_exp_f32 v115, v135\n"
      "v_exp_f32 v112, v136\n"
      "v_mfma_f32_16x16x32_bf16 v[84:87], a[124:127], v[196:199], v[84:87]\n"
      "s_mov_b32 s62, 0x9818e9\n"
      "v_cvt_pk_bf16_f32 v19, v114, v115\n"
      "v_exp_f32 v113, v137\n"
      "v_mfma_f32_16x16x32_bf16 v[88:91], a[124:127], v[200:203], v[88:91]\n"
      "s_mov_b32 s70, 0xdd9dc9\n"
      "v_xor_b32 v248, 0x2000, v244\n"
      "v_or3_b32 v116, v116, v18, v19\n"
      "v_mfma_f32_16x16x32_bf16 v[92:95], a[124:127], v[204:207], v[92:95]\n"
      "s_waitcnt lgkmcnt(6)\n"
      "v_exp_f32 v114, v138\n"
      "v_cvt_pk_bf16_f32 v20, v112, v113\n"
      "v_mfma_f32_16x16x32_bf16 v[160:163], a[64:67], a[0:3], v[224:227]\n"
      "s_mov_b32 s71, 0x8ed905\n"
      "v_exp_f32 v115, v139\n"
      "v_mfma_f32_16x16x32_bf16 v[168:171], a[64:67], a[4:7], v[228:231]\n"
      "s_sub_u32 s88, 331, s38\n"
      "v_xor_b32 v120, 0x2000, v245\n"
      "v_exp_f32 v112, v140\n"
      "v_cvt_pk_bf16_f32 v21, v114, v115\n"
      "v_mfma_f32_16x16x32_bf16 v[176:179], a[64:67], a[8:11], v[232:235]\n"
      "s_add_u32 s88, s88, s66\n"
      "v_exp_f32 v113, v141\n"
      "v_mfma_f32_16x16x32_bf16 v[184:187], a[64:67], a[12:15], v[236:239]\n"
      "s_add_u32 s88, s88, s100\n"
      "v_or3_b32 v116, v116, v20, v21\n"
      "v_exp_f32 v114, v142\n"
      "v_mfma_f32_16x16x32_bf16 v[164:167], a[68:71], a[0:3], v[224:227]\n"
      "v_mbcnt_lo_u32_b32 v127, -1, 0\n"
      "v_cvt_pk_bf16_f32 v22, v112, v113\n"
      "v_exp_f32 v115, v143\n"
      "v_mfma_f32_16x16x32_bf16 v[172:175], a[68:71], a[4:7], v[228:231]\n"
      "v_exp_f32 v112, v144\n"
      "v_mfma_f32_16x16x32_bf16 v[180:183], a[68:71], a[8:11], v[232:235]\n"
      "v_cvt_pk_bf16_f32 v23, v114, v115\n"
      "v_exp_f32 v113, v145\n"
      "v_mfma_f32_16x16x32_bf16 v[188:191], a[68:71], a[12:15], v[236:239]\n"
      "s_waitcnt lgkmcnt(4)\n"
      "v_mbcnt_hi_u32_b32 v127, -1, v127\n"
      "v_or3_b32 v116, v116, v22, v23\n"
      "v_mfma_f32_16x16x32_bf16 v[160:163], a[72:75], a[16:19], v[160:163]\n"
      "v_exp_f32 v114, v146\n"
      "v_cvt_pk_bf16_f32 v24, v112, v113\n"
      "v_mfma_f32_16x16x32_bf16 v[168:171], a[72:75], a[20:23], v[168:171]\n"
      "s_add_u32 m0, %[wb], 49152\n"
      "v_exp_f32 v115, v147\n"
      "v_exp_f32 v112, v148\n"
      "v_mfma_f32_16x16x32_bf16 v[176:179], a[72:75], a[24:27], v[176:179]\n"
      "buffer_load_dwordx4 v249, s[24:27], s61 offen nt lds\n"
      "v_lshlrev_b32 v127, 4, v127\n"
      "v_mfma_f32_16x16x32_bf16 v[184:187], a[72:75], a[28:31], v[184:187]\n"
      "s_add_u32 m0, %[wb], 57344\n"
      "v_cvt_pk_bf16_f32 v25, v114, v115\n"
      "v_mfma_f32_16x16x32_bf16 v[164:167], a[76:79], a[16:19], v[164:167]\n"
      "buffer_load_dwordx4 v122, s[24:27], s61 offen nt lds\n"
      "v_mfma_f32_16x16x32_bf16 v[172:175], a[76:79], a[20:23], v[172:175]\n"
      "v_add_u32 v125, s33, v127\n"
      "v_exp_f32 v113, v149\n"
      "v_mfma_f32_16x16x32_bf16 v[180:183], a[76:79], a[24:27], v[180:183]\n"
      "v_or3_b32 v116, v116, v24, v25\n"
      "v_exp_f32 v114, v150\n"
      "v_mfma_f32_16x16x32_bf16 v[188:191], a[76:79], a[28:31], v[188:191]\n"
      "s_waitcnt lgkmcnt(2)\n"
      "v_cvt_pk_bf16_f32 v26, v112, v113\n"
      "v_exp_f32 v115, v151\n"
      "v_mfma_f32_16x16x32_bf16 v[160:163], a[80:83], a[32:35], v[160:163]\n"
      "v_add_u32 v126, s34, v127\n"
      "v_exp_f32 v112, v152\n"
      "v_mfma_f32_16x16x32_bf16 v[168:171], a[80:83], a[36:39], v[168:171]\n"
      "v_cvt_pk_bf16_f32 v27, v114, v115\n"
      "v_exp_f32 v113, v153\n"
      "v_mfma_f32_16x16x32_bf16 v[176:179], a[80:83], a[40:43], v[176:179]\n"
      "v_or3_b32 v116, v116, v26, v27\n"
      "v_mfma_f32_16x16x32_bf16 v[184:187], a[80:83], a[44:47], v[184:187]\n"
      "v_exp_f32 v114, v154\n"
      "v_cvt_pk_bf16_f32 v28, v112, v113\n"
      "v_mfma_f32_16x16x32_bf16 v[164:167], a[84:87], a[32:35], v[164:167]\n"
      "v_exp_f32 v115, v155\n"
      "v_exp_f32 v112, v156\n"
      "v_mfma_f32_16x16x32_bf16 v[172:175], a[84:87], a[36:39], v[172:175]\n"
      "v_cvt_pk_bf16_f32 v29, v114, v115\n"
      "v_exp_f32 v113, v157\n"
      "v_mfma_f32_16x16x32_bf16 v[180:183], a[84:87], a[40:43], v[180:183]\n"
      "v_or3_b32 v116, v116, v28, v29\n"
      "v_exp_f32 v114, v158\n"
      "v_mov_b32 v123, s88\n"
      "v_mfma_f32_16x16x32_bf16 v[188:191], a[84:87], a[44:47], v[188:191]\n"
      "s_waitcnt lgkmcnt(0)\n"
      "v_cvt_pk_bf16_f32 v30, v112, v113\n"
      "v_exp_f32 v115, v159\n"
      "v_add_u32 v124, 12288, v125\n"
      "s_waitcnt vmcnt(4)\n"
      "s_barrier\n"
      "v_mfma_f32_16x16x32_bf16 v[160:163], a[88:91], a[48:51], v[160:163]\n"
      "ds_read_b64_tr_b16 a[96:97], v250 offset:33792\n"
      "ds_read_b64_tr_b16 a[98:99], v250 offset:37888\n"
      "s_nop 0\n"
      "v_mfma_f32_16x16x32_bf16 v[168:171], a[88:91], a[52:55], v[168:171]\n"
      "s_add_u32 m0, %[wb], 81920\n"
      "ds_read_b64_tr_b16 a[100:101], v251 offset:33792\n"
      "ds_read_b64_tr_b16 a[102:103], v251 offset:37888\n"
      "v_cvt_pk_bf16_f32 v31, v114, v115\n"
      "v_mfma_f32_16x16x32_bf16 v[176:179], a[88:91], a[56:59], v[176:179]\n"
      "buffer_load_dwordx4 v249, s[24:27], s94 offen nt lds\n"
      "ds_read_b64_tr_b16 a[112:113], v246 offset:1024\n"
      "ds_read_b64_tr_b16 a[114:115], v246 offset:5120\n"
      "v_mfma_f32_16x16x32_bf16 v[184:187], a[88:91], a[60:63], v[184:187]\n"
      "s_add_u32 m0, %[wb], 90112\n"
      "ds_read_b64_tr_b16 a[116:117], v247 offset:1024\n"
      "ds_read_b64_tr_b16 a[118:119], v247 offset:5120\n"
      "v_mfma_f32_16x16x32_bf16 v[164:167], a[92:95], a[48:51], v[164:167]\n"
      "buffer_load_dwordx4 v122, s[24:27], s94 offen nt lds\n"
      "ds_read_b64_tr_b16 a[104:105], v240 offset:33792\n"
      "v_mfma_f32_16x16x32_bf16 v[172:175], a[92:95], a[52:55], v[172:175]\n"
      "s_mov_b32 s64, 5\n"
      "ds_read_b64_tr_b16 a[106:107], v240 offset:37888\n"
      "v_mfma_f32_16x16x32_bf16 v[180:183], a[92:95], a[56:59], v[180:183]\n"
      "ds_read_b64_tr_b16 a[108:109], v241 offset:33792\n"
      "v_or3_b32 v116, v116, v30, v31\n"
      "v_mfma_f32_16x16x32_bf16 v[188:191], a[92:95], a[60:63], v[188:191]\n"
      "ds_read_b64_tr_b16 a[110:111], v241 offset:37888\n"
      "v_and_b32 v116, 0x40004000, v116\n"
      "v_cmp_ne_u32 vcc, 0, v116\n"
      "s_waitcnt lgkmcnt(4)\n"
      "s_cbranch_vccnz FB0_%=\n"
      "RET0_5_%=:\n"
      "v_mfma_f32_16x16x32_bf16 a[160:163], a[96:99], v[0:3], a[160:163]\n"
      "ds_read_b64_tr_b16 a[120:121], v244 offset:1024\n"
      "v_mov_b32 v117, v224\n"
      "v_xor_b32 v242, 0x10000, v242\n"
      "v_xor_b32 v243, 0x10000, v243\n"
      "v_mfma_f32_16x16x32_bf16 a[176:179], a[100:103], v[0:3], a[176:179]\n"
      "ds_read_b64_tr_b16 a[122:123], v244 offset:5120\n"
      "v_exp_f32 v112, v160\n"
      "v_mfma_f32_16x16x32_bf16 a[160:163], a[112:115], v[16:19], a[160:163]\n"
      "s_mov_b32 s64, 6\n"
      "ds_read_b64_tr_b16 a[124:125], v245 offset:1024\n"
      "v_exp_f32 v113, v161\n"
      "v_exp_f32 v114, v162\n"
      "v_mfma_f32_16x16x32_bf16 a[176:179], a[116:119], v[16:19], a[176:179]\n"
      "ds_read_b64_tr_b16 a[126:127], v245 offset:5120\n"
      "v_cvt_pk_bf16_f32 v96, v112, v113\n"
      "v_exp_f32 v115, v163\n"
      "v_mfma_f32_16x16x32_bf16 a[164:167], a[96:99], v[4:7], a[164:167]\n"
      "v_mov_b32_dpp v117, v228 quad_perm:[0,1,2,3] row_mask:0x2 bank_mask:0xf\n"
      "v_exp_f32 v112, v164\n"
      "v_mfma_f32_16x16x32_bf16 a[180:183], a[100:103], v[4:7], a[180:183]\n"
      "v_cvt_pk_bf16_f32 v97, v114, v115\n"
      "v_exp_f32 v113, v165\n"
      "v_or_b32 v116, v96, v97\n"
      "v_mfma_f32_16x16x32_bf16 a[164:167], a[112:115], v[20:23], a[164:167]\n"
      "v_exp_f32 v114, v166\n"
      "v_cvt_pk_bf16_f32 v98, v112, v113\n"
      "v_mfma_f32_16x16x32_bf16 a[180:183], a[116:119], v[20:23], a[180:183]\n"
      "ds_write_b128 v125, a[160:163] offset:0\n"
      "v_exp_f32 v115, v167\n"
      "v_mfma_f32_16x16x32_bf16 a[168:171], a[96:99], v[8:11], a[168:171]\n"
      "v_mov_b32_dpp v117, v232 quad_perm:[0,1,2,3] row_mask:0x4 bank_mask:0xf\n"
      "v_exp_f32 v112, v168\n"
      "v_cvt_pk_bf16_f32 v99, v114, v115\n"
      "v_mfma_f32_16x16x32_bf16 a[184:187], a[100:103], v[8:11], a[184:187]\n"
      "s_add_u32 m0, %[wb], 114688\n"
      "ds_write_b128 v125, v[96:99] offset:8192\n"
      "v_exp_f32 v113, v169\n"
      "v_mfma_f32_16x16x32_bf16 a[168:171], a[112:115], v[24:27], a[168:171]\n"
      "buffer_load_dwordx4 v249, s[24:27], s95 offen nt lds\n"
      "v_or3_b32 v116, v116, v98, v99\n"
      "v_mfma_f32_16x16x32_bf16 a[184:187], a[116:119], v[24:27], a[184:187]\n"
      "s_add_u32 m0, %[wb], 122880\n"
      "v_exp_f32 v114, v170\n"
      "v_cvt_pk_bf16_f32 v100, v112, v113\n"
      "v_mfma_f32_16x16x32_bf16 a[172:175], a[96:99], v[12:15], a[172:175]\n"
      "buffer_load_dwordx4 v122, s[24:27], s95 offen nt lds\n"
      "ds_write_b128 v125, a[176:179] offset:4096\n"
      "v_mov_b32_dpp v117, v236 quad_perm:[0,1,2,3] row_mask:0x8 bank_mask:0xf\n"
      "v_mfma_f32_16x16x32_bf16 a[188:191], a[100:103], v[12:15], a[188:191]\n"
      "v_exp_f32 v115, v171\n"
      "v_mfma_f32_16x16x32_bf16 a[172:175], a[112:115], v[28:31], a[172:175]\n"
      "v_exp_f32 v112, v172\n"
      "v_cvt_pk_bf16_f32 v101, v114, v115\n"
      "v_exp_f32 v113, v173\n"
      "v_mfma_f32_16x16x32_bf16 a[188:191], a[116:119], v[28:31], a[188:191]\n"
      "ds_write_b128 v125, a[164:167] offset:1024\n"
      "v_or3_b32 v116, v116, v100, v101\n"
      "v_mfma_f32_16x16x32_bf16 a[192:195], a[196:199], v[16:19], a[192:195]\n"
      "ds_write2_b32 v124, v117, v123 offset0:1 offset1:2\n"
      "v_exp_f32 v114, v174\n"
      "v_cvt_pk_bf16_f32 v102, v112, v113\n"
      "v_mfma_f32_16x16x32_bf16 a[192:195], a[200:203], v[20:23], a[192:195]\n"
      "v_exp_f32 v115, v175\n"
      "v_exp_f32 v112, v176\n"
      "v_mfma_f32_16x16x32_bf16 a[192:195], a[204:207], v[24:27], a[192:195]\n"
      "ds_write_b128 v125, a[180:183] offset:5120\n"
      "v_cvt_pk_bf16_f32 v103, v114, v115\n"
      "v_mfma_f32_16x16x32_bf16 a[192:195], a[208:211], v[28:31], a[192:195]\n"
      "s_waitcnt lgkmcnt(6)\n"
      "v_exp_f32 v113, v177\n"
      "v_or3_b32 v116, v116, v102, v103\n"
      "v_exp_f32 v114, v178\n"
      "v_mfma_f32_16x16x32_bf16 v[64:67], a[104:107], v[0:3], v[64:67]\n"
      "v_cvt_pk_bf16_f32 v104, v112, v113\n"
      "v_exp_f32 v115, v179\n"
      "v_mfma_f32_16x16x32_bf16 v[68:71], a[104:107], v[4:7], v[68:71]\n"
      "ds_write_b128 v125, v[100:103] offset:9216\n"
      "v_exp_f32 v112, v180\n"
      "s_waitcnt vmcnt(6)\n"
      "s_barrier\n"
      "v_mfma_f32_16x16x32_bf16 v[72:75], a[104:107], v[8:11], v[72:75]\n"
      "ds_read_b64_tr_b16 a[212:213], v244 offset:33792\n"
      "v_cvt_pk_bf16_f32 v105, v114, v115\n"
      "v_exp_f32 v113, v181\n"
      "v_mfma_f32_16x16x32_bf16 v[76:79], a[104:107], v[12:15], v[76:79]\n"
      "s_add_u32 m0, %[wb], 16384\n"
      "ds_write_b128 v125, a[168:171] offset:2048\n"
      "ds_read_b64_tr_b16 a[214:215], v244 offset:37888\n"
      "v_or3_b32 v116, v116, v104, v105\n"
      "v_mfma_f32_16x16x32_bf16 a[228:231], a[196:199], v[96:99], a[192:195]\n"
      "buffer_load_dwordx4 v249, s[24:27], s77 offen nt lds\n"
      "ds_read_b64_tr_b16 a[216:217], v245 offset:33792\n"
      "v_mfma_f32_16x16x32_bf16 v[80:83], a[108:111], v[0:3], v[80:83]\n"
      "s_add_u32 m0, %[wb], 24576\n"
      "ds_read_b64_tr_b16 a[218:219], v245 offset:37888\n"
      "v_exp_f32 v114, v182\n"
      "v_mfma_f32_16x16x32_bf16 v[84:87], a[108:111], v[4:7], v[84:87]\n"
      "buffer_load_dwordx4 v122, s[24:27], s77 offen nt lds\n"
      "ds_write_b128 v125, a[184:187] offset:6144\n"
      "ds_read_b64_tr_b16 a[220:221], v248 offset:33792\n"
      "v_mfma_f32_16x16x32_bf16 v[88:91], a[108:111], v[8:11], v[88:91]\n"
      "ds_read_b64_tr_b16 a[222:223], v248 offset:37888\n"
      "v_cvt_pk_bf16_f32 v106, v112, v113\n"
      "v_mfma_f32_16x16x32_bf16 v[92:95], a[108:111], v[12:15], v[92:95]\n"
      "ds_read_b64_tr_b16 a[224:225], v120 offset:33792\n"
      "v_exp_f32 v115, v183\n"
      "v_mfma_f32_16x16x32_bf16 v[64:67], a[120:123], v[16:19], v[64:67]\n"
      "ds_write_b128 v125, a[172:175] offset:3072\n"
      "ds_read_b64_tr_b16 a[226:227], v120 offset:37888\n"
      "v_exp_f32 v112, v184\n"
      "v_mfma_f32_16x16x32_bf16 v[68:71], a[120:123], v[20:23], v[68:71]\n"
      "v_cvt_pk_bf16_f32 v107, v114, v115\n"
      "v_exp_f32 v113, v185\n"
      "v_mfma_f32_16x16x32_bf16 v[72:75], a[120:123], v[24:27], v[72:75]\n"
      "v_or3_b32 v116, v116, v106, v107\n"
      "v_exp_f32 v114, v186\n"
      "v_cvt_pk_bf16_f32 v108, v112, v113\n"
      "v_mfma_f32_16x16x32_bf16 v[76:79], a[120:123], v[28:31], v[76:79]\n"
      "ds_write_b128 v125, v[104:107] offset:10240\n"
      "v_exp_f32 v115, v187\n"
      "v_mfma_f32_16x16x32_bf16 a[228:231], a[200:203], v[100:103], a[228:231]\n"
      "v_exp_f32 v112, v188\n"
      "v_cvt_pk_bf16_f32 v109, v114, v115\n"
      "v_mfma_f32_16x16x32_bf16 v[80:83], a[124:127], v[16:19], v[80:83]\n"
      "v_exp_f32 v113, v189\n"
      "v_or3_b32 v116, v116, v108, v109\n"
      "v_exp_f32 v114, v190\n"
      "v_mfma_f32_16x16x32_bf16 v[84:87], a[124:127], v[20:23], v[84:87]\n"
      "s_add_u32 m0, %[wb], 82944\n"
      "ds_write_b128 v125, a[188:191] offset:7168\n"
      "v_cvt_pk_bf16_f32 v110, v112, v113\n"
      "v_mfma_f32_16x16x32_bf16 v[88:91], a[124:127], v[24:27], v[88:91]\n"
      "buffer_load_dwordx4 v249, s[24:27], s78 offen nt lds\n"
      "v_exp_f32 v115, v191\n"
      "v_mfma_f32_16x16x32_bf16 v[92:95], a[124:127], v[28:31], v[92:95]\n"
      "s_add_u32 m0, %[wb], 91136\n"
      "s_nop 0\n"
      "v_cvt_pk_bf16_f32 v111, v114, v115\n"
      "v_or3_b32 v116, v116, v110, v111\n"
      "v_and_b32 v116, 0x40004000, v116\n"
      "v_mfma_f32_16x16x32_bf16 a[228:231], a[204:207], v[104:107], a[228:231]\n"
      "buffer_load_dwordx4 v122, s[24:27], s78 offen nt lds\n"
      "v_cmp_ne_u32 vcc, 0, v116\n"
      "s_cbranch_vccnz FB1_%=\n"
      "RET1_6_%=:\n"
      "ds_write_b128 v125, v[108:111] offset:11264\n"
      "v_mfma_f32_16x16x32_bf16 a[228:231], a[208:211], v[108:111], a[228:231]\n"
      "s_waitcnt lgkmcnt(9)\n"
      "v_mfma_f32_16x16x32_bf16 v[64:67], a[212:215], v[96:99], v[64:67]\n"
      "v_mov_b32 v148, 1.0\n"
      "v_mfma_f32_16x16x32_bf16 v[80:83], a[216:219], v[96:99], v[80:83]\n"
      "v_mov_b32 v149, 1.0\n"
      "v_mfma_f32_16x16x32_bf16 v[68:71], a[212:215], v[100:103], v[68:71]\n"
      "v_mov_b32 v157, s37\n"
      "v_mfma_f32_16x16x32_bf16 v[84:87], a[216:219], v[100:103], v[84:87]\n"
      "v_mov_b32 v150, s72\n"
      "v_mfma_f32_16x16x32_bf16 v[72:75], a[212:215], v[104:107], v[72:75]\n"
      "v_mov_b32 v151, s76\n"
      "ds_write_b32 v125, a228 offset:12288\n"
      "v_mfma_f32_16x16x32_bf16 v[88:91], a[216:219], v[104:107], v[88:91]\n"
      "v_xor_b32 v158, 8, v157\n"
      "v_mfma_f32_16x16x32_bf16 v[76:79], a[212:215], v[108:111], v[76:79]\n"
      "v_mfma_f32_16x16x32_bf16 v[92:95], a[216:219], v[108:111], v[92:95]\n"
      "s_waitcnt lgkmcnt(0)\n"
      "s_barrier\n"
      "s_memrealtime s[68:69]\n"
      "ds_read_b32 v237, v127 offset:12296\n"
      "ds_read_b64 v[152:153], v126 offset:12288\n"
      "ds_read_b128 v[160:163], v126 offset:0\n"
      "ds_read_b128 v[176:179], v126 offset:4096\n"
      "ds_read_b128 v[164:167], v126 offset:1024\n"
      "ds_read_b128 v[180:183], v126 offset:5120\n"
      "ds_read_b128 v[168:171], v126 offset:2048\n"
      "ds_read_b128 v[184:187], v126 offset:6144\n"
      "ds_read_b128 v[172:175], v126 offset:3072\n"
      "ds_read_b128 v[188:191], v126 offset:7168\n"
      "ds_read_b128 a[64:67], v126 offset:8192\n"
      "ds_read_b128 a[68:71], v126 offset:9216\n"
      "ds_read_b128 a[72:75], v126 offset:10240\n"
      "ds_read_b128 a[76:79], v126 offset:11264\n"
      "v_accvgpr_read_b32 v144, a228\n"
      "v_mov_b32 v236, v150\n"
      "s_waitcnt lgkmcnt(12)\n"
      "v_cmp_neq_f32 vcc, v153, v117\n"
      "v_readfirstlane_b32 s88, v237\n"
      "s_cbranch_vccnz MIS_%=\n"
      "MR_%=:\n"
      "v_mul_f32 v144, v144, v148\n"
      "v_mov_b32 v145, v117\n"
      "v_fmac_f32 v144, v152, v149\n"
      "s_waitcnt lgkmcnt(10)\n"
      "v_pk_add_f32 v[64:65], v[64:65], v[160:161]\n"
      "v_pk_add_f32 v[66:67], v[66:67], v[162:163]\n"
      "v_pk_add_f32 v[80:81], v[80:81], v[176:177]\n"
      "v_pk_add_f32 v[82:83], v[82:83], v[178:179]\n"
      "s_waitcnt lgkmcnt(8)\n"
      "v_pk_add_f32 v[68:69], v[68:69], v[164:165]\n"
      "v_pk_add_f32 v[70:71], v[70:71], v[166:167]\n"
      "v_pk_add_f32 v[84:85], v[84:85], v[180:181]\n"
      "v_pk_add_f32 v[86:87], v[86:87], v[182:183]\n"
      "s_waitcnt lgkmcnt(6)\n"
      "v_pk_add_f32 v[72:73], v[72:73], v[168:169]\n"
      "v_pk_add_f32 v[74:75], v[74:75], v[170:171]\n"
      "v_pk_add_f32 v[88:89], v[88:89], v[184:185]\n"
      "v_pk_add_f32 v[90:91], v[90:91], v[186:187]\n"
      "s_waitcnt lgkmcnt(4)\n"
      "v_pk_add_f32 v[76:77], v[76:77], v[172:173]\n"
      "v_pk_add_f32 v[78:79], v[78:79], v[174:175]\n"
      "v_pk_add_f32 v[92:93], v[92:93], v[188:189]\n"
      "v_pk_add_f32 v[94:95], v[94:95], v[190:191]\n"
      "s_waitcnt lgkmcnt(0)\n"
      "s_add_u32 s65, s68, 55\n"
      "s_mov_b32 s66, s88\n"
      "s_sub_u32 s67, s65, s66\n"
      "s_cmp_lt_i32 s67, 0\n"
      "s_cselect_b32 s88, s66, s65\n"
      "v_mov_b32 v237, s88\n"
      "s_mov_b32 s88, s66\n"
      "s_cmp_lg_u32 s33, 0\n"
      "s_cbranch_scc1 NH_%=\n"
      "s_mov_b64 exec, 1\n"
      "global_store_dwordx2 v157, v[236:237], s[18:19] sc1\n"
      "s_mov_b64 exec, -1\n"
      "NH_%=:\n"
      "v_mfma_f32_16x16x32_bf16 v[64:67], a[220:223], a[64:67], v[64:67]\n"
      "v_and_b32 v118, 0xf0, v127\n"
      "v_lshrrev_b32 v156, 3, v127\n"
      "v_mfma_f32_16x16x32_bf16 v[80:83], a[224:227], a[64:67], v[80:83]\n"
      "v_and_b32 v156, 32, v156\n"
      "v_lshl_or_b32 v156, v118, 4, v156\n"
      "v_mfma_f32_16x16x32_bf16 v[68:71], a[220:223], a[68:71], v[68:71]\n"
      "v_lshrrev_b32 v118, 5, v127\n"
      "v_and_or_b32 v156, v118, 16, v156\n"
      "v_mfma_f32_16x16x32_bf16 v[84:87], a[224:227], a[68:71], v[84:87]\n"
      "v_add_u32 v156, s36, v156\n"
      "v_add_u32 v155, 0x2000, v156\n"
      "v_mfma_f32_16x16x32_bf16 v[72:75], a[220:223], a[72:75], v[72:75]\n"
      "v_cvt_pk_bf16_f32 v128, v64, v65\n"
      "v_cvt_pk_bf16_f32 v129, v66, v67\n"
      "v_mfma_f32_16x16x32_bf16 v[88:91], a[224:227], a[72:75], v[88:91]\n"
      "v_cvt_pk_bf16_f32 v130, v80, v81\n"
      "v_cvt_pk_bf16_f32 v131, v82, v83\n"
      "v_mfma_f32_16x16x32_bf16 v[76:79], a[220:223], a[76:79], v[76:79]\n"
      "v_xor_b32 v242, 0x2000, v240\n"
      "v_xor_b32 v243, 0x2000, v241\n"
      "v_mfma_f32_16x16x32_bf16 v[92:95], a[224:227], a[76:79], v[92:95]\n"
      "v_cvt_pk_bf16_f32 v132, v68, v69\n"
      "v_cvt_pk_bf16_f32 v133, v70, v71\n"
      "s_memrealtime s[68:69]\n"
      "v_cvt_pk_bf16_f32 v134, v84, v85\n"
      "v_cvt_pk_bf16_f32 v135, v86, v87\n"
      "v_mad_u32_u24 v147, v144, s60, v151\n"
      "v_mad_u32_u24 v147, v145, s62, v147\n"
      "v_add3_u32 v146, v150, v144, v145\n"
      "v_mad_u32_u24 v147, v128, s60, v147\n"
      "v_mad_u32_u24 v147, v129, s62, v147\n"
      "v_add3_u32 v146, v146, v128, v129\n"
      "v_mad_u32_u24 v147, v130, s70, v147\n"
      "v_mad_u32_u24 v147, v131, s71, v147\n"
      "v_add3_u32 v146, v146, v130, v131\n"
      "v_cvt_pk_bf16_f32 v136, v72, v73\n"
      "v_cvt_pk_bf16_f32 v137, v74, v75\n"
      "v_cvt_pk_bf16_f32 v138, v88, v89\n"
      "v_cvt_pk_bf16_f32 v139, v90, v91\n"
      "v_cvt_pk_bf16_f32 v140, v76, v77\n"
      "v_cvt_pk_bf16_f32 v141, v78, v79\n"
      "v_cvt_pk_bf16_f32 v142, v92, v93\n"
      "v_cvt_pk_bf16_f32 v143, v94, v95\n"
      "v_mad_u32_u24 v147, v132, s60, v147\n"
      "v_mad_u32_u24 v147, v133, s62, v147\n"
      "v_add3_u32 v146, v146, v132, v133\n"
      "v_mad_u32_u24 v147, v134, s70, v147\n"
      "v_mad_u32_u24 v147, v135, s71, v147\n"
      "v_add3_u32 v146, v146, v134, v135\n"
      "v_mad_u32_u24 v147, v136, s60, v147\n"
      "v_mad_u32_u24 v147, v137, s62, v147\n"
      "v_add3_u32 v146, v146, v136, v137\n"
      "v_mad_u32_u24 v147, v138, s70, v147\n"
      "v_mad_u32_u24 v147, v139, s71, v147\n"
      "v_add3_u32 v146, v146, v138, v139\n"
      "v_mad_u32_u24 v147, v140, s60, v147\n"
      "v_mad_u32_u24 v147, v141, s62, v147\n"
      "v_add3_u32 v146, v146, v140, v141\n"
      "v_mad_u32_u24 v147, v142, s70, v147\n"
      "v_mad_u32_u24 v147, v143, s71, v147\n"
      "v_add3_u32 v146, v146, v142, v143\n"
      "s_waitcnt lgkmcnt(0)\n"
      "s_call_b64 s[84:85], SPIN_%=\n"
      "global_load_dwordx2 v[232:233], v158, s[18:19] sc1\n"
      "buffer_store_dwordx4 v[128:131], v127, s[28:31], s35 offen offset:0 sc1\n"
      "buffer_store_dwordx4 v[132:135], v127, s[28:31], s35 offen offset:1024 sc1\n"
      "buffer_store_dwordx4 v[136:139], v127, s[28:31], s35 offen offset:2048 sc1\n"
      "buffer_store_dwordx4 v[140:143], v127, s[28:31], s35 offen offset:3072 sc1\n"
      "buffer_store_dwordx4 v[144:147], v127, s[28:31], s81 offen sc1\n"
      "global_load_dwordx2 v[234:235], v158, s[18:19] sc1\n"
      "s_waitcnt vmcnt(7)\n"
      "s_barrier\n"
      "ds_read_b64_tr_b16 a[96:97], v246 offset:32768\n"
      "ds_read_b64_tr_b16 a[98:99], v246 offset:36864\n"
      "ds_read_b64_tr_b16 a[100:101], v247 offset:32768\n"
      "ds_read_b64_tr_b16 a[102:103], v247 offset:36864\n"
      "ds_read_b64_tr_b16 a[112:113], v250 offset:0\n"
      "ds_read_b64_tr_b16 a[114:115], v250 offset:4096\n"
      "ds_read_b64_tr_b16 a[116:117], v251 offset:0\n"
      "ds_read_b64_tr_b16 a[118:119], v251 offset:4096\n"
      "ds_read_b64_tr_b16 a[0:1], v250 offset:32768\n"
      "ds_read_b64_tr_b16 a[2:3], v250 offset:36864\n"
      "ds_read_b64_tr_b16 a[4:5], v251 offset:32768\n"
      "ds_read_b64_tr_b16 a[6:7], v251 offset:36864\n"
      "s_waitcnt lgkmcnt(8)\n"
      "v_mfma_f32_16x16x32_bf16 a[128:131], a[96:99], v[208:211], a[128:131]\n"
      "ds_read_b64_tr_b16 a[104:105], v244 offset:32768\n"
      "ds_read_b64_tr_b16 a[106:107], v244 offset:36864\n"
      "v_mfma_f32_16x16x32_bf16 a[144:147], a[100:103], v[208:211], a[144:147]\n"
      "ds_read_b64_tr_b16 a[108:109], v245 offset:32768\n"
      "ds_read_b64_tr_b16 a[110:111], v245 offset:36864\n"
      "s_waitcnt lgkmcnt(8)\n"
      "v_mfma_f32_16x16x32_bf16 a[128:131], a[112:115], v[192:195], a[128:131]\n"
      "ds_read_b64_tr_b16 a[120:121], v240 offset:0\n"
      "ds_read_b64_tr_b16 a[122:123], v240 offset:4096\n"
      "v_mfma_f32_16x16x32_bf16 a[144:147], a[116:119], v[192:195], a[144:147]\n"
      "ds_read_b64_tr_b16 a[124:125], v241 offset:0\n"
      "ds_read_b64_tr_b16 a[126:127], v241 offset:4096\n"
      "s_waitcnt lgkmcnt(8)\n"
      "v_mfma_f32_16x16x32_bf16 a[128:131], a[0:3], v[0:3], a[128:131]\n"
      "ds_read_b64_tr_b16 a[8:9], v240 offset:32768\n"
      "ds_read_b64_tr_b16 a[10:11], v240 offset:36864\n"
      "v_mfma_f32_16x16x32_bf16 a[144:147], a[4:7], v[0:3], a[144:147]\n"
      "ds_read_b64_tr_b16 a[12:13], v241 offset:32768\n"
      "ds_read_b64_tr_b16 a[14:15], v241 offset:36864\n"
      "v_mfma_f32_16x16x32_bf16 a[132:135], a[96:99], v[212:215], a[132:135]\n"
      "ds_write_b128 v125, v[16:19] offset:8192\n"
      "v_lshrrev_b32 v118, 2, v127\n"
      "v_mfma_f32_16x16x32_bf16 a[148:151], a[100:103], v[212:215], a[148:151]\n"
      "v_xor_b32 v118, v118, v127\n"
      "v_mfma_f32_16x16x32_bf16 a[132:135], a[112:115], v[196:199], a[132:135]\n"
      "v_and_b32 v118, 0xc0, v118\n"
      "v_mfma_f32_16x16x32_bf16 a[148:151], a[116:119], v[196:199], a[148:151]\n"
      "v_and_or_b32 v118, v127, 32, v118\n"
      "v_mfma_f32_16x16x32_bf16 a[132:135], a[0:3], v[4:7], a[132:135]\n"
      "ds_write_b128 v125, v[20:23] offset:9216\n"
      "v_and_b32 v119, 16, v127\n"
      "v_mfma_f32_16x16x32_bf16 a[148:151], a[4:7], v[4:7], a[148:151]\n"
      "v_mov_b32 v84, 0x3f80\n"
      "v_mfma_f32_16x16x32_bf16 a[136:139], a[96:99], v[216:219], a[136:139]\n"
      "v_lshlrev_b32 v119, v119, v84\n"
      "v_mfma_f32_16x16x32_bf16 a[152:155], a[100:103], v[216:219], a[152:155]\n"
      "v_cmp_eq_u32 vcc, 0, v118\n"
      "v_mfma_f32_16x16x32_bf16 a[136:139], a[112:115], v[200:203], a[136:139]\n"
      "ds_write_b128 v125, v[24:27] offset:10240\n"
      "v_mov_b32 v84, 0\n"
      "v_mfma_f32_16x16x32_bf16 a[152:155], a[116:119], v[200:203], a[152:155]\n"
      "v_cndmask_b32 v86, 0, v119, vcc\n"
      "v_mfma_f32_16x16x32_bf16 a[136:139], a[0:3], v[8:11], a[136:139]\n"
      "v_cmp_eq_u32 vcc, 32, v118\n"
      "v_mfma_f32_16x16x32_bf16 a[152:155], a[4:7], v[8:11], a[152:155]\n"
      "v_mov_b32 v85, 0\n"
      "v_mfma_f32_16x16x32_bf16 a[140:143], a[96:99], v[220:223], a[140:143]\n"
      "ds_write_b128 v125, v[28:31] offset:11264\n"
      "v_cndmask_b32 v87, 0, v119, vcc\n"
      "v_mfma_f32_16x16x32_bf16 a[156:159], a[100:103], v[220:223], a[156:159]\n"
      "v_mov_b32 v88, 0\n"
      "v_mfma_f32_16x16x32_bf16 a[140:143], a[112:115], v[204:207], a[140:143]\n"
      "v_mov_b32 v89, 0\n"
      "v_mfma_f32_16x16x32_bf16 a[156:159], a[116:119], v[204:207], a[156:159]\n"
      "v_mfma_f32_16x16x32_bf16 a[140:143], a[0:3], v[12:15], a[140:143]\n"
      "ds_write_b128 v125, a[128:131] offset:0\n"
      "v_mfma_f32_16x16x32_bf16 a[156:159], a[4:7], v[12:15], a[156:159]\n"
      "s_waitcnt lgkmcnt(13)\n"
      "v_mfma_f32_16x16x32_bf16 v[32:35], a[104:107], v[208:211], v[32:35]\n"
      "ds_read_b64_tr_b16 a[32:33], v240 offset:1024\n"
      "v_mfma_f32_16x16x32_bf16 v[36:39], a[104:107], v[212:215], v[36:39]\n"
      "ds_read_b64_tr_b16 a[34:35], v240 offset:5120\n"
      "v_mfma_f32_16x16x32_bf16 v[40:43], a[104:107], v[216:219], v[40:43]\n"
      "ds_write_b128 v125, a[144:147] offset:4096\n"
      "ds_read_b64_tr_b16 a[36:37], v241 offset:1024\n"
      "v_mfma_f32_16x16x32_bf16 v[44:47], a[104:107], v[220:223], v[44:47]\n"
      "ds_read_b64_tr_b16 a[38:39], v241 offset:5120\n"
      "v_mfma_f32_16x16x32_bf16 v[48:51], a[108:111], v[208:211], v[48:51]\n"
      "ds_read_b64_tr_b16 a[40:41], v242 offset:1024\n"
      "v_mfma_f32_16x16x32_bf16 v[52:55], a[108:111], v[212:215], v[52:55]\n"
      "ds_read_b64_tr_b16 a[42:43], v242 offset:5120\n"
      "v_mfma_f32_16x16x32_bf16 v[56:59], a[108:111], v[216:219], v[56:59]\n"
      "ds_write_b128 v125, a[132:135] offset:1024\n"
      "ds_read_b64_tr_b16 a[44:45], v243 offset:1024\n"
      "v_mfma_f32_16x16x32_bf16 v[60:63], a[108:111], v[220:223], v[60:63]\n"
      "ds_read_b64_tr_b16 a[46:47], v243 offset:5120\n"
      "s_waitcnt lgkmcnt(15)\n"
      "v_mfma_f32_16x16x32_bf16 v[32:35], a[120:123], v[192:195], v[32:35]\n"
      "ds_read_b64_tr_b16 a[16:17], v244 offset:0\n"
      "v_mfma_f32_16x16x32_bf16 v[36:39], a[120:123], v[196:199], v[36:39]\n"
      "ds_read_b64_tr_b16 a[18:19], v244 offset:4096\n"
      "v_mfma_f32_16x16x32_bf16 v[40:43], a[120:123], v[200:203], v[40:43]\n"
      "ds_write_b128 v125, a[148:151] offset:5120\n"
      "ds_read_b64_tr_b16 a[20:21], v245 offset:0\n"
      "v_mfma_f32_16x16x32_bf16 v[44:47], a[120:123], v[204:207], v[44:47]\n"
      "ds_read_b64_tr_b16 a[22:23], v245 offset:4096\n"
      "v_mfma_f32_16x16x32_bf16 v[48:51], a[124:127], v[192:195], v[48:51]\n"
      "ds_read_b64_tr_b16 a[24:25], v248 offset:0\n"
      "v_mfma_f32_16x16x32_bf16 v[52:55], a[124:127], v[196:199], v[52:55]\n"
      "ds_read_b64_tr_b16 a[26:27], v248 offset:4096\n"
      "v_mfma_f32_16x16x32_bf16 v[56:59], a[124:127], v[200:203], v[56:59]\n"
      "ds_write_b128 v125, a[136:139] offset:2048\n"
      "ds_read_b64_tr_b16 a[28:29], v120 offset:0\n"
      "v_mfma_f32_16x16x32_bf16 v[60:63], a[124:127], v[204:207], v[60:63]\n"
      "ds_read_b64_tr_b16 a[30:31], v120 offset:4096\n"
      "s_waitcnt lgkmcnt(15)\n"
      "v_mfma_f32_16x16x32_bf16 v[32:35], a[8:11], v[0:3], v[32:35]\n"
      "v_mfma_f32_16x16x32_bf16 v[36:39], a[8:11], v[4:7], v[36:39]\n"
      "v_mfma_f32_16x16x32_bf16 v[40:43], a[8:11], v[8:11], v[40:43]\n"
      "ds_write_b128 v125, a[152:155] offset:6144\n"
      "v_mfma_f32_16x16x32_bf16 v[44:47], a[8:11], v[12:15], v[44:47]\n"
      "s_waitcnt vmcnt(6)\n"
      "v_cmp_ne_u32 vcc, v232, v150\n"
      "s_memrealtime s[68:69]\n"
      "s_cbranch_vccnz POLL_%=\n"
      "GOT_%=:\n"
      "v_readfirstlane_b32 s88, v233\n"
      "s_waitcnt lgkmcnt(0)\n"
      "s_add_u32 s88, s88, 60\n"
      "s_sub_u32 s67, s88, s68\n"
      "s_cmp_gt_u32 s67, 1000\n"
      "s_cbranch_scc1 LD_%=\n"
      "s_call_b64 s[84:85], SPIN_%=\n"
      "LD_%=:\n"
      "buffer_load_dwordx4 v[80:83], v127, s[28:31], s79 offen sc1\n"
      "buffer_load_dwordx4 v[64:67], v127, s[28:31], s80 offen offset:0 sc1\n"
      "buffer_load_dwordx4 v[68:71], v127, s[28:31], s80 offen offset:1024 sc1\n"
      "buffer_load_dwordx4 v[72:75], v127, s[28:31], s80 offen offset:2048 sc1\n"
      "buffer_load_dwordx4 v[76:79], v127, s[28:31], s80 offen offset:3072 sc1\n"
      "v_mfma_f32_16x16x32_bf16 v[48:51], a[12:15], v[0:3], v[48:51]\n"
      "v_mfma_f32_16x16x32_bf16 v[52:55], a[12:15], v[4:7], v[52:55]\n"
      "v_mfma_f32_16x16x32_bf16 v[56:59], a[12:15], v[8:11], v[56:59]\n"
      "ds_write_b128 v125, a[140:143] offset:3072\n"
      "v_mfma_f32_16x16x32_bf16 v[60:63], a[12:15], v[12:15], v[60:63]\n"
      "s_waitcnt lgkmcnt(15)\n"
      "v_mfma_f32_16x16x32_bf16 v[32:35], a[32:35], v[96:99], v[32:35]\n"
      "v_mfma_f32_16x16x32_bf16 v[48:51], a[36:39], v[96:99], v[48:51]\n"
      "v_mfma_f32_16x16x32_bf16 v[36:39], a[32:35], v[100:103], v[36:39]\n"
      "ds_write_b128 v125, a[156:159] offset:7168\n"
      "v_mfma_f32_16x16x32_bf16 v[52:55], a[36:39], v[100:103], v[52:55]\n"
      "v_mfma_f32_16x16x32_bf16 v[40:43], a[32:35], v[104:107], v[40:43]\n"
      "v_mfma_f32_16x16x32_bf16 v[56:59], a[36:39], v[104:107], v[56:59]\n"
      "v_mfma_f32_16x16x32_bf16 v[44:47], a[32:35], v[108:111], v[44:47]\n"
      "v_mfma_f32_16x16x32_bf16 v[60:63], a[36:39], v[108:111], v[60:63]\n"
      "s_waitcnt lgkmcnt(13)\n"
      "v_mfma_f32_16x16x32_bf16 v[32:35], a[40:43], a[64:67], v[32:35]\n"
      "v_mfma_f32_16x16x32_bf16 v[48:51], a[44:47], a[64:67], v[48:51]\n"
      "v_mfma_f32_16x16x32_bf16 v[36:39], a[40:43], a[68:71], v[36:39]\n"
      "v_mfma_f32_16x16x32_bf16 v[52:55], a[44:47], a[68:71], v[52:55]\n"
      "v_mfma_f32_16x16x32_bf16 v[40:43], a[40:43], a[72:75], v[40:43]\n"
      "v_mfma_f32_16x16x32_bf16 v[56:59], a[44:47], a[72:75], v[56:59]\n"
      "v_mfma_f32_16x16x32_bf16 v[44:47], a[40:43], a[76:79], v[44:47]\n"
      "v_mfma_f32_16x16x32_bf16 v[60:63], a[44:47], a[76:79], v[60:63]\n"
      "s_waitcnt lgkmcnt(8)\n"
      "v_mfma_f32_16x16x32_bf16 v[32:35], a[16:19], v[16:19], v[32:35]\n"
      "v_mfma_f32_16x16x32_bf16 v[48:51], a[20:23], v[16:19], v[48:51]\n"
      "v_mfma_f32_16x16x32_bf16 v[36:39], a[16:19], v[20:23], v[36:39]\n"
      "v_mfma_f32_16x16x32_bf16 v[52:55], a[20:23], v[20:23], v[52:55]\n"
      "v_mfma_f32_16x16x32_bf16 v[40:43], a[16:19], v[24:27], v[40:43]\n"
      "v_mfma_f32_16x16x32_bf16 v[56:59], a[20:23], v[24:27], v[56:59]\n"
      "v_mfma_f32_16x16x32_bf16 v[44:47], a[16:19], v[28:31], v[44:47]\n"
      "v_mfma_f32_16x16x32_bf16 v[60:63], a[20:23], v[28:31], v[60:63]\n"
      "s_waitcnt lgkmcnt(0)\n"
      "s_barrier\n"
      "ds_read_b128 v[160:163], v126 offset:0\n"
      "ds_read_b128 v[176:179], v126 offset:4096\n"
      "ds_read_b128 v[164:167], v126 offset:1024\n"
      "ds_read_b128 v[180:183], v126 offset:5120\n"
      "ds_read_b128 v[168:171], v126 offset:2048\n"
      "ds_read_b128 v[184:187], v126 offset:6144\n"
      "ds_read_b128 v[172:175], v126 offset:3072\n"
      "ds_read_b128 v[188:191], v126 offset:7168\n"
      "ds_read_b128 a[80:83], v126 offset:8192\n"
      "ds_read_b128 a[84:87], v126 offset:9216\n"
      "ds_read_b128 a[88:91], v126 offset:10240\n"
      "ds_read_b128 a[92:95], v126 offset:11264\n"
      "s_waitcnt lgkmcnt(10)\n"
      "v_pk_add_f32 v[32:33], v[32:33], v[160:161]\n"
      "v_pk_add_f32 v[34:35], v[34:35], v[162:163]\n"
      "v_pk_add_f32 v[48:49], v[48:49], v[176:177]\n"
      "v_pk_add_f32 v[50:51], v[50:51], v[178:179]\n"
      "s_waitcnt lgkmcnt(8)\n"
      "v_pk_add_f32 v[36:37], v[36:37], v[164:165]\n"
      "v_pk_add_f32 v[38:39], v[38:39], v[166:167]\n"
      "v_pk_add_f32 v[52:53], v[52:53], v[180:181]\n"
      "v_pk_add_f32 v[54:55], v[54:55], v[182:183]\n"
      "s_waitcnt lgkmcnt(6)\n"
      "v_pk_add_f32 v[40:41], v[40:41], v[168:169]\n"
      "v_pk_add_f32 v[42:43], v[42:43], v[170:171]\n"
      "v_pk_add_f32 v[56:57], v[56:57], v[184:185]\n"
      "v_pk_add_f32 v[58:59], v[58:59], v[186:187]\n"
      "s_waitcnt lgkmcnt(4)\n"
      "v_pk_add_f32 v[44:45], v[44:45], v[172:173]\n"
      "v_pk_add_f32 v[46:47], v[46:47], v[174:175]\n"
      "v_pk_add_f32 v[60:61], v[60:61], v[188:189]\n"
      "v_pk_add_f32 v[62:63], v[62:63], v[190:191]\n"
      "s_waitcnt lgkmcnt(0)\n"
      "v_mfma_f32_16x16x32_bf16 v[32:35], a[24:27], a[80:83], v[32:35]\n"
      "v_mfma_f32_16x16x32_bf16 v[48:51], a[28:31], a[80:83], v[48:51]\n"
      "v_mfma_f32_16x16x32_bf16 v[36:39], a[24:27], a[84:87], v[36:39]\n"
      "v_mfma_f32_16x16x32_bf16 v[52:55], a[28:31], a[84:87], v[52:55]\n"
      "v_mfma_f32_16x16x32_bf16 v[40:43], a[24:27], a[88:91], v[40:43]\n"
      "v_mfma_f32_16x16x32_bf16 v[56:59], a[28:31], a[88:91], v[56:59]\n"
      "v_mfma_f32_16x16x32_bf16 v[44:47], a[24:27], a[92:95], v[44:47]\n"
      "v_mfma_f32_16x16x32_bf16 v[60:63], a[28:31], a[92:95], v[60:63]\n"
      "CHK_%=:\n"
      "s_waitcnt vmcnt(4)\n"
      "v_add_f32 v224, v144, v80\n"
      "v_rcp_f32 v224, v224\n"
      "v_mad_u32_u24 v119, v80, s60, v151\n"
      "v_mad_u32_u24 v119, v81, s62, v119\n"
      "v_add3_u32 v118, v150, v80, v81\n"
      "s_waitcnt vmcnt(3)\n"
      "v_mfma_f32_16x16x32_bf16 v[160:163], v[86:89], v[64:67], v[32:35]\n"
      "v_mad_u32_u24 v119, v64, s60, v119\n"
      "v_mad_u32_u24 v119, v65, s62, v119\n"
      "v_add3_u32 v118, v118, v64, v65\n"
      "v_mov_b32 v226, v224\n"
      "v_mfma_f32_16x16x32_bf16 v[176:179], v[84:87], v[64:67], v[48:51]\n"
      "v_mad_u32_u24 v119, v66, s70, v119\n"
      "v_mad_u32_u24 v119, v67, s71, v119\n"
      "v_add3_u32 v118, v118, v66, v67\n"
      "v_permlane32_swap_b32 v224, v226\n"
      "s_waitcnt vmcnt(2)\n"
      "v_mfma_f32_16x16x32_bf16 v[164:167], v[86:89], v[68:71], v[36:39]\n"
      "v_mad_u32_u24 v119, v68, s60, v119\n"
      "v_mad_u32_u24 v119, v69, s62, v119\n"
      "v_add3_u32 v118, v118, v68, v69\n"
      "v_mov_b32 v225, v224\n"
      "v_mov_b32 v227, v226\n"
      "v_mfma_f32_16x16x32_bf16 v[180:183], v[84:87], v[68:71], v[52:55]\n"
      "v_mad_u32_u24 v119, v70, s70, v119\n"
      "v_mad_u32_u24 v119, v71, s71, v119\n"
      "v_add3_u32 v118, v118, v70, v71\n"
      "v_permlane16_swap_b32 v224, v225\n"
      "s_waitcnt vmcnt(1)\n"
      "v_mfma_f32_16x16x32_bf16 v[168:171], v[86:89], v[72:75], v[40:43]\n"
      "v_mad_u32_u24 v119, v72, s60, v119\n"
      "v_mad_u32_u24 v119, v73, s62, v119\n"
      "v_add3_u32 v118, v118, v72, v73\n"
      "v_permlane16_swap_b32 v226, v227\n"
      "v_mfma_f32_16x16x32_bf16 v[184:187], v[84:87], v[72:75], v[56:59]\n"
      "v_mad_u32_u24 v119, v74, s70, v119\n"
      "v_mad_u32_u24 v119, v75, s71, v119\n"
      "v_add3_u32 v118, v118, v74, v75\n"
      "s_waitcnt vmcnt(0)\n"
      "v_mfma_f32_16x16x32_bf16 v[172:175], v[86:89], v[76:79], v[44:47]\n"
      "v_mad_u32_u24 v119, v76, s60, v119\n"
      "v_mad_u32_u24 v119, v77, s62, v119\n"
      "v_add3_u32 v118, v118, v76, v77\n"
      "v_mfma_f32_16x16x32_bf16 v[188:191], v[84:87], v[76:79], v[60:63]\n"
      "v_mad_u32_u24 v119, v78, s70, v119\n"
      "v_mad_u32_u24 v119, v79, s71, v119\n"
      "v_add3_u32 v118, v118, v78, v79\n"
      "v_cmp_ne_u64 vcc, v[118:119], v[82:83]\n"
      "s_cbranch_vccnz RETRY_%=\n"
      "OK_%=:\n"
      "v_cmp_neq_f32 vcc, v81, v117\n"
      "s_cbranch_vccnz FIX_%=\n"
      "FR_%=:\n"
      "v_pk_mul_f32 v[160:161], v[160:161], v[224:225] op_sel:[0,0] op_sel_hi:[1,0]\n"
      "v_pk_mul_f32 v[162:163], v[162:163], v[224:225] op_sel:[0,0] op_sel_hi:[1,0]\n"
      "v_pk_mul_f32 v[176:177], v[176:177], v[224:225] op_sel:[0,0] op_sel_hi:[1,0]\n"
      "v_pk_mul_f32 v[178:179], v[178:179], v[224:225] op_sel:[0,0] op_sel_hi:[1,0]\n"
      "v_cvt_pk_bf16_f32 v128, v160, v161\n"
      "v_cvt_pk_bf16_f32 v130, v176, v177\n"
      "v_cvt_pk_bf16_f32 v129, v162, v163\n"
      "v_cvt_pk_bf16_f32 v131, v178, v179\n"
      "v_pk_mul_f32 v[164:165], v[164:165], v[224:225] op_sel:[0,1] op_sel_hi:[1,1]\n"
      "v_permlane16_swap_b32 v128, v130\n"
      "v_permlane16_swap_b32 v129, v131\n"
      "global_store_dwordx4 v156, v[128:131], s[16:17] offset:-4096 sc1\n"
      "v_pk_mul_f32 v[166:167], v[166:167], v[224:225] op_sel:[0,1] op_sel_hi:[1,1]\n"
      "v_pk_mul_f32 v[180:181], v[180:181], v[224:225] op_sel:[0,1] op_sel_hi:[1,1]\n"
      "v_pk_mul_f32 v[182:183], v[182:183], v[224:225] op_sel:[0,1] op_sel_hi:[1,1]\n"
      "v_cvt_pk_bf16_f32 v132, v164, v165\n"
      "v_cvt_pk_bf16_f32 v134, v180, v181\n"
      "v_cvt_pk_bf16_f32 v133, v166, v167\n"
      "v_cvt_pk_bf16_f32 v135, v182, v183\n"
      "v_pk_mul_f32 v[168:169], v[168:169], v[226:227] op_sel:[0,0] op_sel_hi:[1,0]\n"
      "v_permlane16_swap_b32 v132, v134\n"
      "v_permlane16_swap_b32 v133, v135\n"
      "global_store_dwordx4 v156, v[132:135], s[16:17] offset:0 sc1\n"
      "v_pk_mul_f32 v[170:171], v[170:171], v[226:227] op_sel:[0,0] op_sel_hi:[1,0]\n"
      "v_pk_mul_f32 v[184:185], v[184:185], v[226:227] op_sel:[0,0] op_sel_hi:[1,0]\n"
      "v_pk_mul_f32 v[186:187], v[186:187], v[226:227] op_sel:[0,0] op_sel_hi:[1,0]\n"
      "v_cvt_pk_bf16_f32 v136, v168, v169\n"
      "v_cvt_pk_bf16_f32 v138, v184, v185\n"
      "v_cvt_pk_bf16_f32 v137, v170, v171\n"
      "v_cvt_pk_bf16_f32 v139, v186, v187\n"
      "v_pk_mul_f32 v[172:173], v[172:173], v[226:227] op_sel:[0,1] op_sel_hi:[1,1]\n"
      "v_permlane16_swap_b32 v136, v138\n"
      "v_permlane16_swap_b32 v137, v139\n"
      "global_store_dwordx4 v155, v[136:139], s[16:17] offset:-4096 sc1\n"
      "v_pk_mul_f32 v[174:175], v[174:175], v[226:227] op_sel:[0,1] op_sel_hi:[1,1]\n"
      "v_pk_mul_f32 v[188:189], v[188:189], v[226:227] op_sel:[0,1] op_sel_hi:[1,1]\n"
      "v_pk_mul_f32 v[190:191], v[190:191], v[226:227] op_sel:[0,1] op_sel_hi:[1,1]\n"
      "v_cvt_pk_bf16_f32 v140, v172, v173\n"
      "v_cvt_pk_bf16_f32 v142, v188, v189\n"
      "v_cvt_pk_bf16_f32 v141, v174, v175\n"
      "v_cvt_pk_bf16_f32 v143, v190, v191\n"
      "v_and_b32 v157, 0xffffff80, v157\n"
      "v_permlane16_swap_b32 v140, v142\n"
      "v_permlane16_swap_b32 v141, v143\n"
      "global_store_dwordx4 v155, v[140:143], s[16:17] offset:0 sc1\n"
      "s_and_b32 s67, s37, 8\n"
      "s_or_b32 s67, s67, s33\n"
      "s_cbranch_scc1 END_%=\n"
      "s_mov_b64 exec, 1\n"
      "global_store_dwordx2 v157, v[150:151], s[18:19] sc1\n"
      "s_mov_b64 exec, -1\n"
      "END_%=:\n"
      ".subsection 1\n"
      "POLL_%=:\n"
      "s_waitcnt vmcnt(0) lgkmcnt(0)\n"
      "v_cmp_ne_u32 vcc, v234, v150\n"
      "v_mov_b32 v233, v235\n"
      "s_memrealtime s[68:69]\n"
      "s_sub_u32 s63, s63, 1\n"
      "s_cbranch_vccz GOT_%=\n"
      "global_load_dwordx2 v[234:235], v158, s[18:19] sc1\n"
      "s_cbranch_scc0 POLL_%=\n"
      "s_branch GOT_%=\n"
      "RETRY_%=:\n"
      "s_sub_u32 s63, s63, 1\n"
      "s_cbranch_scc1 OK_%=\n"
      "s_sleep 4\n"
      "buffer_load_dwordx4 v[80:83], v127, s[28:31], s79 offen sc1\n"
      "buffer_load_dwordx4 v[64:67], v127, s[28:31], s80 offen offset:0 sc1\n"
      "buffer_load_dwordx4 v[68:71], v127, s[28:31], s80 offen offset:1024 sc1\n"
      "buffer_load_dwordx4 v[72:75], v127, s[28:31], s80 offen offset:2048 sc1\n"
      "buffer_load_dwordx4 v[76:79], v127, s[28:31], s80 offen offset:3072 sc1\n"
      "s_branch CHK_%=\n"
      "GW0_%=:\n"
      "s_call_b64 s[84:85], WAIT_%=\n"
      "s_branch GX0_%=\n"
      "GW1_%=:\n"
      "s_call_b64 s[84:85], WAIT_%=\n"
      "s_branch GX1_%=\n"
      "GW2_%=:\n"
      "s_call_b64 s[84:85], WAIT_%=\n"
      "s_branch GX2_%=\n"
      "GW3_%=:\n"
      "s_call_b64 s[84:85], WAIT_%=\n"
      "s_add_u32 s87, s87, 1\n"
      "s_branch GX3_%=\n"
      "GW4_%=:\n"
      "s_call_b64 s[84:85], WAIT_%=\n"
      "s_branch GX4_%=\n"
      "GW5_%=:\n"
      "s_call_b64 s[84:85], WAIT_%=\n"
      "s_branch GX5_%=\n"
      "GW6_%=:\n"
      "s_call_b64 s[84:85], WAIT_%=\n"
      "s_branch GX6_%=\n"
      "GW7_%=:\n"
      "s_call_b64 s[84:85], WAIT_%=\n"
      "s_branch GX7_%=\n"
      "GW8_%=:\n"
      "s_call_b64 s[84:85], WAIT_%=\n"
      "s_branch GX8_%=\n"
      "WAIT_%=:\n"
      "s_add_u32 s87, s87, 1\n"
      "s_bitcmp1_b32 s89, s87\n"
      "s_cbranch_scc1 H1_%=\n"
      "SPIN_%=:\n"
      "s_sub_u32 s67, s68, s88\n"
      "s_cmp_ge_i32 s67, 0\n"
      "s_cbranch_scc1 GO_%=\n"
      "s_sleep 1\n"
      "s_memrealtime s[68:69]\n"
      "s_waitcnt lgkmcnt(0)\n"
      "s_branch SPIN_%=\n"
      "GO_%=:\n"
      "s_setpc_b64 s[84:85]\n"
      "H1_%=:\n"
      "s_cmp_gt_i32 s93, 50\n"
      "s_cbranch_scc1 HS_%=\n"
      "s_bitcmp1_b32 0x2a80, s87\n"
      "s_cbranch_scc0 H2_%=\n"
      "s_add_u32 s67, s66, 400\n"
      "s_waitcnt vmcnt(8)\n"
      "v_sub_u32 v250, s67, v250\n"
      "v_cmp_gt_u32 vcc, 900, v250\n"
      "v_add_u32 v250, 0xa00, v250\n"
      "v_xor_b32 v251, 0x100, v251\n"
      "s_sub_u32 s69, s66, s71\n"
      "s_cmp_ge_u32 s87, 12\n"
      "s_cselect_b32 s101, 50, s101\n"
      "s_bcnt1_i32_b32 s74, vcc_lo\n"
      "s_bcnt1_i32_b32 s75, vcc_hi\n"
      "v_cmp_gt_u32 vcc, 0x800, v250\n"
      "s_add_u32 s67, s74, s75\n"
      "s_cmp_eq_u32 s87, 7\n"
      "s_cselect_b32 s76, 1, 0\n"
      "s_lshr_b32 s76, s67, s76\n"
      "s_cmp_le_u32 s76, 2\n"
      "s_cselect_b32 s86, 107, s98\n"
      "s_cmp_ge_u32 s76, 9\n"
      "s_cselect_b32 s86, 112, s86\n"
      "s_cbranch_vccnz H6_%=\n"
      "s_cmp_ge_u32 s74, 12\n"
      "s_cbranch_scc1 H3_%=\n"
      "s_cmp_eq_u32 s87, 13\n"
      "s_cbranch_scc1 H4_%=\n"
      "H8_%=:\n"
      "s_lshr_b32 s67, s39, 7\n"
      "s_cmp_eq_u32 s67, 1\n"
      "s_cbranch_scc0 SPIN_%=\n"
      "H5_%=:\n"
      "s_cmp_gt_i32 s93, 50\n"
      "s_cselect_b32 s75, -10, 0\n"
      "v_mov_b32 v127, s66\n"
      "s_add_u32 s74, s87, 2\n"
      "s_bfe_u32 s74, s74, 0x10001\n"
      "s_lshl_b32 s74, s74, 8\n"
      "s_add_u32 s74, s74, s39\n"
      "RS_%=:\n"
      "s_sub_u32 s67, s68, s88\n"
      "s_cmp_ge_i32 s67, s75\n"
      "s_cbranch_scc1 RG_%=\n"
      "s_sleep 1\n"
      "s_memrealtime s[68:69]\n"
      "s_waitcnt lgkmcnt(0)\n"
      "s_branch RS_%=\n"
      "RG_%=:\n"
      "s_getreg_b32 s67, hwreg(HW_REG_IB_STS, 0, 4)\n"
      "s_cmp_le_u32 s67, 4\n"
      "s_cbranch_scc1 SPIN_%=\n"
      "buffer_store_dword v127, v124, s[28:31], s74 offen sc1\n"
      "s_branch SPIN_%=\n"
      "H6_%=:\n"
      "s_ff1_i32_b64 s67, vcc\n"
      "v_sub_u32 v250, s66, v250\n"
      "s_nop 3\n"
      "v_readlane_b32 s76, v250, s67\n"
      "s_add_u32 s76, s76, 2960\n"
      "s_and_b32 s76, s76, 31\n"
      "s_lshl_b32 s76, s76, 3\n"
      "s_lshl_b32 s89, 4, s87\n"
      "s_branch H7_%=\n"
      "H3_%=:\n"
      "s_mul_i32 s76, s69, 110\n"
      "s_ashr_i32 s76, s76, 10\n"
      "s_min_i32 s76, s76, 40\n"
      "s_add_u32 s76, s76, 76\n"
      "s_cmp_lt_u32 s75, 24\n"
      "s_cselect_b32 s76, 56, s76\n"
      "s_cmp_lt_u32 s75, 13\n"
      "s_cselect_b32 s76, 40, s76\n"
      "s_cmp_lt_u32 s75, 5\n"
      "s_cselect_b32 s76, 24, s76\n"
      "s_add_u32 s67, s66, 2000\n"
      "s_and_b32 s67, s67, -32\n"
      "s_lshr_b32 s75, s76, 3\n"
      "s_or_b32 s67, s67, s75\n"
      "v_mov_b32 v127, s67\n"
      "s_add_u32 s74, s87, 2\n"
      "s_bfe_u32 s74, s74, 0x10001\n"
      "s_lshl_b32 s74, s74, 8\n"
      "s_add_u32 s74, s74, s39\n"
      "s_cmp_lt_u32 s39, 128\n"
      "s_cselect_b32 s89, 22, 20\n"
      "s_lshl_b32 s89, s89, s87\n"
      "s_sub_u32 s75, s39, 128\n"
      "s_cmp_lt_u32 s75, 32\n"
      "s_cbranch_scc0 H7_%=\n"
      "buffer_store_dword v127, v124, s[28:31], s74 offen sc1\n"
      "H7_%=:\n"
      "s_add_u32 s66, s66, s76\n"
      "s_mov_b32 s99, 17\n"
      "s_mov_b32 s100, 288\n"
      "s_add_u32 s93, s93, 99\n"
      "s_mov_b32 s86, 110\n"
      "s_mov_b32 s80, 0xc853212b\n"
      "s_mov_b32 s81, 0x842210\n"
      "s_cmp_le_u32 s76, 40\n"
      "s_cselect_b32 s75, 15, 0\n"
      "s_cmp_le_u32 s76, 24\n"
      "s_cselect_b32 s75, 25, s75\n"
      "s_lshr_b64 s[80:81], s[80:81], s75\n"
      "s_sub_u32 s67, s65, s66\n"
      "s_cmp_lt_i32 s67, 0\n"
      "s_cselect_b32 s88, s66, s65\n"
      "s_mov_b32 s101, 200\n"
      "s_branch SPIN_%=\n"
      "H4_%=:\n"
      "s_mov_b32 s91, 0\n"
      "s_mov_b32 s89, 0\n"
      "v_mov_b32 v251, 0x4000000\n"
      "s_branch SPIN_%=\n"
      "HS_%=:\n"
      "s_bitcmp1_b32 s87, 0\n"
      "s_cbranch_scc0 H5_%=\n"
      "v_xor_b32 v251, 0x100, v251\n"
      "s_lshr_b32 s67, s89, s87\n"
      "s_cmp_gt_u32 s67, 1\n"
      "s_cbranch_scc1 H8_%=\n"
      "s_add_u32 s67, s66, 400\n"
      "s_waitcnt vmcnt(8)\n"
      "v_sub_u32 v250, s67, v250\n"
      "v_cmp_gt_u32 vcc, 900, v250\n"
      "s_nop 3\n"
      "s_bcnt1_i32_b32 s74, vcc_lo\n"
      "s_bcnt1_i32_b32 s75, vcc_hi\n"
      "s_cmp_ge_u32 s75, 5\n"
      "s_cbranch_scc1 HT_%=\n"
      "s_cmp_gt_u32 s74, 2\n"
      "s_cbranch_scc1 H4_%=\n"
      "s_mov_b32 s99, 27\n"
      "s_mov_b32 s100, 285\n"
      "s_mov_b32 s86, 107\n"
      "s_mov_b64 s[80:81], 0\n"
      "s_branch H4_%=\n"
      "HT_%=:\n"
      "s_add_u32 s66, s66, 64\n"
      "s_mov_b32 s80, 0x86429909\n"
      "s_mov_b32 s81, 0x42110\n"
      "s_sub_u32 s67, s65, s66\n"
      "s_cmp_lt_i32 s67, 0\n"
      "s_cselect_b32 s88, s66, s65\n"
      "s_branch H4_%=\n"
      "H0_%=:\n"
      "buffer_load_dword v250, v251, s[28:31], 0 offen sc1\n"
      "s_branch SPIN_%=\n"
      "H9_%=:\n"
      "s_bitcmp1_b32 s87, 0\n"
      "s_cbranch_scc0 H5_%=\n"
      "v_xor_b32 v251, 0x100, v251\n"
      "s_cmp_eq_u32 s87, 5\n"
      "s_cbranch_scc1 H8_%=\n"
      "s_branch SPIN_%=\n"
      "H2_%=:\n"
      "s_cmp_gt_u32 s87, 2\n"
      "s_cbranch_scc1 H9_%=\n"
      "s_cmp_eq_u32 s87, 1\n"
      "s_cbranch_scc1 H0_%=\n"
      "s_waitcnt vmcnt(8)\n"
      "v_subrev_u32 v250, s71, v250\n"
      "v_min_u32 v250, 0x258, v250\n"
      "v_mbcnt_lo_u32_b32 v251, -1, 0\n"
      "v_mbcnt_hi_u32_b32 v251, -1, v251\n"
      "v_add_u32_dpp v250, v250, v250 row_shr:1 row_mask:0xf bank_mask:0xf bound_ctrl:0\n"
      "v_lshlrev_b32 v124, 23, v251\n"
      "s_nop 0\n"
      "v_add_u32_dpp v250, v250, v250 row_shr:2 row_mask:0xf bank_mask:0xf bound_ctrl:0\n"
      "v_lshlrev_b32 v251, 2, v251\n"
      "v_add_u32 v124, 0x6000, v124\n"
      "v_add_u32_dpp v250, v250, v250 row_shr:4 row_mask:0xf bank_mask:0xf bound_ctrl:0\n"
      "v_add_u32 v251, 0x6100, v251\n"
      "v_readlane_b32 s66, v250, 7\n"
      "s_nop 0\n"
      "s_lshr_b32 s66, s66, 3\n"
      "s_add_u32 s66, s66, s71\n"
      "s_add_u32 s66, s66, s38\n"
      "s_add_u32 s71, s66, 550\n"
      "s_sub_u32 s67, s65, s66\n"
      "s_cmp_lt_i32 s67, 0\n"
      "s_cselect_b32 s88, s66, s65\n"
      "s_branch SPIN_%=\n"
      "FB0_%=:\n"
      "s_nop 7\n"
      "s_nop 7\n"
      "v_xor_b32 v118, 0x0, v242\n"
      "v_xor_b32 v119, 0x0, v243\n"
      "ds_read_b128 a[64:67], v118 offset:0\n"
      "ds_read_b128 a[68:71], v118 offset:4096\n"
      "ds_read_b128 a[72:75], v119 offset:0\n"
      "ds_read_b128 a[76:79], v119 offset:4096\n"
      "ds_read_b128 a[80:83], v118 offset:1024\n"
      "ds_read_b128 a[84:87], v118 offset:5120\n"
      "ds_read_b128 a[88:91], v119 offset:1024\n"
      "ds_read_b128 a[92:95], v119 offset:5120\n"
      "s_waitcnt lgkmcnt(0)\n"
      "v_mfma_f32_16x16x32_bf16 v[128:131], a[64:67], a[0:3], 0\n"
      "v_mfma_f32_16x16x32_bf16 v[136:139], a[64:67], a[4:7], 0\n"
      "v_mfma_f32_16x16x32_bf16 v[144:147], a[64:67], a[8:11], 0\n"
      "v_mfma_f32_16x16x32_bf16 v[152:155], a[64:67], a[12:15], 0\n"
      "v_mfma_f32_16x16x32_bf16 v[132:135], a[68:71], a[0:3], 0\n"
      "v_mfma_f32_16x16x32_bf16 v[140:143], a[68:71], a[4:7], 0\n"
      "v_mfma_f32_16x16x32_bf16 v[148:151], a[68:71], a[8:11], 0\n"
      "v_mfma_f32_16x16x32_bf16 v[156:159], a[68:71], a[12:15], 0\n"
      "v_mfma_f32_16x16x32_bf16 v[128:131], a[72:75], a[16:19], v[128:131]\n"
      "v_mfma_f32_16x16x32_bf16 v[136:139], a[72:75], a[20:23], v[136:139]\n"
      "v_mfma_f32_16x16x32_bf16 v[144:147], a[72:75], a[24:27], v[144:147]\n"
      "v_mfma_f32_16x16x32_bf16 v[152:155], a[72:75], a[28:31], v[152:155]\n"
      "v_mfma_f32_16x16x32_bf16 v[132:135], a[76:79], a[16:19], v[132:135]\n"
      "v_mfma_f32_16x16x32_bf16 v[140:143], a[76:79], a[20:23], v[140:143]\n"
      "v_mfma_f32_16x16x32_bf16 v[148:151], a[76:79], a[24:27], v[148:151]\n"
      "v_mfma_f32_16x16x32_bf16 v[156:159], a[76:79], a[28:31], v[156:159]\n"
      "v_mfma_f32_16x16x32_bf16 v[128:131], a[80:83], a[32:35], v[128:131]\n"
      "v_mfma_f32_16x16x32_bf16 v[136:139], a[80:83], a[36:39], v[136:139]\n"
      "v_mfma_f32_16x16x32_bf16 v[144:147], a[80:83], a[40:43], v[144:147]\n"
      "v_mfma_f32_16x16x32_bf16 v[152:155], a[80:83], a[44:47], v[152:155]\n"
      "v_mfma_f32_16x16x32_bf16 v[132:135], a[84:87], a[32:35], v[132:135]\n"
      "v_mfma_f32_16x16x32_bf16 v[140:143], a[84:87], a[36:39], v[140:143]\n"
      "v_mfma_f32_16x16x32_bf16 v[148:151], a[84:87], a[40:43], v[148:151]\n"
      "v_mfma_f32_16x16x32_bf16 v[156:159], a[84:87], a[44:47], v[156:159]\n"
      "v_mfma_f32_16x16x32_bf16 v[128:131], a[88:91], a[48:51], v[128:131]\n"
      "v_mfma_f32_16x16x32_bf16 v[136:139], a[88:91], a[52:55], v[136:139]\n"
      "v_mfma_f32_16x16x32_bf16 v[144:147], a[88:91], a[56:59], v[144:147]\n"
      "v_mfma_f32_16x16x32_bf16 v[152:155], a[88:91], a[60:63], v[152:155]\n"
      "v_mfma_f32_16x16x32_bf16 v[132:135], a[92:95], a[48:51], v[132:135]\n"
      "v_mfma_f32_16x16x32_bf16 v[140:143], a[92:95], a[52:55], v[140:143]\n"
      "v_mfma_f32_16x16x32_bf16 v[148:151], a[92:95], a[56:59], v[148:151]\n"
      "v_mfma_f32_16x16x32_bf16 v[156:159], a[92:95], a[60:63], v[156:159]\n"
      "s_nop 7\n"
      "v_max3_f32 v112, v128, v129, v130\n"
      "v_max3_f32 v113, v136, v137, v138\n"
      "v_max3_f32 v114, v144, v145, v146\n"
      "v_max3_f32 v115, v152, v153, v154\n"
      "v_max3_f32 v112, v112, v131, v132\n"
      "v_max3_f32 v113, v113, v139, v140\n"
      "v_max3_f32 v114, v114, v147, v148\n"
      "v_max3_f32 v115, v115, v155, v156\n"
      "v_max3_f32 v112, v112, v133, v134\n"
      "v_max3_f32 v113, v113, v141, v142\n"
      "v_max3_f32 v114, v114, v149, v150\n"
      "v_max3_f32 v115, v115, v157, v158\n"
      "v_max_f32 v112, v112, v135\n"
      "v_max_f32 v113, v113, v143\n"
      "v_max_f32 v114, v114, v151\n"
      "v_max_f32 v115, v115, v159\n"
      "v_mov_b32 v160, v112\n"
      "v_mov_b32 v161, v113\n"
      "v_mov_b32 v162, v114\n"
      "v_mov_b32 v163, v115\n"
      "v_permlane16_swap_b32 v112, v160\n"
      "v_permlane16_swap_b32 v113, v161\n"
      "v_permlane16_swap_b32 v114, v162\n"
      "v_permlane16_swap_b32 v115, v163\n"
      "v_max_f32 v112, v112, v160\n"
      "v_max_f32 v113, v113, v161\n"
      "v_max_f32 v114, v114, v162\n"
      "v_max_f32 v115, v115, v163\n"
      "v_mov_b32 v160, v112\n"
      "v_mov_b32 v161, v113\n"
      "v_mov_b32 v162, v114\n"
      "v_mov_b32 v163, v115\n"
      "v_permlane32_swap_b32 v112, v160\n"
      "v_permlane32_swap_b32 v113, v161\n"
      "v_permlane32_swap_b32 v114, v162\n"
      "v_permlane32_swap_b32 v115, v163\n"
      "v_max_f32 v112, v112, v160\n"
      "v_max_f32 v113, v113, v161\n"
      "v_max_f32 v114, v114, v162\n"
      "v_max_f32 v115, v115, v163\n"
      "v_sub_f32 v112, 0xc1800000, v112\n"
      "v_sub_f32 v113, 0xc1800000, v113\n"
      "v_sub_f32 v114, 0xc1800000, v114\n"
      "v_sub_f32 v115, 0xc1800000, v115\n"
      "v_floor_f32 v112, v112\n"
      "v_floor_f32 v113, v113\n"
      "v_floor_f32 v114, v114\n"
      "v_floor_f32 v115, v115\n"
      "v_min_f32 v112, v112, v224\n"
      "v_min_f32 v113, v113, v228\n"
      "v_min_f32 v114, v114, v232\n"
      "v_min_f32 v115, v115, v236\n"
      "v_sub_f32 v164, v112, v224\n"
      "v_sub_f32 v165, v113, v228\n"
      "v_sub_f32 v166, v114, v232\n"
      "v_sub_f32 v167, v115, v236\n"
      "v_mov_b32 v224, v112\n"
      "v_mov_b32 v228, v113\n"
      "v_mov_b32 v232, v114\n"
      "v_mov_b32 v236, v115\n"
      "v_exp_f32 v112, v164\n"
      "v_exp_f32 v113, v165\n"
      "v_exp_f32 v114, v166\n"
      "v_exp_f32 v115, v167\n"
      "v_mov_b32 v225, v224\n"
      "v_mov_b32 v229, v228\n"
      "v_mov_b32 v233, v232\n"
      "v_mov_b32 v237, v236\n"
      "v_mov_b64 v[226:227], v[224:225]\n"
      "v_mov_b64 v[230:231], v[228:229]\n"
      "v_mov_b64 v[234:235], v[232:233]\n"
      "v_mov_b64 v[238:239], v[236:237]\n"
      "s_call_b64 s[84:85], SCALE_%=\n"
      "v_pk_add_f32 v[128:129], v[128:129], v[224:225]\n"
      "v_pk_add_f32 v[130:131], v[130:131], v[224:225]\n"
      "v_pk_add_f32 v[132:133], v[132:133], v[224:225]\n"
      "v_pk_add_f32 v[134:135], v[134:135], v[224:225]\n"
      "v_pk_add_f32 v[136:137], v[136:137], v[228:229]\n"
      "v_pk_add_f32 v[138:139], v[138:139], v[228:229]\n"
      "v_pk_add_f32 v[140:141], v[140:141], v[228:229]\n"
      "v_pk_add_f32 v[142:143], v[142:143], v[228:229]\n"
      "v_pk_add_f32 v[144:145], v[144:145], v[232:233]\n"
      "v_pk_add_f32 v[146:147], v[146:147], v[232:233]\n"
      "v_pk_add_f32 v[148:149], v[148:149], v[232:233]\n"
      "v_pk_add_f32 v[150:151], v[150:151], v[232:233]\n"
      "v_pk_add_f32 v[152:153], v[152:153], v[236:237]\n"
      "v_pk_add_f32 v[154:155], v[154:155], v[236:237]\n"
      "v_pk_add_f32 v[156:157], v[156:157], v[236:237]\n"
      "v_pk_add_f32 v[158:159], v[158:159], v[236:237]\n"
      "v_exp_f32 v112, v128\n"
      "v_exp_f32 v113, v129\n"
      "v_exp_f32 v114, v130\n"
      "v_cvt_pk_bf16_f32 v128, v112, v113\n"
      "v_exp_f32 v115, v131\n"
      "v_exp_f32 v112, v132\n"
      "v_cvt_pk_bf16_f32 v129, v114, v115\n"
      "v_exp_f32 v113, v133\n"
      "v_exp_f32 v114, v134\n"
      "v_cvt_pk_bf16_f32 v130, v112, v113\n"
      "v_exp_f32 v115, v135\n"
      "v_exp_f32 v112, v136\n"
      "v_cvt_pk_bf16_f32 v131, v114, v115\n"
      "v_exp_f32 v113, v137\n"
      "v_exp_f32 v114, v138\n"
      "v_cvt_pk_bf16_f32 v132, v112, v113\n"
      "v_exp_f32 v115, v139\n"
      "v_exp_f32 v112, v140\n"
      "v_cvt_pk_bf16_f32 v133, v114, v115\n"
      "v_exp_f32 v113, v141\n"
      "v_exp_f32 v114, v142\n"
      "v_cvt_pk_bf16_f32 v134, v112, v113\n"
      "v_exp_f32 v115, v143\n"
      "v_exp_f32 v112, v144\n"
      "v_cvt_pk_bf16_f32 v135, v114, v115\n"
      "v_exp_f32 v113, v145\n"
      "v_exp_f32 v114, v146\n"
      "v_cvt_pk_bf16_f32 v136, v112, v113\n"
      "v_exp_f32 v115, v147\n"
      "v_exp_f32 v112, v148\n"
      "v_cvt_pk_bf16_f32 v137, v114, v115\n"
      "v_exp_f32 v113, v149\n"
      "v_exp_f32 v114, v150\n"
      "v_cvt_pk_bf16_f32 v138, v112, v113\n"
      "v_exp_f32 v115, v151\n"
      "v_exp_f32 v112, v152\n"
      "v_cvt_pk_bf16_f32 v139, v114, v115\n"
      "v_exp_f32 v113, v153\n"
      "v_exp_f32 v114, v154\n"
      "v_cvt_pk_bf16_f32 v140, v112, v113\n"
      "v_exp_f32 v115, v155\n"
      "v_exp_f32 v112, v156\n"
      "v_cvt_pk_bf16_f32 v141, v114, v115\n"
      "v_exp_f32 v113, v157\n"
      "v_exp_f32 v114, v158\n"
      "v_cvt_pk_bf16_f32 v142, v112, v113\n"
      "v_exp_f32 v115, v159\n"
      "s_nop 0\n"
      "v_cvt_pk_bf16_f32 v143, v114, v115\n"
      "ds_read_b128 a[64:67], v242 offset:32768\n"
      "ds_read_b128 a[68:71], v242 offset:36864\n"
      "ds_read_b128 a[72:75], v243 offset:32768\n"
      "ds_read_b128 a[76:79], v243 offset:36864\n"
      "ds_read_b128 a[80:83], v242 offset:33792\n"
      "ds_read_b128 a[84:87], v242 offset:37888\n"
      "ds_read_b128 a[88:91], v243 offset:33792\n"
      "ds_read_b128 a[92:95], v243 offset:37888\n"
      "s_waitcnt lgkmcnt(0)\n"
      "v_mfma_f32_16x16x32_bf16 v[160:163], a[64:67], a[0:3], v[224:227]\n"
      "v_mfma_f32_16x16x32_bf16 v[168:171], a[64:67], a[4:7], v[228:231]\n"
      "v_mfma_f32_16x16x32_bf16 v[176:179], a[64:67], a[8:11], v[232:235]\n"
      "v_mfma_f32_16x16x32_bf16 v[184:187], a[64:67], a[12:15], v[236:239]\n"
      "v_mfma_f32_16x16x32_bf16 v[164:167], a[68:71], a[0:3], v[224:227]\n"
      "v_mfma_f32_16x16x32_bf16 v[172:175], a[68:71], a[4:7], v[228:231]\n"
      "v_mfma_f32_16x16x32_bf16 v[180:183], a[68:71], a[8:11], v[232:235]\n"
      "v_mfma_f32_16x16x32_bf16 v[188:191], a[68:71], a[12:15], v[236:239]\n"
      "v_mfma_f32_16x16x32_bf16 v[160:163], a[72:75], a[16:19], v[160:163]\n"
      "v_mfma_f32_16x16x32_bf16 v[168:171], a[72:75], a[20:23], v[168:171]\n"
      "v_mfma_f32_16x16x32_bf16 v[176:179], a[72:75], a[24:27], v[176:179]\n"
      "v_mfma_f32_16x16x32_bf16 v[184:187], a[72:75], a[28:31], v[184:187]\n"
      "v_mfma_f32_16x16x32_bf16 v[164:167], a[76:79], a[16:19], v[164:167]\n"
      "v_mfma_f32_16x16x32_bf16 v[172:175], a[76:79], a[20:23], v[172:175]\n"
      "v_mfma_f32_16x16x32_bf16 v[180:183], a[76:79], a[24:27], v[180:183]\n"
      "v_mfma_f32_16x16x32_bf16 v[188:191], a[76:79], a[28:31], v[188:191]\n"
      "v_mfma_f32_16x16x32_bf16 v[160:163], a[80:83], a[32:35], v[160:163]\n"
      "v_mfma_f32_16x16x32_bf16 v[168:171], a[80:83], a[36:39], v[168:171]\n"
      "v_mfma_f32_16x16x32_bf16 v[176:179], a[80:83], a[40:43], v[176:179]\n"
      "v_mfma_f32_16x16x32_bf16 v[184:187], a[80:83], a[44:47], v[184:187]\n"
      "v_mfma_f32_16x16x32_bf16 v[164:167], a[84:87], a[32:35], v[164:167]\n"
      "v_mfma_f32_16x16x32_bf16 v[172:175], a[84:87], a[36:39], v[172:175]\n"
      "v_mfma_f32_16x16x32_bf16 v[180:183], a[84:87], a[40:43], v[180:183]\n"
      "v_mfma_f32_16x16x32_bf16 v[188:191], a[84:87], a[44:47], v[188:191]\n"
      "v_mfma_f32_16x16x32_bf16 v[160:163], a[88:91], a[48:51], v[160:163]\n"
      "v_mfma_f32_16x16x32_bf16 v[168:171], a[88:91], a[52:55], v[168:171]\n"
      "v_mfma_f32_16x16x32_bf16 v[176:179], a[88:91], a[56:59], v[176:179]\n"
      "v_mfma_f32_16x16x32_bf16 v[184:187], a[88:91], a[60:63], v[184:187]\n"
      "v_mfma_f32_16x16x32_bf16 v[164:167], a[92:95], a[48:51], v[164:167]\n"
      "v_mfma_f32_16x16x32_bf16 v[172:175], a[92:95], a[52:55], v[172:175]\n"
      "v_mfma_f32_16x16x32_bf16 v[180:183], a[92:95], a[56:59], v[180:183]\n"
      "v_mfma_f32_16x16x32_bf16 v[188:191], a[92:95], a[60:63], v[188:191]\n"
      "s_cmp_eq_u32 s64, 0\n"
      "s_cbranch_scc0 NRET0_0_%=\n"
      "v_mov_b64 v[192:193], v[128:129]\n"
      "v_mov_b64 v[194:195], v[130:131]\n"
      "v_mov_b64 v[196:197], v[132:133]\n"
      "v_mov_b64 v[198:199], v[134:135]\n"
      "v_mov_b64 v[200:201], v[136:137]\n"
      "v_mov_b64 v[202:203], v[138:139]\n"
      "v_mov_b64 v[204:205], v[140:141]\n"
      "v_mov_b64 v[206:207], v[142:143]\n"
      "s_nop 1\n"
      "s_branch RET0_0_%=\n"
      "NRET0_0_%=:\n"
      "s_cmp_eq_u32 s64, 1\n"
      "s_cbranch_scc0 NRET0_1_%=\n"
      "v_mov_b64 v[192:193], v[128:129]\n"
      "v_mov_b64 v[194:195], v[130:131]\n"
      "v_mov_b64 v[196:197], v[132:133]\n"
      "v_mov_b64 v[198:199], v[134:135]\n"
      "v_mov_b64 v[200:201], v[136:137]\n"
      "v_mov_b64 v[202:203], v[138:139]\n"
      "v_mov_b64 v[204:205], v[140:141]\n"
      "v_mov_b64 v[206:207], v[142:143]\n"
      "s_nop 1\n"
      "s_branch RET0_1_%=\n"
      "NRET0_1_%=:\n"
      "s_cmp_eq_u32 s64, 2\n"
      "s_cbranch_scc0 NRET0_2_%=\n"
      "v_mov_b64 v[192:193], v[128:129]\n"
      "v_mov_b64 v[194:195], v[130:131]\n"
      "v_mov_b64 v[196:197], v[132:133]\n"
      "v_mov_b64 v[198:199], v[134:135]\n"
      "v_mov_b64 v[200:201], v[136:137]\n"
      "v_mov_b64 v[202:203], v[138:139]\n"
      "v_mov_b64 v[204:205], v[140:141]\n"
      "v_mov_b64 v[206:207], v[142:143]\n"
      "s_nop 1\n"
      "s_branch RET0_2_%=\n"
      "NRET0_2_%=:\n"
      "s_cmp_eq_u32 s64, 3\n"
      "s_cbranch_scc0 NRET0_3_%=\n"
      "v_mov_b64 v[192:193], v[128:129]\n"
      "v_mov_b64 v[194:195], v[130:131]\n"
      "v_mov_b64 v[196:197], v[132:133]\n"
      "v_mov_b64 v[198:199], v[134:135]\n"
      "v_mov_b64 v[200:201], v[136:137]\n"
      "v_mov_b64 v[202:203], v[138:139]\n"
      "v_mov_b64 v[204:205], v[140:141]\n"
      "v_mov_b64 v[206:207], v[142:143]\n"
      "s_nop 1\n"
      "s_branch RET0_3_%=\n"
      "NRET0_3_%=:\n"
      "s_cmp_eq_u32 s64, 4\n"
      "s_cbranch_scc0 NRET0_4_%=\n"
      "v_mov_b64 v[192:193], v[128:129]\n"
      "v_mov_b64 v[194:195], v[130:131]\n"
      "v_mov_b64 v[196:197], v[132:133]\n"
      "v_mov_b64 v[198:199], v[134:135]\n"
      "v_mov_b64 v[200:201], v[136:137]\n"
      "v_mov_b64 v[202:203], v[138:139]\n"
      "v_mov_b64 v[204:205], v[140:141]\n"
      "v_mov_b64 v[206:207], v[142:143]\n"
      "s_nop 1\n"
      "s_branch RET0_4_%=\n"
      "NRET0_4_%=:\n"
      "s_cmp_eq_u32 s64, 5\n"
      "s_cbranch_scc0 NRET0_5_%=\n"
      "v_mov_b64 v[16:17], v[128:129]\n"
      "v_mov_b64 v[18:19], v[130:131]\n"
      "v_mov_b64 v[20:21], v[132:133]\n"
      "v_mov_b64 v[22:23], v[134:135]\n"
      "v_mov_b64 v[24:25], v[136:137]\n"
      "v_mov_b64 v[26:27], v[138:139]\n"
      "v_mov_b64 v[28:29], v[140:141]\n"
      "v_mov_b64 v[30:31], v[142:143]\n"
      "s_nop 1\n"
      "s_branch RET0_5_%=\n"
      "NRET0_5_%=:\n"
      "FB1_%=:\n"
      "s_add_u32 s84, s63, 1\n"
      "s_sub_u32 s84, s84, s40\n"
      "s_or_b32 s84, s84, s64\n"
      "s_cbranch_scc0 RET1_0_%=\n"
      "s_nop 7\n"
      "s_nop 7\n"
      "v_xor_b32 v118, 0x10000, v242\n"
      "v_xor_b32 v119, 0x10000, v243\n"
      "ds_read_b128 a[64:67], v118 offset:32768\n"
      "ds_read_b128 a[68:71], v118 offset:36864\n"
      "ds_read_b128 a[72:75], v119 offset:32768\n"
      "ds_read_b128 a[76:79], v119 offset:36864\n"
      "ds_read_b128 a[80:83], v118 offset:33792\n"
      "ds_read_b128 a[84:87], v118 offset:37888\n"
      "ds_read_b128 a[88:91], v119 offset:33792\n"
      "ds_read_b128 a[92:95], v119 offset:37888\n"
      "s_waitcnt lgkmcnt(0)\n"
      "v_mfma_f32_16x16x32_bf16 v[160:163], a[64:67], a[0:3], 0\n"
      "v_mfma_f32_16x16x32_bf16 v[168:171], a[64:67], a[4:7], 0\n"
      "v_mfma_f32_16x16x32_bf16 v[176:179], a[64:67], a[8:11], 0\n"
      "v_mfma_f32_16x16x32_bf16 v[184:187], a[64:67], a[12:15], 0\n"
      "v_mfma_f32_16x16x32_bf16 v[164:167], a[68:71], a[0:3], 0\n"
      "v_mfma_f32_16x16x32_bf16 v[172:175], a[68:71], a[4:7], 0\n"
      "v_mfma_f32_16x16x32_bf16 v[180:183], a[68:71], a[8:11], 0\n"
      "v_mfma_f32_16x16x32_bf16 v[188:191], a[68:71], a[12:15], 0\n"
      "v_mfma_f32_16x16x32_bf16 v[160:163], a[72:75], a[16:19], v[160:163]\n"
      "v_mfma_f32_16x16x32_bf16 v[168:171], a[72:75], a[20:23], v[168:171]\n"
      "v_mfma_f32_16x16x32_bf16 v[176:179], a[72:75], a[24:27], v[176:179]\n"
      "v_mfma_f32_16x16x32_bf16 v[184:187], a[72:75], a[28:31], v[184:187]\n"
      "v_mfma_f32_16x16x32_bf16 v[164:167], a[76:79], a[16:19], v[164:167]\n"
      "v_mfma_f32_16x16x32_bf16 v[172:175], a[76:79], a[20:23], v[172:175]\n"
      "v_mfma_f32_16x16x32_bf16 v[180:183], a[76:79], a[24:27], v[180:183]\n"
      "v_mfma_f32_16x16x32_bf16 v[188:191], a[76:79], a[28:31], v[188:191]\n"
      "v_mfma_f32_16x16x32_bf16 v[160:163], a[80:83], a[32:35], v[160:163]\n"
      "v_mfma_f32_16x16x32_bf16 v[168:171], a[80:83], a[36:39], v[168:171]\n"
      "v_mfma_f32_16x16x32_bf16 v[176:179], a[80:83], a[40:43], v[176:179]\n"
      "v_mfma_f32_16x16x32_bf16 v[184:187], a[80:83], a[44:47], v[184:187]\n"
      "v_mfma_f32_16x16x32_bf16 v[164:167], a[84:87], a[32:35], v[164:167]\n"
      "v_mfma_f32_16x16x32_bf16 v[172:175], a[84:87], a[36:39], v[172:175]\n"
      "v_mfma_f32_16x16x32_bf16 v[180:183], a[84:87], a[40:43], v[180:183]\n"
      "v_mfma_f32_16x16x32_bf16 v[188:191], a[84:87], a[44:47], v[188:191]\n"
      "v_mfma_f32_16x16x32_bf16 v[160:163], a[88:91], a[48:51], v[160:163]\n"
      "v_mfma_f32_16x16x32_bf16 v[168:171], a[88:91], a[52:55], v[168:171]\n"
      "v_mfma_f32_16x16x32_bf16 v[176:179], a[88:91], a[56:59], v[176:179]\n"
      "v_mfma_f32_16x16x32_bf16 v[184:187], a[88:91], a[60:63], v[184:187]\n"
      "v_mfma_f32_16x16x32_bf16 v[164:167], a[92:95], a[48:51], v[164:167]\n"
      "v_mfma_f32_16x16x32_bf16 v[172:175], a[92:95], a[52:55], v[172:175]\n"
      "v_mfma_f32_16x16x32_bf16 v[180:183], a[92:95], a[56:59], v[180:183]\n"
      "v_mfma_f32_16x16x32_bf16 v[188:191], a[92:95], a[60:63], v[188:191]\n"
      "s_nop 7\n"
      "v_max3_f32 v112, v160, v161, v162\n"
      "v_max3_f32 v113, v168, v169, v170\n"
      "v_max3_f32 v114, v176, v177, v178\n"
      "v_max3_f32 v115, v184, v185, v186\n"
      "v_max3_f32 v112, v112, v163, v164\n"
      "v_max3_f32 v113, v113, v171, v172\n"
      "v_max3_f32 v114, v114, v179, v180\n"
      "v_max3_f32 v115, v115, v187, v188\n"
      "v_max3_f32 v112, v112, v165, v166\n"
      "v_max3_f32 v113, v113, v173, v174\n"
      "v_max3_f32 v114, v114, v181, v182\n"
      "v_max3_f32 v115, v115, v189, v190\n"
      "v_max_f32 v112, v112, v167\n"
      "v_max_f32 v113, v113, v175\n"
      "v_max_f32 v114, v114, v183\n"
      "v_max_f32 v115, v115, v191\n"
      "v_mov_b32 v128, v112\n"
      "v_mov_b32 v129, v113\n"
      "v_mov_b32 v130, v114\n"
      "v_mov_b32 v131, v115\n"
      "v_permlane16_swap_b32 v112, v128\n"
      "v_permlane16_swap_b32 v113, v129\n"
      "v_permlane16_swap_b32 v114, v130\n"
      "v_permlane16_swap_b32 v115, v131\n"
      "v_max_f32 v112, v112, v128\n"
      "v_max_f32 v113, v113, v129\n"
      "v_max_f32 v114, v114, v130\n"
      "v_max_f32 v115, v115, v131\n"
      "v_mov_b32 v128, v112\n"
      "v_mov_b32 v129, v113\n"
      "v_mov_b32 v130, v114\n"
      "v_mov_b32 v131, v115\n"
      "v_permlane32_swap_b32 v112, v128\n"
      "v_permlane32_swap_b32 v113, v129\n"
      "v_permlane32_swap_b32 v114, v130\n"
      "v_permlane32_swap_b32 v115, v131\n"
      "v_max_f32 v112, v112, v128\n"
      "v_max_f32 v113, v113, v129\n"
      "v_max_f32 v114, v114, v130\n"
      "v_max_f32 v115, v115, v131\n"
      "v_sub_f32 v112, 0xc1800000, v112\n"
      "v_sub_f32 v113, 0xc1800000, v113\n"
      "v_sub_f32 v114, 0xc1800000, v114\n"
      "v_sub_f32 v115, 0xc1800000, v115\n"
      "v_floor_f32 v112, v112\n"
      "v_floor_f32 v113, v113\n"
      "v_floor_f32 v114, v114\n"
      "v_floor_f32 v115, v115\n"
      "v_min_f32 v112, v112, v224\n"
      "v_min_f32 v113, v113, v228\n"
      "v_min_f32 v114, v114, v232\n"
      "v_min_f32 v115, v115, v236\n"
      "v_sub_f32 v132, v112, v224\n"
      "v_sub_f32 v133, v113, v228\n"
      "v_sub_f32 v134, v114, v232\n"
      "v_sub_f32 v135, v115, v236\n"
      "v_mov_b32 v224, v112\n"
      "v_mov_b32 v228, v113\n"
      "v_mov_b32 v232, v114\n"
      "v_mov_b32 v236, v115\n"
      "v_exp_f32 v112, v132\n"
      "v_exp_f32 v113, v133\n"
      "v_exp_f32 v114, v134\n"
      "v_exp_f32 v115, v135\n"
      "v_mov_b32 v225, v224\n"
      "v_mov_b32 v229, v228\n"
      "v_mov_b32 v233, v232\n"
      "v_mov_b32 v237, v236\n"
      "v_mov_b64 v[226:227], v[224:225]\n"
      "v_mov_b64 v[230:231], v[228:229]\n"
      "v_mov_b64 v[234:235], v[232:233]\n"
      "v_mov_b64 v[238:239], v[236:237]\n"
      "s_call_b64 s[84:85], SCALE_%=\n"
      "v_pk_add_f32 v[160:161], v[160:161], v[224:225]\n"
      "v_pk_add_f32 v[162:163], v[162:163], v[224:225]\n"
      "v_pk_add_f32 v[164:165], v[164:165], v[224:225]\n"
      "v_pk_add_f32 v[166:167], v[166:167], v[224:225]\n"
      "v_pk_add_f32 v[168:169], v[168:169], v[228:229]\n"
      "v_pk_add_f32 v[170:171], v[170:171], v[228:229]\n"
      "v_pk_add_f32 v[172:173], v[172:173], v[228:229]\n"
      "v_pk_add_f32 v[174:175], v[174:175], v[228:229]\n"
      "v_pk_add_f32 v[176:177], v[176:177], v[232:233]\n"
      "v_pk_add_f32 v[178:179], v[178:179], v[232:233]\n"
      "v_pk_add_f32 v[180:181], v[180:181], v[232:233]\n"
      "v_pk_add_f32 v[182:183], v[182:183], v[232:233]\n"
      "v_pk_add_f32 v[184:185], v[184:185], v[236:237]\n"
      "v_pk_add_f32 v[186:187], v[186:187], v[236:237]\n"
      "v_pk_add_f32 v[188:189], v[188:189], v[236:237]\n"
      "v_pk_add_f32 v[190:191], v[190:191], v[236:237]\n"
      "v_exp_f32 v112, v160\n"
      "v_exp_f32 v113, v161\n"
      "v_exp_f32 v114, v162\n"
      "v_cvt_pk_bf16_f32 v160, v112, v113\n"
      "v_exp_f32 v115, v163\n"
      "v_exp_f32 v112, v164\n"
      "v_cvt_pk_bf16_f32 v161, v114, v115\n"
      "v_exp_f32 v113, v165\n"
      "v_exp_f32 v114, v166\n"
      "v_cvt_pk_bf16_f32 v162, v112, v113\n"
      "v_exp_f32 v115, v167\n"
      "v_exp_f32 v112, v168\n"
      "v_cvt_pk_bf16_f32 v163, v114, v115\n"
      "v_exp_f32 v113, v169\n"
      "v_exp_f32 v114, v170\n"
      "v_cvt_pk_bf16_f32 v164, v112, v113\n"
      "v_exp_f32 v115, v171\n"
      "v_exp_f32 v112, v172\n"
      "v_cvt_pk_bf16_f32 v165, v114, v115\n"
      "v_exp_f32 v113, v173\n"
      "v_exp_f32 v114, v174\n"
      "v_cvt_pk_bf16_f32 v166, v112, v113\n"
      "v_exp_f32 v115, v175\n"
      "v_exp_f32 v112, v176\n"
      "v_cvt_pk_bf16_f32 v167, v114, v115\n"
      "v_exp_f32 v113, v177\n"
      "v_exp_f32 v114, v178\n"
      "v_cvt_pk_bf16_f32 v168, v112, v113\n"
      "v_exp_f32 v115, v179\n"
      "v_exp_f32 v112, v180\n"
      "v_cvt_pk_bf16_f32 v169, v114, v115\n"
      "v_exp_f32 v113, v181\n"
      "v_exp_f32 v114, v182\n"
      "v_cvt_pk_bf16_f32 v170, v112, v113\n"
      "v_exp_f32 v115, v183\n"
      "v_exp_f32 v112, v184\n"
      "v_cvt_pk_bf16_f32 v171, v114, v115\n"
      "v_exp_f32 v113, v185\n"
      "v_exp_f32 v114, v186\n"
      "v_cvt_pk_bf16_f32 v172, v112, v113\n"
      "v_exp_f32 v115, v187\n"
      "v_exp_f32 v112, v188\n"
      "v_cvt_pk_bf16_f32 v173, v114, v115\n"
      "v_exp_f32 v113, v189\n"
      "v_exp_f32 v114, v190\n"
      "v_cvt_pk_bf16_f32 v174, v112, v113\n"
      "v_exp_f32 v115, v191\n"
      "s_nop 0\n"
      "v_cvt_pk_bf16_f32 v175, v114, v115\n"
      "ds_read_b128 a[64:67], v242 offset:0\n"
      "ds_read_b128 a[68:71], v242 offset:4096\n"
      "ds_read_b128 a[72:75], v243 offset:0\n"
      "ds_read_b128 a[76:79], v243 offset:4096\n"
      "ds_read_b128 a[80:83], v242 offset:1024\n"
      "ds_read_b128 a[84:87], v242 offset:5120\n"
      "ds_read_b128 a[88:91], v243 offset:1024\n"
      "ds_read_b128 a[92:95], v243 offset:5120\n"
      "s_waitcnt lgkmcnt(0)\n"
      "v_mfma_f32_16x16x32_bf16 v[128:131], a[64:67], a[0:3], v[224:227]\n"
      "v_mfma_f32_16x16x32_bf16 v[136:139], a[64:67], a[4:7], v[228:231]\n"
      "v_mfma_f32_16x16x32_bf16 v[144:147], a[64:67], a[8:11], v[232:235]\n"
      "v_mfma_f32_16x16x32_bf16 v[152:155], a[64:67], a[12:15], v[236:239]\n"
      "v_mfma_f32_16x16x32_bf16 v[132:135], a[68:71], a[0:3], v[224:227]\n"
      "v_mfma_f32_16x16x32_bf16 v[140:143], a[68:71], a[4:7], v[228:231]\n"
      "v_mfma_f32_16x16x32_bf16 v[148:151], a[68:71], a[8:11], v[232:235]\n"
      "v_mfma_f32_16x16x32_bf16 v[156:159], a[68:71], a[12:15], v[236:239]\n"
      "v_mfma_f32_16x16x32_bf16 v[128:131], a[72:75], a[16:19], v[128:131]\n"
      "v_mfma_f32_16x16x32_bf16 v[136:139], a[72:75], a[20:23], v[136:139]\n"
      "v_mfma_f32_16x16x32_bf16 v[144:147], a[72:75], a[24:27], v[144:147]\n"
      "v_mfma_f32_16x16x32_bf16 v[152:155], a[72:75], a[28:31], v[152:155]\n"
      "v_mfma_f32_16x16x32_bf16 v[132:135], a[76:79], a[16:19], v[132:135]\n"
      "v_mfma_f32_16x16x32_bf16 v[140:143], a[76:79], a[20:23], v[140:143]\n"
      "v_mfma_f32_16x16x32_bf16 v[148:151], a[76:79], a[24:27], v[148:151]\n"
      "v_mfma_f32_16x16x32_bf16 v[156:159], a[76:79], a[28:31], v[156:159]\n"
      "v_mfma_f32_16x16x32_bf16 v[128:131], a[80:83], a[32:35], v[128:131]\n"
      "v_mfma_f32_16x16x32_bf16 v[136:139], a[80:83], a[36:39], v[136:139]\n"
      "v_mfma_f32_16x16x32_bf16 v[144:147], a[80:83], a[40:43], v[144:147]\n"
      "v_mfma_f32_16x16x32_bf16 v[152:155], a[80:83], a[44:47], v[152:155]\n"
      "v_mfma_f32_16x16x32_bf16 v[132:135], a[84:87], a[32:35], v[132:135]\n"
      "v_mfma_f32_16x16x32_bf16 v[140:143], a[84:87], a[36:39], v[140:143]\n"
      "v_mfma_f32_16x16x32_bf16 v[148:151], a[84:87], a[40:43], v[148:151]\n"
      "v_mfma_f32_16x16x32_bf16 v[156:159], a[84:87], a[44:47], v[156:159]\n"
      "v_mfma_f32_16x16x32_bf16 v[128:131], a[88:91], a[48:51], v[128:131]\n"
      "v_mfma_f32_16x16x32_bf16 v[136:139], a[88:91], a[52:55], v[136:139]\n"
      "v_mfma_f32_16x16x32_bf16 v[144:147], a[88:91], a[56:59], v[144:147]\n"
      "v_mfma_f32_16x16x32_bf16 v[152:155], a[88:91], a[60:63], v[152:155]\n"
      "v_mfma_f32_16x16x32_bf16 v[132:135], a[92:95], a[48:51], v[132:135]\n"
      "v_mfma_f32_16x16x32_bf16 v[140:143], a[92:95], a[52:55], v[140:143]\n"
      "v_mfma_f32_16x16x32_bf16 v[148:151], a[92:95], a[56:59], v[148:151]\n"
      "v_mfma_f32_16x16x32_bf16 v[156:159], a[92:95], a[60:63], v[156:159]\n"
      "s_cmp_eq_u32 s64, 0\n"
      "s_cbranch_scc0 NRET1_0_%=\n"
      "v_mov_b64 v[208:209], v[160:161]\n"
      "v_mov_b64 v[210:211], v[162:163]\n"
      "v_mov_b64 v[212:213], v[164:165]\n"
      "v_mov_b64 v[214:215], v[166:167]\n"
      "v_mov_b64 v[216:217], v[168:169]\n"
      "v_mov_b64 v[218:219], v[170:171]\n"
      "v_mov_b64 v[220:221], v[172:173]\n"
      "v_mov_b64 v[222:223], v[174:175]\n"
      "s_nop 1\n"
      "s_branch RET1_0_%=\n"
      "NRET1_0_%=:\n"
      "s_cmp_eq_u32 s64, 1\n"
      "s_cbranch_scc0 NRET1_1_%=\n"
      "v_mov_b64 v[208:209], v[160:161]\n"
      "v_mov_b64 v[210:211], v[162:163]\n"
      "v_mov_b64 v[212:213], v[164:165]\n"
      "v_mov_b64 v[214:215], v[166:167]\n"
      "v_mov_b64 v[216:217], v[168:169]\n"
      "v_mov_b64 v[218:219], v[170:171]\n"
      "v_mov_b64 v[220:221], v[172:173]\n"
      "v_mov_b64 v[222:223], v[174:175]\n"
      "s_nop 1\n"
      "s_branch RET1_1_%=\n"
      "NRET1_1_%=:\n"
      "s_cmp_eq_u32 s64, 2\n"
      "s_cbranch_scc0 NRET1_2_%=\n"
      "v_mov_b64 v[208:209], v[160:161]\n"
      "v_mov_b64 v[210:211], v[162:163]\n"
      "v_mov_b64 v[212:213], v[164:165]\n"
      "v_mov_b64 v[214:215], v[166:167]\n"
      "v_mov_b64 v[216:217], v[168:169]\n"
      "v_mov_b64 v[218:219], v[170:171]\n"
      "v_mov_b64 v[220:221], v[172:173]\n"
      "v_mov_b64 v[222:223], v[174:175]\n"
      "s_nop 1\n"
      "s_branch RET1_2_%=\n"
      "NRET1_2_%=:\n"
      "s_cmp_eq_u32 s64, 3\n"
      "s_cbranch_scc0 NRET1_3_%=\n"
      "v_mov_b64 v[208:209], v[160:161]\n"
      "v_mov_b64 v[210:211], v[162:163]\n"
      "v_mov_b64 v[212:213], v[164:165]\n"
      "v_mov_b64 v[214:215], v[166:167]\n"
      "v_mov_b64 v[216:217], v[168:169]\n"
      "v_mov_b64 v[218:219], v[170:171]\n"
      "v_mov_b64 v[220:221], v[172:173]\n"
      "v_mov_b64 v[222:223], v[174:175]\n"
      "s_nop 1\n"
      "s_branch RET1_3_%=\n"
      "NRET1_3_%=:\n"
      "s_cmp_eq_u32 s64, 4\n"
      "s_cbranch_scc0 NRET1_4_%=\n"
      "v_mov_b64 v[208:209], v[160:161]\n"
      "v_mov_b64 v[210:211], v[162:163]\n"
      "v_mov_b64 v[212:213], v[164:165]\n"
      "v_mov_b64 v[214:215], v[166:167]\n"
      "v_mov_b64 v[216:217], v[168:169]\n"
      "v_mov_b64 v[218:219], v[170:171]\n"
      "v_mov_b64 v[220:221], v[172:173]\n"
      "v_mov_b64 v[222:223], v[174:175]\n"
      "s_nop 1\n"
      "s_branch RET1_4_%=\n"
      "NRET1_4_%=:\n"
      "s_cmp_eq_u32 s64, 5\n"
      "s_cbranch_scc0 NRET1_5_%=\n"
      "v_mov_b64 v[0:1], v[160:161]\n"
      "v_mov_b64 v[2:3], v[162:163]\n"
      "v_mov_b64 v[4:5], v[164:165]\n"
      "v_mov_b64 v[6:7], v[166:167]\n"
      "v_mov_b64 v[8:9], v[168:169]\n"
      "v_mov_b64 v[10:11], v[170:171]\n"
      "v_mov_b64 v[12:13], v[172:173]\n"
      "v_mov_b64 v[14:15], v[174:175]\n"
      "s_nop 1\n"
      "s_branch RET1_5_%=\n"
      "NRET1_5_%=:\n"
      "s_cmp_eq_u32 s64, 6\n"
      "s_cbranch_scc0 NRET1_6_%=\n"
      "v_mov_b64 v[96:97], v[160:161]\n"
      "v_mov_b64 v[98:99], v[162:163]\n"
      "v_mov_b64 v[100:101], v[164:165]\n"
      "v_mov_b64 v[102:103], v[166:167]\n"
      "v_mov_b64 v[104:105], v[168:169]\n"
      "v_mov_b64 v[106:107], v[170:171]\n"
      "v_mov_b64 v[108:109], v[172:173]\n"
      "v_mov_b64 v[110:111], v[174:175]\n"
      "v_mov_b32 v117, v224\n"
      "s_nop 1\n"
      "v_mov_b32_dpp v117, v228 quad_perm:[0,1,2,3] row_mask:0x2 bank_mask:0xf\n"
      "s_nop 1\n"
      "v_mov_b32_dpp v117, v232 quad_perm:[0,1,2,3] row_mask:0x4 bank_mask:0xf\n"
      "s_nop 1\n"
      "v_mov_b32_dpp v117, v236 quad_perm:[0,1,2,3] row_mask:0x8 bank_mask:0xf\n"
      "v_mfma_f32_16x16x32_bf16 a[228:231], a[196:199], v[96:99], a[192:195]\n"
      "v_mfma_f32_16x16x32_bf16 a[228:231], a[200:203], v[100:103], a[228:231]\n"
      "v_mfma_f32_16x16x32_bf16 a[228:231], a[204:207], v[104:107], a[228:231]\n"
      "ds_write_b128 v125, a[160:163] offset:0\n"
      "ds_write_b128 v125, a[164:167] offset:1024\n"
      "ds_write_b128 v125, a[168:171] offset:2048\n"
      "ds_write_b128 v125, a[172:175] offset:3072\n"
      "ds_write_b128 v125, a[176:179] offset:4096\n"
      "ds_write_b128 v125, a[180:183] offset:5120\n"
      "ds_write_b128 v125, a[184:187] offset:6144\n"
      "ds_write_b128 v125, a[188:191] offset:7168\n"
      "ds_write_b128 v125, v[96:99] offset:8192\n"
      "ds_write_b128 v125, v[100:103] offset:9216\n"
      "ds_write_b128 v125, v[104:107] offset:10240\n"
      "ds_write2_b32 v124, v117, v123 offset0:1 offset1:2\n"
      "s_nop 1\n"
      "s_branch RET1_6_%=\n"
      "NRET1_6_%=:\n"
      "SCALE_%=:\n"
      "v_pk_mul_f32 v[32:33], v[32:33], v[112:113] op_sel:[0,0] op_sel_hi:[1,0]\n"
      "v_pk_mul_f32 v[34:35], v[34:35], v[112:113] op_sel:[0,0] op_sel_hi:[1,0]\n"
      "v_pk_mul_f32 v[36:37], v[36:37], v[112:113] op_sel:[0,1] op_sel_hi:[1,1]\n"
      "v_pk_mul_f32 v[38:39], v[38:39], v[112:113] op_sel:[0,1] op_sel_hi:[1,1]\n"
      "v_pk_mul_f32 v[40:41], v[40:41], v[114:115] op_sel:[0,0] op_sel_hi:[1,0]\n"
      "v_pk_mul_f32 v[42:43], v[42:43], v[114:115] op_sel:[0,0] op_sel_hi:[1,0]\n"
      "v_pk_mul_f32 v[44:45], v[44:45], v[114:115] op_sel:[0,1] op_sel_hi:[1,1]\n"
      "v_pk_mul_f32 v[46:47], v[46:47], v[114:115] op_sel:[0,1] op_sel_hi:[1,1]\n"
      "v_pk_mul_f32 v[48:49], v[48:49], v[112:113] op_sel:[0,0] op_sel_hi:[1,0]\n"
      "v_pk_mul_f32 v[50:51], v[50:51], v[112:113] op_sel:[0,0] op_sel_hi:[1,0]\n"
      "v_pk_mul_f32 v[52:53], v[52:53], v[112:113] op_sel:[0,1] op_sel_hi:[1,1]\n"
      "v_pk_mul_f32 v[54:55], v[54:55], v[112:113] op_sel:[0,1] op_sel_hi:[1,1]\n"
      "v_pk_mul_f32 v[56:57], v[56:57], v[114:115] op_sel:[0,0] op_sel_hi:[1,0]\n"
      "v_pk_mul_f32 v[58:59], v[58:59], v[114:115] op_sel:[0,0] op_sel_hi:[1,0]\n"
      "v_pk_mul_f32 v[60:61], v[60:61], v[114:115] op_sel:[0,1] op_sel_hi:[1,1]\n"
      "v_pk_mul_f32 v[62:63], v[62:63], v[114:115] op_sel:[0,1] op_sel_hi:[1,1]\n"
      "v_pk_mul_f32 v[64:65], v[64:65], v[112:113] op_sel:[0,0] op_sel_hi:[1,0]\n"
      "v_pk_mul_f32 v[66:67], v[66:67], v[112:113] op_sel:[0,0] op_sel_hi:[1,0]\n"
      "v_pk_mul_f32 v[68:69], v[68:69], v[112:113] op_sel:[0,1] op_sel_hi:[1,1]\n"
      "v_pk_mul_f32 v[70:71], v[70:71], v[112:113] op_sel:[0,1] op_sel_hi:[1,1]\n"
      "v_pk_mul_f32 v[72:73], v[72:73], v[114:115] op_sel:[0,0] op_sel_hi:[1,0]\n"
      "v_pk_mul_f32 v[74:75], v[74:75], v[114:115] op_sel:[0,0] op_sel_hi:[1,0]\n"
      "v_pk_mul_f32 v[76:77], v[76:77], v[114:115] op_sel:[0,1] op_sel_hi:[1,1]\n"
      "v_pk_mul_f32 v[78:79], v[78:79], v[114:115] op_sel:[0,1] op_sel_hi:[1,1]\n"
      "v_pk_mul_f32 v[80:81], v[80:81], v[112:113] op_sel:[0,0] op_sel_hi:[1,0]\n"
      "v_pk_mul_f32 v[82:83], v[82:83], v[112:113] op_sel:[0,0] op_sel_hi:[1,0]\n"
      "v_pk_mul_f32 v[84:85], v[84:85], v[112:113] op_sel:[0,1] op_sel_hi:[1,1]\n"
      "v_pk_mul_f32 v[86:87], v[86:87], v[112:113] op_sel:[0,1] op_sel_hi:[1,1]\n"
      "v_pk_mul_f32 v[88:89], v[88:89], v[114:115] op_sel:[0,0] op_sel_hi:[1,0]\n"
      "v_pk_mul_f32 v[90:91], v[90:91], v[114:115] op_sel:[0,0] op_sel_hi:[1,0]\n"
      "v_pk_mul_f32 v[92:93], v[92:93], v[114:115] op_sel:[0,1] op_sel_hi:[1,1]\n"
      "v_pk_mul_f32 v[94:95], v[94:95], v[114:115] op_sel:[0,1] op_sel_hi:[1,1]\n"
      "v_accvgpr_read_b32 v118, a128\n"
      "v_accvgpr_read_b32 v119, a129\n"
      "v_pk_mul_f32 v[118:119], v[118:119], v[112:113] op_sel:[0,0] op_sel_hi:[1,0]\n"
      "v_accvgpr_write_b32 a128, v118\n"
      "v_accvgpr_write_b32 a129, v119\n"
      "v_accvgpr_read_b32 v118, a130\n"
      "v_accvgpr_read_b32 v119, a131\n"
      "v_pk_mul_f32 v[118:119], v[118:119], v[112:113] op_sel:[0,0] op_sel_hi:[1,0]\n"
      "v_accvgpr_write_b32 a130, v118\n"
      "v_accvgpr_write_b32 a131, v119\n"
      "v_accvgpr_read_b32 v118, a132\n"
      "v_accvgpr_read_b32 v119, a133\n"
      "v_pk_mul_f32 v[118:119], v[118:119], v[112:113] op_sel:[0,1] op_sel_hi:[1,1]\n"
      "v_accvgpr_write_b32 a132, v118\n"
      "v_accvgpr_write_b32 a133, v119\n"
      "v_accvgpr_read_b32 v118, a134\n"
      "v_accvgpr_read_b32 v119, a135\n"
      "v_pk_mul_f32 v[118:119], v[118:119], v[112:113] op_sel:[0,1] op_sel_hi:[1,1]\n"
      "v_accvgpr_write_b32 a134, v118\n"
      "v_accvgpr_write_b32 a135, v119\n"
      "v_accvgpr_read_b32 v118, a136\n"
      "v_accvgpr_read_b32 v119, a137\n"
      "v_pk_mul_f32 v[118:119], v[118:119], v[114:115] op_sel:[0,0] op_sel_hi:[1,0]\n"
      "v_accvgpr_write_b32 a136, v118\n"
      "v_accvgpr_write_b32 a137, v119\n"
      "v_accvgpr_read_b32 v118, a138\n"
      "v_accvgpr_read_b32 v119, a139\n"
      "v_pk_mul_f32 v[118:119], v[118:119], v[114:115] op_sel:[0,0] op_sel_hi:[1,0]\n"
      "v_accvgpr_write_b32 a138, v118\n"
      "v_accvgpr_write_b32 a139, v119\n"
      "v_accvgpr_read_b32 v118, a140\n"
      "v_accvgpr_read_b32 v119, a141\n"
      "v_pk_mul_f32 v[118:119], v[118:119], v[114:115] op_sel:[0,1] op_sel_hi:[1,1]\n"
      "v_accvgpr_write_b32 a140, v118\n"
      "v_accvgpr_write_b32 a141, v119\n"
      "v_accvgpr_read_b32 v118, a142\n"
      "v_accvgpr_read_b32 v119, a143\n"
      "v_pk_mul_f32 v[118:119], v[118:119], v[114:115] op_sel:[0,1] op_sel_hi:[1,1]\n"
      "v_accvgpr_write_b32 a142, v118\n"
      "v_accvgpr_write_b32 a143, v119\n"
      "v_accvgpr_read_b32 v118, a144\n"
      "v_accvgpr_read_b32 v119, a145\n"
      "v_pk_mul_f32 v[118:119], v[118:119], v[112:113] op_sel:[0,0] op_sel_hi:[1,0]\n"
      "v_accvgpr_write_b32 a144, v118\n"
      "v_accvgpr_write_b32 a145, v119\n"
      "v_accvgpr_read_b32 v118, a146\n"
      "v_accvgpr_read_b32 v119, a147\n"
      "v_pk_mul_f32 v[118:119], v[118:119], v[112:113] op_sel:[0,0] op_sel_hi:[1,0]\n"
      "v_accvgpr_write_b32 a146, v118\n"
      "v_accvgpr_write_b32 a147, v119\n"
      "v_accvgpr_read_b32 v118, a148\n"
      "v_accvgpr_read_b32 v119, a149\n"
      "v_pk_mul_f32 v[118:119], v[118:119], v[112:113] op_sel:[0,1] op_sel_hi:[1,1]\n"
      "v_accvgpr_write_b32 a148, v118\n"
      "v_accvgpr_write_b32 a149, v119\n"
      "v_accvgpr_read_b32 v118, a150\n"
      "v_accvgpr_read_b32 v119, a151\n"
      "v_pk_mul_f32 v[118:119], v[118:119], v[112:113] op_sel:[0,1] op_sel_hi:[1,1]\n"
      "v_accvgpr_write_b32 a150, v118\n"
      "v_accvgpr_write_b32 a151, v119\n"
      "v_accvgpr_read_b32 v118, a152\n"
      "v_accvgpr_read_b32 v119, a153\n"
      "v_pk_mul_f32 v[118:119], v[118:119], v[114:115] op_sel:[0,0] op_sel_hi:[1,0]\n"
      "v_accvgpr_write_b32 a152, v118\n"
      "v_accvgpr_write_b32 a153, v119\n"
      "v_accvgpr_read_b32 v118, a154\n"
      "v_accvgpr_read_b32 v119, a155\n"
      "v_pk_mul_f32 v[118:119], v[118:119], v[114:115] op_sel:[0,0] op_sel_hi:[1,0]\n"
      "v_accvgpr_write_b32 a154, v118\n"
      "v_accvgpr_write_b32 a155, v119\n"
      "v_accvgpr_read_b32 v118, a156\n"
      "v_accvgpr_read_b32 v119, a157\n"
      "v_pk_mul_f32 v[118:119], v[118:119], v[114:115] op_sel:[0,1] op_sel_hi:[1,1]\n"
      "v_accvgpr_write_b32 a156, v118\n"
      "v_accvgpr_write_b32 a157, v119\n"
      "v_accvgpr_read_b32 v118, a158\n"
      "v_accvgpr_read_b32 v119, a159\n"
      "v_pk_mul_f32 v[118:119], v[118:119], v[114:115] op_sel:[0,1] op_sel_hi:[1,1]\n"
      "v_accvgpr_write_b32 a158, v118\n"
      "v_accvgpr_write_b32 a159, v119\n"
      "v_accvgpr_read_b32 v118, a160\n"
      "v_accvgpr_read_b32 v119, a161\n"
      "v_pk_mul_f32 v[118:119], v[118:119], v[112:113] op_sel:[0,0] op_sel_hi:[1,0]\n"
      "v_accvgpr_write_b32 a160, v118\n"
      "v_accvgpr_write_b32 a161, v119\n"
      "v_accvgpr_read_b32 v118, a162\n"
      "v_accvgpr_read_b32 v119, a163\n"
      "v_pk_mul_f32 v[118:119], v[118:119], v[112:113] op_sel:[0,0] op_sel_hi:[1,0]\n"
      "v_accvgpr_write_b32 a162, v118\n"
      "v_accvgpr_write_b32 a163, v119\n"
      "v_accvgpr_read_b32 v118, a164\n"
      "v_accvgpr_read_b32 v119, a165\n"
      "v_pk_mul_f32 v[118:119], v[118:119], v[112:113] op_sel:[0,1] op_sel_hi:[1,1]\n"
      "v_accvgpr_write_b32 a164, v118\n"
      "v_accvgpr_write_b32 a165, v119\n"
      "v_accvgpr_read_b32 v118, a166\n"
      "v_accvgpr_read_b32 v119, a167\n"
      "v_pk_mul_f32 v[118:119], v[118:119], v[112:113] op_sel:[0,1] op_sel_hi:[1,1]\n"
      "v_accvgpr_write_b32 a166, v118\n"
      "v_accvgpr_write_b32 a167, v119\n"
      "v_accvgpr_read_b32 v118, a168\n"
      "v_accvgpr_read_b32 v119, a169\n"
      "v_pk_mul_f32 v[118:119], v[118:119], v[114:115] op_sel:[0,0] op_sel_hi:[1,0]\n"
      "v_accvgpr_write_b32 a168, v118\n"
      "v_accvgpr_write_b32 a169, v119\n"
      "v_accvgpr_read_b32 v118, a170\n"
      "v_accvgpr_read_b32 v119, a171\n"
      "v_pk_mul_f32 v[118:119], v[118:119], v[114:115] op_sel:[0,0] op_sel_hi:[1,0]\n"
      "v_accvgpr_write_b32 a170, v118\n"
      "v_accvgpr_write_b32 a171, v119\n"
      "v_accvgpr_read_b32 v118, a172\n"
      "v_accvgpr_read_b32 v119, a173\n"
      "v_pk_mul_f32 v[118:119], v[118:119], v[114:115] op_sel:[0,1] op_sel_hi:[1,1]\n"
      "v_accvgpr_write_b32 a172, v118\n"
      "v_accvgpr_write_b32 a173, v119\n"
      "v_accvgpr_read_b32 v118, a174\n"
      "v_accvgpr_read_b32 v119, a175\n"
      "v_pk_mul_f32 v[118:119], v[118:119], v[114:115] op_sel:[0,1] op_sel_hi:[1,1]\n"
      "v_accvgpr_write_b32 a174, v118\n"
      "v_accvgpr_write_b32 a175, v119\n"
      "v_accvgpr_read_b32 v118, a176\n"
      "v_accvgpr_read_b32 v119, a177\n"
      "v_pk_mul_f32 v[118:119], v[118:119], v[112:113] op_sel:[0,0] op_sel_hi:[1,0]\n"
      "v_accvgpr_write_b32 a176, v118\n"
      "v_accvgpr_write_b32 a177, v119\n"
      "v_accvgpr_read_b32 v118, a178\n"
      "v_accvgpr_read_b32 v119, a179\n"
      "v_pk_mul_f32 v[118:119], v[118:119], v[112:113] op_sel:[0,0] op_sel_hi:[1,0]\n"
      "v_accvgpr_write_b32 a178, v118\n"
      "v_accvgpr_write_b32 a179, v119\n"
      "v_accvgpr_read_b32 v118, a180\n"
      "v_accvgpr_read_b32 v119, a181\n"
      "v_pk_mul_f32 v[118:119], v[118:119], v[112:113] op_sel:[0,1] op_sel_hi:[1,1]\n"
      "v_accvgpr_write_b32 a180, v118\n"
      "v_accvgpr_write_b32 a181, v119\n"
      "v_accvgpr_read_b32 v118, a182\n"
      "v_accvgpr_read_b32 v119, a183\n"
      "v_pk_mul_f32 v[118:119], v[118:119], v[112:113] op_sel:[0,1] op_sel_hi:[1,1]\n"
      "v_accvgpr_write_b32 a182, v118\n"
      "v_accvgpr_write_b32 a183, v119\n"
      "v_accvgpr_read_b32 v118, a184\n"
      "v_accvgpr_read_b32 v119, a185\n"
      "v_pk_mul_f32 v[118:119], v[118:119], v[114:115] op_sel:[0,0] op_sel_hi:[1,0]\n"
      "v_accvgpr_write_b32 a184, v118\n"
      "v_accvgpr_write_b32 a185, v119\n"
      "v_accvgpr_read_b32 v118, a186\n"
      "v_accvgpr_read_b32 v119, a187\n"
      "v_pk_mul_f32 v[118:119], v[118:119], v[114:115] op_sel:[0,0] op_sel_hi:[1,0]\n"
      "v_accvgpr_write_b32 a186, v118\n"
      "v_accvgpr_write_b32 a187, v119\n"
      "v_accvgpr_read_b32 v118, a188\n"
      "v_accvgpr_read_b32 v119, a189\n"
      "v_pk_mul_f32 v[118:119], v[118:119], v[114:115] op_sel:[0,1] op_sel_hi:[1,1]\n"
      "v_accvgpr_write_b32 a188, v118\n"
      "v_accvgpr_write_b32 a189, v119\n"
      "v_accvgpr_read_b32 v118, a190\n"
      "v_accvgpr_read_b32 v119, a191\n"
      "v_pk_mul_f32 v[118:119], v[118:119], v[114:115] op_sel:[0,1] op_sel_hi:[1,1]\n"
      "v_accvgpr_write_b32 a190, v118\n"
      "v_accvgpr_write_b32 a191, v119\n"
      "v_mov_b32 v118, v112\n"
      "s_nop 1\n"
      "v_mov_b32_dpp v118, v113 quad_perm:[0,1,2,3] row_mask:0x2 bank_mask:0xf\n"
      "s_nop 1\n"
      "v_mov_b32_dpp v118, v114 quad_perm:[0,1,2,3] row_mask:0x4 bank_mask:0xf\n"
      "s_nop 1\n"
      "v_mov_b32_dpp v118, v115 quad_perm:[0,1,2,3] row_mask:0x8 bank_mask:0xf\n"
      "v_accvgpr_read_b32 v119, a192\n"
      "v_mul_f32 v118, v118, v119\n"
      "v_accvgpr_write_b32 a192, v118\n"
      "v_lshlrev_b32 v118, 16, v208\n"
      "v_and_b32 v119, 0xffff0000, v208\n"
      "v_pk_mul_f32 v[118:119], v[118:119], v[112:113] op_sel:[0,0] op_sel_hi:[1,0]\n"
      "v_cvt_pk_bf16_f32 v208, v118, v119\n"
      "v_lshlrev_b32 v118, 16, v209\n"
      "v_and_b32 v119, 0xffff0000, v209\n"
      "v_pk_mul_f32 v[118:119], v[118:119], v[112:113] op_sel:[0,0] op_sel_hi:[1,0]\n"
      "v_cvt_pk_bf16_f32 v209, v118, v119\n"
      "v_lshlrev_b32 v118, 16, v210\n"
      "v_and_b32 v119, 0xffff0000, v210\n"
      "v_pk_mul_f32 v[118:119], v[118:119], v[112:113] op_sel:[0,0] op_sel_hi:[1,0]\n"
      "v_cvt_pk_bf16_f32 v210, v118, v119\n"
      "v_lshlrev_b32 v118, 16, v211\n"
      "v_and_b32 v119, 0xffff0000, v211\n"
      "v_pk_mul_f32 v[118:119], v[118:119], v[112:113] op_sel:[0,0] op_sel_hi:[1,0]\n"
      "v_cvt_pk_bf16_f32 v211, v118, v119\n"
      "v_lshlrev_b32 v118, 16, v212\n"
      "v_and_b32 v119, 0xffff0000, v212\n"
      "v_pk_mul_f32 v[118:119], v[118:119], v[112:113] op_sel:[0,1] op_sel_hi:[1,1]\n"
      "v_cvt_pk_bf16_f32 v212, v118, v119\n"
      "v_lshlrev_b32 v118, 16, v213\n"
      "v_and_b32 v119, 0xffff0000, v213\n"
      "v_pk_mul_f32 v[118:119], v[118:119], v[112:113] op_sel:[0,1] op_sel_hi:[1,1]\n"
      "v_cvt_pk_bf16_f32 v213, v118, v119\n"
      "v_lshlrev_b32 v118, 16, v214\n"
      "v_and_b32 v119, 0xffff0000, v214\n"
      "v_pk_mul_f32 v[118:119], v[118:119], v[112:113] op_sel:[0,1] op_sel_hi:[1,1]\n"
      "v_cvt_pk_bf16_f32 v214, v118, v119\n"
      "v_lshlrev_b32 v118, 16, v215\n"
      "v_and_b32 v119, 0xffff0000, v215\n"
      "v_pk_mul_f32 v[118:119], v[118:119], v[112:113] op_sel:[0,1] op_sel_hi:[1,1]\n"
      "v_cvt_pk_bf16_f32 v215, v118, v119\n"
      "v_lshlrev_b32 v118, 16, v216\n"
      "v_and_b32 v119, 0xffff0000, v216\n"
      "v_pk_mul_f32 v[118:119], v[118:119], v[114:115] op_sel:[0,0] op_sel_hi:[1,0]\n"
      "v_cvt_pk_bf16_f32 v216, v118, v119\n"
      "v_lshlrev_b32 v118, 16, v217\n"
      "v_and_b32 v119, 0xffff0000, v217\n"
      "v_pk_mul_f32 v[118:119], v[118:119], v[114:115] op_sel:[0,0] op_sel_hi:[1,0]\n"
      "v_cvt_pk_bf16_f32 v217, v118, v119\n"
      "v_lshlrev_b32 v118, 16, v218\n"
      "v_and_b32 v119, 0xffff0000, v218\n"
      "v_pk_mul_f32 v[118:119], v[118:119], v[114:115] op_sel:[0,0] op_sel_hi:[1,0]\n"
      "v_cvt_pk_bf16_f32 v218, v118, v119\n"
      "v_lshlrev_b32 v118, 16, v219\n"
      "v_and_b32 v119, 0xffff0000, v219\n"
      "v_pk_mul_f32 v[118:119], v[118:119], v[114:115] op_sel:[0,0] op_sel_hi:[1,0]\n"
      "v_cvt_pk_bf16_f32 v219, v118, v119\n"
      "v_lshlrev_b32 v118, 16, v220\n"
      "v_and_b32 v119, 0xffff0000, v220\n"
      "v_pk_mul_f32 v[118:119], v[118:119], v[114:115] op_sel:[0,1] op_sel_hi:[1,1]\n"
      "v_cvt_pk_bf16_f32 v220, v118, v119\n"
      "v_lshlrev_b32 v118, 16, v221\n"
      "v_and_b32 v119, 0xffff0000, v221\n"
      "v_pk_mul_f32 v[118:119], v[118:119], v[114:115] op_sel:[0,1] op_sel_hi:[1,1]\n"
      "v_cvt_pk_bf16_f32 v221, v118, v119\n"
      "v_lshlrev_b32 v118, 16, v222\n"
      "v_and_b32 v119, 0xffff0000, v222\n"
      "v_pk_mul_f32 v[118:119], v[118:119], v[114:115] op_sel:[0,1] op_sel_hi:[1,1]\n"
      "v_cvt_pk_bf16_f32 v222, v118, v119\n"
      "v_lshlrev_b32 v118, 16, v223\n"
      "v_and_b32 v119, 0xffff0000, v223\n"
      "v_pk_mul_f32 v[118:119], v[118:119], v[114:115] op_sel:[0,1] op_sel_hi:[1,1]\n"
      "v_cvt_pk_bf16_f32 v223, v118, v119\n"
      "v_lshlrev_b32 v118, 16, v192\n"
      "v_and_b32 v119, 0xffff0000, v192\n"
      "v_pk_mul_f32 v[118:119], v[118:119], v[112:113] op_sel:[0,0] op_sel_hi:[1,0]\n"
      "v_cvt_pk_bf16_f32 v192, v118, v119\n"
      "v_lshlrev_b32 v118, 16, v193\n"
      "v_and_b32 v119, 0xffff0000, v193\n"
      "v_pk_mul_f32 v[118:119], v[118:119], v[112:113] op_sel:[0,0] op_sel_hi:[1,0]\n"
      "v_cvt_pk_bf16_f32 v193, v118, v119\n"
      "v_lshlrev_b32 v118, 16, v194\n"
      "v_and_b32 v119, 0xffff0000, v194\n"
      "v_pk_mul_f32 v[118:119], v[118:119], v[112:113] op_sel:[0,0] op_sel_hi:[1,0]\n"
      "v_cvt_pk_bf16_f32 v194, v118, v119\n"
      "v_lshlrev_b32 v118, 16, v195\n"
      "v_and_b32 v119, 0xffff0000, v195\n"
      "v_pk_mul_f32 v[118:119], v[118:119], v[112:113] op_sel:[0,0] op_sel_hi:[1,0]\n"
      "v_cvt_pk_bf16_f32 v195, v118, v119\n"
      "v_lshlrev_b32 v118, 16, v196\n"
      "v_and_b32 v119, 0xffff0000, v196\n"
      "v_pk_mul_f32 v[118:119], v[118:119], v[112:113] op_sel:[0,1] op_sel_hi:[1,1]\n"
      "v_cvt_pk_bf16_f32 v196, v118, v119\n"
      "v_lshlrev_b32 v118, 16, v197\n"
      "v_and_b32 v119, 0xffff0000, v197\n"
      "v_pk_mul_f32 v[118:119], v[118:119], v[112:113] op_sel:[0,1] op_sel_hi:[1,1]\n"
      "v_cvt_pk_bf16_f32 v197, v118, v119\n"
      "v_lshlrev_b32 v118, 16, v198\n"
      "v_and_b32 v119, 0xffff0000, v198\n"
      "v_pk_mul_f32 v[118:119], v[118:119], v[112:113] op_sel:[0,1] op_sel_hi:[1,1]\n"
      "v_cvt_pk_bf16_f32 v198, v118, v119\n"
      "v_lshlrev_b32 v118, 16, v199\n"
      "v_and_b32 v119, 0xffff0000, v199\n"
      "v_pk_mul_f32 v[118:119], v[118:119], v[112:113] op_sel:[0,1] op_sel_hi:[1,1]\n"
      "v_cvt_pk_bf16_f32 v199, v118, v119\n"
      "v_lshlrev_b32 v118, 16, v200\n"
      "v_and_b32 v119, 0xffff0000, v200\n"
      "v_pk_mul_f32 v[118:119], v[118:119], v[114:115] op_sel:[0,0] op_sel_hi:[1,0]\n"
      "v_cvt_pk_bf16_f32 v200, v118, v119\n"
      "v_lshlrev_b32 v118, 16, v201\n"
      "v_and_b32 v119, 0xffff0000, v201\n"
      "v_pk_mul_f32 v[118:119], v[118:119], v[114:115] op_sel:[0,0] op_sel_hi:[1,0]\n"
      "v_cvt_pk_bf16_f32 v201, v118, v119\n"
      "v_lshlrev_b32 v118, 16, v202\n"
      "v_and_b32 v119, 0xffff0000, v202\n"
      "v_pk_mul_f32 v[118:119], v[118:119], v[114:115] op_sel:[0,0] op_sel_hi:[1,0]\n"
      "v_cvt_pk_bf16_f32 v202, v118, v119\n"
      "v_lshlrev_b32 v118, 16, v203\n"
      "v_and_b32 v119, 0xffff0000, v203\n"
      "v_pk_mul_f32 v[118:119], v[118:119], v[114:115] op_sel:[0,0] op_sel_hi:[1,0]\n"
      "v_cvt_pk_bf16_f32 v203, v118, v119\n"
      "v_lshlrev_b32 v118, 16, v204\n"
      "v_and_b32 v119, 0xffff0000, v204\n"
      "v_pk_mul_f32 v[118:119], v[118:119], v[114:115] op_sel:[0,1] op_sel_hi:[1,1]\n"
      "v_cvt_pk_bf16_f32 v204, v118, v119\n"
      "v_lshlrev_b32 v118, 16, v205\n"
      "v_and_b32 v119, 0xffff0000, v205\n"
      "v_pk_mul_f32 v[118:119], v[118:119], v[114:115] op_sel:[0,1] op_sel_hi:[1,1]\n"
      "v_cvt_pk_bf16_f32 v205, v118, v119\n"
      "v_lshlrev_b32 v118, 16, v206\n"
      "v_and_b32 v119, 0xffff0000, v206\n"
      "v_pk_mul_f32 v[118:119], v[118:119], v[114:115] op_sel:[0,1] op_sel_hi:[1,1]\n"
      "v_cvt_pk_bf16_f32 v206, v118, v119\n"
      "v_lshlrev_b32 v118, 16, v207\n"
      "v_and_b32 v119, 0xffff0000, v207\n"
      "v_pk_mul_f32 v[118:119], v[118:119], v[114:115] op_sel:[0,1] op_sel_hi:[1,1]\n"
      "v_cvt_pk_bf16_f32 v207, v118, v119\n"
      "v_lshlrev_b32 v118, 16, v0\n"
      "v_and_b32 v119, 0xffff0000, v0\n"
      "v_pk_mul_f32 v[118:119], v[118:119], v[112:113] op_sel:[0,0] op_sel_hi:[1,0]\n"
      "v_cvt_pk_bf16_f32 v0, v118, v119\n"
      "v_lshlrev_b32 v118, 16, v1\n"
      "v_and_b32 v119, 0xffff0000, v1\n"
      "v_pk_mul_f32 v[118:119], v[118:119], v[112:113] op_sel:[0,0] op_sel_hi:[1,0]\n"
      "v_cvt_pk_bf16_f32 v1, v118, v119\n"
      "v_lshlrev_b32 v118, 16, v2\n"
      "v_and_b32 v119, 0xffff0000, v2\n"
      "v_pk_mul_f32 v[118:119], v[118:119], v[112:113] op_sel:[0,0] op_sel_hi:[1,0]\n"
      "v_cvt_pk_bf16_f32 v2, v118, v119\n"
      "v_lshlrev_b32 v118, 16, v3\n"
      "v_and_b32 v119, 0xffff0000, v3\n"
      "v_pk_mul_f32 v[118:119], v[118:119], v[112:113] op_sel:[0,0] op_sel_hi:[1,0]\n"
      "v_cvt_pk_bf16_f32 v3, v118, v119\n"
      "v_lshlrev_b32 v118, 16, v4\n"
      "v_and_b32 v119, 0xffff0000, v4\n"
      "v_pk_mul_f32 v[118:119], v[118:119], v[112:113] op_sel:[0,1] op_sel_hi:[1,1]\n"
      "v_cvt_pk_bf16_f32 v4, v118, v119\n"
      "v_lshlrev_b32 v118, 16, v5\n"
      "v_and_b32 v119, 0xffff0000, v5\n"
      "v_pk_mul_f32 v[118:119], v[118:119], v[112:113] op_sel:[0,1] op_sel_hi:[1,1]\n"
      "v_cvt_pk_bf16_f32 v5, v118, v119\n"
      "v_lshlrev_b32 v118, 16, v6\n"
      "v_and_b32 v119, 0xffff0000, v6\n"
      "v_pk_mul_f32 v[118:119], v[118:119], v[112:113] op_sel:[0,1] op_sel_hi:[1,1]\n"
      "v_cvt_pk_bf16_f32 v6, v118, v119\n"
      "v_lshlrev_b32 v118, 16, v7\n"
      "v_and_b32 v119, 0xffff0000, v7\n"
      "v_pk_mul_f32 v[118:119], v[118:119], v[112:113] op_sel:[0,1] op_sel_hi:[1,1]\n"
      "v_cvt_pk_bf16_f32 v7, v118, v119\n"
      "v_lshlrev_b32 v118, 16, v8\n"
      "v_and_b32 v119, 0xffff0000, v8\n"
      "v_pk_mul_f32 v[118:119], v[118:119], v[114:115] op_sel:[0,0] op_sel_hi:[1,0]\n"
      "v_cvt_pk_bf16_f32 v8, v118, v119\n"
      "v_lshlrev_b32 v118, 16, v9\n"
      "v_and_b32 v119, 0xffff0000, v9\n"
      "v_pk_mul_f32 v[118:119], v[118:119], v[114:115] op_sel:[0,0] op_sel_hi:[1,0]\n"
      "v_cvt_pk_bf16_f32 v9, v118, v119\n"
      "v_lshlrev_b32 v118, 16, v10\n"
      "v_and_b32 v119, 0xffff0000, v10\n"
      "v_pk_mul_f32 v[118:119], v[118:119], v[114:115] op_sel:[0,0] op_sel_hi:[1,0]\n"
      "v_cvt_pk_bf16_f32 v10, v118, v119\n"
      "v_lshlrev_b32 v118, 16, v11\n"
      "v_and_b32 v119, 0xffff0000, v11\n"
      "v_pk_mul_f32 v[118:119], v[118:119], v[114:115] op_sel:[0,0] op_sel_hi:[1,0]\n"
      "v_cvt_pk_bf16_f32 v11, v118, v119\n"
      "v_lshlrev_b32 v118, 16, v12\n"
      "v_and_b32 v119, 0xffff0000, v12\n"
      "v_pk_mul_f32 v[118:119], v[118:119], v[114:115] op_sel:[0,1] op_sel_hi:[1,1]\n"
      "v_cvt_pk_bf16_f32 v12, v118, v119\n"
      "v_lshlrev_b32 v118, 16, v13\n"
      "v_and_b32 v119, 0xffff0000, v13\n"
      "v_pk_mul_f32 v[118:119], v[118:119], v[114:115] op_sel:[0,1] op_sel_hi:[1,1]\n"
      "v_cvt_pk_bf16_f32 v13, v118, v119\n"
      "v_lshlrev_b32 v118, 16, v14\n"
      "v_and_b32 v119, 0xffff0000, v14\n"
      "v_pk_mul_f32 v[118:119], v[118:119], v[114:115] op_sel:[0,1] op_sel_hi:[1,1]\n"
      "v_cvt_pk_bf16_f32 v14, v118, v119\n"
      "v_lshlrev_b32 v118, 16, v15\n"
      "v_and_b32 v119, 0xffff0000, v15\n"
      "v_pk_mul_f32 v[118:119], v[118:119], v[114:115] op_sel:[0,1] op_sel_hi:[1,1]\n"
      "v_cvt_pk_bf16_f32 v15, v118, v119\n"
      "v_lshlrev_b32 v118, 16, v16\n"
      "v_and_b32 v119, 0xffff0000, v16\n"
      "v_pk_mul_f32 v[118:119], v[118:119], v[112:113] op_sel:[0,0] op_sel_hi:[1,0]\n"
      "v_cvt_pk_bf16_f32 v16, v118, v119\n"
      "v_lshlrev_b32 v118, 16, v17\n"
      "v_and_b32 v119, 0xffff0000, v17\n"
      "v_pk_mul_f32 v[118:119], v[118:119], v[112:113] op_sel:[0,0] op_sel_hi:[1,0]\n"
      "v_cvt_pk_bf16_f32 v17, v118, v119\n"
      "v_lshlrev_b32 v118, 16, v18\n"
      "v_and_b32 v119, 0xffff0000, v18\n"
      "v_pk_mul_f32 v[118:119], v[118:119], v[112:113] op_sel:[0,0] op_sel_hi:[1,0]\n"
      "v_cvt_pk_bf16_f32 v18, v118, v119\n"
      "v_lshlrev_b32 v118, 16, v19\n"
      "v_and_b32 v119, 0xffff0000, v19\n"
      "v_pk_mul_f32 v[118:119], v[118:119], v[112:113] op_sel:[0,0] op_sel_hi:[1,0]\n"
      "v_cvt_pk_bf16_f32 v19, v118, v119\n"
      "v_lshlrev_b32 v118, 16, v20\n"
      "v_and_b32 v119, 0xffff0000, v20\n"
      "v_pk_mul_f32 v[118:119], v[118:119], v[112:113] op_sel:[0,1] op_sel_hi:[1,1]\n"
      "v_cvt_pk_bf16_f32 v20, v118, v119\n"
      "v_lshlrev_b32 v118, 16, v21\n"
      "v_and_b32 v119, 0xffff0000, v21\n"
      "v_pk_mul_f32 v[118:119], v[118:119], v[112:113] op_sel:[0,1] op_sel_hi:[1,1]\n"
      "v_cvt_pk_bf16_f32 v21, v118, v119\n"
      "v_lshlrev_b32 v118, 16, v22\n"
      "v_and_b32 v119, 0xffff0000, v22\n"
      "v_pk_mul_f32 v[118:119], v[118:119], v[112:113] op_sel:[0,1] op_sel_hi:[1,1]\n"
      "v_cvt_pk_bf16_f32 v22, v118, v119\n"
      "v_lshlrev_b32 v118, 16, v23\n"
      "v_and_b32 v119, 0xffff0000, v23\n"
      "v_pk_mul_f32 v[118:119], v[118:119], v[112:113] op_sel:[0,1] op_sel_hi:[1,1]\n"
      "v_cvt_pk_bf16_f32 v23, v118, v119\n"
      "v_lshlrev_b32 v118, 16, v24\n"
      "v_and_b32 v119, 0xffff0000, v24\n"
      "v_pk_mul_f32 v[118:119], v[118:119], v[114:115] op_sel:[0,0] op_sel_hi:[1,0]\n"
      "v_cvt_pk_bf16_f32 v24, v118, v119\n"
      "v_lshlrev_b32 v118, 16, v25\n"
      "v_and_b32 v119, 0xffff0000, v25\n"
      "v_pk_mul_f32 v[118:119], v[118:119], v[114:115] op_sel:[0,0] op_sel_hi:[1,0]\n"
      "v_cvt_pk_bf16_f32 v25, v118, v119\n"
      "v_lshlrev_b32 v118, 16, v26\n"
      "v_and_b32 v119, 0xffff0000, v26\n"
      "v_pk_mul_f32 v[118:119], v[118:119], v[114:115] op_sel:[0,0] op_sel_hi:[1,0]\n"
      "v_cvt_pk_bf16_f32 v26, v118, v119\n"
      "v_lshlrev_b32 v118, 16, v27\n"
      "v_and_b32 v119, 0xffff0000, v27\n"
      "v_pk_mul_f32 v[118:119], v[118:119], v[114:115] op_sel:[0,0] op_sel_hi:[1,0]\n"
      "v_cvt_pk_bf16_f32 v27, v118, v119\n"
      "v_lshlrev_b32 v118, 16, v28\n"
      "v_and_b32 v119, 0xffff0000, v28\n"
      "v_pk_mul_f32 v[118:119], v[118:119], v[114:115] op_sel:[0,1] op_sel_hi:[1,1]\n"
      "v_cvt_pk_bf16_f32 v28, v118, v119\n"
      "v_lshlrev_b32 v118, 16, v29\n"
      "v_and_b32 v119, 0xffff0000, v29\n"
      "v_pk_mul_f32 v[118:119], v[118:119], v[114:115] op_sel:[0,1] op_sel_hi:[1,1]\n"
      "v_cvt_pk_bf16_f32 v29, v118, v119\n"
      "v_lshlrev_b32 v118, 16, v30\n"
      "v_and_b32 v119, 0xffff0000, v30\n"
      "v_pk_mul_f32 v[118:119], v[118:119], v[114:115] op_sel:[0,1] op_sel_hi:[1,1]\n"
      "v_cvt_pk_bf16_f32 v30, v118, v119\n"
      "v_lshlrev_b32 v118, 16, v31\n"
      "v_and_b32 v119, 0xffff0000, v31\n"
      "v_pk_mul_f32 v[118:119], v[118:119], v[114:115] op_sel:[0,1] op_sel_hi:[1,1]\n"
      "v_cvt_pk_bf16_f32 v31, v118, v119\n"
      "v_lshlrev_b32 v118, 16, v96\n"
      "v_and_b32 v119, 0xffff0000, v96\n"
      "v_pk_mul_f32 v[118:119], v[118:119], v[112:113] op_sel:[0,0] op_sel_hi:[1,0]\n"
      "v_cvt_pk_bf16_f32 v96, v118, v119\n"
      "v_lshlrev_b32 v118, 16, v97\n"
      "v_and_b32 v119, 0xffff0000, v97\n"
      "v_pk_mul_f32 v[118:119], v[118:119], v[112:113] op_sel:[0,0] op_sel_hi:[1,0]\n"
      "v_cvt_pk_bf16_f32 v97, v118, v119\n"
      "v_lshlrev_b32 v118, 16, v98\n"
      "v_and_b32 v119, 0xffff0000, v98\n"
      "v_pk_mul_f32 v[118:119], v[118:119], v[112:113] op_sel:[0,0] op_sel_hi:[1,0]\n"
      "v_cvt_pk_bf16_f32 v98, v118, v119\n"
      "v_lshlrev_b32 v118, 16, v99\n"
      "v_and_b32 v119, 0xffff0000, v99\n"
      "v_pk_mul_f32 v[118:119], v[118:119], v[112:113] op_sel:[0,0] op_sel_hi:[1,0]\n"
      "v_cvt_pk_bf16_f32 v99, v118, v119\n"
      "v_lshlrev_b32 v118, 16, v100\n"
      "v_and_b32 v119, 0xffff0000, v100\n"
      "v_pk_mul_f32 v[118:119], v[118:119], v[112:113] op_sel:[0,1] op_sel_hi:[1,1]\n"
      "v_cvt_pk_bf16_f32 v100, v118, v119\n"
      "v_lshlrev_b32 v118, 16, v101\n"
      "v_and_b32 v119, 0xffff0000, v101\n"
      "v_pk_mul_f32 v[118:119], v[118:119], v[112:113] op_sel:[0,1] op_sel_hi:[1,1]\n"
      "v_cvt_pk_bf16_f32 v101, v118, v119\n"
      "v_lshlrev_b32 v118, 16, v102\n"
      "v_and_b32 v119, 0xffff0000, v102\n"
      "v_pk_mul_f32 v[118:119], v[118:119], v[112:113] op_sel:[0,1] op_sel_hi:[1,1]\n"
      "v_cvt_pk_bf16_f32 v102, v118, v119\n"
      "v_lshlrev_b32 v118, 16, v103\n"
      "v_and_b32 v119, 0xffff0000, v103\n"
      "v_pk_mul_f32 v[118:119], v[118:119], v[112:113] op_sel:[0,1] op_sel_hi:[1,1]\n"
      "v_cvt_pk_bf16_f32 v103, v118, v119\n"
      "v_lshlrev_b32 v118, 16, v104\n"
      "v_and_b32 v119, 0xffff0000, v104\n"
      "v_pk_mul_f32 v[118:119], v[118:119], v[114:115] op_sel:[0,0] op_sel_hi:[1,0]\n"
      "v_cvt_pk_bf16_f32 v104, v118, v119\n"
      "v_lshlrev_b32 v118, 16, v105\n"
      "v_and_b32 v119, 0xffff0000, v105\n"
      "v_pk_mul_f32 v[118:119], v[118:119], v[114:115] op_sel:[0,0] op_sel_hi:[1,0]\n"
      "v_cvt_pk_bf16_f32 v105, v118, v119\n"
      "v_lshlrev_b32 v118, 16, v106\n"
      "v_and_b32 v119, 0xffff0000, v106\n"
      "v_pk_mul_f32 v[118:119], v[118:119], v[114:115] op_sel:[0,0] op_sel_hi:[1,0]\n"
      "v_cvt_pk_bf16_f32 v106, v118, v119\n"
      "v_lshlrev_b32 v118, 16, v107\n"
      "v_and_b32 v119, 0xffff0000, v107\n"
      "v_pk_mul_f32 v[118:119], v[118:119], v[114:115] op_sel:[0,0] op_sel_hi:[1,0]\n"
      "v_cvt_pk_bf16_f32 v107, v118, v119\n"
      "v_lshlrev_b32 v118, 16, v108\n"
      "v_and_b32 v119, 0xffff0000, v108\n"
      "v_pk_mul_f32 v[118:119], v[118:119], v[114:115] op_sel:[0,1] op_sel_hi:[1,1]\n"
      "v_cvt_pk_bf16_f32 v108, v118, v119\n"
      "v_lshlrev_b32 v118, 16, v109\n"
      "v_and_b32 v119, 0xffff0000, v109\n"
      "v_pk_mul_f32 v[118:119], v[118:119], v[114:115] op_sel:[0,1] op_sel_hi:[1,1]\n"
      "v_cvt_pk_bf16_f32 v109, v118, v119\n"
      "v_lshlrev_b32 v118, 16, v110\n"
      "v_and_b32 v119, 0xffff0000, v110\n"
      "v_pk_mul_f32 v[118:119], v[118:119], v[114:115] op_sel:[0,1] op_sel_hi:[1,1]\n"
      "v_cvt_pk_bf16_f32 v110, v118, v119\n"
      "v_lshlrev_b32 v118, 16, v111\n"
      "v_and_b32 v119, 0xffff0000, v111\n"
      "v_pk_mul_f32 v[118:119], v[118:119], v[114:115] op_sel:[0,1] op_sel_hi:[1,1]\n"
      "v_cvt_pk_bf16_f32 v111, v118, v119\n"
      "s_setpc_b64 s[84:85]\n"
      "MIS_%=:\n"
      "s_waitcnt lgkmcnt(0)\n"
      "v_sub_f32 v148, v153, v117\n"
      "v_min_f32 v149, 0, -v148\n"
      "v_min_f32 v148, 0, v148\n"
      "v_exp_f32 v149, v149\n"
      "v_exp_f32 v148, v148\n"
      "v_min_f32 v117, v117, v153\n"
      "v_mov_b32 v112, v148\n"
      "v_mov_b32 v228, v149\n"
      "v_mov_b32 v114, v112\n"
      "v_mov_b32 v230, v228\n"
      "s_nop 0\n"
      "v_permlane32_swap_b32 v112, v114\n"
      "v_permlane32_swap_b32 v228, v230\n"
      "v_mov_b32 v113, v112\n"
      "v_mov_b32 v229, v228\n"
      "v_mov_b32 v115, v114\n"
      "v_mov_b32 v231, v230\n"
      "v_permlane16_swap_b32 v112, v113\n"
      "v_permlane16_swap_b32 v228, v229\n"
      "v_permlane16_swap_b32 v114, v115\n"
      "v_permlane16_swap_b32 v230, v231\n"
      "s_call_b64 s[84:85], SCALE_%=\n"
      "v_pk_mul_f32 v[160:161], v[160:161], v[228:229] op_sel:[0,0] op_sel_hi:[1,0]\n"
      "v_pk_mul_f32 v[162:163], v[162:163], v[228:229] op_sel:[0,0] op_sel_hi:[1,0]\n"
      "v_pk_mul_f32 v[164:165], v[164:165], v[228:229] op_sel:[0,1] op_sel_hi:[1,1]\n"
      "v_pk_mul_f32 v[166:167], v[166:167], v[228:229] op_sel:[0,1] op_sel_hi:[1,1]\n"
      "v_pk_mul_f32 v[168:169], v[168:169], v[230:231] op_sel:[0,0] op_sel_hi:[1,0]\n"
      "v_pk_mul_f32 v[170:171], v[170:171], v[230:231] op_sel:[0,0] op_sel_hi:[1,0]\n"
      "v_pk_mul_f32 v[172:173], v[172:173], v[230:231] op_sel:[0,1] op_sel_hi:[1,1]\n"
      "v_pk_mul_f32 v[174:175], v[174:175], v[230:231] op_sel:[0,1] op_sel_hi:[1,1]\n"
      "v_pk_mul_f32 v[176:177], v[176:177], v[228:229] op_sel:[0,0] op_sel_hi:[1,0]\n"
      "v_pk_mul_f32 v[178:179], v[178:179], v[228:229] op_sel:[0,0] op_sel_hi:[1,0]\n"
      "v_pk_mul_f32 v[180:181], v[180:181], v[228:229] op_sel:[0,1] op_sel_hi:[1,1]\n"
      "v_pk_mul_f32 v[182:183], v[182:183], v[228:229] op_sel:[0,1] op_sel_hi:[1,1]\n"
      "v_pk_mul_f32 v[184:185], v[184:185], v[230:231] op_sel:[0,0] op_sel_hi:[1,0]\n"
      "v_pk_mul_f32 v[186:187], v[186:187], v[230:231] op_sel:[0,0] op_sel_hi:[1,0]\n"
      "v_pk_mul_f32 v[188:189], v[188:189], v[230:231] op_sel:[0,1] op_sel_hi:[1,1]\n"
      "v_pk_mul_f32 v[190:191], v[190:191], v[230:231] op_sel:[0,1] op_sel_hi:[1,1]\n"
      "v_mov_b32 v112, v228\n"
      "v_mov_b32 v113, v229\n"
      "v_mov_b32 v114, v230\n"
      "v_mov_b32 v115, v231\n"
      "v_accvgpr_read_b32 v128, a64\n"
      "v_accvgpr_read_b32 v129, a65\n"
      "v_accvgpr_read_b32 v130, a66\n"
      "v_accvgpr_read_b32 v131, a67\n"
      "v_accvgpr_read_b32 v132, a68\n"
      "v_accvgpr_read_b32 v133, a69\n"
      "v_accvgpr_read_b32 v134, a70\n"
      "v_accvgpr_read_b32 v135, a71\n"
      "v_accvgpr_read_b32 v136, a72\n"
      "v_accvgpr_read_b32 v137, a73\n"
      "v_accvgpr_read_b32 v138, a74\n"
      "v_accvgpr_read_b32 v139, a75\n"
      "v_accvgpr_read_b32 v140, a76\n"
      "v_accvgpr_read_b32 v141, a77\n"
      "v_accvgpr_read_b32 v142, a78\n"
      "v_accvgpr_read_b32 v143, a79\n"
      "v_lshlrev_b32 v118, 16, v128\n"
      "v_and_b32 v119, 0xffff0000, v128\n"
      "v_pk_mul_f32 v[118:119], v[118:119], v[112:113] op_sel:[0,0] op_sel_hi:[1,0]\n"
      "v_cvt_pk_bf16_f32 v128, v118, v119\n"
      "v_lshlrev_b32 v118, 16, v129\n"
      "v_and_b32 v119, 0xffff0000, v129\n"
      "v_pk_mul_f32 v[118:119], v[118:119], v[112:113] op_sel:[0,0] op_sel_hi:[1,0]\n"
      "v_cvt_pk_bf16_f32 v129, v118, v119\n"
      "v_lshlrev_b32 v118, 16, v130\n"
      "v_and_b32 v119, 0xffff0000, v130\n"
      "v_pk_mul_f32 v[118:119], v[118:119], v[112:113] op_sel:[0,0] op_sel_hi:[1,0]\n"
      "v_cvt_pk_bf16_f32 v130, v118, v119\n"
      "v_lshlrev_b32 v118, 16, v131\n"
      "v_and_b32 v119, 0xffff0000, v131\n"
      "v_pk_mul_f32 v[118:119], v[118:119], v[112:113] op_sel:[0,0] op_sel_hi:[1,0]\n"
      "v_cvt_pk_bf16_f32 v131, v118, v119\n"
      "v_lshlrev_b32 v118, 16, v132\n"
      "v_and_b32 v119, 0xffff0000, v132\n"
      "v_pk_mul_f32 v[118:119], v[118:119], v[112:113] op_sel:[0,1] op_sel_hi:[1,1]\n"
      "v_cvt_pk_bf16_f32 v132, v118, v119\n"
      "v_lshlrev_b32 v118, 16, v133\n"
      "v_and_b32 v119, 0xffff0000, v133\n"
      "v_pk_mul_f32 v[118:119], v[118:119], v[112:113] op_sel:[0,1] op_sel_hi:[1,1]\n"
      "v_cvt_pk_bf16_f32 v133, v118, v119\n"
      "v_lshlrev_b32 v118, 16, v134\n"
      "v_and_b32 v119, 0xffff0000, v134\n"
      "v_pk_mul_f32 v[118:119], v[118:119], v[112:113] op_sel:[0,1] op_sel_hi:[1,1]\n"
      "v_cvt_pk_bf16_f32 v134, v118, v119\n"
      "v_lshlrev_b32 v118, 16, v135\n"
      "v_and_b32 v119, 0xffff0000, v135\n"
      "v_pk_mul_f32 v[118:119], v[118:119], v[112:113] op_sel:[0,1] op_sel_hi:[1,1]\n"
      "v_cvt_pk_bf16_f32 v135, v118, v119\n"
      "v_lshlrev_b32 v118, 16, v136\n"
      "v_and_b32 v119, 0xffff0000, v136\n"
      "v_pk_mul_f32 v[118:119], v[118:119], v[114:115] op_sel:[0,0] op_sel_hi:[1,0]\n"
      "v_cvt_pk_bf16_f32 v136, v118, v119\n"
      "v_lshlrev_b32 v118, 16, v137\n"
      "v_and_b32 v119, 0xffff0000, v137\n"
      "v_pk_mul_f32 v[118:119], v[118:119], v[114:115] op_sel:[0,0] op_sel_hi:[1,0]\n"
      "v_cvt_pk_bf16_f32 v137, v118, v119\n"
      "v_lshlrev_b32 v118, 16, v138\n"
      "v_and_b32 v119, 0xffff0000, v138\n"
      "v_pk_mul_f32 v[118:119], v[118:119], v[114:115] op_sel:[0,0] op_sel_hi:[1,0]\n"
      "v_cvt_pk_bf16_f32 v138, v118, v119\n"
      "v_lshlrev_b32 v118, 16, v139\n"
      "v_and_b32 v119, 0xffff0000, v139\n"
      "v_pk_mul_f32 v[118:119], v[118:119], v[114:115] op_sel:[0,0] op_sel_hi:[1,0]\n"
      "v_cvt_pk_bf16_f32 v139, v118, v119\n"
      "v_lshlrev_b32 v118, 16, v140\n"
      "v_and_b32 v119, 0xffff0000, v140\n"
      "v_pk_mul_f32 v[118:119], v[118:119], v[114:115] op_sel:[0,1] op_sel_hi:[1,1]\n"
      "v_cvt_pk_bf16_f32 v140, v118, v119\n"
      "v_lshlrev_b32 v118, 16, v141\n"
      "v_and_b32 v119, 0xffff0000, v141\n"
      "v_pk_mul_f32 v[118:119], v[118:119], v[114:115] op_sel:[0,1] op_sel_hi:[1,1]\n"
      "v_cvt_pk_bf16_f32 v141, v118, v119\n"
      "v_lshlrev_b32 v118, 16, v142\n"
      "v_and_b32 v119, 0xffff0000, v142\n"
      "v_pk_mul_f32 v[118:119], v[118:119], v[114:115] op_sel:[0,1] op_sel_hi:[1,1]\n"
      "v_cvt_pk_bf16_f32 v142, v118, v119\n"
      "v_lshlrev_b32 v118, 16, v143\n"
      "v_and_b32 v119, 0xffff0000, v143\n"
      "v_pk_mul_f32 v[118:119], v[118:119], v[114:115] op_sel:[0,1] op_sel_hi:[1,1]\n"
      "v_cvt_pk_bf16_f32 v143, v118, v119\n"
      "v_accvgpr_write_b32 a64, v128\n"
      "v_accvgpr_write_b32 a65, v129\n"
      "v_accvgpr_write_b32 a66, v130\n"
      "v_accvgpr_write_b32 a67, v131\n"
      "v_accvgpr_write_b32 a68, v132\n"
      "v_accvgpr_write_b32 a69, v133\n"
      "v_accvgpr_write_b32 a70, v134\n"
      "v_accvgpr_write_b32 a71, v135\n"
      "v_accvgpr_write_b32 a72, v136\n"
      "v_accvgpr_write_b32 a73, v137\n"
      "v_accvgpr_write_b32 a74, v138\n"
      "v_accvgpr_write_b32 a75, v139\n"
      "v_accvgpr_write_b32 a76, v140\n"
      "v_accvgpr_write_b32 a77, v141\n"
      "v_accvgpr_write_b32 a78, v142\n"
      "v_accvgpr_write_b32 a79, v143\n"
      "s_branch MR_%=\n"
      "FIX_%=:\n"
      "s_nop 7\n"
      "v_sub_f32 v224, v81, v117\n"
      "v_min_f32 v112, 0, -v224\n"
      "v_min_f32 v224, 0, v224\n"
      "v_exp_f32 v112, v112\n"
      "v_exp_f32 v224, v224\n"
      "s_nop 0\n"
      "v_mul_f32 v144, v144, v224\n"
      "v_mul_f32 v80, v80, v112\n"
      "v_mov_b32 v226, v224\n"
      "v_mov_b32 v114, v112\n"
      "s_nop 0\n"
      "v_permlane32_swap_b32 v224, v226\n"
      "v_permlane32_swap_b32 v112, v114\n"
      "v_mov_b32 v225, v224\n"
      "v_mov_b32 v113, v112\n"
      "v_mov_b32 v227, v226\n"
      "v_mov_b32 v115, v114\n"
      "v_permlane16_swap_b32 v224, v225\n"
      "v_permlane16_swap_b32 v112, v113\n"
      "v_permlane16_swap_b32 v226, v227\n"
      "v_permlane16_swap_b32 v114, v115\n"
      "v_pk_mul_f32 v[32:33], v[32:33], v[224:225] op_sel:[0,0] op_sel_hi:[1,0]\n"
      "v_pk_mul_f32 v[34:35], v[34:35], v[224:225] op_sel:[0,0] op_sel_hi:[1,0]\n"
      "v_pk_mul_f32 v[36:37], v[36:37], v[224:225] op_sel:[0,1] op_sel_hi:[1,1]\n"
      "v_pk_mul_f32 v[38:39], v[38:39], v[224:225] op_sel:[0,1] op_sel_hi:[1,1]\n"
      "v_pk_mul_f32 v[40:41], v[40:41], v[226:227] op_sel:[0,0] op_sel_hi:[1,0]\n"
      "v_pk_mul_f32 v[42:43], v[42:43], v[226:227] op_sel:[0,0] op_sel_hi:[1,0]\n"
      "v_pk_mul_f32 v[44:45], v[44:45], v[226:227] op_sel:[0,1] op_sel_hi:[1,1]\n"
      "v_pk_mul_f32 v[46:47], v[46:47], v[226:227] op_sel:[0,1] op_sel_hi:[1,1]\n"
      "v_pk_mul_f32 v[48:49], v[48:49], v[224:225] op_sel:[0,0] op_sel_hi:[1,0]\n"
      "v_pk_mul_f32 v[50:51], v[50:51], v[224:225] op_sel:[0,0] op_sel_hi:[1,0]\n"
      "v_pk_mul_f32 v[52:53], v[52:53], v[224:225] op_sel:[0,1] op_sel_hi:[1,1]\n"
      "v_pk_mul_f32 v[54:55], v[54:55], v[224:225] op_sel:[0,1] op_sel_hi:[1,1]\n"
      "v_pk_mul_f32 v[56:57], v[56:57], v[226:227] op_sel:[0,0] op_sel_hi:[1,0]\n"
      "v_pk_mul_f32 v[58:59], v[58:59], v[226:227] op_sel:[0,0] op_sel_hi:[1,0]\n"
      "v_pk_mul_f32 v[60:61], v[60:61], v[226:227] op_sel:[0,1] op_sel_hi:[1,1]\n"
      "v_pk_mul_f32 v[62:63], v[62:63], v[226:227] op_sel:[0,1] op_sel_hi:[1,1]\n"
      "v_lshlrev_b32 v118, 16, v64\n"
      "v_and_b32 v119, 0xffff0000, v64\n"
      "v_pk_mul_f32 v[118:119], v[118:119], v[112:113] op_sel:[0,0] op_sel_hi:[1,0]\n"
      "v_cvt_pk_bf16_f32 v64, v118, v119\n"
      "v_lshlrev_b32 v118, 16, v65\n"
      "v_and_b32 v119, 0xffff0000, v65\n"
      "v_pk_mul_f32 v[118:119], v[118:119], v[112:113] op_sel:[0,0] op_sel_hi:[1,0]\n"
      "v_cvt_pk_bf16_f32 v65, v118, v119\n"
      "v_lshlrev_b32 v118, 16, v66\n"
      "v_and_b32 v119, 0xffff0000, v66\n"
      "v_pk_mul_f32 v[118:119], v[118:119], v[112:113] op_sel:[0,0] op_sel_hi:[1,0]\n"
      "v_cvt_pk_bf16_f32 v66, v118, v119\n"
      "v_lshlrev_b32 v118, 16, v67\n"
      "v_and_b32 v119, 0xffff0000, v67\n"
      "v_pk_mul_f32 v[118:119], v[118:119], v[112:113] op_sel:[0,0] op_sel_hi:[1,0]\n"
      "v_cvt_pk_bf16_f32 v67, v118, v119\n"
      "v_lshlrev_b32 v118, 16, v68\n"
      "v_and_b32 v119, 0xffff0000, v68\n"
      "v_pk_mul_f32 v[118:119], v[118:119], v[112:113] op_sel:[0,1] op_sel_hi:[1,1]\n"
      "v_cvt_pk_bf16_f32 v68, v118, v119\n"
      "v_lshlrev_b32 v118, 16, v69\n"
      "v_and_b32 v119, 0xffff0000, v69\n"
      "v_pk_mul_f32 v[118:119], v[118:119], v[112:113] op_sel:[0,1] op_sel_hi:[1,1]\n"
      "v_cvt_pk_bf16_f32 v69, v118, v119\n"
      "v_lshlrev_b32 v118, 16, v70\n"
      "v_and_b32 v119, 0xffff0000, v70\n"
      "v_pk_mul_f32 v[118:119], v[118:119], v[112:113] op_sel:[0,1] op_sel_hi:[1,1]\n"
      "v_cvt_pk_bf16_f32 v70, v118, v119\n"
      "v_lshlrev_b32 v118, 16, v71\n"
      "v_and_b32 v119, 0xffff0000, v71\n"
      "v_pk_mul_f32 v[118:119], v[118:119], v[112:113] op_sel:[0,1] op_sel_hi:[1,1]\n"
      "v_cvt_pk_bf16_f32 v71, v118, v119\n"
      "v_lshlrev_b32 v118, 16, v72\n"
      "v_and_b32 v119, 0xffff0000, v72\n"
      "v_pk_mul_f32 v[118:119], v[118:119], v[114:115] op_sel:[0,0] op_sel_hi:[1,0]\n"
      "v_cvt_pk_bf16_f32 v72, v118, v119\n"
      "v_lshlrev_b32 v118, 16, v73\n"
      "v_and_b32 v119, 0xffff0000, v73\n"
      "v_pk_mul_f32 v[118:119], v[118:119], v[114:115] op_sel:[0,0] op_sel_hi:[1,0]\n"
      "v_cvt_pk_bf16_f32 v73, v118, v119\n"
      "v_lshlrev_b32 v118, 16, v74\n"
      "v_and_b32 v119, 0xffff0000, v74\n"
      "v_pk_mul_f32 v[118:119], v[118:119], v[114:115] op_sel:[0,0] op_sel_hi:[1,0]\n"
      "v_cvt_pk_bf16_f32 v74, v118, v119\n"
      "v_lshlrev_b32 v118, 16, v75\n"
      "v_and_b32 v119, 0xffff0000, v75\n"
      "v_pk_mul_f32 v[118:119], v[118:119], v[114:115] op_sel:[0,0] op_sel_hi:[1,0]\n"
      "v_cvt_pk_bf16_f32 v75, v118, v119\n"
      "v_lshlrev_b32 v118, 16, v76\n"
      "v_and_b32 v119, 0xffff0000, v76\n"
      "v_pk_mul_f32 v[118:119], v[118:119], v[114:115] op_sel:[0,1] op_sel_hi:[1,1]\n"
      "v_cvt_pk_bf16_f32 v76, v118, v119\n"
      "v_lshlrev_b32 v118, 16, v77\n"
      "v_and_b32 v119, 0xffff0000, v77\n"
      "v_pk_mul_f32 v[118:119], v[118:119], v[114:115] op_sel:[0,1] op_sel_hi:[1,1]\n"
      "v_cvt_pk_bf16_f32 v77, v118, v119\n"
      "v_lshlrev_b32 v118, 16, v78\n"
      "v_and_b32 v119, 0xffff0000, v78\n"
      "v_pk_mul_f32 v[118:119], v[118:119], v[114:115] op_sel:[0,1] op_sel_hi:[1,1]\n"
      "v_cvt_pk_bf16_f32 v78, v118, v119\n"
      "v_lshlrev_b32 v118, 16, v79\n"
      "v_and_b32 v119, 0xffff0000, v79\n"
      "v_pk_mul_f32 v[118:119], v[118:119], v[114:115] op_sel:[0,1] op_sel_hi:[1,1]\n"
      "v_cvt_pk_bf16_f32 v79, v118, v119\n"
      "v_add_f32 v224, v144, v80\n"
      "v_rcp_f32 v224, v224\n"
      "v_mfma_f32_16x16x32_bf16 v[160:163], v[86:89], v[64:67], v[32:35]\n"
      "v_mfma_f32_16x16x32_bf16 v[176:179], v[84:87], v[64:67], v[48:51]\n"
      "v_mfma_f32_16x16x32_bf16 v[164:167], v[86:89], v[68:71], v[36:39]\n"
      "v_mfma_f32_16x16x32_bf16 v[180:183], v[84:87], v[68:71], v[52:55]\n"
      "v_mfma_f32_16x16x32_bf16 v[168:171], v[86:89], v[72:75], v[40:43]\n"
      "v_mfma_f32_16x16x32_bf16 v[184:187], v[84:87], v[72:75], v[56:59]\n"
      "v_mfma_f32_16x16x32_bf16 v[172:175], v[86:89], v[76:79], v[44:47]\n"
      "v_mfma_f32_16x16x32_bf16 v[188:191], v[84:87], v[76:79], v[60:63]\n"
      "v_mov_b32 v226, v224\n"
      "s_nop 1\n"
      "s_nop 0\n"
      "s_nop 1\n"
      "v_permlane32_swap_b32 v224, v226\n"
      "s_nop 1\n"
      "v_mov_b32 v225, v224\n"
      "s_nop 1\n"
      "v_mov_b32 v227, v226\n"
      "s_nop 1\n"
      "v_permlane16_swap_b32 v224, v225\n"
      "s_nop 1\n"
      "v_permlane16_swap_b32 v226, v227\n"
      "s_nop 1\n"
      "s_branch FR_%=\n"
      ".subsection 0\n"
      : "+{s[4:19]}"(ka), "+{v128}"(pre), "+{v[160:191]}"(qq), "+{s92}"(tch), "+{v[242:249]}"(ad), "+{v[250:251]}"(ra)
      : [blk] "s"(blk), [qo] "s"(bk * 128 * D_ * 2), [so0] "s"(so0), [inc] "s"(inc), [wb] "s"(wbase), [tv] "s"(wbase + STAGE_BYTES + KIMG + split * 8192),
        [qoff] "s"(Q_OFF + rh * 16384 - kh * 8192), [toff] "s"(TREE_OFF - kh * 8192)
      : "v0", "v1", "v2", "v3", "v4", "v5", "v6", "v7", "v8", "v9", "v10", "v11", "v12", "v13", "v14", "v15", "v16", "v17", "v18", "v19", "v20", "v21", "v22", "v23", "v24", "v25", "v26", "v27", "v28", "v29", "v30", "v31", "v32", "v33", "v34", "v35", "v36", "v37", "v38", "v39", "v40", "v41", "v42", "v43", "v44", "v45", "v46", "v47", "v48", "v49", "v50", "v51", "v52", "v53", "v54", "v55", "v56", "v57", "v58", "v59", "v60", "v61", "v62", "v63", "v64", "v65", "v66", "v67", "v68", "v69", "v70", "v71", "v72", "v73", "v74", "v75", "v76", "v77", "v78", "v79", "v80", "v81", "v82", "v83", "v84", "v85", "v86", "v87", "v88", "v89", "v90", "v91", "v92", "v93", "v94", "v95", "v96", "v97", "v98", "v99", "v100", "v101", "v102", "v103", "v104", "v105", "v106", "v107", "v108", "v109", "v110", "v111", "v112", "v113", "v114", "v115", "v116", "v117", "v118", "v119", "v120", "v121", "v122", "v123", "v124", "v125", "v126", "v127", "v129", "v130", "v131", "v132", "v133", "v134", "v135", "v136", "v137", "v138", "v139", "v140", "v141", "v142", "v143", "v144", "v145", "v146", "v147", "v148", "v149", "v150", "v151", "v152", "v153", "v154", "v155", "v156", "v157", "v158", "v159", "v192", "v193", "v194", "v195", "v196", "v197", "v198", "v199", "v200", "v201", "v202", "v203", "v204", "v205", "v206", "v207", "v208", "v209", "v210", "v211", "v212", "v213", "v214", "v215", "v216", "v217", "v218", "v219", "v220", "v221", "v222", "v223", "v224", "v225", "v226", "v227", "v228", "v229", "v230", "v231", "v232", "v233", "v234", "v235", "v236", "v237", "v238", "v239", "v240", "v241", "a0", "a1", "a2", "a3", "a4", "a5", "a6", "a7", "a8", "a9", "a10", "a11", "a12", "a13", "a14", "a15", "a16", "a17", "a18", "a19", "a20", "a21", "a22", "a23", "a24", "a25", "a26", "a27", "a28", "a29", "a30", "a31", "a32", "a33", "a34", "a35", "a36", "a37", "a38", "a39", "a40", "a41", "a42", "a43", "a44", "a45", "a46", "a47", "a48", "a49", "a50", "a51", "a52", "a53", "a54", "a55", "a56", "a57", "a58", "a59", "a60", "a61", "a62", "a63", "a64", "a65", "a66", "a67", "a68", "a69", "a70", "a71", "a72", "a73", "a74", "a75", "a76", "a77", "a78", "a79", "a80", "a81", "a82", "a83", "a84", "a85", "a86", "a87", "a88", "a89", "a90", "a91", "a92", "a93", "a94", "a95", "a96", "a97", "a98", "a99", "a100", "a101", "a102", "a103", "a104", "a105", "a106", "a107", "a108", "a109", "a110", "a111", "a112", "a113", "a114", "a115", "a116", "a117", "a118", "a119", "a120", "a121", "a122", "a123", "a124", "a125", "a126", "a127", "a128", "a129", "a130", "a131", "a132", "a133", "a134", "a135", "a136", "a137", "a138", "a139", "a140", "a141", "a142", "a143", "a144", "a145", "a146", "a147", "a148", "a149", "a150", "a151", "a152", "a153", "a154", "a155", "a156", "a157", "a158", "a159", "a160", "a161", "a162", "a163", "a164", "a165", "a166", "a167", "a168", "a169", "a170", "a171", "a172", "a173", "a174", "a175", "a176", "a177", "a178", "a179", "a180", "a181", "a182", "a183", "a184", "a185", "a186", "a187", "a188", "a189", "a190", "a191", "a192", "a193", "a194", "a195", "a196", "a197", "a198", "a199", "a200", "a201", "a202", "a203", "a204", "a205", "a206", "a207", "a208", "a209", "a210", "a211", "a212", "a213", "a214", "a215", "a216", "a217", "a218", "a219", "a220", "a221", "a222", "a223", "a224", "a225", "a226", "a227", "a228", "a229", "a230", "a231", "s20", "s21", "s22", "s23", "s24", "s25", "s26", "s27", "s28", "s29", "s30", "s31", "s33", "s34", "s35", "s36", "s37", "s38", "s39", "s40", "s41", "s42", "s43", "s44", "s60", "s61", "s62", "s63", "s64", "s65", "s66", "s67", "s68", "s69", "s70", "s71", "s72", "s73", "s74", "s75", "s76", "s77", "s78", "s79", "s80", "s81", "s82", "s83", "s84", "s85", "s86", "s87", "s88", "s89", "s90", "s91", "s93", "s94", "s95", "s98", "s99", "s100", "s101", "vcc", "scc", "memory");
}

}

extern "C" void kernel_launch(void* const* d_in, const int* in_sizes, int n_in, void* d_out, int out_size,
                              void* d_ws, size_t ws_size, hipStream_t stream) {
  if (n_in != 6 || ws_size < WS_END) return;
  const __hip_bfloat16* q = (const __hip_bfloat16*)d_in[0];
  const __hip_bfloat16* kc = (const __hip_bfloat16*)d_in[1];
  const __hip_bfloat16* vc = (const __hip_bfloat16*)d_in[2];
  const __hip_bfloat16* kt = (const __hip_bfloat16*)d_in[3];
  const __hip_bfloat16* vt = (const __hip_bfloat16*)d_in[4];
  const __hip_bfloat16* tm = (const __hip_bfloat16*)d_in[5];
  hipLaunchKernelGGL(tree_attn_main, dim3(B_ * HKV_ * 2), dim3(256), 0, stream, q, kc, vc, kt, vt, tm,
                     (__hip_bfloat16*)d_out, (char*)d_ws);
}
```

```cpp
#include <hip/hip_runtime.h>
#include <hip/hip_bf16.h>
#include <stdint.h>

#pragma clang diagnostic ignored "-Wint-to-pointer-cast"
#pragma clang diagnostic ignored "-Winline-asm"

namespace {

constexpr int B_ = 16, HQ_ = 32, HKV_ = 8, T_ = 32, C_ = 4096, D_ = 128;
constexpr int TILE = 64;
constexpr int KIMG = TILE * D_ * 2;
constexpr int STAGE_BYTES = 2 * KIMG;
constexpr int TREE_OFF = 4 * STAGE_BYTES;
constexpr int Q_OFF = 3 * STAGE_BYTES;
constexpr int LDS_BYTES = TREE_OFF + 2 * 13312;

constexpr size_t WS_EPOCH = 0;
constexpr size_t WS_REF = WS_EPOCH + 128 * 128;
constexpr size_t WS_X = WS_REF + 16384, WS_X1 = 4 << 20;
constexpr size_t XW_BYTES = 4096 + 1024;
constexpr size_t WS_END = 0x680000 + WS_X;
static_assert(WS_X1 + WS_X + 128 * 4 * XW_BYTES <= WS_END, "");

typedef unsigned long long u64x8 __attribute__((ext_vector_type(8)));
typedef unsigned u32x32 __attribute__((ext_vector_type(32)));
typedef unsigned u32x8  __attribute__((ext_vector_type(8)));
typedef unsigned u32x4  __attribute__((ext_vector_type(4)));
typedef unsigned u32x2  __attribute__((ext_vector_type(2)));
typedef __attribute__((address_space(1))) unsigned long long gu64;

__global__ __launch_bounds__(256, 1)
void tree_attn_main(const __hip_bfloat16*, const __hip_bfloat16*, const __hip_bfloat16*, const __hip_bfloat16*,
                    const __hip_bfloat16*, const __hip_bfloat16*, __hip_bfloat16*, char*) {
  __shared__ __attribute__((aligned(1024))) char smem[LDS_BYTES];
  unsigned blk = blockIdx.x;
  u64x8 ka; u32x32 qq; unsigned pre, tch, voffK; int tid = threadIdx.x;
  asm volatile(
      "s_load_dwordx16 s[4:19], %[kp], 0x0\n"
      "s_getpc_b64 s[84:85]\n"
      "v_lshlrev_b32 v112, 4, %[tid]\n"
      "s_lshl_b32 s69, %[blk], 7\n"
      "s_and_b32 s69, s69, 0x7c00\n"
      "v_lshl_add_u32 v120, %[tid], 2, s69\n"
      "global_load_dword v128, v120, s[84:85]\n"
      "v_readfirstlane_b32 s77, %[tid]\n"
      "v_lshrrev_b32 %[vk], 4, %[tid]\n"
      "s_and_b32 s78, %[blk], 7\n"
      "v_xor_b32 %[vk], %[vk], %[tid]\n"
      "s_lshr_b32 s74, %[blk], 4\n"
      "v_and_b32 %[vk], 7, %[vk]\n"
      "s_lshl4_add_u32 s78, s78, s74\n"
      "v_lshrrev_b32 v120, 3, %[tid]\n"
      "s_lshl_b32 s78, s78, 15\n"
      "v_lshlrev_b32 %[vk], 4, %[vk]\n"
      "s_lshl_b32 s77, s77, 5\n"
      "v_lshl_or_b32 %[vk], v120, 8, %[vk]\n"
      "s_bfm_b32 s66, 1, 22\n"
      "s_bfm_b32 s67, 1, 17\n"
      "v_add_u32 v120, 0x2000, %[vk]\n"
      "s_add_u32 s74, s78, 0x4000\n"
      "s_lshl_b32 s76, s78, 5\n"
      "s_add_u32 m0, s77, 0x20000\n"
      "s_mov_b32 s86, 0x100000\n"
      "s_mov_b32 s87, 0x20000\n"
      "s_lshr_b32 s75, s78, 2\n"
      "v_mov_b64 v[116:117], 0\n"
      "v_mov_b64 v[118:119], 0\n"
      "s_lshl_b32 s69, %[blk], 10\n"
      "s_andn2_b32 s69, 0x2000, s69\n"
      "v_add_u32 v113, s69, v112\n"
      "ds_write_b128 v112, v[116:119] offset:16384\n"
      "ds_write_b128 v112, v[116:119] offset:20480\n"
      "ds_write_b128 v112, v[116:119] offset:24576\n"
      "ds_write_b128 v112, v[116:119] offset:28672\n"
      "s_waitcnt lgkmcnt(0)\n"
      "s_mov_b32 s64, s4\n"
      "s_and_b32 s65, s5, 0xffff\n"
      "buffer_load_dwordx4 v[160:163], %[vk], s[64:67], s78 offen nt\n"
      "buffer_load_dwordx4 v[164:167], %[vk], s[64:67], s78 offen offset:128 nt\n"
      "buffer_load_dwordx4 v[168:171], v120, s[64:67], s78 offen nt\n"
      "buffer_load_dwordx4 v[172:175], v120, s[64:67], s78 offen offset:128 nt\n"
      "s_load_dword s92, s[8:9], s76\n"
      "buffer_load_dwordx4 v[176:179], %[vk], s[64:67], s74 offen nt\n"
      "buffer_load_dwordx4 v[180:183], %[vk], s[64:67], s74 offen offset:128 nt\n"
      "buffer_load_dwordx4 v[184:187], v120, s[64:67], s74 offen nt\n"
      "buffer_load_dwordx4 v[188:191], v120, s[64:67], s74 offen offset:128 nt\n"
      "s_mov_b32 s84, s10\n"
      "s_and_b32 s85, s11, 0xffff\n"
      "s_lshr_b32 s76, s78, 18\n"
      "s_lshl_b32 s76, s76, 11\n"
      "s_lshr_b32 s69, s77, 1\n"
      "buffer_load_dwordx4 %[vk], s[84:87], s75 offen nt lds\n"
      "s_add_u32 m0, s77, 131968\n"
      "s_cmp_gt_u32 s77, 2048\n"
      "buffer_load_dwordx4 %[vk], s[84:87], s75 offen offset:128 nt lds\n"
      "s_cbranch_scc1 NOMASK_%=\n"
      "s_add_u32 m0, s69, 140288\n"
      "s_mov_b32 s64, s14\n"
      "s_and_b32 s65, s15, 0xffff\n"
      "s_mov_b32 s66, 0x8000\n"
      "buffer_load_dwordx4 v112, s[64:67], s76 offen lds\n"
      "NOMASK_%=:\n"
      "ds_write_b128 v113, v[116:119] offset:49152\n"
      "ds_write_b128 v113, v[116:119] offset:53248\n"
      "s_mov_b32 s64, s6\n"
      "s_and_b32 s65, s7, 0xffff\n"
      "s_bfm_b32 s66, 1, 27\n"
      "s_lshl_b32 s76, s78, 5\n"
      "s_bfe_u32 s74, %[blk], 0x10003\n"
      "s_lshl_b32 s74, s74, 15\n"
      "s_add_u32 s76, s76, s74\n"
      "s_bfe_u32 s74, %[blk], 0x10004\n"
      "s_lshl_b32 s74, s74, 14\n"
      "s_add_u32 s76, s76, s74\n"
      "s_add_u32 m0, s77, 0\n"
      "s_nop 0\n"
      "buffer_load_dwordx4 %[vk], s[64:67], s76 offen nt lds\n"
      "s_add_u32 m0, s77, 896\n"
      "s_nop 0\n"
      "buffer_load_dwordx4 %[vk], s[64:67], s76 offen offset:128 nt lds\n"
      "s_add_u32 m0, s77, 8192\n"
      "s_nop 0\n"
      "buffer_load_dwordx4 v120, s[64:67], s76 offen nt lds\n"
      : "=&{s[4:19]}"(ka), "=&{v128}"(pre), "=&{v[160:191]}"(qq), "=&{s92}"(tch), [vk] "=&v"(voffK), [tid] "+v"(tid)
      : [kp] "s"(__builtin_amdgcn_kernarg_segment_ptr()), [blk] "s"(blk)
      : "v112", "v113", "v116", "v117", "v118", "v119", "v120", "s64", "s65", "s66", "s67", "s69", "s70", "s71", "s74", "s75", "s76", "s77", "s78", "s84", "s85", "s86", "s87", "s96", "s97", "scc", "memory");
  asm volatile("" : "+s"(blk));
  const int lane = tid & 63;
  const int wid = __builtin_amdgcn_readfirstlane(tid >> 6);
  const unsigned wbase = wid * 2048;
  const int rh = wid & 1, kh = wid >> 1;
  const int xg = blk & 7, jj = blk >> 3, bk = xg * 16 + (jj >> 1), split = jj & 1;
  const int c16 = lane & 15, g = lane >> 4;
  const unsigned lds0 = (unsigned)(uintptr_t)smem;

  const unsigned voffV = (unsigned)((8 * wid + (lane >> 3)) * 256 + (((lane & 7) ^ (g << 1)) << 4));
  const unsigned so0 = (unsigned)(bk * C_ + split * 2 * TILE) * (D_ * 2) + (bk & 1) * KIMG, inc = KIMG - 2 * (bk & 1) * KIMG;
  u32x8 ad;
#pragma unroll
  for (int j = 0; j < 2; ++j)
    ad[j] = lds0 + (unsigned)(2048 * (c16 >> 3) + 128 * (c16 & 7) + (((4 * j + g) ^ (c16 >> 1)) << 4) + kh * 8192);
  {
    const int q4 = c16 >> 2, p = lane & 3;
#pragma unroll
    for (int j = 0; j < 4; ++j)
      ad[2 + j] = lds0 + (unsigned)(2048 * (g >> 1) + 128 * (4 * (g & 1) + q4) + kh * 8192 + KIMG +
                                    (((2 * (j ^ (2 * kh)) + (p >> 1)) ^ ((2 * (g & 1) + (q4 >> 1)) << 1)) << 4) + 8 * (p & 1));
  }
  ad[6] = voffK; ad[7] = voffV + 128 * split;
  u32x2 ra = {(unsigned)(lane < 4 ? WS_REF + lane * 512 : 1 << 26), (unsigned)(WS_REF + (jj & (4 - 1)) * 512 + (lane & 7) * 4 + (wid << 26))};
  asm volatile(
      "s_memrealtime s[82:83]\n"
      "s_mov_b32 s60, %[so0]\n"
      "s_mov_b32 s62, %[wb]\n"
      "v_add_u32 v120, 0x2000, v248\n"
      "s_lshr_b32 s80, %[qo], 8\n"
      "v_mov_b32 v240, s80\n"
      "s_mov_b32 s22, 0x8000000\n"
      "s_mov_b32 s23, 0x20000\n"
      "s_mov_b32 s20, s6\n"
      "s_and_b32 s21, s7, 0xffff\n"
      "s_nop 0\n"
      "v_mov_b64 v[216:217], 0\n"
      "v_mov_b64 v[218:219], 0\n"
      "s_mov_b32 s86, 0x100000\n"
      "s_mov_b32 s87, 0x20000\n"
      "v_mfma_f32_32x32x16_bf16 v[32:47], v[216:219], v[216:219], 0\n"
      "s_lshr_b32 s75, %[qo], 2\n"
      "s_mov_b32 s84, s12\n"
      "s_and_b32 s85, s13, 0xffff\n"
      "v_mfma_f32_32x32x16_bf16 v[48:63], v[216:219], v[216:219], 0\n"
      "s_nop 0\n"
      "v_xor_b32 v121, 0x80, v249\n"
      "s_sub_u32 s78, 0x10000, %[inc]\n"
      "s_mov_b32 s94, 0x3e0293ee\n"
      "v_mfma_f32_32x32x16_bf16 v[64:79], v[216:219], v[216:219], 0\n"
      "s_mov_b32 s95, 0x3e0293ee\n"
      "s_mov_b32 s74, 0xffff0000\n"
      "s_mov_b32 s76, 0xceabfb8f\n"
      "v_mfma_f32_32x32x16_bf16 v[80:95], v[216:219], v[216:219], 0\n"
      "v_mbcnt_lo_u32_b32 v112, -1, 0\n"
      "s_nop 0\n"
      "v_mbcnt_hi_u32_b32 v112, -1, v112\n"
      "s_add_u32 s69, %[wb], 0x18000\n"
      "v_mfma_f32_32x32x16_bf16 v[192:207], v[216:219], v[216:219], 0\n"
      "v_lshl_add_u32 v226, v112, 4, s69\n"
      "v_lshrrev_b32 v115, 4, v112\n"
      "v_and_b32 v112, 15, v112\n"
      "v_mfma_f32_32x32x16_bf16 a[128:143], v[216:219], v[216:219], 0\n"
      "v_lshlrev_b32 v115, 3, v115\n"
      "v_lshl_add_u32 v115, v112, 6, v115\n"
      "s_add_u32 m0, s62, 9088\n"
      "s_nop 0\n"
      "buffer_load_dwordx4 v120, s[20:23], s60 offen offset:128 nt lds\n"
      "v_add_u32 v115, 140288, v115\n"
      "v_mfma_f32_32x32x16_bf16 a[144:159], v[216:219], v[216:219], 0\n"
      "v_add_u32 v118, %[qoff], v242\n"
      "v_add_u32 v224, %[toff], v242\n"
      "v_add_u32 v119, %[qoff], v243\n"
      "v_mfma_f32_32x32x16_bf16 a[160:175], v[216:219], v[216:219], 0\n"
      "v_add_u32 v225, %[toff], v243\n"
      "v_mfma_f32_32x32x16_bf16 a[176:191], v[216:219], v[216:219], 0\n"
      "s_add_u32 s62, s62, 0x8000\n"
      "s_and_b32 s62, s62, 0x1ffff\n"
      "s_add_u32 s60, s60, %[inc]\n"
      "s_waitcnt vmcnt(13)\n"
      "v_lshlrev_b32 v232, 16, v160\n"
      "v_and_b32 v233, s74, v160\n"
      "v_lshlrev_b32 v234, 16, v161\n"
      "v_and_b32 v235, s74, v161\n"
      "v_lshlrev_b32 v236, 16, v162\n"
      "v_and_b32 v237, s74, v162\n"
      "v_lshlrev_b32 v238, 16, v163\n"
      "v_and_b32 v239, s74, v163\n"
      "v_pk_mul_f32 v[232:233], v[232:233], s[94:95]\n"
      "v_pk_mul_f32 v[234:235], v[234:235], s[94:95]\n"
      "v_pk_mul_f32 v[236:237], v[236:237], s[94:95]\n"
      "v_pk_mul_f32 v[238:239], v[238:239], s[94:95]\n"
      "v_cvt_pk_bf16_f32 v160, v232, v233\n"
      "v_cvt_pk_bf16_f32 v161, v234, v235\n"
      "v_cvt_pk_bf16_f32 v162, v236, v237\n"
      "v_cvt_pk_bf16_f32 v163, v238, v239\n"
      "ds_write_b128 v226, v[160:163] offset:0\n"
      "s_waitcnt vmcnt(12)\n"
      "v_lshlrev_b32 v232, 16, v164\n"
      "v_and_b32 v233, s74, v164\n"
      "v_lshlrev_b32 v234, 16, v165\n"
      "v_and_b32 v235, s74, v165\n"
      "v_lshlrev_b32 v236, 16, v166\n"
      "v_and_b32 v237, s74, v166\n"
      "v_lshlrev_b32 v238, 16, v167\n"
      "v_and_b32 v239, s74, v167\n"
      "v_pk_mul_f32 v[232:233], v[232:233], s[94:95]\n"
      "v_pk_mul_f32 v[234:235], v[234:235], s[94:95]\n"
      "v_pk_mul_f32 v[236:237], v[236:237], s[94:95]\n"
      "v_pk_mul_f32 v[238:239], v[238:239], s[94:95]\n"
      "v_cvt_pk_bf16_f32 v164, v232, v233\n"
      "v_cvt_pk_bf16_f32 v165, v234, v235\n"
      "v_cvt_pk_bf16_f32 v166, v236, v237\n"
      "v_cvt_pk_bf16_f32 v167, v238, v239\n"
      "ds_write_b128 v226, v[164:167] offset:1024\n"
      "s_waitcnt vmcnt(11)\n"
      "v_lshlrev_b32 v232, 16, v168\n"
      "v_and_b32 v233, s74, v168\n"
      "v_lshlrev_b32 v234, 16, v169\n"
      "v_and_b32 v235, s74, v169\n"
      "v_lshlrev_b32 v236, 16, v170\n"
      "v_and_b32 v237, s74, v170\n"
      "v_lshlrev_b32 v238, 16, v171\n"
      "v_and_b32 v239, s74, v171\n"
      "v_pk_mul_f32 v[232:233], v[232:233], s[94:95]\n"
      "v_pk_mul_f32 v[234:235], v[234:235], s[94:95]\n"
      "v_pk_mul_f32 v[236:237], v[236:237], s[94:95]\n"
      "v_pk_mul_f32 v[238:239], v[238:239], s[94:95]\n"
      "v_cvt_pk_bf16_f32 v168, v232, v233\n"
      "v_cvt_pk_bf16_f32 v169, v234, v235\n"
      "v_cvt_pk_bf16_f32 v170, v236, v237\n"
      "v_cvt_pk_bf16_f32 v171, v238, v239\n"
      "ds_write_b128 v226, v[168:171] offset:8192\n"
      "s_add_u32 m0, s62, 0\n"
      "s_nop 0\n"
      "buffer_load_dwordx4 v248, s[20:23], s60 offen nt lds\n"
      "s_waitcnt vmcnt(11)\n"
      "v_lshlrev_b32 v232, 16, v172\n"
      "v_and_b32 v233, s74, v172\n"
      "v_lshlrev_b32 v234, 16, v173\n"
      "v_and_b32 v235, s74, v173\n"
      "v_lshlrev_b32 v236, 16, v174\n"
      "v_and_b32 v237, s74, v174\n"
      "v_lshlrev_b32 v238, 16, v175\n"
      "v_and_b32 v239, s74, v175\n"
      "v_pk_mul_f32 v[232:233], v[232:233], s[94:95]\n"
      "v_pk_mul_f32 v[234:235], v[234:235], s[94:95]\n"
      "v_pk_mul_f32 v[236:237], v[236:237], s[94:95]\n"
      "v_pk_mul_f32 v[238:239], v[238:239], s[94:95]\n"
      "v_cvt_pk_bf16_f32 v172, v232, v233\n"
      "v_cvt_pk_bf16_f32 v173, v234, v235\n"
      "v_cvt_pk_bf16_f32 v174, v236, v237\n"
      "v_cvt_pk_bf16_f32 v175, v238, v239\n"
      "ds_write_b128 v226, v[172:175] offset:9216\n"
      "s_add_u32 m0, s62, 896\n"
      "s_nop 0\n"
      "buffer_load_dwordx4 v248, s[20:23], s60 offen offset:128 nt lds\n"
      "s_waitcnt vmcnt(11)\n"
      "v_lshlrev_b32 v232, 16, v176\n"
      "v_and_b32 v233, s74, v176\n"
      "v_lshlrev_b32 v234, 16, v177\n"
      "v_and_b32 v235, s74, v177\n"
      "v_lshlrev_b32 v236, 16, v178\n"
      "v_and_b32 v237, s74, v178\n"
      "v_lshlrev_b32 v238, 16, v179\n"
      "v_and_b32 v239, s74, v179\n"
      "v_pk_mul_f32 v[232:233], v[232:233], s[94:95]\n"
      "v_pk_mul_f32 v[234:235], v[234:235], s[94:95]\n"
      "v_pk_mul_f32 v[236:237], v[236:237], s[94:95]\n"
      "v_pk_mul_f32 v[238:239], v[238:239], s[94:95]\n"
      "v_cvt_pk_bf16_f32 v176, v232, v233\n"
      "v_cvt_pk_bf16_f32 v177, v234, v235\n"
      "v_cvt_pk_bf16_f32 v178, v236, v237\n"
      "v_cvt_pk_bf16_f32 v179, v238, v239\n"
      "ds_write_b128 v226, v[176:179] offset:16384\n"
      "s_add_u32 m0, s62, 8192\n"
      "s_nop 0\n"
      "buffer_load_dwordx4 v120, s[20:23], s60 offen nt lds\n"
      "s_waitcnt vmcnt(11)\n"
      "v_lshlrev_b32 v232, 16, v180\n"
      "v_and_b32 v233, s74, v180\n"
      "v_lshlrev_b32 v234, 16, v181\n"
      "v_and_b32 v235, s74, v181\n"
      "v_lshlrev_b32 v236, 16, v182\n"
      "v_and_b32 v237, s74, v182\n"
      "v_lshlrev_b32 v238, 16, v183\n"
      "v_and_b32 v239, s74, v183\n"
      "v_pk_mul_f32 v[232:233], v[232:233], s[94:95]\n"
      "v_pk_mul_f32 v[234:235], v[234:235], s[94:95]\n"
      "v_pk_mul_f32 v[236:237], v[236:237], s[94:95]\n"
      "v_pk_mul_f32 v[238:239], v[238:239], s[94:95]\n"
      "v_cvt_pk_bf16_f32 v180, v232, v233\n"
      "v_cvt_pk_bf16_f32 v181, v234, v235\n"
      "v_cvt_pk_bf16_f32 v182, v236, v237\n"
      "v_cvt_pk_bf16_f32 v183, v238, v239\n"
      "ds_write_b128 v226, v[180:183] offset:17408\n"
      "s_add_u32 m0, s62, 9088\n"
      "s_nop 0\n"
      "buffer_load_dwordx4 v120, s[20:23], s60 offen offset:128 nt lds\n"
      "s_waitcnt vmcnt(11)\n"
      "v_lshlrev_b32 v232, 16, v184\n"
      "v_and_b32 v233, s74, v184\n"
      "v_lshlrev_b32 v234, 16, v185\n"
      "v_and_b32 v235, s74, v185\n"
      "v_lshlrev_b32 v236, 16, v186\n"
      "v_and_b32 v237, s74, v186\n"
      "v_lshlrev_b32 v238, 16, v187\n"
      "v_and_b32 v239, s74, v187\n"
      "v_pk_mul_f32 v[232:233], v[232:233], s[94:95]\n"
      "v_pk_mul_f32 v[234:235], v[234:235], s[94:95]\n"
      "v_pk_mul_f32 v[236:237], v[236:237], s[94:95]\n"
      "v_pk_mul_f32 v[238:239], v[238:239], s[94:95]\n"
      "v_cvt_pk_bf16_f32 v184, v232, v233\n"
      "v_cvt_pk_bf16_f32 v185, v234, v235\n"
      "v_cvt_pk_bf16_f32 v186, v236, v237\n"
      "v_cvt_pk_bf16_f32 v187, v238, v239\n"
      "ds_write_b128 v226, v[184:187] offset:24576\n"
      "s_add_u32 m0, %[tv], 0\n"
      "s_nop 0\n"
      "buffer_load_dwordx4 v249, s[84:87], s75 offen nt lds\n"
      "s_add_u32 m0, %[tv], 1024\n"
      "s_nop 0\n"
      "buffer_load_dwordx4 v121, s[84:87], s75 offen nt lds\n"
      "s_waitcnt vmcnt(12)\n"
      "v_lshlrev_b32 v232, 16, v188\n"
      "v_and_b32 v233, s74, v188\n"
      "v_lshlrev_b32 v234, 16, v189\n"
      "v_and_b32 v235, s74, v189\n"
      "v_lshlrev_b32 v236, 16, v190\n"
      "v_and_b32 v237, s74, v190\n"
      "v_lshlrev_b32 v238, 16, v191\n"
      "v_and_b32 v239, s74, v191\n"
      "v_pk_mul_f32 v[232:233], v[232:233], s[94:95]\n"
      "v_pk_mul_f32 v[234:235], v[234:235], s[94:95]\n"
      "v_pk_mul_f32 v[236:237], v[236:237], s[94:95]\n"
      "v_pk_mul_f32 v[238:239], v[238:239], s[94:95]\n"
      "v_cvt_pk_bf16_f32 v188, v232, v233\n"
      "v_cvt_pk_bf16_f32 v189, v234, v235\n"
      "v_cvt_pk_bf16_f32 v190, v236, v237\n"
      "v_cvt_pk_bf16_f32 v191, v238, v239\n"
      "ds_write_b128 v226, v[188:191] offset:25600\n"
      "s_add_u32 s62, s62, 0x8000\n"
      "s_and_b32 s62, s62, 0x1ffff\n"
      "s_add_u32 s60, s60, s78\n"
      "s_waitcnt vmcnt(10) lgkmcnt(0)\n"
      "s_barrier\n"
      "s_memrealtime s[68:69]\n"
      "ds_read_b64 v[208:209], v115 offset:0\n"
      "ds_read_b64 v[210:211], v115 offset:32\n"
      "ds_read_b64 v[212:213], v115 offset:1024\n"
      "ds_read_b64 v[214:215], v115 offset:1056\n"
      "ds_read_b128 a[96:99], v224 offset:0\n"
      "ds_read_b128 a[100:103], v224 offset:4096\n"
      "ds_read_b128 a[0:3], v118 offset:0\n"
      "ds_read_b128 a[4:7], v118 offset:4096\n"
      "ds_read_b128 a[8:11], v118 offset:8192\n"
      "ds_read_b128 a[12:15], v118 offset:12288\n"
      "ds_read_b128 a[104:107], v225 offset:0\n"
      "ds_read_b128 a[108:111], v225 offset:4096\n"
      "global_load_dwordx2 v[240:241], v240, s[18:19] sc1\n"
      "s_mov_b32 s28, s18\n"
      "s_and_b32 s29, s19, 0xffff\n"
      "s_mov_b32 s30, 0x688000\n"
      "s_mov_b32 s31, 0x20000\n"
      "s_lshl_b32 s41, %[blk], 2\n"
      "s_and_b32 s43, s41, 0x7c\n"
      "s_add_u32 s43, s43, 128\n"
      "s_sub_u32 s39, s41, 896\n"
      "s_lshr_b32 s44, %[blk], 5\n"
      "s_cmp_eq_u32 s44, 5\n"
      "s_cselect_b32 s39, s43, s39\n"
      "s_lshr_b32 s43, %[wb], 6\n"
      "s_add_u32 s41, s41, s43\n"
      "s_cmp_lt_u32 s39, 128\n"
      "s_cselect_b32 s89, 0x2ffe, 0\n"
      "s_or_b32 s89, s89, 0x2aae\n"
      "s_waitcnt lgkmcnt(8)\n"
      "ds_read_b128 a[16:19], v119 offset:0\n"
      "ds_read_b128 a[20:23], v119 offset:4096\n"
      "ds_read_b128 a[24:27], v119 offset:8192\n"
      "ds_read_b128 a[28:31], v119 offset:12288\n"
      "v_lshlrev_b32 v96, 16, v208\n"
      "v_and_b32 v97, s74, v208\n"
      "v_lshlrev_b32 v98, 16, v209\n"
      "v_and_b32 v99, s74, v209\n"
      "v_lshlrev_b32 v100, 16, v210\n"
      "v_and_b32 v101, s74, v210\n"
      "v_lshlrev_b32 v102, 16, v211\n"
      "v_and_b32 v103, s74, v211\n"
      "v_lshlrev_b32 v104, 16, v212\n"
      "v_and_b32 v105, s74, v212\n"
      "v_lshlrev_b32 v106, 16, v213\n"
      "v_and_b32 v107, s74, v213\n"
      "v_lshlrev_b32 v108, 16, v214\n"
      "v_and_b32 v109, s74, v214\n"
      "v_lshlrev_b32 v110, 16, v215\n"
      "v_and_b32 v111, s74, v215\n"
      "v_fma_f32 v96, -v96, s76, s76\n"
      "v_fma_f32 v97, -v97, s76, s76\n"
      "v_fma_f32 v98, -v98, s76, s76\n"
      "v_fma_f32 v99, -v99, s76, s76\n"
      "v_fma_f32 v100, -v100, s76, s76\n"
      "v_fma_f32 v101, -v101, s76, s76\n"
      "v_fma_f32 v102, -v102, s76, s76\n"
      "v_fma_f32 v103, -v103, s76, s76\n"
      "v_fma_f32 v104, -v104, s76, s76\n"
      "v_fma_f32 v105, -v105, s76, s76\n"
      "v_fma_f32 v106, -v106, s76, s76\n"
      "v_fma_f32 v107, -v107, s76, s76\n"
      "v_fma_f32 v108, -v108, s76, s76\n"
      "v_fma_f32 v109, -v109, s76, s76\n"
      "v_fma_f32 v110, -v110, s76, s76\n"
      "v_fma_f32 v111, -v111, s76, s76\n"
      "s_waitcnt lgkmcnt(0)\n"
      "ds_read_b128 a[112:115], v224 offset:1024\n"
      "ds_read_b128 a[116:119], v224 offset:5120\n"
      "ds_read_b128 a[32:35], v118 offset:1024\n"
      "ds_read_b128 a[36:39], v118 offset:5120\n"
      "ds_read_b128 a[40:43], v118 offset:9216\n"
      "ds_read_b128 a[44:47], v118 offset:13312\n"
      "ds_read_b128 a[120:123], v225 offset:1024\n"
      "ds_read_b128 a[124:127], v225 offset:5120\n"
      "ds_read_b128 a[48:51], v119 offset:1024\n"
      "ds_read_b128 a[52:55], v119 offset:5120\n"
      "ds_read_b128 a[56:59], v119 offset:9216\n"
      "ds_read_b128 a[60:63], v119 offset:13312\n"
      "s_cmp_lt_u32 s41, 32\n"
      "s_cbranch_scc0 NOLEAD_%=\n"
      "v_mov_b32 v115, s68\n"
      "buffer_store_dword v115, v250, s[28:31], s41 offen sc1\n"
      "NOLEAD_%=:\n"
      "v_mfma_f32_16x16x32_bf16 v[160:163], a[96:99], a[0:3], v[96:99]\n"
      "s_mov_b32 s83, 0\n"
      "v_add_u32 v122, 0x2000, v249\n"
      "v_add_u32 v123, 0x2000, v121\n"
      "v_mfma_f32_16x16x32_bf16 v[168:171], a[96:99], a[4:7], v[104:107]\n"
      "s_add_u32 m0, s62, 0\n"
      "s_sub_u32 s67, s68, s82\n"
      "v_mov_b32 v113, 0x3f803f80\n"
      "v_accvgpr_write_b32 a192, 0\n"
      "v_mfma_f32_16x16x32_bf16 v[176:179], a[96:99], a[8:11], v[96:99]\n"
      "s_min_u32 s67, s67, 140\n"
      "buffer_load_dwordx4 v248, s[20:23], s60 offen nt lds\n"
      "v_cmp_eq_u32 vcc, 0, v112\n"
      "v_mfma_f32_16x16x32_bf16 v[184:187], a[96:99], a[12:15], v[104:107]\n"
      "s_add_u32 m0, s62, 896\n"
      "s_add_u32 s66, s82, s67\n"
      "v_mfma_f32_16x16x32_bf16 v[164:167], a[100:103], a[0:3], v[100:103]\n"
      "s_add_u32 s66, s66, 91\n"
      "buffer_load_dwordx4 v248, s[20:23], s60 offen offset:128 nt lds\n"
      "v_cndmask_b32 v114, 0, v113, vcc\n"
      "v_mfma_f32_16x16x32_bf16 v[172:175], a[100:103], a[4:7], v[108:111]\n"
      "s_add_u32 m0, s62, 8192\n"
      "s_sub_u32 s71, s68, 400\n"
      "v_accvgpr_write_b32 a196, v114\n"
      "v_accvgpr_write_b32 a197, v114\n"
      "v_mfma_f32_16x16x32_bf16 v[180:183], a[100:103], a[8:11], v[100:103]\n"
      "s_mov_b32 s68, s66\n"
      "buffer_load_dwordx4 v120, s[20:23], s60 offen nt lds\n"
      "v_accvgpr_write_b32 a198, v114\n"
      "v_accvgpr_write_b32 a199, v114\n"
      "v_mfma_f32_16x16x32_bf16 v[188:191], a[100:103], a[12:15], v[108:111]\n"
      "s_add_u32 m0, s62, 9088\n"
      "s_mov_b32 s82, s66\n"
      "v_cmp_eq_u32 vcc, 4, v112\n"
      "v_mfma_f32_16x16x32_bf16 v[160:163], a[104:107], a[16:19], v[160:163]\n"
      "s_add_u32 s62, s62, 0x8000\n"
      "buffer_load_dwordx4 v120, s[20:23], s60 offen offset:128 nt lds\n"
      "s_mov_b32 s61, %[so0]\n"
      "v_mfma_f32_16x16x32_bf16 v[168:171], a[104:107], a[20:23], v[168:171]\n"
      "s_and_b32 s62, s62, 0x1ffff\n"
      "s_add_u32 s77, %[wb], 0x10000\n"
      "v_cndmask_b32 v114, 0, v113, vcc\n"
      "v_mfma_f32_16x16x32_bf16 v[176:179], a[104:107], a[24:27], v[176:179]\n"
      "s_add_u32 s60, s60, %[inc]\n"
      "s_mov_b32 s70, 300\n"
      "v_accvgpr_write_b32 a200, v114\n"
      "v_accvgpr_write_b32 a201, v114\n"
      "v_mfma_f32_16x16x32_bf16 v[184:187], a[104:107], a[28:31], v[184:187]\n"
      "s_mov_b32 s80, 0x12a\n"
      "v_accvgpr_write_b32 a202, v114\n"
      "v_accvgpr_write_b32 a203, v114\n"
      "s_and_b32 s43, %[blk], 7\n"
      "v_mfma_f32_16x16x32_bf16 v[164:167], a[108:111], a[16:19], v[164:167]\n"
      "s_mov_b32 s81, 0x0\n"
      "v_cmp_eq_u32 vcc, 8, v112\n"
      "s_and_b32 s38, %[blk], -32\n"
      "v_mfma_f32_16x16x32_bf16 v[172:175], a[108:111], a[20:23], v[172:175]\n"
      "s_mov_b32 s101, 1000\n"
      "s_lshl2_add_u32 s38, s43, s38\n"
      "v_mfma_f32_16x16x32_bf16 v[180:183], a[108:111], a[24:27], v[180:183]\n"
      "s_mov_b32 s98, 110\n"
      "v_cndmask_b32 v114, 0, v113, vcc\n"
      "s_mul_i32 s38, s38, 52\n"
      "v_mfma_f32_16x16x32_bf16 v[188:191], a[108:111], a[28:31], v[188:191]\n"
      "s_waitcnt lgkmcnt(0)\n"
      "s_mov_b32 s93, 28\n"
      "v_accvgpr_write_b32 a204, v114\n"
      "v_accvgpr_write_b32 a205, v114\n"
      "s_lshr_b32 s38, s38, 8\n"
      "v_mfma_f32_16x16x32_bf16 v[160:163], a[112:115], a[32:35], v[160:163]\n"
      "s_mov_b32 s99, 27\n"
      "v_accvgpr_write_b32 a206, v114\n"
      "v_accvgpr_write_b32 a207, v114\n"
      "s_add_u32 s38, s38, 331\n"
      "v_mfma_f32_16x16x32_bf16 v[168:171], a[112:115], a[36:39], v[168:171]\n"
      "s_mov_b32 s100, 285\n"
      "v_cmp_eq_u32 vcc, 12, v112\n"
      "s_mul_i32 s43, s43, 5\n"
      "v_mfma_f32_16x16x32_bf16 v[176:179], a[112:115], a[40:43], v[176:179]\n"
      "s_lshl_b32 s91, %[wb], 18\n"
      "s_add_u32 s43, s43, s44\n"
      "v_mfma_f32_16x16x32_bf16 v[184:187], a[112:115], a[44:47], v[184:187]\n"
      "s_sub_u32 s91, 0x10000000, s91\n"
      "v_cndmask_b32 v114, 0, v113, vcc\n"
      "s_cmp_gt_u32 s44, 5\n"
      "s_subb_u32 s43, s43, 0\n"
      "v_mfma_f32_16x16x32_bf16 v[164:167], a[116:119], a[32:35], v[164:167]\n"
      "s_mov_b32 s86, 110\n"
      "v_accvgpr_write_b32 a208, v114\n"
      "v_accvgpr_write_b32 a209, v114\n"
      "s_mul_i32 s40, s43, 43\n"
      "v_mfma_f32_16x16x32_bf16 v[172:175], a[116:119], a[36:39], v[172:175]\n"
      "s_mov_b32 s87, 0\n"
      "v_accvgpr_write_b32 a210, v114\n"
      "v_accvgpr_write_b32 a211, v114\n"
      "s_lshr_b32 s40, s40, 8\n"
      "s_waitcnt vmcnt(11)\n"
      "s_barrier\n"
      "v_mfma_f32_16x16x32_bf16 v[180:183], a[116:119], a[40:43], v[180:183]\n"
      "s_mov_b32 s26, 0x8000000\n"
      "ds_read_b128 a[64:67], v242 offset:0\n"
      "s_mul_i32 s40, s40, -6\n"
      "v_mfma_f32_16x16x32_bf16 v[188:191], a[116:119], a[44:47], v[188:191]\n"
      "s_mov_b32 s27, 0x20000\n"
      "ds_read_b128 a[68:71], v242 offset:4096\n"
      "s_add_u32 s40, s40, s43\n"
      "v_mfma_f32_16x16x32_bf16 v[160:163], a[120:123], a[48:51], v[160:163]\n"
      "s_mov_b32 s24, s8\n"
      "ds_read_b128 a[72:75], v243 offset:0\n"
      "s_lshl_b32 s44, s44, 2\n"
      "v_mfma_f32_16x16x32_bf16 v[168:171], a[120:123], a[52:55], v[168:171]\n"
      "s_and_b32 s25, s9, 0xffff\n"
      "ds_read_b128 a[76:79], v243 offset:4096\n"
      "s_lshr_b32 s43, 0x70600000, s44\n"
      "v_mfma_f32_16x16x32_bf16 v[176:179], a[120:123], a[56:59], v[176:179]\n"
      "ds_read_b128 a[80:83], v242 offset:1024\n"
      "s_and_b32 s43, s43, 7\n"
      "v_mfma_f32_16x16x32_bf16 v[184:187], a[120:123], a[60:63], v[184:187]\n"
      "ds_read_b128 a[84:87], v242 offset:5120\n"
      "s_max_u32 s40, s40, s43\n"
      "v_mfma_f32_16x16x32_bf16 v[164:167], a[124:127], a[48:51], v[164:167]\n"
      "ds_read_b128 a[88:91], v243 offset:1024\n"
      "s_add_u32 s40, s40, 4\n"
      "v_mfma_f32_16x16x32_bf16 v[172:175], a[124:127], a[52:55], v[172:175]\n"
      "ds_read_b128 a[92:95], v243 offset:5120\n"
      "s_and_b32 s43, %[blk], 8\n"
      "v_mfma_f32_16x16x32_bf16 v[180:183], a[124:127], a[56:59], v[180:183]\n"
      "s_mul_i32 s43, s43, 768\n"
      "v_mfma_f32_16x16x32_bf16 v[188:191], a[124:127], a[60:63], v[188:191]\n"
      "s_cmp_eq_u32 s43, %[wb]\n"
      "s_cselect_b32 s42, 0, 0xff800000\n"
      "s_nop 3\n"
      "v_max3_f32 v112, v160, v161, v162\n"
      "v_max3_f32 v113, v168, v169, v170\n"
      "v_max3_f32 v114, v176, v177, v178\n"
      "v_max3_f32 v115, v184, v185, v186\n"
      "v_max3_f32 v112, v112, v163, v164\n"
      "v_max3_f32 v113, v113, v171, v172\n"
      "v_max3_f32 v114, v114, v179, v180\n"
      "v_max3_f32 v115, v115, v187, v188\n"
      "v_max3_f32 v112, v112, v165, v166\n"
      "v_max3_f32 v113, v113, v173, v174\n"
      "v_max3_f32 v114, v114, v181, v182\n"
      "v_max3_f32 v115, v115, v189, v190\n"
      "v_max_f32 v112, v112, v167\n"
      "v_max_f32 v113, v113, v175\n"
      "v_max_f32 v114, v114, v183\n"
      "v_max_f32 v115, v115, v191\n"
      "v_mov_b32 v208, v112\n"
      "v_mov_b32 v209, v113\n"
      "v_mov_b32 v210, v114\n"
      "v_mov_b32 v211, v115\n"
      "v_permlane16_swap_b32 v112, v208\n"
      "v_permlane16_swap_b32 v113, v209\n"
      "v_permlane16_swap_b32 v114, v210\n"
      "v_permlane16_swap_b32 v115, v211\n"
      "v_max_f32 v112, v112, v208\n"
      "v_max_f32 v113, v113, v209\n"
      "v_max_f32 v114, v114, v210\n"
      "v_max_f32 v115, v115, v211\n"
      "v_mov_b32 v208, v112\n"
      "v_mov_b32 v209, v113\n"
      "v_mov_b32 v210, v114\n"
      "v_mov_b32 v211, v115\n"
      "v_permlane32_swap_b32 v112, v208\n"
      "v_permlane32_swap_b32 v113, v209\n"
      "v_permlane32_swap_b32 v114, v210\n"
      "v_permlane32_swap_b32 v115, v211\n"
      "v_max_f32 v112, v112, v208\n"
      "v_max_f32 v113, v113, v209\n"
      "v_max_f32 v114, v114, v210\n"
      "v_max_f32 v115, v115, v211\n"
      "v_sub_f32 v112, 0xc1800000, v112\n"
      "v_sub_f32 v113, 0xc1800000, v113\n"
      "v_sub_f32 v114, 0xc1800000, v114\n"
      "v_sub_f32 v115, 0xc1800000, v115\n"
      "v_floor_f32 v112, v112\n"
      "v_floor_f32 v113, v113\n"
      "v_floor_f32 v114, v114\n"
      "v_floor_f32 v115, v115\n"
      "v_min_f32 v224, 0x7f000000, v112\n"
      "v_min_f32 v228, 0x7f000000, v113\n"
      "v_min_f32 v232, 0x7f000000, v114\n"
      "v_min_f32 v236, 0x7f000000, v115\n"
      "v_add_f32 v112, s42, v224\n"
      "v_add_f32 v113, s42, v228\n"
      "v_add_f32 v114, s42, v232\n"
      "v_add_f32 v115, s42, v236\n"
      "v_mov_b32 v225, v224\n"
      "v_mov_b32 v229, v228\n"
      "v_mov_b32 v233, v232\n"
      "v_mov_b32 v237, v236\n"
      "v_mov_b64 v[226:227], v[224:225]\n"
      "v_mov_b64 v[230:231], v[228:229]\n"
      "v_mov_b64 v[234:235], v[232:233]\n"
      "v_mov_b64 v[238:239], v[236:237]\n"
      "s_add_u32 m0, s77, 16384\n"
      "v_pk_add_f32 v[160:161], v[160:161], v[112:113] op_sel:[0,0] op_sel_hi:[1,0]\n"
      "buffer_load_dwordx4 v249, s[24:27], s61 offen nt lds\n"
      "v_pk_add_f32 v[162:163], v[162:163], v[112:113] op_sel:[0,0] op_sel_hi:[1,0]\n"
      "v_pk_add_f32 v[164:165], v[164:165], v[112:113] op_sel:[0,0] op_sel_hi:[1,0]\n"
      "v_pk_add_f32 v[166:167], v[166:167], v[112:113] op_sel:[0,0] op_sel_hi:[1,0]\n"
      "s_add_u32 m0, s77, 17408\n"
      "v_pk_add_f32 v[168:169], v[168:169], v[112:113] op_sel:[0,1] op_sel_hi:[1,1]\n"
      "buffer_load_dwordx4 v121, s[24:27], s61 offen nt lds\n"
      "v_pk_add_f32 v[170:171], v[170:171], v[112:113] op_sel:[0,1] op_sel_hi:[1,1]\n"
      "v_pk_add_f32 v[172:173], v[172:173], v[112:113] op_sel:[0,1] op_sel_hi:[1,1]\n"
      "v_pk_add_f32 v[174:175], v[174:175], v[112:113] op_sel:[0,1] op_sel_hi:[1,1]\n"
      "s_add_u32 m0, s77, 24576\n"
      "v_pk_add_f32 v[176:177], v[176:177], v[114:115] op_sel:[0,0] op_sel_hi:[1,0]\n"
      "buffer_load_dwordx4 v122, s[24:27], s61 offen nt lds\n"
      "v_pk_add_f32 v[178:179], v[178:179], v[114:115] op_sel:[0,0] op_sel_hi:[1,0]\n"
      "v_pk_add_f32 v[180:181], v[180:181], v[114:115] op_sel:[0,0] op_sel_hi:[1,0]\n"
      "v_pk_add_f32 v[182:183], v[182:183], v[114:115] op_sel:[0,0] op_sel_hi:[1,0]\n"
      "s_add_u32 m0, s77, 25600\n"
      "v_pk_add_f32 v[184:185], v[184:185], v[114:115] op_sel:[0,1] op_sel_hi:[1,1]\n"
      "buffer_load_dwordx4 v123, s[24:27], s61 offen nt lds\n"
      "v_pk_add_f32 v[186:187], v[186:187], v[114:115] op_sel:[0,1] op_sel_hi:[1,1]\n"
      "v_pk_add_f32 v[188:189], v[188:189], v[114:115] op_sel:[0,1] op_sel_hi:[1,1]\n"
      "v_pk_add_f32 v[190:191], v[190:191], v[114:115] op_sel:[0,1] op_sel_hi:[1,1]\n"
      "s_add_u32 s77, s77, 0x8000\n"
      "s_and_b32 s77, s77, 0x1ffff\n"
      "s_add_u32 s61, s61, %[inc]\n"
      "v_exp_f32 v112, v160\n"
      "v_exp_f32 v113, v161\n"
      "v_exp_f32 v114, v162\n"
      "v_cvt_pk_bf16_f32 v208, v112, v113\n"
      "v_exp_f32 v115, v163\n"
      "v_exp_f32 v112, v164\n"
      "v_cvt_pk_bf16_f32 v209, v114, v115\n"
      "v_exp_f32 v113, v165\n"
      "v_or_b32 v116, v208, v209\n"
      "v_exp_f32 v114, v166\n"
      "v_cvt_pk_bf16_f32 v210, v112, v113\n"
      "v_exp_f32 v115, v167\n"
      "v_exp_f32 v112, v168\n"
      "v_cvt_pk_bf16_f32 v211, v114, v115\n"
      "v_exp_f32 v113, v169\n"
      "v_or3_b32 v116, v116, v210, v211\n"
      "v_exp_f32 v114, v170\n"
      "v_cvt_pk_bf16_f32 v212, v112, v113\n"
      "v_exp_f32 v115, v171\n"
      "v_exp_f32 v112, v172\n"
      "v_cvt_pk_bf16_f32 v213, v114, v115\n"
      "v_exp_f32 v113, v173\n"
      "v_or3_b32 v116, v116, v212, v213\n"
      "s_waitcnt lgkmcnt(0)\n"
      "v_cvt_pk_bf16_f32 v214, v112, v113\n"
      "s_sub_u32 s63, s40, 1\n"
      "s_branch ENTRY_%=\n"
      "LOOP1_%=:\n"
      "s_waitcnt vmcnt(12) lgkmcnt(0)\n"
      "s_sub_u32 s67, s68, s90\n"
      "s_cmp_lt_i32 s67, 0\n"
      "s_cbranch_scc1 GW0_%=\n"
      "GX0_%=:\n"
      "s_barrier\n"
      "v_mfma_f32_16x16x32_bf16 v[40:43], a[96:99], v[216:219], v[40:43]\n"
      "s_memrealtime s[68:69]\n"
      "buffer_load_dword v250, v251, s[28:31], 0 offen sc1\n"
      "ds_read_b128 a[64:67], v242 offset:0\n"
      "v_exp_f32 v114, v166\n"
      "v_mfma_f32_16x16x32_bf16 v[44:47], a[96:99], v[220:223], v[44:47]\n"
      "s_add_u32 m0, s77, 16384\n"
      "ds_read_b128 a[68:71], v242 offset:4096\n"
      "ds_read_b128 a[72:75], v243 offset:0\n"
      "v_mfma_f32_16x16x32_bf16 v[60:63], a[100:103], v[220:223], v[60:63]\n"
      "buffer_load_dwordx4 v249, s[24:27], s61 offen nt lds\n"
      "v_exp_f32 v115, v167\n"
      "v_mfma_f32_16x16x32_bf16 v[56:59], a[100:103], v[216:219], v[56:59]\n"
      "s_add_u32 m0, s77, 17408\n"
      "ds_read_b128 a[76:79], v243 offset:4096\n"
      "v_cvt_pk_bf16_f32 v211, v114, v115\n"
      "v_mfma_f32_16x16x32_bf16 a[136:139], a[104:107], v[216:219], a[136:139]\n"
      "buffer_load_dwordx4 v121, s[24:27], s61 offen nt lds\n"
      "v_exp_f32 v112, v168\n"
      "v_mfma_f32_16x16x32_bf16 a[140:143], a[104:107], v[220:223], a[140:143]\n"
      "s_add_u32 m0, s77, 24576\n"
      "ds_read_b128 a[80:83], v242 offset:1024\n"
      "v_or3_b32 v116, v116, v210, v211\n"
      "v_mfma_f32_16x16x32_bf16 a[156:159], a[108:111], v[220:223], a[156:159]\n"
      "buffer_load_dwordx4 v122, s[24:27], s61 offen nt lds\n"
      "v_exp_f32 v113, v169\n"
      "v_mfma_f32_16x16x32_bf16 a[152:155], a[108:111], v[216:219], a[152:155]\n"
      "s_add_u32 m0, s77, 25600\n"
      "ds_read_b128 a[84:87], v242 offset:5120\n"
      "v_cvt_pk_bf16_f32 v212, v112, v113\n"
      "v_mfma_f32_16x16x32_bf16 v[72:75], a[112:115], v[216:219], v[72:75]\n"
      "s_add_u32 s77, s77, 0x8000\n"
      "buffer_load_dwordx4 v123, s[24:27], s61 offen nt lds\n"
      "v_exp_f32 v114, v170\n"
      "v_mfma_f32_16x16x32_bf16 v[76:79], a[112:115], v[220:223], v[76:79]\n"
      "s_and_b32 s77, s77, 0x1ffff\n"
      "ds_read_b128 a[88:91], v243 offset:1024\n"
      "ds_read_b128 a[92:95], v243 offset:5120\n"
      "v_mfma_f32_16x16x32_bf16 v[92:95], a[116:119], v[220:223], v[92:95]\n"
      "s_add_u32 s61, s61, %[inc]\n"
      "v_exp_f32 v115, v171\n"
      "v_mfma_f32_16x16x32_bf16 v[88:91], a[116:119], v[216:219], v[88:91]\n"
      "v_cvt_pk_bf16_f32 v213, v114, v115\n"
      "v_mfma_f32_16x16x32_bf16 a[168:171], a[120:123], v[216:219], a[168:171]\n"
      "v_exp_f32 v112, v172\n"
      "v_mfma_f32_16x16x32_bf16 a[172:175], a[120:123], v[220:223], a[172:175]\n"
      "v_or3_b32 v116, v116, v212, v213\n"
      "v_mfma_f32_16x16x32_bf16 a[188:191], a[124:127], v[220:223], a[188:191]\n"
      "v_exp_f32 v113, v173\n"
      "v_mfma_f32_16x16x32_bf16 a[184:187], a[124:127], v[216:219], a[184:187]\n"
      "s_waitcnt lgkmcnt(0)\n"
      "v_cvt_pk_bf16_f32 v214, v112, v113\n"
      "ENTRY_%=:\n"
      "v_mfma_f32_16x16x32_bf16 v[128:131], a[64:67], a[0:3], v[224:227]\n"
      "s_sub_u32 s67, s68, s66\n"
      "v_exp_f32 v114, v174\n"
      "v_mfma_f32_16x16x32_bf16 v[128:131], a[72:75], a[16:19], v[128:131]\n"
      "s_cmp_gt_i32 s67, s70\n"
      "ds_read_b64_tr_b16 a[96:97], v244 offset:0\n"
      "ds_read_b64_tr_b16 a[98:99], v244 offset:4096\n"
      "v_mfma_f32_16x16x32_bf16 v[128:131], a[80:83], a[32:35], v[128:131]\n"
      "s_cselect_b32 s69, s86, 0\n"
      "v_exp_f32 v115, v175\n"
      "v_mfma_f32_16x16x32_bf16 v[128:131], a[88:91], a[48:51], v[128:131]\n"
      "s_add_u32 s66, s66, s69\n"
      "ds_read_b64_tr_b16 a[100:101], v245 offset:0\n"
      "v_cvt_pk_bf16_f32 v215, v114, v115\n"
      "v_mfma_f32_16x16x32_bf16 v[132:135], a[92:95], a[48:51], v[224:227]\n"
      "s_add_u32 s67, s67, s101\n"
      "v_exp_f32 v112, v176\n"
      "v_mfma_f32_16x16x32_bf16 v[132:135], a[84:87], a[32:35], v[132:135]\n"
      "s_min_i32 s70, s70, s67\n"
      "ds_read_b64_tr_b16 a[102:103], v245 offset:4096\n"
      "v_or3_b32 v116, v116, v214, v215\n"
      "v_mfma_f32_16x16x32_bf16 v[132:135], a[76:79], a[16:19], v[132:135]\n"
      "s_max_i32 s70, s70, 85\n"
      "v_exp_f32 v113, v177\n"
      "v_mfma_f32_16x16x32_bf16 v[132:135], a[68:71], a[0:3], v[132:135]\n"
      "s_add_u32 s66, s66, s86\n"
      "ds_read_b64_tr_b16 a[104:105], v246 offset:0\n"
      "v_cvt_pk_bf16_f32 v216, v112, v113\n"
      "v_mfma_f32_16x16x32_bf16 v[140:143], a[68:71], a[4:7], v[228:231]\n"
      "s_and_b32 s76, s80, 31\n"
      "v_exp_f32 v114, v178\n"
      "v_mfma_f32_16x16x32_bf16 v[140:143], a[76:79], a[20:23], v[140:143]\n"
      "s_lshr_b64 s[80:81], s[80:81], 5\n"
      "ds_read_b64_tr_b16 a[106:107], v246 offset:4096\n"
      "ds_read_b64_tr_b16 a[108:109], v247 offset:0\n"
      "v_mfma_f32_16x16x32_bf16 v[140:143], a[84:87], a[36:39], v[140:143]\n"
      "s_add_u32 s66, s66, s76\n"
      "v_exp_f32 v115, v179\n"
      "v_mfma_f32_16x16x32_bf16 v[140:143], a[92:95], a[52:55], v[140:143]\n"
      "s_sub_u32 s67, s68, s82\n"
      "ds_read_b64_tr_b16 a[110:111], v247 offset:4096\n"
      "v_cvt_pk_bf16_f32 v217, v114, v115\n"
      "v_mfma_f32_16x16x32_bf16 v[136:139], a[88:91], a[52:55], v[228:231]\n"
      "s_min_i32 s76, s67, s83\n"
      "v_exp_f32 v112, v180\n"
      "v_mfma_f32_16x16x32_bf16 v[136:139], a[80:83], a[36:39], v[136:139]\n"
      "s_mov_b32 s83, s67\n"
      "ds_read_b64_tr_b16 a[112:113], v244 offset:1024\n"
      "v_or3_b32 v116, v116, v216, v217\n"
      "v_mfma_f32_16x16x32_bf16 v[136:139], a[72:75], a[20:23], v[136:139]\n"
      "s_sub_u32 s76, s76, 36\n"
      "v_exp_f32 v113, v181\n"
      "v_mfma_f32_16x16x32_bf16 v[136:139], a[64:67], a[4:7], v[136:139]\n"
      "s_min_i32 s76, s76, 148\n"
      "ds_read_b64_tr_b16 a[114:115], v244 offset:5120\n"
      "v_cvt_pk_bf16_f32 v218, v112, v113\n"
      "v_mfma_f32_16x16x32_bf16 v[144:147], a[64:67], a[8:11], v[232:235]\n"
      "s_max_i32 s76, s76, 70\n"
      "v_exp_f32 v114, v182\n"
      "v_mfma_f32_16x16x32_bf16 v[144:147], a[72:75], a[24:27], v[144:147]\n"
      "s_mov_b32 s82, s68\n"
      "ds_read_b64_tr_b16 a[116:117], v245 offset:1024\n"
      "ds_read_b64_tr_b16 a[118:119], v245 offset:5120\n"
      "v_mfma_f32_16x16x32_bf16 v[144:147], a[80:83], a[40:43], v[144:147]\n"
      "s_add_u32 s65, s82, s76\n"
      "v_exp_f32 v115, v183\n"
      "v_mfma_f32_16x16x32_bf16 v[144:147], a[88:91], a[56:59], v[144:147]\n"
      "s_sub_u32 s93, s93, 1\n"
      "ds_read_b64_tr_b16 a[120:121], v246 offset:1024\n"
      "v_cvt_pk_bf16_f32 v219, v114, v115\n"
      "v_mfma_f32_16x16x32_bf16 v[148:151], a[92:95], a[56:59], v[232:235]\n"
      "s_cmp_le_i32 s93, 14\n"
      "v_exp_f32 v112, v184\n"
      "v_mfma_f32_16x16x32_bf16 v[148:151], a[84:87], a[40:43], v[148:151]\n"
      "s_cselect_b32 s86, 107, s86\n"
      "ds_read_b64_tr_b16 a[122:123], v246 offset:5120\n"
      "v_or3_b32 v116, v116, v218, v219\n"
      "v_mfma_f32_16x16x32_bf16 v[148:151], a[76:79], a[24:27], v[148:151]\n"
      "s_sub_u32 s67, s65, s66\n"
      "v_exp_f32 v113, v185\n"
      "v_mfma_f32_16x16x32_bf16 v[148:151], a[68:71], a[8:11], v[148:151]\n"
      "s_cmp_lt_i32 s67, 0\n"
      "ds_read_b64_tr_b16 a[124:125], v247 offset:1024\n"
      "v_cvt_pk_bf16_f32 v220, v112, v113\n"
      "v_mfma_f32_16x16x32_bf16 v[156:159], a[68:71], a[12:15], v[236:239]\n"
      "s_cselect_b32 s88, s66, s65\n"
      "v_exp_f32 v114, v186\n"
      "v_mfma_f32_16x16x32_bf16 v[156:159], a[76:79], a[28:31], v[156:159]\n"
      "s_add_u32 s90, s88, s91\n"
      "ds_read_b64_tr_b16 a[126:127], v247 offset:5120\n"
      "v_mfma_f32_16x16x32_bf16 v[156:159], a[84:87], a[44:47], v[156:159]\n"
      "v_exp_f32 v115, v187\n"
      "v_mfma_f32_16x16x32_bf16 v[156:159], a[92:95], a[60:63], v[156:159]\n"
      "v_cvt_pk_bf16_f32 v221, v114, v115\n"
      "v_mfma_f32_16x16x32_bf16 v[152:155], a[88:91], a[60:63], v[236:239]\n"
      "v_exp_f32 v112, v188\n"
      "v_mfma_f32_16x16x32_bf16 v[152:155], a[80:83], a[44:47], v[152:155]\n"
      "v_or3_b32 v116, v116, v220, v221\n"
      "v_mfma_f32_16x16x32_bf16 v[152:155], a[72:75], a[28:31], v[152:155]\n"
      "v_exp_f32 v113, v189\n"
      "v_mfma_f32_16x16x32_bf16 v[152:155], a[64:67], a[12:15], v[152:155]\n"
      "s_waitcnt lgkmcnt(10)\n"
      "v_cvt_pk_bf16_f32 v222, v112, v113\n"
      "v_mfma_f32_16x16x32_bf16 v[32:35], a[96:99], v[192:195], v[32:35]\n"
      "v_exp_f32 v114, v190\n"
      "v_mfma_f32_16x16x32_bf16 v[48:51], a[100:103], v[192:195], v[48:51]\n"
      "v_mfma_f32_16x16x32_bf16 a[128:131], a[104:107], v[192:195], a[128:131]\n"
      "s_waitcnt lgkmcnt(6)\n"
      "v_exp_f32 v115, v191\n"
      "v_mfma_f32_16x16x32_bf16 a[144:147], a[108:111], v[192:195], a[144:147]\n"
      "v_cvt_pk_bf16_f32 v223, v114, v115\n"
      "v_mfma_f32_16x16x32_bf16 v[64:67], a[112:115], v[192:195], v[64:67]\n"
      "s_waitcnt lgkmcnt(2)\n"
      "v_mfma_f32_16x16x32_bf16 v[80:83], a[116:119], v[192:195], v[80:83]\n"
      "v_or3_b32 v116, v116, v222, v223\n"
      "v_mfma_f32_16x16x32_bf16 a[160:163], a[120:123], v[192:195], a[160:163]\n"
      "s_mov_b32 s64, 0\n"
      "s_waitcnt lgkmcnt(0)\n"
      "v_and_b32 v116, 0x40004000, v116\n"
      "v_mfma_f32_16x16x32_bf16 a[176:179], a[124:127], v[192:195], a[176:179]\n"
      "v_cmp_ne_u32 vcc, 0, v116\n"
      "s_cbranch_vccnz FB1_%=\n"
      "RET1_0_%=:\n"
      "v_mfma_f32_16x16x32_bf16 a[180:183], a[124:127], v[196:199], a[180:183]\n"
      "v_exp_f32 v112, v128\n"
      "s_barrier\n"
      "v_mfma_f32_16x16x32_bf16 a[164:167], a[120:123], v[196:199], a[164:167]\n"
      "v_mfma_f32_16x16x32_bf16 a[192:195], a[196:199], v[208:211], a[192:195]\n"
      "s_add_u32 m0, s62, 0\n"
      "v_exp_f32 v113, v129\n"
      "v_mfma_f32_16x16x32_bf16 v[84:87], a[116:119], v[196:199], v[84:87]\n"
      "buffer_load_dwordx4 v248, s[20:23], s60 offen nt lds\n"
      "v_cvt_pk_bf16_f32 v192, v112, v113\n"
      "v_mfma_f32_16x16x32_bf16 v[68:71], a[112:115], v[196:199], v[68:71]\n"
      "s_add_u32 m0, s62, 896\n"
      "v_exp_f32 v114, v130\n"
      "v_mfma_f32_16x16x32_bf16 a[192:195], a[200:203], v[212:215], a[192:195]\n"
      "buffer_load_dwordx4 v248, s[20:23], s60 offen offset:128 nt lds\n"
      "v_mfma_f32_16x16x32_bf16 a[148:151], a[108:111], v[196:199], a[148:151]\n"
      "s_add_u32 m0, s62, 8192\n"
      "v_exp_f32 v115, v131\n"
      "v_mfma_f32_16x16x32_bf16 a[132:135], a[104:107], v[196:199], a[132:135]\n"
      "s_memrealtime s[68:69]\n"
      "buffer_load_dwordx4 v120, s[20:23], s60 offen nt lds\n"
      "v_cvt_pk_bf16_f32 v193, v114, v115\n"
      "v_mfma_f32_16x16x32_bf16 a[192:195], a[204:207], v[216:219], a[192:195]\n"
      "s_add_u32 m0, s62, 9088\n"
      "v_exp_f32 v112, v132\n"
      "v_mfma_f32_16x16x32_bf16 v[52:55], a[100:103], v[196:199], v[52:55]\n"
      "s_add_u32 s62, s62, 0x8000\n"
      "buffer_load_dwordx4 v120, s[20:23], s60 offen offset:128 nt lds\n"
      "v_or_b32 v116, v192, v193\n"
      "v_mfma_f32_16x16x32_bf16 v[36:39], a[96:99], v[196:199], v[36:39]\n"
      "s_and_b32 s62, s62, 0x1ffff\n"
      "v_exp_f32 v113, v133\n"
      "v_mfma_f32_16x16x32_bf16 a[192:195], a[208:211], v[220:223], a[192:195]\n"
      "s_add_u32 s60, s60, s78\n"
      "v_cvt_pk_bf16_f32 v194, v112, v113\n"
      "s_waitcnt vmcnt(12) lgkmcnt(0)\n"
      "s_sub_u32 s67, s68, s90\n"
      "s_cmp_lt_i32 s67, 0\n"
      "s_cbranch_scc1 GW1_%=\n"
      "GX1_%=:\n"
      "s_barrier\n"
      "v_mfma_f32_16x16x32_bf16 v[40:43], a[96:99], v[200:203], v[40:43]\n"
      "s_memrealtime s[68:69]\n"
      "ds_read_b128 a[64:67], v242 offset:32768\n"
      "v_exp_f32 v114, v134\n"
      "v_mfma_f32_16x16x32_bf16 v[44:47], a[96:99], v[204:207], v[44:47]\n"
      "s_add_u32 m0, s77, 16384\n"
      "ds_read_b128 a[68:71], v242 offset:36864\n"
      "ds_read_b128 a[72:75], v243 offset:32768\n"
      "v_mfma_f32_16x16x32_bf16 v[60:63], a[100:103], v[204:207], v[60:63]\n"
      "buffer_load_dwordx4 v249, s[24:27], s61 offen nt lds\n"
      "v_exp_f32 v115, v135\n"
      "v_mfma_f32_16x16x32_bf16 v[56:59], a[100:103], v[200:203], v[56:59]\n"
      "s_add_u32 m0, s77, 17408\n"
      "ds_read_b128 a[76:79], v243 offset:36864\n"
      "v_cvt_pk_bf16_f32 v195, v114, v115\n"
      "v_mfma_f32_16x16x32_bf16 a[136:139], a[104:107], v[200:203], a[136:139]\n"
      "buffer_load_dwordx4 v121, s[24:27], s61 offen nt lds\n"
      "v_exp_f32 v112, v136\n"
      "v_mfma_f32_16x16x32_bf16 a[140:143], a[104:107], v[204:207], a[140:143]\n"
      "s_add_u32 m0, s77, 24576\n"
      "ds_read_b128 a[80:83], v242 offset:33792\n"
      "v_or3_b32 v116, v116, v194, v195\n"
      "v_mfma_f32_16x16x32_bf16 a[156:159], a[108:111], v[204:207], a[156:159]\n"
      "buffer_load_dwordx4 v122, s[24:27], s61 offen nt lds\n"
      "v_exp_f32 v113, v137\n"
      "v_mfma_f32_16x16x32_bf16 a[152:155], a[108:111], v[200:203], a[152:155]\n"
      "s_add_u32 m0, s77, 25600\n"
      "ds_read_b128 a[84:87], v242 offset:37888\n"
      "v_cvt_pk_bf16_f32 v196, v112, v113\n"
      "v_mfma_f32_16x16x32_bf16 v[72:75], a[112:115], v[200:203], v[72:75]\n"
      "s_add_u32 s77, s77, 0x8000\n"
      "buffer_load_dwordx4 v123, s[24:27], s61 offen nt lds\n"
      "v_exp_f32 v114, v138\n"
      "v_mfma_f32_16x16x32_bf16 v[76:79], a[112:115], v[204:207], v[76:79]\n"
      "s_and_b32 s77, s77, 0x1ffff\n"
      "ds_read_b128 a[88:91], v243 offset:33792\n"
      "ds_read_b128 a[92:95], v243 offset:37888\n"
      "v_mfma_f32_16x16x32_bf16 v[92:95], a[116:119], v[204:207], v[92:95]\n"
      "s_add_u32 s61, s61, s78\n"
      "v_exp_f32 v115, v139\n"
      "v_mfma_f32_16x16x32_bf16 v[88:91], a[116:119], v[200:203], v[88:91]\n"
      "v_cvt_pk_bf16_f32 v197, v114, v115\n"
      "v_mfma_f32_16x16x32_bf16 a[168:171], a[120:123], v[200:203], a[168:171]\n"
      "v_exp_f32 v112, v140\n"
      "v_mfma_f32_16x16x32_bf16 a[172:175], a[120:123], v[204:207], a[172:175]\n"
      "v_or3_b32 v116, v116, v196, v197\n"
      "v_mfma_f32_16x16x32_bf16 a[188:191], a[124:127], v[204:207], a[188:191]\n"
      "v_exp_f32 v113, v141\n"
      "v_mfma_f32_16x16x32_bf16 a[184:187], a[124:127], v[200:203], a[184:187]\n"
      "s_waitcnt lgkmcnt(0)\n"
      "v_cvt_pk_bf16_f32 v198, v112, v113\n"
      "v_mfma_f32_16x16x32_bf16 v[160:163], a[64:67], a[0:3], v[224:227]\n"
      "s_sub_u32 s67, s68, s66\n"
      "v_exp_f32 v114, v142\n"
      "v_mfma_f32_16x16x32_bf16 v[160:163], a[72:75], a[16:19], v[160:163]\n"
      "s_cmp_gt_i32 s67, s70\n"
      "ds_read_b64_tr_b16 a[96:97], v244 offset:32768\n"
      "ds_read_b64_tr_b16 a[98:99], v244 offset:36864\n"
      "v_mfma_f32_16x16x32_bf16 v[160:163], a[80:83], a[32:35], v[160:163]\n"
      "s_cselect_b32 s69, s86, 0\n"
      "v_exp_f32 v115, v143\n"
      "v_mfma_f32_16x16x32_bf16 v[160:163], a[88:91], a[48:51], v[160:163]\n"
      "s_add_u32 s66, s66, s69\n"
      "ds_read_b64_tr_b16 a[100:101], v245 offset:32768\n"
      "v_cvt_pk_bf16_f32 v199, v114, v115\n"
      "v_mfma_f32_16x16x32_bf16 v[164:167], a[92:95], a[48:51], v[224:227]\n"
      "s_add_u32 s67, s67, s101\n"
      "v_exp_f32 v112, v144\n"
      "v_mfma_f32_16x16x32_bf16 v[164:167], a[84:87], a[32:35], v[164:167]\n"
      "s_min_i32 s70, s70, s67\n"
      "ds_read_b64_tr_b16 a[102:103], v245 offset:36864\n"
      "v_or3_b32 v116, v116, v198, v199\n"
      "v_mfma_f32_16x16x32_bf16 v[164:167], a[76:79], a[16:19], v[164:167]\n"
      "s_max_i32 s70, s70, 85\n"
      "v_exp_f32 v113, v145\n"
      "v_mfma_f32_16x16x32_bf16 v[164:167], a[68:71], a[0:3], v[164:167]\n"
      "s_add_u32 s66, s66, s86\n"
      "ds_read_b64_tr_b16 a[104:105], v246 offset:32768\n"
      "v_cvt_pk_bf16_f32 v200, v112, v113\n"
      "v_mfma_f32_16x16x32_bf16 v[172:175], a[68:71], a[4:7], v[228:231]\n"
      "s_and_b32 s76, s80, 31\n"
      "v_exp_f32 v114, v146\n"
      "v_mfma_f32_16x16x32_bf16 v[172:175], a[76:79], a[20:23], v[172:175]\n"
      "s_lshr_b64 s[80:81], s[80:81], 5\n"
      "ds_read_b64_tr_b16 a[106:107], v246 offset:36864\n"
      "ds_read_b64_tr_b16 a[108:109], v247 offset:32768\n"
      "v_mfma_f32_16x16x32_bf16 v[172:175], a[84:87], a[36:39], v[172:175]\n"
      "s_add_u32 s66, s66, s76\n"
      "v_exp_f32 v115, v147\n"
      "v_mfma_f32_16x16x32_bf16 v[172:175], a[92:95], a[52:55], v[172:175]\n"
      "s_sub_u32 s67, s68, s82\n"
      "ds_read_b64_tr_b16 a[110:111], v247 offset:36864\n"
      "v_cvt_pk_bf16_f32 v201, v114, v115\n"
      "v_mfma_f32_16x16x32_bf16 v[168:171], a[88:91], a[52:55], v[228:231]\n"
      "s_min_i32 s76, s67, s83\n"
      "v_exp_f32 v112, v148\n"
      "v_mfma_f32_16x16x32_bf16 v[168:171], a[80:83], a[36:39], v[168:171]\n"
      "s_mov_b32 s83, s67\n"
      "ds_read_b64_tr_b16 a[112:113], v244 offset:33792\n"
      "v_or3_b32 v116, v116, v200, v201\n"
      "v_mfma_f32_16x16x32_bf16 v[168:171], a[72:75], a[20:23], v[168:171]\n"
      "s_sub_u32 s76, s76, 36\n"
      "v_exp_f32 v113, v149\n"
      "v_mfma_f32_16x16x32_bf16 v[168:171], a[64:67], a[4:7], v[168:171]\n"
      "s_min_i32 s76, s76, 148\n"
      "ds_read_b64_tr_b16 a[114:115], v244 offset:37888\n"
      "v_cvt_pk_bf16_f32 v202, v112, v113\n"
      "v_mfma_f32_16x16x32_bf16 v[176:179], a[64:67], a[8:11], v[232:235]\n"
      "s_max_i32 s76, s76, 70\n"
      "v_exp_f32 v114, v150\n"
      "v_mfma_f32_16x16x32_bf16 v[176:179], a[72:75], a[24:27], v[176:179]\n"
      "s_mov_b32 s82, s68\n"
      "ds_read_b64_tr_b16 a[116:117], v245 offset:33792\n"
      "ds_read_b64_tr_b16 a[118:119], v245 offset:37888\n"
      "v_mfma_f32_16x16x32_bf16 v[176:179], a[80:83], a[40:43], v[176:179]\n"
      "s_add_u32 s65, s82, s76\n"
      "v_exp_f32 v115, v151\n"
      "v_mfma_f32_16x16x32_bf16 v[176:179], a[88:91], a[56:59], v[176:179]\n"
      "s_sub_u32 s93, s93, 1\n"
      "ds_read_b64_tr_b16 a[120:121], v246 offset:33792\n"
      "v_cvt_pk_bf16_f32 v203, v114, v115\n"
      "v_mfma_f32_16x16x32_bf16 v[180:183], a[92:95], a[56:59], v[232:235]\n"
      "s_cmp_le_i32 s93, 14\n"
      "v_exp_f32 v112, v152\n"
      "v_mfma_f32_16x16x32_bf16 v[180:183], a[84:87], a[40:43], v[180:183]\n"
      "s_cselect_b32 s86, 107, s86\n"
      "ds_read_b64_tr_b16 a[122:123], v246 offset:37888\n"
      "v_or3_b32 v116, v116, v202, v203\n"
      "v_mfma_f32_16x16x32_bf16 v[180:183], a[76:79], a[24:27], v[180:183]\n"
      "s_sub_u32 s67, s65, s66\n"
      "v_exp_f32 v113, v153\n"
      "v_mfma_f32_16x16x32_bf16 v[180:183], a[68:71], a[8:11], v[180:183]\n"
      "s_cmp_lt_i32 s67, 0\n"
      "ds_read_b64_tr_b16 a[124:125], v247 offset:33792\n"
      "v_cvt_pk_bf16_f32 v204, v112, v113\n"
      "v_mfma_f32_16x16x32_bf16 v[188:191], a[68:71], a[12:15], v[236:239]\n"
      "s_cselect_b32 s88, s66, s65\n"
      "v_exp_f32 v114, v154\n"
      "v_mfma_f32_16x16x32_bf16 v[188:191], a[76:79], a[28:31], v[188:191]\n"
      "s_add_u32 s90, s88, s91\n"
      "ds_read_b64_tr_b16 a[126:127], v247 offset:37888\n"
      "v_mfma_f32_16x16x32_bf16 v[188:191], a[84:87], a[44:47], v[188:191]\n"
      "v_exp_f32 v115, v155\n"
      "v_mfma_f32_16x16x32_bf16 v[188:191], a[92:95], a[60:63], v[188:191]\n"
      "v_cvt_pk_bf16_f32 v205, v114, v115\n"
      "v_mfma_f32_16x16x32_bf16 v[184:187], a[88:91], a[60:63], v[236:239]\n"
      "v_exp_f32 v112, v156\n"
      "v_mfma_f32_16x16x32_bf16 v[184:187], a[80:83], a[44:47], v[184:187]\n"
      "v_or3_b32 v116, v116, v204, v205\n"
      "v_mfma_f32_16x16x32_bf16 v[184:187], a[72:75], a[28:31], v[184:187]\n"
      "v_exp_f32 v113, v157\n"
      "v_mfma_f32_16x16x32_bf16 v[184:187], a[64:67], a[12:15], v[184:187]\n"
      "s_waitcnt lgkmcnt(10)\n"
      "v_cvt_pk_bf16_f32 v206, v112, v113\n"
      "v_mfma_f32_16x16x32_bf16 v[32:35], a[96:99], v[208:211], v[32:35]\n"
      "v_exp_f32 v114, v158\n"
      "v_mfma_f32_16x16x32_bf16 v[48:51], a[100:103], v[208:211], v[48:51]\n"
      "v_mfma_f32_16x16x32_bf16 a[128:131], a[104:107], v[208:211], a[128:131]\n"
      "s_waitcnt lgkmcnt(6)\n"
      "v_exp_f32 v115, v159\n"
      "v_mfma_f32_16x16x32_bf16 a[144:147], a[108:111], v[208:211], a[144:147]\n"
      "v_cvt_pk_bf16_f32 v207, v114, v115\n"
      "v_mfma_f32_16x16x32_bf16 v[64:67], a[112:115], v[208:211], v[64:67]\n"
      "s_waitcnt lgkmcnt(2)\n"
      "v_mfma_f32_16x16x32_bf16 v[80:83], a[116:119], v[208:211], v[80:83]\n"
      "v_or3_b32 v116, v116, v206, v207\n"
      "v_mfma_f32_16x16x32_bf16 a[160:163], a[120:123], v[208:211], a[160:163]\n"
      "s_mov_b32 s64, 0\n"
      "s_waitcnt lgkmcnt(0)\n"
      "v_and_b32 v116, 0x40004000, v116\n"
      "v_mfma_f32_16x16x32_bf16 a[176:179], a[124:127], v[208:211], a[176:179]\n"
      "v_cmp_ne_u32 vcc, 0, v116\n"
      "s_cbranch_vccnz FB0_%=\n"
      "RET0_0_%=:\n"
      "v_mfma_f32_16x16x32_bf16 a[180:183], a[124:127], v[212:215], a[180:183]\n"
      "v_exp_f32 v112, v160\n"
      "s_barrier\n"
      "v_mfma_f32_16x16x32_bf16 a[164:167], a[120:123], v[212:215], a[164:167]\n"
      "v_xor_b32 v242, 0x10000, v242\n"
      "v_mfma_f32_16x16x32_bf16 a[192:195], a[196:199], v[192:195], a[192:195]\n"
      "s_add_u32 m0, s62, 0\n"
      "v_exp_f32 v113, v161\n"
      "v_mfma_f32_16x16x32_bf16 v[84:87], a[116:119], v[212:215], v[84:87]\n"
      "buffer_load_dwordx4 v248, s[20:23], s60 offen nt lds\n"
      "v_cvt_pk_bf16_f32 v208, v112, v113\n"
      "v_xor_b32 v243, 0x10000, v243\n"
      "v_mfma_f32_16x16x32_bf16 v[68:71], a[112:115], v[212:215], v[68:71]\n"
      "s_add_u32 m0, s62, 896\n"
      "v_exp_f32 v114, v162\n"
      "v_mfma_f32_16x16x32_bf16 a[192:195], a[200:203], v[196:199], a[192:195]\n"
      "buffer_load_dwordx4 v248, s[20:23], s60 offen offset:128 nt lds\n"
      "v_xor_b32 v244, 0x10000, v244\n"
      "v_mfma_f32_16x16x32_bf16 a[148:151], a[108:111], v[212:215], a[148:151]\n"
      "s_add_u32 m0, s62, 8192\n"
      "v_exp_f32 v115, v163\n"
      "v_mfma_f32_16x16x32_bf16 a[132:135], a[104:107], v[212:215], a[132:135]\n"
      "s_memrealtime s[68:69]\n"
      "buffer_load_dwordx4 v120, s[20:23], s60 offen nt lds\n"
      "v_cvt_pk_bf16_f32 v209, v114, v115\n"
      "v_xor_b32 v245, 0x10000, v245\n"
      "v_mfma_f32_16x16x32_bf16 a[192:195], a[204:207], v[200:203], a[192:195]\n"
      "s_add_u32 m0, s62, 9088\n"
      "v_exp_f32 v112, v164\n"
      "v_mfma_f32_16x16x32_bf16 v[52:55], a[100:103], v[212:215], v[52:55]\n"
      "s_add_u32 s62, s62, 0x8000\n"
      "buffer_load_dwordx4 v120, s[20:23], s60 offen offset:128 nt lds\n"
      "v_or_b32 v116, v208, v209\n"
      "v_xor_b32 v246, 0x10000, v246\n"
      "v_mfma_f32_16x16x32_bf16 v[36:39], a[96:99], v[212:215], v[36:39]\n"
      "s_and_b32 s62, s62, 0x1ffff\n"
      "s_add_u32 s60, s60, %[inc]\n"
      "v_exp_f32 v113, v165\n"
      "v_mfma_f32_16x16x32_bf16 a[192:195], a[208:211], v[204:207], a[192:195]\n"
      "s_sub_u32 s63, s63, 1\n"
      "v_cvt_pk_bf16_f32 v210, v112, v113\n"
      "v_xor_b32 v247, 0x10000, v247\n"
      "s_cbranch_scc0 LOOP1_%=\n"
      "v_readfirstlane_b32 s72, v240\n"
      "v_readfirstlane_b32 s79, v241\n"
      "s_waitcnt vmcnt(12) lgkmcnt(0)\n"
      "s_sub_u32 s67, s68, s90\n"
      "s_cmp_lt_i32 s67, 0\n"
      "s_cbranch_scc1 GW2_%=\n"
      "GX2_%=:\n"
      "s_barrier\n"
      "v_mfma_f32_16x16x32_bf16 v[40:43], a[96:99], v[216:219], v[40:43]\n"
      "s_memrealtime s[68:69]\n"
      "buffer_load_dword v250, v251, s[28:31], 0 offen sc1\n"
      "ds_read_b128 a[64:67], v242 offset:0\n"
      "v_exp_f32 v114, v166\n"
      "v_mfma_f32_16x16x32_bf16 v[44:47], a[96:99], v[220:223], v[44:47]\n"
      "s_add_u32 m0, s77, 16384\n"
      "ds_read_b128 a[68:71], v242 offset:4096\n"
      "ds_read_b128 a[72:75], v243 offset:0\n"
      "v_mfma_f32_16x16x32_bf16 v[60:63], a[100:103], v[220:223], v[60:63]\n"
      "buffer_load_dwordx4 v249, s[24:27], s61 offen nt lds\n"
      "v_exp_f32 v115, v167\n"
      "v_mfma_f32_16x16x32_bf16 v[56:59], a[100:103], v[216:219], v[56:59]\n"
      "s_add_u32 m0, s77, 17408\n"
      "ds_read_b128 a[76:79], v243 offset:4096\n"
      "v_cvt_pk_bf16_f32 v211, v114, v115\n"
      "v_mfma_f32_16x16x32_bf16 a[136:139], a[104:107], v[216:219], a[136:139]\n"
      "buffer_load_dwordx4 v121, s[24:27], s61 offen nt lds\n"
      "v_exp_f32 v112, v168\n"
      "v_mfma_f32_16x16x32_bf16 a[140:143], a[104:107], v[220:223], a[140:143]\n"
      "s_add_u32 m0, s77, 24576\n"
      "ds_read_b128 a[80:83], v242 offset:1024\n"
      "v_or3_b32 v116, v116, v210, v211\n"
      "v_mfma_f32_16x16x32_bf16 a[156:159], a[108:111], v[220:223], a[156:159]\n"
      "buffer_load_dwordx4 v122, s[24:27], s61 offen nt lds\n"
      "v_exp_f32 v113, v169\n"
      "v_mfma_f32_16x16x32_bf16 a[152:155], a[108:111], v[216:219], a[152:155]\n"
      "s_add_u32 m0, s77, 25600\n"
      "ds_read_b128 a[84:87], v242 offset:5120\n"
      "v_cvt_pk_bf16_f32 v212, v112, v113\n"
      "v_mfma_f32_16x16x32_bf16 v[72:75], a[112:115], v[216:219], v[72:75]\n"
      "s_add_u32 s77, s77, 0x8000\n"
      "buffer_load_dwordx4 v123, s[24:27], s61 offen nt lds\n"
      "v_exp_f32 v114, v170\n"
      "v_mfma_f32_16x16x32_bf16 v[76:79], a[112:115], v[220:223], v[76:79]\n"
      "s_and_b32 s77, s77, 0x1ffff\n"
      "ds_read_b128 a[88:91], v243 offset:1024\n"
      "ds_read_b128 a[92:95], v243 offset:5120\n"
      "v_mfma_f32_16x16x32_bf16 v[92:95], a[116:119], v[220:223], v[92:95]\n"
      "s_add_u32 s61, s61, %[inc]\n"
      "v_exp_f32 v115, v171\n"
      "v_mfma_f32_16x16x32_bf16 v[88:91], a[116:119], v[216:219], v[88:91]\n"
      "v_cvt_pk_bf16_f32 v213, v114, v115\n"
      "v_mfma_f32_16x16x32_bf16 a[168:171], a[120:123], v[216:219], a[168:171]\n"
      "v_exp_f32 v112, v172\n"
      "v_mfma_f32_16x16x32_bf16 a[172:175], a[120:123], v[220:223], a[172:175]\n"
      "v_or3_b32 v116, v116, v212, v213\n"
      "v_mfma_f32_16x16x32_bf16 a[188:191], a[124:127], v[220:223], a[188:191]\n"
      "v_exp_f32 v113, v173\n"
      "v_mfma_f32_16x16x32_bf16 a[184:187], a[124:127], v[216:219], a[184:187]\n"
      "s_waitcnt lgkmcnt(0)\n"
      "v_cvt_pk_bf16_f32 v214, v112, v113\n"
      "v_mfma_f32_16x16x32_bf16 v[128:131], a[64:67], a[0:3], v[224:227]\n"
      "s_sub_u32 s67, s68, s66\n"
      "v_exp_f32 v114, v174\n"
      "v_mfma_f32_16x16x32_bf16 v[128:131], a[72:75], a[16:19], v[128:131]\n"
      "s_cmp_gt_i32 s67, s70\n"
      "ds_read_b64_tr_b16 a[96:97], v244 offset:0\n"
      "ds_read_b64_tr_b16 a[98:99], v244 offset:4096\n"
      "v_mfma_f32_16x16x32_bf16 v[128:131], a[80:83], a[32:35], v[128:131]\n"
      "s_cselect_b32 s69, s86, 0\n"
      "v_exp_f32 v115, v175\n"
      "v_mfma_f32_16x16x32_bf16 v[128:131], a[88:91], a[48:51], v[128:131]\n"
      "s_add_u32 s66, s66, s69\n"
      "ds_read_b64_tr_b16 a[100:101], v245 offset:0\n"
      "v_cvt_pk_bf16_f32 v215, v114, v115\n"
      "v_mfma_f32_16x16x32_bf16 v[132:135], a[92:95], a[48:51], v[224:227]\n"
      "s_add_u32 s67, s67, s101\n"
      "v_exp_f32 v112, v176\n"
      "v_mfma_f32_16x16x32_bf16 v[132:135], a[84:87], a[32:35], v[132:135]\n"
      "s_min_i32 s70, s70, s67\n"
      "ds_read_b64_tr_b16 a[102:103], v245 offset:4096\n"
      "v_or3_b32 v116, v116, v214, v215\n"
      "v_mfma_f32_16x16x32_bf16 v[132:135], a[76:79], a[16:19], v[132:135]\n"
      "s_max_i32 s70, s70, 85\n"
      "v_exp_f32 v113, v177\n"
      "v_mfma_f32_16x16x32_bf16 v[132:135], a[68:71], a[0:3], v[132:135]\n"
      "s_add_u32 s66, s66, s86\n"
      "ds_read_b64_tr_b16 a[104:105], v246 offset:0\n"
      "v_cvt_pk_bf16_f32 v216, v112, v113\n"
      "v_mfma_f32_16x16x32_bf16 v[140:143], a[68:71], a[4:7], v[228:231]\n"
      "s_and_b32 s76, s80, 31\n"
      "v_exp_f32 v114, v178\n"
      "v_mfma_f32_16x16x32_bf16 v[140:143], a[76:79], a[20:23], v[140:143]\n"
      "s_lshr_b64 s[80:81], s[80:81], 5\n"
      "ds_read_b64_tr_b16 a[106:107], v246 offset:4096\n"
      "ds_read_b64_tr_b16 a[108:109], v247 offset:0\n"
      "v_mfma_f32_16x16x32_bf16 v[140:143], a[84:87], a[36:39], v[140:143]\n"
      "s_add_u32 s66, s66, s76\n"
      "v_exp_f32 v115, v179\n"
      "v_mfma_f32_16x16x32_bf16 v[140:143], a[92:95], a[52:55], v[140:143]\n"
      "s_sub_u32 s67, s68, s82\n"
      "ds_read_b64_tr_b16 a[110:111], v247 offset:4096\n"
      "v_cvt_pk_bf16_f32 v217, v114, v115\n"
      "v_mfma_f32_16x16x32_bf16 v[136:139], a[88:91], a[52:55], v[228:231]\n"
      "s_min_i32 s76, s67, s83\n"
      "v_exp_f32 v112, v180\n"
      "v_mfma_f32_16x16x32_bf16 v[136:139], a[80:83], a[36:39], v[136:139]\n"
      "s_mov_b32 s83, s67\n"
      "ds_read_b64_tr_b16 a[112:113], v244 offset:1024\n"
      "v_or3_b32 v116, v116, v216, v217\n"
      "v_mfma_f32_16x16x32_bf16 v[136:139], a[72:75], a[20:23], v[136:139]\n"
      "s_sub_u32 s76, s76, 36\n"
      "v_exp_f32 v113, v181\n"
      "v_mfma_f32_16x16x32_bf16 v[136:139], a[64:67], a[4:7], v[136:139]\n"
      "s_min_i32 s76, s76, 148\n"
      "ds_read_b64_tr_b16 a[114:115], v244 offset:5120\n"
      "v_cvt_pk_bf16_f32 v218, v112, v113\n"
      "v_mfma_f32_16x16x32_bf16 v[144:147], a[64:67], a[8:11], v[232:235]\n"
      "s_max_i32 s76, s76, 70\n"
      "v_exp_f32 v114, v182\n"
      "v_mfma_f32_16x16x32_bf16 v[144:147], a[72:75], a[24:27], v[144:147]\n"
      "s_mov_b32 s82, s68\n"
      "ds_read_b64_tr_b16 a[116:117], v245 offset:1024\n"
      "ds_read_b64_tr_b16 a[118:119], v245 offset:5120\n"
      "v_mfma_f32_16x16x32_bf16 v[144:147], a[80:83], a[40:43], v[144:147]\n"
      "s_lshr_b32 s69, s86, 1\n"
      "v_exp_f32 v115, v183\n"
      "v_mfma_f32_16x16x32_bf16 v[144:147], a[88:91], a[56:59], v[144:147]\n"
      "s_add_u32 s66, s66, s69\n"
      "ds_read_b64_tr_b16 a[120:121], v246 offset:1024\n"
      "v_cvt_pk_bf16_f32 v219, v114, v115\n"
      "v_mfma_f32_16x16x32_bf16 v[148:151], a[92:95], a[56:59], v[232:235]\n"
      "s_add_u32 s82, s82, s69\n"
      "v_exp_f32 v112, v184\n"
      "v_mfma_f32_16x16x32_bf16 v[148:151], a[84:87], a[40:43], v[148:151]\n"
      "s_add_u32 s65, s82, s76\n"
      "ds_read_b64_tr_b16 a[122:123], v246 offset:5120\n"
      "v_or3_b32 v116, v116, v218, v219\n"
      "v_mfma_f32_16x16x32_bf16 v[148:151], a[76:79], a[24:27], v[148:151]\n"
      "s_sub_u32 s93, s93, 1\n"
      "v_exp_f32 v113, v185\n"
      "v_mfma_f32_16x16x32_bf16 v[148:151], a[68:71], a[8:11], v[148:151]\n"
      "s_cmp_le_i32 s93, 14\n"
      "ds_read_b64_tr_b16 a[124:125], v247 offset:1024\n"
      "v_cvt_pk_bf16_f32 v220, v112, v113\n"
      "v_mfma_f32_16x16x32_bf16 v[156:159], a[68:71], a[12:15], v[236:239]\n"
      "s_cselect_b32 s86, 107, s86\n"
      "v_exp_f32 v114, v186\n"
      "v_mfma_f32_16x16x32_bf16 v[156:159], a[76:79], a[28:31], v[156:159]\n"
      "s_sub_u32 s67, s65, s66\n"
      "ds_read_b64_tr_b16 a[126:127], v247 offset:5120\n"
      "v_mfma_f32_16x16x32_bf16 v[156:159], a[84:87], a[44:47], v[156:159]\n"
      "s_cmp_lt_i32 s67, 0\n"
      "v_exp_f32 v115, v187\n"
      "v_mfma_f32_16x16x32_bf16 v[156:159], a[92:95], a[60:63], v[156:159]\n"
      "s_cselect_b32 s88, s66, s65\n"
      "v_cvt_pk_bf16_f32 v221, v114, v115\n"
      "v_mfma_f32_16x16x32_bf16 v[152:155], a[88:91], a[60:63], v[236:239]\n"
      "s_add_u32 s90, s88, s91\n"
      "v_exp_f32 v112, v188\n"
      "v_mfma_f32_16x16x32_bf16 v[152:155], a[80:83], a[44:47], v[152:155]\n"
      "v_or3_b32 v116, v116, v220, v221\n"
      "v_mfma_f32_16x16x32_bf16 v[152:155], a[72:75], a[28:31], v[152:155]\n"
      "v_exp_f32 v113, v189\n"
      "v_mfma_f32_16x16x32_bf16 v[152:155], a[64:67], a[12:15], v[152:155]\n"
      "s_waitcnt lgkmcnt(10)\n"
      "v_cvt_pk_bf16_f32 v222, v112, v113\n"
      "v_mfma_f32_16x16x32_bf16 v[32:35], a[96:99], v[192:195], v[32:35]\n"
      "v_exp_f32 v114, v190\n"
      "v_mfma_f32_16x16x32_bf16 v[48:51], a[100:103], v[192:195], v[48:51]\n"
      "v_mfma_f32_16x16x32_bf16 a[128:131], a[104:107], v[192:195], a[128:131]\n"
      "s_waitcnt lgkmcnt(6)\n"
      "v_exp_f32 v115, v191\n"
      "v_mfma_f32_16x16x32_bf16 a[144:147], a[108:111], v[192:195], a[144:147]\n"
      "v_cvt_pk_bf16_f32 v223, v114, v115\n"
      "v_mfma_f32_16x16x32_bf16 v[64:67], a[112:115], v[192:195], v[64:67]\n"
      "s_waitcnt lgkmcnt(2)\n"
      "v_mfma_f32_16x16x32_bf16 v[80:83], a[116:119], v[192:195], v[80:83]\n"
      "v_or3_b32 v116, v116, v222, v223\n"
      "v_mfma_f32_16x16x32_bf16 a[160:163], a[120:123], v[192:195], a[160:163]\n"
      "s_mov_b32 s64, 1\n"
      "s_waitcnt lgkmcnt(0)\n"
      "v_and_b32 v116, 0x40004000, v116\n"
      "v_mfma_f32_16x16x32_bf16 a[176:179], a[124:127], v[192:195], a[176:179]\n"
      "v_cmp_ne_u32 vcc, 0, v116\n"
      "s_cbranch_vccnz FB1_%=\n"
      "RET1_1_%=:\n"
      "v_mfma_f32_16x16x32_bf16 a[180:183], a[124:127], v[196:199], a[180:183]\n"
      "v_exp_f32 v112, v128\n"
      "s_barrier\n"
      "v_mfma_f32_16x16x32_bf16 a[164:167], a[120:123], v[196:199], a[164:167]\n"
      "v_mfma_f32_16x16x32_bf16 a[192:195], a[196:199], v[208:211], a[192:195]\n"
      "s_add_u32 m0, s62, 0\n"
      "v_exp_f32 v113, v129\n"
      "v_mfma_f32_16x16x32_bf16 v[84:87], a[116:119], v[196:199], v[84:87]\n"
      "buffer_load_dwordx4 v248, s[20:23], s60 offen nt lds\n"
      "v_cvt_pk_bf16_f32 v192, v112, v113\n"
      "v_mfma_f32_16x16x32_bf16 v[68:71], a[112:115], v[196:199], v[68:71]\n"
      "s_add_u32 m0, s62, 896\n"
      "v_exp_f32 v114, v130\n"
      "v_mfma_f32_16x16x32_bf16 a[192:195], a[200:203], v[212:215], a[192:195]\n"
      "buffer_load_dwordx4 v248, s[20:23], s60 offen offset:128 nt lds\n"
      "v_mfma_f32_16x16x32_bf16 a[148:151], a[108:111], v[196:199], a[148:151]\n"
      "s_add_u32 m0, s62, 8192\n"
      "v_exp_f32 v115, v131\n"
      "v_mfma_f32_16x16x32_bf16 a[132:135], a[104:107], v[196:199], a[132:135]\n"
      "s_memrealtime s[68:69]\n"
      "buffer_load_dwordx4 v120, s[20:23], s60 offen nt lds\n"
      "v_cvt_pk_bf16_f32 v193, v114, v115\n"
      "v_mfma_f32_16x16x32_bf16 a[192:195], a[204:207], v[216:219], a[192:195]\n"
      "s_add_u32 m0, s62, 9088\n"
      "v_exp_f32 v112, v132\n"
      "v_mfma_f32_16x16x32_bf16 v[52:55], a[100:103], v[196:199], v[52:55]\n"
      "s_add_u32 s62, s62, 0x8000\n"
      "buffer_load_dwordx4 v120, s[20:23], s60 offen offset:128 nt lds\n"
      "v_or_b32 v116, v192, v193\n"
      "v_mfma_f32_16x16x32_bf16 v[36:39], a[96:99], v[196:199], v[36:39]\n"
      "s_and_b32 s62, s62, 0x1ffff\n"
      "v_exp_f32 v113, v133\n"
      "v_mfma_f32_16x16x32_bf16 a[192:195], a[208:211], v[220:223], a[192:195]\n"
      "s_add_u32 s60, s60, s78\n"
      "v_cvt_pk_bf16_f32 v194, v112, v113\n"
      "s_waitcnt vmcnt(12) lgkmcnt(0)\n"
      "s_barrier\n"
      "v_mfma_f32_16x16x32_bf16 v[40:43], a[96:99], v[200:203], v[40:43]\n"
      "s_lshr_b32 s43, %[wb], 8\n"
      "ds_read_b128 a[64:67], v242 offset:32768\n"
      "v_exp_f32 v114, v134\n"
      "v_mfma_f32_16x16x32_bf16 v[44:47], a[96:99], v[204:207], v[44:47]\n"
      "s_lshr_b32 s33, 0x8d802000, s43\n"
      "ds_read_b128 a[68:71], v242 offset:36864\n"
      "ds_read_b128 a[72:75], v243 offset:32768\n"
      "v_mfma_f32_16x16x32_bf16 v[60:63], a[100:103], v[204:207], v[60:63]\n"
      "s_and_b32 s33, s33, 255\n"
      "v_exp_f32 v115, v135\n"
      "v_mfma_f32_16x16x32_bf16 v[56:59], a[100:103], v[200:203], v[56:59]\n"
      "s_lshl_b32 s33, s33, 10\n"
      "ds_read_b128 a[76:79], v243 offset:36864\n"
      "v_cvt_pk_bf16_f32 v195, v114, v115\n"
      "v_mfma_f32_16x16x32_bf16 a[136:139], a[104:107], v[200:203], a[136:139]\n"
      "s_xor_b32 s43, s43, 16\n"
      "v_exp_f32 v112, v136\n"
      "v_mfma_f32_16x16x32_bf16 a[140:143], a[104:107], v[204:207], a[140:143]\n"
      "s_lshr_b32 s34, 0x8d802000, s43\n"
      "ds_read_b128 a[80:83], v242 offset:33792\n"
      "v_or3_b32 v116, v116, v194, v195\n"
      "v_mfma_f32_16x16x32_bf16 a[156:159], a[108:111], v[204:207], a[156:159]\n"
      "s_and_b32 s34, s34, 255\n"
      "v_exp_f32 v113, v137\n"
      "v_mfma_f32_16x16x32_bf16 a[152:155], a[108:111], v[200:203], a[152:155]\n"
      "s_lshl_b32 s34, s34, 10\n"
      "ds_read_b128 a[84:87], v242 offset:37888\n"
      "v_cvt_pk_bf16_f32 v196, v112, v113\n"
      "v_mfma_f32_16x16x32_bf16 v[72:75], a[112:115], v[200:203], v[72:75]\n"
      "s_lshr_b32 s35, %[qo], 13\n"
      "v_exp_f32 v114, v138\n"
      "v_mfma_f32_16x16x32_bf16 v[76:79], a[112:115], v[204:207], v[76:79]\n"
      "s_lshr_b32 s43, %[wb], 11\n"
      "ds_read_b128 a[88:91], v243 offset:33792\n"
      "ds_read_b128 a[92:95], v243 offset:37888\n"
      "v_mfma_f32_16x16x32_bf16 v[92:95], a[116:119], v[204:207], v[92:95]\n"
      "s_add_u32 s35, s35, s43\n"
      "v_exp_f32 v115, v139\n"
      "v_mfma_f32_16x16x32_bf16 v[88:91], a[116:119], v[200:203], v[88:91]\n"
      "s_mul_i32 s35, s35, 5120\n"
      "v_cvt_pk_bf16_f32 v197, v114, v115\n"
      "v_mfma_f32_16x16x32_bf16 a[168:171], a[120:123], v[200:203], a[168:171]\n"
      "s_and_b32 s43, %[blk], 8\n"
      "v_exp_f32 v112, v140\n"
      "v_mfma_f32_16x16x32_bf16 a[172:175], a[120:123], v[204:207], a[172:175]\n"
      "s_lshr_b32 s37, %[qo], 8\n"
      "v_or3_b32 v116, v116, v196, v197\n"
      "v_mfma_f32_16x16x32_bf16 a[188:191], a[124:127], v[204:207], a[188:191]\n"
      "s_add_u32 s37, s37, s43\n"
      "v_exp_f32 v113, v141\n"
      "v_mfma_f32_16x16x32_bf16 a[184:187], a[124:127], v[200:203], a[184:187]\n"
      "s_waitcnt lgkmcnt(0)\n"
      "v_cvt_pk_bf16_f32 v198, v112, v113\n"
      "v_mfma_f32_16x16x32_bf16 v[160:163], a[64:67], a[0:3], v[224:227]\n"
      "s_add_u32 s37, s37, 64\n"
      "v_exp_f32 v114, v142\n"
      "v_mfma_f32_16x16x32_bf16 v[160:163], a[72:75], a[16:19], v[160:163]\n"
      "s_lshl_b32 s36, s43, 4\n"
      "ds_read_b64_tr_b16 a[96:97], v244 offset:32768\n"
      "ds_read_b64_tr_b16 a[98:99], v244 offset:36864\n"
      "v_mfma_f32_16x16x32_bf16 v[160:163], a[80:83], a[32:35], v[160:163]\n"
      "s_lshl_b32 s43, s43, 19\n"
      "v_exp_f32 v115, v143\n"
      "v_mfma_f32_16x16x32_bf16 v[160:163], a[88:91], a[48:51], v[160:163]\n"
      "s_add_u32 s35, s35, s43\n"
      "ds_read_b64_tr_b16 a[100:101], v245 offset:32768\n"
      "v_cvt_pk_bf16_f32 v199, v114, v115\n"
      "v_mfma_f32_16x16x32_bf16 v[164:167], a[92:95], a[48:51], v[224:227]\n"
      "s_add_u32 s35, s35, 0x8000\n"
      "v_exp_f32 v112, v144\n"
      "v_mfma_f32_16x16x32_bf16 v[164:167], a[84:87], a[32:35], v[164:167]\n"
      "s_add_u32 s36, s36, %[qo]\n"
      "ds_read_b64_tr_b16 a[102:103], v245 offset:36864\n"
      "v_or3_b32 v116, v116, v198, v199\n"
      "v_mfma_f32_16x16x32_bf16 v[164:167], a[76:79], a[16:19], v[164:167]\n"
      "s_and_b32 s43, %[wb], 2048\n"
      "v_exp_f32 v113, v145\n"
      "v_mfma_f32_16x16x32_bf16 v[164:167], a[68:71], a[0:3], v[164:167]\n"
      "s_lshl3_add_u32 s36, s43, s36\n"
      "ds_read_b64_tr_b16 a[104:105], v246 offset:32768\n"
      "v_cvt_pk_bf16_f32 v200, v112, v113\n"
      "v_mfma_f32_16x16x32_bf16 v[172:175], a[68:71], a[4:7], v[228:231]\n"
      "s_lshr_b32 s43, %[wb], 6\n"
      "v_exp_f32 v114, v146\n"
      "v_mfma_f32_16x16x32_bf16 v[172:175], a[76:79], a[20:23], v[172:175]\n"
      "s_and_b32 s43, s43, 64\n"
      "ds_read_b64_tr_b16 a[106:107], v246 offset:36864\n"
      "ds_read_b64_tr_b16 a[108:109], v247 offset:32768\n"
      "v_mfma_f32_16x16x32_bf16 v[172:175], a[84:87], a[36:39], v[172:175]\n"
      "s_add_u32 s36, s36, s43\n"
      "v_exp_f32 v115, v147\n"
      "v_mfma_f32_16x16x32_bf16 v[172:175], a[92:95], a[52:55], v[172:175]\n"
      "s_add_u32 s36, s36, 4096\n"
      "ds_read_b64_tr_b16 a[110:111], v247 offset:36864\n"
      "v_cvt_pk_bf16_f32 v201, v114, v115\n"
      "v_mfma_f32_16x16x32_bf16 v[168:171], a[88:91], a[52:55], v[228:231]\n"
      "v_exp_f32 v112, v148\n"
      "v_mfma_f32_16x16x32_bf16 v[168:171], a[80:83], a[36:39], v[168:171]\n"
      "ds_read_b64_tr_b16 a[112:113], v244 offset:33792\n"
      "v_or3_b32 v116, v116, v200, v201\n"
      "v_mfma_f32_16x16x32_bf16 v[168:171], a[72:75], a[20:23], v[168:171]\n"
      "v_exp_f32 v113, v149\n"
      "v_mfma_f32_16x16x32_bf16 v[168:171], a[64:67], a[4:7], v[168:171]\n"
      "ds_read_b64_tr_b16 a[114:115], v244 offset:37888\n"
      "v_cvt_pk_bf16_f32 v202, v112, v113\n"
      "v_mfma_f32_16x16x32_bf16 v[176:179], a[64:67], a[8:11], v[232:235]\n"
      "v_exp_f32 v114, v150\n"
      "v_mfma_f32_16x16x32_bf16 v[176:179], a[72:75], a[24:27], v[176:179]\n"
      "ds_read_b64_tr_b16 a[116:117], v245 offset:33792\n"
      "ds_read_b64_tr_b16 a[118:119], v245 offset:37888\n"
      "v_mfma_f32_16x16x32_bf16 v[176:179], a[80:83], a[40:43], v[176:179]\n"
      "v_exp_f32 v115, v151\n"
      "v_mfma_f32_16x16x32_bf16 v[176:179], a[88:91], a[56:59], v[176:179]\n"
      "ds_read_b64_tr_b16 a[120:121], v246 offset:33792\n"
      "v_cvt_pk_bf16_f32 v203, v114, v115\n"
      "v_mfma_f32_16x16x32_bf16 v[180:183], a[92:95], a[56:59], v[232:235]\n"
      "v_exp_f32 v112, v152\n"
      "v_mfma_f32_16x16x32_bf16 v[180:183], a[84:87], a[40:43], v[180:183]\n"
      "ds_read_b64_tr_b16 a[122:123], v246 offset:37888\n"
      "v_or3_b32 v116, v116, v202, v203\n"
      "v_mfma_f32_16x16x32_bf16 v[180:183], a[76:79], a[24:27], v[180:183]\n"
      "v_exp_f32 v113, v153\n"
      "v_mfma_f32_16x16x32_bf16 v[180:183], a[68:71], a[8:11], v[180:183]\n"
      "ds_read_b64_tr_b16 a[124:125], v247 offset:33792\n"
      "v_cvt_pk_bf16_f32 v204, v112, v113\n"
      "v_mfma_f32_16x16x32_bf16 v[188:191], a[68:71], a[12:15], v[236:239]\n"
      "v_exp_f32 v114, v154\n"
      "v_mfma_f32_16x16x32_bf16 v[188:191], a[76:79], a[28:31], v[188:191]\n"
      "ds_read_b64_tr_b16 a[126:127], v247 offset:37888\n"
      "v_mfma_f32_16x16x32_bf16 v[188:191], a[84:87], a[44:47], v[188:191]\n"
      "v_exp_f32 v115, v155\n"
      "v_mfma_f32_16x16x32_bf16 v[188:191], a[92:95], a[60:63], v[188:191]\n"
      "v_cvt_pk_bf16_f32 v205, v114, v115\n"
      "v_mfma_f32_16x16x32_bf16 v[184:187], a[88:91], a[60:63], v[236:239]\n"
      "v_exp_f32 v112, v156\n"
      "v_mfma_f32_16x16x32_bf16 v[184:187], a[80:83], a[44:47], v[184:187]\n"
      "v_or3_b32 v116, v116, v204, v205\n"
      "v_mfma_f32_16x16x32_bf16 v[184:187], a[72:75], a[28:31], v[184:187]\n"
      "v_exp_f32 v113, v157\n"
      "v_mfma_f32_16x16x32_bf16 v[184:187], a[64:67], a[12:15], v[184:187]\n"
      "s_waitcnt lgkmcnt(10)\n"
      "v_cvt_pk_bf16_f32 v206, v112, v113\n"
      "v_mfma_f32_16x16x32_bf16 v[32:35], a[96:99], v[208:211], v[32:35]\n"
      "v_exp_f32 v114, v158\n"
      "v_mfma_f32_16x16x32_bf16 v[48:51], a[100:103], v[208:211], v[48:51]\n"
      "v_mfma_f32_16x16x32_bf16 a[128:131], a[104:107], v[208:211], a[128:131]\n"
      "s_waitcnt lgkmcnt(6)\n"
      "v_exp_f32 v115, v159\n"
      "v_mfma_f32_16x16x32_bf16 a[144:147], a[108:111], v[208:211], a[144:147]\n"
      "v_cvt_pk_bf16_f32 v207, v114, v115\n"
      "v_mfma_f32_16x16x32_bf16 v[64:67], a[112:115], v[208:211], v[64:67]\n"
      "s_waitcnt lgkmcnt(2)\n"
      "v_mfma_f32_16x16x32_bf16 v[80:83], a[116:119], v[208:211], v[80:83]\n"
      "v_or3_b32 v116, v116, v206, v207\n"
      "v_mfma_f32_16x16x32_bf16 a[160:163], a[120:123], v[208:211], a[160:163]\n"
      "s_mov_b32 s64, 1\n"
      "s_waitcnt lgkmcnt(0)\n"
      "v_and_b32 v116, 0x40004000, v116\n"
      "v_mfma_f32_16x16x32_bf16 a[176:179], a[124:127], v[208:211], a[176:179]\n"
      "v_cmp_ne_u32 vcc, 0, v116\n"
      "s_cbranch_vccnz FB0_%=\n"
      "RET0_1_%=:\n"
      "v_mfma_f32_16x16x32_bf16 a[180:183], a[124:127], v[212:215], a[180:183]\n"
      "v_exp_f32 v112, v160\n"
      "v_mfma_f32_16x16x32_bf16 a[164:167], a[120:123], v[212:215], a[164:167]\n"
      "v_xor_b32 v242, 0x10000, v242\n"
      "v_mfma_f32_16x16x32_bf16 a[192:195], a[196:199], v[192:195], a[192:195]\n"
      "s_add_u32 m0, s77, 16384\n"
      "v_exp_f32 v113, v161\n"
      "v_mfma_f32_16x16x32_bf16 v[84:87], a[116:119], v[212:215], v[84:87]\n"
      "buffer_load_dwordx4 v249, s[24:27], s61 offen nt lds\n"
      "v_cvt_pk_bf16_f32 v208, v112, v113\n"
      "v_xor_b32 v243, 0x10000, v243\n"
      "v_mfma_f32_16x16x32_bf16 v[68:71], a[112:115], v[212:215], v[68:71]\n"
      "s_add_u32 m0, s77, 17408\n"
      "v_exp_f32 v114, v162\n"
      "v_mfma_f32_16x16x32_bf16 a[192:195], a[200:203], v[196:199], a[192:195]\n"
      "buffer_load_dwordx4 v121, s[24:27], s61 offen nt lds\n"
      "v_xor_b32 v244, 0x10000, v244\n"
      "v_mfma_f32_16x16x32_bf16 a[148:151], a[108:111], v[212:215], a[148:151]\n"
      "s_add_u32 m0, s77, 24576\n"
      "v_exp_f32 v115, v163\n"
      "v_mfma_f32_16x16x32_bf16 a[132:135], a[104:107], v[212:215], a[132:135]\n"
      "s_memrealtime s[68:69]\n"
      "buffer_load_dwordx4 v122, s[24:27], s61 offen nt lds\n"
      "v_cvt_pk_bf16_f32 v209, v114, v115\n"
      "v_xor_b32 v245, 0x10000, v245\n"
      "v_mfma_f32_16x16x32_bf16 a[192:195], a[204:207], v[200:203], a[192:195]\n"
      "s_add_u32 m0, s77, 25600\n"
      "v_exp_f32 v112, v164\n"
      "v_mfma_f32_16x16x32_bf16 v[52:55], a[100:103], v[212:215], v[52:55]\n"
      "s_add_u32 s77, s77, 0x8000\n"
      "buffer_load_dwordx4 v123, s[24:27], s61 offen nt lds\n"
      "v_or_b32 v116, v208, v209\n"
      "v_xor_b32 v246, 0x10000, v246\n"
      "v_mfma_f32_16x16x32_bf16 v[36:39], a[96:99], v[212:215], v[36:39]\n"
      "s_and_b32 s77, s77, 0x1ffff\n"
      "v_exp_f32 v113, v165\n"
      "v_mfma_f32_16x16x32_bf16 a[192:195], a[208:211], v[204:207], a[192:195]\n"
      "s_add_u32 s61, s61, s78\n"
      "v_cvt_pk_bf16_f32 v210, v112, v113\n"
      "v_xor_b32 v247, 0x10000, v247\n"
      "s_waitcnt vmcnt(8) lgkmcnt(0)\n"
      "s_sub_u32 s67, s68, s90\n"
      "s_cmp_lt_i32 s67, 0\n"
      "s_cbranch_scc1 GW3_%=\n"
      "GX3_%=:\n"
      "s_barrier\n"
      "v_mfma_f32_16x16x32_bf16 v[40:43], a[96:99], v[216:219], v[40:43]\n"
      "s_memrealtime s[68:69]\n"
      "buffer_load_dword v250, v251, s[28:31], 0 offen sc1\n"
      "ds_read_b128 a[64:67], v242 offset:0\n"
      "v_exp_f32 v114, v166\n"
      "v_mfma_f32_16x16x32_bf16 v[44:47], a[96:99], v[220:223], v[44:47]\n"
      "s_add_u32 m0, s62, 0\n"
      "ds_read_b128 a[68:71], v242 offset:4096\n"
      "ds_read_b128 a[72:75], v243 offset:0\n"
      "v_mfma_f32_16x16x32_bf16 v[60:63], a[100:103], v[220:223], v[60:63]\n"
      "buffer_load_dwordx4 v248, s[20:23], s60 offen nt lds\n"
      "v_exp_f32 v115, v167\n"
      "v_mfma_f32_16x16x32_bf16 v[56:59], a[100:103], v[216:219], v[56:59]\n"
      "s_add_u32 m0, s62, 896\n"
      "ds_read_b128 a[76:79], v243 offset:4096\n"
      "v_cvt_pk_bf16_f32 v211, v114, v115\n"
      "v_mfma_f32_16x16x32_bf16 a[136:139], a[104:107], v[216:219], a[136:139]\n"
      "buffer_load_dwordx4 v248, s[20:23], s60 offen offset:128 nt lds\n"
      "v_exp_f32 v112, v168\n"
      "v_mfma_f32_16x16x32_bf16 a[140:143], a[104:107], v[220:223], a[140:143]\n"
      "s_add_u32 m0, s62, 8192\n"
      "ds_read_b128 a[80:83], v242 offset:1024\n"
      "v_or3_b32 v116, v116, v210, v211\n"
      "v_mfma_f32_16x16x32_bf16 a[156:159], a[108:111], v[220:223], a[156:159]\n"
      "buffer_load_dwordx4 v120, s[20:23], s60 offen nt lds\n"
      "v_exp_f32 v113, v169\n"
      "v_mfma_f32_16x16x32_bf16 a[152:155], a[108:111], v[216:219], a[152:155]\n"
      "s_add_u32 m0, s62, 9088\n"
      "ds_read_b128 a[84:87], v242 offset:5120\n"
      "v_cvt_pk_bf16_f32 v212, v112, v113\n"
      "v_mfma_f32_16x16x32_bf16 v[72:75], a[112:115], v[216:219], v[72:75]\n"
      "s_add_u32 s62, s62, 0x8000\n"
      "buffer_load_dwordx4 v120, s[20:23], s60 offen offset:128 nt lds\n"
      "v_exp_f32 v114, v170\n"
      "v_mfma_f32_16x16x32_bf16 v[76:79], a[112:115], v[220:223], v[76:79]\n"
      "s_and_b32 s62, s62, 0x1ffff\n"
      "ds_read_b128 a[88:91], v243 offset:1024\n"
      "ds_read_b128 a[92:95], v243 offset:5120\n"
      "v_mfma_f32_16x16x32_bf16 v[92:95], a[116:119], v[220:223], v[92:95]\n"
      "s_add_u32 s60, s60, %[inc]\n"
      "v_exp_f32 v115, v171\n"
      "v_mfma_f32_16x16x32_bf16 v[88:91], a[116:119], v[216:219], v[88:91]\n"
      "v_cvt_pk_bf16_f32 v213, v114, v115\n"
      "v_mfma_f32_16x16x32_bf16 a[168:171], a[120:123], v[216:219], a[168:171]\n"
      "v_exp_f32 v112, v172\n"
      "v_mfma_f32_16x16x32_bf16 a[172:175], a[120:123], v[220:223], a[172:175]\n"
      "v_or3_b32 v116, v116, v212, v213\n"
      "v_mfma_f32_16x16x32_bf16 a[188:191], a[124:127], v[220:223], a[188:191]\n"
      "v_exp_f32 v113, v173\n"
      "v_mfma_f32_16x16x32_bf16 a[184:187], a[124:127], v[216:219], a[184:187]\n"
      "s_waitcnt lgkmcnt(0)\n"
      "v_cvt_pk_bf16_f32 v214, v112, v113\n"
      "v_mfma_f32_16x16x32_bf16 v[128:131], a[64:67], a[0:3], v[224:227]\n"
      "s_sub_u32 s67, s68, s66\n"
      "v_exp_f32 v114, v174\n"
      "v_mfma_f32_16x16x32_bf16 v[128:131], a[72:75], a[16:19], v[128:131]\n"
      "s_cmp_gt_i32 s67, s70\n"
      "ds_read_b64_tr_b16 a[96:97], v244 offset:0\n"
      "ds_read_b64_tr_b16 a[98:99], v244 offset:4096\n"
      "v_mfma_f32_16x16x32_bf16 v[128:131], a[80:83], a[32:35], v[128:131]\n"
      "s_cselect_b32 s69, s86, 0\n"
      "v_exp_f32 v115, v175\n"
      "v_mfma_f32_16x16x32_bf16 v[128:131], a[88:91], a[48:51], v[128:131]\n"
      "s_add_u32 s66, s66, s69\n"
      "ds_read_b64_tr_b16 a[100:101], v245 offset:0\n"
      "v_cvt_pk_bf16_f32 v215, v114, v115\n"
      "v_mfma_f32_16x16x32_bf16 v[132:135], a[92:95], a[48:51], v[224:227]\n"
      "s_add_u32 s67, s67, s101\n"
      "v_exp_f32 v112, v176\n"
      "v_mfma_f32_16x16x32_bf16 v[132:135], a[84:87], a[32:35], v[132:135]\n"
      "s_min_i32 s70, s70, s67\n"
      "ds_read_b64_tr_b16 a[102:103], v245 offset:4096\n"
      "v_or3_b32 v116, v116, v214, v215\n"
      "v_mfma_f32_16x16x32_bf16 v[132:135], a[76:79], a[16:19], v[132:135]\n"
      "s_max_i32 s70, s70, 85\n"
      "v_exp_f32 v113, v177\n"
      "v_mfma_f32_16x16x32_bf16 v[132:135], a[68:71], a[0:3], v[132:135]\n"
      "s_add_u32 s66, s66, s86\n"
      "ds_read_b64_tr_b16 a[104:105], v246 offset:0\n"
      "v_cvt_pk_bf16_f32 v216, v112, v113\n"
      "v_mfma_f32_16x16x32_bf16 v[140:143], a[68:71], a[4:7], v[228:231]\n"
      "s_and_b32 s76, s80, 31\n"
      "v_exp_f32 v114, v178\n"
      "v_mfma_f32_16x16x32_bf16 v[140:143], a[76:79], a[20:23], v[140:143]\n"
      "s_lshr_b64 s[80:81], s[80:81], 5\n"
      "ds_read_b64_tr_b16 a[106:107], v246 offset:4096\n"
      "ds_read_b64_tr_b16 a[108:109], v247 offset:0\n"
      "v_mfma_f32_16x16x32_bf16 v[140:143], a[84:87], a[36:39], v[140:143]\n"
      "s_add_u32 s66, s66, s76\n"
      "v_exp_f32 v115, v179\n"
      "v_mfma_f32_16x16x32_bf16 v[140:143], a[92:95], a[52:55], v[140:143]\n"
      "s_sub_u32 s67, s68, s82\n"
      "ds_read_b64_tr_b16 a[110:111], v247 offset:4096\n"
      "v_cvt_pk_bf16_f32 v217, v114, v115\n"
      "v_mfma_f32_16x16x32_bf16 v[136:139], a[88:91], a[52:55], v[228:231]\n"
      "s_min_i32 s76, s67, s83\n"
      "v_exp_f32 v112, v180\n"
      "v_mfma_f32_16x16x32_bf16 v[136:139], a[80:83], a[36:39], v[136:139]\n"
      "s_mov_b32 s83, s67\n"
      "ds_read_b64_tr_b16 a[112:113], v244 offset:1024\n"
      "v_or3_b32 v116, v116, v216, v217\n"
      "v_mfma_f32_16x16x32_bf16 v[136:139], a[72:75], a[20:23], v[136:139]\n"
      "s_sub_u32 s76, s76, 36\n"
      "v_exp_f32 v113, v181\n"
      "v_mfma_f32_16x16x32_bf16 v[136:139], a[64:67], a[4:7], v[136:139]\n"
      "s_min_i32 s76, s76, 148\n"
      "ds_read_b64_tr_b16 a[114:115], v244 offset:5120\n"
      "v_cvt_pk_bf16_f32 v218, v112, v113\n"
      "v_mfma_f32_16x16x32_bf16 v[144:147], a[64:67], a[8:11], v[232:235]\n"
      "s_max_i32 s76, s76, 70\n"
      "v_exp_f32 v114, v182\n"
      "v_mfma_f32_16x16x32_bf16 v[144:147], a[72:75], a[24:27], v[144:147]\n"
      "s_mov_b32 s82, s68\n"
      "ds_read_b64_tr_b16 a[116:117], v245 offset:1024\n"
      "ds_read_b64_tr_b16 a[118:119], v245 offset:5120\n"
      "v_mfma_f32_16x16x32_bf16 v[144:147], a[80:83], a[40:43], v[144:147]\n"
      "s_lshr_b32 s69, s86, 1\n"
      "v_exp_f32 v115, v183\n"
      "v_mfma_f32_16x16x32_bf16 v[144:147], a[88:91], a[56:59], v[144:147]\n"
      "s_add_u32 s66, s66, s69\n"
      "ds_read_b64_tr_b16 a[120:121], v246 offset:1024\n"
      "v_cvt_pk_bf16_f32 v219, v114, v115\n"
      "v_mfma_f32_16x16x32_bf16 v[148:151], a[92:95], a[56:59], v[232:235]\n"
      "s_add_u32 s82, s82, s69\n"
      "v_exp_f32 v112, v184\n"
      "v_mfma_f32_16x16x32_bf16 v[148:151], a[84:87], a[40:43], v[148:151]\n"
      "s_add_u32 s65, s82, s76\n"
      "ds_read_b64_tr_b16 a[122:123], v246 offset:5120\n"
      "v_or3_b32 v116, v116, v218, v219\n"
      "v_mfma_f32_16x16x32_bf16 v[148:151], a[76:79], a[24:27], v[148:151]\n"
      "s_sub_u32 s93, s93, 1\n"
      "v_exp_f32 v113, v185\n"
      "v_mfma_f32_16x16x32_bf16 v[148:151], a[68:71], a[8:11], v[148:151]\n"
      "s_cmp_le_i32 s93, 14\n"
      "ds_read_b64_tr_b16 a[124:125], v247 offset:1024\n"
      "v_cvt_pk_bf16_f32 v220, v112, v113\n"
      "v_mfma_f32_16x16x32_bf16 v[156:159], a[68:71], a[12:15], v[236:239]\n"
      "s_cselect_b32 s86, 107, s86\n"
      "v_exp_f32 v114, v186\n"
      "v_mfma_f32_16x16x32_bf16 v[156:159], a[76:79], a[28:31], v[156:159]\n"
      "s_sub_u32 s67, s65, s66\n"
      "ds_read_b64_tr_b16 a[126:127], v247 offset:5120\n"
      "v_mfma_f32_16x16x32_bf16 v[156:159], a[84:87], a[44:47], v[156:159]\n"
      "s_cmp_lt_i32 s67, 0\n"
      "v_exp_f32 v115, v187\n"
      "v_mfma_f32_16x16x32_bf16 v[156:159], a[92:95], a[60:63], v[156:159]\n"
      "s_cselect_b32 s88, s66, s65\n"
      "v_cvt_pk_bf16_f32 v221, v114, v115\n"
      "v_mfma_f32_16x16x32_bf16 v[152:155], a[88:91], a[60:63], v[236:239]\n"
      "s_add_u32 s90, s88, s91\n"
      "v_exp_f32 v112, v188\n"
      "v_mfma_f32_16x16x32_bf16 v[152:155], a[80:83], a[44:47], v[152:155]\n"
      "v_or3_b32 v116, v116, v220, v221\n"
      "v_mfma_f32_16x16x32_bf16 v[152:155], a[72:75], a[28:31], v[152:155]\n"
      "v_exp_f32 v113, v189\n"
      "v_mfma_f32_16x16x32_bf16 v[152:155], a[64:67], a[12:15], v[152:155]\n"
      "s_waitcnt lgkmcnt(10)\n"
      "v_cvt_pk_bf16_f32 v222, v112, v113\n"
      "v_mfma_f32_16x16x32_bf16 v[32:35], a[96:99], v[192:195], v[32:35]\n"
      "v_exp_f32 v114, v190\n"
      "v_mfma_f32_16x16x32_bf16 v[48:51], a[100:103], v[192:195], v[48:51]\n"
      "v_mfma_f32_16x16x32_bf16 a[128:131], a[104:107], v[192:195], a[128:131]\n"
      "s_waitcnt lgkmcnt(6)\n"
      "v_exp_f32 v115, v191\n"
      "v_mfma_f32_16x16x32_bf16 a[144:147], a[108:111], v[192:195], a[144:147]\n"
      "v_cvt_pk_bf16_f32 v223, v114, v115\n"
      "v_mfma_f32_16x16x32_bf16 v[64:67], a[112:115], v[192:195], v[64:67]\n"
      "s_waitcnt lgkmcnt(2)\n"
      "v_mfma_f32_16x16x32_bf16 v[80:83], a[116:119], v[192:195], v[80:83]\n"
      "v_or3_b32 v116, v116, v222, v223\n"
      "v_mfma_f32_16x16x32_bf16 a[160:163], a[120:123], v[192:195], a[160:163]\n"
      "s_mov_b32 s64, 2\n"
      "s_waitcnt lgkmcnt(0)\n"
      "v_and_b32 v116, 0x40004000, v116\n"
      "v_mfma_f32_16x16x32_bf16 a[176:179], a[124:127], v[192:195], a[176:179]\n"
      "v_cmp_ne_u32 vcc, 0, v116\n"
      "s_cbranch_vccnz FB1_%=\n"
      "RET1_2_%=:\n"
      "v_mfma_f32_16x16x32_bf16 a[180:183], a[124:127], v[196:199], a[180:183]\n"
      "v_exp_f32 v112, v128\n"
      "v_mfma_f32_16x16x32_bf16 a[164:167], a[120:123], v[196:199], a[164:167]\n"
      "v_mfma_f32_16x16x32_bf16 a[192:195], a[196:199], v[208:211], a[192:195]\n"
      "s_add_u32 m0, s77, 16384\n"
      "v_exp_f32 v113, v129\n"
      "v_mfma_f32_16x16x32_bf16 v[84:87], a[116:119], v[196:199], v[84:87]\n"
      "buffer_load_dwordx4 v249, s[24:27], s61 offen nt lds\n"
      "v_cvt_pk_bf16_f32 v192, v112, v113\n"
      "v_mfma_f32_16x16x32_bf16 v[68:71], a[112:115], v[196:199], v[68:71]\n"
      "s_add_u32 m0, s77, 17408\n"
      "v_exp_f32 v114, v130\n"
      "v_mfma_f32_16x16x32_bf16 a[192:195], a[200:203], v[212:215], a[192:195]\n"
      "buffer_load_dwordx4 v121, s[24:27], s61 offen nt lds\n"
      "v_mfma_f32_16x16x32_bf16 a[148:151], a[108:111], v[196:199], a[148:151]\n"
      "s_add_u32 m0, s77, 24576\n"
      "v_exp_f32 v115, v131\n"
      "v_mfma_f32_16x16x32_bf16 a[132:135], a[104:107], v[196:199], a[132:135]\n"
      "s_memrealtime s[68:69]\n"
      "buffer_load_dwordx4 v122, s[24:27], s61 offen nt lds\n"
      "v_cvt_pk_bf16_f32 v193, v114, v115\n"
      "v_mfma_f32_16x16x32_bf16 a[192:195], a[204:207], v[216:219], a[192:195]\n"
      "s_add_u32 m0, s77, 25600\n"
      "v_exp_f32 v112, v132\n"
      "v_mfma_f32_16x16x32_bf16 v[52:55], a[100:103], v[196:199], v[52:55]\n"
      "s_add_u32 s77, s77, 0x8000\n"
      "buffer_load_dwordx4 v123, s[24:27], s61 offen nt lds\n"
      "v_or_b32 v116, v192, v193\n"
      "v_mfma_f32_16x16x32_bf16 v[36:39], a[96:99], v[196:199], v[36:39]\n"
      "s_and_b32 s77, s77, 0x1ffff\n"
      "v_exp_f32 v113, v133\n"
      "v_mfma_f32_16x16x32_bf16 a[192:195], a[208:211], v[220:223], a[192:195]\n"
      "s_add_u32 s61, s61, %[inc]\n"
      "v_cvt_pk_bf16_f32 v194, v112, v113\n"
      "s_waitcnt vmcnt(9) lgkmcnt(0)\n"
      "s_barrier\n"
      "v_mfma_f32_16x16x32_bf16 v[40:43], a[96:99], v[200:203], v[40:43]\n"
      "ds_read_b128 a[64:67], v242 offset:32768\n"
      "v_exp_f32 v114, v134\n"
      "v_mfma_f32_16x16x32_bf16 v[44:47], a[96:99], v[204:207], v[44:47]\n"
      "ds_read_b128 a[68:71], v242 offset:36864\n"
      "ds_read_b128 a[72:75], v243 offset:32768\n"
      "v_mfma_f32_16x16x32_bf16 v[60:63], a[100:103], v[204:207], v[60:63]\n"
      "v_exp_f32 v115, v135\n"
      "v_mfma_f32_16x16x32_bf16 v[56:59], a[100:103], v[200:203], v[56:59]\n"
      "ds_read_b128 a[76:79], v243 offset:36864\n"
      "v_cvt_pk_bf16_f32 v195, v114, v115\n"
      "v_mfma_f32_16x16x32_bf16 a[136:139], a[104:107], v[200:203], a[136:139]\n"
      "v_exp_f32 v112, v136\n"
      "v_mfma_f32_16x16x32_bf16 a[140:143], a[104:107], v[204:207], a[140:143]\n"
      "ds_read_b128 a[80:83], v242 offset:33792\n"
      "v_or3_b32 v116, v116, v194, v195\n"
      "v_mfma_f32_16x16x32_bf16 a[156:159], a[108:111], v[204:207], a[156:159]\n"
      "v_exp_f32 v113, v137\n"
      "v_mfma_f32_16x16x32_bf16 a[152:155], a[108:111], v[200:203], a[152:155]\n"
      "ds_read_b128 a[84:87], v242 offset:37888\n"
      "v_cvt_pk_bf16_f32 v196, v112, v113\n"
      "v_mfma_f32_16x16x32_bf16 v[72:75], a[112:115], v[200:203], v[72:75]\n"
      "v_exp_f32 v114, v138\n"
      "v_mfma_f32_16x16x32_bf16 v[76:79], a[112:115], v[204:207], v[76:79]\n"
      "ds_read_b128 a[88:91], v243 offset:33792\n"
      "ds_read_b128 a[92:95], v243 offset:37888\n"
      "v_mfma_f32_16x16x32_bf16 v[92:95], a[116:119], v[204:207], v[92:95]\n"
      "v_exp_f32 v115, v139\n"
      "v_mfma_f32_16x16x32_bf16 v[88:91], a[116:119], v[200:203], v[88:91]\n"
      "v_cvt_pk_bf16_f32 v197, v114, v115\n"
      "v_mfma_f32_16x16x32_bf16 a[168:171], a[120:123], v[200:203], a[168:171]\n"
      "v_exp_f32 v112, v140\n"
      "v_mfma_f32_16x16x32_bf16 a[172:175], a[120:123], v[204:207], a[172:175]\n"
      "v_or3_b32 v116, v116, v196, v197\n"
      "v_mfma_f32_16x16x32_bf16 a[188:191], a[124:127], v[204:207], a[188:191]\n"
      "v_exp_f32 v113, v141\n"
      "v_mfma_f32_16x16x32_bf16 a[184:187], a[124:127], v[200:203], a[184:187]\n"
      "s_waitcnt lgkmcnt(0)\n"
      "v_cvt_pk_bf16_f32 v198, v112, v113\n"
      "v_mfma_f32_16x16x32_bf16 v[160:163], a[64:67], a[0:3], v[224:227]\n"
      "v_exp_f32 v114, v142\n"
      "v_mfma_f32_16x16x32_bf16 v[160:163], a[72:75], a[16:19], v[160:163]\n"
      "ds_read_b64_tr_b16 a[96:97], v244 offset:32768\n"
      "ds_read_b64_tr_b16 a[98:99], v244 offset:36864\n"
      "v_mfma_f32_16x16x32_bf16 v[160:163], a[80:83], a[32:35], v[160:163]\n"
      "v_exp_f32 v115, v143\n"
      "v_mfma_f32_16x16x32_bf16 v[160:163], a[88:91], a[48:51], v[160:163]\n"
      "ds_read_b64_tr_b16 a[100:101], v245 offset:32768\n"
      "v_cvt_pk_bf16_f32 v199, v114, v115\n"
      "v_mfma_f32_16x16x32_bf16 v[164:167], a[92:95], a[48:51], v[224:227]\n"
      "v_exp_f32 v112, v144\n"
      "v_mfma_f32_16x16x32_bf16 v[164:167], a[84:87], a[32:35], v[164:167]\n"
      "ds_read_b64_tr_b16 a[102:103], v245 offset:36864\n"
      "v_or3_b32 v116, v116, v198, v199\n"
      "v_mfma_f32_16x16x32_bf16 v[164:167], a[76:79], a[16:19], v[164:167]\n"
      "v_exp_f32 v113, v145\n"
      "v_mfma_f32_16x16x32_bf16 v[164:167], a[68:71], a[0:3], v[164:167]\n"
      "ds_read_b64_tr_b16 a[104:105], v246 offset:32768\n"
      "v_cvt_pk_bf16_f32 v200, v112, v113\n"
      "v_mfma_f32_16x16x32_bf16 v[172:175], a[68:71], a[4:7], v[228:231]\n"
      "v_exp_f32 v114, v146\n"
      "v_mfma_f32_16x16x32_bf16 v[172:175], a[76:79], a[20:23], v[172:175]\n"
      "ds_read_b64_tr_b16 a[106:107], v246 offset:36864\n"
      "ds_read_b64_tr_b16 a[108:109], v247 offset:32768\n"
      "v_mfma_f32_16x16x32_bf16 v[172:175], a[84:87], a[36:39], v[172:175]\n"
      "v_exp_f32 v115, v147\n"
      "v_mfma_f32_16x16x32_bf16 v[172:175], a[92:95], a[52:55], v[172:175]\n"
      "ds_read_b64_tr_b16 a[110:111], v247 offset:36864\n"
      "v_cvt_pk_bf16_f32 v201, v114, v115\n"
      "v_mfma_f32_16x16x32_bf16 v[168:171], a[88:91], a[52:55], v[228:231]\n"
      "v_exp_f32 v112, v148\n"
      "v_mfma_f32_16x16x32_bf16 v[168:171], a[80:83], a[36:39], v[168:171]\n"
      "ds_read_b64_tr_b16 a[112:113], v244 offset:33792\n"
      "v_or3_b32 v116, v116, v200, v201\n"
      "v_mfma_f32_16x16x32_bf16 v[168:171], a[72:75], a[20:23], v[168:171]\n"
      "v_exp_f32 v113, v149\n"
      "v_mfma_f32_16x16x32_bf16 v[168:171], a[64:67], a[4:7], v[168:171]\n"
      "ds_read_b64_tr_b16 a[114:115], v244 offset:37888\n"
      "v_cvt_pk_bf16_f32 v202, v112, v113\n"
      "v_mfma_f32_16x16x32_bf16 v[176:179], a[64:67], a[8:11], v[232:235]\n"
      "v_exp_f32 v114, v150\n"
      "v_mfma_f32_16x16x32_bf16 v[176:179], a[72:75], a[24:27], v[176:179]\n"
      "ds_read_b64_tr_b16 a[116:117], v245 offset:33792\n"
      "ds_read_b64_tr_b16 a[118:119], v245 offset:37888\n"
      "v_mfma_f32_16x16x32_bf16 v[176:179], a[80:83], a[40:43], v[176:179]\n"
      "v_exp_f32 v115, v151\n"
      "v_mfma_f32_16x16x32_bf16 v[176:179], a[88:91], a[56:59], v[176:179]\n"
      "ds_read_b64_tr_b16 a[120:121], v246 offset:33792\n"
      "v_cvt_pk_bf16_f32 v203, v114, v115\n"
      "v_mfma_f32_16x16x32_bf16 v[180:183], a[92:95], a[56:59], v[232:235]\n"
      "v_exp_f32 v112, v152\n"
      "v_mfma_f32_16x16x32_bf16 v[180:183], a[84:87], a[40:43], v[180:183]\n"
      "ds_read_b64_tr_b16 a[122:123], v246 offset:37888\n"
      "v_or3_b32 v116, v116, v202, v203\n"
      "v_mfma_f32_16x16x32_bf16 v[180:183], a[76:79], a[24:27], v[180:183]\n"
      "v_exp_f32 v113, v153\n"
      "v_mfma_f32_16x16x32_bf16 v[180:183], a[68:71], a[8:11], v[180:183]\n"
      "ds_read_b64_tr_b16 a[124:125], v247 offset:33792\n"
      "v_cvt_pk_bf16_f32 v204, v112, v113\n"
      "v_mfma_f32_16x16x32_bf16 v[188:191], a[68:71], a[12:15], v[236:239]\n"
      "v_exp_f32 v114, v154\n"
      "v_mfma_f32_16x16x32_bf16 v[188:191], a[76:79], a[28:31], v[188:191]\n"
      "ds_read_b64_tr_b16 a[126:127], v247 offset:37888\n"
      "v_mfma_f32_16x16x32_bf16 v[188:191], a[84:87], a[44:47], v[188:191]\n"
      "v_exp_f32 v115, v155\n"
      "v_mfma_f32_16x16x32_bf16 v[188:191], a[92:95], a[60:63], v[188:191]\n"
      "v_cvt_pk_bf16_f32 v205, v114, v115\n"
      "v_mfma_f32_16x16x32_bf16 v[184:187], a[88:91], a[60:63], v[236:239]\n"
      "v_exp_f32 v112, v156\n"
      "v_mfma_f32_16x16x32_bf16 v[184:187], a[80:83], a[44:47], v[184:187]\n"
      "v_or3_b32 v116, v116, v204, v205\n"
      "v_mfma_f32_16x16x32_bf16 v[184:187], a[72:75], a[28:31], v[184:187]\n"
      "v_exp_f32 v113, v157\n"
      "v_mfma_f32_16x16x32_bf16 v[184:187], a[64:67], a[12:15], v[184:187]\n"
      "s_waitcnt lgkmcnt(10)\n"
      "v_cvt_pk_bf16_f32 v206, v112, v113\n"
      "v_mfma_f32_16x16x32_bf16 v[32:35], a[96:99], v[208:211], v[32:35]\n"
      "v_exp_f32 v114, v158\n"
      "v_mfma_f32_16x16x32_bf16 v[48:51], a[100:103], v[208:211], v[48:51]\n"
      "v_mfma_f32_16x16x32_bf16 a[128:131], a[104:107], v[208:211], a[128:131]\n"
      "s_waitcnt lgkmcnt(6)\n"
      "v_exp_f32 v115, v159\n"
      "v_mfma_f32_16x16x32_bf16 a[144:147], a[108:111], v[208:211], a[144:147]\n"
      "v_cvt_pk_bf16_f32 v207, v114, v115\n"
      "v_mfma_f32_16x16x32_bf16 v[64:67], a[112:115], v[208:211], v[64:67]\n"
      "s_waitcnt lgkmcnt(2)\n"
      "v_mfma_f32_16x16x32_bf16 v[80:83], a[116:119], v[208:211], v[80:83]\n"
      "v_or3_b32 v116, v116, v206, v207\n"
      "v_mfma_f32_16x16x32_bf16 a[160:163], a[120:123], v[208:211], a[160:163]\n"
      "s_mov_b32 s64, 2\n"
      "s_waitcnt lgkmcnt(0)\n"
      "v_and_b32 v116, 0x40004000, v116\n"
      "v_mfma_f32_16x16x32_bf16 a[176:179], a[124:127], v[208:211], a[176:179]\n"
      "v_cmp_ne_u32 vcc, 0, v116\n"
      "s_cbranch_vccnz FB0_%=\n"
      "RET0_2_%=:\n"
      "v_mfma_f32_16x16x32_bf16 a[180:183], a[124:127], v[212:215], a[180:183]\n"
      "v_exp_f32 v112, v160\n"
      "v_mfma_f32_16x16x32_bf16 a[164:167], a[120:123], v[212:215], a[164:167]\n"
      "v_xor_b32 v242, 0x10000, v242\n"
      "v_mfma_f32_16x16x32_bf16 a[192:195], a[196:199], v[192:195], a[192:195]\n"
      "s_add_u32 m0, s62, 0\n"
      "v_exp_f32 v113, v161\n"
      "v_mfma_f32_16x16x32_bf16 v[84:87], a[116:119], v[212:215], v[84:87]\n"
      "buffer_load_dwordx4 v248, s[20:23], s60 offen nt lds\n"
      "v_cvt_pk_bf16_f32 v208, v112, v113\n"
      "v_xor_b32 v243, 0x10000, v243\n"
      "v_mfma_f32_16x16x32_bf16 v[68:71], a[112:115], v[212:215], v[68:71]\n"
      "s_add_u32 m0, s62, 896\n"
      "v_exp_f32 v114, v162\n"
      "v_mfma_f32_16x16x32_bf16 a[192:195], a[200:203], v[196:199], a[192:195]\n"
      "buffer_load_dwordx4 v248, s[20:23], s60 offen offset:128 nt lds\n"
      "v_xor_b32 v244, 0x10000, v244\n"
      "v_mfma_f32_16x16x32_bf16 a[148:151], a[108:111], v[212:215], a[148:151]\n"
      "s_add_u32 m0, s62, 8192\n"
      "v_exp_f32 v115, v163\n"
      "v_mfma_f32_16x16x32_bf16 a[132:135], a[104:107], v[212:215], a[132:135]\n"
      "s_memrealtime s[68:69]\n"
      "buffer_load_dwordx4 v120, s[20:23], s60 offen nt lds\n"
      "v_cvt_pk_bf16_f32 v209, v114, v115\n"
      "v_xor_b32 v245, 0x10000, v245\n"
      "v_mfma_f32_16x16x32_bf16 a[192:195], a[204:207], v[200:203], a[192:195]\n"
      "s_add_u32 m0, s62, 9088\n"
      "v_exp_f32 v112, v164\n"
      "v_mfma_f32_16x16x32_bf16 v[52:55], a[100:103], v[212:215], v[52:55]\n"
      "s_add_u32 s62, s62, 0x8000\n"
      "buffer_load_dwordx4 v120, s[20:23], s60 offen offset:128 nt lds\n"
      "v_or_b32 v116, v208, v209\n"
      "v_xor_b32 v246, 0x10000, v246\n"
      "v_mfma_f32_16x16x32_bf16 v[36:39], a[96:99], v[212:215], v[36:39]\n"
      "s_and_b32 s62, s62, 0x1ffff\n"
      "v_exp_f32 v113, v165\n"
      "v_mfma_f32_16x16x32_bf16 a[192:195], a[208:211], v[204:207], a[192:195]\n"
      "s_add_u32 s60, s60, s78\n"
      "v_cvt_pk_bf16_f32 v210, v112, v113\n"
      "v_xor_b32 v247, 0x10000, v247\n"
      "s_waitcnt vmcnt(8)\n"
      "s_sub_u32 s63, 11, s40\n"
      "LOOP2_%=:\n"
      "s_waitcnt vmcnt(9) lgkmcnt(0)\n"
      "s_sub_u32 s67, s68, s90\n"
      "s_cmp_lt_i32 s67, 0\n"
      "s_cbranch_scc1 GW4_%=\n"
      "GX4_%=:\n"
      "s_barrier\n"
      "v_mfma_f32_16x16x32_bf16 v[40:43], a[96:99], v[216:219], v[40:43]\n"
      "s_memrealtime s[68:69]\n"
      "ds_read_b128 a[64:67], v242 offset:0\n"
      "v_exp_f32 v114, v166\n"
      "v_mfma_f32_16x16x32_bf16 v[44:47], a[96:99], v[220:223], v[44:47]\n"
      "s_add_u32 m0, s77, 16384\n"
      "ds_read_b128 a[68:71], v242 offset:4096\n"
      "ds_read_b128 a[72:75], v243 offset:0\n"
      "v_mfma_f32_16x16x32_bf16 v[60:63], a[100:103], v[220:223], v[60:63]\n"
      "buffer_load_dwordx4 v249, s[24:27], s61 offen nt lds\n"
      "v_exp_f32 v115, v167\n"
      "v_mfma_f32_16x16x32_bf16 v[56:59], a[100:103], v[216:219], v[56:59]\n"
      "s_add_u32 m0, s77, 17408\n"
      "ds_read_b128 a[76:79], v243 offset:4096\n"
      "v_cvt_pk_bf16_f32 v211, v114, v115\n"
      "v_mfma_f32_16x16x32_bf16 a[136:139], a[104:107], v[216:219], a[136:139]\n"
      "buffer_load_dwordx4 v121, s[24:27], s61 offen nt lds\n"
      "v_exp_f32 v112, v168\n"
      "v_mfma_f32_16x16x32_bf16 a[140:143], a[104:107], v[220:223], a[140:143]\n"
      "s_add_u32 m0, s77, 24576\n"
      "ds_read_b128 a[80:83], v242 offset:1024\n"
      "v_or3_b32 v116, v116, v210, v211\n"
      "v_mfma_f32_16x16x32_bf16 a[156:159], a[108:111], v[220:223], a[156:159]\n"
      "buffer_load_dwordx4 v122, s[24:27], s61 offen nt lds\n"
      "v_exp_f32 v113, v169\n"
      "v_mfma_f32_16x16x32_bf16 a[152:155], a[108:111], v[216:219], a[152:155]\n"
      "s_add_u32 m0, s77, 25600\n"
      "ds_read_b128 a[84:87], v242 offset:5120\n"
      "v_cvt_pk_bf16_f32 v212, v112, v113\n"
      "v_mfma_f32_16x16x32_bf16 v[72:75], a[112:115], v[216:219], v[72:75]\n"
      "s_add_u32 s77, s77, 0x8000\n"
      "buffer_load_dwordx4 v123, s[24:27], s61 offen nt lds\n"
      "v_exp_f32 v114, v170\n"
      "v_mfma_f32_16x16x32_bf16 v[76:79], a[112:115], v[220:223], v[76:79]\n"
      "s_and_b32 s77, s77, 0x1ffff\n"
      "ds_read_b128 a[88:91], v243 offset:1024\n"
      "ds_read_b128 a[92:95], v243 offset:5120\n"
      "v_mfma_f32_16x16x32_bf16 v[92:95], a[116:119], v[220:223], v[92:95]\n"
      "s_add_u32 s61, s61, s78\n"
      "v_exp_f32 v115, v171\n"
      "v_mfma_f32_16x16x32_bf16 v[88:91], a[116:119], v[216:219], v[88:91]\n"
      "v_cvt_pk_bf16_f32 v213, v114, v115\n"
      "v_mfma_f32_16x16x32_bf16 a[168:171], a[120:123], v[216:219], a[168:171]\n"
      "v_exp_f32 v112, v172\n"
      "v_mfma_f32_16x16x32_bf16 a[172:175], a[120:123], v[220:223], a[172:175]\n"
      "v_or3_b32 v116, v116, v212, v213\n"
      "v_mfma_f32_16x16x32_bf16 a[188:191], a[124:127], v[220:223], a[188:191]\n"
      "v_exp_f32 v113, v173\n"
      "v_mfma_f32_16x16x32_bf16 a[184:187], a[124:127], v[216:219], a[184:187]\n"
      "s_waitcnt lgkmcnt(0)\n"
      "v_cvt_pk_bf16_f32 v214, v112, v113\n"
      "v_mfma_f32_16x16x32_bf16 v[128:131], a[64:67], a[0:3], v[224:227]\n"
      "s_sub_u32 s67, s68, s66\n"
      "v_exp_f32 v114, v174\n"
      "v_mfma_f32_16x16x32_bf16 v[128:131], a[72:75], a[16:19], v[128:131]\n"
      "s_cmp_gt_i32 s67, s70\n"
      "v_mfma_f32_16x16x32_bf16 v[128:131], a[80:83], a[32:35], v[128:131]\n"
      "s_cselect_b32 s69, s86, 0\n"
      "v_exp_f32 v115, v175\n"
      "v_mfma_f32_16x16x32_bf16 v[128:131], a[88:91], a[48:51], v[128:131]\n"
      "s_add_u32 s66, s66, s69\n"
      "v_cvt_pk_bf16_f32 v215, v114, v115\n"
      "v_mfma_f32_16x16x32_bf16 v[132:135], a[92:95], a[48:51], v[224:227]\n"
      "s_add_u32 s67, s67, s101\n"
      "v_exp_f32 v112, v176\n"
      "v_mfma_f32_16x16x32_bf16 v[132:135], a[84:87], a[32:35], v[132:135]\n"
      "s_min_i32 s70, s70, s67\n"
      "v_or3_b32 v116, v116, v214, v215\n"
      "v_mfma_f32_16x16x32_bf16 v[132:135], a[76:79], a[16:19], v[132:135]\n"
      "s_max_i32 s70, s70, 85\n"
      "v_exp_f32 v113, v177\n"
      "v_mfma_f32_16x16x32_bf16 v[132:135], a[68:71], a[0:3], v[132:135]\n"
      "s_add_u32 s66, s66, s86\n"
      "v_cvt_pk_bf16_f32 v216, v112, v113\n"
      "v_mfma_f32_16x16x32_bf16 v[140:143], a[68:71], a[4:7], v[228:231]\n"
      "s_and_b32 s76, s80, 31\n"
      "v_exp_f32 v114, v178\n"
      "v_mfma_f32_16x16x32_bf16 v[140:143], a[76:79], a[20:23], v[140:143]\n"
      "s_lshr_b64 s[80:81], s[80:81], 5\n"
      "v_mfma_f32_16x16x32_bf16 v[140:143], a[84:87], a[36:39], v[140:143]\n"
      "s_add_u32 s66, s66, s76\n"
      "v_exp_f32 v115, v179\n"
      "v_mfma_f32_16x16x32_bf16 v[140:143], a[92:95], a[52:55], v[140:143]\n"
      "s_sub_u32 s67, s68, s82\n"
      "v_cvt_pk_bf16_f32 v217, v114, v115\n"
      "v_mfma_f32_16x16x32_bf16 v[136:139], a[88:91], a[52:55], v[228:231]\n"
      "s_min_i32 s76, s67, s83\n"
      "v_exp_f32 v112, v180\n"
      "s_waitcnt vmcnt(8)\n"
      "s_barrier\n"
      "v_mfma_f32_16x16x32_bf16 v[136:139], a[80:83], a[36:39], v[136:139]\n"
      "s_mov_b32 s83, s67\n"
      "ds_read_b64_tr_b16 a[96:97], v244 offset:0\n"
      "v_or3_b32 v116, v116, v216, v217\n"
      "v_mfma_f32_16x16x32_bf16 v[136:139], a[72:75], a[20:23], v[136:139]\n"
      "s_sub_u32 s76, s76, 36\n"
      "v_exp_f32 v113, v181\n"
      "v_mfma_f32_16x16x32_bf16 v[136:139], a[64:67], a[4:7], v[136:139]\n"
      "s_min_i32 s76, s76, 148\n"
      "ds_read_b64_tr_b16 a[98:99], v244 offset:4096\n"
      "v_cvt_pk_bf16_f32 v218, v112, v113\n"
      "v_mfma_f32_16x16x32_bf16 v[144:147], a[64:67], a[8:11], v[232:235]\n"
      "s_max_i32 s76, s76, 70\n"
      "v_exp_f32 v114, v182\n"
      "v_mfma_f32_16x16x32_bf16 v[144:147], a[72:75], a[24:27], v[144:147]\n"
      "s_mov_b32 s82, s68\n"
      "ds_read_b64_tr_b16 a[100:101], v245 offset:0\n"
      "ds_read_b64_tr_b16 a[102:103], v245 offset:4096\n"
      "v_mfma_f32_16x16x32_bf16 v[144:147], a[80:83], a[40:43], v[144:147]\n"
      "s_add_u32 s65, s82, s76\n"
      "v_exp_f32 v115, v183\n"
      "v_mfma_f32_16x16x32_bf16 v[144:147], a[88:91], a[56:59], v[144:147]\n"
      "s_sub_u32 s93, s93, 1\n"
      "ds_read_b64_tr_b16 a[104:105], v246 offset:0\n"
      "v_cvt_pk_bf16_f32 v219, v114, v115\n"
      "v_mfma_f32_16x16x32_bf16 v[148:151], a[92:95], a[56:59], v[232:235]\n"
      "s_cmp_le_i32 s93, 14\n"
      "v_exp_f32 v112, v184\n"
      "v_mfma_f32_16x16x32_bf16 v[148:151], a[84:87], a[40:43], v[148:151]\n"
      "s_cselect_b32 s86, 107, s86\n"
      "ds_read_b64_tr_b16 a[106:107], v246 offset:4096\n"
      "v_or3_b32 v116, v116, v218, v219\n"
      "v_mfma_f32_16x16x32_bf16 v[148:151], a[76:79], a[24:27], v[148:151]\n"
      "s_sub_u32 s67, s65, s66\n"
      "v_exp_f32 v113, v185\n"
      "v_mfma_f32_16x16x32_bf16 v[148:151], a[68:71], a[8:11], v[148:151]\n"
      "s_cmp_lt_i32 s67, 0\n"
      "ds_read_b64_tr_b16 a[108:109], v247 offset:0\n"
      "v_cvt_pk_bf16_f32 v220, v112, v113\n"
      "v_mfma_f32_16x16x32_bf16 v[156:159], a[68:71], a[12:15], v[236:239]\n"
      "s_cselect_b32 s88, s66, s65\n"
      "v_exp_f32 v114, v186\n"
      "v_mfma_f32_16x16x32_bf16 v[156:159], a[76:79], a[28:31], v[156:159]\n"
      "s_add_u32 s90, s88, s91\n"
      "ds_read_b64_tr_b16 a[110:111], v247 offset:4096\n"
      "ds_read_b64_tr_b16 a[112:113], v244 offset:1024\n"
      "v_mfma_f32_16x16x32_bf16 v[156:159], a[84:87], a[44:47], v[156:159]\n"
      "v_exp_f32 v115, v187\n"
      "v_mfma_f32_16x16x32_bf16 v[156:159], a[92:95], a[60:63], v[156:159]\n"
      "ds_read_b64_tr_b16 a[114:115], v244 offset:5120\n"
      "v_cvt_pk_bf16_f32 v221, v114, v115\n"
      "v_mfma_f32_16x16x32_bf16 v[152:155], a[88:91], a[60:63], v[236:239]\n"
      "v_exp_f32 v112, v188\n"
      "v_mfma_f32_16x16x32_bf16 v[152:155], a[80:83], a[44:47], v[152:155]\n"
      "ds_read_b64_tr_b16 a[116:117], v245 offset:1024\n"
      "v_or3_b32 v116, v116, v220, v221\n"
      "v_mfma_f32_16x16x32_bf16 v[152:155], a[72:75], a[28:31], v[152:155]\n"
      "v_exp_f32 v113, v189\n"
      "v_mfma_f32_16x16x32_bf16 v[152:155], a[64:67], a[12:15], v[152:155]\n"
      "ds_read_b64_tr_b16 a[118:119], v245 offset:5120\n"
      "v_cvt_pk_bf16_f32 v222, v112, v113\n"
      "s_waitcnt lgkmcnt(6)\n"
      "v_mfma_f32_16x16x32_bf16 v[32:35], a[96:99], v[192:195], v[32:35]\n"
      "v_exp_f32 v114, v190\n"
      "v_mfma_f32_16x16x32_bf16 v[48:51], a[100:103], v[192:195], v[48:51]\n"
      "ds_read_b64_tr_b16 a[120:121], v246 offset:1024\n"
      "ds_read_b64_tr_b16 a[122:123], v246 offset:5120\n"
      "v_mfma_f32_16x16x32_bf16 a[128:131], a[104:107], v[192:195], a[128:131]\n"
      "s_waitcnt lgkmcnt(4)\n"
      "v_exp_f32 v115, v191\n"
      "v_mfma_f32_16x16x32_bf16 a[144:147], a[108:111], v[192:195], a[144:147]\n"
      "ds_read_b64_tr_b16 a[124:125], v247 offset:1024\n"
      "v_cvt_pk_bf16_f32 v223, v114, v115\n"
      "v_mfma_f32_16x16x32_bf16 v[64:67], a[112:115], v[192:195], v[64:67]\n"
      "ds_read_b64_tr_b16 a[126:127], v247 offset:5120\n"
      "s_waitcnt lgkmcnt(2)\n"
      "v_mfma_f32_16x16x32_bf16 v[80:83], a[116:119], v[192:195], v[80:83]\n"
      "v_or3_b32 v116, v116, v222, v223\n"
      "v_mfma_f32_16x16x32_bf16 a[160:163], a[120:123], v[192:195], a[160:163]\n"
      "s_mov_b32 s64, 3\n"
      "s_waitcnt lgkmcnt(0)\n"
      "v_and_b32 v116, 0x40004000, v116\n"
      "v_mfma_f32_16x16x32_bf16 a[176:179], a[124:127], v[192:195], a[176:179]\n"
      "v_cmp_ne_u32 vcc, 0, v116\n"
      "s_cbranch_vccnz FB1_%=\n"
      "RET1_3_%=:\n"
      "v_mfma_f32_16x16x32_bf16 a[180:183], a[124:127], v[196:199], a[180:183]\n"
      "v_exp_f32 v112, v128\n"
      "v_mfma_f32_16x16x32_bf16 a[164:167], a[120:123], v[196:199], a[164:167]\n"
      "v_mfma_f32_16x16x32_bf16 a[192:195], a[196:199], v[208:211], a[192:195]\n"
      "s_add_u32 m0, s62, 0\n"
      "v_exp_f32 v113, v129\n"
      "v_mfma_f32_16x16x32_bf16 v[84:87], a[116:119], v[196:199], v[84:87]\n"
      "buffer_load_dwordx4 v248, s[20:23], s60 offen nt lds\n"
      "v_cvt_pk_bf16_f32 v192, v112, v113\n"
      "v_mfma_f32_16x16x32_bf16 v[68:71], a[112:115], v[196:199], v[68:71]\n"
      "s_add_u32 m0, s62, 896\n"
      "v_exp_f32 v114, v130\n"
      "v_mfma_f32_16x16x32_bf16 a[192:195], a[200:203], v[212:215], a[192:195]\n"
      "buffer_load_dwordx4 v248, s[20:23], s60 offen offset:128 nt lds\n"
      "v_mfma_f32_16x16x32_bf16 a[148:151], a[108:111], v[196:199], a[148:151]\n"
      "s_add_u32 m0, s62, 8192\n"
      "v_exp_f32 v115, v131\n"
      "v_mfma_f32_16x16x32_bf16 a[132:135], a[104:107], v[196:199], a[132:135]\n"
      "s_memrealtime s[68:69]\n"
      "buffer_load_dwordx4 v120, s[20:23], s60 offen nt lds\n"
      "v_cvt_pk_bf16_f32 v193, v114, v115\n"
      "v_mfma_f32_16x16x32_bf16 a[192:195], a[204:207], v[216:219], a[192:195]\n"
      "s_add_u32 m0, s62, 9088\n"
      "v_exp_f32 v112, v132\n"
      "v_mfma_f32_16x16x32_bf16 v[52:55], a[100:103], v[196:199], v[52:55]\n"
      "s_add_u32 s62, s62, 0x8000\n"
      "buffer_load_dwordx4 v120, s[20:23], s60 offen offset:128 nt lds\n"
      "v_or_b32 v116, v192, v193\n"
      "v_mfma_f32_16x16x32_bf16 v[36:39], a[96:99], v[196:199], v[36:39]\n"
      "s_and_b32 s62, s62, 0x1ffff\n"
      "v_exp_f32 v113, v133\n"
      "v_mfma_f32_16x16x32_bf16 a[192:195], a[208:211], v[220:223], a[192:195]\n"
      "s_add_u32 s60, s60, %[inc]\n"
      "v_cvt_pk_bf16_f32 v194, v112, v113\n"
      "s_waitcnt vmcnt(8) lgkmcnt(0)\n"
      "s_sub_u32 s67, s68, s90\n"
      "s_cmp_lt_i32 s67, 0\n"
      "s_cbranch_scc1 GW5_%=\n"
      "GX5_%=:\n"
      "s_barrier\n"
      "v_mfma_f32_16x16x32_bf16 v[40:43], a[96:99], v[200:203], v[40:43]\n"
      "s_memrealtime s[68:69]\n"
      "buffer_load_dword v250, v251, s[28:31], 0 offen sc1\n"
      "ds_read_b128 a[64:67], v242 offset:32768\n"
      "v_exp_f32 v114, v134\n"
      "v_mfma_f32_16x16x32_bf16 v[44:47], a[96:99], v[204:207], v[44:47]\n"
      "s_add_u32 m0, s77, 16384\n"
      "ds_read_b128 a[68:71], v242 offset:36864\n"
      "ds_read_b128 a[72:75], v243 offset:32768\n"
      "v_mfma_f32_16x16x32_bf16 v[60:63], a[100:103], v[204:207], v[60:63]\n"
      "buffer_load_dwordx4 v249, s[24:27], s61 offen nt lds\n"
      "v_exp_f32 v115, v135\n"
      "v_mfma_f32_16x16x32_bf16 v[56:59], a[100:103], v[200:203], v[56:59]\n"
      "s_add_u32 m0, s77, 17408\n"
      "ds_read_b128 a[76:79], v243 offset:36864\n"
      "v_cvt_pk_bf16_f32 v195, v114, v115\n"
      "v_mfma_f32_16x16x32_bf16 a[136:139], a[104:107], v[200:203], a[136:139]\n"
      "buffer_load_dwordx4 v121, s[24:27], s61 offen nt lds\n"
      "v_exp_f32 v112, v136\n"
      "v_mfma_f32_16x16x32_bf16 a[140:143], a[104:107], v[204:207], a[140:143]\n"
      "s_add_u32 m0, s77, 24576\n"
      "ds_read_b128 a[80:83], v242 offset:33792\n"
      "v_or3_b32 v116, v116, v194, v195\n"
      "v_mfma_f32_16x16x32_bf16 a[156:159], a[108:111], v[204:207], a[156:159]\n"
      "buffer_load_dwordx4 v122, s[24:27], s61 offen nt lds\n"
      "v_exp_f32 v113, v137\n"
      "v_mfma_f32_16x16x32_bf16 a[152:155], a[108:111], v[200:203], a[152:155]\n"
      "s_add_u32 m0, s77, 25600\n"
      "ds_read_b128 a[84:87], v242 offset:37888\n"
      "v_cvt_pk_bf16_f32 v196, v112, v113\n"
      "v_mfma_f32_16x16x32_bf16 v[72:75], a[112:115], v[200:203], v[72:75]\n"
      "s_add_u32 s77, s77, 0x8000\n"
      "buffer_load_dwordx4 v123, s[24:27], s61 offen nt lds\n"
      "v_exp_f32 v114, v138\n"
      "v_mfma_f32_16x16x32_bf16 v[76:79], a[112:115], v[204:207], v[76:79]\n"
      "s_and_b32 s77, s77, 0x1ffff\n"
      "ds_read_b128 a[88:91], v243 offset:33792\n"
      "ds_read_b128 a[92:95], v243 offset:37888\n"
      "v_mfma_f32_16x16x32_bf16 v[92:95], a[116:119], v[204:207], v[92:95]\n"
      "s_add_u32 s61, s61, %[inc]\n"
      "v_exp_f32 v115, v139\n"
      "v_mfma_f32_16x16x32_bf16 v[88:91], a[116:119], v[200:203], v[88:91]\n"
      "v_cvt_pk_bf16_f32 v197, v114, v115\n"
      "v_mfma_f32_16x16x32_bf16 a[168:171], a[120:123], v[200:203], a[168:171]\n"
      "v_exp_f32 v112, v140\n"
      "v_mfma_f32_16x16x32_bf16 a[172:175], a[120:123], v[204:207], a[172:175]\n"
      "v_or3_b32 v116, v116, v196, v197\n"
      "v_mfma_f32_16x16x32_bf16 a[188:191], a[124:127], v[204:207], a[188:191]\n"
      "v_exp_f32 v113, v141\n"
      "v_mfma_f32_16x16x32_bf16 a[184:187], a[124:127], v[200:203], a[184:187]\n"
      "s_waitcnt lgkmcnt(0)\n"
      "v_cvt_pk_bf16_f32 v198, v112, v113\n"
      "v_mfma_f32_16x16x32_bf16 v[160:163], a[64:67], a[0:3], v[224:227]\n"
      "s_sub_u32 s67, s68, s66\n"
      "v_exp_f32 v114, v142\n"
      "v_mfma_f32_16x16x32_bf16 v[160:163], a[72:75], a[16:19], v[160:163]\n"
      "s_cmp_gt_i32 s67, s70\n"
      "v_mfma_f32_16x16x32_bf16 v[160:163], a[80:83], a[32:35], v[160:163]\n"
      "s_cselect_b32 s69, s86, 0\n"
      "v_exp_f32 v115, v143\n"
      "v_mfma_f32_16x16x32_bf16 v[160:163], a[88:91], a[48:51], v[160:163]\n"
      "s_add_u32 s66, s66, s69\n"
      "v_cvt_pk_bf16_f32 v199, v114, v115\n"
      "v_mfma_f32_16x16x32_bf16 v[164:167], a[92:95], a[48:51], v[224:227]\n"
      "s_add_u32 s67, s67, s101\n"
      "v_exp_f32 v112, v144\n"
      "v_mfma_f32_16x16x32_bf16 v[164:167], a[84:87], a[32:35], v[164:167]\n"
      "s_min_i32 s70, s70, s67\n"
      "v_or3_b32 v116, v116, v198, v199\n"
      "v_mfma_f32_16x16x32_bf16 v[164:167], a[76:79], a[16:19], v[164:167]\n"
      "s_max_i32 s70, s70, 85\n"
      "v_exp_f32 v113, v145\n"
      "v_mfma_f32_16x16x32_bf16 v[164:167], a[68:71], a[0:3], v[164:167]\n"
      "s_add_u32 s66, s66, s86\n"
      "v_cvt_pk_bf16_f32 v200, v112, v113\n"
      "v_mfma_f32_16x16x32_bf16 v[172:175], a[68:71], a[4:7], v[228:231]\n"
      "s_and_b32 s76, s80, 31\n"
      "v_exp_f32 v114, v146\n"
      "v_mfma_f32_16x16x32_bf16 v[172:175], a[76:79], a[20:23], v[172:175]\n"
      "s_lshr_b64 s[80:81], s[80:81], 5\n"
      "v_mfma_f32_16x16x32_bf16 v[172:175], a[84:87], a[36:39], v[172:175]\n"
      "s_add_u32 s66, s66, s76\n"
      "v_exp_f32 v115, v147\n"
      "v_mfma_f32_16x16x32_bf16 v[172:175], a[92:95], a[52:55], v[172:175]\n"
      "s_sub_u32 s67, s68, s82\n"
      "v_cvt_pk_bf16_f32 v201, v114, v115\n"
      "v_mfma_f32_16x16x32_bf16 v[168:171], a[88:91], a[52:55], v[228:231]\n"
      "s_min_i32 s76, s67, s83\n"
      "v_exp_f32 v112, v148\n"
      "s_waitcnt vmcnt(9)\n"
      "s_barrier\n"
      "v_mfma_f32_16x16x32_bf16 v[168:171], a[80:83], a[36:39], v[168:171]\n"
      "s_mov_b32 s83, s67\n"
      "ds_read_b64_tr_b16 a[96:97], v244 offset:32768\n"
      "v_or3_b32 v116, v116, v200, v201\n"
      "v_mfma_f32_16x16x32_bf16 v[168:171], a[72:75], a[20:23], v[168:171]\n"
      "s_sub_u32 s76, s76, 36\n"
      "v_exp_f32 v113, v149\n"
      "v_mfma_f32_16x16x32_bf16 v[168:171], a[64:67], a[4:7], v[168:171]\n"
      "s_min_i32 s76, s76, 148\n"
      "ds_read_b64_tr_b16 a[98:99], v244 offset:36864\n"
      "v_cvt_pk_bf16_f32 v202, v112, v113\n"
      "v_mfma_f32_16x16x32_bf16 v[176:179], a[64:67], a[8:11], v[232:235]\n"
      "s_max_i32 s76, s76, 70\n"
      "v_exp_f32 v114, v150\n"
      "v_mfma_f32_16x16x32_bf16 v[176:179], a[72:75], a[24:27], v[176:179]\n"
      "s_mov_b32 s82, s68\n"
      "ds_read_b64_tr_b16 a[100:101], v245 offset:32768\n"
      "ds_read_b64_tr_b16 a[102:103], v245 offset:36864\n"
      "v_mfma_f32_16x16x32_bf16 v[176:179], a[80:83], a[40:43], v[176:179]\n"
      "s_add_u32 s65, s82, s76\n"
      "v_exp_f32 v115, v151\n"
      "v_mfma_f32_16x16x32_bf16 v[176:179], a[88:91], a[56:59], v[176:179]\n"
      "s_sub_u32 s93, s93, 1\n"
      "ds_read_b64_tr_b16 a[104:105], v246 offset:32768\n"
      "v_cvt_pk_bf16_f32 v203, v114, v115\n"
      "v_mfma_f32_16x16x32_bf16 v[180:183], a[92:95], a[56:59], v[232:235]\n"
      "s_cmp_le_i32 s93, 14\n"
      "v_exp_f32 v112, v152\n"
      "v_mfma_f32_16x16x32_bf16 v[180:183], a[84:87], a[40:43], v[180:183]\n"
      "s_cselect_b32 s86, 107, s86\n"
      "ds_read_b64_tr_b16 a[106:107], v246 offset:36864\n"
      "v_or3_b32 v116, v116, v202, v203\n"
      "v_mfma_f32_16x16x32_bf16 v[180:183], a[76:79], a[24:27], v[180:183]\n"
      "s_sub_u32 s67, s65, s66\n"
      "v_exp_f32 v113, v153\n"
      "v_mfma_f32_16x16x32_bf16 v[180:183], a[68:71], a[8:11], v[180:183]\n"
      "s_cmp_lt_i32 s67, 0\n"
      "ds_read_b64_tr_b16 a[108:109], v247 offset:32768\n"
      "v_cvt_pk_bf16_f32 v204, v112, v113\n"
      "v_mfma_f32_16x16x32_bf16 v[188:191], a[68:71], a[12:15], v[236:239]\n"
      "s_cselect_b32 s88, s66, s65\n"
      "v_exp_f32 v114, v154\n"
      "v_mfma_f32_16x16x32_bf16 v[188:191], a[76:79], a[28:31], v[188:191]\n"
      "s_add_u32 s90, s88, s91\n"
      "ds_read_b64_tr_b16 a[110:111], v247 offset:36864\n"
      "ds_read_b64_tr_b16 a[112:113], v244 offset:33792\n"
      "v_mfma_f32_16x16x32_bf16 v[188:191], a[84:87], a[44:47], v[188:191]\n"
      "v_exp_f32 v115, v155\n"
      "v_mfma_f32_16x16x32_bf16 v[188:191], a[92:95], a[60:63], v[188:191]\n"
      "ds_read_b64_tr_b16 a[114:115], v244 offset:37888\n"
      "v_cvt_pk_bf16_f32 v205, v114, v115\n"
      "v_mfma_f32_16x16x32_bf16 v[184:187], a[88:91], a[60:63], v[236:239]\n"
      "v_exp_f32 v112, v156\n"
      "v_mfma_f32_16x16x32_bf16 v[184:187], a[80:83], a[44:47], v[184:187]\n"
      "ds_read_b64_tr_b16 a[116:117], v245 offset:33792\n"
      "v_or3_b32 v116, v116, v204, v205\n"
      "v_mfma_f32_16x16x32_bf16 v[184:187], a[72:75], a[28:31], v[184:187]\n"
      "v_exp_f32 v113, v157\n"
      "v_mfma_f32_16x16x32_bf16 v[184:187], a[64:67], a[12:15], v[184:187]\n"
      "ds_read_b64_tr_b16 a[118:119], v245 offset:37888\n"
      "v_cvt_pk_bf16_f32 v206, v112, v113\n"
      "s_waitcnt lgkmcnt(6)\n"
      "v_mfma_f32_16x16x32_bf16 v[32:35], a[96:99], v[208:211], v[32:35]\n"
      "v_exp_f32 v114, v158\n"
      "v_mfma_f32_16x16x32_bf16 v[48:51], a[100:103], v[208:211], v[48:51]\n"
      "ds_read_b64_tr_b16 a[120:121], v246 offset:33792\n"
      "ds_read_b64_tr_b16 a[122:123], v246 offset:37888\n"
      "v_mfma_f32_16x16x32_bf16 a[128:131], a[104:107], v[208:211], a[128:131]\n"
      "s_waitcnt lgkmcnt(4)\n"
      "v_exp_f32 v115, v159\n"
      "v_mfma_f32_16x16x32_bf16 a[144:147], a[108:111], v[208:211], a[144:147]\n"
      "ds_read_b64_tr_b16 a[124:125], v247 offset:33792\n"
      "v_cvt_pk_bf16_f32 v207, v114, v115\n"
      "v_mfma_f32_16x16x32_bf16 v[64:67], a[112:115], v[208:211], v[64:67]\n"
      "ds_read_b64_tr_b16 a[126:127], v247 offset:37888\n"
      "s_waitcnt lgkmcnt(2)\n"
      "v_mfma_f32_16x16x32_bf16 v[80:83], a[116:119], v[208:211], v[80:83]\n"
      "v_or3_b32 v116, v116, v206, v207\n"
      "v_mfma_f32_16x16x32_bf16 a[160:163], a[120:123], v[208:211], a[160:163]\n"
      "s_mov_b32 s64, 3\n"
      "s_waitcnt lgkmcnt(0)\n"
      "v_and_b32 v116, 0x40004000, v116\n"
      "v_mfma_f32_16x16x32_bf16 a[176:179], a[124:127], v[208:211], a[176:179]\n"
      "v_cmp_ne_u32 vcc, 0, v116\n"
      "s_cbranch_vccnz FB0_%=\n"
      "RET0_3_%=:\n"
      "v_mfma_f32_16x16x32_bf16 a[180:183], a[124:127], v[212:215], a[180:183]\n"
      "v_exp_f32 v112, v160\n"
      "v_mfma_f32_16x16x32_bf16 a[164:167], a[120:123], v[212:215], a[164:167]\n"
      "v_xor_b32 v242, 0x10000, v242\n"
      "v_mfma_f32_16x16x32_bf16 a[192:195], a[196:199], v[192:195], a[192:195]\n"
      "s_add_u32 m0, s62, 0\n"
      "v_exp_f32 v113, v161\n"
      "v_mfma_f32_16x16x32_bf16 v[84:87], a[116:119], v[212:215], v[84:87]\n"
      "buffer_load_dwordx4 v248, s[20:23], s60 offen nt lds\n"
      "v_cvt_pk_bf16_f32 v208, v112, v113\n"
      "v_xor_b32 v243, 0x10000, v243\n"
      "v_mfma_f32_16x16x32_bf16 v[68:71], a[112:115], v[212:215], v[68:71]\n"
      "s_add_u32 m0, s62, 896\n"
      "v_exp_f32 v114, v162\n"
      "v_mfma_f32_16x16x32_bf16 a[192:195], a[200:203], v[196:199], a[192:195]\n"
      "buffer_load_dwordx4 v248, s[20:23], s60 offen offset:128 nt lds\n"
      "v_xor_b32 v244, 0x10000, v244\n"
      "v_mfma_f32_16x16x32_bf16 a[148:151], a[108:111], v[212:215], a[148:151]\n"
      "s_add_u32 m0, s62, 8192\n"
      "v_exp_f32 v115, v163\n"
      "v_mfma_f32_16x16x32_bf16 a[132:135], a[104:107], v[212:215], a[132:135]\n"
      "s_memrealtime s[68:69]\n"
      "buffer_load_dwordx4 v120, s[20:23], s60 offen nt lds\n"
      "v_cvt_pk_bf16_f32 v209, v114, v115\n"
      "v_xor_b32 v245, 0x10000, v245\n"
      "v_mfma_f32_16x16x32_bf16 a[192:195], a[204:207], v[200:203], a[192:195]\n"
      "s_add_u32 m0, s62, 9088\n"
      "v_exp_f32 v112, v164\n"
      "v_mfma_f32_16x16x32_bf16 v[52:55], a[100:103], v[212:215], v[52:55]\n"
      "s_add_u32 s62, s62, 0x8000\n"
      "buffer_load_dwordx4 v120, s[20:23], s60 offen offset:128 nt lds\n"
      "v_or_b32 v116, v208, v209\n"
      "v_xor_b32 v246, 0x10000, v246\n"
      "v_mfma_f32_16x16x32_bf16 v[36:39], a[96:99], v[212:215], v[36:39]\n"
      "s_and_b32 s62, s62, 0x1ffff\n"
      "s_add_u32 s60, s60, s78\n"
      "v_exp_f32 v113, v165\n"
      "v_mfma_f32_16x16x32_bf16 a[192:195], a[208:211], v[204:207], a[192:195]\n"
      "s_sub_u32 s63, s63, 1\n"
      "v_cvt_pk_bf16_f32 v210, v112, v113\n"
      "v_xor_b32 v247, 0x10000, v247\n"
      "s_cbranch_scc0 LOOP2_%=\n"
      "s_waitcnt vmcnt(9) lgkmcnt(0)\n"
      "s_sub_u32 s67, s68, s90\n"
      "s_cmp_lt_i32 s67, 0\n"
      "s_cbranch_scc1 GW6_%=\n"
      "GX6_%=:\n"
      "s_barrier\n"
      "v_mfma_f32_16x16x32_bf16 v[40:43], a[96:99], v[216:219], v[40:43]\n"
      "s_memrealtime s[68:69]\n"
      "ds_read_b128 a[64:67], v242 offset:0\n"
      "v_exp_f32 v114, v166\n"
      "v_mfma_f32_16x16x32_bf16 v[44:47], a[96:99], v[220:223], v[44:47]\n"
      "s_add_u32 m0, %[wb], 50176\n"
      "ds_read_b128 a[68:71], v242 offset:4096\n"
      "ds_read_b128 a[72:75], v243 offset:0\n"
      "v_mfma_f32_16x16x32_bf16 v[60:63], a[100:103], v[220:223], v[60:63]\n"
      "s_add_u32 s94, s61, s78\n"
      "buffer_load_dwordx4 v121, s[24:27], s61 offen nt lds\n"
      "v_exp_f32 v115, v167\n"
      "v_mfma_f32_16x16x32_bf16 v[56:59], a[100:103], v[216:219], v[56:59]\n"
      "s_add_u32 m0, %[wb], 58368\n"
      "ds_read_b128 a[76:79], v243 offset:4096\n"
      "v_cvt_pk_bf16_f32 v211, v114, v115\n"
      "v_mfma_f32_16x16x32_bf16 a[136:139], a[104:107], v[216:219], a[136:139]\n"
      "s_add_u32 s95, s61, 0x10000\n"
      "buffer_load_dwordx4 v123, s[24:27], s61 offen nt lds\n"
      "v_exp_f32 v112, v168\n"
      "v_mfma_f32_16x16x32_bf16 a[140:143], a[104:107], v[220:223], a[140:143]\n"
      "s_add_u32 s77, s94, 0x10000\n"
      "ds_read_b128 a[80:83], v242 offset:1024\n"
      "v_or3_b32 v116, v116, v210, v211\n"
      "v_mfma_f32_16x16x32_bf16 a[156:159], a[108:111], v[220:223], a[156:159]\n"
      "s_add_u32 s78, s61, 0x20000\n"
      "v_exp_f32 v113, v169\n"
      "v_mfma_f32_16x16x32_bf16 a[152:155], a[108:111], v[216:219], a[152:155]\n"
      "ds_read_b128 a[84:87], v242 offset:5120\n"
      "v_cvt_pk_bf16_f32 v212, v112, v113\n"
      "v_mfma_f32_16x16x32_bf16 v[72:75], a[112:115], v[216:219], v[72:75]\n"
      "v_exp_f32 v114, v170\n"
      "v_mfma_f32_16x16x32_bf16 v[76:79], a[112:115], v[220:223], v[76:79]\n"
      "ds_read_b128 a[88:91], v243 offset:1024\n"
      "ds_read_b128 a[92:95], v243 offset:5120\n"
      "v_mfma_f32_16x16x32_bf16 v[92:95], a[116:119], v[220:223], v[92:95]\n"
      "v_exp_f32 v115, v171\n"
      "v_mfma_f32_16x16x32_bf16 v[88:91], a[116:119], v[216:219], v[88:91]\n"
      "v_cvt_pk_bf16_f32 v213, v114, v115\n"
      "v_mfma_f32_16x16x32_bf16 a[168:171], a[120:123], v[216:219], a[168:171]\n"
      "v_exp_f32 v112, v172\n"
      "v_mfma_f32_16x16x32_bf16 a[172:175], a[120:123], v[220:223], a[172:175]\n"
      "v_or3_b32 v116, v116, v212, v213\n"
      "v_mfma_f32_16x16x32_bf16 a[188:191], a[124:127], v[220:223], a[188:191]\n"
      "v_exp_f32 v113, v173\n"
      "v_mfma_f32_16x16x32_bf16 a[184:187], a[124:127], v[216:219], a[184:187]\n"
      "s_waitcnt lgkmcnt(0)\n"
      "v_cvt_pk_bf16_f32 v214, v112, v113\n"
      "v_mfma_f32_16x16x32_bf16 v[128:131], a[64:67], a[0:3], v[224:227]\n"
      "s_sub_u32 s67, s68, s66\n"
      "v_exp_f32 v114, v174\n"
      "v_mfma_f32_16x16x32_bf16 v[128:131], a[72:75], a[16:19], v[128:131]\n"
      "s_cmp_gt_i32 s67, s70\n"
      "v_mfma_f32_16x16x32_bf16 v[128:131], a[80:83], a[32:35], v[128:131]\n"
      "s_cselect_b32 s69, s86, 0\n"
      "v_exp_f32 v115, v175\n"
      "v_mfma_f32_16x16x32_bf16 v[128:131], a[88:91], a[48:51], v[128:131]\n"
      "s_add_u32 s66, s66, s69\n"
      "v_cvt_pk_bf16_f32 v215, v114, v115\n"
      "v_mfma_f32_16x16x32_bf16 v[132:135], a[92:95], a[48:51], v[224:227]\n"
      "s_add_u32 s67, s67, s101\n"
      "v_exp_f32 v112, v176\n"
      "v_mfma_f32_16x16x32_bf16 v[132:135], a[84:87], a[32:35], v[132:135]\n"
      "s_min_i32 s70, s70, s67\n"
      "v_or3_b32 v116, v116, v214, v215\n"
      "v_mfma_f32_16x16x32_bf16 v[132:135], a[76:79], a[16:19], v[132:135]\n"
      "s_max_i32 s70, s70, 85\n"
      "v_exp_f32 v113, v177\n"
      "v_mfma_f32_16x16x32_bf16 v[132:135], a[68:71], a[0:3], v[132:135]\n"
      "s_add_u32 s66, s66, s86\n"
      "v_cvt_pk_bf16_f32 v216, v112, v113\n"
      "v_mfma_f32_16x16x32_bf16 v[140:143], a[68:71], a[4:7], v[228:231]\n"
      "s_and_b32 s76, s80, 31\n"
      "v_exp_f32 v114, v178\n"
      "v_mfma_f32_16x16x32_bf16 v[140:143], a[76:79], a[20:23], v[140:143]\n"
      "s_lshr_b64 s[80:81], s[80:81], 5\n"
      "v_mfma_f32_16x16x32_bf16 v[140:143], a[84:87], a[36:39], v[140:143]\n"
      "s_add_u32 s66, s66, s76\n"
      "v_exp_f32 v115, v179\n"
      "v_mfma_f32_16x16x32_bf16 v[140:143], a[92:95], a[52:55], v[140:143]\n"
      "s_sub_u32 s67, s68, s82\n"
      "v_cvt_pk_bf16_f32 v217, v114, v115\n"
      "v_mfma_f32_16x16x32_bf16 v[136:139], a[88:91], a[52:55], v[228:231]\n"
      "s_min_i32 s76, s67, s83\n"
      "v_exp_f32 v112, v180\n"
      "s_waitcnt vmcnt(6)\n"
      "s_barrier\n"
      "v_mfma_f32_16x16x32_bf16 v[136:139], a[80:83], a[36:39], v[136:139]\n"
      "s_mov_b32 s83, s67\n"
      "ds_read_b64_tr_b16 a[96:97], v244 offset:0\n"
      "v_or3_b32 v116, v116, v216, v217\n"
      "v_mfma_f32_16x16x32_bf16 v[136:139], a[72:75], a[20:23], v[136:139]\n"
      "s_add_u32 m0, s62, 0\n"
      "v_exp_f32 v113, v181\n"
      "v_mfma_f32_16x16x32_bf16 v[136:139], a[64:67], a[4:7], v[136:139]\n"
      "s_sub_u32 s76, s76, 36\n"
      "ds_read_b64_tr_b16 a[98:99], v244 offset:4096\n"
      "buffer_load_dwordx4 v248, s[20:23], s60 offen nt lds\n"
      "v_cvt_pk_bf16_f32 v218, v112, v113\n"
      "v_mfma_f32_16x16x32_bf16 v[144:147], a[64:67], a[8:11], v[232:235]\n"
      "s_add_u32 m0, s62, 896\n"
      "v_exp_f32 v114, v182\n"
      "v_mfma_f32_16x16x32_bf16 v[144:147], a[72:75], a[24:27], v[144:147]\n"
      "s_min_i32 s76, s76, 148\n"
      "ds_read_b64_tr_b16 a[100:101], v245 offset:0\n"
      "ds_read_b64_tr_b16 a[102:103], v245 offset:4096\n"
      "buffer_load_dwordx4 v248, s[20:23], s60 offen offset:128 nt lds\n"
      "v_mfma_f32_16x16x32_bf16 v[144:147], a[80:83], a[40:43], v[144:147]\n"
      "s_add_u32 m0, s62, 8192\n"
      "v_exp_f32 v115, v183\n"
      "v_mfma_f32_16x16x32_bf16 v[144:147], a[88:91], a[56:59], v[144:147]\n"
      "s_max_i32 s76, s76, 70\n"
      "ds_read_b64_tr_b16 a[104:105], v246 offset:0\n"
      "buffer_load_dwordx4 v120, s[20:23], s60 offen nt lds\n"
      "v_cvt_pk_bf16_f32 v219, v114, v115\n"
      "v_mfma_f32_16x16x32_bf16 v[148:151], a[92:95], a[56:59], v[232:235]\n"
      "s_add_u32 m0, s62, 9088\n"
      "v_exp_f32 v112, v184\n"
      "v_mfma_f32_16x16x32_bf16 v[148:151], a[84:87], a[40:43], v[148:151]\n"
      "s_add_u32 s62, s62, 0x8000\n"
      "ds_read_b64_tr_b16 a[106:107], v246 offset:4096\n"
      "buffer_load_dwordx4 v120, s[20:23], s60 offen offset:128 nt lds\n"
      "v_or3_b32 v116, v116, v218, v219\n"
      "v_mfma_f32_16x16x32_bf16 v[148:151], a[76:79], a[24:27], v[148:151]\n"
      "s_and_b32 s62, s62, 0x1ffff\n"
      "v_exp_f32 v113, v185\n"
      "v_mfma_f32_16x16x32_bf16 v[148:151], a[68:71], a[8:11], v[148:151]\n"
      "s_add_u32 s60, s60, %[inc]\n"
      "ds_read_b64_tr_b16 a[108:109], v247 offset:0\n"
      "v_cvt_pk_bf16_f32 v220, v112, v113\n"
      "v_mfma_f32_16x16x32_bf16 v[156:159], a[68:71], a[12:15], v[236:239]\n"
      "s_mov_b32 s82, s68\n"
      "v_exp_f32 v114, v186\n"
      "v_mfma_f32_16x16x32_bf16 v[156:159], a[76:79], a[28:31], v[156:159]\n"
      "s_sub_u32 s66, s66, s99\n"
      "ds_read_b64_tr_b16 a[110:111], v247 offset:4096\n"
      "ds_read_b64_tr_b16 a[112:113], v244 offset:1024\n"
      "v_mfma_f32_16x16x32_bf16 v[156:159], a[84:87], a[44:47], v[156:159]\n"
      "s_sub_u32 s82, s82, s99\n"
      "v_exp_f32 v115, v187\n"
      "v_mfma_f32_16x16x32_bf16 v[156:159], a[92:95], a[60:63], v[156:159]\n"
      "s_add_u32 s65, s82, s76\n"
      "ds_read_b64_tr_b16 a[114:115], v244 offset:5120\n"
      "v_cvt_pk_bf16_f32 v221, v114, v115\n"
      "v_mfma_f32_16x16x32_bf16 v[152:155], a[88:91], a[60:63], v[236:239]\n"
      "s_sub_u32 s93, s93, 1\n"
      "v_exp_f32 v112, v188\n"
      "v_mfma_f32_16x16x32_bf16 v[152:155], a[80:83], a[44:47], v[152:155]\n"
      "s_cmp_le_i32 s93, 14\n"
      "ds_read_b64_tr_b16 a[116:117], v245 offset:1024\n"
      "v_or3_b32 v116, v116, v220, v221\n"
      "v_mfma_f32_16x16x32_bf16 v[152:155], a[72:75], a[28:31], v[152:155]\n"
      "s_cselect_b32 s86, 107, s86\n"
      "v_exp_f32 v113, v189\n"
      "v_mfma_f32_16x16x32_bf16 v[152:155], a[64:67], a[12:15], v[152:155]\n"
      "ds_read_b64_tr_b16 a[118:119], v245 offset:5120\n"
      "v_cvt_pk_bf16_f32 v222, v112, v113\n"
      "s_waitcnt lgkmcnt(6)\n"
      "v_mfma_f32_16x16x32_bf16 v[32:35], a[96:99], v[192:195], v[32:35]\n"
      "s_sub_u32 s67, s65, s66\n"
      "v_exp_f32 v114, v190\n"
      "v_mfma_f32_16x16x32_bf16 v[48:51], a[100:103], v[192:195], v[48:51]\n"
      "s_cmp_lt_i32 s67, 0\n"
      "s_cselect_b32 s88, s66, s65\n"
      "ds_read_b64_tr_b16 a[120:121], v246 offset:1024\n"
      "ds_read_b64_tr_b16 a[122:123], v246 offset:5120\n"
      "v_mfma_f32_16x16x32_bf16 a[128:131], a[104:107], v[192:195], a[128:131]\n"
      "s_waitcnt lgkmcnt(4)\n"
      "v_exp_f32 v115, v191\n"
      "v_mfma_f32_16x16x32_bf16 a[144:147], a[108:111], v[192:195], a[144:147]\n"
      "s_add_u32 s90, s88, s91\n"
      "ds_read_b64_tr_b16 a[124:125], v247 offset:1024\n"
      "v_cvt_pk_bf16_f32 v223, v114, v115\n"
      "v_mfma_f32_16x16x32_bf16 v[64:67], a[112:115], v[192:195], v[64:67]\n"
      "ds_read_b64_tr_b16 a[126:127], v247 offset:5120\n"
      "s_waitcnt lgkmcnt(2)\n"
      "v_mfma_f32_16x16x32_bf16 v[80:83], a[116:119], v[192:195], v[80:83]\n"
      "v_or3_b32 v116, v116, v222, v223\n"
      "v_mfma_f32_16x16x32_bf16 a[160:163], a[120:123], v[192:195], a[160:163]\n"
      "s_mov_b32 s64, 4\n"
      "s_waitcnt lgkmcnt(0)\n"
      "v_and_b32 v116, 0x40004000, v116\n"
      "v_mfma_f32_16x16x32_bf16 a[176:179], a[124:127], v[192:195], a[176:179]\n"
      "v_cmp_ne_u32 vcc, 0, v116\n"
      "s_cbranch_vccnz FB1_%=\n"
      "RET1_4_%=:\n"
      "v_mfma_f32_16x16x32_bf16 a[180:183], a[124:127], v[196:199], a[180:183]\n"
      "v_exp_f32 v112, v128\n"
      "v_mfma_f32_16x16x32_bf16 a[164:167], a[120:123], v[196:199], a[164:167]\n"
      "v_xor_b32 v240, 0x10000, v244\n"
      "v_mfma_f32_16x16x32_bf16 a[192:195], a[196:199], v[208:211], a[192:195]\n"
      "v_exp_f32 v113, v129\n"
      "v_mfma_f32_16x16x32_bf16 v[84:87], a[116:119], v[196:199], v[84:87]\n"
      "v_cvt_pk_bf16_f32 v192, v112, v113\n"
      "v_xor_b32 v241, 0x10000, v245\n"
      "v_mfma_f32_16x16x32_bf16 v[68:71], a[112:115], v[196:199], v[68:71]\n"
      "v_exp_f32 v114, v130\n"
      "v_mfma_f32_16x16x32_bf16 a[192:195], a[200:203], v[212:215], a[192:195]\n"
      "v_xor_b32 v250, 0x10000, v246\n"
      "v_mfma_f32_16x16x32_bf16 a[148:151], a[108:111], v[196:199], a[148:151]\n"
      "v_exp_f32 v115, v131\n"
      "v_mfma_f32_16x16x32_bf16 a[132:135], a[104:107], v[196:199], a[132:135]\n"
      "s_memrealtime s[68:69]\n"
      "v_cvt_pk_bf16_f32 v193, v114, v115\n"
      "v_xor_b32 v251, 0x10000, v247\n"
      "v_mfma_f32_16x16x32_bf16 a[192:195], a[204:207], v[216:219], a[192:195]\n"
      "v_exp_f32 v112, v132\n"
      "v_mfma_f32_16x16x32_bf16 v[52:55], a[100:103], v[196:199], v[52:55]\n"
      "v_or_b32 v116, v192, v193\n"
      "v_mfma_f32_16x16x32_bf16 v[36:39], a[96:99], v[196:199], v[36:39]\n"
      "v_exp_f32 v113, v133\n"
      "v_mfma_f32_16x16x32_bf16 a[192:195], a[208:211], v[220:223], a[192:195]\n"
      "v_cvt_pk_bf16_f32 v194, v112, v113\n"
      "s_waitcnt vmcnt(6) lgkmcnt(0)\n"
      "s_sub_u32 s67, s68, s90\n"
      "s_cmp_lt_i32 s67, 0\n"
      "s_cbranch_scc1 GW7_%=\n"
      "GX7_%=:\n"
      "s_barrier\n"
      "v_mfma_f32_16x16x32_bf16 v[40:43], a[96:99], v[200:203], v[40:43]\n"
      "s_memrealtime s[68:69]\n"
      "ds_read_b128 a[64:67], v242 offset:32768\n"
      "v_exp_f32 v114, v134\n"
      "v_mfma_f32_16x16x32_bf16 v[44:47], a[96:99], v[204:207], v[44:47]\n"
      "s_add_u32 m0, %[wb], 82944\n"
      "ds_read_b128 a[68:71], v242 offset:36864\n"
      "v_mfma_f32_16x16x32_bf16 v[60:63], a[100:103], v[204:207], v[60:63]\n"
      "ds_read_b128 a[72:75], v243 offset:32768\n"
      "buffer_load_dwordx4 v121, s[24:27], s94 offen nt lds\n"
      "v_mfma_f32_16x16x32_bf16 v[56:59], a[100:103], v[200:203], v[56:59]\n"
      "s_add_u32 m0, %[wb], 91136\n"
      "ds_read_b128 a[76:79], v243 offset:36864\n"
      "v_mfma_f32_16x16x32_bf16 a[136:139], a[104:107], v[200:203], a[136:139]\n"
      "ds_read_b128 a[80:83], v242 offset:33792\n"
      "buffer_load_dwordx4 v123, s[24:27], s94 offen nt lds\n"
      "v_mfma_f32_16x16x32_bf16 a[140:143], a[104:107], v[204:207], a[140:143]\n"
      "ds_read_b128 a[84:87], v242 offset:37888\n"
      "v_mfma_f32_16x16x32_bf16 a[156:159], a[108:111], v[204:207], a[156:159]\n"
      "ds_read_b128 a[88:91], v243 offset:33792\n"
      "v_mfma_f32_16x16x32_bf16 a[152:155], a[108:111], v[200:203], a[152:155]\n"
      "ds_read_b128 a[92:95], v243 offset:37888\n"
      "v_mfma_f32_16x16x32_bf16 v[72:75], a[112:115], v[200:203], v[72:75]\n"
      "v_exp_f32 v115, v135\n"
      "v_mfma_f32_16x16x32_bf16 v[76:79], a[112:115], v[204:207], v[76:79]\n"
      "v_exp_f32 v112, v136\n"
      "v_mfma_f32_16x16x32_bf16 v[92:95], a[116:119], v[204:207], v[92:95]\n"
      "v_cvt_pk_bf16_f32 v195, v114, v115\n"
      "v_exp_f32 v113, v137\n"
      "v_mfma_f32_16x16x32_bf16 v[88:91], a[116:119], v[200:203], v[88:91]\n"
      "v_or3_b32 v116, v116, v194, v195\n"
      "v_mfma_f32_16x16x32_bf16 a[168:171], a[120:123], v[200:203], a[168:171]\n"
      "v_exp_f32 v114, v138\n"
      "v_mfma_f32_16x16x32_bf16 a[172:175], a[120:123], v[204:207], a[172:175]\n"
      "v_cvt_pk_bf16_f32 v196, v112, v113\n"
      "v_exp_f32 v115, v139\n"
      "v_mfma_f32_16x16x32_bf16 a[188:191], a[124:127], v[204:207], a[188:191]\n"
      "v_exp_f32 v112, v140\n"
      "v_mfma_f32_16x16x32_bf16 a[184:187], a[124:127], v[200:203], a[184:187]\n"
      "s_waitcnt lgkmcnt(0)\n"
      "v_cvt_pk_bf16_f32 v197, v114, v115\n"
      "v_mfma_f32_16x16x32_bf16 v[160:163], a[64:67], a[0:3], v[224:227]\n"
      "s_sub_u32 s67, s68, s66\n"
      "v_exp_f32 v113, v141\n"
      "v_mfma_f32_16x16x32_bf16 v[168:171], a[64:67], a[4:7], v[228:231]\n"
      "s_cmp_gt_i32 s67, s70\n"
      "v_or3_b32 v116, v116, v196, v197\n"
      "v_exp_f32 v114, v142\n"
      "v_mfma_f32_16x16x32_bf16 v[176:179], a[64:67], a[8:11], v[232:235]\n"
      "s_cselect_b32 s69, s86, 0\n"
      "v_cvt_pk_bf16_f32 v198, v112, v113\n"
      "v_mfma_f32_16x16x32_bf16 v[184:187], a[64:67], a[12:15], v[236:239]\n"
      "s_add_u32 s66, s66, s69\n"
      "v_exp_f32 v115, v143\n"
      "v_mfma_f32_16x16x32_bf16 v[164:167], a[68:71], a[0:3], v[224:227]\n"
      "s_add_u32 s67, s67, s101\n"
      "v_exp_f32 v112, v144\n"
      "v_mfma_f32_16x16x32_bf16 v[172:175], a[68:71], a[4:7], v[228:231]\n"
      "s_min_i32 s70, s70, s67\n"
      "v_cvt_pk_bf16_f32 v199, v114, v115\n"
      "v_exp_f32 v113, v145\n"
      "v_mfma_f32_16x16x32_bf16 v[180:183], a[68:71], a[8:11], v[232:235]\n"
      "s_max_i32 s70, s70, 85\n"
      "v_or3_b32 v116, v116, v198, v199\n"
      "v_mfma_f32_16x16x32_bf16 v[188:191], a[68:71], a[12:15], v[236:239]\n"
      "s_waitcnt lgkmcnt(4)\n"
      "v_exp_f32 v114, v146\n"
      "v_mfma_f32_16x16x32_bf16 v[160:163], a[72:75], a[16:19], v[160:163]\n"
      "s_add_u32 s66, s66, s86\n"
      "v_cvt_pk_bf16_f32 v200, v112, v113\n"
      "v_exp_f32 v115, v147\n"
      "v_mfma_f32_16x16x32_bf16 v[168:171], a[72:75], a[20:23], v[168:171]\n"
      "s_and_b32 s76, s80, 31\n"
      "v_exp_f32 v112, v148\n"
      "v_mfma_f32_16x16x32_bf16 v[176:179], a[72:75], a[24:27], v[176:179]\n"
      "s_lshr_b64 s[80:81], s[80:81], 5\n"
      "v_cvt_pk_bf16_f32 v201, v114, v115\n"
      "v_mfma_f32_16x16x32_bf16 v[184:187], a[72:75], a[28:31], v[184:187]\n"
      "s_add_u32 s66, s66, s76\n"
      "v_exp_f32 v113, v149\n"
      "v_mfma_f32_16x16x32_bf16 v[164:167], a[76:79], a[16:19], v[164:167]\n"
      "s_sub_u32 s67, s68, s82\n"
      "v_or3_b32 v116, v116, v200, v201\n"
      "v_exp_f32 v114, v150\n"
      "v_mfma_f32_16x16x32_bf16 v[172:175], a[76:79], a[20:23], v[172:175]\n"
      "s_min_i32 s76, s67, s83\n"
      "v_cvt_pk_bf16_f32 v202, v112, v113\n"
      "v_mfma_f32_16x16x32_bf16 v[180:183], a[76:79], a[24:27], v[180:183]\n"
      "s_add_u32 m0, s62, 0\n"
      "v_exp_f32 v115, v151\n"
      "v_mfma_f32_16x16x32_bf16 v[188:191], a[76:79], a[28:31], v[188:191]\n"
      "buffer_load_dwordx4 v248, s[20:23], s60 offen nt lds\n"
      "s_waitcnt lgkmcnt(2)\n"
      "v_mfma_f32_16x16x32_bf16 v[160:163], a[80:83], a[32:35], v[160:163]\n"
      "s_add_u32 m0, s62, 896\n"
      "v_exp_f32 v112, v152\n"
      "v_mfma_f32_16x16x32_bf16 v[168:171], a[80:83], a[36:39], v[168:171]\n"
      "s_mov_b32 s83, s67\n"
      "buffer_load_dwordx4 v248, s[20:23], s60 offen offset:128 nt lds\n"
      "v_mfma_f32_16x16x32_bf16 v[176:179], a[80:83], a[40:43], v[176:179]\n"
      "s_add_u32 m0, s62, 8192\n"
      "v_mfma_f32_16x16x32_bf16 v[184:187], a[80:83], a[44:47], v[184:187]\n"
      "s_sub_u32 s76, s76, 36\n"
      "buffer_load_dwordx4 v120, s[20:23], s60 offen nt lds\n"
      "s_waitcnt vmcnt(9)\n"
      "s_barrier\n"
      "v_mfma_f32_16x16x32_bf16 v[164:167], a[84:87], a[32:35], v[164:167]\n"
      "s_add_u32 m0, s62, 9088\n"
      "ds_read_b64_tr_b16 a[96:97], v246 offset:33792\n"
      "v_mfma_f32_16x16x32_bf16 v[172:175], a[84:87], a[36:39], v[172:175]\n"
      "s_min_i32 s76, s76, 148\n"
      "ds_read_b64_tr_b16 a[98:99], v246 offset:37888\n"
      "buffer_load_dwordx4 v120, s[20:23], s60 offen offset:128 nt lds\n"
      "v_mfma_f32_16x16x32_bf16 v[180:183], a[84:87], a[40:43], v[180:183]\n"
      "s_max_i32 s76, s76, 70\n"
      "ds_read_b64_tr_b16 a[100:101], v247 offset:33792\n"
      "v_mfma_f32_16x16x32_bf16 v[188:191], a[84:87], a[44:47], v[188:191]\n"
      "ds_read_b64_tr_b16 a[102:103], v247 offset:37888\n"
      "s_waitcnt lgkmcnt(4)\n"
      "v_mfma_f32_16x16x32_bf16 v[160:163], a[88:91], a[48:51], v[160:163]\n"
      "s_mov_b32 s82, s68\n"
      "ds_read_b64_tr_b16 a[104:105], v244 offset:33792\n"
      "v_cvt_pk_bf16_f32 v203, v114, v115\n"
      "v_mfma_f32_16x16x32_bf16 v[168:171], a[88:91], a[52:55], v[168:171]\n"
      "s_sub_u32 s66, s66, s99\n"
      "ds_read_b64_tr_b16 a[106:107], v244 offset:37888\n"
      "v_exp_f32 v113, v153\n"
      "v_mfma_f32_16x16x32_bf16 v[176:179], a[88:91], a[56:59], v[176:179]\n"
      "s_sub_u32 s82, s82, s99\n"
      "ds_read_b64_tr_b16 a[108:109], v245 offset:33792\n"
      "v_mfma_f32_16x16x32_bf16 v[184:187], a[88:91], a[60:63], v[184:187]\n"
      "s_add_u32 s65, s82, s76\n"
      "ds_read_b64_tr_b16 a[110:111], v245 offset:37888\n"
      "v_or3_b32 v116, v116, v202, v203\n"
      "v_mfma_f32_16x16x32_bf16 v[164:167], a[92:95], a[48:51], v[164:167]\n"
      "s_sub_u32 s93, s93, 1\n"
      "v_exp_f32 v114, v154\n"
      "v_mfma_f32_16x16x32_bf16 v[172:175], a[92:95], a[52:55], v[172:175]\n"
      "s_cmp_le_i32 s93, 14\n"
      "v_cvt_pk_bf16_f32 v204, v112, v113\n"
      "v_exp_f32 v115, v155\n"
      "v_mfma_f32_16x16x32_bf16 v[180:183], a[92:95], a[56:59], v[180:183]\n"
      "s_cselect_b32 s86, 107, s86\n"
      "v_exp_f32 v112, v156\n"
      "v_mfma_f32_16x16x32_bf16 v[188:191], a[92:95], a[60:63], v[188:191]\n"
      "s_waitcnt lgkmcnt(4)\n"
      "v_cvt_pk_bf16_f32 v205, v114, v115\n"
      "v_mfma_f32_16x16x32_bf16 a[160:163], a[96:99], v[208:211], a[160:163]\n"
      "s_sub_u32 s67, s65, s66\n"
      "v_exp_f32 v113, v157\n"
      "v_mfma_f32_16x16x32_bf16 a[164:167], a[96:99], v[212:215], a[164:167]\n"
      "s_cmp_lt_i32 s67, 0\n"
      "v_or3_b32 v116, v116, v204, v205\n"
      "v_exp_f32 v114, v158\n"
      "v_mfma_f32_16x16x32_bf16 a[168:171], a[96:99], v[216:219], a[168:171]\n"
      "s_cselect_b32 s88, s66, s65\n"
      "v_cvt_pk_bf16_f32 v206, v112, v113\n"
      "v_mfma_f32_16x16x32_bf16 a[172:175], a[96:99], v[220:223], a[172:175]\n"
      "s_add_u32 s90, s88, s91\n"
      "v_exp_f32 v115, v159\n"
      "s_nop 0\n"
      "v_mfma_f32_16x16x32_bf16 a[176:179], a[100:103], v[208:211], a[176:179]\n"
      "s_mov_b32 s64, 4\n"
      "v_cvt_pk_bf16_f32 v207, v114, v115\n"
      "v_or3_b32 v116, v116, v206, v207\n"
      "v_mfma_f32_16x16x32_bf16 a[180:183], a[100:103], v[212:215], a[180:183]\n"
      "v_and_b32 v116, 0x40004000, v116\n"
      "v_cmp_ne_u32 vcc, 0, v116\n"
      "s_cbranch_vccnz FB0_%=\n"
      "RET0_4_%=:\n"
      "v_mfma_f32_16x16x32_bf16 a[184:187], a[100:103], v[216:219], a[184:187]\n"
      "v_exp_f32 v112, v160\n"
      "v_mfma_f32_16x16x32_bf16 a[188:191], a[100:103], v[220:223], a[188:191]\n"
      "s_memrealtime s[68:69]\n"
      "v_xor_b32 v242, 0x10000, v242\n"
      "v_mfma_f32_16x16x32_bf16 a[192:195], a[196:199], v[192:195], a[192:195]\n"
      "v_exp_f32 v113, v161\n"
      "v_mfma_f32_16x16x32_bf16 a[192:195], a[200:203], v[196:199], a[192:195]\n"
      "v_xor_b32 v243, 0x10000, v243\n"
      "v_exp_f32 v114, v162\n"
      "v_mfma_f32_16x16x32_bf16 a[192:195], a[204:207], v[200:203], a[192:195]\n"
      "v_cvt_pk_bf16_f32 v0, v112, v113\n"
      "v_mfma_f32_16x16x32_bf16 a[192:195], a[208:211], v[204:207], a[192:195]\n"
      "v_exp_f32 v115, v163\n"
      "s_waitcnt vmcnt(6) lgkmcnt(0)\n"
      "s_sub_u32 s67, s68, s90\n"
      "s_cmp_lt_i32 s67, 0\n"
      "s_cbranch_scc1 GW8_%=\n"
      "GX8_%=:\n"
      "s_barrier\n"
      "v_mfma_f32_16x16x32_bf16 v[64:67], a[104:107], v[208:211], v[64:67]\n"
      "ds_read_b128 a[64:67], v242 offset:0\n"
      "ds_read_b128 a[68:71], v242 offset:4096\n"
      "v_mfma_f32_16x16x32_bf16 v[68:71], a[104:107], v[212:215], v[68:71]\n"
      "ds_read_b128 a[72:75], v243 offset:0\n"
      "ds_read_b128 a[76:79], v243 offset:4096\n"
      "v_mfma_f32_16x16x32_bf16 v[72:75], a[104:107], v[216:219], v[72:75]\n"
      "ds_read_b128 a[80:83], v242 offset:1024\n"
      "ds_read_b128 a[84:87], v242 offset:5120\n"
      "v_exp_f32 v112, v164\n"
      "v_mfma_f32_16x16x32_bf16 v[76:79], a[104:107], v[220:223], v[76:79]\n"
      "ds_read_b128 a[88:91], v243 offset:1024\n"
      "ds_read_b128 a[92:95], v243 offset:5120\n"
      "v_mfma_f32_16x16x32_bf16 v[80:83], a[108:111], v[208:211], v[80:83]\n"
      "v_cvt_pk_bf16_f32 v1, v114, v115\n"
      "v_mfma_f32_16x16x32_bf16 v[84:87], a[108:111], v[212:215], v[84:87]\n"
      "v_exp_f32 v113, v165\n"
      "v_or_b32 v116, v0, v1\n"
      "v_mfma_f32_16x16x32_bf16 v[88:91], a[108:111], v[216:219], v[88:91]\n"
      "v_exp_f32 v114, v166\n"
      "v_cvt_pk_bf16_f32 v2, v112, v113\n"
      "v_mfma_f32_16x16x32_bf16 v[92:95], a[108:111], v[220:223], v[92:95]\n"
      "s_waitcnt lgkmcnt(6)\n"
      "v_exp_f32 v115, v167\n"
      "v_mfma_f32_16x16x32_bf16 v[128:131], a[64:67], a[0:3], v[224:227]\n"
      "v_exp_f32 v112, v168\n"
      "v_mfma_f32_16x16x32_bf16 v[136:139], a[64:67], a[4:7], v[228:231]\n"
      "s_add_u32 m0, %[wb], 115712\n"
      "v_cvt_pk_bf16_f32 v3, v114, v115\n"
      "v_exp_f32 v113, v169\n"
      "v_mfma_f32_16x16x32_bf16 v[144:147], a[64:67], a[8:11], v[232:235]\n"
      "buffer_load_dwordx4 v121, s[24:27], s95 offen nt lds\n"
      "v_mfma_f32_16x16x32_bf16 v[152:155], a[64:67], a[12:15], v[236:239]\n"
      "s_add_u32 m0, %[wb], 123904\n"
      "v_or3_b32 v116, v116, v2, v3\n"
      "v_mfma_f32_16x16x32_bf16 v[132:135], a[68:71], a[0:3], v[224:227]\n"
      "buffer_load_dwordx4 v123, s[24:27], s95 offen nt lds\n"
      "v_mfma_f32_16x16x32_bf16 v[140:143], a[68:71], a[4:7], v[228:231]\n"
      "s_add_u32 m0, %[wb], 17408\n"
      "v_exp_f32 v114, v170\n"
      "v_mfma_f32_16x16x32_bf16 v[148:151], a[68:71], a[8:11], v[232:235]\n"
      "buffer_load_dwordx4 v121, s[24:27], s77 offen nt lds\n"
      "v_mfma_f32_16x16x32_bf16 v[156:159], a[68:71], a[12:15], v[236:239]\n"
      "s_waitcnt lgkmcnt(4)\n"
      "s_add_u32 m0, %[wb], 25600\n"
      "v_cvt_pk_bf16_f32 v4, v112, v113\n"
      "v_mfma_f32_16x16x32_bf16 v[128:131], a[72:75], a[16:19], v[128:131]\n"
      "buffer_load_dwordx4 v123, s[24:27], s77 offen nt lds\n"
      "v_mfma_f32_16x16x32_bf16 v[136:139], a[72:75], a[20:23], v[136:139]\n"
      "v_exp_f32 v115, v171\n"
      "v_mfma_f32_16x16x32_bf16 v[144:147], a[72:75], a[24:27], v[144:147]\n"
      "v_exp_f32 v112, v172\n"
      "v_cvt_pk_bf16_f32 v5, v114, v115\n"
      "v_mfma_f32_16x16x32_bf16 v[152:155], a[72:75], a[28:31], v[152:155]\n"
      "v_exp_f32 v113, v173\n"
      "v_mfma_f32_16x16x32_bf16 v[132:135], a[76:79], a[16:19], v[132:135]\n"
      "v_or3_b32 v116, v116, v4, v5\n"
      "v_exp_f32 v114, v174\n"
      "v_mfma_f32_16x16x32_bf16 v[140:143], a[76:79], a[20:23], v[140:143]\n"
      "v_cvt_pk_bf16_f32 v6, v112, v113\n"
      "v_exp_f32 v115, v175\n"
      "v_mfma_f32_16x16x32_bf16 v[148:151], a[76:79], a[24:27], v[148:151]\n"
      "v_exp_f32 v112, v176\n"
      "v_mfma_f32_16x16x32_bf16 v[156:159], a[76:79], a[28:31], v[156:159]\n"
      "s_waitcnt lgkmcnt(2)\n"
      "v_cvt_pk_bf16_f32 v7, v114, v115\n"
      "v_mfma_f32_16x16x32_bf16 v[128:131], a[80:83], a[32:35], v[128:131]\n"
      "v_exp_f32 v113, v177\n"
      "v_or3_b32 v116, v116, v6, v7\n"
      "v_mfma_f32_16x16x32_bf16 v[136:139], a[80:83], a[36:39], v[136:139]\n"
      "v_exp_f32 v114, v178\n"
      "v_cvt_pk_bf16_f32 v8, v112, v113\n"
      "v_mfma_f32_16x16x32_bf16 v[144:147], a[80:83], a[40:43], v[144:147]\n"
      "v_exp_f32 v115, v179\n"
      "v_mfma_f32_16x16x32_bf16 v[152:155], a[80:83], a[44:47], v[152:155]\n"
      "v_exp_f32 v112, v180\n"
      "s_waitcnt vmcnt(8)\n"
      "s_barrier\n"
      "v_mfma_f32_16x16x32_bf16 v[132:135], a[84:87], a[32:35], v[132:135]\n"
      "ds_read_b64_tr_b16 a[112:113], v250 offset:1024\n"
      "v_cvt_pk_bf16_f32 v9, v114, v115\n"
      "v_mfma_f32_16x16x32_bf16 v[140:143], a[84:87], a[36:39], v[140:143]\n"
      "ds_read_b64_tr_b16 a[114:115], v250 offset:5120\n"
      "v_exp_f32 v113, v181\n"
      "v_mfma_f32_16x16x32_bf16 v[148:151], a[84:87], a[40:43], v[148:151]\n"
      "ds_read_b64_tr_b16 a[116:117], v251 offset:1024\n"
      "v_or3_b32 v116, v116, v8, v9\n"
      "v_mfma_f32_16x16x32_bf16 v[156:159], a[84:87], a[44:47], v[156:159]\n"
      "ds_read_b64_tr_b16 a[118:119], v251 offset:5120\n"
      "v_exp_f32 v114, v182\n"
      "s_waitcnt lgkmcnt(4)\n"
      "v_mfma_f32_16x16x32_bf16 v[128:131], a[88:91], a[48:51], v[128:131]\n"
      "ds_read_b64_tr_b16 a[120:121], v240 offset:1024\n"
      "v_cvt_pk_bf16_f32 v10, v112, v113\n"
      "v_mfma_f32_16x16x32_bf16 v[136:139], a[88:91], a[52:55], v[136:139]\n"
      "ds_read_b64_tr_b16 a[122:123], v240 offset:5120\n"
      "v_exp_f32 v115, v183\n"
      "v_mfma_f32_16x16x32_bf16 v[144:147], a[88:91], a[56:59], v[144:147]\n"
      "ds_read_b64_tr_b16 a[124:125], v241 offset:1024\n"
      "v_mfma_f32_16x16x32_bf16 v[152:155], a[88:91], a[60:63], v[152:155]\n"
      "ds_read_b64_tr_b16 a[126:127], v241 offset:5120\n"
      "v_exp_f32 v112, v184\n"
      "v_mfma_f32_16x16x32_bf16 v[132:135], a[92:95], a[48:51], v[132:135]\n"
      "v_cvt_pk_bf16_f32 v11, v114, v115\n"
      "v_exp_f32 v113, v185\n"
      "v_mfma_f32_16x16x32_bf16 v[140:143], a[92:95], a[52:55], v[140:143]\n"
      "v_or3_b32 v116, v116, v10, v11\n"
      "v_exp_f32 v114, v186\n"
      "v_mfma_f32_16x16x32_bf16 v[148:151], a[92:95], a[56:59], v[148:151]\n"
      "v_cvt_pk_bf16_f32 v12, v112, v113\n"
      "v_mfma_f32_16x16x32_bf16 v[156:159], a[92:95], a[60:63], v[156:159]\n"
      "s_waitcnt lgkmcnt(4)\n"
      "v_exp_f32 v115, v187\n"
      "v_mfma_f32_16x16x32_bf16 a[160:163], a[112:115], v[192:195], a[160:163]\n"
      "v_exp_f32 v112, v188\n"
      "v_cvt_pk_bf16_f32 v13, v114, v115\n"
      "v_mfma_f32_16x16x32_bf16 a[164:167], a[112:115], v[196:199], a[164:167]\n"
      "v_exp_f32 v113, v189\n"
      "v_or3_b32 v116, v116, v12, v13\n"
      "v_mfma_f32_16x16x32_bf16 a[168:171], a[112:115], v[200:203], a[168:171]\n"
      "v_exp_f32 v114, v190\n"
      "v_mfma_f32_16x16x32_bf16 a[172:175], a[112:115], v[204:207], a[172:175]\n"
      "v_cvt_pk_bf16_f32 v14, v112, v113\n"
      "v_exp_f32 v115, v191\n"
      "v_mfma_f32_16x16x32_bf16 a[176:179], a[116:119], v[192:195], a[176:179]\n"
      "s_mov_b32 s64, 5\n"
      "s_nop 0\n"
      "v_cvt_pk_bf16_f32 v15, v114, v115\n"
      "v_or3_b32 v116, v116, v14, v15\n"
      "v_mfma_f32_16x16x32_bf16 a[180:183], a[116:119], v[196:199], a[180:183]\n"
      "v_and_b32 v116, 0x40004000, v116\n"
      "v_cmp_ne_u32 vcc, 0, v116\n"
      "s_cbranch_vccnz FB1_%=\n"
      "RET1_5_%=:\n"
      "v_mfma_f32_16x16x32_bf16 a[184:187], a[116:119], v[200:203], a[184:187]\n"
      "v_exp_f32 v112, v128\n"
      "v_exp_f32 v113, v129\n"
      "v_mfma_f32_16x16x32_bf16 a[188:191], a[116:119], v[204:207], a[188:191]\n"
      "s_memrealtime s[68:69]\n"
      "s_add_u32 m0, %[wb], 50176\n"
      "v_exp_f32 v114, v130\n"
      "v_cvt_pk_bf16_f32 v16, v112, v113\n"
      "v_mfma_f32_16x16x32_bf16 a[192:195], a[196:199], v[0:3], a[192:195]\n"
      "buffer_load_dwordx4 v121, s[24:27], s78 offen nt lds\n"
      "v_mfma_f32_16x16x32_bf16 a[192:195], a[200:203], v[4:7], a[192:195]\n"
      "s_add_u32 m0, %[wb], 58368\n"
      "v_exp_f32 v115, v131\n"
      "v_exp_f32 v112, v132\n"
      "v_mfma_f32_16x16x32_bf16 a[192:195], a[204:207], v[8:11], a[192:195]\n"
      "buffer_load_dwordx4 v123, s[24:27], s78 offen nt lds\n"
      "v_cvt_pk_bf16_f32 v17, v114, v115\n"
      "v_mfma_f32_16x16x32_bf16 a[192:195], a[208:211], v[12:15], a[192:195]\n"
      "v_exp_f32 v113, v133\n"
      "v_or_b32 v116, v16, v17\n"
      "s_add_u32 s72, s72, 0x7f4a7c15\n"
      "s_addc_u32 s76, s79, 0x9e3779b9\n"
      "s_waitcnt vmcnt(6) lgkmcnt(0)\n"
      "s_barrier\n"
      "v_mfma_f32_16x16x32_bf16 v[64:67], a[120:123], v[192:195], v[64:67]\n"
      "s_xor_b32 s80, s35, 0x400000\n"
      "ds_read_b128 a[64:67], v242 offset:32768\n"
      "ds_read_b128 a[68:71], v242 offset:36864\n"
      "v_mfma_f32_16x16x32_bf16 v[68:71], a[120:123], v[196:199], v[68:71]\n"
      "s_add_u32 s79, s80, 0x1000\n"
      "ds_read_b128 a[72:75], v243 offset:32768\n"
      "ds_read_b128 a[76:79], v243 offset:36864\n"
      "v_exp_f32 v114, v134\n"
      "v_mfma_f32_16x16x32_bf16 v[72:75], a[120:123], v[200:203], v[72:75]\n"
      "s_add_u32 s81, s35, 0x1000\n"
      "ds_read_b128 a[80:83], v242 offset:33792\n"
      "ds_read_b128 a[84:87], v242 offset:37888\n"
      "v_cvt_pk_bf16_f32 v18, v112, v113\n"
      "v_mfma_f32_16x16x32_bf16 v[76:79], a[120:123], v[204:207], v[76:79]\n"
      "s_mov_b32 s63, 0x100000\n"
      "ds_read_b128 a[88:91], v243 offset:33792\n"
      "ds_read_b128 a[92:95], v243 offset:37888\n"
      "v_mfma_f32_16x16x32_bf16 v[80:83], a[124:127], v[192:195], v[80:83]\n"
      "s_mov_b32 s60, 0x928b2f\n"
      "v_exp_f32 v115, v135\n"
      "v_exp_f32 v112, v136\n"
      "v_mfma_f32_16x16x32_bf16 v[84:87], a[124:127], v[196:199], v[84:87]\n"
      "s_mov_b32 s62, 0x9818e9\n"
      "v_cvt_pk_bf16_f32 v19, v114, v115\n"
      "v_exp_f32 v113, v137\n"
      "v_mfma_f32_16x16x32_bf16 v[88:91], a[124:127], v[200:203], v[88:91]\n"
      "s_mov_b32 s70, 0xdd9dc9\n"
      "v_xor_b32 v248, 0x2000, v244\n"
      "v_or3_b32 v116, v116, v18, v19\n"
      "v_mfma_f32_16x16x32_bf16 v[92:95], a[124:127], v[204:207], v[92:95]\n"
      "s_waitcnt lgkmcnt(6)\n"
      "v_exp_f32 v114, v138\n"
      "v_cvt_pk_bf16_f32 v20, v112, v113\n"
      "v_mfma_f32_16x16x32_bf16 v[160:163], a[64:67], a[0:3], v[224:227]\n"
      "s_mov_b32 s71, 0x8ed905\n"
      "v_exp_f32 v115, v139\n"
      "v_mfma_f32_16x16x32_bf16 v[168:171], a[64:67], a[4:7], v[228:231]\n"
      "s_sub_u32 s88, 331, s38\n"
      "v_xor_b32 v120, 0x2000, v245\n"
      "v_exp_f32 v112, v140\n"
      "v_cvt_pk_bf16_f32 v21, v114, v115\n"
      "v_mfma_f32_16x16x32_bf16 v[176:179], a[64:67], a[8:11], v[232:235]\n"
      "s_add_u32 s88, s88, s66\n"
      "v_exp_f32 v113, v141\n"
      "v_mfma_f32_16x16x32_bf16 v[184:187], a[64:67], a[12:15], v[236:239]\n"
      "s_add_u32 s88, s88, s100\n"
      "v_or3_b32 v116, v116, v20, v21\n"
      "v_exp_f32 v114, v142\n"
      "v_mfma_f32_16x16x32_bf16 v[164:167], a[68:71], a[0:3], v[224:227]\n"
      "v_mbcnt_lo_u32_b32 v127, -1, 0\n"
      "v_cvt_pk_bf16_f32 v22, v112, v113\n"
      "v_exp_f32 v115, v143\n"
      "v_mfma_f32_16x16x32_bf16 v[172:175], a[68:71], a[4:7], v[228:231]\n"
      "v_exp_f32 v112, v144\n"
      "v_mfma_f32_16x16x32_bf16 v[180:183], a[68:71], a[8:11], v[232:235]\n"
      "v_cvt_pk_bf16_f32 v23, v114, v115\n"
      "v_exp_f32 v113, v145\n"
      "v_mfma_f32_16x16x32_bf16 v[188:191], a[68:71], a[12:15], v[236:239]\n"
      "s_waitcnt lgkmcnt(4)\n"
      "v_mbcnt_hi_u32_b32 v127, -1, v127\n"
      "v_or3_b32 v116, v116, v22, v23\n"
      "v_mfma_f32_16x16x32_bf16 v[160:163], a[72:75], a[16:19], v[160:163]\n"
      "v_exp_f32 v114, v146\n"
      "v_cvt_pk_bf16_f32 v24, v112, v113\n"
      "v_mfma_f32_16x16x32_bf16 v[168:171], a[72:75], a[20:23], v[168:171]\n"
      "s_add_u32 m0, %[wb], 49152\n"
      "v_exp_f32 v115, v147\n"
      "v_exp_f32 v112, v148\n"
      "v_mfma_f32_16x16x32_bf16 v[176:179], a[72:75], a[24:27], v[176:179]\n"
      "buffer_load_dwordx4 v249, s[24:27], s61 offen nt lds\n"
      "v_lshlrev_b32 v127, 4, v127\n"
      "v_mfma_f32_16x16x32_bf16 v[184:187], a[72:75], a[28:31], v[184:187]\n"
      "s_add_u32 m0, %[wb], 57344\n"
      "v_cvt_pk_bf16_f32 v25, v114, v115\n"
      "v_mfma_f32_16x16x32_bf16 v[164:167], a[76:79], a[16:19], v[164:167]\n"
      "buffer_load_dwordx4 v122, s[24:27], s61 offen nt lds\n"
      "v_mfma_f32_16x16x32_bf16 v[172:175], a[76:79], a[20:23], v[172:175]\n"
      "v_add_u32 v125, s33, v127\n"
      "v_exp_f32 v113, v149\n"
      "v_mfma_f32_16x16x32_bf16 v[180:183], a[76:79], a[24:27], v[180:183]\n"
      "v_or3_b32 v116, v116, v24, v25\n"
      "v_exp_f32 v114, v150\n"
      "v_mfma_f32_16x16x32_bf16 v[188:191], a[76:79], a[28:31], v[188:191]\n"
      "s_waitcnt lgkmcnt(2)\n"
      "v_cvt_pk_bf16_f32 v26, v112, v113\n"
      "v_exp_f32 v115, v151\n"
      "v_mfma_f32_16x16x32_bf16 v[160:163], a[80:83], a[32:35], v[160:163]\n"
      "v_add_u32 v126, s34, v127\n"
      "v_exp_f32 v112, v152\n"
      "v_mfma_f32_16x16x32_bf16 v[168:171], a[80:83], a[36:39], v[168:171]\n"
      "v_cvt_pk_bf16_f32 v27, v114, v115\n"
      "v_exp_f32 v113, v153\n"
      "v_mfma_f32_16x16x32_bf16 v[176:179], a[80:83], a[40:43], v[176:179]\n"
      "v_or3_b32 v116, v116, v26, v27\n"
      "v_mfma_f32_16x16x32_bf16 v[184:187], a[80:83], a[44:47], v[184:187]\n"
      "v_exp_f32 v114, v154\n"
      "v_cvt_pk_bf16_f32 v28, v112, v113\n"
      "v_mfma_f32_16x16x32_bf16 v[164:167], a[84:87], a[32:35], v[164:167]\n"
      "v_exp_f32 v115, v155\n"
      "v_exp_f32 v112, v156\n"
      "v_mfma_f32_16x16x32_bf16 v[172:175], a[84:87], a[36:39], v[172:175]\n"
      "v_cvt_pk_bf16_f32 v29, v114, v115\n"
      "v_exp_f32 v113, v157\n"
      "v_mfma_f32_16x16x32_bf16 v[180:183], a[84:87], a[40:43], v[180:183]\n"
      "v_or3_b32 v116, v116, v28, v29\n"
      "v_exp_f32 v114, v158\n"
      "v_mov_b32 v123, s88\n"
      "v_mfma_f32_16x16x32_bf16 v[188:191], a[84:87], a[44:47], v[188:191]\n"
      "s_waitcnt lgkmcnt(0)\n"
      "v_cvt_pk_bf16_f32 v30, v112, v113\n"
      "v_exp_f32 v115, v159\n"
      "v_add_u32 v124, 12288, v125\n"
      "s_waitcnt vmcnt(4)\n"
      "s_barrier\n"
      "v_mfma_f32_16x16x32_bf16 v[160:163], a[88:91], a[48:51], v[160:163]\n"
      "ds_read_b64_tr_b16 a[96:97], v250 offset:33792\n"
      "ds_read_b64_tr_b16 a[98:99], v250 offset:37888\n"
      "s_nop 0\n"
      "v_mfma_f32_16x16x32_bf16 v[168:171], a[88:91], a[52:55], v[168:171]\n"
      "s_add_u32 m0, %[wb], 81920\n"
      "ds_read_b64_tr_b16 a[100:101], v251 offset:33792\n"
      "ds_read_b64_tr_b16 a[102:103], v251 offset:37888\n"
      "v_cvt_pk_bf16_f32 v31, v114, v115\n"
      "v_mfma_f32_16x16x32_bf16 v[176:179], a[88:91], a[56:59], v[176:179]\n"
      "buffer_load_dwordx4 v249, s[24:27], s94 offen nt lds\n"
      "ds_read_b64_tr_b16 a[112:113], v246 offset:1024\n"
      "ds_read_b64_tr_b16 a[114:115], v246 offset:5120\n"
      "v_mfma_f32_16x16x32_bf16 v[184:187], a[88:91], a[60:63], v[184:187]\n"
      "s_add_u32 m0, %[wb], 90112\n"
      "ds_read_b64_tr_b16 a[116:117], v247 offset:1024\n"
      "ds_read_b64_tr_b16 a[118:119], v247 offset:5120\n"
      "v_mfma_f32_16x16x32_bf16 v[164:167], a[92:95], a[48:51], v[164:167]\n"
      "buffer_load_dwordx4 v122, s[24:27], s94 offen nt lds\n"
      "ds_read_b64_tr_b16 a[104:105], v240 offset:33792\n"
      "v_mfma_f32_16x16x32_bf16 v[172:175], a[92:95], a[52:55], v[172:175]\n"
      "s_mov_b32 s64, 5\n"
      "ds_read_b64_tr_b16 a[106:107], v240 offset:37888\n"
      "v_mfma_f32_16x16x32_bf16 v[180:183], a[92:95], a[56:59], v[180:183]\n"
      "ds_read_b64_tr_b16 a[108:109], v241 offset:33792\n"
      "v_or3_b32 v116, v116, v30, v31\n"
      "v_mfma_f32_16x16x32_bf16 v[188:191], a[92:95], a[60:63], v[188:191]\n"
      "ds_read_b64_tr_b16 a[110:111], v241 offset:37888\n"
      "v_and_b32 v116, 0x40004000, v116\n"
      "v_cmp_ne_u32 vcc, 0, v116\n"
      "s_waitcnt lgkmcnt(4)\n"
      "s_cbranch_vccnz FB0_%=\n"
      "RET0_5_%=:\n"
      "v_mfma_f32_16x16x32_bf16 a[160:163], a[96:99], v[0:3], a[160:163]\n"
      "ds_read_b64_tr_b16 a[120:121], v244 offset:1024\n"
      "v_mov_b32 v117, v224\n"
      "v_xor_b32 v242, 0x10000, v242\n"
      "v_xor_b32 v243, 0x10000, v243\n"
      "v_mfma_f32_16x16x32_bf16 a[176:179], a[100:103], v[0:3], a[176:179]\n"
      "ds_read_b64_tr_b16 a[122:123], v244 offset:5120\n"
      "v_exp_f32 v112, v160\n"
      "v_mfma_f32_16x16x32_bf16 a[160:163], a[112:115], v[16:19], a[160:163]\n"
      "s_mov_b32 s64, 6\n"
      "ds_read_b64_tr_b16 a[124:125], v245 offset:1024\n"
      "v_exp_f32 v113, v161\n"
      "v_exp_f32 v114, v162\n"
      "v_mfma_f32_16x16x32_bf16 a[176:179], a[116:119], v[16:19], a[176:179]\n"
      "ds_read_b64_tr_b16 a[126:127], v245 offset:5120\n"
      "v_cvt_pk_bf16_f32 v96, v112, v113\n"
      "v_exp_f32 v115, v163\n"
      "v_mfma_f32_16x16x32_bf16 a[164:167], a[96:99], v[4:7], a[164:167]\n"
      "v_mov_b32_dpp v117, v228 quad_perm:[0,1,2,3] row_mask:0x2 bank_mask:0xf\n"
      "v_exp_f32 v112, v164\n"
      "v_mfma_f32_16x16x32_bf16 a[180:183], a[100:103], v[4:7], a[180:183]\n"
      "v_cvt_pk_bf16_f32 v97, v114, v115\n"
      "v_exp_f32 v113, v165\n"
      "v_or_b32 v116, v96, v97\n"
      "v_mfma_f32_16x16x32_bf16 a[164:167], a[112:115], v[20:23], a[164:167]\n"
      "v_exp_f32 v114, v166\n"
      "v_cvt_pk_bf16_f32 v98, v112, v113\n"
      "v_mfma_f32_16x16x32_bf16 a[180:183], a[116:119], v[20:23], a[180:183]\n"
      "ds_write_b128 v125, a[160:163] offset:0\n"
      "v_exp_f32 v115, v167\n"
      "v_mfma_f32_16x16x32_bf16 a[168:171], a[96:99], v[8:11], a[168:171]\n"
      "v_mov_b32_dpp v117, v232 quad_perm:[0,1,2,3] row_mask:0x4 bank_mask:0xf\n"
      "v_exp_f32 v112, v168\n"
      "v_cvt_pk_bf16_f32 v99, v114, v115\n"
      "v_mfma_f32_16x16x32_bf16 a[184:187], a[100:103], v[8:11], a[184:187]\n"
      "s_add_u32 m0, %[wb], 114688\n"
      "ds_write_b128 v125, v[96:99] offset:8192\n"
      "v_exp_f32 v113, v169\n"
      "v_mfma_f32_16x16x32_bf16 a[168:171], a[112:115], v[24:27], a[168:171]\n"
      "buffer_load_dwordx4 v249, s[24:27], s95 offen nt lds\n"
      "v_or3_b32 v116, v116, v98, v99\n"
      "v_mfma_f32_16x16x32_bf16 a[184:187], a[116:119], v[24:27], a[184:187]\n"
      "s_add_u32 m0, %[wb], 122880\n"
      "v_exp_f32 v114, v170\n"
      "v_cvt_pk_bf16_f32 v100, v112, v113\n"
      "v_mfma_f32_16x16x32_bf16 a[172:175], a[96:99], v[12:15], a[172:175]\n"
      "buffer_load_dwordx4 v122, s[24:27], s95 offen nt lds\n"
      "ds_write_b128 v125, a[176:179] offset:4096\n"
      "v_mov_b32_dpp v117, v236 quad_perm:[0,1,2,3] row_mask:0x8 bank_mask:0xf\n"
      "v_mfma_f32_16x16x32_bf16 a[188:191], a[100:103], v[12:15], a[188:191]\n"
      "v_exp_f32 v115, v171\n"
      "v_mfma_f32_16x16x32_bf16 a[172:175], a[112:115], v[28:31], a[172:175]\n"
      "v_exp_f32 v112, v172\n"
      "v_cvt_pk_bf16_f32 v101, v114, v115\n"
      "v_exp_f32 v113, v173\n"
      "v_mfma_f32_16x16x32_bf16 a[188:191], a[116:119], v[28:31], a[188:191]\n"
      "ds_write_b128 v125, a[164:167] offset:1024\n"
      "v_or3_b32 v116, v116, v100, v101\n"
      "v_mfma_f32_16x16x32_bf16 a[192:195], a[196:199], v[16:19], a[192:195]\n"
      "ds_write2_b32 v124, v117, v123 offset0:1 offset1:2\n"
      "v_exp_f32 v114, v174\n"
      "v_cvt_pk_bf16_f32 v102, v112, v113\n"
      "v_mfma_f32_16x16x32_bf16 a[192:195], a[200:203], v[20:23], a[192:195]\n"
      "v_exp_f32 v115, v175\n"
      "v_exp_f32 v112, v176\n"
      "v_mfma_f32_16x16x32_bf16 a[192:195], a[204:207], v[24:27], a[192:195]\n"
      "ds_write_b128 v125, a[180:183] offset:5120\n"
      "v_cvt_pk_bf16_f32 v103, v114, v115\n"
      "v_mfma_f32_16x16x32_bf16 a[192:195], a[208:211], v[28:31], a[192:195]\n"
      "s_waitcnt lgkmcnt(6)\n"
      "v_exp_f32 v113, v177\n"
      "v_or3_b32 v116, v116, v102, v103\n"
      "v_exp_f32 v114, v178\n"
      "v_mfma_f32_16x16x32_bf16 v[64:67], a[104:107], v[0:3], v[64:67]\n"
      "v_cvt_pk_bf16_f32 v104, v112, v113\n"
      "v_exp_f32 v115, v179\n"
      "v_mfma_f32_16x16x32_bf16 v[68:71], a[104:107], v[4:7], v[68:71]\n"
      "ds_write_b128 v125, v[100:103] offset:9216\n"
      "v_exp_f32 v112, v180\n"
      "s_waitcnt vmcnt(6)\n"
      "s_barrier\n"
      "v_mfma_f32_16x16x32_bf16 v[72:75], a[104:107], v[8:11], v[72:75]\n"
      "ds_read_b64_tr_b16 a[212:213], v244 offset:33792\n"
      "v_cvt_pk_bf16_f32 v105, v114, v115\n"
      "v_exp_f32 v113, v181\n"
      "v_mfma_f32_16x16x32_bf16 v[76:79], a[104:107], v[12:15], v[76:79]\n"
      "s_add_u32 m0, %[wb], 16384\n"
      "ds_write_b128 v125, a[168:171] offset:2048\n"
      "ds_read_b64_tr_b16 a[214:215], v244 offset:37888\n"
      "v_or3_b32 v116, v116, v104, v105\n"
      "v_mfma_f32_16x16x32_bf16 a[228:231], a[196:199], v[96:99], a[192:195]\n"
      "buffer_load_dwordx4 v249, s[24:27], s77 offen nt lds\n"
      "ds_read_b64_tr_b16 a[216:217], v245 offset:33792\n"
      "v_mfma_f32_16x16x32_bf16 v[80:83], a[108:111], v[0:3], v[80:83]\n"
      "s_add_u32 m0, %[wb], 24576\n"
      "ds_read_b64_tr_b16 a[218:219], v245 offset:37888\n"
      "v_exp_f32 v114, v182\n"
      "v_mfma_f32_16x16x32_bf16 v[84:87], a[108:111], v[4:7], v[84:87]\n"
      "buffer_load_dwordx4 v122, s[24:27], s77 offen nt lds\n"
      "ds_write_b128 v125, a[184:187] offset:6144\n"
      "ds_read_b64_tr_b16 a[220:221], v248 offset:33792\n"
      "v_mfma_f32_16x16x32_bf16 v[88:91], a[108:111], v[8:11], v[88:91]\n"
      "ds_read_b64_tr_b16 a[222:223], v248 offset:37888\n"
      "v_cvt_pk_bf16_f32 v106, v112, v113\n"
      "v_mfma_f32_16x16x32_bf16 v[92:95], a[108:111], v[12:15], v[92:95]\n"
      "ds_read_b64_tr_b16 a[224:225], v120 offset:33792\n"
      "v_exp_f32 v115, v183\n"
      "v_mfma_f32_16x16x32_bf16 v[64:67], a[120:123], v[16:19], v[64:67]\n"
      "ds_write_b128 v125, a[172:175] offset:3072\n"
      "ds_read_b64_tr_b16 a[226:227], v120 offset:37888\n"
      "v_exp_f32 v112, v184\n"
      "v_mfma_f32_16x16x32_bf16 v[68:71], a[120:123], v[20:23], v[68:71]\n"
      "v_cvt_pk_bf16_f32 v107, v114, v115\n"
      "v_exp_f32 v113, v185\n"
      "v_mfma_f32_16x16x32_bf16 v[72:75], a[120:123], v[24:27], v[72:75]\n"
      "v_or3_b32 v116, v116, v106, v107\n"
      "v_exp_f32 v114, v186\n"
      "v_cvt_pk_bf16_f32 v108, v112, v113\n"
      "v_mfma_f32_16x16x32_bf16 v[76:79], a[120:123], v[28:31], v[76:79]\n"
      "ds_write_b128 v125, v[104:107] offset:10240\n"
      "v_exp_f32 v115, v187\n"
      "v_mfma_f32_16x16x32_bf16 a[228:231], a[200:203], v[100:103], a[228:231]\n"
      "v_exp_f32 v112, v188\n"
      "v_cvt_pk_bf16_f32 v109, v114, v115\n"
      "v_mfma_f32_16x16x32_bf16 v[80:83], a[124:127], v[16:19], v[80:83]\n"
      "v_exp_f32 v113, v189\n"
      "v_or3_b32 v116, v116, v108, v109\n"
      "v_exp_f32 v114, v190\n"
      "v_mfma_f32_16x16x32_bf16 v[84:87], a[124:127], v[20:23], v[84:87]\n"
      "s_add_u32 m0, %[wb], 82944\n"
      "ds_write_b128 v125, a[188:191] offset:7168\n"
      "v_cvt_pk_bf16_f32 v110, v112, v113\n"
      "v_mfma_f32_16x16x32_bf16 v[88:91], a[124:127], v[24:27], v[88:91]\n"
      "buffer_load_dwordx4 v249, s[24:27], s78 offen nt lds\n"
      "v_exp_f32 v115, v191\n"
      "v_mfma_f32_16x16x32_bf16 v[92:95], a[124:127], v[28:31], v[92:95]\n"
      "s_add_u32 m0, %[wb], 91136\n"
      "s_nop 0\n"
      "v_cvt_pk_bf16_f32 v111, v114, v115\n"
      "v_or3_b32 v116, v116, v110, v111\n"
      "v_and_b32 v116, 0x40004000, v116\n"
      "v_mfma_f32_16x16x32_bf16 a[228:231], a[204:207], v[104:107], a[228:231]\n"
      "buffer_load_dwordx4 v122, s[24:27], s78 offen nt lds\n"
      "v_cmp_ne_u32 vcc, 0, v116\n"
      "s_cbranch_vccnz FB1_%=\n"
      "RET1_6_%=:\n"
      "ds_write_b128 v125, v[108:111] offset:11264\n"
      "v_mfma_f32_16x16x32_bf16 a[228:231], a[208:211], v[108:111], a[228:231]\n"
      "s_waitcnt lgkmcnt(9)\n"
      "v_mfma_f32_16x16x32_bf16 v[64:67], a[212:215], v[96:99], v[64:67]\n"
      "v_mov_b32 v148, 1.0\n"
      "v_mfma_f32_16x16x32_bf16 v[80:83], a[216:219], v[96:99], v[80:83]\n"
      "v_mov_b32 v149, 1.0\n"
      "v_mfma_f32_16x16x32_bf16 v[68:71], a[212:215], v[100:103], v[68:71]\n"
      "v_mov_b32 v157, s37\n"
      "v_mfma_f32_16x16x32_bf16 v[84:87], a[216:219], v[100:103], v[84:87]\n"
      "v_mov_b32 v150, s72\n"
      "v_mfma_f32_16x16x32_bf16 v[72:75], a[212:215], v[104:107], v[72:75]\n"
      "v_mov_b32 v151, s76\n"
      "ds_write_b32 v125, a228 offset:12288\n"
      "v_mfma_f32_16x16x32_bf16 v[88:91], a[216:219], v[104:107], v[88:91]\n"
      "v_xor_b32 v158, 8, v157\n"
      "v_mfma_f32_16x16x32_bf16 v[76:79], a[212:215], v[108:111], v[76:79]\n"
      "v_mfma_f32_16x16x32_bf16 v[92:95], a[216:219], v[108:111], v[92:95]\n"
      "s_waitcnt lgkmcnt(0)\n"
      "s_barrier\n"
      "s_memrealtime s[68:69]\n"
      "ds_read_b32 v237, v127 offset:12296\n"
      "ds_read_b64 v[152:153], v126 offset:12288\n"
      "ds_read_b128 v[160:163], v126 offset:0\n"
      "ds_read_b128 v[176:179], v126 offset:4096\n"
      "ds_read_b128 v[164:167], v126 offset:1024\n"
      "ds_read_b128 v[180:183], v126 offset:5120\n"
      "ds_read_b128 v[168:171], v126 offset:2048\n"
      "ds_read_b128 v[184:187], v126 offset:6144\n"
      "ds_read_b128 v[172:175], v126 offset:3072\n"
      "ds_read_b128 v[188:191], v126 offset:7168\n"
      "ds_read_b128 a[64:67], v126 offset:8192\n"
      "ds_read_b128 a[68:71], v126 offset:9216\n"
      "ds_read_b128 a[72:75], v126 offset:10240\n"
      "ds_read_b128 a[76:79], v126 offset:11264\n"
      "v_accvgpr_read_b32 v144, a228\n"
      "v_mov_b32 v236, v150\n"
      "s_waitcnt lgkmcnt(12)\n"
      "v_cmp_neq_f32 vcc, v153, v117\n"
      "v_readfirstlane_b32 s88, v237\n"
      "s_cbranch_vccnz MIS_%=\n"
      "MR_%=:\n"
      "v_mul_f32 v144, v144, v148\n"
      "v_mov_b32 v145, v117\n"
      "v_fmac_f32 v144, v152, v149\n"
      "s_waitcnt lgkmcnt(10)\n"
      "v_pk_add_f32 v[64:65], v[64:65], v[160:161]\n"
      "v_pk_add_f32 v[66:67], v[66:67], v[162:163]\n"
      "v_pk_add_f32 v[80:81], v[80:81], v[176:177]\n"
      "v_pk_add_f32 v[82:83], v[82:83], v[178:179]\n"
      "s_waitcnt lgkmcnt(8)\n"
      "v_pk_add_f32 v[68:69], v[68:69], v[164:165]\n"
      "v_pk_add_f32 v[70:71], v[70:71], v[166:167]\n"
      "v_pk_add_f32 v[84:85], v[84:85], v[180:181]\n"
      "v_pk_add_f32 v[86:87], v[86:87], v[182:183]\n"
      "s_waitcnt lgkmcnt(6)\n"
      "v_pk_add_f32 v[72:73], v[72:73], v[168:169]\n"
      "v_pk_add_f32 v[74:75], v[74:75], v[170:171]\n"
      "v_pk_add_f32 v[88:89], v[88:89], v[184:185]\n"
      "v_pk_add_f32 v[90:91], v[90:91], v[186:187]\n"
      "s_waitcnt lgkmcnt(4)\n"
      "v_pk_add_f32 v[76:77], v[76:77], v[172:173]\n"
      "v_pk_add_f32 v[78:79], v[78:79], v[174:175]\n"
      "v_pk_add_f32 v[92:93], v[92:93], v[188:189]\n"
      "v_pk_add_f32 v[94:95], v[94:95], v[190:191]\n"
      "s_waitcnt lgkmcnt(0)\n"
      "s_add_u32 s65, s68, 55\n"
      "s_mov_b32 s66, s88\n"
      "s_sub_u32 s67, s65, s66\n"
      "s_cmp_lt_i32 s67, 0\n"
      "s_cselect_b32 s88, s66, s65\n"
      "v_mov_b32 v237, s88\n"
      "s_mov_b32 s88, s66\n"
      "s_cmp_lg_u32 s33, 0\n"
      "s_cbranch_scc1 NH_%=\n"
      "s_mov_b64 exec, 1\n"
      "global_store_dwordx2 v157, v[236:237], s[18:19] sc1\n"
      "s_mov_b64 exec, -1\n"
      "NH_%=:\n"
      "v_mfma_f32_16x16x32_bf16 v[64:67], a[220:223], a[64:67], v[64:67]\n"
      "v_and_b32 v118, 0xf0, v127\n"
      "v_lshrrev_b32 v156, 3, v127\n"
      "v_mfma_f32_16x16x32_bf16 v[80:83], a[224:227], a[64:67], v[80:83]\n"
      "v_and_b32 v156, 32, v156\n"
      "v_lshl_or_b32 v156, v118, 4, v156\n"
      "v_mfma_f32_16x16x32_bf16 v[68:71], a[220:223], a[68:71], v[68:71]\n"
      "v_lshrrev_b32 v118, 5, v127\n"
      "v_and_or_b32 v156, v118, 16, v156\n"
      "v_mfma_f32_16x16x32_bf16 v[84:87], a[224:227], a[68:71], v[84:87]\n"
      "v_add_u32 v156, s36, v156\n"
      "v_add_u32 v155, 0x2000, v156\n"
      "v_mfma_f32_16x16x32_bf16 v[72:75], a[220:223], a[72:75], v[72:75]\n"
      "v_cvt_pk_bf16_f32 v128, v64, v65\n"
      "v_cvt_pk_bf16_f32 v129, v66, v67\n"
      "v_mfma_f32_16x16x32_bf16 v[88:91], a[224:227], a[72:75], v[88:91]\n"
      "v_cvt_pk_bf16_f32 v130, v80, v81\n"
      "v_cvt_pk_bf16_f32 v131, v82, v83\n"
      "v_mfma_f32_16x16x32_bf16 v[76:79], a[220:223], a[76:79], v[76:79]\n"
      "v_xor_b32 v242, 0x2000, v240\n"
      "v_xor_b32 v243, 0x2000, v241\n"
      "v_mfma_f32_16x16x32_bf16 v[92:95], a[224:227], a[76:79], v[92:95]\n"
      "v_cvt_pk_bf16_f32 v132, v68, v69\n"
      "v_cvt_pk_bf16_f32 v133, v70, v71\n"
      "s_memrealtime s[68:69]\n"
      "v_cvt_pk_bf16_f32 v134, v84, v85\n"
      "v_cvt_pk_bf16_f32 v135, v86, v87\n"
      "v_mad_u32_u24 v147, v144, s60, v151\n"
      "v_mad_u32_u24 v147, v145, s62, v147\n"
      "v_add3_u32 v146, v150, v144, v145\n"
      "v_mad_u32_u24 v147, v128, s60, v147\n"
      "v_mad_u32_u24 v147, v129, s62, v147\n"
      "v_add3_u32 v146, v146, v128, v129\n"
      "v_mad_u32_u24 v147, v130, s70, v147\n"
      "v_mad_u32_u24 v147, v131, s71, v147\n"
      "v_add3_u32 v146, v146, v130, v131\n"
      "v_cvt_pk_bf16_f32 v136, v72, v73\n"
      "v_cvt_pk_bf16_f32 v137, v74, v75\n"
      "v_cvt_pk_bf16_f32 v138, v88, v89\n"
      "v_cvt_pk_bf16_f32 v139, v90, v91\n"
      "v_cvt_pk_bf16_f32 v140, v76, v77\n"
      "v_cvt_pk_bf16_f32 v141, v78, v79\n"
      "v_cvt_pk_bf16_f32 v142, v92, v93\n"
      "v_cvt_pk_bf16_f32 v143, v94, v95\n"
      "v_mad_u32_u24 v147, v132, s60, v147\n"
      "v_mad_u32_u24 v147, v133, s62, v147\n"
      "v_add3_u32 v146, v146, v132, v133\n"
      "v_mad_u32_u24 v147, v134, s70, v147\n"
      "v_mad_u32_u24 v147, v135, s71, v147\n"
      "v_add3_u32 v146, v146, v134, v135\n"
      "v_mad_u32_u24 v147, v136, s60, v147\n"
      "v_mad_u32_u24 v147, v137, s62, v147\n"
      "v_add3_u32 v146, v146, v136, v137\n"
      "v_mad_u32_u24 v147, v138, s70, v147\n"
      "v_mad_u32_u24 v147, v139, s71, v147\n"
      "v_add3_u32 v146, v146, v138, v139\n"
      "v_mad_u32_u24 v147, v140, s60, v147\n"
      "v_mad_u32_u24 v147, v141, s62, v147\n"
      "v_add3_u32 v146, v146, v140, v141\n"
      "v_mad_u32_u24 v147, v142, s70, v147\n"
      "v_mad_u32_u24 v147, v143, s71, v147\n"
      "v_add3_u32 v146, v146, v142, v143\n"
      "s_waitcnt lgkmcnt(0)\n"
      "s_call_b64 s[84:85], SPIN_%=\n"
      "global_load_dwordx2 v[232:233], v158, s[18:19] sc1\n"
      "buffer_store_dwordx4 v[128:131], v127, s[28:31], s35 offen offset:0 sc1\n"
      "buffer_store_dwordx4 v[132:135], v127, s[28:31], s35 offen offset:1024 sc1\n"
      "buffer_store_dwordx4 v[136:139], v127, s[28:31], s35 offen offset:2048 sc1\n"
      "buffer_store_dwordx4 v[140:143], v127, s[28:31], s35 offen offset:3072 sc1\n"
      "buffer_store_dwordx4 v[144:147], v127, s[28:31], s81 offen sc1\n"
      "global_load_dwordx2 v[234:235], v158, s[18:19] sc1\n"
      "s_waitcnt vmcnt(7)\n"
      "s_barrier\n"
      "ds_read_b64_tr_b16 a[96:97], v246 offset:32768\n"
      "ds_read_b64_tr_b16 a[98:99], v246 offset:36864\n"
      "ds_read_b64_tr_b16 a[100:101], v247 offset:32768\n"
      "ds_read_b64_tr_b16 a[102:103], v247 offset:36864\n"
      "ds_read_b64_tr_b16 a[112:113], v250 offset:0\n"
      "ds_read_b64_tr_b16 a[114:115], v250 offset:4096\n"
      "ds_read_b64_tr_b16 a[116:117], v251 offset:0\n"
      "ds_read_b64_tr_b16 a[118:119], v251 offset:4096\n"
      "ds_read_b64_tr_b16 a[0:1], v250 offset:32768\n"
      "ds_read_b64_tr_b16 a[2:3], v250 offset:36864\n"
      "ds_read_b64_tr_b16 a[4:5], v251 offset:32768\n"
      "ds_read_b64_tr_b16 a[6:7], v251 offset:36864\n"
      "s_waitcnt lgkmcnt(8)\n"
      "v_mfma_f32_16x16x32_bf16 a[128:131], a[96:99], v[208:211], a[128:131]\n"
      "ds_read_b64_tr_b16 a[16:17], v246 offset:0\n"
      "ds_read_b64_tr_b16 a[18:19], v246 offset:4096\n"
      "v_mfma_f32_16x16x32_bf16 a[144:147], a[100:103], v[208:211], a[144:147]\n"
      "ds_read_b64_tr_b16 a[20:21], v247 offset:0\n"
      "ds_read_b64_tr_b16 a[22:23], v247 offset:4096\n"
      "s_waitcnt lgkmcnt(8)\n"
      "v_mfma_f32_16x16x32_bf16 a[128:131], a[112:115], v[192:195], a[128:131]\n"
      "ds_read_b64_tr_b16 a[104:105], v244 offset:32768\n"
      "ds_read_b64_tr_b16 a[106:107], v244 offset:36864\n"
      "v_mfma_f32_16x16x32_bf16 a[144:147], a[116:119], v[192:195], a[144:147]\n"
      "ds_read_b64_tr_b16 a[108:109], v245 offset:32768\n"
      "ds_read_b64_tr_b16 a[110:111], v245 offset:36864\n"
      "s_waitcnt lgkmcnt(8)\n"
      "v_mfma_f32_16x16x32_bf16 a[128:131], a[0:3], v[0:3], a[128:131]\n"
      "ds_read_b64_tr_b16 a[120:121], v240 offset:0\n"
      "ds_read_b64_tr_b16 a[122:123], v240 offset:4096\n"
      "v_mfma_f32_16x16x32_bf16 a[144:147], a[4:7], v[0:3], a[144:147]\n"
      "ds_read_b64_tr_b16 a[124:125], v241 offset:0\n"
      "ds_read_b64_tr_b16 a[126:127], v241 offset:4096\n"
      "s_waitcnt lgkmcnt(8)\n"
      "v_mfma_f32_16x16x32_bf16 a[128:131], a[16:19], v[16:19], a[128:131]\n"
      "ds_read_b64_tr_b16 a[8:9], v240 offset:32768\n"
      "ds_read_b64_tr_b16 a[10:11], v240 offset:36864\n"
      "v_mfma_f32_16x16x32_bf16 a[144:147], a[20:23], v[16:19], a[144:147]\n"
      "ds_read_b64_tr_b16 a[12:13], v241 offset:32768\n"
      "ds_read_b64_tr_b16 a[14:15], v241 offset:36864\n"
      "v_mfma_f32_16x16x32_bf16 a[132:135], a[96:99], v[212:215], a[132:135]\n"
      "ds_read_b64_tr_b16 a[24:25], v244 offset:0\n"
      "ds_read_b64_tr_b16 a[26:27], v244 offset:4096\n"
      "v_mfma_f32_16x16x32_bf16 a[148:151], a[100:103], v[212:215], a[148:151]\n"
      "ds_read_b64_tr_b16 a[28:29], v245 offset:0\n"
      "ds_read_b64_tr_b16 a[30:31], v245 offset:4096\n"
      "v_mfma_f32_16x16x32_bf16 a[132:135], a[112:115], v[196:199], a[132:135]\n"
      "ds_read_b64_tr_b16 a[32:33], v240 offset:1024\n"
      "ds_read_b64_tr_b16 a[34:35], v240 offset:5120\n"
      "v_mfma_f32_16x16x32_bf16 a[148:151], a[116:119], v[196:199], a[148:151]\n"
      "ds_read_b64_tr_b16 a[36:37], v241 offset:1024\n"
      "ds_read_b64_tr_b16 a[38:39], v241 offset:5120\n"
      "v_mfma_f32_16x16x32_bf16 a[132:135], a[0:3], v[4:7], a[132:135]\n"
      "ds_read_b64_tr_b16 a[40:41], v242 offset:1024\n"
      "ds_read_b64_tr_b16 a[42:43], v242 offset:5120\n"
      "v_mfma_f32_16x16x32_bf16 a[148:151], a[4:7], v[4:7], a[148:151]\n"
      "ds_read_b64_tr_b16 a[44:45], v243 offset:1024\n"
      "ds_read_b64_tr_b16 a[46:47], v243 offset:5120\n"
      "v_mfma_f32_16x16x32_bf16 a[132:135], a[16:19], v[20:23], a[132:135]\n"
      "v_lshrrev_b32 v118, 2, v127\n"
      "v_mfma_f32_16x16x32_bf16 a[148:151], a[20:23], v[20:23], a[148:151]\n"
      "v_xor_b32 v118, v118, v127\n"
      "v_mfma_f32_16x16x32_bf16 a[136:139], a[96:99], v[216:219], a[136:139]\n"
      "ds_write_b128 v125, a[128:131] offset:0\n"
      "v_and_b32 v118, 0xc0, v118\n"
      "v_mfma_f32_16x16x32_bf16 a[152:155], a[100:103], v[216:219], a[152:155]\n"
      "v_and_or_b32 v118, v127, 32, v118\n"
      "v_mfma_f32_16x16x32_bf16 a[136:139], a[112:115], v[200:203], a[136:139]\n"
      "v_and_b32 v119, 16, v127\n"
      "v_mfma_f32_16x16x32_bf16 a[152:155], a[116:119], v[200:203], a[152:155]\n"
      "v_mov_b32 v84, 0x3f80\n"
      "v_mfma_f32_16x16x32_bf16 a[136:139], a[0:3], v[8:11], a[136:139]\n"
      "ds_write_b128 v125, a[144:147] offset:4096\n"
      "v_lshlrev_b32 v119, v119, v84\n"
      "v_mfma_f32_16x16x32_bf16 a[152:155], a[4:7], v[8:11], a[152:155]\n"
      "v_cmp_eq_u32 vcc, 0, v118\n"
      "v_mfma_f32_16x16x32_bf16 a[136:139], a[16:19], v[24:27], a[136:139]\n"
      "v_mov_b32 v84, 0\n"
      "v_mfma_f32_16x16x32_bf16 a[152:155], a[20:23], v[24:27], a[152:155]\n"
      "v_cndmask_b32 v86, 0, v119, vcc\n"
      "v_mfma_f32_16x16x32_bf16 a[140:143], a[96:99], v[220:223], a[140:143]\n"
      "ds_write_b128 v125, a[132:135] offset:1024\n"
      "v_cmp_eq_u32 vcc, 32, v118\n"
      "v_mfma_f32_16x16x32_bf16 a[156:159], a[100:103], v[220:223], a[156:159]\n"
      "v_mov_b32 v85, 0\n"
      "v_mfma_f32_16x16x32_bf16 a[140:143], a[112:115], v[204:207], a[140:143]\n"
      "v_cndmask_b32 v87, 0, v119, vcc\n"
      "v_mfma_f32_16x16x32_bf16 a[156:159], a[116:119], v[204:207], a[156:159]\n"
      "v_mov_b32 v88, 0\n"
      "v_mfma_f32_16x16x32_bf16 a[140:143], a[0:3], v[12:15], a[140:143]\n"
      "ds_write_b128 v125, a[148:151] offset:5120\n"
      "v_mov_b32 v89, 0\n"
      "v_mfma_f32_16x16x32_bf16 a[156:159], a[4:7], v[12:15], a[156:159]\n"
      "v_mfma_f32_16x16x32_bf16 a[140:143], a[16:19], v[28:31], a[140:143]\n"
      "v_mfma_f32_16x16x32_bf16 a[156:159], a[20:23], v[28:31], a[156:159]\n"
      "s_waitcnt lgkmcnt(15)\n"
      "v_mfma_f32_16x16x32_bf16 v[32:35], a[104:107], v[208:211], v[32:35]\n"
      "ds_write_b128 v125, a[136:139] offset:2048\n"
      "v_mfma_f32_16x16x32_bf16 v[36:39], a[104:107], v[212:215], v[36:39]\n"
      "v_mfma_f32_16x16x32_bf16 v[40:43], a[104:107], v[216:219], v[40:43]\n"
      "v_mfma_f32_16x16x32_bf16 v[44:47], a[104:107], v[220:223], v[44:47]\n"
      "v_mfma_f32_16x16x32_bf16 v[48:51], a[108:111], v[208:211], v[48:51]\n"
      "ds_write_b128 v125, a[152:155] offset:6144\n"
      "v_mfma_f32_16x16x32_bf16 v[52:55], a[108:111], v[212:215], v[52:55]\n"
      "v_mfma_f32_16x16x32_bf16 v[56:59], a[108:111], v[216:219], v[56:59]\n"
      "v_mfma_f32_16x16x32_bf16 v[60:63], a[108:111], v[220:223], v[60:63]\n"
      "s_waitcnt lgkmcnt(15)\n"
      "v_mfma_f32_16x16x32_bf16 v[32:35], a[120:123], v[192:195], v[32:35]\n"
      "ds_write_b128 v125, a[140:143] offset:3072\n"
      "v_mfma_f32_16x16x32_bf16 v[36:39], a[120:123], v[196:199], v[36:39]\n"
      "v_mfma_f32_16x16x32_bf16 v[40:43], a[120:123], v[200:203], v[40:43]\n"
      "v_mfma_f32_16x16x32_bf16 v[44:47], a[120:123], v[204:207], v[44:47]\n"
      "v_mfma_f32_16x16x32_bf16 v[48:51], a[124:127], v[192:195], v[48:51]\n"
      "ds_write_b128 v125, a[156:159] offset:7168\n"
      "v_mfma_f32_16x16x32_bf16 v[52:55], a[124:127], v[196:199], v[52:55]\n"
      "v_mfma_f32_16x16x32_bf16 v[56:59], a[124:127], v[200:203], v[56:59]\n"
      "v_mfma_f32_16x16x32_bf16 v[60:63], a[124:127], v[204:207], v[60:63]\n"
      "s_waitcnt lgkmcnt(15)\n"
      "s_waitcnt vmcnt(6)\n"
      "v_cmp_ne_u32 vcc, v232, v150\n"
      "s_memrealtime s[68:69]\n"
      "s_cbranch_vccnz POLL_%=\n"
      "GOT_%=:\n"
      "v_readfirstlane_b32 s88, v233\n"
      "s_waitcnt lgkmcnt(0)\n"
      "s_add_u32 s88, s88, 57\n"
      "s_sub_u32 s67, s88, s68\n"
      "s_cmp_gt_u32 s67, 1000\n"
      "s_cbranch_scc1 LD_%=\n"
      "s_call_b64 s[84:85], SPIN_%=\n"
      "LD_%=:\n"
      "buffer_load_dwordx4 v[80:83], v127, s[28:31], s79 offen sc1\n"
      "buffer_load_dwordx4 v[64:67], v127, s[28:31], s80 offen offset:0 sc1\n"
      "buffer_load_dwordx4 v[68:71], v127, s[28:31], s80 offen offset:1024 sc1\n"
      "buffer_load_dwordx4 v[72:75], v127, s[28:31], s80 offen offset:2048 sc1\n"
      "buffer_load_dwordx4 v[76:79], v127, s[28:31], s80 offen offset:3072 sc1\n"
      "v_mfma_f32_16x16x32_bf16 v[32:35], a[8:11], v[0:3], v[32:35]\n"
      "v_mfma_f32_16x16x32_bf16 v[36:39], a[8:11], v[4:7], v[36:39]\n"
      "v_mfma_f32_16x16x32_bf16 v[40:43], a[8:11], v[8:11], v[40:43]\n"
      "v_mfma_f32_16x16x32_bf16 v[44:47], a[8:11], v[12:15], v[44:47]\n"
      "v_mfma_f32_16x16x32_bf16 v[48:51], a[12:15], v[0:3], v[48:51]\n"
      "v_mfma_f32_16x16x32_bf16 v[52:55], a[12:15], v[4:7], v[52:55]\n"
      "v_mfma_f32_16x16x32_bf16 v[56:59], a[12:15], v[8:11], v[56:59]\n"
      "v_mfma_f32_16x16x32_bf16 v[60:63], a[12:15], v[12:15], v[60:63]\n"
      "s_waitcnt lgkmcnt(15)\n"
      "v_mfma_f32_16x16x32_bf16 v[32:35], a[24:27], v[16:19], v[32:35]\n"
      "v_mfma_f32_16x16x32_bf16 v[36:39], a[24:27], v[20:23], v[36:39]\n"
      "v_mfma_f32_16x16x32_bf16 v[40:43], a[24:27], v[24:27], v[40:43]\n"
      "v_mfma_f32_16x16x32_bf16 v[44:47], a[24:27], v[28:31], v[44:47]\n"
      "v_mfma_f32_16x16x32_bf16 v[48:51], a[28:31], v[16:19], v[48:51]\n"
      "v_mfma_f32_16x16x32_bf16 v[52:55], a[28:31], v[20:23], v[52:55]\n"
      "v_mfma_f32_16x16x32_bf16 v[56:59], a[28:31], v[24:27], v[56:59]\n"
      "v_mfma_f32_16x16x32_bf16 v[60:63], a[28:31], v[28:31], v[60:63]\n"
      "s_waitcnt lgkmcnt(12)\n"
      "v_mfma_f32_16x16x32_bf16 v[32:35], a[32:35], v[96:99], v[32:35]\n"
      "v_mfma_f32_16x16x32_bf16 v[48:51], a[36:39], v[96:99], v[48:51]\n"
      "v_mfma_f32_16x16x32_bf16 v[36:39], a[32:35], v[100:103], v[36:39]\n"
      "v_mfma_f32_16x16x32_bf16 v[52:55], a[36:39], v[100:103], v[52:55]\n"
      "v_mfma_f32_16x16x32_bf16 v[40:43], a[32:35], v[104:107], v[40:43]\n"
      "v_mfma_f32_16x16x32_bf16 v[56:59], a[36:39], v[104:107], v[56:59]\n"
      "v_mfma_f32_16x16x32_bf16 v[44:47], a[32:35], v[108:111], v[44:47]\n"
      "v_mfma_f32_16x16x32_bf16 v[60:63], a[36:39], v[108:111], v[60:63]\n"
      "s_waitcnt lgkmcnt(8)\n"
      "v_mfma_f32_16x16x32_bf16 v[32:35], a[40:43], a[64:67], v[32:35]\n"
      "v_mfma_f32_16x16x32_bf16 v[48:51], a[44:47], a[64:67], v[48:51]\n"
      "v_mfma_f32_16x16x32_bf16 v[36:39], a[40:43], a[68:71], v[36:39]\n"
      "v_mfma_f32_16x16x32_bf16 v[52:55], a[44:47], a[68:71], v[52:55]\n"
      "v_mfma_f32_16x16x32_bf16 v[40:43], a[40:43], a[72:75], v[40:43]\n"
      "v_mfma_f32_16x16x32_bf16 v[56:59], a[44:47], a[72:75], v[56:59]\n"
      "v_mfma_f32_16x16x32_bf16 v[44:47], a[40:43], a[76:79], v[44:47]\n"
      "v_mfma_f32_16x16x32_bf16 v[60:63], a[44:47], a[76:79], v[60:63]\n"
      "s_waitcnt lgkmcnt(0)\n"
      "s_barrier\n"
      "ds_read_b128 v[160:163], v126 offset:0\n"
      "ds_read_b128 v[176:179], v126 offset:4096\n"
      "ds_read_b128 v[164:167], v126 offset:1024\n"
      "ds_read_b128 v[180:183], v126 offset:5120\n"
      "ds_read_b128 v[168:171], v126 offset:2048\n"
      "ds_read_b128 v[184:187], v126 offset:6144\n"
      "ds_read_b128 v[172:175], v126 offset:3072\n"
      "ds_read_b128 v[188:191], v126 offset:7168\n"
      "s_waitcnt lgkmcnt(6)\n"
      "v_pk_add_f32 v[32:33], v[32:33], v[160:161]\n"
      "v_pk_add_f32 v[34:35], v[34:35], v[162:163]\n"
      "v_pk_add_f32 v[48:49], v[48:49], v[176:177]\n"
      "v_pk_add_f32 v[50:51], v[50:51], v[178:179]\n"
      "s_waitcnt lgkmcnt(4)\n"
      "v_pk_add_f32 v[36:37], v[36:37], v[164:165]\n"
      "v_pk_add_f32 v[38:39], v[38:39], v[166:167]\n"
      "v_pk_add_f32 v[52:53], v[52:53], v[180:181]\n"
      "v_pk_add_f32 v[54:55], v[54:55], v[182:183]\n"
      "s_waitcnt lgkmcnt(2)\n"
      "v_pk_add_f32 v[40:41], v[40:41], v[168:169]\n"
      "v_pk_add_f32 v[42:43], v[42:43], v[170:171]\n"
      "v_pk_add_f32 v[56:57], v[56:57], v[184:185]\n"
      "v_pk_add_f32 v[58:59], v[58:59], v[186:187]\n"
      "s_waitcnt lgkmcnt(0)\n"
      "v_pk_add_f32 v[44:45], v[44:45], v[172:173]\n"
      "v_pk_add_f32 v[46:47], v[46:47], v[174:175]\n"
      "v_pk_add_f32 v[60:61], v[60:61], v[188:189]\n"
      "v_pk_add_f32 v[62:63], v[62:63], v[190:191]\n"
      "CHK_%=:\n"
      "s_waitcnt vmcnt(4)\n"
      "v_add_f32 v224, v144, v80\n"
      "v_rcp_f32 v224, v224\n"
      "v_mad_u32_u24 v119, v80, s60, v151\n"
      "v_mad_u32_u24 v119, v81, s62, v119\n"
      "v_add3_u32 v118, v150, v80, v81\n"
      "s_waitcnt vmcnt(3)\n"
      "v_mfma_f32_16x16x32_bf16 v[160:163], v[86:89], v[64:67], v[32:35]\n"
      "v_mad_u32_u24 v119, v64, s60, v119\n"
      "v_mad_u32_u24 v119, v65, s62, v119\n"
      "v_add3_u32 v118, v118, v64, v65\n"
      "v_mov_b32 v226, v224\n"
      "v_mfma_f32_16x16x32_bf16 v[176:179], v[84:87], v[64:67], v[48:51]\n"
      "v_mad_u32_u24 v119, v66, s70, v119\n"
      "v_mad_u32_u24 v119, v67, s71, v119\n"
      "v_add3_u32 v118, v118, v66, v67\n"
      "v_permlane32_swap_b32 v224, v226\n"
      "s_waitcnt vmcnt(2)\n"
      "v_mfma_f32_16x16x32_bf16 v[164:167], v[86:89], v[68:71], v[36:39]\n"
      "v_mad_u32_u24 v119, v68, s60, v119\n"
      "v_mad_u32_u24 v119, v69, s62, v119\n"
      "v_add3_u32 v118, v118, v68, v69\n"
      "v_mov_b32 v225, v224\n"
      "v_mov_b32 v227, v226\n"
      "v_mfma_f32_16x16x32_bf16 v[180:183], v[84:87], v[68:71], v[52:55]\n"
      "v_mad_u32_u24 v119, v70, s70, v119\n"
      "v_mad_u32_u24 v119, v71, s71, v119\n"
      "v_add3_u32 v118, v118, v70, v71\n"
      "v_permlane16_swap_b32 v224, v225\n"
      "s_waitcnt vmcnt(1)\n"
      "v_mfma_f32_16x16x32_bf16 v[168:171], v[86:89], v[72:75], v[40:43]\n"
      "v_mad_u32_u24 v119, v72, s60, v119\n"
      "v_mad_u32_u24 v119, v73, s62, v119\n"
      "v_add3_u32 v118, v118, v72, v73\n"
      "v_permlane16_swap_b32 v226, v227\n"
      "v_mfma_f32_16x16x32_bf16 v[184:187], v[84:87], v[72:75], v[56:59]\n"
      "v_mad_u32_u24 v119, v74, s70, v119\n"
      "v_mad_u32_u24 v119, v75, s71, v119\n"
      "v_add3_u32 v118, v118, v74, v75\n"
      "s_waitcnt vmcnt(0)\n"
      "v_mfma_f32_16x16x32_bf16 v[172:175], v[86:89], v[76:79], v[44:47]\n"
      "v_mad_u32_u24 v119, v76, s60, v119\n"
      "v_mad_u32_u24 v119, v77, s62, v119\n"
      "v_add3_u32 v118, v118, v76, v77\n"
      "v_mfma_f32_16x16x32_bf16 v[188:191], v[84:87], v[76:79], v[60:63]\n"
      "v_mad_u32_u24 v119, v78, s70, v119\n"
      "v_mad_u32_u24 v119, v79, s71, v119\n"
      "v_add3_u32 v118, v118, v78, v79\n"
      "v_cmp_ne_u64 vcc, v[118:119], v[82:83]\n"
      "s_cbranch_vccnz RETRY_%=\n"
      "OK_%=:\n"
      "v_cmp_neq_f32 vcc, v81, v117\n"
      "s_cbranch_vccnz FIX_%=\n"
      "FR_%=:\n"
      "v_pk_mul_f32 v[160:161], v[160:161], v[224:225] op_sel:[0,0] op_sel_hi:[1,0]\n"
      "v_pk_mul_f32 v[162:163], v[162:163], v[224:225] op_sel:[0,0] op_sel_hi:[1,0]\n"
      "v_pk_mul_f32 v[176:177], v[176:177], v[224:225] op_sel:[0,0] op_sel_hi:[1,0]\n"
      "v_pk_mul_f32 v[178:179], v[178:179], v[224:225] op_sel:[0,0] op_sel_hi:[1,0]\n"
      "v_cvt_pk_bf16_f32 v128, v160, v161\n"
      "v_cvt_pk_bf16_f32 v130, v176, v177\n"
      "v_cvt_pk_bf16_f32 v129, v162, v163\n"
      "v_cvt_pk_bf16_f32 v131, v178, v179\n"
      "v_pk_mul_f32 v[164:165], v[164:165], v[224:225] op_sel:[0,1] op_sel_hi:[1,1]\n"
      "v_permlane16_swap_b32 v128, v130\n"
      "v_permlane16_swap_b32 v129, v131\n"
      "global_store_dwordx4 v156, v[128:131], s[16:17] offset:-4096 sc1\n"
      "v_pk_mul_f32 v[166:167], v[166:167], v[224:225] op_sel:[0,1] op_sel_hi:[1,1]\n"
      "v_pk_mul_f32 v[180:181], v[180:181], v[224:225] op_sel:[0,1] op_sel_hi:[1,1]\n"
      "v_pk_mul_f32 v[182:183], v[182:183], v[224:225] op_sel:[0,1] op_sel_hi:[1,1]\n"
      "v_cvt_pk_bf16_f32 v132, v164, v165\n"
      "v_cvt_pk_bf16_f32 v134, v180, v181\n"
      "v_cvt_pk_bf16_f32 v133, v166, v167\n"
      "v_cvt_pk_bf16_f32 v135, v182, v183\n"
      "v_pk_mul_f32 v[168:169], v[168:169], v[226:227] op_sel:[0,0] op_sel_hi:[1,0]\n"
      "v_permlane16_swap_b32 v132, v134\n"
      "v_permlane16_swap_b32 v133, v135\n"
      "global_store_dwordx4 v156, v[132:135], s[16:17] offset:0 sc1\n"
      "v_pk_mul_f32 v[170:171], v[170:171], v[226:227] op_sel:[0,0] op_sel_hi:[1,0]\n"
      "v_pk_mul_f32 v[184:185], v[184:185], v[226:227] op_sel:[0,0] op_sel_hi:[1,0]\n"
      "v_pk_mul_f32 v[186:187], v[186:187], v[226:227] op_sel:[0,0] op_sel_hi:[1,0]\n"
      "v_cvt_pk_bf16_f32 v136, v168, v169\n"
      "v_cvt_pk_bf16_f32 v138, v184, v185\n"
      "v_cvt_pk_bf16_f32 v137, v170, v171\n"
      "v_cvt_pk_bf16_f32 v139, v186, v187\n"
      "v_pk_mul_f32 v[172:173], v[172:173], v[226:227] op_sel:[0,1] op_sel_hi:[1,1]\n"
      "v_permlane16_swap_b32 v136, v138\n"
      "v_permlane16_swap_b32 v137, v139\n"
      "global_store_dwordx4 v155, v[136:139], s[16:17] offset:-4096 sc1\n"
      "v_pk_mul_f32 v[174:175], v[174:175], v[226:227] op_sel:[0,1] op_sel_hi:[1,1]\n"
      "v_pk_mul_f32 v[188:189], v[188:189], v[226:227] op_sel:[0,1] op_sel_hi:[1,1]\n"
      "v_pk_mul_f32 v[190:191], v[190:191], v[226:227] op_sel:[0,1] op_sel_hi:[1,1]\n"
      "v_cvt_pk_bf16_f32 v140, v172, v173\n"
      "v_cvt_pk_bf16_f32 v142, v188, v189\n"
      "v_cvt_pk_bf16_f32 v141, v174, v175\n"
      "v_cvt_pk_bf16_f32 v143, v190, v191\n"
      "v_and_b32 v157, 0xffffff80, v157\n"
      "v_permlane16_swap_b32 v140, v142\n"
      "v_permlane16_swap_b32 v141, v143\n"
      "global_store_dwordx4 v155, v[140:143], s[16:17] offset:0 sc1\n"
      "s_and_b32 s67, s37, 8\n"
      "s_or_b32 s67, s67, s33\n"
      "s_cbranch_scc1 END_%=\n"
      "s_mov_b64 exec, 1\n"
      "global_store_dwordx2 v157, v[150:151], s[18:19] sc1\n"
      "s_mov_b64 exec, -1\n"
      "END_%=:\n"
      ".subsection 1\n"
      "POLL_%=:\n"
      "s_waitcnt vmcnt(0) lgkmcnt(0)\n"
      "v_cmp_ne_u32 vcc, v234, v150\n"
      "v_mov_b32 v233, v235\n"
      "s_memrealtime s[68:69]\n"
      "s_sub_u32 s63, s63, 1\n"
      "s_cbranch_vccz GOT_%=\n"
      "global_load_dwordx2 v[234:235], v158, s[18:19] sc1\n"
      "s_cbranch_scc0 POLL_%=\n"
      "s_branch GOT_%=\n"
      "RETRY_%=:\n"
      "s_sub_u32 s63, s63, 1\n"
      "s_cbranch_scc1 OK_%=\n"
      "s_sleep 4\n"
      "buffer_load_dwordx4 v[80:83], v127, s[28:31], s79 offen sc1\n"
      "buffer_load_dwordx4 v[64:67], v127, s[28:31], s80 offen offset:0 sc1\n"
      "buffer_load_dwordx4 v[68:71], v127, s[28:31], s80 offen offset:1024 sc1\n"
      "buffer_load_dwordx4 v[72:75], v127, s[28:31], s80 offen offset:2048 sc1\n"
      "buffer_load_dwordx4 v[76:79], v127, s[28:31], s80 offen offset:3072 sc1\n"
      "s_branch CHK_%=\n"
      "GW0_%=:\n"
      "s_call_b64 s[84:85], WAIT_%=\n"
      "s_branch GX0_%=\n"
      "GW1_%=:\n"
      "s_call_b64 s[84:85], WAIT_%=\n"
      "s_branch GX1_%=\n"
      "GW2_%=:\n"
      "s_call_b64 s[84:85], WAIT_%=\n"
      "s_branch GX2_%=\n"
      "GW3_%=:\n"
      "s_call_b64 s[84:85], WAIT_%=\n"
      "s_add_u32 s87, s87, 1\n"
      "s_branch GX3_%=\n"
      "GW4_%=:\n"
      "s_call_b64 s[84:85], WAIT_%=\n"
      "s_branch GX4_%=\n"
      "GW5_%=:\n"
      "s_call_b64 s[84:85], WAIT_%=\n"
      "s_branch GX5_%=\n"
      "GW6_%=:\n"
      "s_call_b64 s[84:85], WAIT_%=\n"
      "s_branch GX6_%=\n"
      "GW7_%=:\n"
      "s_call_b64 s[84:85], WAIT_%=\n"
      "s_branch GX7_%=\n"
      "GW8_%=:\n"
      "s_call_b64 s[84:85], WAIT_%=\n"
      "s_branch GX8_%=\n"
      "WAIT_%=:\n"
      "s_add_u32 s87, s87, 1\n"
      "s_bitcmp1_b32 s89, s87\n"
      "s_cbranch_scc1 H1_%=\n"
      "SPIN_%=:\n"
      "s_sub_u32 s67, s68, s88\n"
      "s_cmp_ge_i32 s67, 0\n"
      "s_cbranch_scc1 GO_%=\n"
      "s_sleep 1\n"
      "s_memrealtime s[68:69]\n"
      "s_waitcnt lgkmcnt(0)\n"
      "s_branch SPIN_%=\n"
      "GO_%=:\n"
      "s_setpc_b64 s[84:85]\n"
      "H1_%=:\n"
      "s_cmp_gt_i32 s93, 50\n"
      "s_cbranch_scc1 HS_%=\n"
      "s_bitcmp1_b32 0x2a80, s87\n"
      "s_cbranch_scc0 H2_%=\n"
      "s_add_u32 s67, s66, 400\n"
      "s_waitcnt vmcnt(8)\n"
      "v_sub_u32 v250, s67, v250\n"
      "v_cmp_gt_u32 vcc, 900, v250\n"
      "v_add_u32 v250, 0xa00, v250\n"
      "v_xor_b32 v251, 0x100, v251\n"
      "s_sub_u32 s69, s66, s71\n"
      "s_cmp_ge_u32 s87, 12\n"
      "s_cselect_b32 s101, 50, s101\n"
      "s_bcnt1_i32_b32 s74, vcc_lo\n"
      "s_bcnt1_i32_b32 s75, vcc_hi\n"
      "v_cmp_gt_u32 vcc, 0x800, v250\n"
      "s_add_u32 s67, s74, s75\n"
      "s_cmp_eq_u32 s87, 7\n"
      "s_cselect_b32 s76, 1, 0\n"
      "s_lshr_b32 s76, s67, s76\n"
      "s_cmp_le_u32 s76, 2\n"
      "s_cselect_b32 s86, 107, s98\n"
      "s_cmp_ge_u32 s76, 9\n"
      "s_cselect_b32 s86, 112, s86\n"
      "s_cbranch_vccnz H6_%=\n"
      "s_cmp_ge_u32 s74, 12\n"
      "s_cbranch_scc1 H3_%=\n"
      "s_cmp_eq_u32 s87, 13\n"
      "s_cbranch_scc1 H4_%=\n"
      "H8_%=:\n"
      "s_lshr_b32 s67, s39, 7\n"
      "s_cmp_eq_u32 s67, 1\n"
      "s_cbranch_scc0 SPIN_%=\n"
      "H5_%=:\n"
      "s_cmp_gt_i32 s93, 50\n"
      "s_cselect_b32 s75, -10, 0\n"
      "v_mov_b32 v127, s66\n"
      "s_add_u32 s74, s87, 2\n"
      "s_bfe_u32 s74, s74, 0x10001\n"
      "s_lshl_b32 s74, s74, 8\n"
      "s_add_u32 s74, s74, s39\n"
      "RS_%=:\n"
      "s_sub_u32 s67, s68, s88\n"
      "s_cmp_ge_i32 s67, s75\n"
      "s_cbranch_scc1 RG_%=\n"
      "s_sleep 1\n"
      "s_memrealtime s[68:69]\n"
      "s_waitcnt lgkmcnt(0)\n"
      "s_branch RS_%=\n"
      "RG_%=:\n"
      "s_getreg_b32 s67, hwreg(HW_REG_IB_STS, 0, 4)\n"
      "s_cmp_le_u32 s67, 4\n"
      "s_cbranch_scc1 SPIN_%=\n"
      "buffer_store_dword v127, v124, s[28:31], s74 offen sc1\n"
      "s_branch SPIN_%=\n"
      "H6_%=:\n"
      "s_ff1_i32_b64 s67, vcc\n"
      "v_sub_u32 v250, s66, v250\n"
      "s_nop 3\n"
      "v_readlane_b32 s76, v250, s67\n"
      "s_add_u32 s76, s76, 2960\n"
      "s_and_b32 s76, s76, 31\n"
      "s_lshl_b32 s76, s76, 3\n"
      "s_lshl_b32 s89, 4, s87\n"
      "s_branch H7_%=\n"
      "H3_%=:\n"
      "s_mul_i32 s76, s69, 110\n"
      "s_ashr_i32 s76, s76, 10\n"
      "s_min_i32 s76, s76, 40\n"
      "s_add_u32 s76, s76, 76\n"
      "s_cmp_lt_u32 s75, 24\n"
      "s_cselect_b32 s76, 56, s76\n"
      "s_cmp_lt_u32 s75, 13\n"
      "s_cselect_b32 s76, 40, s76\n"
      "s_cmp_lt_u32 s75, 5\n"
      "s_cselect_b32 s76, 24, s76\n"
      "s_add_u32 s67, s66, 2000\n"
      "s_and_b32 s67, s67, -32\n"
      "s_lshr_b32 s75, s76, 3\n"
      "s_or_b32 s67, s67, s75\n"
      "v_mov_b32 v127, s67\n"
      "s_add_u32 s74, s87, 2\n"
      "s_bfe_u32 s74, s74, 0x10001\n"
      "s_lshl_b32 s74, s74, 8\n"
      "s_add_u32 s74, s74, s39\n"
      "s_cmp_lt_u32 s39, 128\n"
      "s_cselect_b32 s89, 22, 20\n"
      "s_lshl_b32 s89, s89, s87\n"
      "s_sub_u32 s75, s39, 128\n"
      "s_cmp_lt_u32 s75, 32\n"
      "s_cbranch_scc0 H7_%=\n"
      "buffer_store_dword v127, v124, s[28:31], s74 offen sc1\n"
      "H7_%=:\n"
      "s_add_u32 s66, s66, s76\n"
      "s_mov_b32 s99, 17\n"
      "s_mov_b32 s100, 288\n"
      "s_add_u32 s93, s93, 99\n"
      "s_mov_b32 s86, 110\n"
      "s_mov_b32 s80, 0xc853212b\n"
      "s_mov_b32 s81, 0x842210\n"
      "s_cmp_le_u32 s76, 40\n"
      "s_cselect_b32 s75, 15, 0\n"
      "s_cmp_le_u32 s76, 24\n"
      "s_cselect_b32 s75, 25, s75\n"
      "s_lshr_b64 s[80:81], s[80:81], s75\n"
      "s_sub_u32 s67, s65, s66\n"
      "s_cmp_lt_i32 s67, 0\n"
      "s_cselect_b32 s88, s66, s65\n"
      "s_mov_b32 s101, 200\n"
      "s_branch SPIN_%=\n"
      "H4_%=:\n"
      "s_mov_b32 s91, 0\n"
      "s_mov_b32 s89, 0\n"
      "v_mov_b32 v251, 0x4000000\n"
      "s_branch SPIN_%=\n"
      "HS_%=:\n"
      "s_bitcmp1_b32 s87, 0\n"
      "s_cbranch_scc0 H5_%=\n"
      "v_xor_b32 v251, 0x100, v251\n"
      "s_lshr_b32 s67, s89, s87\n"
      "s_cmp_gt_u32 s67, 1\n"
      "s_cbranch_scc1 H8_%=\n"
      "s_add_u32 s67, s66, 400\n"
      "s_waitcnt vmcnt(8)\n"
      "v_sub_u32 v250, s67, v250\n"
      "v_cmp_gt_u32 vcc, 900, v250\n"
      "s_nop 3\n"
      "s_bcnt1_i32_b32 s74, vcc_lo\n"
      "s_bcnt1_i32_b32 s75, vcc_hi\n"
      "s_cmp_ge_u32 s75, 5\n"
      "s_cbranch_scc1 HT_%=\n"
      "s_cmp_gt_u32 s74, 2\n"
      "s_cbranch_scc1 H4_%=\n"
      "s_mov_b32 s99, 27\n"
      "s_mov_b32 s100, 285\n"
      "s_mov_b32 s86, 107\n"
      "s_mov_b64 s[80:81], 0\n"
      "s_branch H4_%=\n"
      "HT_%=:\n"
      "s_add_u32 s66, s66, 64\n"
      "s_mov_b32 s80, 0x86429909\n"
      "s_mov_b32 s81, 0x42110\n"
      "s_sub_u32 s67, s65, s66\n"
      "s_cmp_lt_i32 s67, 0\n"
      "s_cselect_b32 s88, s66, s65\n"
      "s_branch H4_%=\n"
      "H0_%=:\n"
      "buffer_load_dword v250, v251, s[28:31], 0 offen sc1\n"
      "s_branch SPIN_%=\n"
      "H9_%=:\n"
      "s_bitcmp1_b32 s87, 0\n"
      "s_cbranch_scc0 H5_%=\n"
      "v_xor_b32 v251, 0x100, v251\n"
      "s_cmp_eq_u32 s87, 5\n"
      "s_cbranch_scc1 H8_%=\n"
      "s_branch SPIN_%=\n"
      "H2_%=:\n"
      "s_cmp_gt_u32 s87, 2\n"
      "s_cbranch_scc1 H9_%=\n"
      "s_cmp_eq_u32 s87, 1\n"
      "s_cbranch_scc1 H0_%=\n"
      "s_waitcnt vmcnt(8)\n"
      "v_subrev_u32 v250, s71, v250\n"
      "v_min_u32 v250, 0x258, v250\n"
      "v_mbcnt_lo_u32_b32 v251, -1, 0\n"
      "v_mbcnt_hi_u32_b32 v251, -1, v251\n"
      "v_add_u32_dpp v250, v250, v250 row_shr:1 row_mask:0xf bank_mask:0xf bound_ctrl:0\n"
      "v_lshlrev_b32 v124, 23, v251\n"
      "s_nop 0\n"
      "v_add_u32_dpp v250, v250, v250 row_shr:2 row_mask:0xf bank_mask:0xf bound_ctrl:0\n"
      "v_lshlrev_b32 v251, 2, v251\n"
      "v_add_u32 v124, 0x6000, v124\n"
      "v_add_u32_dpp v250, v250, v250 row_shr:4 row_mask:0xf bank_mask:0xf bound_ctrl:0\n"
      "v_add_u32 v251, 0x6100, v251\n"
      "v_readlane_b32 s66, v250, 7\n"
      "s_nop 0\n"
      "s_lshr_b32 s66, s66, 3\n"
      "s_add_u32 s66, s66, s71\n"
      "s_add_u32 s66, s66, s38\n"
      "s_add_u32 s71, s66, 550\n"
      "s_sub_u32 s67, s65, s66\n"
      "s_cmp_lt_i32 s67, 0\n"
      "s_cselect_b32 s88, s66, s65\n"
      "s_branch SPIN_%=\n"
      "FB0_%=:\n"
      "s_nop 7\n"
      "s_nop 7\n"
      "v_xor_b32 v118, 0x0, v242\n"
      "v_xor_b32 v119, 0x0, v243\n"
      "ds_read_b128 a[64:67], v118 offset:0\n"
      "ds_read_b128 a[68:71], v118 offset:4096\n"
      "ds_read_b128 a[72:75], v119 offset:0\n"
      "ds_read_b128 a[76:79], v119 offset:4096\n"
      "ds_read_b128 a[80:83], v118 offset:1024\n"
      "ds_read_b128 a[84:87], v118 offset:5120\n"
      "ds_read_b128 a[88:91], v119 offset:1024\n"
      "ds_read_b128 a[92:95], v119 offset:5120\n"
      "s_waitcnt lgkmcnt(0)\n"
      "v_mfma_f32_16x16x32_bf16 v[128:131], a[64:67], a[0:3], 0\n"
      "v_mfma_f32_16x16x32_bf16 v[136:139], a[64:67], a[4:7], 0\n"
      "v_mfma_f32_16x16x32_bf16 v[144:147], a[64:67], a[8:11], 0\n"
      "v_mfma_f32_16x16x32_bf16 v[152:155], a[64:67], a[12:15], 0\n"
      "v_mfma_f32_16x16x32_bf16 v[132:135], a[68:71], a[0:3], 0\n"
      "v_mfma_f32_16x16x32_bf16 v[140:143], a[68:71], a[4:7], 0\n"
      "v_mfma_f32_16x16x32_bf16 v[148:151], a[68:71], a[8:11], 0\n"
      "v_mfma_f32_16x16x32_bf16 v[156:159], a[68:71], a[12:15], 0\n"
      "v_mfma_f32_16x16x32_bf16 v[128:131], a[72:75], a[16:19], v[128:131]\n"
      "v_mfma_f32_16x16x32_bf16 v[136:139], a[72:75], a[20:23], v[136:139]\n"
      "v_mfma_f32_16x16x32_bf16 v[144:147], a[72:75], a[24:27], v[144:147]\n"
      "v_mfma_f32_16x16x32_bf16 v[152:155], a[72:75], a[28:31], v[152:155]\n"
      "v_mfma_f32_16x16x32_bf16 v[132:135], a[76:79], a[16:19], v[132:135]\n"
      "v_mfma_f32_16x16x32_bf16 v[140:143], a[76:79], a[20:23], v[140:143]\n"
      "v_mfma_f32_16x16x32_bf16 v[148:151], a[76:79], a[24:27], v[148:151]\n"
      "v_mfma_f32_16x16x32_bf16 v[156:159], a[76:79], a[28:31], v[156:159]\n"
      "v_mfma_f32_16x16x32_bf16 v[128:131], a[80:83], a[32:35], v[128:131]\n"
      "v_mfma_f32_16x16x32_bf16 v[136:139], a[80:83], a[36:39], v[136:139]\n"
      "v_mfma_f32_16x16x32_bf16 v[144:147], a[80:83], a[40:43], v[144:147]\n"
      "v_mfma_f32_16x16x32_bf16 v[152:155], a[80:83], a[44:47], v[152:155]\n"
      "v_mfma_f32_16x16x32_bf16 v[132:135], a[84:87], a[32:35], v[132:135]\n"
      "v_mfma_f32_16x16x32_bf16 v[140:143], a[84:87], a[36:39], v[140:143]\n"
      "v_mfma_f32_16x16x32_bf16 v[148:151], a[84:87], a[40:43], v[148:151]\n"
      "v_mfma_f32_16x16x32_bf16 v[156:159], a[84:87], a[44:47], v[156:159]\n"
      "v_mfma_f32_16x16x32_bf16 v[128:131], a[88:91], a[48:51], v[128:131]\n"
      "v_mfma_f32_16x16x32_bf16 v[136:139], a[88:91], a[52:55], v[136:139]\n"
      "v_mfma_f32_16x16x32_bf16 v[144:147], a[88:91], a[56:59], v[144:147]\n"
      "v_mfma_f32_16x16x32_bf16 v[152:155], a[88:91], a[60:63], v[152:155]\n"
      "v_mfma_f32_16x16x32_bf16 v[132:135], a[92:95], a[48:51], v[132:135]\n"
      "v_mfma_f32_16x16x32_bf16 v[140:143], a[92:95], a[52:55], v[140:143]\n"
      "v_mfma_f32_16x16x32_bf16 v[148:151], a[92:95], a[56:59], v[148:151]\n"
      "v_mfma_f32_16x16x32_bf16 v[156:159], a[92:95], a[60:63], v[156:159]\n"
      "s_nop 7\n"
      "v_max3_f32 v112, v128, v129, v130\n"
      "v_max3_f32 v113, v136, v137, v138\n"
      "v_max3_f32 v114, v144, v145, v146\n"
      "v_max3_f32 v115, v152, v153, v154\n"
      "v_max3_f32 v112, v112, v131, v132\n"
      "v_max3_f32 v113, v113, v139, v140\n"
      "v_max3_f32 v114, v114, v147, v148\n"
      "v_max3_f32 v115, v115, v155, v156\n"
      "v_max3_f32 v112, v112, v133, v134\n"
      "v_max3_f32 v113, v113, v141, v142\n"
      "v_max3_f32 v114, v114, v149, v150\n"
      "v_max3_f32 v115, v115, v157, v158\n"
      "v_max_f32 v112, v112, v135\n"
      "v_max_f32 v113, v113, v143\n"
      "v_max_f32 v114, v114, v151\n"
      "v_max_f32 v115, v115, v159\n"
      "v_mov_b32 v160, v112\n"
      "v_mov_b32 v161, v113\n"
      "v_mov_b32 v162, v114\n"
      "v_mov_b32 v163, v115\n"
      "v_permlane16_swap_b32 v112, v160\n"
      "v_permlane16_swap_b32 v113, v161\n"
      "v_permlane16_swap_b32 v114, v162\n"
      "v_permlane16_swap_b32 v115, v163\n"
      "v_max_f32 v112, v112, v160\n"
      "v_max_f32 v113, v113, v161\n"
      "v_max_f32 v114, v114, v162\n"
      "v_max_f32 v115, v115, v163\n"
      "v_mov_b32 v160, v112\n"
      "v_mov_b32 v161, v113\n"
      "v_mov_b32 v162, v114\n"
      "v_mov_b32 v163, v115\n"
      "v_permlane32_swap_b32 v112, v160\n"
      "v_permlane32_swap_b32 v113, v161\n"
      "v_permlane32_swap_b32 v114, v162\n"
      "v_permlane32_swap_b32 v115, v163\n"
      "v_max_f32 v112, v112, v160\n"
      "v_max_f32 v113, v113, v161\n"
      "v_max_f32 v114, v114, v162\n"
      "v_max_f32 v115, v115, v163\n"
      "v_sub_f32 v112, 0xc1800000, v112\n"
      "v_sub_f32 v113, 0xc1800000, v113\n"
      "v_sub_f32 v114, 0xc1800000, v114\n"
      "v_sub_f32 v115, 0xc1800000, v115\n"
      "v_floor_f32 v112, v112\n"
      "v_floor_f32 v113, v113\n"
      "v_floor_f32 v114, v114\n"
      "v_floor_f32 v115, v115\n"
      "v_min_f32 v112, v112, v224\n"
      "v_min_f32 v113, v113, v228\n"
      "v_min_f32 v114, v114, v232\n"
      "v_min_f32 v115, v115, v236\n"
      "v_sub_f32 v164, v112, v224\n"
      "v_sub_f32 v165, v113, v228\n"
      "v_sub_f32 v166, v114, v232\n"
      "v_sub_f32 v167, v115, v236\n"
      "v_mov_b32 v224, v112\n"
      "v_mov_b32 v228, v113\n"
      "v_mov_b32 v232, v114\n"
      "v_mov_b32 v236, v115\n"
      "v_exp_f32 v112, v164\n"
      "v_exp_f32 v113, v165\n"
      "v_exp_f32 v114, v166\n"
      "v_exp_f32 v115, v167\n"
      "v_mov_b32 v225, v224\n"
      "v_mov_b32 v229, v228\n"
      "v_mov_b32 v233, v232\n"
      "v_mov_b32 v237, v236\n"
      "v_mov_b64 v[226:227], v[224:225]\n"
      "v_mov_b64 v[230:231], v[228:229]\n"
      "v_mov_b64 v[234:235], v[232:233]\n"
      "v_mov_b64 v[238:239], v[236:237]\n"
      "s_call_b64 s[84:85], SCALE_%=\n"
      "v_pk_add_f32 v[128:129], v[128:129], v[224:225]\n"
      "v_pk_add_f32 v[130:131], v[130:131], v[224:225]\n"
      "v_pk_add_f32 v[132:133], v[132:133], v[224:225]\n"
      "v_pk_add_f32 v[134:135], v[134:135], v[224:225]\n"
      "v_pk_add_f32 v[136:137], v[136:137], v[228:229]\n"
      "v_pk_add_f32 v[138:139], v[138:139], v[228:229]\n"
      "v_pk_add_f32 v[140:141], v[140:141], v[228:229]\n"
      "v_pk_add_f32 v[142:143], v[142:143], v[228:229]\n"
      "v_pk_add_f32 v[144:145], v[144:145], v[232:233]\n"
      "v_pk_add_f32 v[146:147], v[146:147], v[232:233]\n"
      "v_pk_add_f32 v[148:149], v[148:149], v[232:233]\n"
      "v_pk_add_f32 v[150:151], v[150:151], v[232:233]\n"
      "v_pk_add_f32 v[152:153], v[152:153], v[236:237]\n"
      "v_pk_add_f32 v[154:155], v[154:155], v[236:237]\n"
      "v_pk_add_f32 v[156:157], v[156:157], v[236:237]\n"
      "v_pk_add_f32 v[158:159], v[158:159], v[236:237]\n"
      "v_exp_f32 v112, v128\n"
      "v_exp_f32 v113, v129\n"
      "v_exp_f32 v114, v130\n"
      "v_cvt_pk_bf16_f32 v128, v112, v113\n"
      "v_exp_f32 v115, v131\n"
      "v_exp_f32 v112, v132\n"
      "v_cvt_pk_bf16_f32 v129, v114, v115\n"
      "v_exp_f32 v113, v133\n"
      "v_exp_f32 v114, v134\n"
      "v_cvt_pk_bf16_f32 v130, v112, v113\n"
      "v_exp_f32 v115, v135\n"
      "v_exp_f32 v112, v136\n"
      "v_cvt_pk_bf16_f32 v131, v114, v115\n"
      "v_exp_f32 v113, v137\n"
      "v_exp_f32 v114, v138\n"
      "v_cvt_pk_bf16_f32 v132, v112, v113\n"
      "v_exp_f32 v115, v139\n"
      "v_exp_f32 v112, v140\n"
      "v_cvt_pk_bf16_f32 v133, v114, v115\n"
      "v_exp_f32 v113, v141\n"
      "v_exp_f32 v114, v142\n"
      "v_cvt_pk_bf16_f32 v134, v112, v113\n"
      "v_exp_f32 v115, v143\n"
      "v_exp_f32 v112, v144\n"
      "v_cvt_pk_bf16_f32 v135, v114, v115\n"
      "v_exp_f32 v113, v145\n"
      "v_exp_f32 v114, v146\n"
      "v_cvt_pk_bf16_f32 v136, v112, v113\n"
      "v_exp_f32 v115, v147\n"
      "v_exp_f32 v112, v148\n"
      "v_cvt_pk_bf16_f32 v137, v114, v115\n"
      "v_exp_f32 v113, v149\n"
      "v_exp_f32 v114, v150\n"
      "v_cvt_pk_bf16_f32 v138, v112, v113\n"
      "v_exp_f32 v115, v151\n"
      "v_exp_f32 v112, v152\n"
      "v_cvt_pk_bf16_f32 v139, v114, v115\n"
      "v_exp_f32 v113, v153\n"
      "v_exp_f32 v114, v154\n"
      "v_cvt_pk_bf16_f32 v140, v112, v113\n"
      "v_exp_f32 v115, v155\n"
      "v_exp_f32 v112, v156\n"
      "v_cvt_pk_bf16_f32 v141, v114, v115\n"
      "v_exp_f32 v113, v157\n"
      "v_exp_f32 v114, v158\n"
      "v_cvt_pk_bf16_f32 v142, v112, v113\n"
      "v_exp_f32 v115, v159\n"
      "s_nop 0\n"
      "v_cvt_pk_bf16_f32 v143, v114, v115\n"
      "ds_read_b128 a[64:67], v242 offset:32768\n"
      "ds_read_b128 a[68:71], v242 offset:36864\n"
      "ds_read_b128 a[72:75], v243 offset:32768\n"
      "ds_read_b128 a[76:79], v243 offset:36864\n"
      "ds_read_b128 a[80:83], v242 offset:33792\n"
      "ds_read_b128 a[84:87], v242 offset:37888\n"
      "ds_read_b128 a[88:91], v243 offset:33792\n"
      "ds_read_b128 a[92:95], v243 offset:37888\n"
      "s_waitcnt lgkmcnt(0)\n"
      "v_mfma_f32_16x16x32_bf16 v[160:163], a[64:67], a[0:3], v[224:227]\n"
      "v_mfma_f32_16x16x32_bf16 v[168:171], a[64:67], a[4:7], v[228:231]\n"
      "v_mfma_f32_16x16x32_bf16 v[176:179], a[64:67], a[8:11], v[232:235]\n"
      "v_mfma_f32_16x16x32_bf16 v[184:187], a[64:67], a[12:15], v[236:239]\n"
      "v_mfma_f32_16x16x32_bf16 v[164:167], a[68:71], a[0:3], v[224:227]\n"
      "v_mfma_f32_16x16x32_bf16 v[172:175], a[68:71], a[4:7], v[228:231]\n"
      "v_mfma_f32_16x16x32_bf16 v[180:183], a[68:71], a[8:11], v[232:235]\n"
      "v_mfma_f32_16x16x32_bf16 v[188:191], a[68:71], a[12:15], v[236:239]\n"
      "v_mfma_f32_16x16x32_bf16 v[160:163], a[72:75], a[16:19], v[160:163]\n"
      "v_mfma_f32_16x16x32_bf16 v[168:171], a[72:75], a[20:23], v[168:171]\n"
      "v_mfma_f32_16x16x32_bf16 v[176:179], a[72:75], a[24:27], v[176:179]\n"
      "v_mfma_f32_16x16x32_bf16 v[184:187], a[72:75], a[28:31], v[184:187]\n"
      "v_mfma_f32_16x16x32_bf16 v[164:167], a[76:79], a[16:19], v[164:167]\n"
      "v_mfma_f32_16x16x32_bf16 v[172:175], a[76:79], a[20:23], v[172:175]\n"
      "v_mfma_f32_16x16x32_bf16 v[180:183], a[76:79], a[24:27], v[180:183]\n"
      "v_mfma_f32_16x16x32_bf16 v[188:191], a[76:79], a[28:31], v[188:191]\n"
      "v_mfma_f32_16x16x32_bf16 v[160:163], a[80:83], a[32:35], v[160:163]\n"
      "v_mfma_f32_16x16x32_bf16 v[168:171], a[80:83], a[36:39], v[168:171]\n"
      "v_mfma_f32_16x16x32_bf16 v[176:179], a[80:83], a[40:43], v[176:179]\n"
      "v_mfma_f32_16x16x32_bf16 v[184:187], a[80:83], a[44:47], v[184:187]\n"
      "v_mfma_f32_16x16x32_bf16 v[164:167], a[84:87], a[32:35], v[164:167]\n"
      "v_mfma_f32_16x16x32_bf16 v[172:175], a[84:87], a[36:39], v[172:175]\n"
      "v_mfma_f32_16x16x32_bf16 v[180:183], a[84:87], a[40:43], v[180:183]\n"
      "v_mfma_f32_16x16x32_bf16 v[188:191], a[84:87], a[44:47], v[188:191]\n"
      "v_mfma_f32_16x16x32_bf16 v[160:163], a[88:91], a[48:51], v[160:163]\n"
      "v_mfma_f32_16x16x32_bf16 v[168:171], a[88:91], a[52:55], v[168:171]\n"
      "v_mfma_f32_16x16x32_bf16 v[176:179], a[88:91], a[56:59], v[176:179]\n"
      "v_mfma_f32_16x16x32_bf16 v[184:187], a[88:91], a[60:63], v[184:187]\n"
      "v_mfma_f32_16x16x32_bf16 v[164:167], a[92:95], a[48:51], v[164:167]\n"
      "v_mfma_f32_16x16x32_bf16 v[172:175], a[92:95], a[52:55], v[172:175]\n"
      "v_mfma_f32_16x16x32_bf16 v[180:183], a[92:95], a[56:59], v[180:183]\n"
      "v_mfma_f32_16x16x32_bf16 v[188:191], a[92:95], a[60:63], v[188:191]\n"
      "s_cmp_eq_u32 s64, 0\n"
      "s_cbranch_scc0 NRET0_0_%=\n"
      "v_mov_b64 v[192:193], v[128:129]\n"
      "v_mov_b64 v[194:195], v[130:131]\n"
      "v_mov_b64 v[196:197], v[132:133]\n"
      "v_mov_b64 v[198:199], v[134:135]\n"
      "v_mov_b64 v[200:201], v[136:137]\n"
      "v_mov_b64 v[202:203], v[138:139]\n"
      "v_mov_b64 v[204:205], v[140:141]\n"
      "v_mov_b64 v[206:207], v[142:143]\n"
      "s_nop 1\n"
      "s_branch RET0_0_%=\n"
      "NRET0_0_%=:\n"
      "s_cmp_eq_u32 s64, 1\n"
      "s_cbranch_scc0 NRET0_1_%=\n"
      "v_mov_b64 v[192:193], v[128:129]\n"
      "v_mov_b64 v[194:195], v[130:131]\n"
      "v_mov_b64 v[196:197], v[132:133]\n"
      "v_mov_b64 v[198:199], v[134:135]\n"
      "v_mov_b64 v[200:201], v[136:137]\n"
      "v_mov_b64 v[202:203], v[138:139]\n"
      "v_mov_b64 v[204:205], v[140:141]\n"
      "v_mov_b64 v[206:207], v[142:143]\n"
      "s_nop 1\n"
      "s_branch RET0_1_%=\n"
      "NRET0_1_%=:\n"
      "s_cmp_eq_u32 s64, 2\n"
      "s_cbranch_scc0 NRET0_2_%=\n"
      "v_mov_b64 v[192:193], v[128:129]\n"
      "v_mov_b64 v[194:195], v[130:131]\n"
      "v_mov_b64 v[196:197], v[132:133]\n"
      "v_mov_b64 v[198:199], v[134:135]\n"
      "v_mov_b64 v[200:201], v[136:137]\n"
      "v_mov_b64 v[202:203], v[138:139]\n"
      "v_mov_b64 v[204:205], v[140:141]\n"
      "v_mov_b64 v[206:207], v[142:143]\n"
      "s_nop 1\n"
      "s_branch RET0_2_%=\n"
      "NRET0_2_%=:\n"
      "s_cmp_eq_u32 s64, 3\n"
      "s_cbranch_scc0 NRET0_3_%=\n"
      "v_mov_b64 v[192:193], v[128:129]\n"
      "v_mov_b64 v[194:195], v[130:131]\n"
      "v_mov_b64 v[196:197], v[132:133]\n"
      "v_mov_b64 v[198:199], v[134:135]\n"
      "v_mov_b64 v[200:201], v[136:137]\n"
      "v_mov_b64 v[202:203], v[138:139]\n"
      "v_mov_b64 v[204:205], v[140:141]\n"
      "v_mov_b64 v[206:207], v[142:143]\n"
      "s_nop 1\n"
      "s_branch RET0_3_%=\n"
      "NRET0_3_%=:\n"
      "s_cmp_eq_u32 s64, 4\n"
      "s_cbranch_scc0 NRET0_4_%=\n"
      "v_mov_b64 v[192:193], v[128:129]\n"
      "v_mov_b64 v[194:195], v[130:131]\n"
      "v_mov_b64 v[196:197], v[132:133]\n"
      "v_mov_b64 v[198:199], v[134:135]\n"
      "v_mov_b64 v[200:201], v[136:137]\n"
      "v_mov_b64 v[202:203], v[138:139]\n"
      "v_mov_b64 v[204:205], v[140:141]\n"
      "v_mov_b64 v[206:207], v[142:143]\n"
      "s_nop 1\n"
      "s_branch RET0_4_%=\n"
      "NRET0_4_%=:\n"
      "s_cmp_eq_u32 s64, 5\n"
      "s_cbranch_scc0 NRET0_5_%=\n"
      "v_mov_b64 v[16:17], v[128:129]\n"
      "v_mov_b64 v[18:19], v[130:131]\n"
      "v_mov_b64 v[20:21], v[132:133]\n"
      "v_mov_b64 v[22:23], v[134:135]\n"
      "v_mov_b64 v[24:25], v[136:137]\n"
      "v_mov_b64 v[26:27], v[138:139]\n"
      "v_mov_b64 v[28:29], v[140:141]\n"
      "v_mov_b64 v[30:31], v[142:143]\n"
      "s_nop 1\n"
      "s_branch RET0_5_%=\n"
      "NRET0_5_%=:\n"
      "FB1_%=:\n"
      "s_add_u32 s84, s63, 1\n"
      "s_sub_u32 s84, s84, s40\n"
      "s_or_b32 s84, s84, s64\n"
      "s_cbranch_scc0 RET1_0_%=\n"
      "s_nop 7\n"
      "s_nop 7\n"
      "v_xor_b32 v118, 0x10000, v242\n"
      "v_xor_b32 v119, 0x10000, v243\n"
      "ds_read_b128 a[64:67], v118 offset:32768\n"
      "ds_read_b128 a[68:71], v118 offset:36864\n"
      "ds_read_b128 a[72:75], v119 offset:32768\n"
      "ds_read_b128 a[76:79], v119 offset:36864\n"
      "ds_read_b128 a[80:83], v118 offset:33792\n"
      "ds_read_b128 a[84:87], v118 offset:37888\n"
      "ds_read_b128 a[88:91], v119 offset:33792\n"
      "ds_read_b128 a[92:95], v119 offset:37888\n"
      "s_waitcnt lgkmcnt(0)\n"
      "v_mfma_f32_16x16x32_bf16 v[160:163], a[64:67], a[0:3], 0\n"
      "v_mfma_f32_16x16x32_bf16 v[168:171], a[64:67], a[4:7], 0\n"
      "v_mfma_f32_16x16x32_bf16 v[176:179], a[64:67], a[8:11], 0\n"
      "v_mfma_f32_16x16x32_bf16 v[184:187], a[64:67], a[12:15], 0\n"
      "v_mfma_f32_16x16x32_bf16 v[164:167], a[68:71], a[0:3], 0\n"
      "v_mfma_f32_16x16x32_bf16 v[172:175], a[68:71], a[4:7], 0\n"
      "v_mfma_f32_16x16x32_bf16 v[180:183], a[68:71], a[8:11], 0\n"
      "v_mfma_f32_16x16x32_bf16 v[188:191], a[68:71], a[12:15], 0\n"
      "v_mfma_f32_16x16x32_bf16 v[160:163], a[72:75], a[16:19], v[160:163]\n"
      "v_mfma_f32_16x16x32_bf16 v[168:171], a[72:75], a[20:23], v[168:171]\n"
      "v_mfma_f32_16x16x32_bf16 v[176:179], a[72:75], a[24:27], v[176:179]\n"
      "v_mfma_f32_16x16x32_bf16 v[184:187], a[72:75], a[28:31], v[184:187]\n"
      "v_mfma_f32_16x16x32_bf16 v[164:167], a[76:79], a[16:19], v[164:167]\n"
      "v_mfma_f32_16x16x32_bf16 v[172:175], a[76:79], a[20:23], v[172:175]\n"
      "v_mfma_f32_16x16x32_bf16 v[180:183], a[76:79], a[24:27], v[180:183]\n"
      "v_mfma_f32_16x16x32_bf16 v[188:191], a[76:79], a[28:31], v[188:191]\n"
      "v_mfma_f32_16x16x32_bf16 v[160:163], a[80:83], a[32:35], v[160:163]\n"
      "v_mfma_f32_16x16x32_bf16 v[168:171], a[80:83], a[36:39], v[168:171]\n"
      "v_mfma_f32_16x16x32_bf16 v[176:179], a[80:83], a[40:43], v[176:179]\n"
      "v_mfma_f32_16x16x32_bf16 v[184:187], a[80:83], a[44:47], v[184:187]\n"
      "v_mfma_f32_16x16x32_bf16 v[164:167], a[84:87], a[32:35], v[164:167]\n"
      "v_mfma_f32_16x16x32_bf16 v[172:175], a[84:87], a[36:39], v[172:175]\n"
      "v_mfma_f32_16x16x32_bf16 v[180:183], a[84:87], a[40:43], v[180:183]\n"
      "v_mfma_f32_16x16x32_bf16 v[188:191], a[84:87], a[44:47], v[188:191]\n"
      "v_mfma_f32_16x16x32_bf16 v[160:163], a[88:91], a[48:51], v[160:163]\n"
      "v_mfma_f32_16x16x32_bf16 v[168:171], a[88:91], a[52:55], v[168:171]\n"
      "v_mfma_f32_16x16x32_bf16 v[176:179], a[88:91], a[56:59], v[176:179]\n"
      "v_mfma_f32_16x16x32_bf16 v[184:187], a[88:91], a[60:63], v[184:187]\n"
      "v_mfma_f32_16x16x32_bf16 v[164:167], a[92:95], a[48:51], v[164:167]\n"
      "v_mfma_f32_16x16x32_bf16 v[172:175], a[92:95], a[52:55], v[172:175]\n"
      "v_mfma_f32_16x16x32_bf16 v[180:183], a[92:95], a[56:59], v[180:183]\n"
      "v_mfma_f32_16x16x32_bf16 v[188:191], a[92:95], a[60:63], v[188:191]\n"
      "s_nop 7\n"
      "v_max3_f32 v112, v160, v161, v162\n"
      "v_max3_f32 v113, v168, v169, v170\n"
      "v_max3_f32 v114, v176, v177, v178\n"
      "v_max3_f32 v115, v184, v185, v186\n"
      "v_max3_f32 v112, v112, v163, v164\n"
      "v_max3_f32 v113, v113, v171, v172\n"
      "v_max3_f32 v114, v114, v179, v180\n"
      "v_max3_f32 v115, v115, v187, v188\n"
      "v_max3_f32 v112, v112, v165, v166\n"
      "v_max3_f32 v113, v113, v173, v174\n"
      "v_max3_f32 v114, v114, v181, v182\n"
      "v_max3_f32 v115, v115, v189, v190\n"
      "v_max_f32 v112, v112, v167\n"
      "v_max_f32 v113, v113, v175\n"
      "v_max_f32 v114, v114, v183\n"
      "v_max_f32 v115, v115, v191\n"
      "v_mov_b32 v128, v112\n"
      "v_mov_b32 v129, v113\n"
      "v_mov_b32 v130, v114\n"
      "v_mov_b32 v131, v115\n"
      "v_permlane16_swap_b32 v112, v128\n"
      "v_permlane16_swap_b32 v113, v129\n"
      "v_permlane16_swap_b32 v114, v130\n"
      "v_permlane16_swap_b32 v115, v131\n"
      "v_max_f32 v112, v112, v128\n"
      "v_max_f32 v113, v113, v129\n"
      "v_max_f32 v114, v114, v130\n"
      "v_max_f32 v115, v115, v131\n"
      "v_mov_b32 v128, v112\n"
      "v_mov_b32 v129, v113\n"
      "v_mov_b32 v130, v114\n"
      "v_mov_b32 v131, v115\n"
      "v_permlane32_swap_b32 v112, v128\n"
      "v_permlane32_swap_b32 v113, v129\n"
      "v_permlane32_swap_b32 v114, v130\n"
      "v_permlane32_swap_b32 v115, v131\n"
      "v_max_f32 v112, v112, v128\n"
      "v_max_f32 v113, v113, v129\n"
      "v_max_f32 v114, v114, v130\n"
      "v_max_f32 v115, v115, v131\n"
      "v_sub_f32 v112, 0xc1800000, v112\n"
      "v_sub_f32 v113, 0xc1800000, v113\n"
      "v_sub_f32 v114, 0xc1800000, v114\n"
      "v_sub_f32 v115, 0xc1800000, v115\n"
      "v_floor_f32 v112, v112\n"
      "v_floor_f32 v113, v113\n"
      "v_floor_f32 v114, v114\n"
      "v_floor_f32 v115, v115\n"
      "v_min_f32 v112, v112, v224\n"
      "v_min_f32 v113, v113, v228\n"
      "v_min_f32 v114, v114, v232\n"
      "v_min_f32 v115, v115, v236\n"
      "v_sub_f32 v132, v112, v224\n"
      "v_sub_f32 v133, v113, v228\n"
      "v_sub_f32 v134, v114, v232\n"
      "v_sub_f32 v135, v115, v236\n"
      "v_mov_b32 v224, v112\n"
      "v_mov_b32 v228, v113\n"
      "v_mov_b32 v232, v114\n"
      "v_mov_b32 v236, v115\n"
      "v_exp_f32 v112, v132\n"
      "v_exp_f32 v113, v133\n"
      "v_exp_f32 v114, v134\n"
      "v_exp_f32 v115, v135\n"
      "v_mov_b32 v225, v224\n"
      "v_mov_b32 v229, v228\n"
      "v_mov_b32 v233, v232\n"
      "v_mov_b32 v237, v236\n"
      "v_mov_b64 v[226:227], v[224:225]\n"
      "v_mov_b64 v[230:231], v[228:229]\n"
      "v_mov_b64 v[234:235], v[232:233]\n"
      "v_mov_b64 v[238:239], v[236:237]\n"
      "s_call_b64 s[84:85], SCALE_%=\n"
      "v_pk_add_f32 v[160:161], v[160:161], v[224:225]\n"
      "v_pk_add_f32 v[162:163], v[162:163], v[224:225]\n"
      "v_pk_add_f32 v[164:165], v[164:165], v[224:225]\n"
      "v_pk_add_f32 v[166:167], v[166:167], v[224:225]\n"
      "v_pk_add_f32 v[168:169], v[168:169], v[228:229]\n"
      "v_pk_add_f32 v[170:171], v[170:171], v[228:229]\n"
      "v_pk_add_f32 v[172:173], v[172:173], v[228:229]\n"
      "v_pk_add_f32 v[174:175], v[174:175], v[228:229]\n"
      "v_pk_add_f32 v[176:177], v[176:177], v[232:233]\n"
      "v_pk_add_f32 v[178:179], v[178:179], v[232:233]\n"
      "v_pk_add_f32 v[180:181], v[180:181], v[232:233]\n"
      "v_pk_add_f32 v[182:183], v[182:183], v[232:233]\n"
      "v_pk_add_f32 v[184:185], v[184:185], v[236:237]\n"
      "v_pk_add_f32 v[186:187], v[186:187], v[236:237]\n"
      "v_pk_add_f32 v[188:189], v[188:189], v[236:237]\n"
      "v_pk_add_f32 v[190:191], v[190:191], v[236:237]\n"
      "v_exp_f32 v112, v160\n"
      "v_exp_f32 v113, v161\n"
      "v_exp_f32 v114, v162\n"
      "v_cvt_pk_bf16_f32 v160, v112, v113\n"
      "v_exp_f32 v115, v163\n"
      "v_exp_f32 v112, v164\n"
      "v_cvt_pk_bf16_f32 v161, v114, v115\n"
      "v_exp_f32 v113, v165\n"
      "v_exp_f32 v114, v166\n"
      "v_cvt_pk_bf16_f32 v162, v112, v113\n"
      "v_exp_f32 v115, v167\n"
      "v_exp_f32 v112, v168\n"
      "v_cvt_pk_bf16_f32 v163, v114, v115\n"
      "v_exp_f32 v113, v169\n"
      "v_exp_f32 v114, v170\n"
      "v_cvt_pk_bf16_f32 v164, v112, v113\n"
      "v_exp_f32 v115, v171\n"
      "v_exp_f32 v112, v172\n"
      "v_cvt_pk_bf16_f32 v165, v114, v115\n"
      "v_exp_f32 v113, v173\n"
      "v_exp_f32 v114, v174\n"
      "v_cvt_pk_bf16_f32 v166, v112, v113\n"
      "v_exp_f32 v115, v175\n"
      "v_exp_f32 v112, v176\n"
      "v_cvt_pk_bf16_f32 v167, v114, v115\n"
      "v_exp_f32 v113, v177\n"
      "v_exp_f32 v114, v178\n"
      "v_cvt_pk_bf16_f32 v168, v112, v113\n"
      "v_exp_f32 v115, v179\n"
      "v_exp_f32 v112, v180\n"
      "v_cvt_pk_bf16_f32 v169, v114, v115\n"
      "v_exp_f32 v113, v181\n"
      "v_exp_f32 v114, v182\n"
      "v_cvt_pk_bf16_f32 v170, v112, v113\n"
      "v_exp_f32 v115, v183\n"
      "v_exp_f32 v112, v184\n"
      "v_cvt_pk_bf16_f32 v171, v114, v115\n"
      "v_exp_f32 v113, v185\n"
      "v_exp_f32 v114, v186\n"
      "v_cvt_pk_bf16_f32 v172, v112, v113\n"
      "v_exp_f32 v115, v187\n"
      "v_exp_f32 v112, v188\n"
      "v_cvt_pk_bf16_f32 v173, v114, v115\n"
      "v_exp_f32 v113, v189\n"
      "v_exp_f32 v114, v190\n"
      "v_cvt_pk_bf16_f32 v174, v112, v113\n"
      "v_exp_f32 v115, v191\n"
      "s_nop 0\n"
      "v_cvt_pk_bf16_f32 v175, v114, v115\n"
      "ds_read_b128 a[64:67], v242 offset:0\n"
      "ds_read_b128 a[68:71], v242 offset:4096\n"
      "ds_read_b128 a[72:75], v243 offset:0\n"
      "ds_read_b128 a[76:79], v243 offset:4096\n"
      "ds_read_b128 a[80:83], v242 offset:1024\n"
      "ds_read_b128 a[84:87], v242 offset:5120\n"
      "ds_read_b128 a[88:91], v243 offset:1024\n"
      "ds_read_b128 a[92:95], v243 offset:5120\n"
      "s_waitcnt lgkmcnt(0)\n"
      "v_mfma_f32_16x16x32_bf16 v[128:131], a[64:67], a[0:3], v[224:227]\n"
      "v_mfma_f32_16x16x32_bf16 v[136:139], a[64:67], a[4:7], v[228:231]\n"
      "v_mfma_f32_16x16x32_bf16 v[144:147], a[64:67], a[8:11], v[232:235]\n"
      "v_mfma_f32_16x16x32_bf16 v[152:155], a[64:67], a[12:15], v[236:239]\n"
      "v_mfma_f32_16x16x32_bf16 v[132:135], a[68:71], a[0:3], v[224:227]\n"
      "v_mfma_f32_16x16x32_bf16 v[140:143], a[68:71], a[4:7], v[228:231]\n"
      "v_mfma_f32_16x16x32_bf16 v[148:151], a[68:71], a[8:11], v[232:235]\n"
      "v_mfma_f32_16x16x32_bf16 v[156:159], a[68:71], a[12:15], v[236:239]\n"
      "v_mfma_f32_16x16x32_bf16 v[128:131], a[72:75], a[16:19], v[128:131]\n"
      "v_mfma_f32_16x16x32_bf16 v[136:139], a[72:75], a[20:23], v[136:139]\n"
      "v_mfma_f32_16x16x32_bf16 v[144:147], a[72:75], a[24:27], v[144:147]\n"
      "v_mfma_f32_16x16x32_bf16 v[152:155], a[72:75], a[28:31], v[152:155]\n"
      "v_mfma_f32_16x16x32_bf16 v[132:135], a[76:79], a[16:19], v[132:135]\n"
      "v_mfma_f32_16x16x32_bf16 v[140:143], a[76:79], a[20:23], v[140:143]\n"
      "v_mfma_f32_16x16x32_bf16 v[148:151], a[76:79], a[24:27], v[148:151]\n"
      "v_mfma_f32_16x16x32_bf16 v[156:159], a[76:79], a[28:31], v[156:159]\n"
      "v_mfma_f32_16x16x32_bf16 v[128:131], a[80:83], a[32:35], v[128:131]\n"
      "v_mfma_f32_16x16x32_bf16 v[136:139], a[80:83], a[36:39], v[136:139]\n"
      "v_mfma_f32_16x16x32_bf16 v[144:147], a[80:83], a[40:43], v[144:147]\n"
      "v_mfma_f32_16x16x32_bf16 v[152:155], a[80:83], a[44:47], v[152:155]\n"
      "v_mfma_f32_16x16x32_bf16 v[132:135], a[84:87], a[32:35], v[132:135]\n"
      "v_mfma_f32_16x16x32_bf16 v[140:143], a[84:87], a[36:39], v[140:143]\n"
      "v_mfma_f32_16x16x32_bf16 v[148:151], a[84:87], a[40:43], v[148:151]\n"
      "v_mfma_f32_16x16x32_bf16 v[156:159], a[84:87], a[44:47], v[156:159]\n"
      "v_mfma_f32_16x16x32_bf16 v[128:131], a[88:91], a[48:51], v[128:131]\n"
      "v_mfma_f32_16x16x32_bf16 v[136:139], a[88:91], a[52:55], v[136:139]\n"
      "v_mfma_f32_16x16x32_bf16 v[144:147], a[88:91], a[56:59], v[144:147]\n"
      "v_mfma_f32_16x16x32_bf16 v[152:155], a[88:91], a[60:63], v[152:155]\n"
      "v_mfma_f32_16x16x32_bf16 v[132:135], a[92:95], a[48:51], v[132:135]\n"
      "v_mfma_f32_16x16x32_bf16 v[140:143], a[92:95], a[52:55], v[140:143]\n"
      "v_mfma_f32_16x16x32_bf16 v[148:151], a[92:95], a[56:59], v[148:151]\n"
      "v_mfma_f32_16x16x32_bf16 v[156:159], a[92:95], a[60:63], v[156:159]\n"
      "s_cmp_eq_u32 s64, 0\n"
      "s_cbranch_scc0 NRET1_0_%=\n"
      "v_mov_b64 v[208:209], v[160:161]\n"
      "v_mov_b64 v[210:211], v[162:163]\n"
      "v_mov_b64 v[212:213], v[164:165]\n"
      "v_mov_b64 v[214:215], v[166:167]\n"
      "v_mov_b64 v[216:217], v[168:169]\n"
      "v_mov_b64 v[218:219], v[170:171]\n"
      "v_mov_b64 v[220:221], v[172:173]\n"
      "v_mov_b64 v[222:223], v[174:175]\n"
      "s_nop 1\n"
      "s_branch RET1_0_%=\n"
      "NRET1_0_%=:\n"
      "s_cmp_eq_u32 s64, 1\n"
      "s_cbranch_scc0 NRET1_1_%=\n"
      "v_mov_b64 v[208:209], v[160:161]\n"
      "v_mov_b64 v[210:211], v[162:163]\n"
      "v_mov_b64 v[212:213], v[164:165]\n"
      "v_mov_b64 v[214:215], v[166:167]\n"
      "v_mov_b64 v[216:217], v[168:169]\n"
      "v_mov_b64 v[218:219], v[170:171]\n"
      "v_mov_b64 v[220:221], v[172:173]\n"
      "v_mov_b64 v[222:223], v[174:175]\n"
      "s_nop 1\n"
      "s_branch RET1_1_%=\n"
      "NRET1_1_%=:\n"
      "s_cmp_eq_u32 s64, 2\n"
      "s_cbranch_scc0 NRET1_2_%=\n"
      "v_mov_b64 v[208:209], v[160:161]\n"
      "v_mov_b64 v[210:211], v[162:163]\n"
      "v_mov_b64 v[212:213], v[164:165]\n"
      "v_mov_b64 v[214:215], v[166:167]\n"
      "v_mov_b64 v[216:217], v[168:169]\n"
      "v_mov_b64 v[218:219], v[170:171]\n"
      "v_mov_b64 v[220:221], v[172:173]\n"
      "v_mov_b64 v[222:223], v[174:175]\n"
      "s_nop 1\n"
      "s_branch RET1_2_%=\n"
      "NRET1_2_%=:\n"
      "s_cmp_eq_u32 s64, 3\n"
      "s_cbranch_scc0 NRET1_3_%=\n"
      "v_mov_b64 v[208:209], v[160:161]\n"
      "v_mov_b64 v[210:211], v[162:163]\n"
      "v_mov_b64 v[212:213], v[164:165]\n"
      "v_mov_b64 v[214:215], v[166:167]\n"
      "v_mov_b64 v[216:217], v[168:169]\n"
      "v_mov_b64 v[218:219], v[170:171]\n"
      "v_mov_b64 v[220:221], v[172:173]\n"
      "v_mov_b64 v[222:223], v[174:175]\n"
      "s_nop 1\n"
      "s_branch RET1_3_%=\n"
      "NRET1_3_%=:\n"
      "s_cmp_eq_u32 s64, 4\n"
      "s_cbranch_scc0 NRET1_4_%=\n"
      "v_mov_b64 v[208:209], v[160:161]\n"
      "v_mov_b64 v[210:211], v[162:163]\n"
      "v_mov_b64 v[212:213], v[164:165]\n"
      "v_mov_b64 v[214:215], v[166:167]\n"
      "v_mov_b64 v[216:217], v[168:169]\n"
      "v_mov_b64 v[218:219], v[170:171]\n"
      "v_mov_b64 v[220:221], v[172:173]\n"
      "v_mov_b64 v[222:223], v[174:175]\n"
      "s_nop 1\n"
      "s_branch RET1_4_%=\n"
      "NRET1_4_%=:\n"
      "s_cmp_eq_u32 s64, 5\n"
      "s_cbranch_scc0 NRET1_5_%=\n"
      "v_mov_b64 v[0:1], v[160:161]\n"
      "v_mov_b64 v[2:3], v[162:163]\n"
      "v_mov_b64 v[4:5], v[164:165]\n"
      "v_mov_b64 v[6:7], v[166:167]\n"
      "v_mov_b64 v[8:9], v[168:169]\n"
      "v_mov_b64 v[10:11], v[170:171]\n"
      "v_mov_b64 v[12:13], v[172:173]\n"
      "v_mov_b64 v[14:15], v[174:175]\n"
      "s_nop 1\n"
      "s_branch RET1_5_%=\n"
      "NRET1_5_%=:\n"
      "s_cmp_eq_u32 s64, 6\n"
      "s_cbranch_scc0 NRET1_6_%=\n"
      "v_mov_b64 v[96:97], v[160:161]\n"
      "v_mov_b64 v[98:99], v[162:163]\n"
      "v_mov_b64 v[100:101], v[164:165]\n"
      "v_mov_b64 v[102:103], v[166:167]\n"
      "v_mov_b64 v[104:105], v[168:169]\n"
      "v_mov_b64 v[106:107], v[170:171]\n"
      "v_mov_b64 v[108:109], v[172:173]\n"
      "v_mov_b64 v[110:111], v[174:175]\n"
      "v_mov_b32 v117, v224\n"
      "s_nop 1\n"
      "v_mov_b32_dpp v117, v228 quad_perm:[0,1,2,3] row_mask:0x2 bank_mask:0xf\n"
      "s_nop 1\n"
      "v_mov_b32_dpp v117, v232 quad_perm:[0,1,2,3] row_mask:0x4 bank_mask:0xf\n"
      "s_nop 1\n"
      "v_mov_b32_dpp v117, v236 quad_perm:[0,1,2,3] row_mask:0x8 bank_mask:0xf\n"
      "v_mfma_f32_16x16x32_bf16 a[228:231], a[196:199], v[96:99], a[192:195]\n"
      "v_mfma_f32_16x16x32_bf16 a[228:231], a[200:203], v[100:103], a[228:231]\n"
      "v_mfma_f32_16x16x32_bf16 a[228:231], a[204:207], v[104:107], a[228:231]\n"
      "ds_write_b128 v125, a[160:163] offset:0\n"
      "ds_write_b128 v125, a[164:167] offset:1024\n"
      "ds_write_b128 v125, a[168:171] offset:2048\n"
      "ds_write_b128 v125, a[172:175] offset:3072\n"
      "ds_write_b128 v125, a[176:179] offset:4096\n"
      "ds_write_b128 v125, a[180:183] offset:5120\n"
      "ds_write_b128 v125, a[184:187] offset:6144\n"
      "ds_write_b128 v125, a[188:191] offset:7168\n"
      "ds_write_b128 v125, v[96:99] offset:8192\n"
      "ds_write_b128 v125, v[100:103] offset:9216\n"
      "ds_write_b128 v125, v[104:107] offset:10240\n"
      "ds_write2_b32 v124, v117, v123 offset0:1 offset1:2\n"
      "s_nop 1\n"
      "s_branch RET1_6_%=\n"
      "NRET1_6_%=:\n"
      "SCALE_%=:\n"
      "v_pk_mul_f32 v[32:33], v[32:33], v[112:113] op_sel:[0,0] op_sel_hi:[1,0]\n"
      "v_pk_mul_f32 v[34:35], v[34:35], v[112:113] op_sel:[0,0] op_sel_hi:[1,0]\n"
      "v_pk_mul_f32 v[36:37], v[36:37], v[112:113] op_sel:[0,1] op_sel_hi:[1,1]\n"
      "v_pk_mul_f32 v[38:39], v[38:39], v[112:113] op_sel:[0,1] op_sel_hi:[1,1]\n"
      "v_pk_mul_f32 v[40:41], v[40:41], v[114:115] op_sel:[0,0] op_sel_hi:[1,0]\n"
      "v_pk_mul_f32 v[42:43], v[42:43], v[114:115] op_sel:[0,0] op_sel_hi:[1,0]\n"
      "v_pk_mul_f32 v[44:45], v[44:45], v[114:115] op_sel:[0,1] op_sel_hi:[1,1]\n"
      "v_pk_mul_f32 v[46:47], v[46:47], v[114:115] op_sel:[0,1] op_sel_hi:[1,1]\n"
      "v_pk_mul_f32 v[48:49], v[48:49], v[112:113] op_sel:[0,0] op_sel_hi:[1,0]\n"
      "v_pk_mul_f32 v[50:51], v[50:51], v[112:113] op_sel:[0,0] op_sel_hi:[1,0]\n"
      "v_pk_mul_f32 v[52:53], v[52:53], v[112:113] op_sel:[0,1] op_sel_hi:[1,1]\n"
      "v_pk_mul_f32 v[54:55], v[54:55], v[112:113] op_sel:[0,1] op_sel_hi:[1,1]\n"
      "v_pk_mul_f32 v[56:57], v[56:57], v[114:115] op_sel:[0,0] op_sel_hi:[1,0]\n"
      "v_pk_mul_f32 v[58:59], v[58:59], v[114:115] op_sel:[0,0] op_sel_hi:[1,0]\n"
      "v_pk_mul_f32 v[60:61], v[60:61], v[114:115] op_sel:[0,1] op_sel_hi:[1,1]\n"
      "v_pk_mul_f32 v[62:63], v[62:63], v[114:115] op_sel:[0,1] op_sel_hi:[1,1]\n"
      "v_pk_mul_f32 v[64:65], v[64:65], v[112:113] op_sel:[0,0] op_sel_hi:[1,0]\n"
      "v_pk_mul_f32 v[66:67], v[66:67], v[112:113] op_sel:[0,0] op_sel_hi:[1,0]\n"
      "v_pk_mul_f32 v[68:69], v[68:69], v[112:113] op_sel:[0,1] op_sel_hi:[1,1]\n"
      "v_pk_mul_f32 v[70:71], v[70:71], v[112:113] op_sel:[0,1] op_sel_hi:[1,1]\n"
      "v_pk_mul_f32 v[72:73], v[72:73], v[114:115] op_sel:[0,0] op_sel_hi:[1,0]\n"
      "v_pk_mul_f32 v[74:75], v[74:75], v[114:115] op_sel:[0,0] op_sel_hi:[1,0]\n"
      "v_pk_mul_f32 v[76:77], v[76:77], v[114:115] op_sel:[0,1] op_sel_hi:[1,1]\n"
      "v_pk_mul_f32 v[78:79], v[78:79], v[114:115] op_sel:[0,1] op_sel_hi:[1,1]\n"
      "v_pk_mul_f32 v[80:81], v[80:81], v[112:113] op_sel:[0,0] op_sel_hi:[1,0]\n"
      "v_pk_mul_f32 v[82:83], v[82:83], v[112:113] op_sel:[0,0] op_sel_hi:[1,0]\n"
      "v_pk_mul_f32 v[84:85], v[84:85], v[112:113] op_sel:[0,1] op_sel_hi:[1,1]\n"
      "v_pk_mul_f32 v[86:87], v[86:87], v[112:113] op_sel:[0,1] op_sel_hi:[1,1]\n"
      "v_pk_mul_f32 v[88:89], v[88:89], v[114:115] op_sel:[0,0] op_sel_hi:[1,0]\n"
      "v_pk_mul_f32 v[90:91], v[90:91], v[114:115] op_sel:[0,0] op_sel_hi:[1,0]\n"
      "v_pk_mul_f32 v[92:93], v[92:93], v[114:115] op_sel:[0,1] op_sel_hi:[1,1]\n"
      "v_pk_mul_f32 v[94:95], v[94:95], v[114:115] op_sel:[0,1] op_sel_hi:[1,1]\n"
      "v_accvgpr_read_b32 v118, a128\n"
      "v_accvgpr_read_b32 v119, a129\n"
      "v_pk_mul_f32 v[118:119], v[118:119], v[112:113] op_sel:[0,0] op_sel_hi:[1,0]\n"
      "v_accvgpr_write_b32 a128, v118\n"
      "v_accvgpr_write_b32 a129, v119\n"
      "v_accvgpr_read_b32 v118, a130\n"
      "v_accvgpr_read_b32 v119, a131\n"
      "v_pk_mul_f32 v[118:119], v[118:119], v[112:113] op_sel:[0,0] op_sel_hi:[1,0]\n"
      "v_accvgpr_write_b32 a130, v118\n"
      "v_accvgpr_write_b32 a131, v119\n"
      "v_accvgpr_read_b32 v118, a132\n"
      "v_accvgpr_read_b32 v119, a133\n"
      "v_pk_mul_f32 v[118:119], v[118:119], v[112:113] op_sel:[0,1] op_sel_hi:[1,1]\n"
      "v_accvgpr_write_b32 a132, v118\n"
      "v_accvgpr_write_b32 a133, v119\n"
      "v_accvgpr_read_b32 v118, a134\n"
      "v_accvgpr_read_b32 v119, a135\n"
      "v_pk_mul_f32 v[118:119], v[118:119], v[112:113] op_sel:[0,1] op_sel_hi:[1,1]\n"
      "v_accvgpr_write_b32 a134, v118\n"
      "v_accvgpr_write_b32 a135, v119\n"
      "v_accvgpr_read_b32 v118, a136\n"
      "v_accvgpr_read_b32 v119, a137\n"
      "v_pk_mul_f32 v[118:119], v[118:119], v[114:115] op_sel:[0,0] op_sel_hi:[1,0]\n"
      "v_accvgpr_write_b32 a136, v118\n"
      "v_accvgpr_write_b32 a137, v119\n"
      "v_accvgpr_read_b32 v118, a138\n"
      "v_accvgpr_read_b32 v119, a139\n"
      "v_pk_mul_f32 v[118:119], v[118:119], v[114:115] op_sel:[0,0] op_sel_hi:[1,0]\n"
      "v_accvgpr_write_b32 a138, v118\n"
      "v_accvgpr_write_b32 a139, v119\n"
      "v_accvgpr_read_b32 v118, a140\n"
      "v_accvgpr_read_b32 v119, a141\n"
      "v_pk_mul_f32 v[118:119], v[118:119], v[114:115] op_sel:[0,1] op_sel_hi:[1,1]\n"
      "v_accvgpr_write_b32 a140, v118\n"
      "v_accvgpr_write_b32 a141, v119\n"
      "v_accvgpr_read_b32 v118, a142\n"
      "v_accvgpr_read_b32 v119, a143\n"
      "v_pk_mul_f32 v[118:119], v[118:119], v[114:115] op_sel:[0,1] op_sel_hi:[1,1]\n"
      "v_accvgpr_write_b32 a142, v118\n"
      "v_accvgpr_write_b32 a143, v119\n"
      "v_accvgpr_read_b32 v118, a144\n"
      "v_accvgpr_read_b32 v119, a145\n"
      "v_pk_mul_f32 v[118:119], v[118:119], v[112:113] op_sel:[0,0] op_sel_hi:[1,0]\n"
      "v_accvgpr_write_b32 a144, v118\n"
      "v_accvgpr_write_b32 a145, v119\n"
      "v_accvgpr_read_b32 v118, a146\n"
      "v_accvgpr_read_b32 v119, a147\n"
      "v_pk_mul_f32 v[118:119], v[118:119], v[112:113] op_sel:[0,0] op_sel_hi:[1,0]\n"
      "v_accvgpr_write_b32 a146, v118\n"
      "v_accvgpr_write_b32 a147, v119\n"
      "v_accvgpr_read_b32 v118, a148\n"
      "v_accvgpr_read_b32 v119, a149\n"
      "v_pk_mul_f32 v[118:119], v[118:119], v[112:113] op_sel:[0,1] op_sel_hi:[1,1]\n"
      "v_accvgpr_write_b32 a148, v118\n"
      "v_accvgpr_write_b32 a149, v119\n"
      "v_accvgpr_read_b32 v118, a150\n"
      "v_accvgpr_read_b32 v119, a151\n"
      "v_pk_mul_f32 v[118:119], v[118:119], v[112:113] op_sel:[0,1] op_sel_hi:[1,1]\n"
      "v_accvgpr_write_b32 a150, v118\n"
      "v_accvgpr_write_b32 a151, v119\n"
      "v_accvgpr_read_b32 v118, a152\n"
      "v_accvgpr_read_b32 v119, a153\n"
      "v_pk_mul_f32 v[118:119], v[118:119], v[114:115] op_sel:[0,0] op_sel_hi:[1,0]\n"
      "v_accvgpr_write_b32 a152, v118\n"
      "v_accvgpr_write_b32 a153, v119\n"
      "v_accvgpr_read_b32 v118, a154\n"
      "v_accvgpr_read_b32 v119, a155\n"
      "v_pk_mul_f32 v[118:119], v[118:119], v[114:115] op_sel:[0,0] op_sel_hi:[1,0]\n"
      "v_accvgpr_write_b32 a154, v118\n"
      "v_accvgpr_write_b32 a155, v119\n"
      "v_accvgpr_read_b32 v118, a156\n"
      "v_accvgpr_read_b32 v119, a157\n"
      "v_pk_mul_f32 v[118:119], v[118:119], v[114:115] op_sel:[0,1] op_sel_hi:[1,1]\n"
      "v_accvgpr_write_b32 a156, v118\n"
      "v_accvgpr_write_b32 a157, v119\n"
      "v_accvgpr_read_b32 v118, a158\n"
      "v_accvgpr_read_b32 v119, a159\n"
      "v_pk_mul_f32 v[118:119], v[118:119], v[114:115] op_sel:[0,1] op_sel_hi:[1,1]\n"
      "v_accvgpr_write_b32 a158, v118\n"
      "v_accvgpr_write_b32 a159, v119\n"
      "v_accvgpr_read_b32 v118, a160\n"
      "v_accvgpr_read_b32 v119, a161\n"
      "v_pk_mul_f32 v[118:119], v[118:119], v[112:113] op_sel:[0,0] op_sel_hi:[1,0]\n"
      "v_accvgpr_write_b32 a160, v118\n"
      "v_accvgpr_write_b32 a161, v119\n"
      "v_accvgpr_read_b32 v118, a162\n"
      "v_accvgpr_read_b32 v119, a163\n"
      "v_pk_mul_f32 v[118:119], v[118:119], v[112:113] op_sel:[0,0] op_sel_hi:[1,0]\n"
      "v_accvgpr_write_b32 a162, v118\n"
      "v_accvgpr_write_b32 a163, v119\n"
      "v_accvgpr_read_b32 v118, a164\n"
      "v_accvgpr_read_b32 v119, a165\n"
      "v_pk_mul_f32 v[118:119], v[118:119], v[112:113] op_sel:[0,1] op_sel_hi:[1,1]\n"
      "v_accvgpr_write_b32 a164, v118\n"
      "v_accvgpr_write_b32 a165, v119\n"
      "v_accvgpr_read_b32 v118, a166\n"
      "v_accvgpr_read_b32 v119, a167\n"
      "v_pk_mul_f32 v[118:119], v[118:119], v[112:113] op_sel:[0,1] op_sel_hi:[1,1]\n"
      "v_accvgpr_write_b32 a166, v118\n"
      "v_accvgpr_write_b32 a167, v119\n"
      "v_accvgpr_read_b32 v118, a168\n"
      "v_accvgpr_read_b32 v119, a169\n"
      "v_pk_mul_f32 v[118:119], v[118:119], v[114:115] op_sel:[0,0] op_sel_hi:[1,0]\n"
      "v_accvgpr_write_b32 a168, v118\n"
      "v_accvgpr_write_b32 a169, v119\n"
      "v_accvgpr_read_b32 v118, a170\n"
      "v_accvgpr_read_b32 v119, a171\n"
      "v_pk_mul_f32 v[118:119], v[118:119], v[114:115] op_sel:[0,0] op_sel_hi:[1,0]\n"
      "v_accvgpr_write_b32 a170, v118\n"
      "v_accvgpr_write_b32 a171, v119\n"
      "v_accvgpr_read_b32 v118, a172\n"
      "v_accvgpr_read_b32 v119, a173\n"
      "v_pk_mul_f32 v[118:119], v[118:119], v[114:115] op_sel:[0,1] op_sel_hi:[1,1]\n"
      "v_accvgpr_write_b32 a172, v118\n"
      "v_accvgpr_write_b32 a173, v119\n"
      "v_accvgpr_read_b32 v118, a174\n"
      "v_accvgpr_read_b32 v119, a175\n"
      "v_pk_mul_f32 v[118:119], v[118:119], v[114:115] op_sel:[0,1] op_sel_hi:[1,1]\n"
      "v_accvgpr_write_b32 a174, v118\n"
      "v_accvgpr_write_b32 a175, v119\n"
      "v_accvgpr_read_b32 v118, a176\n"
      "v_accvgpr_read_b32 v119, a177\n"
      "v_pk_mul_f32 v[118:119], v[118:119], v[112:113] op_sel:[0,0] op_sel_hi:[1,0]\n"
      "v_accvgpr_write_b32 a176, v118\n"
      "v_accvgpr_write_b32 a177, v119\n"
      "v_accvgpr_read_b32 v118, a178\n"
      "v_accvgpr_read_b32 v119, a179\n"
      "v_pk_mul_f32 v[118:119], v[118:119], v[112:113] op_sel:[0,0] op_sel_hi:[1,0]\n"
      "v_accvgpr_write_b32 a178, v118\n"
      "v_accvgpr_write_b32 a179, v119\n"
      "v_accvgpr_read_b32 v118, a180\n"
      "v_accvgpr_read_b32 v119, a181\n"
      "v_pk_mul_f32 v[118:119], v[118:119], v[112:113] op_sel:[0,1] op_sel_hi:[1,1]\n"
      "v_accvgpr_write_b32 a180, v118\n"
      "v_accvgpr_write_b32 a181, v119\n"
      "v_accvgpr_read_b32 v118, a182\n"
      "v_accvgpr_read_b32 v119, a183\n"
      "v_pk_mul_f32 v[118:119], v[118:119], v[112:113] op_sel:[0,1] op_sel_hi:[1,1]\n"
      "v_accvgpr_write_b32 a182, v118\n"
      "v_accvgpr_write_b32 a183, v119\n"
      "v_accvgpr_read_b32 v118, a184\n"
      "v_accvgpr_read_b32 v119, a185\n"
      "v_pk_mul_f32 v[118:119], v[118:119], v[114:115] op_sel:[0,0] op_sel_hi:[1,0]\n"
      "v_accvgpr_write_b32 a184, v118\n"
      "v_accvgpr_write_b32 a185, v119\n"
      "v_accvgpr_read_b32 v118, a186\n"
      "v_accvgpr_read_b32 v119, a187\n"
      "v_pk_mul_f32 v[118:119], v[118:119], v[114:115] op_sel:[0,0] op_sel_hi:[1,0]\n"
      "v_accvgpr_write_b32 a186, v118\n"
      "v_accvgpr_write_b32 a187, v119\n"
      "v_accvgpr_read_b32 v118, a188\n"
      "v_accvgpr_read_b32 v119, a189\n"
      "v_pk_mul_f32 v[118:119], v[118:119], v[114:115] op_sel:[0,1] op_sel_hi:[1,1]\n"
      "v_accvgpr_write_b32 a188, v118\n"
      "v_accvgpr_write_b32 a189, v119\n"
      "v_accvgpr_read_b32 v118, a190\n"
      "v_accvgpr_read_b32 v119, a191\n"
      "v_pk_mul_f32 v[118:119], v[118:119], v[114:115] op_sel:[0,1] op_sel_hi:[1,1]\n"
      "v_accvgpr_write_b32 a190, v118\n"
      "v_accvgpr_write_b32 a191, v119\n"
      "v_mov_b32 v118, v112\n"
      "s_nop 1\n"
      "v_mov_b32_dpp v118, v113 quad_perm:[0,1,2,3] row_mask:0x2 bank_mask:0xf\n"
      "s_nop 1\n"
      "v_mov_b32_dpp v118, v114 quad_perm:[0,1,2,3] row_mask:0x4 bank_mask:0xf\n"
      "s_nop 1\n"
      "v_mov_b32_dpp v118, v115 quad_perm:[0,1,2,3] row_mask:0x8 bank_mask:0xf\n"
      "v_accvgpr_read_b32 v119, a192\n"
      "v_mul_f32 v118, v118, v119\n"
      "v_accvgpr_write_b32 a192, v118\n"
      "v_lshlrev_b32 v118, 16, v208\n"
      "v_and_b32 v119, 0xffff0000, v208\n"
      "v_pk_mul_f32 v[118:119], v[118:119], v[112:113] op_sel:[0,0] op_sel_hi:[1,0]\n"
      "v_cvt_pk_bf16_f32 v208, v118, v119\n"
      "v_lshlrev_b32 v118, 16, v209\n"
      "v_and_b32 v119, 0xffff0000, v209\n"
      "v_pk_mul_f32 v[118:119], v[118:119], v[112:113] op_sel:[0,0] op_sel_hi:[1,0]\n"
      "v_cvt_pk_bf16_f32 v209, v118, v119\n"
      "v_lshlrev_b32 v118, 16, v210\n"
      "v_and_b32 v119, 0xffff0000, v210\n"
      "v_pk_mul_f32 v[118:119], v[118:119], v[112:113] op_sel:[0,0] op_sel_hi:[1,0]\n"
      "v_cvt_pk_bf16_f32 v210, v118, v119\n"
      "v_lshlrev_b32 v118, 16, v211\n"
      "v_and_b32 v119, 0xffff0000, v211\n"
      "v_pk_mul_f32 v[118:119], v[118:119], v[112:113] op_sel:[0,0] op_sel_hi:[1,0]\n"
      "v_cvt_pk_bf16_f32 v211, v118, v119\n"
      "v_lshlrev_b32 v118, 16, v212\n"
      "v_and_b32 v119, 0xffff0000, v212\n"
      "v_pk_mul_f32 v[118:119], v[118:119], v[112:113] op_sel:[0,1] op_sel_hi:[1,1]\n"
      "v_cvt_pk_bf16_f32 v212, v118, v119\n"
      "v_lshlrev_b32 v118, 16, v213\n"
      "v_and_b32 v119, 0xffff0000, v213\n"
      "v_pk_mul_f32 v[118:119], v[118:119], v[112:113] op_sel:[0,1] op_sel_hi:[1,1]\n"
      "v_cvt_pk_bf16_f32 v213, v118, v119\n"
      "v_lshlrev_b32 v118, 16, v214\n"
      "v_and_b32 v119, 0xffff0000, v214\n"
      "v_pk_mul_f32 v[118:119], v[118:119], v[112:113] op_sel:[0,1] op_sel_hi:[1,1]\n"
      "v_cvt_pk_bf16_f32 v214, v118, v119\n"
      "v_lshlrev_b32 v118, 16, v215\n"
      "v_and_b32 v119, 0xffff0000, v215\n"
      "v_pk_mul_f32 v[118:119], v[118:119], v[112:113] op_sel:[0,1] op_sel_hi:[1,1]\n"
      "v_cvt_pk_bf16_f32 v215, v118, v119\n"
      "v_lshlrev_b32 v118, 16, v216\n"
      "v_and_b32 v119, 0xffff0000, v216\n"
      "v_pk_mul_f32 v[118:119], v[118:119], v[114:115] op_sel:[0,0] op_sel_hi:[1,0]\n"
      "v_cvt_pk_bf16_f32 v216, v118, v119\n"
      "v_lshlrev_b32 v118, 16, v217\n"
      "v_and_b32 v119, 0xffff0000, v217\n"
      "v_pk_mul_f32 v[118:119], v[118:119], v[114:115] op_sel:[0,0] op_sel_hi:[1,0]\n"
      "v_cvt_pk_bf16_f32 v217, v118, v119\n"
      "v_lshlrev_b32 v118, 16, v218\n"
      "v_and_b32 v119, 0xffff0000, v218\n"
      "v_pk_mul_f32 v[118:119], v[118:119], v[114:115] op_sel:[0,0] op_sel_hi:[1,0]\n"
      "v_cvt_pk_bf16_f32 v218, v118, v119\n"
      "v_lshlrev_b32 v118, 16, v219\n"
      "v_and_b32 v119, 0xffff0000, v219\n"
      "v_pk_mul_f32 v[118:119], v[118:119], v[114:115] op_sel:[0,0] op_sel_hi:[1,0]\n"
      "v_cvt_pk_bf16_f32 v219, v118, v119\n"
      "v_lshlrev_b32 v118, 16, v220\n"
      "v_and_b32 v119, 0xffff0000, v220\n"
      "v_pk_mul_f32 v[118:119], v[118:119], v[114:115] op_sel:[0,1] op_sel_hi:[1,1]\n"
      "v_cvt_pk_bf16_f32 v220, v118, v119\n"
      "v_lshlrev_b32 v118, 16, v221\n"
      "v_and_b32 v119, 0xffff0000, v221\n"
      "v_pk_mul_f32 v[118:119], v[118:119], v[114:115] op_sel:[0,1] op_sel_hi:[1,1]\n"
      "v_cvt_pk_bf16_f32 v221, v118, v119\n"
      "v_lshlrev_b32 v118, 16, v222\n"
      "v_and_b32 v119, 0xffff0000, v222\n"
      "v_pk_mul_f32 v[118:119], v[118:119], v[114:115] op_sel:[0,1] op_sel_hi:[1,1]\n"
      "v_cvt_pk_bf16_f32 v222, v118, v119\n"
      "v_lshlrev_b32 v118, 16, v223\n"
      "v_and_b32 v119, 0xffff0000, v223\n"
      "v_pk_mul_f32 v[118:119], v[118:119], v[114:115] op_sel:[0,1] op_sel_hi:[1,1]\n"
      "v_cvt_pk_bf16_f32 v223, v118, v119\n"
      "v_lshlrev_b32 v118, 16, v192\n"
      "v_and_b32 v119, 0xffff0000, v192\n"
      "v_pk_mul_f32 v[118:119], v[118:119], v[112:113] op_sel:[0,0] op_sel_hi:[1,0]\n"
      "v_cvt_pk_bf16_f32 v192, v118, v119\n"
      "v_lshlrev_b32 v118, 16, v193\n"
      "v_and_b32 v119, 0xffff0000, v193\n"
      "v_pk_mul_f32 v[118:119], v[118:119], v[112:113] op_sel:[0,0] op_sel_hi:[1,0]\n"
      "v_cvt_pk_bf16_f32 v193, v118, v119\n"
      "v_lshlrev_b32 v118, 16, v194\n"
      "v_and_b32 v119, 0xffff0000, v194\n"
      "v_pk_mul_f32 v[118:119], v[118:119], v[112:113] op_sel:[0,0] op_sel_hi:[1,0]\n"
      "v_cvt_pk_bf16_f32 v194, v118, v119\n"
      "v_lshlrev_b32 v118, 16, v195\n"
      "v_and_b32 v119, 0xffff0000, v195\n"
      "v_pk_mul_f32 v[118:119], v[118:119], v[112:113] op_sel:[0,0] op_sel_hi:[1,0]\n"
      "v_cvt_pk_bf16_f32 v195, v118, v119\n"
      "v_lshlrev_b32 v118, 16, v196\n"
      "v_and_b32 v119, 0xffff0000, v196\n"
      "v_pk_mul_f32 v[118:119], v[118:119], v[112:113] op_sel:[0,1] op_sel_hi:[1,1]\n"
      "v_cvt_pk_bf16_f32 v196, v118, v119\n"
      "v_lshlrev_b32 v118, 16, v197\n"
      "v_and_b32 v119, 0xffff0000, v197\n"
      "v_pk_mul_f32 v[118:119], v[118:119], v[112:113] op_sel:[0,1] op_sel_hi:[1,1]\n"
      "v_cvt_pk_bf16_f32 v197, v118, v119\n"
      "v_lshlrev_b32 v118, 16, v198\n"
      "v_and_b32 v119, 0xffff0000, v198\n"
      "v_pk_mul_f32 v[118:119], v[118:119], v[112:113] op_sel:[0,1] op_sel_hi:[1,1]\n"
      "v_cvt_pk_bf16_f32 v198, v118, v119\n"
      "v_lshlrev_b32 v118, 16, v199\n"
      "v_and_b32 v119, 0xffff0000, v199\n"
      "v_pk_mul_f32 v[118:119], v[118:119], v[112:113] op_sel:[0,1] op_sel_hi:[1,1]\n"
      "v_cvt_pk_bf16_f32 v199, v118, v119\n"
      "v_lshlrev_b32 v118, 16, v200\n"
      "v_and_b32 v119, 0xffff0000, v200\n"
      "v_pk_mul_f32 v[118:119], v[118:119], v[114:115] op_sel:[0,0] op_sel_hi:[1,0]\n"
      "v_cvt_pk_bf16_f32 v200, v118, v119\n"
      "v_lshlrev_b32 v118, 16, v201\n"
      "v_and_b32 v119, 0xffff0000, v201\n"
      "v_pk_mul_f32 v[118:119], v[118:119], v[114:115] op_sel:[0,0] op_sel_hi:[1,0]\n"
      "v_cvt_pk_bf16_f32 v201, v118, v119\n"
      "v_lshlrev_b32 v118, 16, v202\n"
      "v_and_b32 v119, 0xffff0000, v202\n"
      "v_pk_mul_f32 v[118:119], v[118:119], v[114:115] op_sel:[0,0] op_sel_hi:[1,0]\n"
      "v_cvt_pk_bf16_f32 v202, v118, v119\n"
      "v_lshlrev_b32 v118, 16, v203\n"
      "v_and_b32 v119, 0xffff0000, v203\n"
      "v_pk_mul_f32 v[118:119], v[118:119], v[114:115] op_sel:[0,0] op_sel_hi:[1,0]\n"
      "v_cvt_pk_bf16_f32 v203, v118, v119\n"
      "v_lshlrev_b32 v118, 16, v204\n"
      "v_and_b32 v119, 0xffff0000, v204\n"
      "v_pk_mul_f32 v[118:119], v[118:119], v[114:115] op_sel:[0,1] op_sel_hi:[1,1]\n"
      "v_cvt_pk_bf16_f32 v204, v118, v119\n"
      "v_lshlrev_b32 v118, 16, v205\n"
      "v_and_b32 v119, 0xffff0000, v205\n"
      "v_pk_mul_f32 v[118:119], v[118:119], v[114:115] op_sel:[0,1] op_sel_hi:[1,1]\n"
      "v_cvt_pk_bf16_f32 v205, v118, v119\n"
      "v_lshlrev_b32 v118, 16, v206\n"
      "v_and_b32 v119, 0xffff0000, v206\n"
      "v_pk_mul_f32 v[118:119], v[118:119], v[114:115] op_sel:[0,1] op_sel_hi:[1,1]\n"
      "v_cvt_pk_bf16_f32 v206, v118, v119\n"
      "v_lshlrev_b32 v118, 16, v207\n"
      "v_and_b32 v119, 0xffff0000, v207\n"
      "v_pk_mul_f32 v[118:119], v[118:119], v[114:115] op_sel:[0,1] op_sel_hi:[1,1]\n"
      "v_cvt_pk_bf16_f32 v207, v118, v119\n"
      "v_lshlrev_b32 v118, 16, v0\n"
      "v_and_b32 v119, 0xffff0000, v0\n"
      "v_pk_mul_f32 v[118:119], v[118:119], v[112:113] op_sel:[0,0] op_sel_hi:[1,0]\n"
      "v_cvt_pk_bf16_f32 v0, v118, v119\n"
      "v_lshlrev_b32 v118, 16, v1\n"
      "v_and_b32 v119, 0xffff0000, v1\n"
      "v_pk_mul_f32 v[118:119], v[118:119], v[112:113] op_sel:[0,0] op_sel_hi:[1,0]\n"
      "v_cvt_pk_bf16_f32 v1, v118, v119\n"
      "v_lshlrev_b32 v118, 16, v2\n"
      "v_and_b32 v119, 0xffff0000, v2\n"
      "v_pk_mul_f32 v[118:119], v[118:119], v[112:113] op_sel:[0,0] op_sel_hi:[1,0]\n"
      "v_cvt_pk_bf16_f32 v2, v118, v119\n"
      "v_lshlrev_b32 v118, 16, v3\n"
      "v_and_b32 v119, 0xffff0000, v3\n"
      "v_pk_mul_f32 v[118:119], v[118:119], v[112:113] op_sel:[0,0] op_sel_hi:[1,0]\n"
      "v_cvt_pk_bf16_f32 v3, v118, v119\n"
      "v_lshlrev_b32 v118, 16, v4\n"
      "v_and_b32 v119, 0xffff0000, v4\n"
      "v_pk_mul_f32 v[118:119], v[118:119], v[112:113] op_sel:[0,1] op_sel_hi:[1,1]\n"
      "v_cvt_pk_bf16_f32 v4, v118, v119\n"
      "v_lshlrev_b32 v118, 16, v5\n"
      "v_and_b32 v119, 0xffff0000, v5\n"
      "v_pk_mul_f32 v[118:119], v[118:119], v[112:113] op_sel:[0,1] op_sel_hi:[1,1]\n"
      "v_cvt_pk_bf16_f32 v5, v118, v119\n"
      "v_lshlrev_b32 v118, 16, v6\n"
      "v_and_b32 v119, 0xffff0000, v6\n"
      "v_pk_mul_f32 v[118:119], v[118:119], v[112:113] op_sel:[0,1] op_sel_hi:[1,1]\n"
      "v_cvt_pk_bf16_f32 v6, v118, v119\n"
      "v_lshlrev_b32 v118, 16, v7\n"
      "v_and_b32 v119, 0xffff0000, v7\n"
      "v_pk_mul_f32 v[118:119], v[118:119], v[112:113] op_sel:[0,1] op_sel_hi:[1,1]\n"
      "v_cvt_pk_bf16_f32 v7, v118, v119\n"
      "v_lshlrev_b32 v118, 16, v8\n"
      "v_and_b32 v119, 0xffff0000, v8\n"
      "v_pk_mul_f32 v[118:119], v[118:119], v[114:115] op_sel:[0,0] op_sel_hi:[1,0]\n"
      "v_cvt_pk_bf16_f32 v8, v118, v119\n"
      "v_lshlrev_b32 v118, 16, v9\n"
      "v_and_b32 v119, 0xffff0000, v9\n"
      "v_pk_mul_f32 v[118:119], v[118:119], v[114:115] op_sel:[0,0] op_sel_hi:[1,0]\n"
      "v_cvt_pk_bf16_f32 v9, v118, v119\n"
      "v_lshlrev_b32 v118, 16, v10\n"
      "v_and_b32 v119, 0xffff0000, v10\n"
      "v_pk_mul_f32 v[118:119], v[118:119], v[114:115] op_sel:[0,0] op_sel_hi:[1,0]\n"
      "v_cvt_pk_bf16_f32 v10, v118, v119\n"
      "v_lshlrev_b32 v118, 16, v11\n"
      "v_and_b32 v119, 0xffff0000, v11\n"
      "v_pk_mul_f32 v[118:119], v[118:119], v[114:115] op_sel:[0,0] op_sel_hi:[1,0]\n"
      "v_cvt_pk_bf16_f32 v11, v118, v119\n"
      "v_lshlrev_b32 v118, 16, v12\n"
      "v_and_b32 v119, 0xffff0000, v12\n"
      "v_pk_mul_f32 v[118:119], v[118:119], v[114:115] op_sel:[0,1] op_sel_hi:[1,1]\n"
      "v_cvt_pk_bf16_f32 v12, v118, v119\n"
      "v_lshlrev_b32 v118, 16, v13\n"
      "v_and_b32 v119, 0xffff0000, v13\n"
      "v_pk_mul_f32 v[118:119], v[118:119], v[114:115] op_sel:[0,1] op_sel_hi:[1,1]\n"
      "v_cvt_pk_bf16_f32 v13, v118, v119\n"
      "v_lshlrev_b32 v118, 16, v14\n"
      "v_and_b32 v119, 0xffff0000, v14\n"
      "v_pk_mul_f32 v[118:119], v[118:119], v[114:115] op_sel:[0,1] op_sel_hi:[1,1]\n"
      "v_cvt_pk_bf16_f32 v14, v118, v119\n"
      "v_lshlrev_b32 v118, 16, v15\n"
      "v_and_b32 v119, 0xffff0000, v15\n"
      "v_pk_mul_f32 v[118:119], v[118:119], v[114:115] op_sel:[0,1] op_sel_hi:[1,1]\n"
      "v_cvt_pk_bf16_f32 v15, v118, v119\n"
      "v_lshlrev_b32 v118, 16, v16\n"
      "v_and_b32 v119, 0xffff0000, v16\n"
      "v_pk_mul_f32 v[118:119], v[118:119], v[112:113] op_sel:[0,0] op_sel_hi:[1,0]\n"
      "v_cvt_pk_bf16_f32 v16, v118, v119\n"
      "v_lshlrev_b32 v118, 16, v17\n"
      "v_and_b32 v119, 0xffff0000, v17\n"
      "v_pk_mul_f32 v[118:119], v[118:119], v[112:113] op_sel:[0,0] op_sel_hi:[1,0]\n"
      "v_cvt_pk_bf16_f32 v17, v118, v119\n"
      "v_lshlrev_b32 v118, 16, v18\n"
      "v_and_b32 v119, 0xffff0000, v18\n"
      "v_pk_mul_f32 v[118:119], v[118:119], v[112:113] op_sel:[0,0] op_sel_hi:[1,0]\n"
      "v_cvt_pk_bf16_f32 v18, v118, v119\n"
      "v_lshlrev_b32 v118, 16, v19\n"
      "v_and_b32 v119, 0xffff0000, v19\n"
      "v_pk_mul_f32 v[118:119], v[118:119], v[112:113] op_sel:[0,0] op_sel_hi:[1,0]\n"
      "v_cvt_pk_bf16_f32 v19, v118, v119\n"
      "v_lshlrev_b32 v118, 16, v20\n"
      "v_and_b32 v119, 0xffff0000, v20\n"
      "v_pk_mul_f32 v[118:119], v[118:119], v[112:113] op_sel:[0,1] op_sel_hi:[1,1]\n"
      "v_cvt_pk_bf16_f32 v20, v118, v119\n"
      "v_lshlrev_b32 v118, 16, v21\n"
      "v_and_b32 v119, 0xffff0000, v21\n"
      "v_pk_mul_f32 v[118:119], v[118:119], v[112:113] op_sel:[0,1] op_sel_hi:[1,1]\n"
      "v_cvt_pk_bf16_f32 v21, v118, v119\n"
      "v_lshlrev_b32 v118, 16, v22\n"
      "v_and_b32 v119, 0xffff0000, v22\n"
      "v_pk_mul_f32 v[118:119], v[118:119], v[112:113] op_sel:[0,1] op_sel_hi:[1,1]\n"
      "v_cvt_pk_bf16_f32 v22, v118, v119\n"
      "v_lshlrev_b32 v118, 16, v23\n"
      "v_and_b32 v119, 0xffff0000, v23\n"
      "v_pk_mul_f32 v[118:119], v[118:119], v[112:113] op_sel:[0,1] op_sel_hi:[1,1]\n"
      "v_cvt_pk_bf16_f32 v23, v118, v119\n"
      "v_lshlrev_b32 v118, 16, v24\n"
      "v_and_b32 v119, 0xffff0000, v24\n"
      "v_pk_mul_f32 v[118:119], v[118:119], v[114:115] op_sel:[0,0] op_sel_hi:[1,0]\n"
      "v_cvt_pk_bf16_f32 v24, v118, v119\n"
      "v_lshlrev_b32 v118, 16, v25\n"
      "v_and_b32 v119, 0xffff0000, v25\n"
      "v_pk_mul_f32 v[118:119], v[118:119], v[114:115] op_sel:[0,0] op_sel_hi:[1,0]\n"
      "v_cvt_pk_bf16_f32 v25, v118, v119\n"
      "v_lshlrev_b32 v118, 16, v26\n"
      "v_and_b32 v119, 0xffff0000, v26\n"
      "v_pk_mul_f32 v[118:119], v[118:119], v[114:115] op_sel:[0,0] op_sel_hi:[1,0]\n"
      "v_cvt_pk_bf16_f32 v26, v118, v119\n"
      "v_lshlrev_b32 v118, 16, v27\n"
      "v_and_b32 v119, 0xffff0000, v27\n"
      "v_pk_mul_f32 v[118:119], v[118:119], v[114:115] op_sel:[0,0] op_sel_hi:[1,0]\n"
      "v_cvt_pk_bf16_f32 v27, v118, v119\n"
      "v_lshlrev_b32 v118, 16, v28\n"
      "v_and_b32 v119, 0xffff0000, v28\n"
      "v_pk_mul_f32 v[118:119], v[118:119], v[114:115] op_sel:[0,1] op_sel_hi:[1,1]\n"
      "v_cvt_pk_bf16_f32 v28, v118, v119\n"
      "v_lshlrev_b32 v118, 16, v29\n"
      "v_and_b32 v119, 0xffff0000, v29\n"
      "v_pk_mul_f32 v[118:119], v[118:119], v[114:115] op_sel:[0,1] op_sel_hi:[1,1]\n"
      "v_cvt_pk_bf16_f32 v29, v118, v119\n"
      "v_lshlrev_b32 v118, 16, v30\n"
      "v_and_b32 v119, 0xffff0000, v30\n"
      "v_pk_mul_f32 v[118:119], v[118:119], v[114:115] op_sel:[0,1] op_sel_hi:[1,1]\n"
      "v_cvt_pk_bf16_f32 v30, v118, v119\n"
      "v_lshlrev_b32 v118, 16, v31\n"
      "v_and_b32 v119, 0xffff0000, v31\n"
      "v_pk_mul_f32 v[118:119], v[118:119], v[114:115] op_sel:[0,1] op_sel_hi:[1,1]\n"
      "v_cvt_pk_bf16_f32 v31, v118, v119\n"
      "v_lshlrev_b32 v118, 16, v96\n"
      "v_and_b32 v119, 0xffff0000, v96\n"
      "v_pk_mul_f32 v[118:119], v[118:119], v[112:113] op_sel:[0,0] op_sel_hi:[1,0]\n"
      "v_cvt_pk_bf16_f32 v96, v118, v119\n"
      "v_lshlrev_b32 v118, 16, v97\n"
      "v_and_b32 v119, 0xffff0000, v97\n"
      "v_pk_mul_f32 v[118:119], v[118:119], v[112:113] op_sel:[0,0] op_sel_hi:[1,0]\n"
      "v_cvt_pk_bf16_f32 v97, v118, v119\n"
      "v_lshlrev_b32 v118, 16, v98\n"
      "v_and_b32 v119, 0xffff0000, v98\n"
      "v_pk_mul_f32 v[118:119], v[118:119], v[112:113] op_sel:[0,0] op_sel_hi:[1,0]\n"
      "v_cvt_pk_bf16_f32 v98, v118, v119\n"
      "v_lshlrev_b32 v118, 16, v99\n"
      "v_and_b32 v119, 0xffff0000, v99\n"
      "v_pk_mul_f32 v[118:119], v[118:119], v[112:113] op_sel:[0,0] op_sel_hi:[1,0]\n"
      "v_cvt_pk_bf16_f32 v99, v118, v119\n"
      "v_lshlrev_b32 v118, 16, v100\n"
      "v_and_b32 v119, 0xffff0000, v100\n"
      "v_pk_mul_f32 v[118:119], v[118:119], v[112:113] op_sel:[0,1] op_sel_hi:[1,1]\n"
      "v_cvt_pk_bf16_f32 v100, v118, v119\n"
      "v_lshlrev_b32 v118, 16, v101\n"
      "v_and_b32 v119, 0xffff0000, v101\n"
      "v_pk_mul_f32 v[118:119], v[118:119], v[112:113] op_sel:[0,1] op_sel_hi:[1,1]\n"
      "v_cvt_pk_bf16_f32 v101, v118, v119\n"
      "v_lshlrev_b32 v118, 16, v102\n"
      "v_and_b32 v119, 0xffff0000, v102\n"
      "v_pk_mul_f32 v[118:119], v[118:119], v[112:113] op_sel:[0,1] op_sel_hi:[1,1]\n"
      "v_cvt_pk_bf16_f32 v102, v118, v119\n"
      "v_lshlrev_b32 v118, 16, v103\n"
      "v_and_b32 v119, 0xffff0000, v103\n"
      "v_pk_mul_f32 v[118:119], v[118:119], v[112:113] op_sel:[0,1] op_sel_hi:[1,1]\n"
      "v_cvt_pk_bf16_f32 v103, v118, v119\n"
      "v_lshlrev_b32 v118, 16, v104\n"
      "v_and_b32 v119, 0xffff0000, v104\n"
      "v_pk_mul_f32 v[118:119], v[118:119], v[114:115] op_sel:[0,0] op_sel_hi:[1,0]\n"
      "v_cvt_pk_bf16_f32 v104, v118, v119\n"
      "v_lshlrev_b32 v118, 16, v105\n"
      "v_and_b32 v119, 0xffff0000, v105\n"
      "v_pk_mul_f32 v[118:119], v[118:119], v[114:115] op_sel:[0,0] op_sel_hi:[1,0]\n"
      "v_cvt_pk_bf16_f32 v105, v118, v119\n"
      "v_lshlrev_b32 v118, 16, v106\n"
      "v_and_b32 v119, 0xffff0000, v106\n"
      "v_pk_mul_f32 v[118:119], v[118:119], v[114:115] op_sel:[0,0] op_sel_hi:[1,0]\n"
      "v_cvt_pk_bf16_f32 v106, v118, v119\n"
      "v_lshlrev_b32 v118, 16, v107\n"
      "v_and_b32 v119, 0xffff0000, v107\n"
      "v_pk_mul_f32 v[118:119], v[118:119], v[114:115] op_sel:[0,0] op_sel_hi:[1,0]\n"
      "v_cvt_pk_bf16_f32 v107, v118, v119\n"
      "v_lshlrev_b32 v118, 16, v108\n"
      "v_and_b32 v119, 0xffff0000, v108\n"
      "v_pk_mul_f32 v[118:119], v[118:119], v[114:115] op_sel:[0,1] op_sel_hi:[1,1]\n"
      "v_cvt_pk_bf16_f32 v108, v118, v119\n"
      "v_lshlrev_b32 v118, 16, v109\n"
      "v_and_b32 v119, 0xffff0000, v109\n"
      "v_pk_mul_f32 v[118:119], v[118:119], v[114:115] op_sel:[0,1] op_sel_hi:[1,1]\n"
      "v_cvt_pk_bf16_f32 v109, v118, v119\n"
      "v_lshlrev_b32 v118, 16, v110\n"
      "v_and_b32 v119, 0xffff0000, v110\n"
      "v_pk_mul_f32 v[118:119], v[118:119], v[114:115] op_sel:[0,1] op_sel_hi:[1,1]\n"
      "v_cvt_pk_bf16_f32 v110, v118, v119\n"
      "v_lshlrev_b32 v118, 16, v111\n"
      "v_and_b32 v119, 0xffff0000, v111\n"
      "v_pk_mul_f32 v[118:119], v[118:119], v[114:115] op_sel:[0,1] op_sel_hi:[1,1]\n"
      "v_cvt_pk_bf16_f32 v111, v118, v119\n"
      "s_setpc_b64 s[84:85]\n"
      "MIS_%=:\n"
      "s_waitcnt lgkmcnt(0)\n"
      "v_sub_f32 v148, v153, v117\n"
      "v_min_f32 v149, 0, -v148\n"
      "v_min_f32 v148, 0, v148\n"
      "v_exp_f32 v149, v149\n"
      "v_exp_f32 v148, v148\n"
      "v_min_f32 v117, v117, v153\n"
      "v_mov_b32 v112, v148\n"
      "v_mov_b32 v228, v149\n"
      "v_mov_b32 v114, v112\n"
      "v_mov_b32 v230, v228\n"
      "s_nop 0\n"
      "v_permlane32_swap_b32 v112, v114\n"
      "v_permlane32_swap_b32 v228, v230\n"
      "v_mov_b32 v113, v112\n"
      "v_mov_b32 v229, v228\n"
      "v_mov_b32 v115, v114\n"
      "v_mov_b32 v231, v230\n"
      "v_permlane16_swap_b32 v112, v113\n"
      "v_permlane16_swap_b32 v228, v229\n"
      "v_permlane16_swap_b32 v114, v115\n"
      "v_permlane16_swap_b32 v230, v231\n"
      "s_call_b64 s[84:85], SCALE_%=\n"
      "v_pk_mul_f32 v[160:161], v[160:161], v[228:229] op_sel:[0,0] op_sel_hi:[1,0]\n"
      "v_pk_mul_f32 v[162:163], v[162:163], v[228:229] op_sel:[0,0] op_sel_hi:[1,0]\n"
      "v_pk_mul_f32 v[164:165], v[164:165], v[228:229] op_sel:[0,1] op_sel_hi:[1,1]\n"
      "v_pk_mul_f32 v[166:167], v[166:167], v[228:229] op_sel:[0,1] op_sel_hi:[1,1]\n"
      "v_pk_mul_f32 v[168:169], v[168:169], v[230:231] op_sel:[0,0] op_sel_hi:[1,0]\n"
      "v_pk_mul_f32 v[170:171], v[170:171], v[230:231] op_sel:[0,0] op_sel_hi:[1,0]\n"
      "v_pk_mul_f32 v[172:173], v[172:173], v[230:231] op_sel:[0,1] op_sel_hi:[1,1]\n"
      "v_pk_mul_f32 v[174:175], v[174:175], v[230:231] op_sel:[0,1] op_sel_hi:[1,1]\n"
      "v_pk_mul_f32 v[176:177], v[176:177], v[228:229] op_sel:[0,0] op_sel_hi:[1,0]\n"
      "v_pk_mul_f32 v[178:179], v[178:179], v[228:229] op_sel:[0,0] op_sel_hi:[1,0]\n"
      "v_pk_mul_f32 v[180:181], v[180:181], v[228:229] op_sel:[0,1] op_sel_hi:[1,1]\n"
      "v_pk_mul_f32 v[182:183], v[182:183], v[228:229] op_sel:[0,1] op_sel_hi:[1,1]\n"
      "v_pk_mul_f32 v[184:185], v[184:185], v[230:231] op_sel:[0,0] op_sel_hi:[1,0]\n"
      "v_pk_mul_f32 v[186:187], v[186:187], v[230:231] op_sel:[0,0] op_sel_hi:[1,0]\n"
      "v_pk_mul_f32 v[188:189], v[188:189], v[230:231] op_sel:[0,1] op_sel_hi:[1,1]\n"
      "v_pk_mul_f32 v[190:191], v[190:191], v[230:231] op_sel:[0,1] op_sel_hi:[1,1]\n"
      "v_mov_b32 v112, v228\n"
      "v_mov_b32 v113, v229\n"
      "v_mov_b32 v114, v230\n"
      "v_mov_b32 v115, v231\n"
      "v_accvgpr_read_b32 v128, a64\n"
      "v_accvgpr_read_b32 v129, a65\n"
      "v_accvgpr_read_b32 v130, a66\n"
      "v_accvgpr_read_b32 v131, a67\n"
      "v_accvgpr_read_b32 v132, a68\n"
      "v_accvgpr_read_b32 v133, a69\n"
      "v_accvgpr_read_b32 v134, a70\n"
      "v_accvgpr_read_b32 v135, a71\n"
      "v_accvgpr_read_b32 v136, a72\n"
      "v_accvgpr_read_b32 v137, a73\n"
      "v_accvgpr_read_b32 v138, a74\n"
      "v_accvgpr_read_b32 v139, a75\n"
      "v_accvgpr_read_b32 v140, a76\n"
      "v_accvgpr_read_b32 v141, a77\n"
      "v_accvgpr_read_b32 v142, a78\n"
      "v_accvgpr_read_b32 v143, a79\n"
      "v_lshlrev_b32 v118, 16, v128\n"
      "v_and_b32 v119, 0xffff0000, v128\n"
      "v_pk_mul_f32 v[118:119], v[118:119], v[112:113] op_sel:[0,0] op_sel_hi:[1,0]\n"
      "v_cvt_pk_bf16_f32 v128, v118, v119\n"
      "v_lshlrev_b32 v118, 16, v129\n"
      "v_and_b32 v119, 0xffff0000, v129\n"
      "v_pk_mul_f32 v[118:119], v[118:119], v[112:113] op_sel:[0,0] op_sel_hi:[1,0]\n"
      "v_cvt_pk_bf16_f32 v129, v118, v119\n"
      "v_lshlrev_b32 v118, 16, v130\n"
      "v_and_b32 v119, 0xffff0000, v130\n"
      "v_pk_mul_f32 v[118:119], v[118:119], v[112:113] op_sel:[0,0] op_sel_hi:[1,0]\n"
      "v_cvt_pk_bf16_f32 v130, v118, v119\n"
      "v_lshlrev_b32 v118, 16, v131\n"
      "v_and_b32 v119, 0xffff0000, v131\n"
      "v_pk_mul_f32 v[118:119], v[118:119], v[112:113] op_sel:[0,0] op_sel_hi:[1,0]\n"
      "v_cvt_pk_bf16_f32 v131, v118, v119\n"
      "v_lshlrev_b32 v118, 16, v132\n"
      "v_and_b32 v119, 0xffff0000, v132\n"
      "v_pk_mul_f32 v[118:119], v[118:119], v[112:113] op_sel:[0,1] op_sel_hi:[1,1]\n"
      "v_cvt_pk_bf16_f32 v132, v118, v119\n"
      "v_lshlrev_b32 v118, 16, v133\n"
      "v_and_b32 v119, 0xffff0000, v133\n"
      "v_pk_mul_f32 v[118:119], v[118:119], v[112:113] op_sel:[0,1] op_sel_hi:[1,1]\n"
      "v_cvt_pk_bf16_f32 v133, v118, v119\n"
      "v_lshlrev_b32 v118, 16, v134\n"
      "v_and_b32 v119, 0xffff0000, v134\n"
      "v_pk_mul_f32 v[118:119], v[118:119], v[112:113] op_sel:[0,1] op_sel_hi:[1,1]\n"
      "v_cvt_pk_bf16_f32 v134, v118, v119\n"
      "v_lshlrev_b32 v118, 16, v135\n"
      "v_and_b32 v119, 0xffff0000, v135\n"
      "v_pk_mul_f32 v[118:119], v[118:119], v[112:113] op_sel:[0,1] op_sel_hi:[1,1]\n"
      "v_cvt_pk_bf16_f32 v135, v118, v119\n"
      "v_lshlrev_b32 v118, 16, v136\n"
      "v_and_b32 v119, 0xffff0000, v136\n"
      "v_pk_mul_f32 v[118:119], v[118:119], v[114:115] op_sel:[0,0] op_sel_hi:[1,0]\n"
      "v_cvt_pk_bf16_f32 v136, v118, v119\n"
      "v_lshlrev_b32 v118, 16, v137\n"
      "v_and_b32 v119, 0xffff0000, v137\n"
      "v_pk_mul_f32 v[118:119], v[118:119], v[114:115] op_sel:[0,0] op_sel_hi:[1,0]\n"
      "v_cvt_pk_bf16_f32 v137, v118, v119\n"
      "v_lshlrev_b32 v118, 16, v138\n"
      "v_and_b32 v119, 0xffff0000, v138\n"
      "v_pk_mul_f32 v[118:119], v[118:119], v[114:115] op_sel:[0,0] op_sel_hi:[1,0]\n"
      "v_cvt_pk_bf16_f32 v138, v118, v119\n"
      "v_lshlrev_b32 v118, 16, v139\n"
      "v_and_b32 v119, 0xffff0000, v139\n"
      "v_pk_mul_f32 v[118:119], v[118:119], v[114:115] op_sel:[0,0] op_sel_hi:[1,0]\n"
      "v_cvt_pk_bf16_f32 v139, v118, v119\n"
      "v_lshlrev_b32 v118, 16, v140\n"
      "v_and_b32 v119, 0xffff0000, v140\n"
      "v_pk_mul_f32 v[118:119], v[118:119], v[114:115] op_sel:[0,1] op_sel_hi:[1,1]\n"
      "v_cvt_pk_bf16_f32 v140, v118, v119\n"
      "v_lshlrev_b32 v118, 16, v141\n"
      "v_and_b32 v119, 0xffff0000, v141\n"
      "v_pk_mul_f32 v[118:119], v[118:119], v[114:115] op_sel:[0,1] op_sel_hi:[1,1]\n"
      "v_cvt_pk_bf16_f32 v141, v118, v119\n"
      "v_lshlrev_b32 v118, 16, v142\n"
      "v_and_b32 v119, 0xffff0000, v142\n"
      "v_pk_mul_f32 v[118:119], v[118:119], v[114:115] op_sel:[0,1] op_sel_hi:[1,1]\n"
      "v_cvt_pk_bf16_f32 v142, v118, v119\n"
      "v_lshlrev_b32 v118, 16, v143\n"
      "v_and_b32 v119, 0xffff0000, v143\n"
      "v_pk_mul_f32 v[118:119], v[118:119], v[114:115] op_sel:[0,1] op_sel_hi:[1,1]\n"
      "v_cvt_pk_bf16_f32 v143, v118, v119\n"
      "v_accvgpr_write_b32 a64, v128\n"
      "v_accvgpr_write_b32 a65, v129\n"
      "v_accvgpr_write_b32 a66, v130\n"
      "v_accvgpr_write_b32 a67, v131\n"
      "v_accvgpr_write_b32 a68, v132\n"
      "v_accvgpr_write_b32 a69, v133\n"
      "v_accvgpr_write_b32 a70, v134\n"
      "v_accvgpr_write_b32 a71, v135\n"
      "v_accvgpr_write_b32 a72, v136\n"
      "v_accvgpr_write_b32 a73, v137\n"
      "v_accvgpr_write_b32 a74, v138\n"
      "v_accvgpr_write_b32 a75, v139\n"
      "v_accvgpr_write_b32 a76, v140\n"
      "v_accvgpr_write_b32 a77, v141\n"
      "v_accvgpr_write_b32 a78, v142\n"
      "v_accvgpr_write_b32 a79, v143\n"
      "s_branch MR_%=\n"
      "FIX_%=:\n"
      "s_nop 7\n"
      "v_sub_f32 v224, v81, v117\n"
      "v_min_f32 v112, 0, -v224\n"
      "v_min_f32 v224, 0, v224\n"
      "v_exp_f32 v112, v112\n"
      "v_exp_f32 v224, v224\n"
      "s_nop 0\n"
      "v_mul_f32 v144, v144, v224\n"
      "v_mul_f32 v80, v80, v112\n"
      "v_mov_b32 v226, v224\n"
      "v_mov_b32 v114, v112\n"
      "s_nop 0\n"
      "v_permlane32_swap_b32 v224, v226\n"
      "v_permlane32_swap_b32 v112, v114\n"
      "v_mov_b32 v225, v224\n"
      "v_mov_b32 v113, v112\n"
      "v_mov_b32 v227, v226\n"
      "v_mov_b32 v115, v114\n"
      "v_permlane16_swap_b32 v224, v225\n"
      "v_permlane16_swap_b32 v112, v113\n"
      "v_permlane16_swap_b32 v226, v227\n"
      "v_permlane16_swap_b32 v114, v115\n"
      "v_pk_mul_f32 v[32:33], v[32:33], v[224:225] op_sel:[0,0] op_sel_hi:[1,0]\n"
      "v_pk_mul_f32 v[34:35], v[34:35], v[224:225] op_sel:[0,0] op_sel_hi:[1,0]\n"
      "v_pk_mul_f32 v[36:37], v[36:37], v[224:225] op_sel:[0,1] op_sel_hi:[1,1]\n"
      "v_pk_mul_f32 v[38:39], v[38:39], v[224:225] op_sel:[0,1] op_sel_hi:[1,1]\n"
      "v_pk_mul_f32 v[40:41], v[40:41], v[226:227] op_sel:[0,0] op_sel_hi:[1,0]\n"
      "v_pk_mul_f32 v[42:43], v[42:43], v[226:227] op_sel:[0,0] op_sel_hi:[1,0]\n"
      "v_pk_mul_f32 v[44:45], v[44:45], v[226:227] op_sel:[0,1] op_sel_hi:[1,1]\n"
      "v_pk_mul_f32 v[46:47], v[46:47], v[226:227] op_sel:[0,1] op_sel_hi:[1,1]\n"
      "v_pk_mul_f32 v[48:49], v[48:49], v[224:225] op_sel:[0,0] op_sel_hi:[1,0]\n"
      "v_pk_mul_f32 v[50:51], v[50:51], v[224:225] op_sel:[0,0] op_sel_hi:[1,0]\n"
      "v_pk_mul_f32 v[52:53], v[52:53], v[224:225] op_sel:[0,1] op_sel_hi:[1,1]\n"
      "v_pk_mul_f32 v[54:55], v[54:55], v[224:225] op_sel:[0,1] op_sel_hi:[1,1]\n"
      "v_pk_mul_f32 v[56:57], v[56:57], v[226:227] op_sel:[0,0] op_sel_hi:[1,0]\n"
      "v_pk_mul_f32 v[58:59], v[58:59], v[226:227] op_sel:[0,0] op_sel_hi:[1,0]\n"
      "v_pk_mul_f32 v[60:61], v[60:61], v[226:227] op_sel:[0,1] op_sel_hi:[1,1]\n"
      "v_pk_mul_f32 v[62:63], v[62:63], v[226:227] op_sel:[0,1] op_sel_hi:[1,1]\n"
      "v_lshlrev_b32 v118, 16, v64\n"
      "v_and_b32 v119, 0xffff0000, v64\n"
      "v_pk_mul_f32 v[118:119], v[118:119], v[112:113] op_sel:[0,0] op_sel_hi:[1,0]\n"
      "v_cvt_pk_bf16_f32 v64, v118, v119\n"
      "v_lshlrev_b32 v118, 16, v65\n"
      "v_and_b32 v119, 0xffff0000, v65\n"
      "v_pk_mul_f32 v[118:119], v[118:119], v[112:113] op_sel:[0,0] op_sel_hi:[1,0]\n"
      "v_cvt_pk_bf16_f32 v65, v118, v119\n"
      "v_lshlrev_b32 v118, 16, v66\n"
      "v_and_b32 v119, 0xffff0000, v66\n"
      "v_pk_mul_f32 v[118:119], v[118:119], v[112:113] op_sel:[0,0] op_sel_hi:[1,0]\n"
      "v_cvt_pk_bf16_f32 v66, v118, v119\n"
      "v_lshlrev_b32 v118, 16, v67\n"
      "v_and_b32 v119, 0xffff0000, v67\n"
      "v_pk_mul_f32 v[118:119], v[118:119], v[112:113] op_sel:[0,0] op_sel_hi:[1,0]\n"
      "v_cvt_pk_bf16_f32 v67, v118, v119\n"
      "v_lshlrev_b32 v118, 16, v68\n"
      "v_and_b32 v119, 0xffff0000, v68\n"
      "v_pk_mul_f32 v[118:119], v[118:119], v[112:113] op_sel:[0,1] op_sel_hi:[1,1]\n"
      "v_cvt_pk_bf16_f32 v68, v118, v119\n"
      "v_lshlrev_b32 v118, 16, v69\n"
      "v_and_b32 v119, 0xffff0000, v69\n"
      "v_pk_mul_f32 v[118:119], v[118:119], v[112:113] op_sel:[0,1] op_sel_hi:[1,1]\n"
      "v_cvt_pk_bf16_f32 v69, v118, v119\n"
      "v_lshlrev_b32 v118, 16, v70\n"
      "v_and_b32 v119, 0xffff0000, v70\n"
      "v_pk_mul_f32 v[118:119], v[118:119], v[112:113] op_sel:[0,1] op_sel_hi:[1,1]\n"
      "v_cvt_pk_bf16_f32 v70, v118, v119\n"
      "v_lshlrev_b32 v118, 16, v71\n"
      "v_and_b32 v119, 0xffff0000, v71\n"
      "v_pk_mul_f32 v[118:119], v[118:119], v[112:113] op_sel:[0,1] op_sel_hi:[1,1]\n"
      "v_cvt_pk_bf16_f32 v71, v118, v119\n"
      "v_lshlrev_b32 v118, 16, v72\n"
      "v_and_b32 v119, 0xffff0000, v72\n"
      "v_pk_mul_f32 v[118:119], v[118:119], v[114:115] op_sel:[0,0] op_sel_hi:[1,0]\n"
      "v_cvt_pk_bf16_f32 v72, v118, v119\n"
      "v_lshlrev_b32 v118, 16, v73\n"
      "v_and_b32 v119, 0xffff0000, v73\n"
      "v_pk_mul_f32 v[118:119], v[118:119], v[114:115] op_sel:[0,0] op_sel_hi:[1,0]\n"
      "v_cvt_pk_bf16_f32 v73, v118, v119\n"
      "v_lshlrev_b32 v118, 16, v74\n"
      "v_and_b32 v119, 0xffff0000, v74\n"
      "v_pk_mul_f32 v[118:119], v[118:119], v[114:115] op_sel:[0,0] op_sel_hi:[1,0]\n"
      "v_cvt_pk_bf16_f32 v74, v118, v119\n"
      "v_lshlrev_b32 v118, 16, v75\n"
      "v_and_b32 v119, 0xffff0000, v75\n"
      "v_pk_mul_f32 v[118:119], v[118:119], v[114:115] op_sel:[0,0] op_sel_hi:[1,0]\n"
      "v_cvt_pk_bf16_f32 v75, v118, v119\n"
      "v_lshlrev_b32 v118, 16, v76\n"
      "v_and_b32 v119, 0xffff0000, v76\n"
      "v_pk_mul_f32 v[118:119], v[118:119], v[114:115] op_sel:[0,1] op_sel_hi:[1,1]\n"
      "v_cvt_pk_bf16_f32 v76, v118, v119\n"
      "v_lshlrev_b32 v118, 16, v77\n"
      "v_and_b32 v119, 0xffff0000, v77\n"
      "v_pk_mul_f32 v[118:119], v[118:119], v[114:115] op_sel:[0,1] op_sel_hi:[1,1]\n"
      "v_cvt_pk_bf16_f32 v77, v118, v119\n"
      "v_lshlrev_b32 v118, 16, v78\n"
      "v_and_b32 v119, 0xffff0000, v78\n"
      "v_pk_mul_f32 v[118:119], v[118:119], v[114:115] op_sel:[0,1] op_sel_hi:[1,1]\n"
      "v_cvt_pk_bf16_f32 v78, v118, v119\n"
      "v_lshlrev_b32 v118, 16, v79\n"
      "v_and_b32 v119, 0xffff0000, v79\n"
      "v_pk_mul_f32 v[118:119], v[118:119], v[114:115] op_sel:[0,1] op_sel_hi:[1,1]\n"
      "v_cvt_pk_bf16_f32 v79, v118, v119\n"
      "v_add_f32 v224, v144, v80\n"
      "v_rcp_f32 v224, v224\n"
      "v_mfma_f32_16x16x32_bf16 v[160:163], v[86:89], v[64:67], v[32:35]\n"
      "v_mfma_f32_16x16x32_bf16 v[176:179], v[84:87], v[64:67], v[48:51]\n"
      "v_mfma_f32_16x16x32_bf16 v[164:167], v[86:89], v[68:71], v[36:39]\n"
      "v_mfma_f32_16x16x32_bf16 v[180:183], v[84:87], v[68:71], v[52:55]\n"
      "v_mfma_f32_16x16x32_bf16 v[168:171], v[86:89], v[72:75], v[40:43]\n"
      "v_mfma_f32_16x16x32_bf16 v[184:187], v[84:87], v[72:75], v[56:59]\n"
      "v_mfma_f32_16x16x32_bf16 v[172:175], v[86:89], v[76:79], v[44:47]\n"
      "v_mfma_f32_16x16x32_bf16 v[188:191], v[84:87], v[76:79], v[60:63]\n"
      "v_mov_b32 v226, v224\n"
      "s_nop 1\n"
      "s_nop 0\n"
      "s_nop 1\n"
      "v_permlane32_swap_b32 v224, v226\n"
      "s_nop 1\n"
      "v_mov_b32 v225, v224\n"
      "s_nop 1\n"
      "v_mov_b32 v227, v226\n"
      "s_nop 1\n"
      "v_permlane16_swap_b32 v224, v225\n"
      "s_nop 1\n"
      "v_permlane16_swap_b32 v226, v227\n"
      "s_nop 1\n"
      "s_branch FR_%=\n"
      ".subsection 0\n"
      : "+{s[4:19]}"(ka), "+{v128}"(pre), "+{v[160:191]}"(qq), "+{s92}"(tch), "+{v[242:249]}"(ad), "+{v[250:251]}"(ra)
      : [blk] "s"(blk), [qo] "s"(bk * 128 * D_ * 2), [so0] "s"(so0), [inc] "s"(inc), [wb] "s"(wbase), [tv] "s"(wbase + STAGE_BYTES + KIMG + split * 8192),
        [qoff] "s"(Q_OFF + rh * 16384 - kh * 8192), [toff] "s"(TREE_OFF - kh * 8192)
      : "v0", "v1", "v2", "v3", "v4", "v5", "v6", "v7", "v8", "v9", "v10", "v11", "v12", "v13", "v14", "v15", "v16", "v17", "v18", "v19", "v20", "v21", "v22", "v23", "v24", "v25", "v26", "v27", "v28", "v29", "v30", "v31", "v32", "v33", "v34", "v35", "v36", "v37", "v38", "v39", "v40", "v41", "v42", "v43", "v44", "v45", "v46", "v47", "v48", "v49", "v50", "v51", "v52", "v53", "v54", "v55", "v56", "v57", "v58", "v59", "v60", "v61", "v62", "v63", "v64", "v65", "v66", "v67", "v68", "v69", "v70", "v71", "v72", "v73", "v74", "v75", "v76", "v77", "v78", "v79", "v80", "v81", "v82", "v83", "v84", "v85", "v86", "v87", "v88", "v89", "v90", "v91", "v92", "v93", "v94", "v95", "v96", "v97", "v98", "v99", "v100", "v101", "v102", "v103", "v104", "v105", "v106", "v107", "v108", "v109", "v110", "v111", "v112", "v113", "v114", "v115", "v116", "v117", "v118", "v119", "v120", "v121", "v122", "v123", "v124", "v125", "v126", "v127", "v129", "v130", "v131", "v132", "v133", "v134", "v135", "v136", "v137", "v138", "v139", "v140", "v141", "v142", "v143", "v144", "v145", "v146", "v147", "v148", "v149", "v150", "v151", "v152", "v153", "v154", "v155", "v156", "v157", "v158", "v159", "v192", "v193", "v194", "v195", "v196", "v197", "v198", "v199", "v200", "v201", "v202", "v203", "v204", "v205", "v206", "v207", "v208", "v209", "v210", "v211", "v212", "v213", "v214", "v215", "v216", "v217", "v218", "v219", "v220", "v221", "v222", "v223", "v224", "v225", "v226", "v227", "v228", "v229", "v230", "v231", "v232", "v233", "v234", "v235", "v236", "v237", "v238", "v239", "v240", "v241", "a0", "a1", "a2", "a3", "a4", "a5", "a6", "a7", "a8", "a9", "a10", "a11", "a12", "a13", "a14", "a15", "a16", "a17", "a18", "a19", "a20", "a21", "a22", "a23", "a24", "a25", "a26", "a27", "a28", "a29", "a30", "a31", "a32", "a33", "a34", "a35", "a36", "a37", "a38", "a39", "a40", "a41", "a42", "a43", "a44", "a45", "a46", "a47", "a48", "a49", "a50", "a51", "a52", "a53", "a54", "a55", "a56", "a57", "a58", "a59", "a60", "a61", "a62", "a63", "a64", "a65", "a66", "a67", "a68", "a69", "a70", "a71", "a72", "a73", "a74", "a75", "a76", "a77", "a78", "a79", "a80", "a81", "a82", "a83", "a84", "a85", "a86", "a87", "a88", "a89", "a90", "a91", "a92", "a93", "a94", "a95", "a96", "a97", "a98", "a99", "a100", "a101", "a102", "a103", "a104", "a105", "a106", "a107", "a108", "a109", "a110", "a111", "a112", "a113", "a114", "a115", "a116", "a117", "a118", "a119", "a120", "a121", "a122", "a123", "a124", "a125", "a126", "a127", "a128", "a129", "a130", "a131", "a132", "a133", "a134", "a135", "a136", "a137", "a138", "a139", "a140", "a141", "a142", "a143", "a144", "a145", "a146", "a147", "a148", "a149", "a150", "a151", "a152", "a153", "a154", "a155", "a156", "a157", "a158", "a159", "a160", "a161", "a162", "a163", "a164", "a165", "a166", "a167", "a168", "a169", "a170", "a171", "a172", "a173", "a174", "a175", "a176", "a177", "a178", "a179", "a180", "a181", "a182", "a183", "a184", "a185", "a186", "a187", "a188", "a189", "a190", "a191", "a192", "a193", "a194", "a195", "a196", "a197", "a198", "a199", "a200", "a201", "a202", "a203", "a204", "a205", "a206", "a207", "a208", "a209", "a210", "a211", "a212", "a213", "a214", "a215", "a216", "a217", "a218", "a219", "a220", "a221", "a222", "a223", "a224", "a225", "a226", "a227", "a228", "a229", "a230", "a231", "s20", "s21", "s22", "s23", "s24", "s25", "s26", "s27", "s28", "s29", "s30", "s31", "s33", "s34", "s35", "s36", "s37", "s38", "s39", "s40", "s41", "s42", "s43", "s44", "s60", "s61", "s62", "s63", "s64", "s65", "s66", "s67", "s68", "s69", "s70", "s71", "s72", "s73", "s74", "s75", "s76", "s77", "s78", "s79", "s80", "s81", "s82", "s83", "s84", "s85", "s86", "s87", "s88", "s89", "s90", "s91", "s93", "s94", "s95", "s98", "s99", "s100", "s101", "vcc", "scc", "memory");
}

}

extern "C" void kernel_launch(void* const* d_in, const int* in_sizes, int n_in, void* d_out, int out_size,
                              void* d_ws, size_t ws_size, hipStream_t stream) {
  if (n_in != 6 || ws_size < WS_END) return;
  const __hip_bfloat16* q = (const __hip_bfloat16*)d_in[0];
  const __hip_bfloat16* kc = (const __hip_bfloat16*)d_in[1];
  const __hip_bfloat16* vc = (const __hip_bfloat16*)d_in[2];
  const __hip_bfloat16* kt = (const __hip_bfloat16*)d_in[3];
  const __hip_bfloat16* vt = (const __hip_bfloat16*)d_in[4];
  const __hip_bfloat16* tm = (const __hip_bfloat16*)d_in[5];
  hipLaunchKernelGGL(tree_attn_main, dim3(B_ * HKV_ * 2), dim3(256), 0, stream, q, kc, vc, kt, vt, tm,
                     (__hip_bfloat16*)d_out, (char*)d_ws);
}
```
